```python
import torch

def _softmax(x, axis=-1):
    return torch.softmax(x, dim=axis)

def _gelu(x):
    return torch.nn.functional.gelu(x, approximate='tanh')

def _silu(x):
    return torch.nn.functional.silu(x)

def _sigmoid(x):
    return torch.sigmoid(x)

def _layernorm(x, axis=-1, eps=1e-05):
    m = x.mean(dim=axis, keepdim=True)
    v = x.var(dim=axis, keepdim=True, unbiased=False)
    return (x - m) / torch.sqrt(v + eps)

def _scatter_add(tgt, idx, src, axis=0):
    return tgt.clone().index_add_(axis, idx, src)

def _segment_sum(src, seg, n):
    out = torch.zeros((n,) + src.shape[1:], dtype=src.dtype)
    return out.index_add_(0, seg, src)

def _pad(x, pw):
    flat = tuple((v for pair in reversed(pw) for v in pair))
    return torch.nn.functional.pad(x, flat)
kernel_name = 'adaLN_cond__memory_aug_recurrent_mem_tr__glu_gated_swiglu__prompt'

def setup_inputs():

    _w = lambda a: (lambda t: t.to(torch.bfloat16) if t.is_floating_point() else t)(torch.as_tensor(a))

    def _A_setup():
        B, S, Dm = (1, 2048, 1024)
        H, Dh = (32, 128)
        M, seg = (8, 32)
        seg = min(seg, S)
        S = S // seg * seg
        return {'x': _w((lambda *s: torch.randn(*s, dtype=torch.bfloat16) * 0.05)(B, S, Dm)), 'mem0': _w((lambda *s: torch.randn(*s, dtype=torch.bfloat16) * 0.8)(B, M, Dm)), 'Wq': _w((lambda *s: torch.randn(*s, dtype=torch.bfloat16) * 0.8)(Dm, H, Dh) * Dm ** (-0.5)), 'Wk': _w((lambda *s: torch.randn(*s, dtype=torch.bfloat16) * 0.8)(Dm, H, Dh) * Dm ** (-0.5)), 'Wv': _w((lambda *s: torch.randn(*s, dtype=torch.bfloat16) * 0.8)(Dm, H, Dh) * Dm ** (-0.5)), 'Wo': _w((lambda *s: torch.randn(*s, dtype=torch.bfloat16) * 0.8)(H, Dh, Dm) * (H * Dh) ** (-0.5)), 'seg_len': seg}
    _A_in = _A_setup()

    def _B_setup():
        B, S, D, F = (1, 2048, 1024, 3584)
        return {'x': _w((lambda *s: torch.randn(*s, dtype=torch.bfloat16) * 0.05)(B, S, D)), 'w1': _w((lambda *s: torch.randn(*s, dtype=torch.bfloat16) * 0.4)(D, F) * (2.0 / D) ** 0.5), 'w3': _w((lambda *s: torch.randn(*s, dtype=torch.bfloat16) * 0.4)(D, F) * (2.0 / D) ** 0.5), 'w2': _w((lambda *s: torch.randn(*s, dtype=torch.bfloat16) * 0.4)(F, D) * (2.0 / F) ** 0.5)}
    _B_in = _B_setup()
    return {'A_x': _A_in['x'], 'A_mem0': _A_in['mem0'], 'A_Wq': _A_in['Wq'], 'A_Wk': _A_in['Wk'], 'A_Wv': _A_in['Wv'], 'A_Wo': _A_in['Wo'], 'A_seg_len': _A_in['seg_len'], 'B_x': _B_in['x'], 'B_w1': _B_in['w1'], 'B_w3': _B_in['w3'], 'B_w2': _B_in['w2'], 'c': _w((lambda *s: torch.randn(*s, dtype=torch.bfloat16) * 0.1)(1, 1024) * 8.0), 'w_mod': _w((lambda *s: torch.randn(*s, dtype=torch.bfloat16) * 0.1)(1024, 6 * 1024) * 1024 ** (-0.5) * 8.0)}

def reference(A_x, A_mem0, A_Wq, A_Wk, A_Wv, A_Wo, A_seg_len, B_x, B_w1, B_w3, B_w2, c, w_mod):

    def _A_ref(x, mem0, Wq, Wk, Wv, Wo, seg_len):
        B, S, Dm = x.shape
        H, Dh = (Wq.shape[1], Wq.shape[2])
        M = mem0.shape[1]
        scale = Dh ** (-0.5)
        n_seg = S // seg_len
        L = M + seg_len
        idx = torch.arange(L)
        mask = (idx[:, None] < M) | (idx[:, None] >= idx[None, :])
        mem = mem0
        outs = []
        for s in range(n_seg):
            xs = x[:, s * seg_len:(s + 1) * seg_len, :].clone()
            h = torch.concatenate([mem, xs], axis=1)
            q = torch.einsum('btd,dhe->bhte', h, Wq)
            k = torch.einsum('btd,dhe->bhte', h, Wk)
            v = torch.einsum('btd,dhe->bhte', h, Wv)
            scores = torch.einsum('bhqe,bhke->bhqk', q, k) * scale
            scores = torch.where(mask[None, None], scores, -1000000000.0)
            p = _softmax(scores, axis=-1)
            o = torch.einsum('bhqk,bhke->bhqe', p, v)
            o = torch.einsum('bhte,hed->btd', o, Wo)
            mem = o[:, :M, :].clone()
            outs.append(o[:, M:, :])
        return torch.concatenate(outs, axis=1)

    def _B_ref(x, w1, w3, w2):
        gate = torch.einsum('bsd,df->bsf', x, w1)
        up = torch.einsum('bsd,df->bsf', x, w3)
        h = _silu(gate) * up
        out = torch.einsum('bsf,fd->bsd', h, w2)
        return out
    x = A_x
    mod = (_silu(c) @ w_mod).reshape(c.shape[0], 6, 1, -1)
    sa, ca, ga, sb, cb, gb = (mod[:, 0].clone(), mod[:, 1].clone(), mod[:, 2].clone(), mod[:, 3].clone(), mod[:, 4].clone(), mod[:, 5].clone())
    h = _layernorm(x, axis=-1) * (1 + ca) + sa
    x = x + ga * _A_ref(h, A_mem0, A_Wq, A_Wk, A_Wv, A_Wo, A_seg_len)
    h = _layernorm(x, axis=-1) * (1 + cb) + sb
    x = x + gb * _B_ref(h, B_w1, B_w3, B_w2)
    return x
```

```cpp
#include <hip/hip_runtime.h>
#include <hip/hip_bf16.h>
#include <stdint.h>
#include <type_traits>
#pragma clang diagnostic ignored "-Winline-asm"
#pragma clang diagnostic ignored "-Wunused-value"

typedef unsigned short u16;
typedef short s16x4 __attribute__((ext_vector_type(4)));
typedef __bf16 bf16x8 __attribute__((ext_vector_type(8)));
typedef float f32x4 __attribute__((ext_vector_type(4)));
typedef float f32x2 __attribute__((ext_vector_type(2)));
typedef unsigned u32x4 __attribute__((ext_vector_type(4)));
typedef unsigned u32x2 __attribute__((ext_vector_type(2)));
#define LAS __attribute__((address_space(3)))

constexpr int SEQ = 2048, DM = 1024, NH = 32, DH = 128, HD = 4096, NMEM = 8, SEG = 32, NSEG = 64, FF = 3584;
constexpr unsigned SENT = 0xFFFFFFFFu;

__device__ __forceinline__ int lane_id() { int l; asm volatile("v_mbcnt_lo_u32_b32 %0, -1, 0\n\tv_mbcnt_hi_u32_b32 %0, -1, %0" : "=v"(l)); return l; }
#define WAVE_ID wave_id_
#define opaque_tid() (WAVE_ID * 64 + lane_id())
__device__ __forceinline__ float bf2f(u16 v) { return __builtin_bit_cast(float, (unsigned)v << 16); }
__device__ __forceinline__ float bflo(unsigned v) { return __builtin_bit_cast(float, v << 16); }
__device__ __forceinline__ float bfhi(unsigned v) { return __builtin_bit_cast(float, v & 0xffff0000u); }
__device__ __forceinline__ float bfr(float x) { return (float)(__bf16)x; }
__device__ __forceinline__ unsigned pack2(float a, float b) {
  typedef __bf16 bf16x2 __attribute__((ext_vector_type(2)));
  bf16x2 v; v[0] = (__bf16)a; v[1] = (__bf16)b; return __builtin_bit_cast(unsigned, v);
}
__device__ __forceinline__ unsigned nosent(unsigned v) { return v == SENT ? 0x7FC07FC0u : v; }
__device__ __forceinline__ bf16x8 asbf8(u32x4 v) { return __builtin_bit_cast(bf16x8, v); }
__device__ __forceinline__ s16x4 ass4(u32x2 v) { return __builtin_bit_cast(s16x4, v); }
__device__ __forceinline__ u32x2 trread(const char* lds_base, unsigned off) {
  s16x4 v = __builtin_amdgcn_ds_read_tr16_b64_v4i16((LAS s16x4*)(lds_base + off));
  return __builtin_bit_cast(u32x2, v);
}
__device__ __forceinline__ void glds16(const void* gsrc, unsigned lds_dst) {
  unsigned keep;
  asm volatile("s_mov_b32 %0, m0\n\ts_mov_b32 m0, %2\n\ts_nop 0\n\tglobal_load_lds_dwordx4 %1, off\n\ts_mov_b32 m0, %0"
               : "=&s"(keep) : "v"(gsrc), "s"(lds_dst) : "memory");
}
#define CNT_ " nt"
#define WNT_ " nt"
__device__ __forceinline__ void glds16_w(const void* gsrc, unsigned lds_dst) {
  unsigned keep;
  asm volatile("s_mov_b32 %0, m0\n\ts_mov_b32 m0, %2\n\ts_nop 0\n\tglobal_load_lds_dwordx4 %1, off" CNT_ "\n\ts_mov_b32 m0, %0"
               : "=&s"(keep) : "v"(gsrc), "s"(lds_dst) : "memory");
}
__device__ __forceinline__ void glds16_sc1(const void* gsrc, unsigned lds_dst) {
  unsigned keep;
  asm volatile("s_mov_b32 %0, m0\n\ts_mov_b32 m0, %2\n\ts_nop 0\n\tglobal_load_lds_dwordx4 %1, off sc1\n\ts_mov_b32 m0, %0"
               : "=&s"(keep) : "v"(gsrc), "s"(lds_dst) : "memory");
}
#define NTM 1
template <int B> __device__ __forceinline__ u32x4 ldnt(const void* p) { if (NTM & B) return __builtin_nontemporal_load((const u32x4*)p); else return *(const u32x4*)p; }
#define MFMA32(a, b, c) __builtin_amdgcn_mfma_f32_16x16x32_bf16((a), (b), (c), 0, 0, 0)

__device__ __forceinline__ void st16_sc1(void* p, u32x4 v) { asm volatile("global_store_dwordx4 %0, %1, off sc1\n\ts_nop 1" :: "v"(p), "v"(v) : "memory"); }
__device__ __forceinline__ void st8_sc1(void* p, u32x2 v) { asm volatile("global_store_dwordx2 %0, %1, off sc1\n\ts_nop 1" :: "v"(p), "v"(v) : "memory"); }
template <bool WT> __device__ __forceinline__ void st8(void* p, u32x2 v) { if (WT) st8_sc1(p, v); else *(u32x2*)p = v; }
template <bool WT> __device__ __forceinline__ void st16(void* p, u32x4 v) { if (WT) st16_sc1(p, v); else *(u32x4*)p = v; }
__device__ __forceinline__ u32x4 ld16_sc1(const void* p) {
  u32x4 r; asm volatile("global_load_dwordx4 %0, %1, off sc1\n\ts_waitcnt vmcnt(0)" : "=&v"(r) : "v"(p) : "memory"); return r;
}
__device__ __forceinline__ void ld16x4_sc1(const void* p, u32x4& a, u32x4& b, u32x4& c, u32x4& d) {
  asm volatile("global_load_dwordx4 %0, %4, off sc1\n\tglobal_load_dwordx4 %1, %4, off offset:1024 sc1\n\t"
               "global_load_dwordx4 %2, %4, off offset:2048 sc1\n\tglobal_load_dwordx4 %3, %4, off offset:3072 sc1\n\ts_waitcnt vmcnt(0)"
               : "=&v"(a), "=&v"(b), "=&v"(c), "=&v"(d) : "v"(p) : "memory");
}
typedef __amdgpu_buffer_rsrc_t rsrc_t;
__device__ __forceinline__ rsrc_t mkrs(const void* p) { return __builtin_amdgcn_make_buffer_rsrc((void*)p, 0, 0x7FFFFFFF, 0x00020000); }
__device__ __forceinline__ u32x4 bld16(rsrc_t r, unsigned off) { return __builtin_amdgcn_raw_buffer_load_b128(r, off, 0, 16); }
__device__ __forceinline__ u32x2 bld8(rsrc_t r, unsigned off) { return __builtin_amdgcn_raw_buffer_load_b64(r, off, 0, 16); }
__device__ __forceinline__ bool ok4(u32x4 v) { return v.x != SENT && v.y != SENT && v.z != SENT && v.w != SENT; }
constexpr int SPIN_CAP = 2000000;
__device__ __forceinline__ bool give_up(int spins, unsigned* tmo, unsigned code) {
  if ((spins & 4095) != 4095) return false;
  if (spins > SPIN_CAP) { __hip_atomic_store(tmo, code, __ATOMIC_RELAXED, __HIP_MEMORY_SCOPE_AGENT); return true; }
  return __hip_atomic_load(tmo, __ATOMIC_RELAXED, __HIP_MEMORY_SCOPE_AGENT) != 0u;
}
template <bool ACQ = true>
__device__ __forceinline__ void wait_cnt(unsigned* c, unsigned n, unsigned* tmo) {
  for (int spins = 0;; ++spins) {
    const unsigned v = __hip_atomic_load(c, __ATOMIC_RELAXED, __HIP_MEMORY_SCOPE_AGENT);
    if (v >= n) break;
    __builtin_amdgcn_s_sleep(8);
    if (give_up(spins, tmo, 5)) break;
  }
  if (ACQ) {
    __builtin_amdgcn_fence(__ATOMIC_ACQUIRE, "agent");
    asm volatile("s_waitcnt vmcnt(0)" ::: "memory");
  }
}

__device__ __forceinline__ const char* uni(const char* p) {
  const unsigned long long v = (unsigned long long)p;
  const unsigned lo = __builtin_amdgcn_readfirstlane((unsigned)v), hi = __builtin_amdgcn_readfirstlane((unsigned)(v >> 32));
  return (const char*)(((unsigned long long)hi << 32) | lo);
}
#define TSCLK "s_memrealtime"
#define TC(k) do { } while (0)
#define TS(k) do { } while (0)
#define TSDECL do { } while (0)
#define TSFLUSH(mask) do { } while (0)

constexpr size_t OFF_MOD  = 0;
constexpr size_t OFF_MOD2 = 65536;
constexpr size_t OFF_TMO  = 98304;
constexpr size_t OFF_CAL  = 98304 + 256;
constexpr int    NCALW    = 16;
constexpr size_t OFF_CNT  = 131072;
constexpr int    NCNT     = 128;
constexpr size_t OFF_H1   = 262144;
constexpr size_t OFF_Q    = OFF_H1 + (size_t)SEQ * DM * 2;
constexpr size_t OFF_K    = OFF_Q + (size_t)SEQ * HD * 2;
constexpr size_t OFF_V    = OFF_K + (size_t)SEQ * HD * 2;
constexpr size_t OFF_O    = OFF_V + (size_t)SEQ * HD * 2;
constexpr size_t OFF_X1   = OFF_O + (size_t)SEQ * HD * 2;
constexpr size_t OFF_H2   = OFF_X1 + (size_t)SEQ * DM * 2;
constexpr size_t OFF_G    = OFF_H2 + (size_t)SEQ * DM * 2;
constexpr size_t OFF_KVM  = (size_t)92 << 20;
constexpr size_t OFF_XCH  = (size_t)100 << 20;
constexpr size_t OFF_P    = (size_t)112 << 20;
constexpr size_t OFF_P2   = (size_t)128 << 20;
constexpr size_t OFF_DUMP = (size_t)144 << 20;
constexpr int PROW = 4 * DM * 2;
#define TLQ 1
#define TLP 1
static_assert(OFF_G + (size_t)SEQ * FF * 2 <= OFF_KVM, "layout");
__device__ __forceinline__ char* xch_base(char* ws) { return (char*)(((size_t)(ws + OFF_XCH) + ((2u << 20) - 1)) & ~(size_t)((2u << 20) - 1)); }
#define XPAR 0
#define XPARF(sum) ((size_t)(((sum) > 0 ? 0 : 1) ^ XPAR))
constexpr int TICK = 0, ARMED = 8;
constexpr int PROG = 16;
constexpr int PQKV = 64;
constexpr int PL1 = 96;
constexpr int PSA = 128;
constexpr int PWO = 192;
constexpr int PLN = 224;
constexpr int PF2 = 320;
constexpr int PF1 = 448;
#define SDIR 56
#define SAW 0
constexpr int L_RED = 131072;
constexpr int L_SLOT = L_RED + 16384;
constexpr int L_GB = L_SLOT + 1024;
constexpr int MEGA_LDS = 163840;
static_assert(L_GB + 12288 + 2048 <= MEGA_LDS, "lds");
constexpr int NMODB = 256;
constexpr int NCALB = 3 * NCALW;
#define XPARC 0

__device__ __forceinline__ int bswz(int row) { return (((row >> 1) & 1) | (((row >> 3) & 1) << 1)) << 1; }
template <int OFF, bool SC1 = false>
__device__ __forceinline__ void glds16s(const char* sbase, unsigned voff, unsigned lds_base) {
  unsigned keep;
  if (SC1)
    asm volatile("s_mov_b32 %0, m0\n\ts_add_u32 m0, %3, %4\n\ts_nop 0\n\tglobal_load_lds_dwordx4 %1, %2 sc1\n\ts_mov_b32 m0, %0"
                 : "=&s"(keep) : "v"(voff), "s"(sbase), "s"(lds_base), "i"(OFF) : "memory", "scc");
  else
    asm volatile("s_mov_b32 %0, m0\n\ts_add_u32 m0, %3, %4\n\ts_nop 0\n\tglobal_load_lds_dwordx4 %1, %2\n\ts_mov_b32 m0, %0"
                 : "=&s"(keep) : "v"(voff), "s"(sbase), "s"(lds_base), "i"(OFF) : "memory", "scc");
}
template <int OFF>
__device__ __forceinline__ void glds16x2s(const char* sbase, unsigned voff, unsigned lds_base) {
  unsigned keep;
  asm volatile("s_mov_b32 %0, m0\n\ts_add_u32 m0, %3, %4\n\ts_nop 0\n\tglobal_load_lds_dwordx4 %1, %2 sc1\n\tglobal_load_lds_dwordx4 %1, %2 offset:1024 sc1\n\ts_mov_b32 m0, %0"
               : "=&s"(keep) : "v"(voff), "s"(sbase), "s"(lds_base), "i"(OFF) : "memory", "scc");
}
__device__ __forceinline__ void st16s(const char* sb, unsigned vo, u32x4 v) { asm volatile("global_store_dwordx4 %0, %1, %2 sc1\n\ts_nop 1" :: "v"(vo), "v"(v), "s"(sb) : "memory"); }
__device__ __forceinline__ void head_to_lds(const char* src, int lane, unsigned lds) {
  if (TLQ) {
    const int c = (lane & 15) ^ (lane >> 4);
    const unsigned v0 = (unsigned)((c >> 2) * 2048 + (lane >> 4) * 64 + (c & 3) * 16);
#pragma unroll
    for (int i = 0; i < 8; ++i) glds16s<0, true>(src + i * 256, v0 ^ ((i & 3) * 2048), lds + i * 1024);
  } else {
#pragma unroll
    for (int i = 0; i < 4; ++i) glds16x2s<0>(src + i * 2048, (unsigned)lane * 16, lds + i * 2048);
  }
}
#define WAIT_V(n) asm volatile("s_waitcnt vmcnt(" #n ")" ::: "memory")
#define BAR() do { asm volatile("s_waitcnt lgkmcnt(0)" ::: "memory"); __builtin_amdgcn_s_barrier(); asm volatile("" ::: "memory"); } while (0)
__device__ __forceinline__ void st4_sc1(void* p, unsigned v) { asm volatile("global_store_dword %0, %1, off sc1" :: "v"(p), "v"(v) : "memory"); }
__device__ __forceinline__ unsigned ld4_sc1(const void* p) {
  unsigned r; asm volatile("global_load_dword %0, %1, off sc1\n\ts_waitcnt vmcnt(0)" : "=&v"(r) : "v"(p) : "memory"); return r;
}
template <int CTRL> __device__ __forceinline__ unsigned dppu(unsigned x) { return (unsigned)__builtin_amdgcn_update_dpp(0, (int)x, CTRL, 0xf, 0xf, false); }
__device__ __forceinline__ unsigned rowmin16(unsigned v) {
  v = min(v, dppu<0x128>(v)); v = min(v, dppu<0x124>(v)); v = min(v, dppu<0x122>(v)); v = min(v, dppu<0x121>(v));
  return v;
}
__device__ __forceinline__ unsigned rdl(unsigned v, int l) { return (unsigned)__builtin_amdgcn_readlane((int)v, l); }
constexpr float SCALE = 0.08838834764831845f;
constexpr float LOG2E = 1.4426950408889634f;
template <int N> __device__ __forceinline__ void delay64() {
  if (N >= 16) { __builtin_amdgcn_s_sleep(15); delay64<(N >= 16 ? N - 15 : 0)>(); } else if (N > 0) __builtin_amdgcn_s_sleep(N);
}
__device__ __forceinline__ void sw16(float x, float& a, float& b) {
  a = x; b = x; asm volatile("s_nop 1\n\tv_permlane16_swap_b32 %0, %1" : "+v"(a), "+v"(b));
}
__device__ __forceinline__ void sw32(float x, float& a, float& b) {
  a = x; b = x; asm volatile("s_nop 1\n\tv_permlane32_swap_b32 %0, %1" : "+v"(a), "+v"(b));
}
__device__ __forceinline__ float red16_32_max(float x) {
  float a, b; sw16(x, a, b); x = fmaxf(a, b); sw32(x, a, b); return fmaxf(a, b);
}
__device__ __forceinline__ float red16_32_add(float x) {
  float a, b; sw16(x, a, b); x = a + b; sw32(x, a, b); return a + b;
}
__device__ __forceinline__ unsigned cvtpk(float a, float b) { unsigned r; asm("v_cvt_pk_bf16_f32 %0, %1, %2" : "=v"(r) : "v"(a), "v"(b)); return r; }
__device__ __forceinline__ float max3(float a, float b, float c) { float r; asm("v_max3_f32 %0, %1, %2, %3" : "=v"(r) : "v"(a), "v"(b), "v"(c)); return r; }
__device__ __forceinline__ float max2(float a, float b) { float r; asm("v_max_f32 %0, %1, %2" : "=v"(r) : "v"(a), "v"(b)); return r; }
__device__ __forceinline__ unsigned ror8u(unsigned x) { return (unsigned)__builtin_amdgcn_update_dpp(0, (int)x, 0x128, 0xf, 0xf, false); }
__device__ __forceinline__ float ror8(float x) { return __builtin_bit_cast(float, ror8u(__builtin_bit_cast(unsigned, x))); }
__device__ __forceinline__ float add_ror8(float x) {
  return x + __builtin_bit_cast(float, __builtin_amdgcn_update_dpp(0, __builtin_bit_cast(int, x), 0x128, 0xf, 0xf, false));
}

template <int M0> __device__ __forceinline__ unsigned qkv_avail(const unsigned* line) {
  const unsigned v = ld4_sc1(line + (lane_id() & 15));
  unsigned r = NSEG;
#pragma unroll
  for (int m = M0; m < 3; ++m) {
    unsigned a = rdl(v, m);
#pragma unroll
    for (int p = 1; p < 3; ++p) { const unsigned w = rdl(v, 4 * p + m); if (a >= (w >> 8)) a = max(a, w & 255u); }
    r = min(r, a);
  }
  return r;
}
__device__ __forceinline__ void wait_l1(const unsigned* cnt, unsigned n, unsigned* tmo) {
  const int lane = lane_id();
  for (int spins = 0;; ++spins) {
    const unsigned v = ld4_sc1(cnt + (PL1 + (lane & 31)) * 32);
    if (__all(v >= n)) break;
    __builtin_amdgcn_s_sleep(8);
    if (give_up(spins, tmo, 12)) break;
  }
}
#define GKV 1
__device__ __forceinline__ void wait_k0(const unsigned* cnt, int h, unsigned* tmo) {
  for (int spins = 0;; ++spins) {
    const int l = lane_id();
    const unsigned v = ld4_sc1(cnt + (PQKV + h) * 32 + (l == 0 ? 3 : 1 + (l & 1)));
    if (__all(v >= (l == 0 ? 1u : (unsigned)GKV))) break;
    __builtin_amdgcn_s_sleep(32);
    if (give_up(spins, tmo, 13)) break;
  }
}
#define KTON 0
#define WCLK(slot, b) do { } while (0)
#define WTM(slot, b, k) do { } while (0)
#define WTS(slot, b, k) do { } while (0)
#define KTS(k_) do { } while (0)
#define STS(ws_, slot_, k_) do { } while (0)
typedef __bf16 bf16x2 __attribute__((ext_vector_type(2)));
__device__ __forceinline__ f32x2 pkfma(f32x2 a, f32x2 b, f32x2 c) { return __builtin_elementwise_fma(a, b, c); }
__device__ __forceinline__ float wavesum(float v) {
  v += __builtin_bit_cast(float, dppu<0x128>(__builtin_bit_cast(unsigned, v))); v += __builtin_bit_cast(float, dppu<0x124>(__builtin_bit_cast(unsigned, v)));
  v += __builtin_bit_cast(float, dppu<0x122>(__builtin_bit_cast(unsigned, v))); v += __builtin_bit_cast(float, dppu<0x121>(__builtin_bit_cast(unsigned, v)));
  return red16_32_add(v);
}
__device__ __forceinline__ void bst16(rsrc_t r, unsigned off, unsigned soff, u32x4 v) { __builtin_amdgcn_raw_buffer_store_b128(v, r, off + soff, 0, 16); }
__device__ __forceinline__ float adds(unsigned v, unsigned sel, float c) { return __builtin_amdgcn_fdot2_f32_bf16(__builtin_bit_cast(bf16x2, v), __builtin_bit_cast(bf16x2, sel), c, false); }
__device__ __forceinline__ void sum4(const u32x4 (&p)[4], f32x2 (&a)[4]) {
  unsigned slo = 0x00003F80u, shi = 0x3F800000u;
  asm("" : "+s"(slo), "+s"(shi));
#pragma unroll
  for (int e = 0; e < 4; ++e) {
    float lo = bflo(p[0][e]), hi = bfhi(p[0][e]);
#pragma unroll
    for (int q = 1; q < 4; ++q) { lo = adds(p[q][e], slo, lo); hi = adds(p[q][e], shi, hi); }
    a[e] = f32x2{lo, hi};
  }
}
struct RowSet { u32x4 p[2][4]; u32x4 rv[2]; };
#define XAUX 16
__device__ __forceinline__ void row_issue2(RowSet& S, rsrc_t rp, rsrc_t rr, int row, unsigned l16) {
#pragma unroll
  for (int i = 0; i < 2; ++i) {
#pragma unroll
    for (int q = 0; q < 4; ++q) S.p[i][q] = TLP ? __builtin_amdgcn_raw_buffer_load_b128(rp, (l16 >> 6) * 2048 + (l16 & 48) + i * 32768, (unsigned)((row >> 5) * 32 * PROW + q * 65536 + (row & 31) * 64), 16)
                                                 : __builtin_amdgcn_raw_buffer_load_b128(rp, l16 + i * 1024, (unsigned)(row * PROW + q * 2048), 16);
    S.rv[i] = __builtin_amdgcn_raw_buffer_load_b128(rr, l16 + i * 1024, (unsigned)(row * DM) * 2, XAUX);
  }
}
__device__ __forceinline__ void row_a(const RowSet& S, f32x2 (&a)[2][4], u32x4 (&xv)[2]) {
#pragma unroll
  for (int i = 0; i < 2; ++i) { sum4(S.p[i], a[i]); xv[i] = S.rv[i]; }
#pragma unroll
  for (int i = 0; i < 2; ++i) asm volatile("" : "+v"(a[i][0]), "+v"(a[i][1]), "+v"(a[i][2]), "+v"(a[i][3]), "+v"(xv[i]));
}
__device__ __forceinline__ u32x4 resid8(const f32x2 (&a)[4], u32x4 xv, const char* mt, f32x2 (&v)[4]) {
  const f32x4 g0 = *(const f32x4*)mt, g1 = *(const f32x4*)(mt + 1024);
  u32x4 o;
  o[0] = [&] { const f32x2 s = pkfma(f32x2{g0[0], g0[1]}, a[0], f32x2{bflo(xv[0]), bfhi(xv[0])}); return pack2(s[0], s[1]); }();
  o[1] = [&] { const f32x2 s = pkfma(f32x2{g0[2], g0[3]}, a[1], f32x2{bflo(xv[1]), bfhi(xv[1])}); return pack2(s[0], s[1]); }();
  o[2] = [&] { const f32x2 s = pkfma(f32x2{g1[0], g1[1]}, a[2], f32x2{bflo(xv[2]), bfhi(xv[2])}); return pack2(s[0], s[1]); }();
  o[3] = [&] { const f32x2 s = pkfma(f32x2{g1[2], g1[3]}, a[3], f32x2{bflo(xv[3]), bfhi(xv[3])}); return pack2(s[0], s[1]); }();
#pragma unroll
  for (int e = 0; e < 4; ++e) v[e] = f32x2{bflo(o[e]), bfhi(o[e])};
  return o;
}
__device__ __forceinline__ u32x4 norm8(const f32x2 (&v)[4], const char* mt, float mb, float rden) {
  const f32x4 h0 = *(const f32x4*)(mt + 4096), h1 = *(const f32x4*)(mt + 4096 + 1024), c0 = *(const f32x4*)(mt + 8192), c1 = *(const f32x4*)(mt + 8192 + 1024);
  const f32x2 m2 = f32x2{mb, mb}, d2 = f32x2{rden, rden};
  const f32x2 r0 = pkfma(v[0] - m2, f32x2{c0[0], c0[1]} * d2, f32x2{h0[0], h0[1]}), r1 = pkfma(v[1] - m2, f32x2{c0[2], c0[3]} * d2, f32x2{h0[2], h0[3]});
  const f32x2 r2 = pkfma(v[2] - m2, f32x2{c1[0], c1[1]} * d2, f32x2{h1[0], h1[1]}), r3 = pkfma(v[3] - m2, f32x2{c1[2], c1[3]} * d2, f32x2{h1[2], h1[3]});
  return u32x4{pack2(r0[0], r0[1]), pack2(r1[0], r1[1]), pack2(r2[0], r2[1]), pack2(r3[0], r3[1])};
}
__device__ __forceinline__ void row_b(const f32x2 (&a)[2][4], const u32x4 (&xv)[2], const char* ml, rsrc_t rx1, rsrc_t rh2, int row, int lane) {
  f32x2 v[2][4];
  unsigned mo = lane * 16; asm volatile("" : "+v"(mo));
  ml += mo;
#pragma unroll
  for (int i = 0; i < 2; ++i) bst16(rx1, mo + i * 1024, (unsigned)(row * DM) * 2, resid8(a[i], xv[i], ml + i * 2048, v[i]));
  const f32x2 s2 = ((v[0][0] + v[0][1]) + (v[0][2] + v[0][3])) + ((v[1][0] + v[1][1]) + (v[1][2] + v[1][3]));
  const float mean = wavesum(s2[0] + s2[1]) * (1.0f / DM);
  f32x2 q2 = f32x2{0, 0};
#pragma unroll
  for (int i = 0; i < 8; ++i) { const f32x2 d = v[i >> 2][i & 3] - f32x2{mean, mean}; q2 = pkfma(d, d, q2); }
  const float q = wavesum(q2[0] + q2[1]);
  const float mb = bfr(mean), vb = bfr(q * (1.0f / DM));
  const float rden = 1.0f / bfr(sqrtf(bfr(vb + 1e-5f)));
  const int rb = row & 31;
  const unsigned hd = (unsigned)((((lane & 7) ^ (rb & 7)) << 4) + (lane >> 3) * 4096);
#pragma unroll
  for (int i = 0; i < 2; ++i) bst16(rh2, hd, (unsigned)((row >> 5) * 65536 + rb * 128 + i * 32768), norm8(v[i], ml + i * 2048, mb, rden));
}
__device__ __forceinline__ void half_load(const char* P, const u16* res, int row, int hf, int lane, f32x2 (&a)[4], u32x4& rv) {
  const rsrc_t rp = mkrs(P), rr = mkrs(res);
  const unsigned po = TLP ? (unsigned)((row >> 5) * 32 * PROW + (hf * 16 + (lane >> 2)) * 2048 + (row & 31) * 64 + (lane & 3) * 16) : (unsigned)(row * PROW + hf * 1024 + lane * 16), ro = (unsigned)(row * DM * 2 + hf * 1024 + lane * 16);
  u32x4 p[4];
#pragma unroll
  for (int q = 0; q < 4; ++q) p[q] = bld16(rp, po + q * (TLP ? 65536 : 2048));
  if (res) rv = bld16(rr, ro);
  sum4(p, a);
}
__device__ __forceinline__ void fin_half(const char* P, const u16* res, const char* gbt, u16* __restrict__ o1, int row, int hf, int lane) {
  f32x2 a[4]; u32x4 rv;
  half_load(P, res, row, hf, lane, a, rv);
  const u32x4 gv = *(const u32x4*)(gbt + hf * 1024 + lane * 16);
  u32x4 o;
#pragma unroll
  for (int e = 0; e < 4; ++e) { const f32x2 s = pkfma(f32x2{bflo(gv[e]), bfhi(gv[e])}, a[e], f32x2{bflo(rv[e]), bfhi(rv[e])}); o[e] = pack2(s[0], s[1]); }
  st16_sc1(o1 + (size_t)row * DM + hf * 512 + lane * 8, o);
}
__device__ __forceinline__ void ln_half(const u32x4 (&p)[4], u32x4 rv, const char* ml, volatile LAS float* xs, u16* x1, char* h2c, int row, int wave, int lane) {
  const int hf = wave & 1;
  f32x2 a[4], v[4];
  sum4(p, a);
  ml += hf * 2048 + lane * 16;
  st16_sc1(x1 + (size_t)row * DM + hf * 512 + lane * 8, resid8(a, rv, ml, v));
  const f32x2 s2 = (v[0] + v[1]) + (v[2] + v[3]);
  const float s = wavesum(s2[0] + s2[1]), mh = s * (2.0f / DM);
  f32x2 q2 = f32x2{0, 0};
#pragma unroll
  for (int i = 0; i < 4; ++i) { const f32x2 d = v[i] - f32x2{mh, mh}; q2 = pkfma(d, d, q2); }
  const float qh = wavesum(q2[0] + q2[1]);
  if (lane == 0) { xs[wave * 2] = s; xs[wave * 2 + 1] = qh; }
  BAR();
  const float so = xs[(wave ^ 1) * 2], qo = xs[(wave ^ 1) * 2 + 1];
  const float mean = (hf ? so + s : s + so) * (1.0f / DM), dm = (s - so) * (2.0f / DM);
  const float q = (hf ? qo + qh : qh + qo) + dm * dm * (DM / 4);
  const float mb = bfr(mean), vb = bfr(q * (1.0f / DM));
  const float rden = 1.0f / bfr(sqrtf(bfr(vb + 1e-5f)));
  const int rb = row & 31;
  st16_sc1(h2c + (size_t)(row >> 5) * 65536 + rb * 128 + (((lane & 7) ^ (rb & 7)) << 4) + (lane >> 3) * 4096 + hf * 32768, norm8(v, ml, mb, rden));
}
__device__ __forceinline__ void row_table(char* tab, const float* mod, const unsigned* cnt, unsigned* tmo, int tid) {
  wait_l1(cnt, 9u, tmo);
  const rsrc_t rm = mkrs((const char*)mod + OFF_MOD2);
#pragma unroll
  for (int i = 0; i < 2; ++i) {
    const int u = tid + 512 * i, a = u >> 8, c = u & 255;
    f32x4 v = __builtin_bit_cast(f32x4, bld16(rm, (unsigned)u * 16));
    v = f32x4{bfr(v[0]), bfr(v[1]), bfr(v[2]), bfr(v[3])};
    if (a == 2) v = f32x4{bfr(1.0f + v[0]), bfr(1.0f + v[1]), bfr(1.0f + v[2]), bfr(1.0f + v[3])};
    if (a < 3) *(f32x4*)(tab + a * 4096 + ((c >> 7) * 2 + (c & 1)) * 1024 + ((c >> 1) & 63) * 16) = v;
    else *(u32x2*)(tab + 12288 + c * 8) = u32x2{pack2(v[0], v[1]), pack2(v[2], v[3])};
  }
  __syncthreads();
}
#define LNB 56
__device__ __forceinline__ void row_role(char* smem, int wave_id_, int rbase, int par, const float* mod, const char* P, const u16* x, u16* x1, char* h2c,
                                         unsigned* cnt, unsigned* tmo, int slot, unsigned long long* mts) {
  const int tid = opaque_tid(), lane = tid & 63, wave = WAVE_ID;
  row_table(smem, mod, cnt, tmo, tid);
  const int rb = rbase + wave + 32 * par;
  const rsrc_t rx1 = mkrs(x1), rh2 = mkrs(h2c), rc = mkrs(cnt), rp = mkrs(P), rr = mkrs(x);
  const unsigned l16 = lane * 16;
  const unsigned pwo = (unsigned)(PWO + (lane & 31)) * 128;
  unsigned* myp = cnt + (PLN + (rb >> 3)) * 32 + wave;
  auto eval = [&](unsigned v) { v = rowmin16(v); return (int)((min(min(rdl(v, 0), rdl(v, 16)), (unsigned)LNB) + 1u - par) >> 1); };
  auto wmk = [&](int n) { return (unsigned)min(2 * n + par, LNB); };
  if (par && lane == 0) st4_sc1(myp, 1u);
  int b = 0, wa = 0;
  f32x2 a[2][4]; u32x4 xv[2];
  while (b < LNB / 2) {
    if (b >= wa) {
      for (int spins = 0;; ++spins) {
        wa = eval(ld4_sc1((const char*)cnt + pwo));
        if (b < wa) break;
        __builtin_amdgcn_s_sleep(2);
        if (give_up(spins, tmo, 11)) { wa = LNB / 2; break; }
      }
    }
    if ((b & 3) == 0 || b >= 28) { WTM(slot, 2 * b + par, 0); }
    if (wa >= b + 8) {
      RowSet S[4];
      const int b0 = b;
      asm volatile("; deep" ::: "memory");
#pragma unroll
      for (int i = 0; i < 4; ++i) row_issue2(S[i], rp, rr, (b + i) * 64 + rb, l16);
      while (b + 7 < wa) {
        const unsigned smp = __builtin_amdgcn_raw_buffer_load_b32(rc, pwo, 0, 16);
#pragma unroll
        for (int i = 0; i < 4; ++i) {
          row_a(S[i], a, xv);
          WAIT_V(42);
          if (lane == 0 && b > b0) st4_sc1(myp, wmk((b + i - 3)));
          __builtin_amdgcn_sched_barrier(0);
          row_issue2(S[i], rp, rr, (b + 4 + i) * 64 + rb, l16);
          __builtin_amdgcn_sched_barrier(0);
          row_b(a, xv, smem, rx1, rh2, (b + i) * 64 + rb, lane);
          __builtin_amdgcn_sched_barrier(0);
        }
        const int w = eval(smp);
        wa = w > wa ? w : wa;
        b += 4;
      }
#pragma unroll
      for (int i = 0; i < 4; ++i) {
        row_a(S[i], a, xv);
        __builtin_amdgcn_sched_barrier(0);
        row_b(a, xv, smem, rx1, rh2, (b + i) * 64 + rb, lane);
        WAIT_V(4);
        if (lane == 0) st4_sc1(myp, wmk((b + i)));
        __builtin_amdgcn_sched_barrier(0);
      }
      WAIT_V(0);
      b += 4;
      if (lane == 0) st4_sc1(myp, wmk(b));
      continue;
    }
    if (wa < b + 2) {
      RowSet T;
      asm volatile("; single" ::: "memory");
      row_issue2(T, rp, rr, b * 64 + rb, l16);
      row_a(T, a, xv);
      row_b(a, xv, smem, rx1, rh2, b * 64 + rb, lane);
      WAIT_V(0);
      ++b;
      if (lane == 0) st4_sc1(myp, wmk(b));
      WTM(slot, 2 * (b - 1) + par, 2);
      continue;
    }
    RowSet S0, S1;
    const int b0 = b;
    asm volatile("; multi" ::: "memory");
    row_issue2(S0, rp, rr, b * 64 + rb, l16);
    row_issue2(S1, rp, rr, (b + 1) * 64 + rb, l16);
    while (b + 3 < wa) {
      const unsigned smp = __builtin_amdgcn_raw_buffer_load_b32(rc, pwo, 0, 16);
      row_a(S0, a, xv);
      WAIT_V(15);
      if (lane == 0 && b > b0) st4_sc1(myp, wmk((b - 1)));
      __builtin_amdgcn_sched_barrier(0);
      row_issue2(S0, rp, rr, (b + 2) * 64 + rb, l16);
      __builtin_amdgcn_sched_barrier(0);
      row_b(a, xv, smem, rx1, rh2, b * 64 + rb, lane);
      __builtin_amdgcn_sched_barrier(0);
      row_a(S1, a, xv);
      WAIT_V(15);
      if (lane == 0 && b > b0) st4_sc1(myp, wmk(b));
      __builtin_amdgcn_sched_barrier(0);
      row_issue2(S1, rp, rr, (b + 3) * 64 + rb, l16);
      __builtin_amdgcn_sched_barrier(0);
      row_b(a, xv, smem, rx1, rh2, (b + 1) * 64 + rb, lane);
      __builtin_amdgcn_sched_barrier(0);
      const int w = eval(smp);
      wa = w > wa ? w : wa;
      b += 2;
    }
    row_a(S0, a, xv);
    row_b(a, xv, smem, rx1, rh2, b * 64 + rb, lane);
    row_a(S1, a, xv);
    WAIT_V(4 - 4);
    if (lane == 0) st4_sc1(myp, wmk((b + 1)));
    WTM(slot, 2 * b + par, 2);
    row_b(a, xv, smem, rx1, rh2, (b + 1) * 64 + rb, lane);
    WAIT_V(0);
    b += 2;
    if (lane == 0) st4_sc1(myp, wmk(b));
    WTM(slot, 2 * (b - 1) + par, 2);
  }
  static_assert(20480 + (NSEG - LNB) * 8192 <= MEGA_LDS && LNB % 2 == 0, "");
  const int hf = wave & 1, r4 = rbase + 4 * par + (wave >> 1);
  const unsigned pw2 = (unsigned)(PWO + ((lane >> 2) & 3) * 8 + hf * 4 + (lane & 3)) * 128;
  u32x4 xa[NSEG - LNB + 1];
#pragma unroll
  for (int i = 0; i < NSEG - LNB; ++i) xa[i] = __builtin_amdgcn_raw_buffer_load_b128(rr, (unsigned)(hf * 1024 + lane * 16), (unsigned)(((LNB + i) * 32 + r4) * DM) * 2, 0);
#pragma unroll
  for (int i = 0; i < NSEG - LNB; ++i) *(u32x4*)(smem + 20480 + (i * 8 + wave) * 1024 + lane * 16) = xa[i];
  static_assert(TLP, "");
  unsigned pv = 0;
  u32x4 p[4];
  bool req = false;
  const char* pb = uni(P); const char* cb = uni((const char*)cnt);
  auto request = [&](int bb, int ln) {
    const unsigned po = (unsigned)(bb * 32 * PROW + (hf * 16 + (ln >> 2)) * 2048 + r4 * 64 + (ln & 3) * 16);
    asm volatile("global_load_dwordx4 %0, %5, %9 sc1\n\tglobal_load_dwordx4 %1, %6, %9 sc1\n\tglobal_load_dwordx4 %2, %7, %9 sc1\n\tglobal_load_dwordx4 %3, %8, %9 sc1\n\t"
                 "global_load_dword %4, %10, %11 sc1\n\ts_waitcnt vmcnt(5)\n\ts_mov_b64 exec, 1\n\tglobal_store_dword %12, %13, off sc1\n\ts_mov_b64 exec, -1\n\ts_waitcnt vmcnt(1)"
                 : "=&v"(p[0]), "=&v"(p[1]), "=&v"(p[2]), "=&v"(p[3]), "=&v"(pv)
                 : "v"(po), "v"(po + 65536), "v"(po + 131072), "v"(po + 196608), "s"(pb), "v"(pw2), "s"(cb), "v"(myp), "v"((unsigned)bb) : "memory");
  };
  for (int bb = LNB; bb < NSEG; ++bb) {
    int ln = lane; asm volatile("" : "+v"(ln));
    const u32x4 rv = *(const u32x4*)(smem + 20480 + ((bb - LNB) * 8 + wave) * 1024 + ln * 16);
    if (!req) {
      for (int spins = 0; !__all(pv > (unsigned)bb); ++spins) {
        pv = ld4_sc1((const char*)cnt + pw2);
        if (__all(pv > (unsigned)bb)) break;
        __builtin_amdgcn_s_sleep(2);
        if (give_up(spins, tmo, 11)) break;
      }
    }
    WTM(slot, bb, 0);
    request(bb, ln);
    ln_half(p, rv, smem, (volatile LAS float*)(smem + 16384 + (bb & 1) * 64), x1, h2c, bb * 32 + r4, wave, ln);
    req = bb + 1 < NSEG && __all(pv > (unsigned)(bb + 1));
    if (!req) {
      if (bb + 1 < NSEG) pv = ld4_sc1((const char*)cnt + pw2);
      WAIT_V(0);
      if (lane == 0) st4_sc1(myp, (unsigned)(bb + 1));
    }
    WTM(slot, bb, 2);
  }
}
__device__ __forceinline__ void l1_role(char* smem, int wave_id_, int c, const u16* __restrict__ x, const float* __restrict__ mod, const u16* __restrict__ cvec,
                                        const u16* __restrict__ wmod, char* __restrict__ h1c, unsigned* cnt, unsigned* tmo, unsigned long long* mts) {
  const int tid = opaque_tid(), lane = tid & 63, wave = WAVE_ID;
  u32x4 xr[8][2];
  const u16* src = x + (size_t)(8 * c + wave) * DM + lane * 8;
  xr[0][0] = ldnt<2>(src); xr[0][1] = ldnt<2>(src + 512);
  {
    f32x4 m[8];
#pragma unroll
    for (int k = 0; k < 8; ++k) m[k] = *(const f32x4*)(mod + k * (2 * DM) + tid * 4);
    const float sva = bf2f(cvec[tid]), svb = bf2f(cvec[tid + 512]);
    f32x4 v = ((m[0] + m[1]) + (m[2] + m[3])) + ((m[4] + m[5]) + (m[6] + m[7]));
    v = f32x4{bfr(v[0]), bfr(v[1]), bfr(v[2]), bfr(v[3])};
    const int a = tid >> 8, cc = tid & 255;
    if (a) v = f32x4{bfr(1.0f + v[0]), bfr(1.0f + v[1]), bfr(1.0f + v[2]), bfr(1.0f + v[3])};
    *(f32x4*)(smem + 4096 + a * 4096 + ((cc >> 7) * 2 + (cc & 1)) * 1024 + ((cc >> 1) & 63) * 16) = v;
    float* sv = (float*)(smem + 12288);
    sv[tid] = bfr(sva / (1.0f + __expf(-sva))); sv[tid + 512] = bfr(svb / (1.0f + __expf(-svb)));
  }
  __syncthreads();
#pragma unroll
  for (int p = 0; p < 8; ++p) {
    f32x2 v[2][4];
#pragma unroll
    for (int i = 0; i < 2; ++i)
#pragma unroll
      for (int e = 0; e < 4; ++e) v[i][e] = f32x2{bflo(xr[p][i][e]), bfhi(xr[p][i][e])};
    const f32x2 s2 = ((v[0][0] + v[0][1]) + (v[0][2] + v[0][3])) + ((v[1][0] + v[1][1]) + (v[1][2] + v[1][3]));
    const float mean = wavesum(s2[0] + s2[1]) * (1.0f / DM);
    f32x2 q2 = f32x2{0, 0};
#pragma unroll
    for (int i = 0; i < 8; ++i) { const f32x2 d = v[i >> 2][i & 3] - f32x2{mean, mean}; q2 = pkfma(d, d, q2); }
    const float q = wavesum(q2[0] + q2[1]);
    if (p == 0 && wave == 0) STS((char*)cnt - OFF_CNT, 128 + (c >> 3) * 32 + 24 + (c & 7), 0);
    const float mb = bfr(mean), vb = bfr(q * (1.0f / DM));
    const float rden = 1.0f / bfr(sqrtf(bfr(vb + 1e-5f)));
    const int row = 256 * p + 8 * c + wave, rb = row & 31;
    char* dst = h1c + (size_t)(row >> 5) * 65536 + (lane >> 3) * 4096 + rb * 128 + (((lane & 7) ^ (rb & 7)) << 4);
    const char* ml = smem + lane * 16;
#pragma unroll
    for (int i = 0; i < 2; ++i) st16_sc1(dst + i * 32768, norm8(v[i], ml + i * 2048, mb, rden));
    WAIT_V(0);
    __builtin_amdgcn_s_barrier();
    if (tid == 0) st4_sc1(cnt + (PL1 + c) * 32, (unsigned)(p + 1));
    if (p == 0) {
#pragma unroll
      for (int pp = 1; pp < 8; ++pp) { xr[pp][0] = ldnt<2>(src + (size_t)pp * 256 * DM); xr[pp][1] = ldnt<2>(src + (size_t)pp * 256 * DM + 512); }
    }
    if (wave == 0) WTS(c, p, 0);
    if (p == 0 && wave == 0) STS((char*)cnt - OFF_CNT, 128 + (c >> 3) * 32 + 24 + (c & 7), 1);
  }
  wait_k0(cnt, c, tmo);
  {
    const int cg = tid & 15, r0 = tid >> 4;
    const u16* wp = wmod + (size_t)r0 * (6 * DM) + 2 * DM + 128 * c + cg * 8;
    const float* sv = (const float*)(smem + 12288) + r0;
    f32x2 acc[4] = {f32x2{0, 0}, f32x2{0, 0}, f32x2{0, 0}, f32x2{0, 0}};
#pragma unroll 1
    for (int o = 0; o < 4; ++o) {
      u32x4 w[8];
#pragma unroll
      for (int i = 0; i < 8; ++i) w[i] = ldnt<1>(wp + (size_t)(o * 8 + i) * 32 * (6 * DM));
#pragma unroll
      for (int i = 0; i < 8; ++i) {
        const float s = sv[(o * 8 + i) * 32];
#pragma unroll
        for (int e = 0; e < 4; ++e) acc[e] = pkfma(f32x2{s, s}, f32x2{bflo(w[i][e]), bfhi(w[i][e])}, acc[e]);
      }
    }
    float* red = (float*)(smem + 16384);
    *(f32x4*)(red + r0 * 128 + cg * 8) = f32x4{acc[0][0], acc[0][1], acc[1][0], acc[1][1]};
    *(f32x4*)(red + r0 * 128 + cg * 8 + 4) = f32x4{acc[2][0], acc[2][1], acc[3][0], acc[3][1]};
    __syncthreads();
    if (tid < 128) {
      float s = 0;
#pragma unroll
      for (int r = 0; r < 32; ++r) s += red[r * 128 + tid];
      st4_sc1((char*)mod + OFF_MOD2 + (128 * c + tid) * 4, __builtin_bit_cast(unsigned, s));
    }
    WAIT_V(0);
    __builtin_amdgcn_s_barrier();
    if (tid == 0) st4_sc1(cnt + (PL1 + c) * 32, 9u);
  }
}
enum { R_QKV = 0, R_PART = 1, R_F1 = 2, R_PART2 = 3 };
struct Dep {
  const char* base; unsigned voff; unsigned mul, add;
};
__device__ __forceinline__ unsigned dep_eval(unsigned v, const Dep& d) {
  v = rowmin16(v * d.mul + d.add);
  return min(min(rdl(v, 0), rdl(v, 16)), min(rdl(v, 32), rdl(v, 48)));
}
typedef unsigned u32x32 __attribute__((ext_vector_type(32)));
typedef unsigned u32x16 __attribute__((ext_vector_type(16)));
__device__ __forceinline__ void fast_ld0(const u32x32 (&W)[4], u32x16& pd, unsigned ea, unsigned da, int& b, int& lf, int cp, unsigned sa, unsigned mu, unsigned ad,
    unsigned rw, unsigned ab, const char* p1, const char* p0, unsigned l1, unsigned l0, const unsigned (&av)[8]) {
  asm volatile("s_setprio 0\n\t"
    "s_waitcnt vmcnt(12)\n\t"
    "s_barrier\n\t"
    "ds_read_b128 v[96:99], %[ea] offset:0\n\t"
    "ds_read_b128 v[100:103], %[ea] offset:2048\n\t"
    "ds_read_b128 v[104:107], %[da] offset:0\n\t"
    "ds_read_b128 v[108:111], %[da] offset:2048\n\t"
    "ds_read_b128 v[112:115], %[ea] offset:4096\n\t"
    "ds_read_b128 v[116:119], %[ea] offset:6144\n\t"
    "s_branch 2f\n\t"
    "1:\n\t"
    "s_waitcnt vmcnt(12)\n\t"
    "s_nop 1\n\t"
    "s_barrier\n\t"
    "2:\n\t"
    "s_waitcnt lgkmcnt(4)\n\t"
    "v_mfma_f32_16x16x32_bf16 v[64:67], v[128:131], v[96:99], 0\n\t"
    "ds_read_b128 v[120:123], %[da] offset:4096\n\t"
    "s_add_u32 m0, %[l1], 0\n\t"
    "ds_write_b128 %[rw], v[80:83] offset:0\n\t"
    "v_mfma_f32_16x16x32_bf16 v[68:71], v[132:135], v[96:99], 0\n\t"
    "ds_read_b128 v[124:127], %[da] offset:6144\n\t"
    "global_load_lds_dwordx4 %[av0], s[36:37] sc1\n\t"
    "ds_write_b128 %[rw], v[84:87] offset:1024\n\t"
    "v_mfma_f32_16x16x32_bf16 v[72:75], v[128:131], v[100:103], 0\n\t"
    "s_add_u32 m0, %[l1], 32768\n\t"
    "ds_write_b128 %[rw], v[88:91] offset:2048\n\t"
    "v_mfma_f32_16x16x32_bf16 v[76:79], v[132:135], v[100:103], 0\n\t"
    "global_load_lds_dwordx4 %[av1], s[36:37] sc1\n\t"
    "ds_write_b128 %[rw], v[92:95] offset:3072\n\t"
    "s_waitcnt lgkmcnt(8)\n\t"
    "v_mfma_f32_16x16x32_bf16 v[64:67], v[136:139], v[104:107], v[64:67]\n\t"
    "ds_read_b128 v[96:99], %[ea] offset:8192\n\t"
    "s_add_u32 m0, %[l1], 4096\n\t"
    "v_mfma_f32_16x16x32_bf16 v[68:71], v[140:143], v[104:107], v[68:71]\n\t"
    "ds_read_b128 v[100:103], %[ea] offset:10240\n\t"
    "global_load_lds_dwordx4 %[av2], s[36:37] sc1\n\t"
    "v_mfma_f32_16x16x32_bf16 v[72:75], v[136:139], v[108:111], v[72:75]\n\t"
    "s_add_u32 m0, %[l1], 36864\n\t"
    "v_mfma_f32_16x16x32_bf16 v[76:79], v[140:143], v[108:111], v[76:79]\n\t"
    "global_load_lds_dwordx4 %[av3], s[36:37] sc1\n\t"
    "s_waitcnt lgkmcnt(8)\n\t"
    "v_mfma_f32_16x16x32_bf16 v[64:67], v[144:147], v[112:115], v[64:67]\n\t"
    "ds_read_b128 v[104:107], %[da] offset:8192\n\t"
    "s_add_u32 m0, %[l1], 8192\n\t"
    "v_mfma_f32_16x16x32_bf16 v[68:71], v[148:151], v[112:115], v[68:71]\n\t"
    "ds_read_b128 v[108:111], %[da] offset:10240\n\t"
    "global_load_lds_dwordx4 %[av4], s[36:37] sc1\n\t"
    "v_mfma_f32_16x16x32_bf16 v[72:75], v[144:147], v[116:119], v[72:75]\n\t"
    "s_add_u32 m0, %[l1], 40960\n\t"
    "v_mfma_f32_16x16x32_bf16 v[76:79], v[148:151], v[116:119], v[76:79]\n\t"
    "global_load_lds_dwordx4 %[av5], s[36:37] sc1\n\t"
    "s_waitcnt lgkmcnt(7)\n\t"
    "v_mfma_f32_16x16x32_bf16 v[64:67], v[152:155], v[120:123], v[64:67]\n\t"
    "ds_read_b128 v[112:115], %[ea] offset:12288\n\t"
    "s_add_u32 m0, %[l1], 12288\n\t"
    "v_mfma_f32_16x16x32_bf16 v[68:71], v[156:159], v[120:123], v[68:71]\n\t"
    "ds_read_b128 v[116:119], %[ea] offset:14336\n\t"
    "global_load_lds_dwordx4 %[av6], s[36:37] sc1\n\t"
    "v_mfma_f32_16x16x32_bf16 v[72:75], v[152:155], v[124:127], v[72:75]\n\t"
    "s_add_u32 m0, %[l1], 45056\n\t"
    "v_mfma_f32_16x16x32_bf16 v[76:79], v[156:159], v[124:127], v[76:79]\n\t"
    "global_load_lds_dwordx4 %[av7], s[36:37] sc1\n\t"
    "s_waitcnt lgkmcnt(4)\n\t"
    "v_mfma_f32_16x16x32_bf16 v[64:67], v[160:163], v[96:99], v[64:67]\n\t"
    "ds_read_b128 v[120:123], %[da] offset:12288\n\t"
    "v_mfma_f32_16x16x32_bf16 v[68:71], v[164:167], v[96:99], v[68:71]\n\t"
    "ds_read_b128 v[124:127], %[da] offset:14336\n\t"
    "v_mfma_f32_16x16x32_bf16 v[72:75], v[160:163], v[100:103], v[72:75]\n\t"
    "v_mfma_f32_16x16x32_bf16 v[76:79], v[164:167], v[100:103], v[76:79]\n\t"
    "s_waitcnt lgkmcnt(4)\n\t"
    "v_mfma_f32_16x16x32_bf16 v[64:67], v[168:171], v[104:107], v[64:67]\n\t"
    "ds_read_b128 v[96:99], %[ea] offset:16384\n\t"
    "v_mfma_f32_16x16x32_bf16 v[68:71], v[172:175], v[104:107], v[68:71]\n\t"
    "ds_read_b128 v[100:103], %[ea] offset:18432\n\t"
    "v_mfma_f32_16x16x32_bf16 v[72:75], v[168:171], v[108:111], v[72:75]\n\t"
    "v_mfma_f32_16x16x32_bf16 v[76:79], v[172:175], v[108:111], v[76:79]\n\t"
    "s_waitcnt lgkmcnt(4)\n\t"
    "v_mfma_f32_16x16x32_bf16 v[64:67], v[176:179], v[112:115], v[64:67]\n\t"
    "ds_read_b128 v[104:107], %[da] offset:16384\n\t"
    "v_mfma_f32_16x16x32_bf16 v[68:71], v[180:183], v[112:115], v[68:71]\n\t"
    "ds_read_b128 v[108:111], %[da] offset:18432\n\t"
    "v_mfma_f32_16x16x32_bf16 v[72:75], v[176:179], v[116:119], v[72:75]\n\t"
    "v_mfma_f32_16x16x32_bf16 v[76:79], v[180:183], v[116:119], v[76:79]\n\t"
    "s_waitcnt lgkmcnt(4)\n\t"
    "v_mfma_f32_16x16x32_bf16 v[64:67], v[184:187], v[120:123], v[64:67]\n\t"
    "ds_read_b128 v[112:115], %[ea] offset:20480\n\t"
    "v_mfma_f32_16x16x32_bf16 v[68:71], v[188:191], v[120:123], v[68:71]\n\t"
    "ds_read_b128 v[116:119], %[ea] offset:22528\n\t"
    "v_mfma_f32_16x16x32_bf16 v[72:75], v[184:187], v[124:127], v[72:75]\n\t"
    "v_mfma_f32_16x16x32_bf16 v[76:79], v[188:191], v[124:127], v[76:79]\n\t"
    "s_waitcnt vmcnt(12)\n\t"
    "s_barrier\n\t"
    "s_waitcnt lgkmcnt(4)\n\t"
    "v_mfma_f32_16x16x32_bf16 v[64:67], v[192:195], v[96:99], v[64:67]\n\t"
    "ds_read_b128 v[120:123], %[da] offset:20480\n\t"
    "s_add_u32 m0, %[l0], 0\n\t"
    "v_mfma_f32_16x16x32_bf16 v[68:71], v[196:199], v[96:99], v[68:71]\n\t"
    "ds_read_b128 v[124:127], %[da] offset:22528\n\t"
    "global_load_lds_dwordx4 %[av0], s[38:39] sc1\n\t"
    "v_mfma_f32_16x16x32_bf16 v[72:75], v[192:195], v[100:103], v[72:75]\n\t"
    "s_add_u32 m0, %[l0], 32768\n\t"
    "v_mfma_f32_16x16x32_bf16 v[76:79], v[196:199], v[100:103], v[76:79]\n\t"
    "global_load_lds_dwordx4 %[av1], s[38:39] sc1\n\t"
    "s_waitcnt lgkmcnt(4)\n\t"
    "v_mfma_f32_16x16x32_bf16 v[64:67], v[200:203], v[104:107], v[64:67]\n\t"
    "ds_read_b128 v[96:99], %[ea] offset:24576\n\t"
    "s_add_u32 m0, %[l0], 4096\n\t"
    "v_mfma_f32_16x16x32_bf16 v[68:71], v[204:207], v[104:107], v[68:71]\n\t"
    "ds_read_b128 v[100:103], %[ea] offset:26624\n\t"
    "global_load_lds_dwordx4 %[av2], s[38:39] sc1\n\t"
    "v_mfma_f32_16x16x32_bf16 v[72:75], v[200:203], v[108:111], v[72:75]\n\t"
    "s_add_u32 m0, %[l0], 36864\n\t"
    "v_mfma_f32_16x16x32_bf16 v[76:79], v[204:207], v[108:111], v[76:79]\n\t"
    "global_load_lds_dwordx4 %[av3], s[38:39] sc1\n\t"
    "s_add_u32 s36, s36, %[ab]\n\t"
    "s_addc_u32 s37, s37, 0\n\t"
    "s_waitcnt lgkmcnt(4)\n\t"
    "v_mfma_f32_16x16x32_bf16 v[64:67], v[208:211], v[112:115], v[64:67]\n\t"
    "ds_read_b128 v[104:107], %[da] offset:24576\n\t"
    "s_add_u32 m0, %[l0], 8192\n\t"
    "v_mfma_f32_16x16x32_bf16 v[68:71], v[212:215], v[112:115], v[68:71]\n\t"
    "ds_read_b128 v[108:111], %[da] offset:26624\n\t"
    "global_load_lds_dwordx4 %[av4], s[38:39] sc1\n\t"
    "v_mfma_f32_16x16x32_bf16 v[72:75], v[208:211], v[116:119], v[72:75]\n\t"
    "ds_read_b32 v39, %[sa]\n\t"
    "s_add_u32 m0, %[l0], 40960\n\t"
    "v_mfma_f32_16x16x32_bf16 v[76:79], v[212:215], v[116:119], v[76:79]\n\t"
    "v_xor_b32 %[sa], 0x100, %[sa]\n\t"
    "global_load_lds_dwordx4 %[av5], s[38:39] sc1\n\t"
    "s_xor_b32 %[l1], %[l1], 0x10000\n\t"
    "s_waitcnt lgkmcnt(5)\n\t"
    "v_mfma_f32_16x16x32_bf16 v[64:67], v[216:219], v[120:123], v[64:67]\n\t"
    "ds_read_b128 v[112:115], %[ea] offset:28672\n\t"
    "s_add_u32 m0, %[l0], 12288\n\t"
    "v_mfma_f32_16x16x32_bf16 v[68:71], v[220:223], v[120:123], v[68:71]\n\t"
    "ds_read_b128 v[116:119], %[ea] offset:30720\n\t"
    "global_load_lds_dwordx4 %[av6], s[38:39] sc1\n\t"
    "v_mfma_f32_16x16x32_bf16 v[72:75], v[216:219], v[124:127], v[72:75]\n\t"
    "v_xor_b32 %[ea], 0x10000, %[ea]\n\t"
    "s_add_u32 m0, %[l0], 45056\n\t"
    "v_mfma_f32_16x16x32_bf16 v[76:79], v[220:223], v[124:127], v[76:79]\n\t"
    "global_load_lds_dwordx4 %[av7], s[38:39] sc1\n\t"
    "s_waitcnt lgkmcnt(5)\n\t"
    "v_mfma_f32_16x16x32_bf16 v[64:67], v[224:227], v[96:99], v[64:67]\n\t"
    "ds_read_b128 v[120:123], %[da] offset:28672\n\t"
    "v_mfma_f32_16x16x32_bf16 v[68:71], v[228:231], v[96:99], v[68:71]\n\t"
    "ds_read_b128 v[124:127], %[da] offset:30720\n\t"
    "v_mfma_f32_16x16x32_bf16 v[72:75], v[224:227], v[100:103], v[72:75]\n\t"
    "v_xor_b32 %[da], 0x10000, %[da]\n\t"
    "v_mfma_f32_16x16x32_bf16 v[76:79], v[228:231], v[100:103], v[76:79]\n\t"
    "s_waitcnt lgkmcnt(5)\n\t"
    "v_mfma_f32_16x16x32_bf16 v[64:67], v[232:235], v[104:107], v[64:67]\n\t"
    "ds_read_b128 v[96:99], %[ea] offset:0\n\t"
    "v_mfma_f32_16x16x32_bf16 v[68:71], v[236:239], v[104:107], v[68:71]\n\t"
    "ds_read_b128 v[100:103], %[ea] offset:2048\n\t"
    "v_mfma_f32_16x16x32_bf16 v[72:75], v[232:235], v[108:111], v[72:75]\n\t"
    "v_mfma_f32_16x16x32_bf16 v[76:79], v[236:239], v[108:111], v[76:79]\n\t"
    "s_waitcnt lgkmcnt(4)\n\t"
    "v_mfma_f32_16x16x32_bf16 v[64:67], v[240:243], v[112:115], v[64:67]\n\t"
    "ds_read_b128 v[104:107], %[da] offset:0\n\t"
    "v_mad_u32_u24 v39, v39, %[mu], %[ad]\n\t"
    "v_mfma_f32_16x16x32_bf16 v[68:71], v[244:247], v[112:115], v[68:71]\n\t"
    "ds_read_b128 v[108:111], %[da] offset:2048\n\t"
    "v_cmp_ge_i32 vcc, %[lf], v39\n\t"
    "s_cmp_eq_u64 vcc, 0\n\t"
    "s_addc_u32 %[lf], %[lf], 0\n\t"
    "v_mfma_f32_16x16x32_bf16 v[72:75], v[240:243], v[116:119], v[72:75]\n\t"
    "v_mfma_f32_16x16x32_bf16 v[76:79], v[244:247], v[116:119], v[76:79]\n\t"
    "v_cmp_ge_i32 vcc, %[lf], v39\n\t"
    "s_cmp_eq_u64 vcc, 0\n\t"
    "s_addc_u32 %[lf], %[lf], 0\n\t"
    "s_waitcnt lgkmcnt(4)\n\t"
    "v_mfma_f32_16x16x32_bf16 v[80:83], v[248:251], v[120:123], v[64:67]\n\t"
    "ds_read_b128 v[112:115], %[ea] offset:4096\n\t"
    "s_min_i32 %[lf], %[lf], %[cp]\n\t"
    "s_add_u32 %[b], %[b], 1\n\t"
    "v_mfma_f32_16x16x32_bf16 v[84:87], v[252:255], v[120:123], v[68:71]\n\t"
    "ds_read_b128 v[116:119], %[ea] offset:6144\n\t"
    "v_mfma_f32_16x16x32_bf16 v[88:91], v[248:251], v[124:127], v[72:75]\n\t"
    "v_mfma_f32_16x16x32_bf16 v[92:95], v[252:255], v[124:127], v[76:79]\n\t"
    "s_add_u32 s38, s38, %[ab]\n\t"
    "s_addc_u32 s39, s39, 0\n\t"
    "s_xor_b32 %[l0], %[l0], 0x10000\n\t"
    "s_cmp_lt_i32 %[b], %[lf]\n\t"
    "s_cbranch_scc1 1b\n\t"
    "s_waitcnt lgkmcnt(0)\n\t"
    "s_nop 4"
    : "=&{v[80:95]}"(pd), [ea] "+&v"(ea), [da] "+&v"(da), [b] "+&s"(b), [lf] "+&s"(lf), "+{s[36:37]}"(p1), "+{s[38:39]}"(p0), [sa] "+&v"(sa), [l1] "+&s"(l1), [l0] "+&s"(l0)
    : [rw] "v"(rw), [ab] "s"(ab), [cp] "s"(cp), [mu] "s"(mu), [ad] "v"(ad), "{v[128:159]}"(W[0]), "{v[160:191]}"(W[1]), "{v[192:223]}"(W[2]), "{v[224:255]}"(W[3]), [av0] "v"(av[0]), [av1] "v"(av[1]), [av2] "v"(av[2]), [av3] "v"(av[3]), [av4] "v"(av[4]), [av5] "v"(av[5]), [av6] "v"(av[6]), [av7] "v"(av[7])
    : "v39", "v64", "v65", "v66", "v67", "v68", "v69", "v70", "v71", "v72", "v73", "v74", "v75", "v76", "v77", "v78", "v79", "v96", "v97", "v98", "v99", "v100", "v101", "v102", "v103", "v104", "v105", "v106", "v107", "v108", "v109", "v110", "v111", "v112", "v113", "v114", "v115", "v116", "v117", "v118", "v119", "v120", "v121", "v122", "v123", "v124", "v125", "v126", "v127", "vcc", "scc", "m0", "memory");
}
__device__ __forceinline__ void fast_ld2(const u32x32 (&W)[4], u32x16& pd, unsigned ea, unsigned da, int& b, int& lf, int cp, unsigned sa, unsigned mu, unsigned ad,
    unsigned rw, unsigned ab, const char* p1, const char* p0, unsigned l1, unsigned l0, const unsigned (&av)[8]) {
  asm volatile("s_setprio 0\n\t"
    "s_waitcnt vmcnt(6)\n\t"
    "s_barrier\n\t"
    "ds_read_b128 v[96:99], %[ea] offset:0\n\t"
    "ds_read_b128 v[100:103], %[ea] offset:2048\n\t"
    "ds_read_b128 v[104:107], %[da] offset:0\n\t"
    "ds_read_b128 v[108:111], %[da] offset:2048\n\t"
    "ds_read_b128 v[112:115], %[ea] offset:4096\n\t"
    "ds_read_b128 v[116:119], %[ea] offset:6144\n\t"
    "s_branch 2f\n\t"
    "1:\n\t"
    "s_waitcnt vmcnt(6)\n\t"
    "s_nop 1\n\t"
    "s_barrier\n\t"
    "2:\n\t"
    "s_waitcnt lgkmcnt(4)\n\t"
    "v_mfma_f32_16x16x32_bf16 v[64:67], v[128:131], v[96:99], 0\n\t"
    "ds_read_b128 v[120:123], %[da] offset:4096\n\t"
    "s_add_u32 m0, %[l1], 0\n\t"
    "ds_write_b128 %[rw], v[80:83] offset:0\n\t"
    "v_mfma_f32_16x16x32_bf16 v[68:71], v[132:135], v[96:99], 0\n\t"
    "ds_read_b128 v[124:127], %[da] offset:6144\n\t"
    "global_load_lds_dwordx4 %[av0], s[36:37] sc1\n\t"
    "ds_write_b128 %[rw], v[84:87] offset:1024\n\t"
    "v_mfma_f32_16x16x32_bf16 v[72:75], v[128:131], v[100:103], 0\n\t"
    "ds_write_b128 %[rw], v[88:91] offset:2048\n\t"
    "v_mfma_f32_16x16x32_bf16 v[76:79], v[132:135], v[100:103], 0\n\t"
    "ds_write_b128 %[rw], v[92:95] offset:3072\n\t"
    "s_waitcnt lgkmcnt(8)\n\t"
    "v_mfma_f32_16x16x32_bf16 v[64:67], v[136:139], v[104:107], v[64:67]\n\t"
    "ds_read_b128 v[96:99], %[ea] offset:8192\n\t"
    "s_add_u32 m0, %[l1], 4096\n\t"
    "v_mfma_f32_16x16x32_bf16 v[68:71], v[140:143], v[104:107], v[68:71]\n\t"
    "ds_read_b128 v[100:103], %[ea] offset:10240\n\t"
    "global_load_lds_dwordx4 %[av2], s[36:37] sc1\n\t"
    "v_mfma_f32_16x16x32_bf16 v[72:75], v[136:139], v[108:111], v[72:75]\n\t"
    "v_mfma_f32_16x16x32_bf16 v[76:79], v[140:143], v[108:111], v[76:79]\n\t"
    "s_waitcnt lgkmcnt(8)\n\t"
    "v_mfma_f32_16x16x32_bf16 v[64:67], v[144:147], v[112:115], v[64:67]\n\t"
    "ds_read_b128 v[104:107], %[da] offset:8192\n\t"
    "s_add_u32 m0, %[l1], 8192\n\t"
    "v_mfma_f32_16x16x32_bf16 v[68:71], v[148:151], v[112:115], v[68:71]\n\t"
    "ds_read_b128 v[108:111], %[da] offset:10240\n\t"
    "global_load_lds_dwordx4 %[av4], s[36:37] sc1\n\t"
    "v_mfma_f32_16x16x32_bf16 v[72:75], v[144:147], v[116:119], v[72:75]\n\t"
    "v_mfma_f32_16x16x32_bf16 v[76:79], v[148:151], v[116:119], v[76:79]\n\t"
    "s_waitcnt lgkmcnt(7)\n\t"
    "v_mfma_f32_16x16x32_bf16 v[64:67], v[152:155], v[120:123], v[64:67]\n\t"
    "ds_read_b128 v[112:115], %[ea] offset:12288\n\t"
    "s_add_u32 m0, %[l1], 12288\n\t"
    "v_mfma_f32_16x16x32_bf16 v[68:71], v[156:159], v[120:123], v[68:71]\n\t"
    "ds_read_b128 v[116:119], %[ea] offset:14336\n\t"
    "global_load_lds_dwordx4 %[av6], s[36:37] sc1\n\t"
    "v_mfma_f32_16x16x32_bf16 v[72:75], v[152:155], v[124:127], v[72:75]\n\t"
    "v_mfma_f32_16x16x32_bf16 v[76:79], v[156:159], v[124:127], v[76:79]\n\t"
    "s_waitcnt lgkmcnt(4)\n\t"
    "v_mfma_f32_16x16x32_bf16 v[64:67], v[160:163], v[96:99], v[64:67]\n\t"
    "ds_read_b128 v[120:123], %[da] offset:12288\n\t"
    "v_mfma_f32_16x16x32_bf16 v[68:71], v[164:167], v[96:99], v[68:71]\n\t"
    "ds_read_b128 v[124:127], %[da] offset:14336\n\t"
    "v_mfma_f32_16x16x32_bf16 v[72:75], v[160:163], v[100:103], v[72:75]\n\t"
    "v_mfma_f32_16x16x32_bf16 v[76:79], v[164:167], v[100:103], v[76:79]\n\t"
    "s_waitcnt lgkmcnt(4)\n\t"
    "v_mfma_f32_16x16x32_bf16 v[64:67], v[168:171], v[104:107], v[64:67]\n\t"
    "ds_read_b128 v[96:99], %[ea] offset:16384\n\t"
    "v_mfma_f32_16x16x32_bf16 v[68:71], v[172:175], v[104:107], v[68:71]\n\t"
    "ds_read_b128 v[100:103], %[ea] offset:18432\n\t"
    "v_mfma_f32_16x16x32_bf16 v[72:75], v[168:171], v[108:111], v[72:75]\n\t"
    "v_mfma_f32_16x16x32_bf16 v[76:79], v[172:175], v[108:111], v[76:79]\n\t"
    "s_waitcnt lgkmcnt(4)\n\t"
    "v_mfma_f32_16x16x32_bf16 v[64:67], v[176:179], v[112:115], v[64:67]\n\t"
    "ds_read_b128 v[104:107], %[da] offset:16384\n\t"
    "v_mfma_f32_16x16x32_bf16 v[68:71], v[180:183], v[112:115], v[68:71]\n\t"
    "ds_read_b128 v[108:111], %[da] offset:18432\n\t"
    "v_mfma_f32_16x16x32_bf16 v[72:75], v[176:179], v[116:119], v[72:75]\n\t"
    "v_mfma_f32_16x16x32_bf16 v[76:79], v[180:183], v[116:119], v[76:79]\n\t"
    "s_waitcnt lgkmcnt(4)\n\t"
    "v_mfma_f32_16x16x32_bf16 v[64:67], v[184:187], v[120:123], v[64:67]\n\t"
    "ds_read_b128 v[112:115], %[ea] offset:20480\n\t"
    "v_mfma_f32_16x16x32_bf16 v[68:71], v[188:191], v[120:123], v[68:71]\n\t"
    "ds_read_b128 v[116:119], %[ea] offset:22528\n\t"
    "v_mfma_f32_16x16x32_bf16 v[72:75], v[184:187], v[124:127], v[72:75]\n\t"
    "v_mfma_f32_16x16x32_bf16 v[76:79], v[188:191], v[124:127], v[76:79]\n\t"
    "s_waitcnt vmcnt(6)\n\t"
    "s_barrier\n\t"
    "s_waitcnt lgkmcnt(4)\n\t"
    "v_mfma_f32_16x16x32_bf16 v[64:67], v[192:195], v[96:99], v[64:67]\n\t"
    "ds_read_b128 v[120:123], %[da] offset:20480\n\t"
    "s_add_u32 m0, %[l0], 0\n\t"
    "v_mfma_f32_16x16x32_bf16 v[68:71], v[196:199], v[96:99], v[68:71]\n\t"
    "ds_read_b128 v[124:127], %[da] offset:22528\n\t"
    "global_load_lds_dwordx4 %[av0], s[38:39] sc1\n\t"
    "v_mfma_f32_16x16x32_bf16 v[72:75], v[192:195], v[100:103], v[72:75]\n\t"
    "v_mfma_f32_16x16x32_bf16 v[76:79], v[196:199], v[100:103], v[76:79]\n\t"
    "s_waitcnt lgkmcnt(4)\n\t"
    "v_mfma_f32_16x16x32_bf16 v[64:67], v[200:203], v[104:107], v[64:67]\n\t"
    "ds_read_b128 v[96:99], %[ea] offset:24576\n\t"
    "s_add_u32 m0, %[l0], 4096\n\t"
    "v_mfma_f32_16x16x32_bf16 v[68:71], v[204:207], v[104:107], v[68:71]\n\t"
    "ds_read_b128 v[100:103], %[ea] offset:26624\n\t"
    "global_load_lds_dwordx4 %[av2], s[38:39] sc1\n\t"
    "v_mfma_f32_16x16x32_bf16 v[72:75], v[200:203], v[108:111], v[72:75]\n\t"
    "v_mfma_f32_16x16x32_bf16 v[76:79], v[204:207], v[108:111], v[76:79]\n\t"
    "s_add_u32 s36, s36, %[ab]\n\t"
    "s_addc_u32 s37, s37, 0\n\t"
    "s_waitcnt lgkmcnt(4)\n\t"
    "v_mfma_f32_16x16x32_bf16 v[64:67], v[208:211], v[112:115], v[64:67]\n\t"
    "ds_read_b128 v[104:107], %[da] offset:24576\n\t"
    "s_add_u32 m0, %[l0], 8192\n\t"
    "v_mfma_f32_16x16x32_bf16 v[68:71], v[212:215], v[112:115], v[68:71]\n\t"
    "ds_read_b128 v[108:111], %[da] offset:26624\n\t"
    "global_load_lds_dwordx4 %[av4], s[38:39] sc1\n\t"
    "v_mfma_f32_16x16x32_bf16 v[72:75], v[208:211], v[116:119], v[72:75]\n\t"
    "ds_read_b32 v39, %[sa]\n\t"
    "v_mfma_f32_16x16x32_bf16 v[76:79], v[212:215], v[116:119], v[76:79]\n\t"
    "v_xor_b32 %[sa], 0x100, %[sa]\n\t"
    "s_xor_b32 %[l1], %[l1], 0x10000\n\t"
    "s_waitcnt lgkmcnt(5)\n\t"
    "v_mfma_f32_16x16x32_bf16 v[64:67], v[216:219], v[120:123], v[64:67]\n\t"
    "ds_read_b128 v[112:115], %[ea] offset:28672\n\t"
    "s_add_u32 m0, %[l0], 12288\n\t"
    "v_mfma_f32_16x16x32_bf16 v[68:71], v[220:223], v[120:123], v[68:71]\n\t"
    "ds_read_b128 v[116:119], %[ea] offset:30720\n\t"
    "global_load_lds_dwordx4 %[av6], s[38:39] sc1\n\t"
    "v_mfma_f32_16x16x32_bf16 v[72:75], v[216:219], v[124:127], v[72:75]\n\t"
    "v_xor_b32 %[ea], 0x10000, %[ea]\n\t"
    "v_mfma_f32_16x16x32_bf16 v[76:79], v[220:223], v[124:127], v[76:79]\n\t"
    "s_waitcnt lgkmcnt(5)\n\t"
    "v_mfma_f32_16x16x32_bf16 v[64:67], v[224:227], v[96:99], v[64:67]\n\t"
    "ds_read_b128 v[120:123], %[da] offset:28672\n\t"
    "v_mfma_f32_16x16x32_bf16 v[68:71], v[228:231], v[96:99], v[68:71]\n\t"
    "ds_read_b128 v[124:127], %[da] offset:30720\n\t"
    "v_mfma_f32_16x16x32_bf16 v[72:75], v[224:227], v[100:103], v[72:75]\n\t"
    "v_xor_b32 %[da], 0x10000, %[da]\n\t"
    "v_mfma_f32_16x16x32_bf16 v[76:79], v[228:231], v[100:103], v[76:79]\n\t"
    "s_waitcnt lgkmcnt(5)\n\t"
    "v_mfma_f32_16x16x32_bf16 v[64:67], v[232:235], v[104:107], v[64:67]\n\t"
    "ds_read_b128 v[96:99], %[ea] offset:0\n\t"
    "v_mfma_f32_16x16x32_bf16 v[68:71], v[236:239], v[104:107], v[68:71]\n\t"
    "ds_read_b128 v[100:103], %[ea] offset:2048\n\t"
    "v_mfma_f32_16x16x32_bf16 v[72:75], v[232:235], v[108:111], v[72:75]\n\t"
    "v_mfma_f32_16x16x32_bf16 v[76:79], v[236:239], v[108:111], v[76:79]\n\t"
    "s_waitcnt lgkmcnt(4)\n\t"
    "v_mfma_f32_16x16x32_bf16 v[64:67], v[240:243], v[112:115], v[64:67]\n\t"
    "ds_read_b128 v[104:107], %[da] offset:0\n\t"
    "v_mad_u32_u24 v39, v39, %[mu], %[ad]\n\t"
    "v_mfma_f32_16x16x32_bf16 v[68:71], v[244:247], v[112:115], v[68:71]\n\t"
    "ds_read_b128 v[108:111], %[da] offset:2048\n\t"
    "v_cmp_ge_i32 vcc, %[lf], v39\n\t"
    "s_cmp_eq_u64 vcc, 0\n\t"
    "s_addc_u32 %[lf], %[lf], 0\n\t"
    "v_mfma_f32_16x16x32_bf16 v[72:75], v[240:243], v[116:119], v[72:75]\n\t"
    "v_mfma_f32_16x16x32_bf16 v[76:79], v[244:247], v[116:119], v[76:79]\n\t"
    "v_cmp_ge_i32 vcc, %[lf], v39\n\t"
    "s_cmp_eq_u64 vcc, 0\n\t"
    "s_addc_u32 %[lf], %[lf], 0\n\t"
    "s_waitcnt lgkmcnt(4)\n\t"
    "v_mfma_f32_16x16x32_bf16 v[80:83], v[248:251], v[120:123], v[64:67]\n\t"
    "ds_read_b128 v[112:115], %[ea] offset:4096\n\t"
    "s_min_i32 %[lf], %[lf], %[cp]\n\t"
    "s_add_u32 %[b], %[b], 1\n\t"
    "v_mfma_f32_16x16x32_bf16 v[84:87], v[252:255], v[120:123], v[68:71]\n\t"
    "ds_read_b128 v[116:119], %[ea] offset:6144\n\t"
    "v_mfma_f32_16x16x32_bf16 v[88:91], v[248:251], v[124:127], v[72:75]\n\t"
    "v_mfma_f32_16x16x32_bf16 v[92:95], v[252:255], v[124:127], v[76:79]\n\t"
    "s_add_u32 s38, s38, %[ab]\n\t"
    "s_addc_u32 s39, s39, 0\n\t"
    "s_xor_b32 %[l0], %[l0], 0x10000\n\t"
    "s_cmp_lt_i32 %[b], %[lf]\n\t"
    "s_cbranch_scc1 1b\n\t"
    "s_waitcnt lgkmcnt(0)\n\t"
    "s_nop 4"
    : "=&{v[80:95]}"(pd), [ea] "+&v"(ea), [da] "+&v"(da), [b] "+&s"(b), [lf] "+&s"(lf), "+{s[36:37]}"(p1), "+{s[38:39]}"(p0), [sa] "+&v"(sa), [l1] "+&s"(l1), [l0] "+&s"(l0)
    : [rw] "v"(rw), [ab] "s"(ab), [cp] "s"(cp), [mu] "s"(mu), [ad] "v"(ad), "{v[128:159]}"(W[0]), "{v[160:191]}"(W[1]), "{v[192:223]}"(W[2]), "{v[224:255]}"(W[3]), [av0] "v"(av[0]), [av1] "v"(av[1]), [av2] "v"(av[2]), [av3] "v"(av[3]), [av4] "v"(av[4]), [av5] "v"(av[5]), [av6] "v"(av[6]), [av7] "v"(av[7])
    : "v39", "v64", "v65", "v66", "v67", "v68", "v69", "v70", "v71", "v72", "v73", "v74", "v75", "v76", "v77", "v78", "v79", "v96", "v97", "v98", "v99", "v100", "v101", "v102", "v103", "v104", "v105", "v106", "v107", "v108", "v109", "v110", "v111", "v112", "v113", "v114", "v115", "v116", "v117", "v118", "v119", "v120", "v121", "v122", "v123", "v124", "v125", "v126", "v127", "vcc", "scc", "m0", "memory");
}
__device__ __forceinline__ void fast_st0(const u32x32 (&W)[4], u32x16& pd, unsigned ea, unsigned da, int& b, int& lf, int cp, unsigned sa, unsigned mu, unsigned ad,
    unsigned rw, unsigned ab, const char* p1, const char* p0, unsigned co0, unsigned co1, int po, int pf, unsigned w0, unsigned sl, unsigned dv, const char* db, const char* pg) {
  unsigned st;
  asm volatile("s_barrier\n\t"
    "ds_read_b128 v[96:99], %[ea] offset:0\n\t"
    "ds_read_b128 v[100:103], %[ea] offset:2048\n\t"
    "ds_read_b128 v[104:107], %[da] offset:0\n\t"
    "ds_read_b128 v[108:111], %[da] offset:2048\n\t"
    "ds_read_b128 v[112:115], %[ea] offset:4096\n\t"
    "ds_read_b128 v[116:119], %[ea] offset:6144\n\t"
    "s_branch 2f\n\t"
    "1:\n\t"
    "s_barrier\n\t"
    "2:\n\t"
    "s_waitcnt lgkmcnt(4)\n\t"
    "v_mfma_f32_16x16x32_bf16 v[64:67], v[128:131], v[96:99], 0\n\t"
    "ds_read_b128 v[120:123], %[da] offset:4096\n\t"
    "v_mfma_f32_16x16x32_bf16 v[68:71], v[132:135], v[96:99], 0\n\t"
    "ds_read_b128 v[124:127], %[da] offset:6144\n\t"
    "v_mfma_f32_16x16x32_bf16 v[72:75], v[128:131], v[100:103], 0\n\t"
    "v_mfma_f32_16x16x32_bf16 v[76:79], v[132:135], v[100:103], 0\n\t"
    "s_waitcnt lgkmcnt(4)\n\t"
    "v_mfma_f32_16x16x32_bf16 v[64:67], v[136:139], v[104:107], v[64:67]\n\t"
    "ds_read_b128 v[96:99], %[ea] offset:8192\n\t"
    "v_mfma_f32_16x16x32_bf16 v[68:71], v[140:143], v[104:107], v[68:71]\n\t"
    "ds_read_b128 v[100:103], %[ea] offset:10240\n\t"
    "v_mfma_f32_16x16x32_bf16 v[72:75], v[136:139], v[108:111], v[72:75]\n\t"
    "v_mfma_f32_16x16x32_bf16 v[76:79], v[140:143], v[108:111], v[76:79]\n\t"
    "s_waitcnt lgkmcnt(4)\n\t"
    "v_mfma_f32_16x16x32_bf16 v[64:67], v[144:147], v[112:115], v[64:67]\n\t"
    "ds_read_b128 v[104:107], %[da] offset:8192\n\t"
    "v_mfma_f32_16x16x32_bf16 v[68:71], v[148:151], v[112:115], v[68:71]\n\t"
    "ds_read_b128 v[108:111], %[da] offset:10240\n\t"
    "v_mfma_f32_16x16x32_bf16 v[72:75], v[144:147], v[116:119], v[72:75]\n\t"
    "v_mfma_f32_16x16x32_bf16 v[76:79], v[148:151], v[116:119], v[76:79]\n\t"
    "s_waitcnt lgkmcnt(4)\n\t"
    "v_mfma_f32_16x16x32_bf16 v[64:67], v[152:155], v[120:123], v[64:67]\n\t"
    "ds_read_b128 v[112:115], %[ea] offset:12288\n\t"
    "v_mfma_f32_16x16x32_bf16 v[68:71], v[156:159], v[120:123], v[68:71]\n\t"
    "ds_read_b128 v[116:119], %[ea] offset:14336\n\t"
    "v_mfma_f32_16x16x32_bf16 v[72:75], v[152:155], v[124:127], v[72:75]\n\t"
    "v_mfma_f32_16x16x32_bf16 v[76:79], v[156:159], v[124:127], v[76:79]\n\t"
    "s_waitcnt lgkmcnt(4)\n\t"
    "v_mfma_f32_16x16x32_bf16 v[64:67], v[160:163], v[96:99], v[64:67]\n\t"
    "ds_read_b128 v[120:123], %[da] offset:12288\n\t"
    "v_mfma_f32_16x16x32_bf16 v[68:71], v[164:167], v[96:99], v[68:71]\n\t"
    "ds_read_b128 v[124:127], %[da] offset:14336\n\t"
    "v_mfma_f32_16x16x32_bf16 v[72:75], v[160:163], v[100:103], v[72:75]\n\t"
    "v_mfma_f32_16x16x32_bf16 v[76:79], v[164:167], v[100:103], v[76:79]\n\t"
    "s_waitcnt lgkmcnt(4)\n\t"
    "v_mfma_f32_16x16x32_bf16 v[64:67], v[168:171], v[104:107], v[64:67]\n\t"
    "ds_read_b128 v[96:99], %[ea] offset:16384\n\t"
    "v_mfma_f32_16x16x32_bf16 v[68:71], v[172:175], v[104:107], v[68:71]\n\t"
    "ds_read_b128 v[100:103], %[ea] offset:18432\n\t"
    "v_mfma_f32_16x16x32_bf16 v[72:75], v[168:171], v[108:111], v[72:75]\n\t"
    "v_mfma_f32_16x16x32_bf16 v[76:79], v[172:175], v[108:111], v[76:79]\n\t"
    "s_waitcnt lgkmcnt(4)\n\t"
    "v_mfma_f32_16x16x32_bf16 v[64:67], v[176:179], v[112:115], v[64:67]\n\t"
    "ds_read_b128 v[104:107], %[da] offset:16384\n\t"
    "v_mfma_f32_16x16x32_bf16 v[68:71], v[180:183], v[112:115], v[68:71]\n\t"
    "ds_read_b128 v[108:111], %[da] offset:18432\n\t"
    "v_mfma_f32_16x16x32_bf16 v[72:75], v[176:179], v[116:119], v[72:75]\n\t"
    "v_mfma_f32_16x16x32_bf16 v[76:79], v[180:183], v[116:119], v[76:79]\n\t"
    "s_waitcnt lgkmcnt(4)\n\t"
    "v_mfma_f32_16x16x32_bf16 v[64:67], v[184:187], v[120:123], v[64:67]\n\t"
    "ds_read_b128 v[112:115], %[ea] offset:20480\n\t"
    "v_mfma_f32_16x16x32_bf16 v[68:71], v[188:191], v[120:123], v[68:71]\n\t"
    "ds_read_b128 v[116:119], %[ea] offset:22528\n\t"
    "v_mfma_f32_16x16x32_bf16 v[72:75], v[184:187], v[124:127], v[72:75]\n\t"
    "v_mfma_f32_16x16x32_bf16 v[76:79], v[188:191], v[124:127], v[76:79]\n\t"
    "s_waitcnt vmcnt(0)\n\t"
    "s_barrier\n\t"
    "s_waitcnt lgkmcnt(4)\n\t"
    "v_mfma_f32_16x16x32_bf16 v[64:67], v[192:195], v[96:99], v[64:67]\n\t"
    "ds_read_b128 v[120:123], %[da] offset:20480\n\t"
    "v_mfma_f32_16x16x32_bf16 v[68:71], v[196:199], v[96:99], v[68:71]\n\t"
    "ds_read_b128 v[124:127], %[da] offset:22528\n\t"
    "v_mfma_f32_16x16x32_bf16 v[72:75], v[192:195], v[100:103], v[72:75]\n\t"
    "ds_read_b128 v[48:51], %[rw] offset:0\n\t"
    "v_mfma_f32_16x16x32_bf16 v[76:79], v[196:199], v[100:103], v[76:79]\n\t"
    "ds_read_b128 v[52:55], %[rw] offset:1024\n\t"
    "s_cmp_eq_u32 %[w0], 0\n\t"
    "s_cbranch_scc1 5f\n\t"
    "6:\n\t"
    "s_waitcnt lgkmcnt(6)\n\t"
    "v_mfma_f32_16x16x32_bf16 v[64:67], v[200:203], v[104:107], v[64:67]\n\t"
    "ds_read_b128 v[96:99], %[ea] offset:24576\n\t"
    "v_mfma_f32_16x16x32_bf16 v[68:71], v[204:207], v[104:107], v[68:71]\n\t"
    "ds_read_b128 v[100:103], %[ea] offset:26624\n\t"
    "v_mfma_f32_16x16x32_bf16 v[72:75], v[200:203], v[108:111], v[72:75]\n\t"
    "ds_read_b128 v[56:59], %[rw] offset:2048\n\t"
    "v_mfma_f32_16x16x32_bf16 v[76:79], v[204:207], v[108:111], v[76:79]\n\t"
    "ds_read_b128 v[60:63], %[rw] offset:3072\n\t"
    "s_waitcnt lgkmcnt(0)\n\t"
    "v_mfma_f32_16x16x32_bf16 v[64:67], v[208:211], v[112:115], v[64:67]\n\t"
    "ds_read_b128 v[104:107], %[da] offset:24576\n\t"
    "v_add_f32 v48, v48, v80\n\t"
    "v_add_f32 v49, v49, v81\n\t"
    "v_mfma_f32_16x16x32_bf16 v[68:71], v[212:215], v[112:115], v[68:71]\n\t"
    "ds_read_b128 v[108:111], %[da] offset:26624\n\t"
    "v_add_f32 v50, v50, v82\n\t"
    "v_mfma_f32_16x16x32_bf16 v[72:75], v[208:211], v[116:119], v[72:75]\n\t"
    "ds_read_b32 v39, %[sa]\n\t"
    "v_add_f32 v51, v51, v83\n\t"
    "v_mfma_f32_16x16x32_bf16 v[76:79], v[212:215], v[116:119], v[76:79]\n\t"
    "v_xor_b32 %[sa], 0x100, %[sa]\n\t"
    "v_add_f32 v52, v52, v84\n\t"
    "v_add_f32 v53, v53, v85\n\t"
    "v_mfma_f32_16x16x32_bf16 v[64:67], v[216:219], v[120:123], v[64:67]\n\t"
    "ds_read_b128 v[112:115], %[ea] offset:28672\n\t"
    "v_add_f32 v54, v54, v86\n\t"
    "v_mfma_f32_16x16x32_bf16 v[68:71], v[220:223], v[120:123], v[68:71]\n\t"
    "ds_read_b128 v[116:119], %[ea] offset:30720\n\t"
    "v_add_f32 v55, v55, v87\n\t"
    "v_mfma_f32_16x16x32_bf16 v[72:75], v[216:219], v[124:127], v[72:75]\n\t"
    "v_xor_b32 %[ea], 0x10000, %[ea]\n\t"
    "v_add_f32 v56, v56, v88\n\t"
    "v_add_f32 v57, v57, v89\n\t"
    "v_mfma_f32_16x16x32_bf16 v[76:79], v[220:223], v[124:127], v[76:79]\n\t"
    "v_add_f32 v58, v58, v90\n\t"
    "v_mfma_f32_16x16x32_bf16 v[64:67], v[224:227], v[96:99], v[64:67]\n\t"
    "ds_read_b128 v[120:123], %[da] offset:28672\n\t"
    "v_add_f32 v59, v59, v91\n\t"
    "v_mfma_f32_16x16x32_bf16 v[68:71], v[228:231], v[96:99], v[68:71]\n\t"
    "ds_read_b128 v[124:127], %[da] offset:30720\n\t"
    "v_add_f32 v60, v60, v92\n\t"
    "v_mfma_f32_16x16x32_bf16 v[72:75], v[224:227], v[100:103], v[72:75]\n\t"
    "v_xor_b32 %[da], 0x10000, %[da]\n\t"
    "v_add_f32 v61, v61, v93\n\t"
    "v_add_f32 v62, v62, v94\n\t"
    "v_mfma_f32_16x16x32_bf16 v[76:79], v[228:231], v[100:103], v[76:79]\n\t"
    "v_add_f32 v63, v63, v95\n\t"
    "s_waitcnt lgkmcnt(5)\n\t"
    "v_mfma_f32_16x16x32_bf16 v[64:67], v[232:235], v[104:107], v[64:67]\n\t"
    "ds_read_b128 v[96:99], %[ea] offset:0\n\t"
    "v_cvt_pk_bf16_f32 v40, v48, v49\n\t"
    "v_mfma_f32_16x16x32_bf16 v[68:71], v[236:239], v[104:107], v[68:71]\n\t"
    "ds_read_b128 v[100:103], %[ea] offset:2048\n\t"
    "v_cvt_pk_bf16_f32 v41, v50, v51\n\t"
    "v_cvt_pk_bf16_f32 v42, v52, v53\n\t"
    "v_mfma_f32_16x16x32_bf16 v[72:75], v[232:235], v[108:111], v[72:75]\n\t"
    "v_cvt_pk_bf16_f32 v43, v54, v55\n\t"
    "v_mfma_f32_16x16x32_bf16 v[76:79], v[236:239], v[108:111], v[76:79]\n\t"
    "global_store_dwordx4 %[co0], v[40:43], s[36:37] sc1\n\t"
    "s_waitcnt lgkmcnt(4)\n\t"
    "v_mfma_f32_16x16x32_bf16 v[64:67], v[240:243], v[112:115], v[64:67]\n\t"
    "ds_read_b128 v[104:107], %[da] offset:0\n\t"
    "v_mad_u32_u24 v39, v39, %[mu], %[ad]\n\t"
    "v_cvt_pk_bf16_f32 v44, v56, v57\n\t"
    "v_cvt_pk_bf16_f32 v45, v58, v59\n\t"
    "v_mfma_f32_16x16x32_bf16 v[68:71], v[244:247], v[112:115], v[68:71]\n\t"
    "ds_read_b128 v[108:111], %[da] offset:2048\n\t"
    "v_cmp_ge_i32 vcc, %[lf], v39\n\t"
    "s_cmp_eq_u64 vcc, 0\n\t"
    "s_addc_u32 %[lf], %[lf], 0\n\t"
    "v_cvt_pk_bf16_f32 v46, v60, v61\n\t"
    "v_mfma_f32_16x16x32_bf16 v[72:75], v[240:243], v[116:119], v[72:75]\n\t"
    "v_cvt_pk_bf16_f32 v47, v62, v63\n\t"
    "v_mfma_f32_16x16x32_bf16 v[76:79], v[244:247], v[116:119], v[76:79]\n\t"
    "v_cmp_ge_i32 vcc, %[lf], v39\n\t"
    "s_cmp_eq_u64 vcc, 0\n\t"
    "s_addc_u32 %[lf], %[lf], 0\n\t"
    "global_store_dwordx4 %[co1], v[44:47], s[36:37] sc1\n\t"
    "s_waitcnt lgkmcnt(4)\n\t"
    "v_mfma_f32_16x16x32_bf16 v[80:83], v[248:251], v[120:123], v[64:67]\n\t"
    "ds_read_b128 v[112:115], %[ea] offset:4096\n\t"
    "s_min_i32 %[lf], %[lf], %[cp]\n\t"
    "s_add_u32 %[b], %[b], 1\n\t"
    "v_mfma_f32_16x16x32_bf16 v[84:87], v[252:255], v[120:123], v[68:71]\n\t"
    "ds_read_b128 v[116:119], %[ea] offset:6144\n\t"
    "v_mfma_f32_16x16x32_bf16 v[88:91], v[248:251], v[124:127], v[72:75]\n\t"
    "v_mfma_f32_16x16x32_bf16 v[92:95], v[252:255], v[124:127], v[76:79]\n\t"
    "s_mov_b64 s[36:37], s[38:39]\n\t"
    "s_add_u32 s38, s38, %[ab]\n\t"
    "s_addc_u32 s39, s39, 0\n\t"
    "s_cmp_lt_i32 %[b], %[lf]\n\t"
    "s_cbranch_scc1 1b\n\t"
    "s_waitcnt lgkmcnt(0)\n\t"
    "s_nop 4\n\t"
    "s_branch 9f\n\t"
    "5:\n\t"
    "s_add_u32 %[st], %[b], %[po]\n\t"
    "s_max_i32 %[st], %[st], %[p0]\n\t"
    "v_mov_b32 v39, %[st]\n\t"
    "v_mov_b32 v38, 0\n\t"
    "s_mov_b64 exec, 1\n\t"
    "global_store_dword v38, v39, %[pg] sc1\n\t"
    "s_mov_b64 exec, -1\n\t"
    "s_mov_b32 %[p0], %[p1]\n\t"
    "s_xor_b32 %[sl], %[sl], 0x100\n\t"
    "s_cmp_ge_i32 %[lf], %[cp]\n\t"
    "s_cbranch_scc1 6b\n\t"
    "s_mov_b32 m0, %[sl]\n\t"
    "s_nop 0\n\t"
    "global_load_lds_dword %[dv], %[db] sc1\n\t"
    "s_branch 6b\n\t"
    "9:"
    : "=&{v[80:95]}"(pd), [ea] "+&v"(ea), [da] "+&v"(da), [b] "+&s"(b), [lf] "+&s"(lf), "+{s[36:37]}"(p1), "+{s[38:39]}"(p0), [sa] "+&v"(sa), [st] "=&s"(st), [sl] "+&s"(sl), [p0] "+&s"(pf)
    : [rw] "v"(rw), [ab] "s"(ab), [cp] "s"(cp), [mu] "s"(mu), [ad] "v"(ad), "{v[128:159]}"(W[0]), "{v[160:191]}"(W[1]), "{v[192:223]}"(W[2]), "{v[224:255]}"(W[3]), [co0] "v"(co0), [w0] "s"(w0), [dv] "v"(dv), [db] "s"(db), [pg] "s"(pg), [po] "s"(po - 1), [p1] "s"(b + po), [co1] "v"(co1)
    : "v38", "v39", "v40", "v41", "v42", "v43", "v44", "v45", "v46", "v47", "v48", "v49", "v50", "v51", "v52", "v53", "v54", "v55", "v56", "v57", "v58", "v59", "v60", "v61", "v62", "v63", "v64", "v65", "v66", "v67", "v68", "v69", "v70", "v71", "v72", "v73", "v74", "v75", "v76", "v77", "v78", "v79", "v96", "v97", "v98", "v99", "v100", "v101", "v102", "v103", "v104", "v105", "v106", "v107", "v108", "v109", "v110", "v111", "v112", "v113", "v114", "v115", "v116", "v117", "v118", "v119", "v120", "v121", "v122", "v123", "v124", "v125", "v126", "v127", "vcc", "scc", "m0", "memory");
}
__device__ __forceinline__ void fast_st1(const u32x32 (&W)[4], u32x16& pd, unsigned ea, unsigned da, int& b, int& lf, int cp, unsigned sa, unsigned mu, unsigned ad,
    unsigned rw, unsigned ab, const char* p1, const char* p0, unsigned co0, unsigned co1, int po, int pf, unsigned w0, unsigned sl, unsigned dv, const char* db, const char* pg) {
  unsigned st;
  asm volatile("s_barrier\n\t"
    "ds_read_b128 v[96:99], %[ea] offset:0\n\t"
    "ds_read_b128 v[100:103], %[ea] offset:2048\n\t"
    "ds_read_b128 v[104:107], %[da] offset:0\n\t"
    "ds_read_b128 v[108:111], %[da] offset:2048\n\t"
    "ds_read_b128 v[112:115], %[ea] offset:4096\n\t"
    "ds_read_b128 v[116:119], %[ea] offset:6144\n\t"
    "s_branch 2f\n\t"
    "1:\n\t"
    "s_barrier\n\t"
    "2:\n\t"
    "s_waitcnt lgkmcnt(4)\n\t"
    "v_mfma_f32_16x16x32_bf16 v[64:67], v[128:131], v[96:99], 0\n\t"
    "ds_read_b128 v[120:123], %[da] offset:4096\n\t"
    "v_mfma_f32_16x16x32_bf16 v[68:71], v[132:135], v[96:99], 0\n\t"
    "ds_read_b128 v[124:127], %[da] offset:6144\n\t"
    "v_mfma_f32_16x16x32_bf16 v[72:75], v[128:131], v[100:103], 0\n\t"
    "v_mfma_f32_16x16x32_bf16 v[76:79], v[132:135], v[100:103], 0\n\t"
    "s_waitcnt lgkmcnt(4)\n\t"
    "v_mfma_f32_16x16x32_bf16 v[64:67], v[136:139], v[104:107], v[64:67]\n\t"
    "ds_read_b128 v[96:99], %[ea] offset:8192\n\t"
    "v_mfma_f32_16x16x32_bf16 v[68:71], v[140:143], v[104:107], v[68:71]\n\t"
    "ds_read_b128 v[100:103], %[ea] offset:10240\n\t"
    "v_mfma_f32_16x16x32_bf16 v[72:75], v[136:139], v[108:111], v[72:75]\n\t"
    "v_mfma_f32_16x16x32_bf16 v[76:79], v[140:143], v[108:111], v[76:79]\n\t"
    "s_waitcnt lgkmcnt(4)\n\t"
    "v_mfma_f32_16x16x32_bf16 v[64:67], v[144:147], v[112:115], v[64:67]\n\t"
    "ds_read_b128 v[104:107], %[da] offset:8192\n\t"
    "v_mfma_f32_16x16x32_bf16 v[68:71], v[148:151], v[112:115], v[68:71]\n\t"
    "ds_read_b128 v[108:111], %[da] offset:10240\n\t"
    "v_mfma_f32_16x16x32_bf16 v[72:75], v[144:147], v[116:119], v[72:75]\n\t"
    "v_mfma_f32_16x16x32_bf16 v[76:79], v[148:151], v[116:119], v[76:79]\n\t"
    "s_waitcnt lgkmcnt(4)\n\t"
    "v_mfma_f32_16x16x32_bf16 v[64:67], v[152:155], v[120:123], v[64:67]\n\t"
    "ds_read_b128 v[112:115], %[ea] offset:12288\n\t"
    "v_mfma_f32_16x16x32_bf16 v[68:71], v[156:159], v[120:123], v[68:71]\n\t"
    "ds_read_b128 v[116:119], %[ea] offset:14336\n\t"
    "v_mfma_f32_16x16x32_bf16 v[72:75], v[152:155], v[124:127], v[72:75]\n\t"
    "v_mfma_f32_16x16x32_bf16 v[76:79], v[156:159], v[124:127], v[76:79]\n\t"
    "s_waitcnt lgkmcnt(4)\n\t"
    "v_mfma_f32_16x16x32_bf16 v[64:67], v[160:163], v[96:99], v[64:67]\n\t"
    "ds_read_b128 v[120:123], %[da] offset:12288\n\t"
    "v_mfma_f32_16x16x32_bf16 v[68:71], v[164:167], v[96:99], v[68:71]\n\t"
    "ds_read_b128 v[124:127], %[da] offset:14336\n\t"
    "v_mfma_f32_16x16x32_bf16 v[72:75], v[160:163], v[100:103], v[72:75]\n\t"
    "v_mfma_f32_16x16x32_bf16 v[76:79], v[164:167], v[100:103], v[76:79]\n\t"
    "s_waitcnt lgkmcnt(4)\n\t"
    "v_mfma_f32_16x16x32_bf16 v[64:67], v[168:171], v[104:107], v[64:67]\n\t"
    "ds_read_b128 v[96:99], %[ea] offset:16384\n\t"
    "v_mfma_f32_16x16x32_bf16 v[68:71], v[172:175], v[104:107], v[68:71]\n\t"
    "ds_read_b128 v[100:103], %[ea] offset:18432\n\t"
    "v_mfma_f32_16x16x32_bf16 v[72:75], v[168:171], v[108:111], v[72:75]\n\t"
    "v_mfma_f32_16x16x32_bf16 v[76:79], v[172:175], v[108:111], v[76:79]\n\t"
    "s_waitcnt lgkmcnt(4)\n\t"
    "v_mfma_f32_16x16x32_bf16 v[64:67], v[176:179], v[112:115], v[64:67]\n\t"
    "ds_read_b128 v[104:107], %[da] offset:16384\n\t"
    "v_mfma_f32_16x16x32_bf16 v[68:71], v[180:183], v[112:115], v[68:71]\n\t"
    "ds_read_b128 v[108:111], %[da] offset:18432\n\t"
    "v_mfma_f32_16x16x32_bf16 v[72:75], v[176:179], v[116:119], v[72:75]\n\t"
    "v_mfma_f32_16x16x32_bf16 v[76:79], v[180:183], v[116:119], v[76:79]\n\t"
    "s_waitcnt lgkmcnt(4)\n\t"
    "v_mfma_f32_16x16x32_bf16 v[64:67], v[184:187], v[120:123], v[64:67]\n\t"
    "ds_read_b128 v[112:115], %[ea] offset:20480\n\t"
    "v_mfma_f32_16x16x32_bf16 v[68:71], v[188:191], v[120:123], v[68:71]\n\t"
    "ds_read_b128 v[116:119], %[ea] offset:22528\n\t"
    "v_mfma_f32_16x16x32_bf16 v[72:75], v[184:187], v[124:127], v[72:75]\n\t"
    "v_mfma_f32_16x16x32_bf16 v[76:79], v[188:191], v[124:127], v[76:79]\n\t"
    "s_waitcnt vmcnt(0)\n\t"
    "s_barrier\n\t"
    "s_waitcnt lgkmcnt(4)\n\t"
    "v_mfma_f32_16x16x32_bf16 v[64:67], v[192:195], v[96:99], v[64:67]\n\t"
    "ds_read_b128 v[120:123], %[da] offset:20480\n\t"
    "v_mfma_f32_16x16x32_bf16 v[68:71], v[196:199], v[96:99], v[68:71]\n\t"
    "ds_read_b128 v[124:127], %[da] offset:22528\n\t"
    "v_mfma_f32_16x16x32_bf16 v[72:75], v[192:195], v[100:103], v[72:75]\n\t"
    "ds_read_b128 v[48:51], %[rw] offset:0\n\t"
    "v_mfma_f32_16x16x32_bf16 v[76:79], v[196:199], v[100:103], v[76:79]\n\t"
    "ds_read_b128 v[52:55], %[rw] offset:1024\n\t"
    "s_cmp_eq_u32 %[w0], 0\n\t"
    "s_cbranch_scc1 5f\n\t"
    "6:\n\t"
    "s_waitcnt lgkmcnt(6)\n\t"
    "v_mfma_f32_16x16x32_bf16 v[64:67], v[200:203], v[104:107], v[64:67]\n\t"
    "ds_read_b128 v[96:99], %[ea] offset:24576\n\t"
    "v_mfma_f32_16x16x32_bf16 v[68:71], v[204:207], v[104:107], v[68:71]\n\t"
    "ds_read_b128 v[100:103], %[ea] offset:26624\n\t"
    "v_mfma_f32_16x16x32_bf16 v[72:75], v[200:203], v[108:111], v[72:75]\n\t"
    "ds_read_b128 v[56:59], %[rw] offset:2048\n\t"
    "v_mfma_f32_16x16x32_bf16 v[76:79], v[204:207], v[108:111], v[76:79]\n\t"
    "ds_read_b128 v[60:63], %[rw] offset:3072\n\t"
    "s_waitcnt lgkmcnt(0)\n\t"
    "v_mfma_f32_16x16x32_bf16 v[64:67], v[208:211], v[112:115], v[64:67]\n\t"
    "ds_read_b128 v[104:107], %[da] offset:24576\n\t"
    "v_add_f32 v48, v48, v80\n\t"
    "v_add_f32 v49, v49, v81\n\t"
    "v_add_f32 v50, v50, v82\n\t"
    "v_mfma_f32_16x16x32_bf16 v[68:71], v[212:215], v[112:115], v[68:71]\n\t"
    "ds_read_b128 v[108:111], %[da] offset:26624\n\t"
    "v_add_f32 v51, v51, v83\n\t"
    "v_add_f32 v52, v52, v84\n\t"
    "v_add_f32 v53, v53, v85\n\t"
    "v_mfma_f32_16x16x32_bf16 v[72:75], v[208:211], v[116:119], v[72:75]\n\t"
    "ds_read_b32 v39, %[sa]\n\t"
    "v_add_f32 v54, v54, v86\n\t"
    "v_add_f32 v55, v55, v87\n\t"
    "v_add_f32 v56, v56, v88\n\t"
    "v_mfma_f32_16x16x32_bf16 v[76:79], v[212:215], v[116:119], v[76:79]\n\t"
    "v_xor_b32 %[sa], 0x100, %[sa]\n\t"
    "v_add_f32 v57, v57, v89\n\t"
    "v_add_f32 v58, v58, v90\n\t"
    "v_add_f32 v59, v59, v91\n\t"
    "v_mfma_f32_16x16x32_bf16 v[64:67], v[216:219], v[120:123], v[64:67]\n\t"
    "ds_read_b128 v[112:115], %[ea] offset:28672\n\t"
    "v_add_f32 v60, v60, v92\n\t"
    "v_add_f32 v61, v61, v93\n\t"
    "v_add_f32 v62, v62, v94\n\t"
    "v_mfma_f32_16x16x32_bf16 v[68:71], v[220:223], v[120:123], v[68:71]\n\t"
    "ds_read_b128 v[116:119], %[ea] offset:30720\n\t"
    "v_add_f32 v63, v63, v95\n\t"
    "v_mul_f32 v40, %[nl], v48\n\t"
    "v_mul_f32 v41, %[nl], v49\n\t"
    "v_mfma_f32_16x16x32_bf16 v[72:75], v[216:219], v[124:127], v[72:75]\n\t"
    "v_xor_b32 %[ea], 0x10000, %[ea]\n\t"
    "v_mul_f32 v42, %[nl], v50\n\t"
    "v_mul_f32 v43, %[nl], v51\n\t"
    "v_mul_f32 v44, %[nl], v56\n\t"
    "v_mfma_f32_16x16x32_bf16 v[76:79], v[220:223], v[124:127], v[76:79]\n\t"
    "v_mul_f32 v45, %[nl], v57\n\t"
    "v_mul_f32 v46, %[nl], v58\n\t"
    "v_mul_f32 v47, %[nl], v59\n\t"
    "v_mfma_f32_16x16x32_bf16 v[64:67], v[224:227], v[96:99], v[64:67]\n\t"
    "ds_read_b128 v[120:123], %[da] offset:28672\n\t"
    "v_exp_f32 v40, v40\n\t"
    "v_exp_f32 v41, v41\n\t"
    "v_exp_f32 v42, v42\n\t"
    "v_mfma_f32_16x16x32_bf16 v[68:71], v[228:231], v[96:99], v[68:71]\n\t"
    "ds_read_b128 v[124:127], %[da] offset:30720\n\t"
    "v_exp_f32 v43, v43\n\t"
    "v_exp_f32 v44, v44\n\t"
    "v_exp_f32 v45, v45\n\t"
    "v_mfma_f32_16x16x32_bf16 v[72:75], v[224:227], v[100:103], v[72:75]\n\t"
    "v_xor_b32 %[da], 0x10000, %[da]\n\t"
    "v_exp_f32 v46, v46\n\t"
    "v_exp_f32 v47, v47\n\t"
    "v_add_f32 v40, 1.0, v40\n\t"
    "v_mfma_f32_16x16x32_bf16 v[76:79], v[228:231], v[100:103], v[76:79]\n\t"
    "v_add_f32 v41, 1.0, v41\n\t"
    "v_add_f32 v42, 1.0, v42\n\t"
    "v_add_f32 v43, 1.0, v43\n\t"
    "s_waitcnt lgkmcnt(5)\n\t"
    "v_mfma_f32_16x16x32_bf16 v[64:67], v[232:235], v[104:107], v[64:67]\n\t"
    "ds_read_b128 v[96:99], %[ea] offset:0\n\t"
    "v_add_f32 v44, 1.0, v44\n\t"
    "v_add_f32 v45, 1.0, v45\n\t"
    "v_add_f32 v46, 1.0, v46\n\t"
    "v_mfma_f32_16x16x32_bf16 v[68:71], v[236:239], v[104:107], v[68:71]\n\t"
    "ds_read_b128 v[100:103], %[ea] offset:2048\n\t"
    "v_add_f32 v47, 1.0, v47\n\t"
    "v_rcp_f32 v40, v40\n\t"
    "v_rcp_f32 v41, v41\n\t"
    "v_mfma_f32_16x16x32_bf16 v[72:75], v[232:235], v[108:111], v[72:75]\n\t"
    "v_rcp_f32 v42, v42\n\t"
    "v_rcp_f32 v43, v43\n\t"
    "v_rcp_f32 v44, v44\n\t"
    "v_mfma_f32_16x16x32_bf16 v[76:79], v[236:239], v[108:111], v[76:79]\n\t"
    "v_rcp_f32 v45, v45\n\t"
    "v_rcp_f32 v46, v46\n\t"
    "v_rcp_f32 v47, v47\n\t"
    "s_waitcnt lgkmcnt(4)\n\t"
    "v_mfma_f32_16x16x32_bf16 v[64:67], v[240:243], v[112:115], v[64:67]\n\t"
    "ds_read_b128 v[104:107], %[da] offset:0\n\t"
    "v_mad_u32_u24 v39, v39, %[mu], %[ad]\n\t"
    "v_mul_f32 v40, v48, v40\n\t"
    "v_mul_f32 v41, v49, v41\n\t"
    "v_mul_f32 v42, v50, v42\n\t"
    "v_mfma_f32_16x16x32_bf16 v[68:71], v[244:247], v[112:115], v[68:71]\n\t"
    "ds_read_b128 v[108:111], %[da] offset:2048\n\t"
    "v_cmp_ge_i32 vcc, %[lf], v39\n\t"
    "s_cmp_eq_u64 vcc, 0\n\t"
    "s_addc_u32 %[lf], %[lf], 0\n\t"
    "v_mul_f32 v43, v51, v43\n\t"
    "v_mul_f32 v44, v56, v44\n\t"
    "v_mul_f32 v45, v57, v45\n\t"
    "v_mfma_f32_16x16x32_bf16 v[72:75], v[240:243], v[116:119], v[72:75]\n\t"
    "v_mul_f32 v46, v58, v46\n\t"
    "v_mul_f32 v47, v59, v47\n\t"
    "v_mul_f32 v40, v40, v52\n\t"
    "v_mfma_f32_16x16x32_bf16 v[76:79], v[244:247], v[116:119], v[76:79]\n\t"
    "v_cmp_ge_i32 vcc, %[lf], v39\n\t"
    "s_cmp_eq_u64 vcc, 0\n\t"
    "s_addc_u32 %[lf], %[lf], 0\n\t"
    "v_mul_f32 v41, v41, v53\n\t"
    "v_mul_f32 v42, v42, v54\n\t"
    "v_mul_f32 v43, v43, v55\n\t"
    "s_waitcnt lgkmcnt(4)\n\t"
    "v_mfma_f32_16x16x32_bf16 v[80:83], v[248:251], v[120:123], v[64:67]\n\t"
    "ds_read_b128 v[112:115], %[ea] offset:4096\n\t"
    "s_min_i32 %[lf], %[lf], %[cp]\n\t"
    "s_add_u32 %[b], %[b], 1\n\t"
    "v_mul_f32 v44, v44, v60\n\t"
    "v_mul_f32 v45, v45, v61\n\t"
    "v_mul_f32 v46, v46, v62\n\t"
    "v_mfma_f32_16x16x32_bf16 v[84:87], v[252:255], v[120:123], v[68:71]\n\t"
    "ds_read_b128 v[116:119], %[ea] offset:6144\n\t"
    "v_mul_f32 v47, v47, v63\n\t"
    "v_cvt_pk_bf16_f32 v48, v40, v41\n\t"
    "v_cvt_pk_bf16_f32 v49, v42, v43\n\t"
    "v_mfma_f32_16x16x32_bf16 v[88:91], v[248:251], v[124:127], v[72:75]\n\t"
    "v_cvt_pk_bf16_f32 v50, v44, v45\n\t"
    "v_cvt_pk_bf16_f32 v51, v46, v47\n\t"
    "s_nop 1\n\t"
    "v_mfma_f32_16x16x32_bf16 v[92:95], v[252:255], v[124:127], v[76:79]\n\t"
    "v_permlane16_swap_b32 v48, v50\n\t"
    "v_permlane16_swap_b32 v49, v51\n\t"
    "global_store_dwordx4 %[co0], v[48:51], s[36:37] sc1\n\t"
    "s_mov_b64 s[36:37], s[38:39]\n\t"
    "s_add_u32 s38, s38, %[ab]\n\t"
    "s_addc_u32 s39, s39, 0\n\t"
    "s_cmp_lt_i32 %[b], %[lf]\n\t"
    "s_cbranch_scc1 1b\n\t"
    "s_waitcnt lgkmcnt(0)\n\t"
    "s_nop 4\n\t"
    "s_branch 9f\n\t"
    "5:\n\t"
    "s_add_u32 %[st], %[b], %[po]\n\t"
    "s_max_i32 %[st], %[st], %[p0]\n\t"
    "v_mov_b32 v39, %[st]\n\t"
    "v_mov_b32 v38, 0\n\t"
    "s_mov_b64 exec, 1\n\t"
    "global_store_dword v38, v39, %[pg] sc1\n\t"
    "s_mov_b64 exec, -1\n\t"
    "s_mov_b32 %[p0], %[p1]\n\t"
    "s_xor_b32 %[sl], %[sl], 0x100\n\t"
    "s_cmp_ge_i32 %[lf], %[cp]\n\t"
    "s_cbranch_scc1 6b\n\t"
    "s_mov_b32 m0, %[sl]\n\t"
    "s_nop 0\n\t"
    "global_load_lds_dword %[dv], %[db] sc1\n\t"
    "s_branch 6b\n\t"
    "9:"
    : "=&{v[80:95]}"(pd), [ea] "+&v"(ea), [da] "+&v"(da), [b] "+&s"(b), [lf] "+&s"(lf), "+{s[36:37]}"(p1), "+{s[38:39]}"(p0), [sa] "+&v"(sa), [st] "=&s"(st), [sl] "+&s"(sl), [p0] "+&s"(pf)
    : [rw] "v"(rw), [ab] "s"(ab), [cp] "s"(cp), [mu] "s"(mu), [ad] "v"(ad), "{v[128:159]}"(W[0]), "{v[160:191]}"(W[1]), "{v[192:223]}"(W[2]), "{v[224:255]}"(W[3]), [co0] "v"(co0), [w0] "s"(w0), [dv] "v"(dv), [db] "s"(db), [pg] "s"(pg), [po] "s"(po - 1), [p1] "s"(b + po), [nl] "s"(-LOG2E)
    : "v38", "v39", "v40", "v41", "v42", "v43", "v44", "v45", "v46", "v47", "v48", "v49", "v50", "v51", "v52", "v53", "v54", "v55", "v56", "v57", "v58", "v59", "v60", "v61", "v62", "v63", "v64", "v65", "v66", "v67", "v68", "v69", "v70", "v71", "v72", "v73", "v74", "v75", "v76", "v77", "v78", "v79", "v96", "v97", "v98", "v99", "v100", "v101", "v102", "v103", "v104", "v105", "v106", "v107", "v108", "v109", "v110", "v111", "v112", "v113", "v114", "v115", "v116", "v117", "v118", "v119", "v120", "v121", "v122", "v123", "v124", "v125", "v126", "v127", "vcc", "scc", "m0", "memory");
}
__device__ __forceinline__ void fast_st2(const u32x32 (&W)[4], u32x16& pd, unsigned ea, unsigned da, int& b, int& lf, int cp, unsigned sa, unsigned mu, unsigned ad,
    unsigned rw, unsigned ab, const char* p1, const char* p0, unsigned co0, unsigned co1, int po, int pf, unsigned w0, unsigned sl, unsigned dv, const char* db, const char* pg) {
  unsigned st;
  asm volatile("s_barrier\n\t"
    "ds_read_b128 v[96:99], %[ea] offset:0\n\t"
    "ds_read_b128 v[100:103], %[ea] offset:2048\n\t"
    "ds_read_b128 v[104:107], %[da] offset:0\n\t"
    "ds_read_b128 v[108:111], %[da] offset:2048\n\t"
    "ds_read_b128 v[112:115], %[ea] offset:4096\n\t"
    "ds_read_b128 v[116:119], %[ea] offset:6144\n\t"
    "s_branch 2f\n\t"
    "1:\n\t"
    "s_barrier\n\t"
    "2:\n\t"
    "s_waitcnt lgkmcnt(4)\n\t"
    "v_mfma_f32_16x16x32_bf16 v[64:67], v[128:131], v[96:99], 0\n\t"
    "ds_read_b128 v[120:123], %[da] offset:4096\n\t"
    "v_mfma_f32_16x16x32_bf16 v[68:71], v[132:135], v[96:99], 0\n\t"
    "ds_read_b128 v[124:127], %[da] offset:6144\n\t"
    "v_mfma_f32_16x16x32_bf16 v[72:75], v[128:131], v[100:103], 0\n\t"
    "v_mfma_f32_16x16x32_bf16 v[76:79], v[132:135], v[100:103], 0\n\t"
    "s_waitcnt lgkmcnt(4)\n\t"
    "v_mfma_f32_16x16x32_bf16 v[64:67], v[136:139], v[104:107], v[64:67]\n\t"
    "ds_read_b128 v[96:99], %[ea] offset:8192\n\t"
    "v_mfma_f32_16x16x32_bf16 v[68:71], v[140:143], v[104:107], v[68:71]\n\t"
    "ds_read_b128 v[100:103], %[ea] offset:10240\n\t"
    "v_mfma_f32_16x16x32_bf16 v[72:75], v[136:139], v[108:111], v[72:75]\n\t"
    "v_mfma_f32_16x16x32_bf16 v[76:79], v[140:143], v[108:111], v[76:79]\n\t"
    "s_waitcnt lgkmcnt(4)\n\t"
    "v_mfma_f32_16x16x32_bf16 v[64:67], v[144:147], v[112:115], v[64:67]\n\t"
    "ds_read_b128 v[104:107], %[da] offset:8192\n\t"
    "v_mfma_f32_16x16x32_bf16 v[68:71], v[148:151], v[112:115], v[68:71]\n\t"
    "ds_read_b128 v[108:111], %[da] offset:10240\n\t"
    "v_mfma_f32_16x16x32_bf16 v[72:75], v[144:147], v[116:119], v[72:75]\n\t"
    "v_mfma_f32_16x16x32_bf16 v[76:79], v[148:151], v[116:119], v[76:79]\n\t"
    "s_waitcnt lgkmcnt(4)\n\t"
    "v_mfma_f32_16x16x32_bf16 v[64:67], v[152:155], v[120:123], v[64:67]\n\t"
    "ds_read_b128 v[112:115], %[ea] offset:12288\n\t"
    "v_mfma_f32_16x16x32_bf16 v[68:71], v[156:159], v[120:123], v[68:71]\n\t"
    "ds_read_b128 v[116:119], %[ea] offset:14336\n\t"
    "v_mfma_f32_16x16x32_bf16 v[72:75], v[152:155], v[124:127], v[72:75]\n\t"
    "v_mfma_f32_16x16x32_bf16 v[76:79], v[156:159], v[124:127], v[76:79]\n\t"
    "s_waitcnt lgkmcnt(4)\n\t"
    "v_mfma_f32_16x16x32_bf16 v[64:67], v[160:163], v[96:99], v[64:67]\n\t"
    "ds_read_b128 v[120:123], %[da] offset:12288\n\t"
    "v_mfma_f32_16x16x32_bf16 v[68:71], v[164:167], v[96:99], v[68:71]\n\t"
    "ds_read_b128 v[124:127], %[da] offset:14336\n\t"
    "v_mfma_f32_16x16x32_bf16 v[72:75], v[160:163], v[100:103], v[72:75]\n\t"
    "v_mfma_f32_16x16x32_bf16 v[76:79], v[164:167], v[100:103], v[76:79]\n\t"
    "s_waitcnt lgkmcnt(4)\n\t"
    "v_mfma_f32_16x16x32_bf16 v[64:67], v[168:171], v[104:107], v[64:67]\n\t"
    "ds_read_b128 v[96:99], %[ea] offset:16384\n\t"
    "v_mfma_f32_16x16x32_bf16 v[68:71], v[172:175], v[104:107], v[68:71]\n\t"
    "ds_read_b128 v[100:103], %[ea] offset:18432\n\t"
    "v_mfma_f32_16x16x32_bf16 v[72:75], v[168:171], v[108:111], v[72:75]\n\t"
    "v_mfma_f32_16x16x32_bf16 v[76:79], v[172:175], v[108:111], v[76:79]\n\t"
    "s_waitcnt lgkmcnt(4)\n\t"
    "v_mfma_f32_16x16x32_bf16 v[64:67], v[176:179], v[112:115], v[64:67]\n\t"
    "ds_read_b128 v[104:107], %[da] offset:16384\n\t"
    "v_mfma_f32_16x16x32_bf16 v[68:71], v[180:183], v[112:115], v[68:71]\n\t"
    "ds_read_b128 v[108:111], %[da] offset:18432\n\t"
    "v_mfma_f32_16x16x32_bf16 v[72:75], v[176:179], v[116:119], v[72:75]\n\t"
    "v_mfma_f32_16x16x32_bf16 v[76:79], v[180:183], v[116:119], v[76:79]\n\t"
    "s_waitcnt lgkmcnt(4)\n\t"
    "v_mfma_f32_16x16x32_bf16 v[64:67], v[184:187], v[120:123], v[64:67]\n\t"
    "ds_read_b128 v[112:115], %[ea] offset:20480\n\t"
    "v_mfma_f32_16x16x32_bf16 v[68:71], v[188:191], v[120:123], v[68:71]\n\t"
    "ds_read_b128 v[116:119], %[ea] offset:22528\n\t"
    "v_mfma_f32_16x16x32_bf16 v[72:75], v[184:187], v[124:127], v[72:75]\n\t"
    "v_mfma_f32_16x16x32_bf16 v[76:79], v[188:191], v[124:127], v[76:79]\n\t"
    "s_waitcnt vmcnt(0)\n\t"
    "s_barrier\n\t"
    "s_waitcnt lgkmcnt(4)\n\t"
    "v_mfma_f32_16x16x32_bf16 v[64:67], v[192:195], v[96:99], v[64:67]\n\t"
    "ds_read_b128 v[120:123], %[da] offset:20480\n\t"
    "v_mfma_f32_16x16x32_bf16 v[68:71], v[196:199], v[96:99], v[68:71]\n\t"
    "ds_read_b128 v[124:127], %[da] offset:22528\n\t"
    "v_mfma_f32_16x16x32_bf16 v[72:75], v[192:195], v[100:103], v[72:75]\n\t"
    "ds_read_b128 v[48:51], %[rw] offset:0\n\t"
    "v_mfma_f32_16x16x32_bf16 v[76:79], v[196:199], v[100:103], v[76:79]\n\t"
    "ds_read_b128 v[52:55], %[rw] offset:1024\n\t"
    "s_cmp_eq_u32 %[w0], 0\n\t"
    "s_cbranch_scc1 5f\n\t"
    "6:\n\t"
    "s_waitcnt lgkmcnt(6)\n\t"
    "v_mfma_f32_16x16x32_bf16 v[64:67], v[200:203], v[104:107], v[64:67]\n\t"
    "ds_read_b128 v[96:99], %[ea] offset:24576\n\t"
    "v_mfma_f32_16x16x32_bf16 v[68:71], v[204:207], v[104:107], v[68:71]\n\t"
    "ds_read_b128 v[100:103], %[ea] offset:26624\n\t"
    "v_mfma_f32_16x16x32_bf16 v[72:75], v[200:203], v[108:111], v[72:75]\n\t"
    "ds_read_b128 v[56:59], %[rw] offset:2048\n\t"
    "v_mfma_f32_16x16x32_bf16 v[76:79], v[204:207], v[108:111], v[76:79]\n\t"
    "ds_read_b128 v[60:63], %[rw] offset:3072\n\t"
    "s_waitcnt lgkmcnt(0)\n\t"
    "v_mfma_f32_16x16x32_bf16 v[64:67], v[208:211], v[112:115], v[64:67]\n\t"
    "ds_read_b128 v[104:107], %[da] offset:24576\n\t"
    "v_cvt_pk_bf16_f32 v40, v80, v81\n\t"
    "v_mfma_f32_16x16x32_bf16 v[68:71], v[212:215], v[112:115], v[68:71]\n\t"
    "ds_read_b128 v[108:111], %[da] offset:26624\n\t"
    "v_cvt_pk_bf16_f32 v41, v82, v83\n\t"
    "v_mfma_f32_16x16x32_bf16 v[72:75], v[208:211], v[116:119], v[72:75]\n\t"
    "ds_read_b32 v39, %[sa]\n\t"
    "v_cvt_pk_bf16_f32 v42, v84, v85\n\t"
    "v_mfma_f32_16x16x32_bf16 v[76:79], v[212:215], v[116:119], v[76:79]\n\t"
    "v_xor_b32 %[sa], 0x100, %[sa]\n\t"
    "v_cvt_pk_bf16_f32 v43, v86, v87\n\t"
    "v_mfma_f32_16x16x32_bf16 v[64:67], v[216:219], v[120:123], v[64:67]\n\t"
    "ds_read_b128 v[112:115], %[ea] offset:28672\n\t"
    "global_store_dwordx4 %[co0], v[40:43], s[36:37] sc1\n\t"
    "v_mfma_f32_16x16x32_bf16 v[68:71], v[220:223], v[120:123], v[68:71]\n\t"
    "ds_read_b128 v[116:119], %[ea] offset:30720\n\t"
    "v_cvt_pk_bf16_f32 v44, v88, v89\n\t"
    "v_mfma_f32_16x16x32_bf16 v[72:75], v[216:219], v[124:127], v[72:75]\n\t"
    "v_xor_b32 %[ea], 0x10000, %[ea]\n\t"
    "v_cvt_pk_bf16_f32 v45, v90, v91\n\t"
    "v_mfma_f32_16x16x32_bf16 v[76:79], v[220:223], v[124:127], v[76:79]\n\t"
    "v_cvt_pk_bf16_f32 v46, v92, v93\n\t"
    "v_mfma_f32_16x16x32_bf16 v[64:67], v[224:227], v[96:99], v[64:67]\n\t"
    "ds_read_b128 v[120:123], %[da] offset:28672\n\t"
    "v_cvt_pk_bf16_f32 v47, v94, v95\n\t"
    "v_mfma_f32_16x16x32_bf16 v[68:71], v[228:231], v[96:99], v[68:71]\n\t"
    "ds_read_b128 v[124:127], %[da] offset:30720\n\t"
    "global_store_dwordx4 %[co1], v[44:47], s[36:37] sc1\n\t"
    "v_mfma_f32_16x16x32_bf16 v[72:75], v[224:227], v[100:103], v[72:75]\n\t"
    "v_xor_b32 %[da], 0x10000, %[da]\n\t"
    "v_cvt_pk_bf16_f32 v48, v48, v49\n\t"
    "v_mfma_f32_16x16x32_bf16 v[76:79], v[228:231], v[100:103], v[76:79]\n\t"
    "v_cvt_pk_bf16_f32 v49, v50, v51\n\t"
    "s_waitcnt lgkmcnt(5)\n\t"
    "v_mfma_f32_16x16x32_bf16 v[64:67], v[232:235], v[104:107], v[64:67]\n\t"
    "ds_read_b128 v[96:99], %[ea] offset:0\n\t"
    "v_cvt_pk_bf16_f32 v50, v52, v53\n\t"
    "v_mfma_f32_16x16x32_bf16 v[68:71], v[236:239], v[104:107], v[68:71]\n\t"
    "ds_read_b128 v[100:103], %[ea] offset:2048\n\t"
    "v_cvt_pk_bf16_f32 v51, v54, v55\n\t"
    "v_mfma_f32_16x16x32_bf16 v[72:75], v[232:235], v[108:111], v[72:75]\n\t"
    "global_store_dwordx4 %[co0], v[48:51], s[36:37] offset:2048 sc1\n\t"
    "v_mfma_f32_16x16x32_bf16 v[76:79], v[236:239], v[108:111], v[76:79]\n\t"
    "v_cvt_pk_bf16_f32 v56, v56, v57\n\t"
    "s_waitcnt lgkmcnt(4)\n\t"
    "v_mfma_f32_16x16x32_bf16 v[64:67], v[240:243], v[112:115], v[64:67]\n\t"
    "ds_read_b128 v[104:107], %[da] offset:0\n\t"
    "v_mad_u32_u24 v39, v39, %[mu], %[ad]\n\t"
    "v_cvt_pk_bf16_f32 v57, v58, v59\n\t"
    "v_mfma_f32_16x16x32_bf16 v[68:71], v[244:247], v[112:115], v[68:71]\n\t"
    "ds_read_b128 v[108:111], %[da] offset:2048\n\t"
    "v_cmp_ge_i32 vcc, %[lf], v39\n\t"
    "s_cmp_eq_u64 vcc, 0\n\t"
    "s_addc_u32 %[lf], %[lf], 0\n\t"
    "v_cvt_pk_bf16_f32 v58, v60, v61\n\t"
    "v_mfma_f32_16x16x32_bf16 v[72:75], v[240:243], v[116:119], v[72:75]\n\t"
    "v_cvt_pk_bf16_f32 v59, v62, v63\n\t"
    "v_mfma_f32_16x16x32_bf16 v[76:79], v[244:247], v[116:119], v[76:79]\n\t"
    "v_cmp_ge_i32 vcc, %[lf], v39\n\t"
    "s_cmp_eq_u64 vcc, 0\n\t"
    "s_addc_u32 %[lf], %[lf], 0\n\t"
    "global_store_dwordx4 %[co1], v[56:59], s[36:37] offset:2048 sc1\n\t"
    "s_waitcnt lgkmcnt(4)\n\t"
    "v_mfma_f32_16x16x32_bf16 v[80:83], v[248:251], v[120:123], v[64:67]\n\t"
    "ds_read_b128 v[112:115], %[ea] offset:4096\n\t"
    "s_min_i32 %[lf], %[lf], %[cp]\n\t"
    "s_add_u32 %[b], %[b], 1\n\t"
    "v_mfma_f32_16x16x32_bf16 v[84:87], v[252:255], v[120:123], v[68:71]\n\t"
    "ds_read_b128 v[116:119], %[ea] offset:6144\n\t"
    "v_mfma_f32_16x16x32_bf16 v[88:91], v[248:251], v[124:127], v[72:75]\n\t"
    "v_mfma_f32_16x16x32_bf16 v[92:95], v[252:255], v[124:127], v[76:79]\n\t"
    "s_mov_b64 s[36:37], s[38:39]\n\t"
    "s_add_u32 s38, s38, %[ab]\n\t"
    "s_addc_u32 s39, s39, 0\n\t"
    "s_cmp_lt_i32 %[b], %[lf]\n\t"
    "s_cbranch_scc1 1b\n\t"
    "s_waitcnt lgkmcnt(0)\n\t"
    "s_nop 4\n\t"
    "s_branch 9f\n\t"
    "5:\n\t"
    "s_add_u32 %[st], %[b], %[po]\n\t"
    "s_max_i32 %[st], %[st], %[p0]\n\t"
    "v_mov_b32 v39, %[st]\n\t"
    "v_mov_b32 v38, 0\n\t"
    "s_mov_b64 exec, 1\n\t"
    "global_store_dword v38, v39, %[pg] sc1\n\t"
    "s_mov_b64 exec, -1\n\t"
    "s_mov_b32 %[p0], %[p1]\n\t"
    "s_xor_b32 %[sl], %[sl], 0x100\n\t"
    "s_cmp_ge_i32 %[lf], %[cp]\n\t"
    "s_cbranch_scc1 6b\n\t"
    "s_mov_b32 m0, %[sl]\n\t"
    "s_nop 0\n\t"
    "global_load_lds_dword %[dv], %[db] sc1\n\t"
    "s_branch 6b\n\t"
    "9:"
    : "=&{v[80:95]}"(pd), [ea] "+&v"(ea), [da] "+&v"(da), [b] "+&s"(b), [lf] "+&s"(lf), "+{s[36:37]}"(p1), "+{s[38:39]}"(p0), [sa] "+&v"(sa), [st] "=&s"(st), [sl] "+&s"(sl), [p0] "+&s"(pf)
    : [rw] "v"(rw), [ab] "s"(ab), [cp] "s"(cp), [mu] "s"(mu), [ad] "v"(ad), "{v[128:159]}"(W[0]), "{v[160:191]}"(W[1]), "{v[192:223]}"(W[2]), "{v[224:255]}"(W[3]), [co0] "v"(co0), [w0] "s"(w0), [dv] "v"(dv), [db] "s"(db), [pg] "s"(pg), [po] "s"(po - 1), [p1] "s"(b + po), [co1] "v"(co1)
    : "v38", "v39", "v40", "v41", "v42", "v43", "v44", "v45", "v46", "v47", "v48", "v49", "v50", "v51", "v52", "v53", "v54", "v55", "v56", "v57", "v58", "v59", "v60", "v61", "v62", "v63", "v64", "v65", "v66", "v67", "v68", "v69", "v70", "v71", "v72", "v73", "v74", "v75", "v76", "v77", "v78", "v79", "v96", "v97", "v98", "v99", "v100", "v101", "v102", "v103", "v104", "v105", "v106", "v107", "v108", "v109", "v110", "v111", "v112", "v113", "v114", "v115", "v116", "v117", "v118", "v119", "v120", "v121", "v122", "v123", "v124", "v125", "v126", "v127", "vcc", "scc", "m0", "memory");
}
__device__ __forceinline__ void near_ld0(const u32x32 (&W)[4], u32x16& pd, unsigned ea, unsigned da, int& b, int& lf, int cp, unsigned sa, unsigned mu, unsigned ad,
    unsigned rw, unsigned ab, const char* p1, const char* p0, unsigned l1, unsigned l0, const unsigned (&av)[8]) {
  asm volatile("s_setprio 0\n\t"
    "s_waitcnt vmcnt(4)\n\t"
    "s_barrier\n\t"
    "ds_read_b128 v[96:99], %[ea] offset:0\n\t"
    "ds_read_b128 v[100:103], %[ea] offset:2048\n\t"
    "ds_read_b128 v[104:107], %[da] offset:0\n\t"
    "ds_read_b128 v[108:111], %[da] offset:2048\n\t"
    "ds_read_b128 v[112:115], %[ea] offset:4096\n\t"
    "ds_read_b128 v[116:119], %[ea] offset:6144\n\t"
    "s_branch 2f\n\t"
    "1:\n\t"
    "s_waitcnt vmcnt(4)\n\t"
    "s_nop 1\n\t"
    "s_barrier\n\t"
    "2:\n\t"
    "s_waitcnt lgkmcnt(4)\n\t"
    "v_mfma_f32_16x16x32_bf16 v[64:67], v[128:131], v[96:99], 0\n\t"
    "ds_read_b128 v[120:123], %[da] offset:4096\n\t"
    "s_add_u32 m0, %[l1], 0\n\t"
    "ds_write_b128 %[rw], v[80:83] offset:0\n\t"
    "v_mfma_f32_16x16x32_bf16 v[68:71], v[132:135], v[96:99], 0\n\t"
    "ds_read_b128 v[124:127], %[da] offset:6144\n\t"
    "global_load_lds_dwordx4 %[av0], s[36:37] sc1\n\t"
    "ds_write_b128 %[rw], v[84:87] offset:1024\n\t"
    "v_mfma_f32_16x16x32_bf16 v[72:75], v[128:131], v[100:103], 0\n\t"
    "s_add_u32 m0, %[l1], 32768\n\t"
    "ds_write_b128 %[rw], v[88:91] offset:2048\n\t"
    "v_mfma_f32_16x16x32_bf16 v[76:79], v[132:135], v[100:103], 0\n\t"
    "global_load_lds_dwordx4 %[av1], s[36:37] sc1\n\t"
    "ds_write_b128 %[rw], v[92:95] offset:3072\n\t"
    "s_waitcnt lgkmcnt(8)\n\t"
    "v_mfma_f32_16x16x32_bf16 v[64:67], v[136:139], v[104:107], v[64:67]\n\t"
    "ds_read_b128 v[96:99], %[ea] offset:8192\n\t"
    "s_add_u32 m0, %[l1], 4096\n\t"
    "v_mfma_f32_16x16x32_bf16 v[68:71], v[140:143], v[104:107], v[68:71]\n\t"
    "ds_read_b128 v[100:103], %[ea] offset:10240\n\t"
    "global_load_lds_dwordx4 %[av2], s[36:37] sc1\n\t"
    "v_mfma_f32_16x16x32_bf16 v[72:75], v[136:139], v[108:111], v[72:75]\n\t"
    "s_add_u32 m0, %[l1], 36864\n\t"
    "v_mfma_f32_16x16x32_bf16 v[76:79], v[140:143], v[108:111], v[76:79]\n\t"
    "global_load_lds_dwordx4 %[av3], s[36:37] sc1\n\t"
    "s_waitcnt lgkmcnt(2)\n\t"
    "v_mfma_f32_16x16x32_bf16 v[64:67], v[144:147], v[112:115], v[64:67]\n\t"
    "ds_read_b128 v[104:107], %[da] offset:8192\n\t"
    "s_add_u32 m0, %[l1], 8192\n\t"
    "v_mfma_f32_16x16x32_bf16 v[68:71], v[148:151], v[112:115], v[68:71]\n\t"
    "ds_read_b128 v[108:111], %[da] offset:10240\n\t"
    "global_load_lds_dwordx4 %[av4], s[36:37] sc1\n\t"
    "v_mfma_f32_16x16x32_bf16 v[72:75], v[144:147], v[116:119], v[72:75]\n\t"
    "s_add_u32 m0, %[l1], 40960\n\t"
    "v_mfma_f32_16x16x32_bf16 v[76:79], v[148:151], v[116:119], v[76:79]\n\t"
    "global_load_lds_dwordx4 %[av5], s[36:37] sc1\n\t"
    "s_waitcnt vmcnt(6)\n\t"
    "s_barrier\n\t"
    "v_mfma_f32_16x16x32_bf16 v[64:67], v[152:155], v[120:123], v[64:67]\n\t"
    "ds_read_b128 v[112:115], %[ea] offset:12288\n\t"
    "s_add_u32 m0, %[l1], 12288\n\t"
    "v_mfma_f32_16x16x32_bf16 v[68:71], v[156:159], v[120:123], v[68:71]\n\t"
    "ds_read_b128 v[116:119], %[ea] offset:14336\n\t"
    "global_load_lds_dwordx4 %[av6], s[36:37] sc1\n\t"
    "v_mfma_f32_16x16x32_bf16 v[72:75], v[152:155], v[124:127], v[72:75]\n\t"
    "s_add_u32 m0, %[l1], 45056\n\t"
    "v_mfma_f32_16x16x32_bf16 v[76:79], v[156:159], v[124:127], v[76:79]\n\t"
    "global_load_lds_dwordx4 %[av7], s[36:37] sc1\n\t"
    "s_waitcnt lgkmcnt(4)\n\t"
    "v_mfma_f32_16x16x32_bf16 v[64:67], v[160:163], v[96:99], v[64:67]\n\t"
    "ds_read_b128 v[120:123], %[da] offset:12288\n\t"
    "s_add_u32 m0, %[l0], 0\n\t"
    "v_mfma_f32_16x16x32_bf16 v[68:71], v[164:167], v[96:99], v[68:71]\n\t"
    "ds_read_b128 v[124:127], %[da] offset:14336\n\t"
    "global_load_lds_dwordx4 %[av0], s[38:39] sc1\n\t"
    "v_mfma_f32_16x16x32_bf16 v[72:75], v[160:163], v[100:103], v[72:75]\n\t"
    "s_add_u32 m0, %[l0], 32768\n\t"
    "v_mfma_f32_16x16x32_bf16 v[76:79], v[164:167], v[100:103], v[76:79]\n\t"
    "global_load_lds_dwordx4 %[av1], s[38:39] sc1\n\t"
    "s_waitcnt lgkmcnt(4)\n\t"
    "v_mfma_f32_16x16x32_bf16 v[64:67], v[168:171], v[104:107], v[64:67]\n\t"
    "ds_read_b128 v[96:99], %[ea] offset:16384\n\t"
    "s_add_u32 m0, %[l0], 4096\n\t"
    "v_mfma_f32_16x16x32_bf16 v[68:71], v[172:175], v[104:107], v[68:71]\n\t"
    "ds_read_b128 v[100:103], %[ea] offset:18432\n\t"
    "global_load_lds_dwordx4 %[av2], s[38:39] sc1\n\t"
    "v_mfma_f32_16x16x32_bf16 v[72:75], v[168:171], v[108:111], v[72:75]\n\t"
    "s_add_u32 m0, %[l0], 36864\n\t"
    "v_mfma_f32_16x16x32_bf16 v[76:79], v[172:175], v[108:111], v[76:79]\n\t"
    "global_load_lds_dwordx4 %[av3], s[38:39] sc1\n\t"
    "s_waitcnt lgkmcnt(4)\n\t"
    "v_mfma_f32_16x16x32_bf16 v[64:67], v[176:179], v[112:115], v[64:67]\n\t"
    "ds_read_b128 v[104:107], %[da] offset:16384\n\t"
    "s_add_u32 m0, %[l0], 8192\n\t"
    "v_mfma_f32_16x16x32_bf16 v[68:71], v[180:183], v[112:115], v[68:71]\n\t"
    "ds_read_b128 v[108:111], %[da] offset:18432\n\t"
    "global_load_lds_dwordx4 %[av4], s[38:39] sc1\n\t"
    "v_mfma_f32_16x16x32_bf16 v[72:75], v[176:179], v[116:119], v[72:75]\n\t"
    "s_add_u32 m0, %[l0], 40960\n\t"
    "v_mfma_f32_16x16x32_bf16 v[76:79], v[180:183], v[116:119], v[76:79]\n\t"
    "global_load_lds_dwordx4 %[av5], s[38:39] sc1\n\t"
    "s_waitcnt lgkmcnt(4)\n\t"
    "v_mfma_f32_16x16x32_bf16 v[64:67], v[184:187], v[120:123], v[64:67]\n\t"
    "ds_read_b128 v[112:115], %[ea] offset:20480\n\t"
    "s_add_u32 m0, %[l0], 12288\n\t"
    "v_mfma_f32_16x16x32_bf16 v[68:71], v[188:191], v[120:123], v[68:71]\n\t"
    "ds_read_b128 v[116:119], %[ea] offset:22528\n\t"
    "global_load_lds_dwordx4 %[av6], s[38:39] sc1\n\t"
    "v_mfma_f32_16x16x32_bf16 v[72:75], v[184:187], v[124:127], v[72:75]\n\t"
    "s_add_u32 m0, %[l0], 45056\n\t"
    "v_mfma_f32_16x16x32_bf16 v[76:79], v[188:191], v[124:127], v[76:79]\n\t"
    "global_load_lds_dwordx4 %[av7], s[38:39] sc1\n\t"
    "s_waitcnt lgkmcnt(4)\n\t"
    "v_mfma_f32_16x16x32_bf16 v[64:67], v[192:195], v[96:99], v[64:67]\n\t"
    "ds_read_b128 v[120:123], %[da] offset:20480\n\t"
    "v_mfma_f32_16x16x32_bf16 v[68:71], v[196:199], v[96:99], v[68:71]\n\t"
    "ds_read_b128 v[124:127], %[da] offset:22528\n\t"
    "v_mfma_f32_16x16x32_bf16 v[72:75], v[192:195], v[100:103], v[72:75]\n\t"
    "v_mfma_f32_16x16x32_bf16 v[76:79], v[196:199], v[100:103], v[76:79]\n\t"
    "s_waitcnt lgkmcnt(4)\n\t"
    "v_mfma_f32_16x16x32_bf16 v[64:67], v[200:203], v[104:107], v[64:67]\n\t"
    "ds_read_b128 v[96:99], %[ea] offset:24576\n\t"
    "v_mfma_f32_16x16x32_bf16 v[68:71], v[204:207], v[104:107], v[68:71]\n\t"
    "ds_read_b128 v[100:103], %[ea] offset:26624\n\t"
    "v_mfma_f32_16x16x32_bf16 v[72:75], v[200:203], v[108:111], v[72:75]\n\t"
    "v_mfma_f32_16x16x32_bf16 v[76:79], v[204:207], v[108:111], v[76:79]\n\t"
    "s_add_u32 s36, s36, %[ab]\n\t"
    "s_addc_u32 s37, s37, 0\n\t"
    "s_waitcnt lgkmcnt(4)\n\t"
    "v_mfma_f32_16x16x32_bf16 v[64:67], v[208:211], v[112:115], v[64:67]\n\t"
    "ds_read_b128 v[104:107], %[da] offset:24576\n\t"
    "v_mfma_f32_16x16x32_bf16 v[68:71], v[212:215], v[112:115], v[68:71]\n\t"
    "ds_read_b128 v[108:111], %[da] offset:26624\n\t"
    "v_mfma_f32_16x16x32_bf16 v[72:75], v[208:211], v[116:119], v[72:75]\n\t"
    "v_mfma_f32_16x16x32_bf16 v[76:79], v[212:215], v[116:119], v[76:79]\n\t"
    "s_xor_b32 %[l1], %[l1], 0x10000\n\t"
    "s_waitcnt lgkmcnt(4)\n\t"
    "v_mfma_f32_16x16x32_bf16 v[64:67], v[216:219], v[120:123], v[64:67]\n\t"
    "ds_read_b128 v[112:115], %[ea] offset:28672\n\t"
    "v_mfma_f32_16x16x32_bf16 v[68:71], v[220:223], v[120:123], v[68:71]\n\t"
    "ds_read_b128 v[116:119], %[ea] offset:30720\n\t"
    "v_mfma_f32_16x16x32_bf16 v[72:75], v[216:219], v[124:127], v[72:75]\n\t"
    "v_xor_b32 %[ea], 0x10000, %[ea]\n\t"
    "v_mfma_f32_16x16x32_bf16 v[76:79], v[220:223], v[124:127], v[76:79]\n\t"
    "s_waitcnt vmcnt(12)\n\t"
    "s_barrier\n\t"
    "s_waitcnt lgkmcnt(4)\n\t"
    "v_mfma_f32_16x16x32_bf16 v[64:67], v[224:227], v[96:99], v[64:67]\n\t"
    "ds_read_b32 v39, %[sa]\n\t"
    "ds_read_b128 v[120:123], %[da] offset:28672\n\t"
    "v_mfma_f32_16x16x32_bf16 v[68:71], v[228:231], v[96:99], v[68:71]\n\t"
    "ds_read_b128 v[124:127], %[da] offset:30720\n\t"
    "v_mfma_f32_16x16x32_bf16 v[72:75], v[224:227], v[100:103], v[72:75]\n\t"
    "v_xor_b32 %[da], 0x10000, %[da]\n\t"
    "v_mfma_f32_16x16x32_bf16 v[76:79], v[228:231], v[100:103], v[76:79]\n\t"
    "s_waitcnt lgkmcnt(5)\n\t"
    "v_mfma_f32_16x16x32_bf16 v[64:67], v[232:235], v[104:107], v[64:67]\n\t"
    "ds_read_b128 v[96:99], %[ea] offset:0\n\t"
    "v_mfma_f32_16x16x32_bf16 v[68:71], v[236:239], v[104:107], v[68:71]\n\t"
    "ds_read_b128 v[100:103], %[ea] offset:2048\n\t"
    "v_mfma_f32_16x16x32_bf16 v[72:75], v[232:235], v[108:111], v[72:75]\n\t"
    "v_mfma_f32_16x16x32_bf16 v[76:79], v[236:239], v[108:111], v[76:79]\n\t"
    "s_waitcnt lgkmcnt(4)\n\t"
    "v_mfma_f32_16x16x32_bf16 v[64:67], v[240:243], v[112:115], v[64:67]\n\t"
    "ds_read_b128 v[104:107], %[da] offset:0\n\t"
    "v_mad_u32_u24 v39, v39, %[mu], %[ad]\n\t"
    "v_mfma_f32_16x16x32_bf16 v[68:71], v[244:247], v[112:115], v[68:71]\n\t"
    "ds_read_b128 v[108:111], %[da] offset:2048\n\t"
    "v_cmp_ge_i32 vcc, %[lf], v39\n\t"
    "s_cmp_eq_u64 vcc, 0\n\t"
    "s_addc_u32 %[lf], %[lf], 0\n\t"
    "v_mfma_f32_16x16x32_bf16 v[72:75], v[240:243], v[116:119], v[72:75]\n\t"
    "v_mfma_f32_16x16x32_bf16 v[76:79], v[244:247], v[116:119], v[76:79]\n\t"
    "v_cmp_ge_i32 vcc, %[lf], v39\n\t"
    "s_cmp_eq_u64 vcc, 0\n\t"
    "s_addc_u32 %[lf], %[lf], 0\n\t"
    "s_waitcnt lgkmcnt(4)\n\t"
    "v_mfma_f32_16x16x32_bf16 v[80:83], v[248:251], v[120:123], v[64:67]\n\t"
    "ds_read_b128 v[112:115], %[ea] offset:4096\n\t"
    "s_min_i32 %[lf], %[lf], %[cp]\n\t"
    "s_add_u32 %[b], %[b], 1\n\t"
    "v_mfma_f32_16x16x32_bf16 v[84:87], v[252:255], v[120:123], v[68:71]\n\t"
    "ds_read_b128 v[116:119], %[ea] offset:6144\n\t"
    "v_mfma_f32_16x16x32_bf16 v[88:91], v[248:251], v[124:127], v[72:75]\n\t"
    "v_mfma_f32_16x16x32_bf16 v[92:95], v[252:255], v[124:127], v[76:79]\n\t"
    "s_add_u32 s38, s38, %[ab]\n\t"
    "s_addc_u32 s39, s39, 0\n\t"
    "s_xor_b32 %[l0], %[l0], 0x10000\n\t"
    "s_cmp_lt_i32 %[b], %[lf]\n\t"
    "s_cbranch_scc1 1b\n\t"
    "s_waitcnt lgkmcnt(0)\n\t"
    "s_nop 4"
    : "=&{v[80:95]}"(pd), [ea] "+&v"(ea), [da] "+&v"(da), [b] "+&s"(b), [lf] "+&s"(lf), "+{s[36:37]}"(p1), "+{s[38:39]}"(p0), [sa] "+&v"(sa), [l1] "+&s"(l1), [l0] "+&s"(l0)
    : [rw] "v"(rw), [ab] "s"(ab), [cp] "s"(cp), [mu] "s"(mu), [ad] "v"(ad), "{v[128:159]}"(W[0]), "{v[160:191]}"(W[1]), "{v[192:223]}"(W[2]), "{v[224:255]}"(W[3]), [av0] "v"(av[0]), [av1] "v"(av[1]), [av2] "v"(av[2]), [av3] "v"(av[3]), [av4] "v"(av[4]), [av5] "v"(av[5]), [av6] "v"(av[6]), [av7] "v"(av[7])
    : "v39", "v64", "v65", "v66", "v67", "v68", "v69", "v70", "v71", "v72", "v73", "v74", "v75", "v76", "v77", "v78", "v79", "v96", "v97", "v98", "v99", "v100", "v101", "v102", "v103", "v104", "v105", "v106", "v107", "v108", "v109", "v110", "v111", "v112", "v113", "v114", "v115", "v116", "v117", "v118", "v119", "v120", "v121", "v122", "v123", "v124", "v125", "v126", "v127", "vcc", "scc", "m0", "memory");
}
__device__ __forceinline__ void near_ld2(const u32x32 (&W)[4], u32x16& pd, unsigned ea, unsigned da, int& b, int& lf, int cp, unsigned sa, unsigned mu, unsigned ad,
    unsigned rw, unsigned ab, const char* p1, const char* p0, unsigned l1, unsigned l0, const unsigned (&av)[8]) {
  asm volatile("s_setprio 0\n\t"
    "s_waitcnt vmcnt(2)\n\t"
    "s_barrier\n\t"
    "ds_read_b128 v[96:99], %[ea] offset:0\n\t"
    "ds_read_b128 v[100:103], %[ea] offset:2048\n\t"
    "ds_read_b128 v[104:107], %[da] offset:0\n\t"
    "ds_read_b128 v[108:111], %[da] offset:2048\n\t"
    "ds_read_b128 v[112:115], %[ea] offset:4096\n\t"
    "ds_read_b128 v[116:119], %[ea] offset:6144\n\t"
    "s_branch 2f\n\t"
    "1:\n\t"
    "s_waitcnt vmcnt(2)\n\t"
    "s_nop 1\n\t"
    "s_barrier\n\t"
    "2:\n\t"
    "s_waitcnt lgkmcnt(4)\n\t"
    "v_mfma_f32_16x16x32_bf16 v[64:67], v[128:131], v[96:99], 0\n\t"
    "ds_read_b128 v[120:123], %[da] offset:4096\n\t"
    "s_add_u32 m0, %[l1], 0\n\t"
    "ds_write_b128 %[rw], v[80:83] offset:0\n\t"
    "v_mfma_f32_16x16x32_bf16 v[68:71], v[132:135], v[96:99], 0\n\t"
    "ds_read_b128 v[124:127], %[da] offset:6144\n\t"
    "global_load_lds_dwordx4 %[av0], s[36:37] sc1\n\t"
    "ds_write_b128 %[rw], v[84:87] offset:1024\n\t"
    "v_mfma_f32_16x16x32_bf16 v[72:75], v[128:131], v[100:103], 0\n\t"
    "ds_write_b128 %[rw], v[88:91] offset:2048\n\t"
    "v_mfma_f32_16x16x32_bf16 v[76:79], v[132:135], v[100:103], 0\n\t"
    "ds_write_b128 %[rw], v[92:95] offset:3072\n\t"
    "s_waitcnt lgkmcnt(8)\n\t"
    "v_mfma_f32_16x16x32_bf16 v[64:67], v[136:139], v[104:107], v[64:67]\n\t"
    "ds_read_b128 v[96:99], %[ea] offset:8192\n\t"
    "s_add_u32 m0, %[l1], 4096\n\t"
    "v_mfma_f32_16x16x32_bf16 v[68:71], v[140:143], v[104:107], v[68:71]\n\t"
    "ds_read_b128 v[100:103], %[ea] offset:10240\n\t"
    "global_load_lds_dwordx4 %[av2], s[36:37] sc1\n\t"
    "v_mfma_f32_16x16x32_bf16 v[72:75], v[136:139], v[108:111], v[72:75]\n\t"
    "v_mfma_f32_16x16x32_bf16 v[76:79], v[140:143], v[108:111], v[76:79]\n\t"
    "s_waitcnt lgkmcnt(2)\n\t"
    "v_mfma_f32_16x16x32_bf16 v[64:67], v[144:147], v[112:115], v[64:67]\n\t"
    "ds_read_b128 v[104:107], %[da] offset:8192\n\t"
    "s_add_u32 m0, %[l1], 8192\n\t"
    "v_mfma_f32_16x16x32_bf16 v[68:71], v[148:151], v[112:115], v[68:71]\n\t"
    "ds_read_b128 v[108:111], %[da] offset:10240\n\t"
    "global_load_lds_dwordx4 %[av4], s[36:37] sc1\n\t"
    "v_mfma_f32_16x16x32_bf16 v[72:75], v[144:147], v[116:119], v[72:75]\n\t"
    "v_mfma_f32_16x16x32_bf16 v[76:79], v[148:151], v[116:119], v[76:79]\n\t"
    "s_waitcnt vmcnt(3)\n\t"
    "s_barrier\n\t"
    "v_mfma_f32_16x16x32_bf16 v[64:67], v[152:155], v[120:123], v[64:67]\n\t"
    "ds_read_b128 v[112:115], %[ea] offset:12288\n\t"
    "s_add_u32 m0, %[l1], 12288\n\t"
    "v_mfma_f32_16x16x32_bf16 v[68:71], v[156:159], v[120:123], v[68:71]\n\t"
    "ds_read_b128 v[116:119], %[ea] offset:14336\n\t"
    "global_load_lds_dwordx4 %[av6], s[36:37] sc1\n\t"
    "v_mfma_f32_16x16x32_bf16 v[72:75], v[152:155], v[124:127], v[72:75]\n\t"
    "v_mfma_f32_16x16x32_bf16 v[76:79], v[156:159], v[124:127], v[76:79]\n\t"
    "s_waitcnt lgkmcnt(4)\n\t"
    "v_mfma_f32_16x16x32_bf16 v[64:67], v[160:163], v[96:99], v[64:67]\n\t"
    "ds_read_b128 v[120:123], %[da] offset:12288\n\t"
    "s_add_u32 m0, %[l0], 0\n\t"
    "v_mfma_f32_16x16x32_bf16 v[68:71], v[164:167], v[96:99], v[68:71]\n\t"
    "ds_read_b128 v[124:127], %[da] offset:14336\n\t"
    "global_load_lds_dwordx4 %[av0], s[38:39] sc1\n\t"
    "v_mfma_f32_16x16x32_bf16 v[72:75], v[160:163], v[100:103], v[72:75]\n\t"
    "v_mfma_f32_16x16x32_bf16 v[76:79], v[164:167], v[100:103], v[76:79]\n\t"
    "s_waitcnt lgkmcnt(4)\n\t"
    "v_mfma_f32_16x16x32_bf16 v[64:67], v[168:171], v[104:107], v[64:67]\n\t"
    "ds_read_b128 v[96:99], %[ea] offset:16384\n\t"
    "s_add_u32 m0, %[l0], 4096\n\t"
    "v_mfma_f32_16x16x32_bf16 v[68:71], v[172:175], v[104:107], v[68:71]\n\t"
    "ds_read_b128 v[100:103], %[ea] offset:18432\n\t"
    "global_load_lds_dwordx4 %[av2], s[38:39] sc1\n\t"
    "v_mfma_f32_16x16x32_bf16 v[72:75], v[168:171], v[108:111], v[72:75]\n\t"
    "v_mfma_f32_16x16x32_bf16 v[76:79], v[172:175], v[108:111], v[76:79]\n\t"
    "s_waitcnt lgkmcnt(4)\n\t"
    "v_mfma_f32_16x16x32_bf16 v[64:67], v[176:179], v[112:115], v[64:67]\n\t"
    "ds_read_b128 v[104:107], %[da] offset:16384\n\t"
    "s_add_u32 m0, %[l0], 8192\n\t"
    "v_mfma_f32_16x16x32_bf16 v[68:71], v[180:183], v[112:115], v[68:71]\n\t"
    "ds_read_b128 v[108:111], %[da] offset:18432\n\t"
    "global_load_lds_dwordx4 %[av4], s[38:39] sc1\n\t"
    "v_mfma_f32_16x16x32_bf16 v[72:75], v[176:179], v[116:119], v[72:75]\n\t"
    "v_mfma_f32_16x16x32_bf16 v[76:79], v[180:183], v[116:119], v[76:79]\n\t"
    "s_waitcnt lgkmcnt(4)\n\t"
    "v_mfma_f32_16x16x32_bf16 v[64:67], v[184:187], v[120:123], v[64:67]\n\t"
    "ds_read_b128 v[112:115], %[ea] offset:20480\n\t"
    "s_add_u32 m0, %[l0], 12288\n\t"
    "v_mfma_f32_16x16x32_bf16 v[68:71], v[188:191], v[120:123], v[68:71]\n\t"
    "ds_read_b128 v[116:119], %[ea] offset:22528\n\t"
    "global_load_lds_dwordx4 %[av6], s[38:39] sc1\n\t"
    "v_mfma_f32_16x16x32_bf16 v[72:75], v[184:187], v[124:127], v[72:75]\n\t"
    "v_mfma_f32_16x16x32_bf16 v[76:79], v[188:191], v[124:127], v[76:79]\n\t"
    "s_waitcnt lgkmcnt(4)\n\t"
    "v_mfma_f32_16x16x32_bf16 v[64:67], v[192:195], v[96:99], v[64:67]\n\t"
    "ds_read_b128 v[120:123], %[da] offset:20480\n\t"
    "v_mfma_f32_16x16x32_bf16 v[68:71], v[196:199], v[96:99], v[68:71]\n\t"
    "ds_read_b128 v[124:127], %[da] offset:22528\n\t"
    "v_mfma_f32_16x16x32_bf16 v[72:75], v[192:195], v[100:103], v[72:75]\n\t"
    "v_mfma_f32_16x16x32_bf16 v[76:79], v[196:199], v[100:103], v[76:79]\n\t"
    "s_waitcnt lgkmcnt(4)\n\t"
    "v_mfma_f32_16x16x32_bf16 v[64:67], v[200:203], v[104:107], v[64:67]\n\t"
    "ds_read_b128 v[96:99], %[ea] offset:24576\n\t"
    "v_mfma_f32_16x16x32_bf16 v[68:71], v[204:207], v[104:107], v[68:71]\n\t"
    "ds_read_b128 v[100:103], %[ea] offset:26624\n\t"
    "v_mfma_f32_16x16x32_bf16 v[72:75], v[200:203], v[108:111], v[72:75]\n\t"
    "v_mfma_f32_16x16x32_bf16 v[76:79], v[204:207], v[108:111], v[76:79]\n\t"
    "s_add_u32 s36, s36, %[ab]\n\t"
    "s_addc_u32 s37, s37, 0\n\t"
    "s_waitcnt lgkmcnt(4)\n\t"
    "v_mfma_f32_16x16x32_bf16 v[64:67], v[208:211], v[112:115], v[64:67]\n\t"
    "ds_read_b128 v[104:107], %[da] offset:24576\n\t"
    "v_mfma_f32_16x16x32_bf16 v[68:71], v[212:215], v[112:115], v[68:71]\n\t"
    "ds_read_b128 v[108:111], %[da] offset:26624\n\t"
    "v_mfma_f32_16x16x32_bf16 v[72:75], v[208:211], v[116:119], v[72:75]\n\t"
    "v_mfma_f32_16x16x32_bf16 v[76:79], v[212:215], v[116:119], v[76:79]\n\t"
    "s_xor_b32 %[l1], %[l1], 0x10000\n\t"
    "s_waitcnt lgkmcnt(4)\n\t"
    "v_mfma_f32_16x16x32_bf16 v[64:67], v[216:219], v[120:123], v[64:67]\n\t"
    "ds_read_b128 v[112:115], %[ea] offset:28672\n\t"
    "v_mfma_f32_16x16x32_bf16 v[68:71], v[220:223], v[120:123], v[68:71]\n\t"
    "ds_read_b128 v[116:119], %[ea] offset:30720\n\t"
    "v_mfma_f32_16x16x32_bf16 v[72:75], v[216:219], v[124:127], v[72:75]\n\t"
    "v_xor_b32 %[ea], 0x10000, %[ea]\n\t"
    "v_mfma_f32_16x16x32_bf16 v[76:79], v[220:223], v[124:127], v[76:79]\n\t"
    "s_waitcnt vmcnt(6)\n\t"
    "s_barrier\n\t"
    "s_waitcnt lgkmcnt(4)\n\t"
    "v_mfma_f32_16x16x32_bf16 v[64:67], v[224:227], v[96:99], v[64:67]\n\t"
    "ds_read_b32 v39, %[sa]\n\t"
    "ds_read_b128 v[120:123], %[da] offset:28672\n\t"
    "v_mfma_f32_16x16x32_bf16 v[68:71], v[228:231], v[96:99], v[68:71]\n\t"
    "ds_read_b128 v[124:127], %[da] offset:30720\n\t"
    "v_mfma_f32_16x16x32_bf16 v[72:75], v[224:227], v[100:103], v[72:75]\n\t"
    "v_xor_b32 %[da], 0x10000, %[da]\n\t"
    "v_mfma_f32_16x16x32_bf16 v[76:79], v[228:231], v[100:103], v[76:79]\n\t"
    "s_waitcnt lgkmcnt(5)\n\t"
    "v_mfma_f32_16x16x32_bf16 v[64:67], v[232:235], v[104:107], v[64:67]\n\t"
    "ds_read_b128 v[96:99], %[ea] offset:0\n\t"
    "v_mfma_f32_16x16x32_bf16 v[68:71], v[236:239], v[104:107], v[68:71]\n\t"
    "ds_read_b128 v[100:103], %[ea] offset:2048\n\t"
    "v_mfma_f32_16x16x32_bf16 v[72:75], v[232:235], v[108:111], v[72:75]\n\t"
    "v_mfma_f32_16x16x32_bf16 v[76:79], v[236:239], v[108:111], v[76:79]\n\t"
    "s_waitcnt lgkmcnt(4)\n\t"
    "v_mfma_f32_16x16x32_bf16 v[64:67], v[240:243], v[112:115], v[64:67]\n\t"
    "ds_read_b128 v[104:107], %[da] offset:0\n\t"
    "v_mad_u32_u24 v39, v39, %[mu], %[ad]\n\t"
    "v_mfma_f32_16x16x32_bf16 v[68:71], v[244:247], v[112:115], v[68:71]\n\t"
    "ds_read_b128 v[108:111], %[da] offset:2048\n\t"
    "v_cmp_ge_i32 vcc, %[lf], v39\n\t"
    "s_cmp_eq_u64 vcc, 0\n\t"
    "s_addc_u32 %[lf], %[lf], 0\n\t"
    "v_mfma_f32_16x16x32_bf16 v[72:75], v[240:243], v[116:119], v[72:75]\n\t"
    "v_mfma_f32_16x16x32_bf16 v[76:79], v[244:247], v[116:119], v[76:79]\n\t"
    "v_cmp_ge_i32 vcc, %[lf], v39\n\t"
    "s_cmp_eq_u64 vcc, 0\n\t"
    "s_addc_u32 %[lf], %[lf], 0\n\t"
    "s_waitcnt lgkmcnt(4)\n\t"
    "v_mfma_f32_16x16x32_bf16 v[80:83], v[248:251], v[120:123], v[64:67]\n\t"
    "ds_read_b128 v[112:115], %[ea] offset:4096\n\t"
    "s_min_i32 %[lf], %[lf], %[cp]\n\t"
    "s_add_u32 %[b], %[b], 1\n\t"
    "v_mfma_f32_16x16x32_bf16 v[84:87], v[252:255], v[120:123], v[68:71]\n\t"
    "ds_read_b128 v[116:119], %[ea] offset:6144\n\t"
    "v_mfma_f32_16x16x32_bf16 v[88:91], v[248:251], v[124:127], v[72:75]\n\t"
    "v_mfma_f32_16x16x32_bf16 v[92:95], v[252:255], v[124:127], v[76:79]\n\t"
    "s_add_u32 s38, s38, %[ab]\n\t"
    "s_addc_u32 s39, s39, 0\n\t"
    "s_xor_b32 %[l0], %[l0], 0x10000\n\t"
    "s_cmp_lt_i32 %[b], %[lf]\n\t"
    "s_cbranch_scc1 1b\n\t"
    "s_waitcnt lgkmcnt(0)\n\t"
    "s_nop 4"
    : "=&{v[80:95]}"(pd), [ea] "+&v"(ea), [da] "+&v"(da), [b] "+&s"(b), [lf] "+&s"(lf), "+{s[36:37]}"(p1), "+{s[38:39]}"(p0), [sa] "+&v"(sa), [l1] "+&s"(l1), [l0] "+&s"(l0)
    : [rw] "v"(rw), [ab] "s"(ab), [cp] "s"(cp), [mu] "s"(mu), [ad] "v"(ad), "{v[128:159]}"(W[0]), "{v[160:191]}"(W[1]), "{v[192:223]}"(W[2]), "{v[224:255]}"(W[3]), [av0] "v"(av[0]), [av1] "v"(av[1]), [av2] "v"(av[2]), [av3] "v"(av[3]), [av4] "v"(av[4]), [av5] "v"(av[5]), [av6] "v"(av[6]), [av7] "v"(av[7])
    : "v39", "v64", "v65", "v66", "v67", "v68", "v69", "v70", "v71", "v72", "v73", "v74", "v75", "v76", "v77", "v78", "v79", "v96", "v97", "v98", "v99", "v100", "v101", "v102", "v103", "v104", "v105", "v106", "v107", "v108", "v109", "v110", "v111", "v112", "v113", "v114", "v115", "v116", "v117", "v118", "v119", "v120", "v121", "v122", "v123", "v124", "v125", "v126", "v127", "vcc", "scc", "m0", "memory");
}
__device__ __forceinline__ void near_st0(const u32x32 (&W)[4], u32x16& pd, unsigned ea, unsigned da, int& b, int& lf, int cp, unsigned sa, unsigned mu, unsigned ad,
    unsigned rw, unsigned ab, const char* p1, const char* p0, unsigned co0, unsigned co1, int po, int pf, unsigned w0, unsigned sl, unsigned dv, const char* db, const char* pg) {
  unsigned st;
  asm volatile("s_barrier\n\t"
    "ds_read_b128 v[96:99], %[ea] offset:0\n\t"
    "ds_read_b128 v[100:103], %[ea] offset:2048\n\t"
    "ds_read_b128 v[104:107], %[da] offset:0\n\t"
    "ds_read_b128 v[108:111], %[da] offset:2048\n\t"
    "ds_read_b128 v[112:115], %[ea] offset:4096\n\t"
    "ds_read_b128 v[116:119], %[ea] offset:6144\n\t"
    "s_branch 2f\n\t"
    "1:\n\t"
    "s_barrier\n\t"
    "2:\n\t"
    "s_waitcnt lgkmcnt(4)\n\t"
    "v_mfma_f32_16x16x32_bf16 v[64:67], v[128:131], v[96:99], 0\n\t"
    "ds_read_b128 v[120:123], %[da] offset:4096\n\t"
    "v_mfma_f32_16x16x32_bf16 v[68:71], v[132:135], v[96:99], 0\n\t"
    "ds_read_b128 v[124:127], %[da] offset:6144\n\t"
    "v_mfma_f32_16x16x32_bf16 v[72:75], v[128:131], v[100:103], 0\n\t"
    "v_mfma_f32_16x16x32_bf16 v[76:79], v[132:135], v[100:103], 0\n\t"
    "s_waitcnt lgkmcnt(4)\n\t"
    "v_mfma_f32_16x16x32_bf16 v[64:67], v[136:139], v[104:107], v[64:67]\n\t"
    "ds_read_b128 v[96:99], %[ea] offset:8192\n\t"
    "v_mfma_f32_16x16x32_bf16 v[68:71], v[140:143], v[104:107], v[68:71]\n\t"
    "ds_read_b128 v[100:103], %[ea] offset:10240\n\t"
    "v_mfma_f32_16x16x32_bf16 v[72:75], v[136:139], v[108:111], v[72:75]\n\t"
    "v_mfma_f32_16x16x32_bf16 v[76:79], v[140:143], v[108:111], v[76:79]\n\t"
    "s_waitcnt lgkmcnt(4)\n\t"
    "v_mfma_f32_16x16x32_bf16 v[64:67], v[144:147], v[112:115], v[64:67]\n\t"
    "ds_read_b128 v[104:107], %[da] offset:8192\n\t"
    "v_mfma_f32_16x16x32_bf16 v[68:71], v[148:151], v[112:115], v[68:71]\n\t"
    "ds_read_b128 v[108:111], %[da] offset:10240\n\t"
    "v_mfma_f32_16x16x32_bf16 v[72:75], v[144:147], v[116:119], v[72:75]\n\t"
    "v_mfma_f32_16x16x32_bf16 v[76:79], v[148:151], v[116:119], v[76:79]\n\t"
    "s_waitcnt vmcnt(0)\n\t"
    "s_barrier\n\t"
    "s_waitcnt lgkmcnt(4)\n\t"
    "v_mfma_f32_16x16x32_bf16 v[64:67], v[152:155], v[120:123], v[64:67]\n\t"
    "ds_read_b128 v[112:115], %[ea] offset:12288\n\t"
    "v_mfma_f32_16x16x32_bf16 v[68:71], v[156:159], v[120:123], v[68:71]\n\t"
    "ds_read_b128 v[116:119], %[ea] offset:14336\n\t"
    "v_mfma_f32_16x16x32_bf16 v[72:75], v[152:155], v[124:127], v[72:75]\n\t"
    "ds_read_b128 v[48:51], %[rw] offset:0\n\t"
    "v_mfma_f32_16x16x32_bf16 v[76:79], v[156:159], v[124:127], v[76:79]\n\t"
    "ds_read_b128 v[52:55], %[rw] offset:1024\n\t"
    "s_cmp_eq_u32 %[w0], 0\n\t"
    "s_cbranch_scc1 5f\n\t"
    "6:\n\t"
    "s_waitcnt lgkmcnt(6)\n\t"
    "v_mfma_f32_16x16x32_bf16 v[64:67], v[160:163], v[96:99], v[64:67]\n\t"
    "ds_read_b128 v[120:123], %[da] offset:12288\n\t"
    "v_mfma_f32_16x16x32_bf16 v[68:71], v[164:167], v[96:99], v[68:71]\n\t"
    "ds_read_b128 v[124:127], %[da] offset:14336\n\t"
    "v_mfma_f32_16x16x32_bf16 v[72:75], v[160:163], v[100:103], v[72:75]\n\t"
    "ds_read_b128 v[56:59], %[rw] offset:2048\n\t"
    "v_mfma_f32_16x16x32_bf16 v[76:79], v[164:167], v[100:103], v[76:79]\n\t"
    "ds_read_b128 v[60:63], %[rw] offset:3072\n\t"
    "s_waitcnt lgkmcnt(0)\n\t"
    "v_mfma_f32_16x16x32_bf16 v[64:67], v[168:171], v[104:107], v[64:67]\n\t"
    "ds_read_b128 v[96:99], %[ea] offset:16384\n\t"
    "v_add_f32 v48, v48, v80\n\t"
    "v_add_f32 v49, v49, v81\n\t"
    "v_mfma_f32_16x16x32_bf16 v[68:71], v[172:175], v[104:107], v[68:71]\n\t"
    "ds_read_b128 v[100:103], %[ea] offset:18432\n\t"
    "v_add_f32 v50, v50, v82\n\t"
    "v_mfma_f32_16x16x32_bf16 v[72:75], v[168:171], v[108:111], v[72:75]\n\t"
    "v_add_f32 v51, v51, v83\n\t"
    "v_mfma_f32_16x16x32_bf16 v[76:79], v[172:175], v[108:111], v[76:79]\n\t"
    "v_add_f32 v52, v52, v84\n\t"
    "v_add_f32 v53, v53, v85\n\t"
    "v_mfma_f32_16x16x32_bf16 v[64:67], v[176:179], v[112:115], v[64:67]\n\t"
    "ds_read_b128 v[104:107], %[da] offset:16384\n\t"
    "v_add_f32 v54, v54, v86\n\t"
    "v_mfma_f32_16x16x32_bf16 v[68:71], v[180:183], v[112:115], v[68:71]\n\t"
    "ds_read_b128 v[108:111], %[da] offset:18432\n\t"
    "v_add_f32 v55, v55, v87\n\t"
    "v_mfma_f32_16x16x32_bf16 v[72:75], v[176:179], v[116:119], v[72:75]\n\t"
    "v_add_f32 v56, v56, v88\n\t"
    "v_add_f32 v57, v57, v89\n\t"
    "v_mfma_f32_16x16x32_bf16 v[76:79], v[180:183], v[116:119], v[76:79]\n\t"
    "v_add_f32 v58, v58, v90\n\t"
    "v_mfma_f32_16x16x32_bf16 v[64:67], v[184:187], v[120:123], v[64:67]\n\t"
    "ds_read_b128 v[112:115], %[ea] offset:20480\n\t"
    "v_add_f32 v59, v59, v91\n\t"
    "v_mfma_f32_16x16x32_bf16 v[68:71], v[188:191], v[120:123], v[68:71]\n\t"
    "ds_read_b128 v[116:119], %[ea] offset:22528\n\t"
    "v_add_f32 v60, v60, v92\n\t"
    "v_mfma_f32_16x16x32_bf16 v[72:75], v[184:187], v[124:127], v[72:75]\n\t"
    "v_add_f32 v61, v61, v93\n\t"
    "v_add_f32 v62, v62, v94\n\t"
    "v_mfma_f32_16x16x32_bf16 v[76:79], v[188:191], v[124:127], v[76:79]\n\t"
    "v_add_f32 v63, v63, v95\n\t"
    "s_waitcnt lgkmcnt(4)\n\t"
    "v_mfma_f32_16x16x32_bf16 v[64:67], v[192:195], v[96:99], v[64:67]\n\t"
    "ds_read_b128 v[120:123], %[da] offset:20480\n\t"
    "v_cvt_pk_bf16_f32 v40, v48, v49\n\t"
    "v_mfma_f32_16x16x32_bf16 v[68:71], v[196:199], v[96:99], v[68:71]\n\t"
    "ds_read_b128 v[124:127], %[da] offset:22528\n\t"
    "v_cvt_pk_bf16_f32 v41, v50, v51\n\t"
    "v_cvt_pk_bf16_f32 v42, v52, v53\n\t"
    "v_mfma_f32_16x16x32_bf16 v[72:75], v[192:195], v[100:103], v[72:75]\n\t"
    "v_cvt_pk_bf16_f32 v43, v54, v55\n\t"
    "v_mfma_f32_16x16x32_bf16 v[76:79], v[196:199], v[100:103], v[76:79]\n\t"
    "global_store_dwordx4 %[co0], v[40:43], s[36:37] sc1\n\t"
    "s_waitcnt lgkmcnt(4)\n\t"
    "v_mfma_f32_16x16x32_bf16 v[64:67], v[200:203], v[104:107], v[64:67]\n\t"
    "ds_read_b128 v[96:99], %[ea] offset:24576\n\t"
    "v_cvt_pk_bf16_f32 v44, v56, v57\n\t"
    "v_cvt_pk_bf16_f32 v45, v58, v59\n\t"
    "v_mfma_f32_16x16x32_bf16 v[68:71], v[204:207], v[104:107], v[68:71]\n\t"
    "ds_read_b128 v[100:103], %[ea] offset:26624\n\t"
    "v_cvt_pk_bf16_f32 v46, v60, v61\n\t"
    "v_mfma_f32_16x16x32_bf16 v[72:75], v[200:203], v[108:111], v[72:75]\n\t"
    "v_cvt_pk_bf16_f32 v47, v62, v63\n\t"
    "v_mfma_f32_16x16x32_bf16 v[76:79], v[204:207], v[108:111], v[76:79]\n\t"
    "global_store_dwordx4 %[co1], v[44:47], s[36:37] sc1\n\t"
    "s_waitcnt lgkmcnt(4)\n\t"
    "v_mfma_f32_16x16x32_bf16 v[64:67], v[208:211], v[112:115], v[64:67]\n\t"
    "ds_read_b128 v[104:107], %[da] offset:24576\n\t"
    "v_mfma_f32_16x16x32_bf16 v[68:71], v[212:215], v[112:115], v[68:71]\n\t"
    "ds_read_b128 v[108:111], %[da] offset:26624\n\t"
    "v_mfma_f32_16x16x32_bf16 v[72:75], v[208:211], v[116:119], v[72:75]\n\t"
    "v_mfma_f32_16x16x32_bf16 v[76:79], v[212:215], v[116:119], v[76:79]\n\t"
    "s_waitcnt lgkmcnt(4)\n\t"
    "v_mfma_f32_16x16x32_bf16 v[64:67], v[216:219], v[120:123], v[64:67]\n\t"
    "ds_read_b128 v[112:115], %[ea] offset:28672\n\t"
    "v_mfma_f32_16x16x32_bf16 v[68:71], v[220:223], v[120:123], v[68:71]\n\t"
    "ds_read_b128 v[116:119], %[ea] offset:30720\n\t"
    "v_mfma_f32_16x16x32_bf16 v[72:75], v[216:219], v[124:127], v[72:75]\n\t"
    "v_xor_b32 %[ea], 0x10000, %[ea]\n\t"
    "v_mfma_f32_16x16x32_bf16 v[76:79], v[220:223], v[124:127], v[76:79]\n\t"
    "s_waitcnt vmcnt(2)\n\t"
    "s_barrier\n\t"
    "s_waitcnt lgkmcnt(4)\n\t"
    "v_mfma_f32_16x16x32_bf16 v[64:67], v[224:227], v[96:99], v[64:67]\n\t"
    "ds_read_b32 v39, %[sa]\n\t"
    "ds_read_b128 v[120:123], %[da] offset:28672\n\t"
    "v_mfma_f32_16x16x32_bf16 v[68:71], v[228:231], v[96:99], v[68:71]\n\t"
    "ds_read_b128 v[124:127], %[da] offset:30720\n\t"
    "v_mfma_f32_16x16x32_bf16 v[72:75], v[224:227], v[100:103], v[72:75]\n\t"
    "v_xor_b32 %[da], 0x10000, %[da]\n\t"
    "v_mfma_f32_16x16x32_bf16 v[76:79], v[228:231], v[100:103], v[76:79]\n\t"
    "s_waitcnt lgkmcnt(5)\n\t"
    "v_mfma_f32_16x16x32_bf16 v[64:67], v[232:235], v[104:107], v[64:67]\n\t"
    "ds_read_b128 v[96:99], %[ea] offset:0\n\t"
    "v_mfma_f32_16x16x32_bf16 v[68:71], v[236:239], v[104:107], v[68:71]\n\t"
    "ds_read_b128 v[100:103], %[ea] offset:2048\n\t"
    "v_mfma_f32_16x16x32_bf16 v[72:75], v[232:235], v[108:111], v[72:75]\n\t"
    "v_mfma_f32_16x16x32_bf16 v[76:79], v[236:239], v[108:111], v[76:79]\n\t"
    "s_waitcnt lgkmcnt(4)\n\t"
    "v_mfma_f32_16x16x32_bf16 v[64:67], v[240:243], v[112:115], v[64:67]\n\t"
    "ds_read_b128 v[104:107], %[da] offset:0\n\t"
    "v_mad_u32_u24 v39, v39, %[mu], %[ad]\n\t"
    "v_mfma_f32_16x16x32_bf16 v[68:71], v[244:247], v[112:115], v[68:71]\n\t"
    "ds_read_b128 v[108:111], %[da] offset:2048\n\t"
    "v_cmp_ge_i32 vcc, %[lf], v39\n\t"
    "s_cmp_eq_u64 vcc, 0\n\t"
    "s_addc_u32 %[lf], %[lf], 0\n\t"
    "v_mfma_f32_16x16x32_bf16 v[72:75], v[240:243], v[116:119], v[72:75]\n\t"
    "v_mfma_f32_16x16x32_bf16 v[76:79], v[244:247], v[116:119], v[76:79]\n\t"
    "v_cmp_ge_i32 vcc, %[lf], v39\n\t"
    "s_cmp_eq_u64 vcc, 0\n\t"
    "s_addc_u32 %[lf], %[lf], 0\n\t"
    "s_waitcnt lgkmcnt(4)\n\t"
    "v_mfma_f32_16x16x32_bf16 v[80:83], v[248:251], v[120:123], v[64:67]\n\t"
    "ds_read_b128 v[112:115], %[ea] offset:4096\n\t"
    "s_min_i32 %[lf], %[lf], %[cp]\n\t"
    "s_add_u32 %[b], %[b], 1\n\t"
    "v_mfma_f32_16x16x32_bf16 v[84:87], v[252:255], v[120:123], v[68:71]\n\t"
    "ds_read_b128 v[116:119], %[ea] offset:6144\n\t"
    "v_mfma_f32_16x16x32_bf16 v[88:91], v[248:251], v[124:127], v[72:75]\n\t"
    "v_mfma_f32_16x16x32_bf16 v[92:95], v[252:255], v[124:127], v[76:79]\n\t"
    "s_mov_b64 s[36:37], s[38:39]\n\t"
    "s_add_u32 s38, s38, %[ab]\n\t"
    "s_addc_u32 s39, s39, 0\n\t"
    "s_cmp_lt_i32 %[b], %[lf]\n\t"
    "s_cbranch_scc1 1b\n\t"
    "s_waitcnt lgkmcnt(0)\n\t"
    "s_nop 4\n\t"
    "s_branch 9f\n\t"
    "5:\n\t"
    "s_add_u32 %[st], %[b], %[po]\n\t"
    "s_max_i32 %[st], %[st], %[p0]\n\t"
    "v_mov_b32 v39, %[st]\n\t"
    "v_mov_b32 v38, 0\n\t"
    "s_mov_b64 exec, 1\n\t"
    "global_store_dword v38, v39, %[pg] sc1\n\t"
    "s_mov_b64 exec, -1\n\t"
    "s_mov_b32 %[p0], %[p1]\n\t"
    "s_cmp_ge_i32 %[lf], %[cp]\n\t"
    "s_cbranch_scc1 6b\n\t"
    "s_mov_b32 m0, %[sl]\n\t"
    "s_nop 0\n\t"
    "global_load_lds_dword %[dv], %[db] sc1\n\t"
    "s_branch 6b\n\t"
    "9:"
    : "=&{v[80:95]}"(pd), [ea] "+&v"(ea), [da] "+&v"(da), [b] "+&s"(b), [lf] "+&s"(lf), "+{s[36:37]}"(p1), "+{s[38:39]}"(p0), [sa] "+&v"(sa), [st] "=&s"(st), [sl] "+&s"(sl), [p0] "+&s"(pf)
    : [rw] "v"(rw), [ab] "s"(ab), [cp] "s"(cp), [mu] "s"(mu), [ad] "v"(ad), "{v[128:159]}"(W[0]), "{v[160:191]}"(W[1]), "{v[192:223]}"(W[2]), "{v[224:255]}"(W[3]), [co0] "v"(co0), [w0] "s"(w0), [dv] "v"(dv), [db] "s"(db), [pg] "s"(pg), [po] "s"(po - 1), [p1] "s"(b + po), [co1] "v"(co1)
    : "v38", "v39", "v40", "v41", "v42", "v43", "v44", "v45", "v46", "v47", "v48", "v49", "v50", "v51", "v52", "v53", "v54", "v55", "v56", "v57", "v58", "v59", "v60", "v61", "v62", "v63", "v64", "v65", "v66", "v67", "v68", "v69", "v70", "v71", "v72", "v73", "v74", "v75", "v76", "v77", "v78", "v79", "v96", "v97", "v98", "v99", "v100", "v101", "v102", "v103", "v104", "v105", "v106", "v107", "v108", "v109", "v110", "v111", "v112", "v113", "v114", "v115", "v116", "v117", "v118", "v119", "v120", "v121", "v122", "v123", "v124", "v125", "v126", "v127", "vcc", "scc", "m0", "memory");
}
__device__ __forceinline__ void near_st1(const u32x32 (&W)[4], u32x16& pd, unsigned ea, unsigned da, int& b, int& lf, int cp, unsigned sa, unsigned mu, unsigned ad,
    unsigned rw, unsigned ab, const char* p1, const char* p0, unsigned co0, unsigned co1, int po, int pf, unsigned w0, unsigned sl, unsigned dv, const char* db, const char* pg) {
  unsigned st;
  asm volatile("s_barrier\n\t"
    "ds_read_b128 v[96:99], %[ea] offset:0\n\t"
    "ds_read_b128 v[100:103], %[ea] offset:2048\n\t"
    "ds_read_b128 v[104:107], %[da] offset:0\n\t"
    "ds_read_b128 v[108:111], %[da] offset:2048\n\t"
    "ds_read_b128 v[112:115], %[ea] offset:4096\n\t"
    "ds_read_b128 v[116:119], %[ea] offset:6144\n\t"
    "s_branch 2f\n\t"
    "1:\n\t"
    "s_barrier\n\t"
    "2:\n\t"
    "s_waitcnt lgkmcnt(4)\n\t"
    "v_mfma_f32_16x16x32_bf16 v[64:67], v[128:131], v[96:99], 0\n\t"
    "ds_read_b128 v[120:123], %[da] offset:4096\n\t"
    "v_mfma_f32_16x16x32_bf16 v[68:71], v[132:135], v[96:99], 0\n\t"
    "ds_read_b128 v[124:127], %[da] offset:6144\n\t"
    "v_mfma_f32_16x16x32_bf16 v[72:75], v[128:131], v[100:103], 0\n\t"
    "v_mfma_f32_16x16x32_bf16 v[76:79], v[132:135], v[100:103], 0\n\t"
    "s_waitcnt lgkmcnt(4)\n\t"
    "v_mfma_f32_16x16x32_bf16 v[64:67], v[136:139], v[104:107], v[64:67]\n\t"
    "ds_read_b128 v[96:99], %[ea] offset:8192\n\t"
    "v_mfma_f32_16x16x32_bf16 v[68:71], v[140:143], v[104:107], v[68:71]\n\t"
    "ds_read_b128 v[100:103], %[ea] offset:10240\n\t"
    "v_mfma_f32_16x16x32_bf16 v[72:75], v[136:139], v[108:111], v[72:75]\n\t"
    "v_mfma_f32_16x16x32_bf16 v[76:79], v[140:143], v[108:111], v[76:79]\n\t"
    "s_waitcnt lgkmcnt(4)\n\t"
    "v_mfma_f32_16x16x32_bf16 v[64:67], v[144:147], v[112:115], v[64:67]\n\t"
    "ds_read_b128 v[104:107], %[da] offset:8192\n\t"
    "v_mfma_f32_16x16x32_bf16 v[68:71], v[148:151], v[112:115], v[68:71]\n\t"
    "ds_read_b128 v[108:111], %[da] offset:10240\n\t"
    "v_mfma_f32_16x16x32_bf16 v[72:75], v[144:147], v[116:119], v[72:75]\n\t"
    "v_mfma_f32_16x16x32_bf16 v[76:79], v[148:151], v[116:119], v[76:79]\n\t"
    "s_waitcnt vmcnt(0)\n\t"
    "s_barrier\n\t"
    "s_waitcnt lgkmcnt(4)\n\t"
    "v_mfma_f32_16x16x32_bf16 v[64:67], v[152:155], v[120:123], v[64:67]\n\t"
    "ds_read_b128 v[112:115], %[ea] offset:12288\n\t"
    "v_mfma_f32_16x16x32_bf16 v[68:71], v[156:159], v[120:123], v[68:71]\n\t"
    "ds_read_b128 v[116:119], %[ea] offset:14336\n\t"
    "v_mfma_f32_16x16x32_bf16 v[72:75], v[152:155], v[124:127], v[72:75]\n\t"
    "ds_read_b128 v[48:51], %[rw] offset:0\n\t"
    "v_mfma_f32_16x16x32_bf16 v[76:79], v[156:159], v[124:127], v[76:79]\n\t"
    "ds_read_b128 v[52:55], %[rw] offset:1024\n\t"
    "s_cmp_eq_u32 %[w0], 0\n\t"
    "s_cbranch_scc1 5f\n\t"
    "6:\n\t"
    "s_waitcnt lgkmcnt(6)\n\t"
    "v_mfma_f32_16x16x32_bf16 v[64:67], v[160:163], v[96:99], v[64:67]\n\t"
    "ds_read_b128 v[120:123], %[da] offset:12288\n\t"
    "v_mfma_f32_16x16x32_bf16 v[68:71], v[164:167], v[96:99], v[68:71]\n\t"
    "ds_read_b128 v[124:127], %[da] offset:14336\n\t"
    "v_mfma_f32_16x16x32_bf16 v[72:75], v[160:163], v[100:103], v[72:75]\n\t"
    "ds_read_b128 v[56:59], %[rw] offset:2048\n\t"
    "v_mfma_f32_16x16x32_bf16 v[76:79], v[164:167], v[100:103], v[76:79]\n\t"
    "ds_read_b128 v[60:63], %[rw] offset:3072\n\t"
    "s_waitcnt lgkmcnt(0)\n\t"
    "v_mfma_f32_16x16x32_bf16 v[64:67], v[168:171], v[104:107], v[64:67]\n\t"
    "ds_read_b128 v[96:99], %[ea] offset:16384\n\t"
    "v_add_f32 v48, v48, v80\n\t"
    "v_add_f32 v49, v49, v81\n\t"
    "v_add_f32 v50, v50, v82\n\t"
    "v_mfma_f32_16x16x32_bf16 v[68:71], v[172:175], v[104:107], v[68:71]\n\t"
    "ds_read_b128 v[100:103], %[ea] offset:18432\n\t"
    "v_add_f32 v51, v51, v83\n\t"
    "v_add_f32 v52, v52, v84\n\t"
    "v_add_f32 v53, v53, v85\n\t"
    "v_mfma_f32_16x16x32_bf16 v[72:75], v[168:171], v[108:111], v[72:75]\n\t"
    "v_add_f32 v54, v54, v86\n\t"
    "v_add_f32 v55, v55, v87\n\t"
    "v_add_f32 v56, v56, v88\n\t"
    "v_mfma_f32_16x16x32_bf16 v[76:79], v[172:175], v[108:111], v[76:79]\n\t"
    "v_add_f32 v57, v57, v89\n\t"
    "v_add_f32 v58, v58, v90\n\t"
    "v_add_f32 v59, v59, v91\n\t"
    "v_mfma_f32_16x16x32_bf16 v[64:67], v[176:179], v[112:115], v[64:67]\n\t"
    "ds_read_b128 v[104:107], %[da] offset:16384\n\t"
    "v_add_f32 v60, v60, v92\n\t"
    "v_add_f32 v61, v61, v93\n\t"
    "v_add_f32 v62, v62, v94\n\t"
    "v_mfma_f32_16x16x32_bf16 v[68:71], v[180:183], v[112:115], v[68:71]\n\t"
    "ds_read_b128 v[108:111], %[da] offset:18432\n\t"
    "v_add_f32 v63, v63, v95\n\t"
    "v_mul_f32 v40, %[nl], v48\n\t"
    "v_mul_f32 v41, %[nl], v49\n\t"
    "v_mfma_f32_16x16x32_bf16 v[72:75], v[176:179], v[116:119], v[72:75]\n\t"
    "v_mul_f32 v42, %[nl], v50\n\t"
    "v_mul_f32 v43, %[nl], v51\n\t"
    "v_mul_f32 v44, %[nl], v56\n\t"
    "v_mfma_f32_16x16x32_bf16 v[76:79], v[180:183], v[116:119], v[76:79]\n\t"
    "v_mul_f32 v45, %[nl], v57\n\t"
    "v_mul_f32 v46, %[nl], v58\n\t"
    "v_mul_f32 v47, %[nl], v59\n\t"
    "v_mfma_f32_16x16x32_bf16 v[64:67], v[184:187], v[120:123], v[64:67]\n\t"
    "ds_read_b128 v[112:115], %[ea] offset:20480\n\t"
    "v_exp_f32 v40, v40\n\t"
    "v_exp_f32 v41, v41\n\t"
    "v_exp_f32 v42, v42\n\t"
    "v_mfma_f32_16x16x32_bf16 v[68:71], v[188:191], v[120:123], v[68:71]\n\t"
    "ds_read_b128 v[116:119], %[ea] offset:22528\n\t"
    "v_exp_f32 v43, v43\n\t"
    "v_exp_f32 v44, v44\n\t"
    "v_exp_f32 v45, v45\n\t"
    "v_mfma_f32_16x16x32_bf16 v[72:75], v[184:187], v[124:127], v[72:75]\n\t"
    "v_exp_f32 v46, v46\n\t"
    "v_exp_f32 v47, v47\n\t"
    "v_add_f32 v40, 1.0, v40\n\t"
    "v_mfma_f32_16x16x32_bf16 v[76:79], v[188:191], v[124:127], v[76:79]\n\t"
    "v_add_f32 v41, 1.0, v41\n\t"
    "v_add_f32 v42, 1.0, v42\n\t"
    "v_add_f32 v43, 1.0, v43\n\t"
    "s_waitcnt lgkmcnt(4)\n\t"
    "v_mfma_f32_16x16x32_bf16 v[64:67], v[192:195], v[96:99], v[64:67]\n\t"
    "ds_read_b128 v[120:123], %[da] offset:20480\n\t"
    "v_add_f32 v44, 1.0, v44\n\t"
    "v_add_f32 v45, 1.0, v45\n\t"
    "v_add_f32 v46, 1.0, v46\n\t"
    "v_mfma_f32_16x16x32_bf16 v[68:71], v[196:199], v[96:99], v[68:71]\n\t"
    "ds_read_b128 v[124:127], %[da] offset:22528\n\t"
    "v_add_f32 v47, 1.0, v47\n\t"
    "v_rcp_f32 v40, v40\n\t"
    "v_rcp_f32 v41, v41\n\t"
    "v_mfma_f32_16x16x32_bf16 v[72:75], v[192:195], v[100:103], v[72:75]\n\t"
    "v_rcp_f32 v42, v42\n\t"
    "v_rcp_f32 v43, v43\n\t"
    "v_rcp_f32 v44, v44\n\t"
    "v_mfma_f32_16x16x32_bf16 v[76:79], v[196:199], v[100:103], v[76:79]\n\t"
    "v_rcp_f32 v45, v45\n\t"
    "v_rcp_f32 v46, v46\n\t"
    "v_rcp_f32 v47, v47\n\t"
    "s_waitcnt lgkmcnt(4)\n\t"
    "v_mfma_f32_16x16x32_bf16 v[64:67], v[200:203], v[104:107], v[64:67]\n\t"
    "ds_read_b128 v[96:99], %[ea] offset:24576\n\t"
    "v_mul_f32 v40, v48, v40\n\t"
    "v_mul_f32 v41, v49, v41\n\t"
    "v_mul_f32 v42, v50, v42\n\t"
    "v_mfma_f32_16x16x32_bf16 v[68:71], v[204:207], v[104:107], v[68:71]\n\t"
    "ds_read_b128 v[100:103], %[ea] offset:26624\n\t"
    "v_mul_f32 v43, v51, v43\n\t"
    "v_mul_f32 v44, v56, v44\n\t"
    "v_mul_f32 v45, v57, v45\n\t"
    "v_mfma_f32_16x16x32_bf16 v[72:75], v[200:203], v[108:111], v[72:75]\n\t"
    "v_mul_f32 v46, v58, v46\n\t"
    "v_mul_f32 v47, v59, v47\n\t"
    "v_mul_f32 v40, v40, v52\n\t"
    "v_mfma_f32_16x16x32_bf16 v[76:79], v[204:207], v[108:111], v[76:79]\n\t"
    "v_mul_f32 v41, v41, v53\n\t"
    "v_mul_f32 v42, v42, v54\n\t"
    "v_mul_f32 v43, v43, v55\n\t"
    "s_waitcnt lgkmcnt(4)\n\t"
    "v_mfma_f32_16x16x32_bf16 v[64:67], v[208:211], v[112:115], v[64:67]\n\t"
    "ds_read_b128 v[104:107], %[da] offset:24576\n\t"
    "v_mul_f32 v44, v44, v60\n\t"
    "v_mul_f32 v45, v45, v61\n\t"
    "v_mul_f32 v46, v46, v62\n\t"
    "v_mfma_f32_16x16x32_bf16 v[68:71], v[212:215], v[112:115], v[68:71]\n\t"
    "ds_read_b128 v[108:111], %[da] offset:26624\n\t"
    "v_mul_f32 v47, v47, v63\n\t"
    "v_cvt_pk_bf16_f32 v48, v40, v41\n\t"
    "v_cvt_pk_bf16_f32 v49, v42, v43\n\t"
    "v_mfma_f32_16x16x32_bf16 v[72:75], v[208:211], v[116:119], v[72:75]\n\t"
    "v_cvt_pk_bf16_f32 v50, v44, v45\n\t"
    "v_cvt_pk_bf16_f32 v51, v46, v47\n\t"
    "s_nop 1\n\t"
    "v_mfma_f32_16x16x32_bf16 v[76:79], v[212:215], v[116:119], v[76:79]\n\t"
    "v_permlane16_swap_b32 v48, v50\n\t"
    "v_permlane16_swap_b32 v49, v51\n\t"
    "global_store_dwordx4 %[co0], v[48:51], s[36:37] sc1\n\t"
    "s_waitcnt lgkmcnt(4)\n\t"
    "v_mfma_f32_16x16x32_bf16 v[64:67], v[216:219], v[120:123], v[64:67]\n\t"
    "ds_read_b128 v[112:115], %[ea] offset:28672\n\t"
    "v_mfma_f32_16x16x32_bf16 v[68:71], v[220:223], v[120:123], v[68:71]\n\t"
    "ds_read_b128 v[116:119], %[ea] offset:30720\n\t"
    "v_mfma_f32_16x16x32_bf16 v[72:75], v[216:219], v[124:127], v[72:75]\n\t"
    "v_xor_b32 %[ea], 0x10000, %[ea]\n\t"
    "v_mfma_f32_16x16x32_bf16 v[76:79], v[220:223], v[124:127], v[76:79]\n\t"
    "s_waitcnt vmcnt(1)\n\t"
    "s_barrier\n\t"
    "s_waitcnt lgkmcnt(4)\n\t"
    "v_mfma_f32_16x16x32_bf16 v[64:67], v[224:227], v[96:99], v[64:67]\n\t"
    "ds_read_b32 v39, %[sa]\n\t"
    "ds_read_b128 v[120:123], %[da] offset:28672\n\t"
    "v_mfma_f32_16x16x32_bf16 v[68:71], v[228:231], v[96:99], v[68:71]\n\t"
    "ds_read_b128 v[124:127], %[da] offset:30720\n\t"
    "v_mfma_f32_16x16x32_bf16 v[72:75], v[224:227], v[100:103], v[72:75]\n\t"
    "v_xor_b32 %[da], 0x10000, %[da]\n\t"
    "v_mfma_f32_16x16x32_bf16 v[76:79], v[228:231], v[100:103], v[76:79]\n\t"
    "s_waitcnt lgkmcnt(5)\n\t"
    "v_mfma_f32_16x16x32_bf16 v[64:67], v[232:235], v[104:107], v[64:67]\n\t"
    "ds_read_b128 v[96:99], %[ea] offset:0\n\t"
    "v_mfma_f32_16x16x32_bf16 v[68:71], v[236:239], v[104:107], v[68:71]\n\t"
    "ds_read_b128 v[100:103], %[ea] offset:2048\n\t"
    "v_mfma_f32_16x16x32_bf16 v[72:75], v[232:235], v[108:111], v[72:75]\n\t"
    "v_mfma_f32_16x16x32_bf16 v[76:79], v[236:239], v[108:111], v[76:79]\n\t"
    "s_waitcnt lgkmcnt(4)\n\t"
    "v_mfma_f32_16x16x32_bf16 v[64:67], v[240:243], v[112:115], v[64:67]\n\t"
    "ds_read_b128 v[104:107], %[da] offset:0\n\t"
    "v_mad_u32_u24 v39, v39, %[mu], %[ad]\n\t"
    "v_mfma_f32_16x16x32_bf16 v[68:71], v[244:247], v[112:115], v[68:71]\n\t"
    "ds_read_b128 v[108:111], %[da] offset:2048\n\t"
    "v_cmp_ge_i32 vcc, %[lf], v39\n\t"
    "s_cmp_eq_u64 vcc, 0\n\t"
    "s_addc_u32 %[lf], %[lf], 0\n\t"
    "v_mfma_f32_16x16x32_bf16 v[72:75], v[240:243], v[116:119], v[72:75]\n\t"
    "v_mfma_f32_16x16x32_bf16 v[76:79], v[244:247], v[116:119], v[76:79]\n\t"
    "v_cmp_ge_i32 vcc, %[lf], v39\n\t"
    "s_cmp_eq_u64 vcc, 0\n\t"
    "s_addc_u32 %[lf], %[lf], 0\n\t"
    "s_waitcnt lgkmcnt(4)\n\t"
    "v_mfma_f32_16x16x32_bf16 v[80:83], v[248:251], v[120:123], v[64:67]\n\t"
    "ds_read_b128 v[112:115], %[ea] offset:4096\n\t"
    "s_min_i32 %[lf], %[lf], %[cp]\n\t"
    "s_add_u32 %[b], %[b], 1\n\t"
    "v_mfma_f32_16x16x32_bf16 v[84:87], v[252:255], v[120:123], v[68:71]\n\t"
    "ds_read_b128 v[116:119], %[ea] offset:6144\n\t"
    "v_mfma_f32_16x16x32_bf16 v[88:91], v[248:251], v[124:127], v[72:75]\n\t"
    "v_mfma_f32_16x16x32_bf16 v[92:95], v[252:255], v[124:127], v[76:79]\n\t"
    "s_mov_b64 s[36:37], s[38:39]\n\t"
    "s_add_u32 s38, s38, %[ab]\n\t"
    "s_addc_u32 s39, s39, 0\n\t"
    "s_cmp_lt_i32 %[b], %[lf]\n\t"
    "s_cbranch_scc1 1b\n\t"
    "s_waitcnt lgkmcnt(0)\n\t"
    "s_nop 4\n\t"
    "s_branch 9f\n\t"
    "5:\n\t"
    "s_add_u32 %[st], %[b], %[po]\n\t"
    "s_max_i32 %[st], %[st], %[p0]\n\t"
    "v_mov_b32 v39, %[st]\n\t"
    "v_mov_b32 v38, 0\n\t"
    "s_mov_b64 exec, 1\n\t"
    "global_store_dword v38, v39, %[pg] sc1\n\t"
    "s_mov_b64 exec, -1\n\t"
    "s_mov_b32 %[p0], %[p1]\n\t"
    "s_cmp_ge_i32 %[lf], %[cp]\n\t"
    "s_cbranch_scc1 6b\n\t"
    "s_mov_b32 m0, %[sl]\n\t"
    "s_nop 0\n\t"
    "global_load_lds_dword %[dv], %[db] sc1\n\t"
    "s_branch 6b\n\t"
    "9:"
    : "=&{v[80:95]}"(pd), [ea] "+&v"(ea), [da] "+&v"(da), [b] "+&s"(b), [lf] "+&s"(lf), "+{s[36:37]}"(p1), "+{s[38:39]}"(p0), [sa] "+&v"(sa), [st] "=&s"(st), [sl] "+&s"(sl), [p0] "+&s"(pf)
    : [rw] "v"(rw), [ab] "s"(ab), [cp] "s"(cp), [mu] "s"(mu), [ad] "v"(ad), "{v[128:159]}"(W[0]), "{v[160:191]}"(W[1]), "{v[192:223]}"(W[2]), "{v[224:255]}"(W[3]), [co0] "v"(co0), [w0] "s"(w0), [dv] "v"(dv), [db] "s"(db), [pg] "s"(pg), [po] "s"(po - 1), [p1] "s"(b + po), [nl] "s"(-LOG2E)
    : "v38", "v39", "v40", "v41", "v42", "v43", "v44", "v45", "v46", "v47", "v48", "v49", "v50", "v51", "v52", "v53", "v54", "v55", "v56", "v57", "v58", "v59", "v60", "v61", "v62", "v63", "v64", "v65", "v66", "v67", "v68", "v69", "v70", "v71", "v72", "v73", "v74", "v75", "v76", "v77", "v78", "v79", "v96", "v97", "v98", "v99", "v100", "v101", "v102", "v103", "v104", "v105", "v106", "v107", "v108", "v109", "v110", "v111", "v112", "v113", "v114", "v115", "v116", "v117", "v118", "v119", "v120", "v121", "v122", "v123", "v124", "v125", "v126", "v127", "vcc", "scc", "m0", "memory");
}
__device__ __forceinline__ void near_st2(const u32x32 (&W)[4], u32x16& pd, unsigned ea, unsigned da, int& b, int& lf, int cp, unsigned sa, unsigned mu, unsigned ad,
    unsigned rw, unsigned ab, const char* p1, const char* p0, unsigned co0, unsigned co1, int po, int pf, unsigned w0, unsigned sl, unsigned dv, const char* db, const char* pg) {
  unsigned st;
  asm volatile("s_barrier\n\t"
    "ds_read_b128 v[96:99], %[ea] offset:0\n\t"
    "ds_read_b128 v[100:103], %[ea] offset:2048\n\t"
    "ds_read_b128 v[104:107], %[da] offset:0\n\t"
    "ds_read_b128 v[108:111], %[da] offset:2048\n\t"
    "ds_read_b128 v[112:115], %[ea] offset:4096\n\t"
    "ds_read_b128 v[116:119], %[ea] offset:6144\n\t"
    "s_branch 2f\n\t"
    "1:\n\t"
    "s_barrier\n\t"
    "2:\n\t"
    "s_waitcnt lgkmcnt(4)\n\t"
    "v_mfma_f32_16x16x32_bf16 v[64:67], v[128:131], v[96:99], 0\n\t"
    "ds_read_b128 v[120:123], %[da] offset:4096\n\t"
    "v_mfma_f32_16x16x32_bf16 v[68:71], v[132:135], v[96:99], 0\n\t"
    "ds_read_b128 v[124:127], %[da] offset:6144\n\t"
    "v_mfma_f32_16x16x32_bf16 v[72:75], v[128:131], v[100:103], 0\n\t"
    "v_mfma_f32_16x16x32_bf16 v[76:79], v[132:135], v[100:103], 0\n\t"
    "s_waitcnt lgkmcnt(4)\n\t"
    "v_mfma_f32_16x16x32_bf16 v[64:67], v[136:139], v[104:107], v[64:67]\n\t"
    "ds_read_b128 v[96:99], %[ea] offset:8192\n\t"
    "v_mfma_f32_16x16x32_bf16 v[68:71], v[140:143], v[104:107], v[68:71]\n\t"
    "ds_read_b128 v[100:103], %[ea] offset:10240\n\t"
    "v_mfma_f32_16x16x32_bf16 v[72:75], v[136:139], v[108:111], v[72:75]\n\t"
    "v_mfma_f32_16x16x32_bf16 v[76:79], v[140:143], v[108:111], v[76:79]\n\t"
    "s_waitcnt lgkmcnt(4)\n\t"
    "v_mfma_f32_16x16x32_bf16 v[64:67], v[144:147], v[112:115], v[64:67]\n\t"
    "ds_read_b128 v[104:107], %[da] offset:8192\n\t"
    "v_mfma_f32_16x16x32_bf16 v[68:71], v[148:151], v[112:115], v[68:71]\n\t"
    "ds_read_b128 v[108:111], %[da] offset:10240\n\t"
    "v_mfma_f32_16x16x32_bf16 v[72:75], v[144:147], v[116:119], v[72:75]\n\t"
    "v_mfma_f32_16x16x32_bf16 v[76:79], v[148:151], v[116:119], v[76:79]\n\t"
    "s_waitcnt vmcnt(0)\n\t"
    "s_barrier\n\t"
    "s_waitcnt lgkmcnt(4)\n\t"
    "v_mfma_f32_16x16x32_bf16 v[64:67], v[152:155], v[120:123], v[64:67]\n\t"
    "ds_read_b128 v[112:115], %[ea] offset:12288\n\t"
    "v_mfma_f32_16x16x32_bf16 v[68:71], v[156:159], v[120:123], v[68:71]\n\t"
    "ds_read_b128 v[116:119], %[ea] offset:14336\n\t"
    "v_mfma_f32_16x16x32_bf16 v[72:75], v[152:155], v[124:127], v[72:75]\n\t"
    "ds_read_b128 v[48:51], %[rw] offset:0\n\t"
    "v_mfma_f32_16x16x32_bf16 v[76:79], v[156:159], v[124:127], v[76:79]\n\t"
    "ds_read_b128 v[52:55], %[rw] offset:1024\n\t"
    "s_cmp_eq_u32 %[w0], 0\n\t"
    "s_cbranch_scc1 5f\n\t"
    "6:\n\t"
    "s_waitcnt lgkmcnt(6)\n\t"
    "v_mfma_f32_16x16x32_bf16 v[64:67], v[160:163], v[96:99], v[64:67]\n\t"
    "ds_read_b128 v[120:123], %[da] offset:12288\n\t"
    "v_mfma_f32_16x16x32_bf16 v[68:71], v[164:167], v[96:99], v[68:71]\n\t"
    "ds_read_b128 v[124:127], %[da] offset:14336\n\t"
    "v_mfma_f32_16x16x32_bf16 v[72:75], v[160:163], v[100:103], v[72:75]\n\t"
    "ds_read_b128 v[56:59], %[rw] offset:2048\n\t"
    "v_mfma_f32_16x16x32_bf16 v[76:79], v[164:167], v[100:103], v[76:79]\n\t"
    "ds_read_b128 v[60:63], %[rw] offset:3072\n\t"
    "s_waitcnt lgkmcnt(0)\n\t"
    "v_mfma_f32_16x16x32_bf16 v[64:67], v[168:171], v[104:107], v[64:67]\n\t"
    "ds_read_b128 v[96:99], %[ea] offset:16384\n\t"
    "v_cvt_pk_bf16_f32 v40, v80, v81\n\t"
    "v_mfma_f32_16x16x32_bf16 v[68:71], v[172:175], v[104:107], v[68:71]\n\t"
    "ds_read_b128 v[100:103], %[ea] offset:18432\n\t"
    "v_cvt_pk_bf16_f32 v41, v82, v83\n\t"
    "v_mfma_f32_16x16x32_bf16 v[72:75], v[168:171], v[108:111], v[72:75]\n\t"
    "v_cvt_pk_bf16_f32 v42, v84, v85\n\t"
    "v_mfma_f32_16x16x32_bf16 v[76:79], v[172:175], v[108:111], v[76:79]\n\t"
    "v_cvt_pk_bf16_f32 v43, v86, v87\n\t"
    "v_mfma_f32_16x16x32_bf16 v[64:67], v[176:179], v[112:115], v[64:67]\n\t"
    "ds_read_b128 v[104:107], %[da] offset:16384\n\t"
    "global_store_dwordx4 %[co0], v[40:43], s[36:37] sc1\n\t"
    "v_mfma_f32_16x16x32_bf16 v[68:71], v[180:183], v[112:115], v[68:71]\n\t"
    "ds_read_b128 v[108:111], %[da] offset:18432\n\t"
    "v_cvt_pk_bf16_f32 v44, v88, v89\n\t"
    "v_mfma_f32_16x16x32_bf16 v[72:75], v[176:179], v[116:119], v[72:75]\n\t"
    "v_cvt_pk_bf16_f32 v45, v90, v91\n\t"
    "v_mfma_f32_16x16x32_bf16 v[76:79], v[180:183], v[116:119], v[76:79]\n\t"
    "v_cvt_pk_bf16_f32 v46, v92, v93\n\t"
    "v_mfma_f32_16x16x32_bf16 v[64:67], v[184:187], v[120:123], v[64:67]\n\t"
    "ds_read_b128 v[112:115], %[ea] offset:20480\n\t"
    "v_cvt_pk_bf16_f32 v47, v94, v95\n\t"
    "v_mfma_f32_16x16x32_bf16 v[68:71], v[188:191], v[120:123], v[68:71]\n\t"
    "ds_read_b128 v[116:119], %[ea] offset:22528\n\t"
    "global_store_dwordx4 %[co1], v[44:47], s[36:37] sc1\n\t"
    "v_mfma_f32_16x16x32_bf16 v[72:75], v[184:187], v[124:127], v[72:75]\n\t"
    "v_cvt_pk_bf16_f32 v48, v48, v49\n\t"
    "v_mfma_f32_16x16x32_bf16 v[76:79], v[188:191], v[124:127], v[76:79]\n\t"
    "v_cvt_pk_bf16_f32 v49, v50, v51\n\t"
    "s_waitcnt lgkmcnt(4)\n\t"
    "v_mfma_f32_16x16x32_bf16 v[64:67], v[192:195], v[96:99], v[64:67]\n\t"
    "ds_read_b128 v[120:123], %[da] offset:20480\n\t"
    "v_cvt_pk_bf16_f32 v50, v52, v53\n\t"
    "v_mfma_f32_16x16x32_bf16 v[68:71], v[196:199], v[96:99], v[68:71]\n\t"
    "ds_read_b128 v[124:127], %[da] offset:22528\n\t"
    "v_cvt_pk_bf16_f32 v51, v54, v55\n\t"
    "v_mfma_f32_16x16x32_bf16 v[72:75], v[192:195], v[100:103], v[72:75]\n\t"
    "global_store_dwordx4 %[co0], v[48:51], s[36:37] offset:2048 sc1\n\t"
    "v_mfma_f32_16x16x32_bf16 v[76:79], v[196:199], v[100:103], v[76:79]\n\t"
    "v_cvt_pk_bf16_f32 v56, v56, v57\n\t"
    "s_waitcnt lgkmcnt(4)\n\t"
    "v_mfma_f32_16x16x32_bf16 v[64:67], v[200:203], v[104:107], v[64:67]\n\t"
    "ds_read_b128 v[96:99], %[ea] offset:24576\n\t"
    "v_cvt_pk_bf16_f32 v57, v58, v59\n\t"
    "v_mfma_f32_16x16x32_bf16 v[68:71], v[204:207], v[104:107], v[68:71]\n\t"
    "ds_read_b128 v[100:103], %[ea] offset:26624\n\t"
    "v_cvt_pk_bf16_f32 v58, v60, v61\n\t"
    "v_mfma_f32_16x16x32_bf16 v[72:75], v[200:203], v[108:111], v[72:75]\n\t"
    "v_cvt_pk_bf16_f32 v59, v62, v63\n\t"
    "v_mfma_f32_16x16x32_bf16 v[76:79], v[204:207], v[108:111], v[76:79]\n\t"
    "global_store_dwordx4 %[co1], v[56:59], s[36:37] offset:2048 sc1\n\t"
    "s_waitcnt lgkmcnt(4)\n\t"
    "v_mfma_f32_16x16x32_bf16 v[64:67], v[208:211], v[112:115], v[64:67]\n\t"
    "ds_read_b128 v[104:107], %[da] offset:24576\n\t"
    "v_mfma_f32_16x16x32_bf16 v[68:71], v[212:215], v[112:115], v[68:71]\n\t"
    "ds_read_b128 v[108:111], %[da] offset:26624\n\t"
    "v_mfma_f32_16x16x32_bf16 v[72:75], v[208:211], v[116:119], v[72:75]\n\t"
    "v_mfma_f32_16x16x32_bf16 v[76:79], v[212:215], v[116:119], v[76:79]\n\t"
    "s_waitcnt lgkmcnt(4)\n\t"
    "v_mfma_f32_16x16x32_bf16 v[64:67], v[216:219], v[120:123], v[64:67]\n\t"
    "ds_read_b128 v[112:115], %[ea] offset:28672\n\t"
    "v_mfma_f32_16x16x32_bf16 v[68:71], v[220:223], v[120:123], v[68:71]\n\t"
    "ds_read_b128 v[116:119], %[ea] offset:30720\n\t"
    "v_mfma_f32_16x16x32_bf16 v[72:75], v[216:219], v[124:127], v[72:75]\n\t"
    "v_xor_b32 %[ea], 0x10000, %[ea]\n\t"
    "v_mfma_f32_16x16x32_bf16 v[76:79], v[220:223], v[124:127], v[76:79]\n\t"
    "s_waitcnt vmcnt(4)\n\t"
    "s_barrier\n\t"
    "s_waitcnt lgkmcnt(4)\n\t"
    "v_mfma_f32_16x16x32_bf16 v[64:67], v[224:227], v[96:99], v[64:67]\n\t"
    "ds_read_b32 v39, %[sa]\n\t"
    "ds_read_b128 v[120:123], %[da] offset:28672\n\t"
    "v_mfma_f32_16x16x32_bf16 v[68:71], v[228:231], v[96:99], v[68:71]\n\t"
    "ds_read_b128 v[124:127], %[da] offset:30720\n\t"
    "v_mfma_f32_16x16x32_bf16 v[72:75], v[224:227], v[100:103], v[72:75]\n\t"
    "v_xor_b32 %[da], 0x10000, %[da]\n\t"
    "v_mfma_f32_16x16x32_bf16 v[76:79], v[228:231], v[100:103], v[76:79]\n\t"
    "s_waitcnt lgkmcnt(5)\n\t"
    "v_mfma_f32_16x16x32_bf16 v[64:67], v[232:235], v[104:107], v[64:67]\n\t"
    "ds_read_b128 v[96:99], %[ea] offset:0\n\t"
    "v_mfma_f32_16x16x32_bf16 v[68:71], v[236:239], v[104:107], v[68:71]\n\t"
    "ds_read_b128 v[100:103], %[ea] offset:2048\n\t"
    "v_mfma_f32_16x16x32_bf16 v[72:75], v[232:235], v[108:111], v[72:75]\n\t"
    "v_mfma_f32_16x16x32_bf16 v[76:79], v[236:239], v[108:111], v[76:79]\n\t"
    "s_waitcnt lgkmcnt(4)\n\t"
    "v_mfma_f32_16x16x32_bf16 v[64:67], v[240:243], v[112:115], v[64:67]\n\t"
    "ds_read_b128 v[104:107], %[da] offset:0\n\t"
    "v_mad_u32_u24 v39, v39, %[mu], %[ad]\n\t"
    "v_mfma_f32_16x16x32_bf16 v[68:71], v[244:247], v[112:115], v[68:71]\n\t"
    "ds_read_b128 v[108:111], %[da] offset:2048\n\t"
    "v_cmp_ge_i32 vcc, %[lf], v39\n\t"
    "s_cmp_eq_u64 vcc, 0\n\t"
    "s_addc_u32 %[lf], %[lf], 0\n\t"
    "v_mfma_f32_16x16x32_bf16 v[72:75], v[240:243], v[116:119], v[72:75]\n\t"
    "v_mfma_f32_16x16x32_bf16 v[76:79], v[244:247], v[116:119], v[76:79]\n\t"
    "v_cmp_ge_i32 vcc, %[lf], v39\n\t"
    "s_cmp_eq_u64 vcc, 0\n\t"
    "s_addc_u32 %[lf], %[lf], 0\n\t"
    "s_waitcnt lgkmcnt(4)\n\t"
    "v_mfma_f32_16x16x32_bf16 v[80:83], v[248:251], v[120:123], v[64:67]\n\t"
    "ds_read_b128 v[112:115], %[ea] offset:4096\n\t"
    "s_min_i32 %[lf], %[lf], %[cp]\n\t"
    "s_add_u32 %[b], %[b], 1\n\t"
    "v_mfma_f32_16x16x32_bf16 v[84:87], v[252:255], v[120:123], v[68:71]\n\t"
    "ds_read_b128 v[116:119], %[ea] offset:6144\n\t"
    "v_mfma_f32_16x16x32_bf16 v[88:91], v[248:251], v[124:127], v[72:75]\n\t"
    "v_mfma_f32_16x16x32_bf16 v[92:95], v[252:255], v[124:127], v[76:79]\n\t"
    "s_mov_b64 s[36:37], s[38:39]\n\t"
    "s_add_u32 s38, s38, %[ab]\n\t"
    "s_addc_u32 s39, s39, 0\n\t"
    "s_cmp_lt_i32 %[b], %[lf]\n\t"
    "s_cbranch_scc1 1b\n\t"
    "s_waitcnt lgkmcnt(0)\n\t"
    "s_nop 4\n\t"
    "s_branch 9f\n\t"
    "5:\n\t"
    "s_add_u32 %[st], %[b], %[po]\n\t"
    "s_max_i32 %[st], %[st], %[p0]\n\t"
    "v_mov_b32 v39, %[st]\n\t"
    "v_mov_b32 v38, 0\n\t"
    "s_mov_b64 exec, 1\n\t"
    "global_store_dword v38, v39, %[pg] sc1\n\t"
    "s_mov_b64 exec, -1\n\t"
    "s_mov_b32 %[p0], %[p1]\n\t"
    "s_cmp_ge_i32 %[lf], %[cp]\n\t"
    "s_cbranch_scc1 6b\n\t"
    "s_mov_b32 m0, %[sl]\n\t"
    "s_nop 0\n\t"
    "global_load_lds_dword %[dv], %[db] sc1\n\t"
    "s_branch 6b\n\t"
    "9:"
    : "=&{v[80:95]}"(pd), [ea] "+&v"(ea), [da] "+&v"(da), [b] "+&s"(b), [lf] "+&s"(lf), "+{s[36:37]}"(p1), "+{s[38:39]}"(p0), [sa] "+&v"(sa), [st] "=&s"(st), [sl] "+&s"(sl), [p0] "+&s"(pf)
    : [rw] "v"(rw), [ab] "s"(ab), [cp] "s"(cp), [mu] "s"(mu), [ad] "v"(ad), "{v[128:159]}"(W[0]), "{v[160:191]}"(W[1]), "{v[192:223]}"(W[2]), "{v[224:255]}"(W[3]), [co0] "v"(co0), [w0] "s"(w0), [dv] "v"(dv), [db] "s"(db), [pg] "s"(pg), [po] "s"(po - 1), [p1] "s"(b + po), [co1] "v"(co1)
    : "v38", "v39", "v40", "v41", "v42", "v43", "v44", "v45", "v46", "v47", "v48", "v49", "v50", "v51", "v52", "v53", "v54", "v55", "v56", "v57", "v58", "v59", "v60", "v61", "v62", "v63", "v64", "v65", "v66", "v67", "v68", "v69", "v70", "v71", "v72", "v73", "v74", "v75", "v76", "v77", "v78", "v79", "v96", "v97", "v98", "v99", "v100", "v101", "v102", "v103", "v104", "v105", "v106", "v107", "v108", "v109", "v110", "v111", "v112", "v113", "v114", "v115", "v116", "v117", "v118", "v119", "v120", "v121", "v122", "v123", "v124", "v125", "v126", "v127", "vcc", "scc", "m0", "memory");
}
__device__ __forceinline__ void lean_ld0(const u32x32 (&W)[4], u32x16& pd, unsigned ea, unsigned da, unsigned x, unsigned lb, const char* s0, const char* s1, const unsigned (&av)[8], unsigned sa, unsigned mu, unsigned ad, int th, unsigned s2, unsigned dv, const char* db) {
  asm volatile("s_setprio 2\n\t"
    "ds_read_b128 v[96:99], %[ea] offset:0\n\t"
    "ds_read_b128 v[100:103], %[ea] offset:2048\n\t"
    "ds_read_b128 v[104:107], %[da] offset:0\n\t"
    "ds_read_b128 v[108:111], %[da] offset:2048\n\t"
    "ds_read_b128 v[112:115], %[ea] offset:4096\n\t"
    "ds_read_b128 v[116:119], %[ea] offset:6144\n\t"
    "s_waitcnt lgkmcnt(4)\n\t"
    "v_mfma_f32_16x16x32_bf16 v[80:83], v[128:131], v[96:99], 0\n\t"
    "ds_read_b128 v[120:123], %[da] offset:4096\n\t"
    "v_mfma_f32_16x16x32_bf16 v[84:87], v[132:135], v[96:99], 0\n\t"
    "ds_read_b128 v[124:127], %[da] offset:6144\n\t"
    "v_mfma_f32_16x16x32_bf16 v[88:91], v[128:131], v[100:103], 0\n\t"
    "v_mfma_f32_16x16x32_bf16 v[92:95], v[132:135], v[100:103], 0\n\t"
    "s_waitcnt vmcnt(24)\n\t"
    "s_cmp_lt_u32 %[x], 2\n\t"
    "s_cbranch_scc0 41f\n\t"
    "s_waitcnt vmcnt(8)\n\t"
    "41:\n\t"
    "s_barrier\n\t"
    "s_waitcnt lgkmcnt(4)\n\t"
    "v_mfma_f32_16x16x32_bf16 v[80:83], v[136:139], v[104:107], v[80:83]\n\t"
    "ds_read_b128 v[96:99], %[ea] offset:8192\n\t"
    "v_mfma_f32_16x16x32_bf16 v[84:87], v[140:143], v[104:107], v[84:87]\n\t"
    "ds_read_b128 v[100:103], %[ea] offset:10240\n\t"
    "v_mfma_f32_16x16x32_bf16 v[88:91], v[136:139], v[108:111], v[88:91]\n\t"
    "v_mfma_f32_16x16x32_bf16 v[92:95], v[140:143], v[108:111], v[92:95]\n\t"
    "s_waitcnt lgkmcnt(4)\n\t"
    "v_mfma_f32_16x16x32_bf16 v[80:83], v[144:147], v[112:115], v[80:83]\n\t"
    "ds_read_b128 v[104:107], %[da] offset:8192\n\t"
    "v_mfma_f32_16x16x32_bf16 v[84:87], v[148:151], v[112:115], v[84:87]\n\t"
    "ds_read_b128 v[108:111], %[da] offset:10240\n\t"
    "v_mfma_f32_16x16x32_bf16 v[88:91], v[144:147], v[116:119], v[88:91]\n\t"
    "v_mfma_f32_16x16x32_bf16 v[92:95], v[148:151], v[116:119], v[92:95]\n\t"
    "s_waitcnt lgkmcnt(4)\n\t"
    "v_mfma_f32_16x16x32_bf16 v[80:83], v[152:155], v[120:123], v[80:83]\n\t"
    "ds_read_b128 v[112:115], %[ea] offset:12288\n\t"
    "v_mfma_f32_16x16x32_bf16 v[84:87], v[156:159], v[120:123], v[84:87]\n\t"
    "ds_read_b128 v[116:119], %[ea] offset:14336\n\t"
    "v_mfma_f32_16x16x32_bf16 v[88:91], v[152:155], v[124:127], v[88:91]\n\t"
    "v_mfma_f32_16x16x32_bf16 v[92:95], v[156:159], v[124:127], v[92:95]\n\t"
    "s_waitcnt lgkmcnt(4)\n\t"
    "v_mfma_f32_16x16x32_bf16 v[80:83], v[160:163], v[96:99], v[80:83]\n\t"
    "ds_read_b128 v[120:123], %[da] offset:12288\n\t"
    "v_mfma_f32_16x16x32_bf16 v[84:87], v[164:167], v[96:99], v[84:87]\n\t"
    "ds_read_b128 v[124:127], %[da] offset:14336\n\t"
    "v_mfma_f32_16x16x32_bf16 v[88:91], v[160:163], v[100:103], v[88:91]\n\t"
    "v_mfma_f32_16x16x32_bf16 v[92:95], v[164:167], v[100:103], v[92:95]\n\t"
    "s_waitcnt vmcnt(20)\n\t"
    "s_cmp_lt_u32 %[x], 2\n\t"
    "s_cbranch_scc0 45f\n\t"
    "s_waitcnt vmcnt(4)\n\t"
    "45:\n\t"
    "s_barrier\n\t"
    "s_waitcnt lgkmcnt(4)\n\t"
    "v_mfma_f32_16x16x32_bf16 v[80:83], v[168:171], v[104:107], v[80:83]\n\t"
    "ds_read_b128 v[96:99], %[ea] offset:16384\n\t"
    "v_mfma_f32_16x16x32_bf16 v[84:87], v[172:175], v[104:107], v[84:87]\n\t"
    "ds_read_b128 v[100:103], %[ea] offset:18432\n\t"
    "v_mfma_f32_16x16x32_bf16 v[88:91], v[168:171], v[108:111], v[88:91]\n\t"
    "v_mfma_f32_16x16x32_bf16 v[92:95], v[172:175], v[108:111], v[92:95]\n\t"
    "s_waitcnt lgkmcnt(4)\n\t"
    "v_mfma_f32_16x16x32_bf16 v[80:83], v[176:179], v[112:115], v[80:83]\n\t"
    "ds_read_b128 v[104:107], %[da] offset:16384\n\t"
    "v_mfma_f32_16x16x32_bf16 v[84:87], v[180:183], v[112:115], v[84:87]\n\t"
    "ds_read_b128 v[108:111], %[da] offset:18432\n\t"
    "v_mfma_f32_16x16x32_bf16 v[88:91], v[176:179], v[116:119], v[88:91]\n\t"
    "v_mfma_f32_16x16x32_bf16 v[92:95], v[180:183], v[116:119], v[92:95]\n\t"
    "s_waitcnt lgkmcnt(4)\n\t"
    "v_mfma_f32_16x16x32_bf16 v[80:83], v[184:187], v[120:123], v[80:83]\n\t"
    "ds_read_b128 v[112:115], %[ea] offset:20480\n\t"
    "v_mfma_f32_16x16x32_bf16 v[84:87], v[188:191], v[120:123], v[84:87]\n\t"
    "ds_read_b128 v[116:119], %[ea] offset:22528\n\t"
    "v_mfma_f32_16x16x32_bf16 v[88:91], v[184:187], v[124:127], v[88:91]\n\t"
    "v_mfma_f32_16x16x32_bf16 v[92:95], v[188:191], v[124:127], v[92:95]\n\t"
    "s_waitcnt lgkmcnt(4)\n\t"
    "v_mfma_f32_16x16x32_bf16 v[80:83], v[192:195], v[96:99], v[80:83]\n\t"
    "ds_read_b128 v[120:123], %[da] offset:20480\n\t"
    "v_mfma_f32_16x16x32_bf16 v[84:87], v[196:199], v[96:99], v[84:87]\n\t"
    "ds_read_b128 v[124:127], %[da] offset:22528\n\t"
    "v_mfma_f32_16x16x32_bf16 v[88:91], v[192:195], v[100:103], v[88:91]\n\t"
    "v_mfma_f32_16x16x32_bf16 v[92:95], v[196:199], v[100:103], v[92:95]\n\t"
    "s_waitcnt vmcnt(16)\n\t"
    "s_cmp_lt_u32 %[x], 2\n\t"
    "s_cbranch_scc0 49f\n\t"
    "s_waitcnt vmcnt(0)\n\t"
    "49:\n\t"
    "s_barrier\n\t"
    "ds_read_b32 v79, %[sa]\n\t"
    "s_waitcnt lgkmcnt(5)\n\t"
    "v_mfma_f32_16x16x32_bf16 v[80:83], v[200:203], v[104:107], v[80:83]\n\t"
    "ds_read_b128 v[96:99], %[ea] offset:24576\n\t"
    "v_mfma_f32_16x16x32_bf16 v[84:87], v[204:207], v[104:107], v[84:87]\n\t"
    "ds_read_b128 v[100:103], %[ea] offset:26624\n\t"
    "v_mfma_f32_16x16x32_bf16 v[88:91], v[200:203], v[108:111], v[88:91]\n\t"
    "v_mfma_f32_16x16x32_bf16 v[92:95], v[204:207], v[108:111], v[92:95]\n\t"
    "s_waitcnt lgkmcnt(5)\n\t"
    "v_mfma_f32_16x16x32_bf16 v[80:83], v[208:211], v[112:115], v[80:83]\n\t"
    "ds_read_b128 v[104:107], %[da] offset:24576\n\t"
    "v_mfma_f32_16x16x32_bf16 v[84:87], v[212:215], v[112:115], v[84:87]\n\t"
    "ds_read_b128 v[108:111], %[da] offset:26624\n\t"
    "v_mfma_f32_16x16x32_bf16 v[88:91], v[208:211], v[116:119], v[88:91]\n\t"
    "v_mfma_f32_16x16x32_bf16 v[92:95], v[212:215], v[116:119], v[92:95]\n\t"
    "s_cmp_eq_u32 %[s2], 0\n\t"
    "s_cbranch_scc1 7f\n\t"
    "s_mov_b32 m0, %[s2]\n\t"
    "s_nop 0\n\t"
    "global_load_lds_dword %[dv], %[db] sc1\n\t"
    "7:\n\t"
    "s_waitcnt lgkmcnt(4)\n\t"
    "v_mfma_f32_16x16x32_bf16 v[80:83], v[216:219], v[120:123], v[80:83]\n\t"
    "ds_read_b128 v[112:115], %[ea] offset:28672\n\t"
    "v_mfma_f32_16x16x32_bf16 v[84:87], v[220:223], v[120:123], v[84:87]\n\t"
    "ds_read_b128 v[116:119], %[ea] offset:30720\n\t"
    "v_mfma_f32_16x16x32_bf16 v[88:91], v[216:219], v[124:127], v[88:91]\n\t"
    "v_mad_u32_u24 v79, v79, %[mu], %[ad]\n\t"
    "v_cmp_ge_i32 vcc, %[th], v79\n\t"
    "v_mfma_f32_16x16x32_bf16 v[92:95], v[220:223], v[124:127], v[92:95]\n\t"
    "s_cbranch_vccz 5f\n\t"
    "s_waitcnt lgkmcnt(4)\n\t"
    "v_mfma_f32_16x16x32_bf16 v[80:83], v[224:227], v[96:99], v[80:83]\n\t"
    "ds_read_b128 v[120:123], %[da] offset:28672\n\t"
    "v_mfma_f32_16x16x32_bf16 v[84:87], v[228:231], v[96:99], v[84:87]\n\t"
    "ds_read_b128 v[124:127], %[da] offset:30720\n\t"
    "v_mfma_f32_16x16x32_bf16 v[88:91], v[224:227], v[100:103], v[88:91]\n\t"
    "v_mfma_f32_16x16x32_bf16 v[92:95], v[228:231], v[100:103], v[92:95]\n\t"
    "s_waitcnt lgkmcnt(4)\n\t"
    "v_mfma_f32_16x16x32_bf16 v[80:83], v[232:235], v[104:107], v[80:83]\n\t"
    "v_mfma_f32_16x16x32_bf16 v[84:87], v[236:239], v[104:107], v[84:87]\n\t"
    "v_mfma_f32_16x16x32_bf16 v[88:91], v[232:235], v[108:111], v[88:91]\n\t"
    "v_mfma_f32_16x16x32_bf16 v[92:95], v[236:239], v[108:111], v[92:95]\n\t"
    "s_waitcnt lgkmcnt(2)\n\t"
    "v_mfma_f32_16x16x32_bf16 v[80:83], v[240:243], v[112:115], v[80:83]\n\t"
    "v_mfma_f32_16x16x32_bf16 v[84:87], v[244:247], v[112:115], v[84:87]\n\t"
    "v_mfma_f32_16x16x32_bf16 v[88:91], v[240:243], v[116:119], v[88:91]\n\t"
    "v_mfma_f32_16x16x32_bf16 v[92:95], v[244:247], v[116:119], v[92:95]\n\t"
    "s_waitcnt lgkmcnt(0)\n\t"
    "v_mfma_f32_16x16x32_bf16 v[80:83], v[248:251], v[120:123], v[80:83]\n\t"
    "v_mfma_f32_16x16x32_bf16 v[84:87], v[252:255], v[120:123], v[84:87]\n\t"
    "v_mfma_f32_16x16x32_bf16 v[88:91], v[248:251], v[124:127], v[88:91]\n\t"
    "v_mfma_f32_16x16x32_bf16 v[92:95], v[252:255], v[124:127], v[92:95]\n\t"
    "s_branch 6f\n\t"
    "5:\n\t"
    "s_waitcnt lgkmcnt(4)\n\t"
    "v_mfma_f32_16x16x32_bf16 v[80:83], v[224:227], v[96:99], v[80:83]\n\t"
    "ds_read_b128 v[120:123], %[da] offset:28672\n\t"
    "s_add_u32 m0, %[lb], 0\n\t"
    "s_nop 0\n\t"
    "global_load_lds_dwordx4 %[av0], %[s0] sc1\n\t"
    "v_mfma_f32_16x16x32_bf16 v[84:87], v[228:231], v[96:99], v[84:87]\n\t"
    "ds_read_b128 v[124:127], %[da] offset:30720\n\t"
    "s_add_u32 m0, %[lb], 32768\n\t"
    "s_nop 0\n\t"
    "global_load_lds_dwordx4 %[av1], %[s0] sc1\n\t"
    "v_mfma_f32_16x16x32_bf16 v[88:91], v[224:227], v[100:103], v[88:91]\n\t"
    "s_add_u32 m0, %[lb], 4096\n\t"
    "s_nop 0\n\t"
    "global_load_lds_dwordx4 %[av2], %[s0] sc1\n\t"
    "v_mfma_f32_16x16x32_bf16 v[92:95], v[228:231], v[100:103], v[92:95]\n\t"
    "s_add_u32 m0, %[lb], 36864\n\t"
    "s_nop 0\n\t"
    "global_load_lds_dwordx4 %[av3], %[s0] sc1\n\t"
    "s_waitcnt lgkmcnt(4)\n\t"
    "v_mfma_f32_16x16x32_bf16 v[80:83], v[232:235], v[104:107], v[80:83]\n\t"
    "s_add_u32 m0, %[lb], 8192\n\t"
    "s_nop 0\n\t"
    "global_load_lds_dwordx4 %[av4], %[s0] sc1\n\t"
    "v_mfma_f32_16x16x32_bf16 v[84:87], v[236:239], v[104:107], v[84:87]\n\t"
    "s_add_u32 m0, %[lb], 40960\n\t"
    "s_nop 0\n\t"
    "global_load_lds_dwordx4 %[av5], %[s0] sc1\n\t"
    "v_mfma_f32_16x16x32_bf16 v[88:91], v[232:235], v[108:111], v[88:91]\n\t"
    "s_add_u32 m0, %[lb], 12288\n\t"
    "s_nop 0\n\t"
    "global_load_lds_dwordx4 %[av6], %[s0] sc1\n\t"
    "v_mfma_f32_16x16x32_bf16 v[92:95], v[236:239], v[108:111], v[92:95]\n\t"
    "s_add_u32 m0, %[lb], 45056\n\t"
    "s_nop 0\n\t"
    "global_load_lds_dwordx4 %[av7], %[s0] sc1\n\t"
    "s_waitcnt lgkmcnt(2)\n\t"
    "v_mfma_f32_16x16x32_bf16 v[80:83], v[240:243], v[112:115], v[80:83]\n\t"
    "s_add_u32 m0, %[lb], 16384\n\t"
    "s_nop 0\n\t"
    "global_load_lds_dwordx4 %[av0], %[s1] sc1\n\t"
    "v_mfma_f32_16x16x32_bf16 v[84:87], v[244:247], v[112:115], v[84:87]\n\t"
    "s_add_u32 m0, %[lb], 49152\n\t"
    "s_nop 0\n\t"
    "global_load_lds_dwordx4 %[av1], %[s1] sc1\n\t"
    "v_mfma_f32_16x16x32_bf16 v[88:91], v[240:243], v[116:119], v[88:91]\n\t"
    "s_add_u32 m0, %[lb], 20480\n\t"
    "s_nop 0\n\t"
    "global_load_lds_dwordx4 %[av2], %[s1] sc1\n\t"
    "v_mfma_f32_16x16x32_bf16 v[92:95], v[244:247], v[116:119], v[92:95]\n\t"
    "s_add_u32 m0, %[lb], 53248\n\t"
    "s_nop 0\n\t"
    "global_load_lds_dwordx4 %[av3], %[s1] sc1\n\t"
    "s_waitcnt lgkmcnt(0)\n\t"
    "v_mfma_f32_16x16x32_bf16 v[80:83], v[248:251], v[120:123], v[80:83]\n\t"
    "s_add_u32 m0, %[lb], 24576\n\t"
    "s_nop 0\n\t"
    "global_load_lds_dwordx4 %[av4], %[s1] sc1\n\t"
    "v_mfma_f32_16x16x32_bf16 v[84:87], v[252:255], v[120:123], v[84:87]\n\t"
    "s_add_u32 m0, %[lb], 57344\n\t"
    "s_nop 0\n\t"
    "global_load_lds_dwordx4 %[av5], %[s1] sc1\n\t"
    "v_mfma_f32_16x16x32_bf16 v[88:91], v[248:251], v[124:127], v[88:91]\n\t"
    "s_add_u32 m0, %[lb], 28672\n\t"
    "s_nop 0\n\t"
    "global_load_lds_dwordx4 %[av6], %[s1] sc1\n\t"
    "v_mfma_f32_16x16x32_bf16 v[92:95], v[252:255], v[124:127], v[92:95]\n\t"
    "s_add_u32 m0, %[lb], 61440\n\t"
    "s_nop 0\n\t"
    "global_load_lds_dwordx4 %[av7], %[s1] sc1\n\t"
    "6:\n\t"
    "s_nop 6\n\t"
    "s_branch 9f\n\t"
    "3:\n\t"
    "9:"
    : "=&{v[80:95]}"(pd)
    : [ea] "v"(ea), [da] "v"(da), [x] "s"(x), "{v[128:159]}"(W[0]), "{v[160:191]}"(W[1]), "{v[192:223]}"(W[2]), "{v[224:255]}"(W[3]), [lb] "s"(lb), [s0] "s"(s0), [s1] "s"(s1), [av0] "v"(av[0]), [av1] "v"(av[1]), [av2] "v"(av[2]), [av3] "v"(av[3]), [av4] "v"(av[4]), [av5] "v"(av[5]), [av6] "v"(av[6]), [av7] "v"(av[7]), [sa] "v"(sa), [mu] "s"(mu), [ad] "v"(ad), [th] "s"(th), [s2] "s"(s2), [dv] "v"(dv), [db] "s"(db)
    : "v96", "v97", "v98", "v99", "v100", "v101", "v102", "v103", "v104", "v105", "v106", "v107", "v108", "v109", "v110", "v111", "v112", "v113", "v114", "v115", "v116", "v117", "v118", "v119", "v120", "v121", "v122", "v123", "v124", "v125", "v126", "v127", "v79", "vcc", "scc", "m0", "memory");
}
__device__ __forceinline__ void lean_dm0(const u32x32 (&W)[4], u32x16& pd, unsigned ea, unsigned da, unsigned x, unsigned lb, const char* s0, const char* s1, const unsigned (&av)[8]) {
  asm volatile("s_setprio 2\n\t"
    "ds_read_b128 v[96:99], %[ea] offset:0\n\t"
    "ds_read_b128 v[100:103], %[ea] offset:2048\n\t"
    "ds_read_b128 v[104:107], %[da] offset:0\n\t"
    "ds_read_b128 v[108:111], %[da] offset:2048\n\t"
    "ds_read_b128 v[112:115], %[ea] offset:4096\n\t"
    "ds_read_b128 v[116:119], %[ea] offset:6144\n\t"
    "s_waitcnt lgkmcnt(4)\n\t"
    "v_mfma_f32_16x16x32_bf16 v[80:83], v[128:131], v[96:99], 0\n\t"
    "ds_read_b128 v[120:123], %[da] offset:4096\n\t"
    "s_add_u32 m0, %[lb], 0\n\t"
    "v_mfma_f32_16x16x32_bf16 v[84:87], v[132:135], v[96:99], 0\n\t"
    "ds_read_b128 v[124:127], %[da] offset:6144\n\t"
    "global_load_lds_dwordx4 %[av0], %[s0] sc1\n\t"
    "v_mfma_f32_16x16x32_bf16 v[88:91], v[128:131], v[100:103], 0\n\t"
    "s_add_u32 m0, %[lb], 32768\n\t"
    "v_mfma_f32_16x16x32_bf16 v[92:95], v[132:135], v[100:103], 0\n\t"
    "global_load_lds_dwordx4 %[av1], %[s0] sc1\n\t"
    "s_waitcnt vmcnt(10)\n\t"
    "s_barrier\n\t"
    "s_waitcnt lgkmcnt(4)\n\t"
    "v_mfma_f32_16x16x32_bf16 v[80:83], v[136:139], v[104:107], v[80:83]\n\t"
    "ds_read_b128 v[96:99], %[ea] offset:8192\n\t"
    "s_add_u32 m0, %[lb], 4096\n\t"
    "v_mfma_f32_16x16x32_bf16 v[84:87], v[140:143], v[104:107], v[84:87]\n\t"
    "ds_read_b128 v[100:103], %[ea] offset:10240\n\t"
    "global_load_lds_dwordx4 %[av2], %[s0] sc1\n\t"
    "v_mfma_f32_16x16x32_bf16 v[88:91], v[136:139], v[108:111], v[88:91]\n\t"
    "s_add_u32 m0, %[lb], 36864\n\t"
    "v_mfma_f32_16x16x32_bf16 v[92:95], v[140:143], v[108:111], v[92:95]\n\t"
    "global_load_lds_dwordx4 %[av3], %[s0] sc1\n\t"
    "s_waitcnt lgkmcnt(4)\n\t"
    "v_mfma_f32_16x16x32_bf16 v[80:83], v[144:147], v[112:115], v[80:83]\n\t"
    "ds_read_b128 v[104:107], %[da] offset:8192\n\t"
    "s_add_u32 m0, %[lb], 8192\n\t"
    "v_mfma_f32_16x16x32_bf16 v[84:87], v[148:151], v[112:115], v[84:87]\n\t"
    "ds_read_b128 v[108:111], %[da] offset:10240\n\t"
    "global_load_lds_dwordx4 %[av4], %[s0] sc1\n\t"
    "v_mfma_f32_16x16x32_bf16 v[88:91], v[144:147], v[116:119], v[88:91]\n\t"
    "s_add_u32 m0, %[lb], 40960\n\t"
    "v_mfma_f32_16x16x32_bf16 v[92:95], v[148:151], v[116:119], v[92:95]\n\t"
    "global_load_lds_dwordx4 %[av5], %[s0] sc1\n\t"
    "s_waitcnt lgkmcnt(4)\n\t"
    "v_mfma_f32_16x16x32_bf16 v[80:83], v[152:155], v[120:123], v[80:83]\n\t"
    "ds_read_b128 v[112:115], %[ea] offset:12288\n\t"
    "s_add_u32 m0, %[lb], 12288\n\t"
    "v_mfma_f32_16x16x32_bf16 v[84:87], v[156:159], v[120:123], v[84:87]\n\t"
    "ds_read_b128 v[116:119], %[ea] offset:14336\n\t"
    "global_load_lds_dwordx4 %[av6], %[s0] sc1\n\t"
    "v_mfma_f32_16x16x32_bf16 v[88:91], v[152:155], v[124:127], v[88:91]\n\t"
    "s_add_u32 m0, %[lb], 45056\n\t"
    "v_mfma_f32_16x16x32_bf16 v[92:95], v[156:159], v[124:127], v[92:95]\n\t"
    "global_load_lds_dwordx4 %[av7], %[s0] sc1\n\t"
    "s_waitcnt lgkmcnt(4)\n\t"
    "v_mfma_f32_16x16x32_bf16 v[80:83], v[160:163], v[96:99], v[80:83]\n\t"
    "ds_read_b128 v[120:123], %[da] offset:12288\n\t"
    "s_add_u32 m0, %[lb], 16384\n\t"
    "v_mfma_f32_16x16x32_bf16 v[84:87], v[164:167], v[96:99], v[84:87]\n\t"
    "ds_read_b128 v[124:127], %[da] offset:14336\n\t"
    "global_load_lds_dwordx4 %[av0], %[s1] sc1\n\t"
    "v_mfma_f32_16x16x32_bf16 v[88:91], v[160:163], v[100:103], v[88:91]\n\t"
    "s_add_u32 m0, %[lb], 49152\n\t"
    "v_mfma_f32_16x16x32_bf16 v[92:95], v[164:167], v[100:103], v[92:95]\n\t"
    "global_load_lds_dwordx4 %[av1], %[s1] sc1\n\t"
    "s_waitcnt vmcnt(14)\n\t"
    "s_barrier\n\t"
    "s_waitcnt lgkmcnt(4)\n\t"
    "v_mfma_f32_16x16x32_bf16 v[80:83], v[168:171], v[104:107], v[80:83]\n\t"
    "ds_read_b128 v[96:99], %[ea] offset:16384\n\t"
    "s_add_u32 m0, %[lb], 20480\n\t"
    "v_mfma_f32_16x16x32_bf16 v[84:87], v[172:175], v[104:107], v[84:87]\n\t"
    "ds_read_b128 v[100:103], %[ea] offset:18432\n\t"
    "global_load_lds_dwordx4 %[av2], %[s1] sc1\n\t"
    "v_mfma_f32_16x16x32_bf16 v[88:91], v[168:171], v[108:111], v[88:91]\n\t"
    "s_add_u32 m0, %[lb], 53248\n\t"
    "v_mfma_f32_16x16x32_bf16 v[92:95], v[172:175], v[108:111], v[92:95]\n\t"
    "global_load_lds_dwordx4 %[av3], %[s1] sc1\n\t"
    "s_waitcnt lgkmcnt(4)\n\t"
    "v_mfma_f32_16x16x32_bf16 v[80:83], v[176:179], v[112:115], v[80:83]\n\t"
    "ds_read_b128 v[104:107], %[da] offset:16384\n\t"
    "s_add_u32 m0, %[lb], 24576\n\t"
    "v_mfma_f32_16x16x32_bf16 v[84:87], v[180:183], v[112:115], v[84:87]\n\t"
    "ds_read_b128 v[108:111], %[da] offset:18432\n\t"
    "global_load_lds_dwordx4 %[av4], %[s1] sc1\n\t"
    "v_mfma_f32_16x16x32_bf16 v[88:91], v[176:179], v[116:119], v[88:91]\n\t"
    "s_add_u32 m0, %[lb], 57344\n\t"
    "v_mfma_f32_16x16x32_bf16 v[92:95], v[180:183], v[116:119], v[92:95]\n\t"
    "global_load_lds_dwordx4 %[av5], %[s1] sc1\n\t"
    "s_waitcnt lgkmcnt(4)\n\t"
    "v_mfma_f32_16x16x32_bf16 v[80:83], v[184:187], v[120:123], v[80:83]\n\t"
    "ds_read_b128 v[112:115], %[ea] offset:20480\n\t"
    "s_add_u32 m0, %[lb], 28672\n\t"
    "v_mfma_f32_16x16x32_bf16 v[84:87], v[188:191], v[120:123], v[84:87]\n\t"
    "ds_read_b128 v[116:119], %[ea] offset:22528\n\t"
    "global_load_lds_dwordx4 %[av6], %[s1] sc1\n\t"
    "v_mfma_f32_16x16x32_bf16 v[88:91], v[184:187], v[124:127], v[88:91]\n\t"
    "s_add_u32 m0, %[lb], 61440\n\t"
    "v_mfma_f32_16x16x32_bf16 v[92:95], v[188:191], v[124:127], v[92:95]\n\t"
    "global_load_lds_dwordx4 %[av7], %[s1] sc1\n\t"
    "s_waitcnt lgkmcnt(4)\n\t"
    "v_mfma_f32_16x16x32_bf16 v[80:83], v[192:195], v[96:99], v[80:83]\n\t"
    "ds_read_b128 v[120:123], %[da] offset:20480\n\t"
    "v_mfma_f32_16x16x32_bf16 v[84:87], v[196:199], v[96:99], v[84:87]\n\t"
    "ds_read_b128 v[124:127], %[da] offset:22528\n\t"
    "v_mfma_f32_16x16x32_bf16 v[88:91], v[192:195], v[100:103], v[88:91]\n\t"
    "v_mfma_f32_16x16x32_bf16 v[92:95], v[196:199], v[100:103], v[92:95]\n\t"
    "s_waitcnt vmcnt(16)\n\t"
    "s_barrier\n\t"
    "s_waitcnt lgkmcnt(4)\n\t"
    "v_mfma_f32_16x16x32_bf16 v[80:83], v[200:203], v[104:107], v[80:83]\n\t"
    "ds_read_b128 v[96:99], %[ea] offset:24576\n\t"
    "v_mfma_f32_16x16x32_bf16 v[84:87], v[204:207], v[104:107], v[84:87]\n\t"
    "ds_read_b128 v[100:103], %[ea] offset:26624\n\t"
    "v_mfma_f32_16x16x32_bf16 v[88:91], v[200:203], v[108:111], v[88:91]\n\t"
    "v_mfma_f32_16x16x32_bf16 v[92:95], v[204:207], v[108:111], v[92:95]\n\t"
    "s_waitcnt lgkmcnt(4)\n\t"
    "v_mfma_f32_16x16x32_bf16 v[80:83], v[208:211], v[112:115], v[80:83]\n\t"
    "ds_read_b128 v[104:107], %[da] offset:24576\n\t"
    "v_mfma_f32_16x16x32_bf16 v[84:87], v[212:215], v[112:115], v[84:87]\n\t"
    "ds_read_b128 v[108:111], %[da] offset:26624\n\t"
    "v_mfma_f32_16x16x32_bf16 v[88:91], v[208:211], v[116:119], v[88:91]\n\t"
    "v_mfma_f32_16x16x32_bf16 v[92:95], v[212:215], v[116:119], v[92:95]\n\t"
    "s_waitcnt lgkmcnt(4)\n\t"
    "v_mfma_f32_16x16x32_bf16 v[80:83], v[216:219], v[120:123], v[80:83]\n\t"
    "ds_read_b128 v[112:115], %[ea] offset:28672\n\t"
    "v_mfma_f32_16x16x32_bf16 v[84:87], v[220:223], v[120:123], v[84:87]\n\t"
    "ds_read_b128 v[116:119], %[ea] offset:30720\n\t"
    "v_mfma_f32_16x16x32_bf16 v[88:91], v[216:219], v[124:127], v[88:91]\n\t"
    "v_mfma_f32_16x16x32_bf16 v[92:95], v[220:223], v[124:127], v[92:95]\n\t"
    "s_waitcnt lgkmcnt(4)\n\t"
    "v_mfma_f32_16x16x32_bf16 v[80:83], v[224:227], v[96:99], v[80:83]\n\t"
    "ds_read_b128 v[120:123], %[da] offset:28672\n\t"
    "v_mfma_f32_16x16x32_bf16 v[84:87], v[228:231], v[96:99], v[84:87]\n\t"
    "ds_read_b128 v[124:127], %[da] offset:30720\n\t"
    "v_mfma_f32_16x16x32_bf16 v[88:91], v[224:227], v[100:103], v[88:91]\n\t"
    "v_mfma_f32_16x16x32_bf16 v[92:95], v[228:231], v[100:103], v[92:95]\n\t"
    "s_waitcnt lgkmcnt(4)\n\t"
    "v_mfma_f32_16x16x32_bf16 v[80:83], v[232:235], v[104:107], v[80:83]\n\t"
    "v_mfma_f32_16x16x32_bf16 v[84:87], v[236:239], v[104:107], v[84:87]\n\t"
    "v_mfma_f32_16x16x32_bf16 v[88:91], v[232:235], v[108:111], v[88:91]\n\t"
    "v_mfma_f32_16x16x32_bf16 v[92:95], v[236:239], v[108:111], v[92:95]\n\t"
    "s_waitcnt lgkmcnt(2)\n\t"
    "v_mfma_f32_16x16x32_bf16 v[80:83], v[240:243], v[112:115], v[80:83]\n\t"
    "v_mfma_f32_16x16x32_bf16 v[84:87], v[244:247], v[112:115], v[84:87]\n\t"
    "v_mfma_f32_16x16x32_bf16 v[88:91], v[240:243], v[116:119], v[88:91]\n\t"
    "v_mfma_f32_16x16x32_bf16 v[92:95], v[244:247], v[116:119], v[92:95]\n\t"
    "s_waitcnt lgkmcnt(0)\n\t"
    "v_mfma_f32_16x16x32_bf16 v[80:83], v[248:251], v[120:123], v[80:83]\n\t"
    "v_mfma_f32_16x16x32_bf16 v[84:87], v[252:255], v[120:123], v[84:87]\n\t"
    "v_mfma_f32_16x16x32_bf16 v[88:91], v[248:251], v[124:127], v[88:91]\n\t"
    "v_mfma_f32_16x16x32_bf16 v[92:95], v[252:255], v[124:127], v[92:95]\n\t"
    "s_nop 6\n\t"
    "s_branch 9f\n\t"
    "3:\n\t"
    "9:"
    : "=&{v[80:95]}"(pd)
    : [ea] "v"(ea), [da] "v"(da), [x] "s"(x), "{v[128:159]}"(W[0]), "{v[160:191]}"(W[1]), "{v[192:223]}"(W[2]), "{v[224:255]}"(W[3]), [lb] "s"(lb), [s0] "s"(s0), [s1] "s"(s1), [av0] "v"(av[0]), [av1] "v"(av[1]), [av2] "v"(av[2]), [av3] "v"(av[3]), [av4] "v"(av[4]), [av5] "v"(av[5]), [av6] "v"(av[6]), [av7] "v"(av[7])
    : "v96", "v97", "v98", "v99", "v100", "v101", "v102", "v103", "v104", "v105", "v106", "v107", "v108", "v109", "v110", "v111", "v112", "v113", "v114", "v115", "v116", "v117", "v118", "v119", "v120", "v121", "v122", "v123", "v124", "v125", "v126", "v127", "vcc", "scc", "m0", "memory");
}
__device__ __forceinline__ void lean_ld1(const u32x32 (&W)[4], u32x16& pd, unsigned ea, unsigned da, unsigned x, unsigned lb, const char* s0, const char* s1, const unsigned (&av)[8], unsigned sa, unsigned mu, unsigned ad, int th, unsigned s2, unsigned dv, const char* db) {
  asm volatile("s_setprio 2\n\t"
    "ds_read_b128 v[96:99], %[ea] offset:0\n\t"
    "ds_read_b128 v[100:103], %[ea] offset:2048\n\t"
    "ds_read_b128 v[104:107], %[da] offset:0\n\t"
    "ds_read_b128 v[108:111], %[da] offset:2048\n\t"
    "ds_read_b128 v[112:115], %[ea] offset:4096\n\t"
    "ds_read_b128 v[116:119], %[ea] offset:6144\n\t"
    "s_waitcnt lgkmcnt(4)\n\t"
    "v_mfma_f32_16x16x32_bf16 v[80:83], v[128:131], v[96:99], 0\n\t"
    "ds_read_b128 v[120:123], %[da] offset:4096\n\t"
    "v_mfma_f32_16x16x32_bf16 v[84:87], v[132:135], v[96:99], 0\n\t"
    "ds_read_b128 v[124:127], %[da] offset:6144\n\t"
    "v_mfma_f32_16x16x32_bf16 v[88:91], v[128:131], v[100:103], 0\n\t"
    "v_mfma_f32_16x16x32_bf16 v[92:95], v[132:135], v[100:103], 0\n\t"
    "s_waitcnt vmcnt(12)\n\t"
    "s_cmp_lt_u32 %[x], 2\n\t"
    "s_cbranch_scc0 41f\n\t"
    "s_waitcnt vmcnt(4)\n\t"
    "41:\n\t"
    "s_barrier\n\t"
    "s_waitcnt lgkmcnt(4)\n\t"
    "v_mfma_f32_16x16x32_bf16 v[80:83], v[136:139], v[104:107], v[80:83]\n\t"
    "ds_read_b128 v[96:99], %[ea] offset:8192\n\t"
    "v_mfma_f32_16x16x32_bf16 v[84:87], v[140:143], v[104:107], v[84:87]\n\t"
    "ds_read_b128 v[100:103], %[ea] offset:10240\n\t"
    "v_mfma_f32_16x16x32_bf16 v[88:91], v[136:139], v[108:111], v[88:91]\n\t"
    "v_mfma_f32_16x16x32_bf16 v[92:95], v[140:143], v[108:111], v[92:95]\n\t"
    "s_waitcnt lgkmcnt(4)\n\t"
    "v_mfma_f32_16x16x32_bf16 v[80:83], v[144:147], v[112:115], v[80:83]\n\t"
    "ds_read_b128 v[104:107], %[da] offset:8192\n\t"
    "v_mfma_f32_16x16x32_bf16 v[84:87], v[148:151], v[112:115], v[84:87]\n\t"
    "ds_read_b128 v[108:111], %[da] offset:10240\n\t"
    "v_mfma_f32_16x16x32_bf16 v[88:91], v[144:147], v[116:119], v[88:91]\n\t"
    "v_mfma_f32_16x16x32_bf16 v[92:95], v[148:151], v[116:119], v[92:95]\n\t"
    "s_waitcnt lgkmcnt(4)\n\t"
    "v_mfma_f32_16x16x32_bf16 v[80:83], v[152:155], v[120:123], v[80:83]\n\t"
    "ds_read_b128 v[112:115], %[ea] offset:12288\n\t"
    "v_mfma_f32_16x16x32_bf16 v[84:87], v[156:159], v[120:123], v[84:87]\n\t"
    "ds_read_b128 v[116:119], %[ea] offset:14336\n\t"
    "v_mfma_f32_16x16x32_bf16 v[88:91], v[152:155], v[124:127], v[88:91]\n\t"
    "v_mfma_f32_16x16x32_bf16 v[92:95], v[156:159], v[124:127], v[92:95]\n\t"
    "s_waitcnt lgkmcnt(4)\n\t"
    "v_mfma_f32_16x16x32_bf16 v[80:83], v[160:163], v[96:99], v[80:83]\n\t"
    "ds_read_b128 v[120:123], %[da] offset:12288\n\t"
    "v_mfma_f32_16x16x32_bf16 v[84:87], v[164:167], v[96:99], v[84:87]\n\t"
    "ds_read_b128 v[124:127], %[da] offset:14336\n\t"
    "v_mfma_f32_16x16x32_bf16 v[88:91], v[160:163], v[100:103], v[88:91]\n\t"
    "v_mfma_f32_16x16x32_bf16 v[92:95], v[164:167], v[100:103], v[92:95]\n\t"
    "s_waitcnt vmcnt(10)\n\t"
    "s_cmp_lt_u32 %[x], 2\n\t"
    "s_cbranch_scc0 45f\n\t"
    "s_waitcnt vmcnt(2)\n\t"
    "45:\n\t"
    "s_barrier\n\t"
    "s_waitcnt lgkmcnt(4)\n\t"
    "v_mfma_f32_16x16x32_bf16 v[80:83], v[168:171], v[104:107], v[80:83]\n\t"
    "ds_read_b128 v[96:99], %[ea] offset:16384\n\t"
    "v_mfma_f32_16x16x32_bf16 v[84:87], v[172:175], v[104:107], v[84:87]\n\t"
    "ds_read_b128 v[100:103], %[ea] offset:18432\n\t"
    "v_mfma_f32_16x16x32_bf16 v[88:91], v[168:171], v[108:111], v[88:91]\n\t"
    "v_mfma_f32_16x16x32_bf16 v[92:95], v[172:175], v[108:111], v[92:95]\n\t"
    "s_waitcnt lgkmcnt(4)\n\t"
    "v_mfma_f32_16x16x32_bf16 v[80:83], v[176:179], v[112:115], v[80:83]\n\t"
    "ds_read_b128 v[104:107], %[da] offset:16384\n\t"
    "v_mfma_f32_16x16x32_bf16 v[84:87], v[180:183], v[112:115], v[84:87]\n\t"
    "ds_read_b128 v[108:111], %[da] offset:18432\n\t"
    "v_mfma_f32_16x16x32_bf16 v[88:91], v[176:179], v[116:119], v[88:91]\n\t"
    "v_mfma_f32_16x16x32_bf16 v[92:95], v[180:183], v[116:119], v[92:95]\n\t"
    "s_waitcnt lgkmcnt(4)\n\t"
    "v_mfma_f32_16x16x32_bf16 v[80:83], v[184:187], v[120:123], v[80:83]\n\t"
    "ds_read_b128 v[112:115], %[ea] offset:20480\n\t"
    "v_mfma_f32_16x16x32_bf16 v[84:87], v[188:191], v[120:123], v[84:87]\n\t"
    "ds_read_b128 v[116:119], %[ea] offset:22528\n\t"
    "v_mfma_f32_16x16x32_bf16 v[88:91], v[184:187], v[124:127], v[88:91]\n\t"
    "v_mfma_f32_16x16x32_bf16 v[92:95], v[188:191], v[124:127], v[92:95]\n\t"
    "s_waitcnt lgkmcnt(4)\n\t"
    "v_mfma_f32_16x16x32_bf16 v[80:83], v[192:195], v[96:99], v[80:83]\n\t"
    "ds_read_b128 v[120:123], %[da] offset:20480\n\t"
    "v_mfma_f32_16x16x32_bf16 v[84:87], v[196:199], v[96:99], v[84:87]\n\t"
    "ds_read_b128 v[124:127], %[da] offset:22528\n\t"
    "v_mfma_f32_16x16x32_bf16 v[88:91], v[192:195], v[100:103], v[88:91]\n\t"
    "v_mfma_f32_16x16x32_bf16 v[92:95], v[196:199], v[100:103], v[92:95]\n\t"
    "s_waitcnt vmcnt(8)\n\t"
    "s_cmp_lt_u32 %[x], 2\n\t"
    "s_cbranch_scc0 49f\n\t"
    "s_waitcnt vmcnt(0)\n\t"
    "49:\n\t"
    "s_barrier\n\t"
    "ds_read_b32 v79, %[sa]\n\t"
    "s_waitcnt lgkmcnt(5)\n\t"
    "v_mfma_f32_16x16x32_bf16 v[80:83], v[200:203], v[104:107], v[80:83]\n\t"
    "ds_read_b128 v[96:99], %[ea] offset:24576\n\t"
    "v_mfma_f32_16x16x32_bf16 v[84:87], v[204:207], v[104:107], v[84:87]\n\t"
    "ds_read_b128 v[100:103], %[ea] offset:26624\n\t"
    "v_mfma_f32_16x16x32_bf16 v[88:91], v[200:203], v[108:111], v[88:91]\n\t"
    "v_mfma_f32_16x16x32_bf16 v[92:95], v[204:207], v[108:111], v[92:95]\n\t"
    "s_waitcnt lgkmcnt(5)\n\t"
    "v_mfma_f32_16x16x32_bf16 v[80:83], v[208:211], v[112:115], v[80:83]\n\t"
    "ds_read_b128 v[104:107], %[da] offset:24576\n\t"
    "v_mfma_f32_16x16x32_bf16 v[84:87], v[212:215], v[112:115], v[84:87]\n\t"
    "ds_read_b128 v[108:111], %[da] offset:26624\n\t"
    "v_mfma_f32_16x16x32_bf16 v[88:91], v[208:211], v[116:119], v[88:91]\n\t"
    "v_mfma_f32_16x16x32_bf16 v[92:95], v[212:215], v[116:119], v[92:95]\n\t"
    "s_cmp_eq_u32 %[s2], 0\n\t"
    "s_cbranch_scc1 7f\n\t"
    "s_mov_b32 m0, %[s2]\n\t"
    "s_nop 0\n\t"
    "global_load_lds_dword %[dv], %[db] sc1\n\t"
    "7:\n\t"
    "s_waitcnt lgkmcnt(4)\n\t"
    "v_mfma_f32_16x16x32_bf16 v[80:83], v[216:219], v[120:123], v[80:83]\n\t"
    "ds_read_b128 v[112:115], %[ea] offset:28672\n\t"
    "v_mfma_f32_16x16x32_bf16 v[84:87], v[220:223], v[120:123], v[84:87]\n\t"
    "ds_read_b128 v[116:119], %[ea] offset:30720\n\t"
    "v_mfma_f32_16x16x32_bf16 v[88:91], v[216:219], v[124:127], v[88:91]\n\t"
    "v_mad_u32_u24 v79, v79, %[mu], %[ad]\n\t"
    "v_cmp_ge_i32 vcc, %[th], v79\n\t"
    "v_mfma_f32_16x16x32_bf16 v[92:95], v[220:223], v[124:127], v[92:95]\n\t"
    "s_cbranch_vccz 5f\n\t"
    "s_waitcnt lgkmcnt(4)\n\t"
    "v_mfma_f32_16x16x32_bf16 v[80:83], v[224:227], v[96:99], v[80:83]\n\t"
    "ds_read_b128 v[120:123], %[da] offset:28672\n\t"
    "v_mfma_f32_16x16x32_bf16 v[84:87], v[228:231], v[96:99], v[84:87]\n\t"
    "ds_read_b128 v[124:127], %[da] offset:30720\n\t"
    "v_mfma_f32_16x16x32_bf16 v[88:91], v[224:227], v[100:103], v[88:91]\n\t"
    "v_mfma_f32_16x16x32_bf16 v[92:95], v[228:231], v[100:103], v[92:95]\n\t"
    "s_waitcnt lgkmcnt(4)\n\t"
    "v_mfma_f32_16x16x32_bf16 v[80:83], v[232:235], v[104:107], v[80:83]\n\t"
    "v_mfma_f32_16x16x32_bf16 v[84:87], v[236:239], v[104:107], v[84:87]\n\t"
    "v_mfma_f32_16x16x32_bf16 v[88:91], v[232:235], v[108:111], v[88:91]\n\t"
    "v_mfma_f32_16x16x32_bf16 v[92:95], v[236:239], v[108:111], v[92:95]\n\t"
    "s_waitcnt lgkmcnt(2)\n\t"
    "v_mfma_f32_16x16x32_bf16 v[80:83], v[240:243], v[112:115], v[80:83]\n\t"
    "v_mfma_f32_16x16x32_bf16 v[84:87], v[244:247], v[112:115], v[84:87]\n\t"
    "v_mfma_f32_16x16x32_bf16 v[88:91], v[240:243], v[116:119], v[88:91]\n\t"
    "v_mfma_f32_16x16x32_bf16 v[92:95], v[244:247], v[116:119], v[92:95]\n\t"
    "s_waitcnt lgkmcnt(0)\n\t"
    "v_mfma_f32_16x16x32_bf16 v[80:83], v[248:251], v[120:123], v[80:83]\n\t"
    "v_mfma_f32_16x16x32_bf16 v[84:87], v[252:255], v[120:123], v[84:87]\n\t"
    "v_mfma_f32_16x16x32_bf16 v[88:91], v[248:251], v[124:127], v[88:91]\n\t"
    "v_mfma_f32_16x16x32_bf16 v[92:95], v[252:255], v[124:127], v[92:95]\n\t"
    "s_branch 6f\n\t"
    "5:\n\t"
    "s_waitcnt lgkmcnt(4)\n\t"
    "v_mfma_f32_16x16x32_bf16 v[80:83], v[224:227], v[96:99], v[80:83]\n\t"
    "ds_read_b128 v[120:123], %[da] offset:28672\n\t"
    "s_add_u32 m0, %[lb], 0\n\t"
    "s_nop 0\n\t"
    "global_load_lds_dwordx4 %[av0], %[s0] sc1\n\t"
    "v_mfma_f32_16x16x32_bf16 v[84:87], v[228:231], v[96:99], v[84:87]\n\t"
    "ds_read_b128 v[124:127], %[da] offset:30720\n\t"
    "v_mfma_f32_16x16x32_bf16 v[88:91], v[224:227], v[100:103], v[88:91]\n\t"
    "s_add_u32 m0, %[lb], 4096\n\t"
    "s_nop 0\n\t"
    "global_load_lds_dwordx4 %[av2], %[s0] sc1\n\t"
    "v_mfma_f32_16x16x32_bf16 v[92:95], v[228:231], v[100:103], v[92:95]\n\t"
    "s_waitcnt lgkmcnt(4)\n\t"
    "v_mfma_f32_16x16x32_bf16 v[80:83], v[232:235], v[104:107], v[80:83]\n\t"
    "s_add_u32 m0, %[lb], 8192\n\t"
    "s_nop 0\n\t"
    "global_load_lds_dwordx4 %[av4], %[s0] sc1\n\t"
    "v_mfma_f32_16x16x32_bf16 v[84:87], v[236:239], v[104:107], v[84:87]\n\t"
    "v_mfma_f32_16x16x32_bf16 v[88:91], v[232:235], v[108:111], v[88:91]\n\t"
    "s_add_u32 m0, %[lb], 12288\n\t"
    "s_nop 0\n\t"
    "global_load_lds_dwordx4 %[av6], %[s0] sc1\n\t"
    "v_mfma_f32_16x16x32_bf16 v[92:95], v[236:239], v[108:111], v[92:95]\n\t"
    "s_waitcnt lgkmcnt(2)\n\t"
    "v_mfma_f32_16x16x32_bf16 v[80:83], v[240:243], v[112:115], v[80:83]\n\t"
    "s_add_u32 m0, %[lb], 16384\n\t"
    "s_nop 0\n\t"
    "global_load_lds_dwordx4 %[av0], %[s1] sc1\n\t"
    "v_mfma_f32_16x16x32_bf16 v[84:87], v[244:247], v[112:115], v[84:87]\n\t"
    "v_mfma_f32_16x16x32_bf16 v[88:91], v[240:243], v[116:119], v[88:91]\n\t"
    "s_add_u32 m0, %[lb], 20480\n\t"
    "s_nop 0\n\t"
    "global_load_lds_dwordx4 %[av2], %[s1] sc1\n\t"
    "v_mfma_f32_16x16x32_bf16 v[92:95], v[244:247], v[116:119], v[92:95]\n\t"
    "s_waitcnt lgkmcnt(0)\n\t"
    "v_mfma_f32_16x16x32_bf16 v[80:83], v[248:251], v[120:123], v[80:83]\n\t"
    "s_add_u32 m0, %[lb], 24576\n\t"
    "s_nop 0\n\t"
    "global_load_lds_dwordx4 %[av4], %[s1] sc1\n\t"
    "v_mfma_f32_16x16x32_bf16 v[84:87], v[252:255], v[120:123], v[84:87]\n\t"
    "v_mfma_f32_16x16x32_bf16 v[88:91], v[248:251], v[124:127], v[88:91]\n\t"
    "s_add_u32 m0, %[lb], 28672\n\t"
    "s_nop 0\n\t"
    "global_load_lds_dwordx4 %[av6], %[s1] sc1\n\t"
    "v_mfma_f32_16x16x32_bf16 v[92:95], v[252:255], v[124:127], v[92:95]\n\t"
    "6:\n\t"
    "s_nop 6\n\t"
    "s_branch 9f\n\t"
    "3:\n\t"
    "9:"
    : "=&{v[80:95]}"(pd)
    : [ea] "v"(ea), [da] "v"(da), [x] "s"(x), "{v[128:159]}"(W[0]), "{v[160:191]}"(W[1]), "{v[192:223]}"(W[2]), "{v[224:255]}"(W[3]), [lb] "s"(lb), [s0] "s"(s0), [s1] "s"(s1), [av0] "v"(av[0]), [av1] "v"(av[1]), [av2] "v"(av[2]), [av3] "v"(av[3]), [av4] "v"(av[4]), [av5] "v"(av[5]), [av6] "v"(av[6]), [av7] "v"(av[7]), [sa] "v"(sa), [mu] "s"(mu), [ad] "v"(ad), [th] "s"(th), [s2] "s"(s2), [dv] "v"(dv), [db] "s"(db)
    : "v96", "v97", "v98", "v99", "v100", "v101", "v102", "v103", "v104", "v105", "v106", "v107", "v108", "v109", "v110", "v111", "v112", "v113", "v114", "v115", "v116", "v117", "v118", "v119", "v120", "v121", "v122", "v123", "v124", "v125", "v126", "v127", "v79", "vcc", "scc", "m0", "memory");
}
__device__ __forceinline__ void lean_dm1(const u32x32 (&W)[4], u32x16& pd, unsigned ea, unsigned da, unsigned x, unsigned lb, const char* s0, const char* s1, const unsigned (&av)[8]) {
  asm volatile("s_setprio 2\n\t"
    "ds_read_b128 v[96:99], %[ea] offset:0\n\t"
    "ds_read_b128 v[100:103], %[ea] offset:2048\n\t"
    "ds_read_b128 v[104:107], %[da] offset:0\n\t"
    "ds_read_b128 v[108:111], %[da] offset:2048\n\t"
    "ds_read_b128 v[112:115], %[ea] offset:4096\n\t"
    "ds_read_b128 v[116:119], %[ea] offset:6144\n\t"
    "s_waitcnt lgkmcnt(4)\n\t"
    "v_mfma_f32_16x16x32_bf16 v[80:83], v[128:131], v[96:99], 0\n\t"
    "ds_read_b128 v[120:123], %[da] offset:4096\n\t"
    "s_add_u32 m0, %[lb], 0\n\t"
    "v_mfma_f32_16x16x32_bf16 v[84:87], v[132:135], v[96:99], 0\n\t"
    "ds_read_b128 v[124:127], %[da] offset:6144\n\t"
    "global_load_lds_dwordx4 %[av0], %[s0] sc1\n\t"
    "v_mfma_f32_16x16x32_bf16 v[88:91], v[128:131], v[100:103], 0\n\t"
    "v_mfma_f32_16x16x32_bf16 v[92:95], v[132:135], v[100:103], 0\n\t"
    "s_waitcnt vmcnt(5)\n\t"
    "s_barrier\n\t"
    "s_waitcnt lgkmcnt(4)\n\t"
    "v_mfma_f32_16x16x32_bf16 v[80:83], v[136:139], v[104:107], v[80:83]\n\t"
    "ds_read_b128 v[96:99], %[ea] offset:8192\n\t"
    "s_add_u32 m0, %[lb], 4096\n\t"
    "v_mfma_f32_16x16x32_bf16 v[84:87], v[140:143], v[104:107], v[84:87]\n\t"
    "ds_read_b128 v[100:103], %[ea] offset:10240\n\t"
    "global_load_lds_dwordx4 %[av2], %[s0] sc1\n\t"
    "v_mfma_f32_16x16x32_bf16 v[88:91], v[136:139], v[108:111], v[88:91]\n\t"
    "v_mfma_f32_16x16x32_bf16 v[92:95], v[140:143], v[108:111], v[92:95]\n\t"
    "s_waitcnt lgkmcnt(4)\n\t"
    "v_mfma_f32_16x16x32_bf16 v[80:83], v[144:147], v[112:115], v[80:83]\n\t"
    "ds_read_b128 v[104:107], %[da] offset:8192\n\t"
    "s_add_u32 m0, %[lb], 8192\n\t"
    "v_mfma_f32_16x16x32_bf16 v[84:87], v[148:151], v[112:115], v[84:87]\n\t"
    "ds_read_b128 v[108:111], %[da] offset:10240\n\t"
    "global_load_lds_dwordx4 %[av4], %[s0] sc1\n\t"
    "v_mfma_f32_16x16x32_bf16 v[88:91], v[144:147], v[116:119], v[88:91]\n\t"
    "v_mfma_f32_16x16x32_bf16 v[92:95], v[148:151], v[116:119], v[92:95]\n\t"
    "s_waitcnt lgkmcnt(4)\n\t"
    "v_mfma_f32_16x16x32_bf16 v[80:83], v[152:155], v[120:123], v[80:83]\n\t"
    "ds_read_b128 v[112:115], %[ea] offset:12288\n\t"
    "s_add_u32 m0, %[lb], 12288\n\t"
    "v_mfma_f32_16x16x32_bf16 v[84:87], v[156:159], v[120:123], v[84:87]\n\t"
    "ds_read_b128 v[116:119], %[ea] offset:14336\n\t"
    "global_load_lds_dwordx4 %[av6], %[s0] sc1\n\t"
    "v_mfma_f32_16x16x32_bf16 v[88:91], v[152:155], v[124:127], v[88:91]\n\t"
    "v_mfma_f32_16x16x32_bf16 v[92:95], v[156:159], v[124:127], v[92:95]\n\t"
    "s_waitcnt lgkmcnt(4)\n\t"
    "v_mfma_f32_16x16x32_bf16 v[80:83], v[160:163], v[96:99], v[80:83]\n\t"
    "ds_read_b128 v[120:123], %[da] offset:12288\n\t"
    "s_add_u32 m0, %[lb], 16384\n\t"
    "v_mfma_f32_16x16x32_bf16 v[84:87], v[164:167], v[96:99], v[84:87]\n\t"
    "ds_read_b128 v[124:127], %[da] offset:14336\n\t"
    "global_load_lds_dwordx4 %[av0], %[s1] sc1\n\t"
    "v_mfma_f32_16x16x32_bf16 v[88:91], v[160:163], v[100:103], v[88:91]\n\t"
    "v_mfma_f32_16x16x32_bf16 v[92:95], v[164:167], v[100:103], v[92:95]\n\t"
    "s_waitcnt vmcnt(7)\n\t"
    "s_barrier\n\t"
    "s_waitcnt lgkmcnt(4)\n\t"
    "v_mfma_f32_16x16x32_bf16 v[80:83], v[168:171], v[104:107], v[80:83]\n\t"
    "ds_read_b128 v[96:99], %[ea] offset:16384\n\t"
    "s_add_u32 m0, %[lb], 20480\n\t"
    "v_mfma_f32_16x16x32_bf16 v[84:87], v[172:175], v[104:107], v[84:87]\n\t"
    "ds_read_b128 v[100:103], %[ea] offset:18432\n\t"
    "global_load_lds_dwordx4 %[av2], %[s1] sc1\n\t"
    "v_mfma_f32_16x16x32_bf16 v[88:91], v[168:171], v[108:111], v[88:91]\n\t"
    "v_mfma_f32_16x16x32_bf16 v[92:95], v[172:175], v[108:111], v[92:95]\n\t"
    "s_waitcnt lgkmcnt(4)\n\t"
    "v_mfma_f32_16x16x32_bf16 v[80:83], v[176:179], v[112:115], v[80:83]\n\t"
    "ds_read_b128 v[104:107], %[da] offset:16384\n\t"
    "s_add_u32 m0, %[lb], 24576\n\t"
    "v_mfma_f32_16x16x32_bf16 v[84:87], v[180:183], v[112:115], v[84:87]\n\t"
    "ds_read_b128 v[108:111], %[da] offset:18432\n\t"
    "global_load_lds_dwordx4 %[av4], %[s1] sc1\n\t"
    "v_mfma_f32_16x16x32_bf16 v[88:91], v[176:179], v[116:119], v[88:91]\n\t"
    "v_mfma_f32_16x16x32_bf16 v[92:95], v[180:183], v[116:119], v[92:95]\n\t"
    "s_waitcnt lgkmcnt(4)\n\t"
    "v_mfma_f32_16x16x32_bf16 v[80:83], v[184:187], v[120:123], v[80:83]\n\t"
    "ds_read_b128 v[112:115], %[ea] offset:20480\n\t"
    "s_add_u32 m0, %[lb], 28672\n\t"
    "v_mfma_f32_16x16x32_bf16 v[84:87], v[188:191], v[120:123], v[84:87]\n\t"
    "ds_read_b128 v[116:119], %[ea] offset:22528\n\t"
    "global_load_lds_dwordx4 %[av6], %[s1] sc1\n\t"
    "v_mfma_f32_16x16x32_bf16 v[88:91], v[184:187], v[124:127], v[88:91]\n\t"
    "v_mfma_f32_16x16x32_bf16 v[92:95], v[188:191], v[124:127], v[92:95]\n\t"
    "s_waitcnt lgkmcnt(4)\n\t"
    "v_mfma_f32_16x16x32_bf16 v[80:83], v[192:195], v[96:99], v[80:83]\n\t"
    "ds_read_b128 v[120:123], %[da] offset:20480\n\t"
    "v_mfma_f32_16x16x32_bf16 v[84:87], v[196:199], v[96:99], v[84:87]\n\t"
    "ds_read_b128 v[124:127], %[da] offset:22528\n\t"
    "v_mfma_f32_16x16x32_bf16 v[88:91], v[192:195], v[100:103], v[88:91]\n\t"
    "v_mfma_f32_16x16x32_bf16 v[92:95], v[196:199], v[100:103], v[92:95]\n\t"
    "s_waitcnt vmcnt(8)\n\t"
    "s_barrier\n\t"
    "s_waitcnt lgkmcnt(4)\n\t"
    "v_mfma_f32_16x16x32_bf16 v[80:83], v[200:203], v[104:107], v[80:83]\n\t"
    "ds_read_b128 v[96:99], %[ea] offset:24576\n\t"
    "v_mfma_f32_16x16x32_bf16 v[84:87], v[204:207], v[104:107], v[84:87]\n\t"
    "ds_read_b128 v[100:103], %[ea] offset:26624\n\t"
    "v_mfma_f32_16x16x32_bf16 v[88:91], v[200:203], v[108:111], v[88:91]\n\t"
    "v_mfma_f32_16x16x32_bf16 v[92:95], v[204:207], v[108:111], v[92:95]\n\t"
    "s_waitcnt lgkmcnt(4)\n\t"
    "v_mfma_f32_16x16x32_bf16 v[80:83], v[208:211], v[112:115], v[80:83]\n\t"
    "ds_read_b128 v[104:107], %[da] offset:24576\n\t"
    "v_mfma_f32_16x16x32_bf16 v[84:87], v[212:215], v[112:115], v[84:87]\n\t"
    "ds_read_b128 v[108:111], %[da] offset:26624\n\t"
    "v_mfma_f32_16x16x32_bf16 v[88:91], v[208:211], v[116:119], v[88:91]\n\t"
    "v_mfma_f32_16x16x32_bf16 v[92:95], v[212:215], v[116:119], v[92:95]\n\t"
    "s_waitcnt lgkmcnt(4)\n\t"
    "v_mfma_f32_16x16x32_bf16 v[80:83], v[216:219], v[120:123], v[80:83]\n\t"
    "ds_read_b128 v[112:115], %[ea] offset:28672\n\t"
    "v_mfma_f32_16x16x32_bf16 v[84:87], v[220:223], v[120:123], v[84:87]\n\t"
    "ds_read_b128 v[116:119], %[ea] offset:30720\n\t"
    "v_mfma_f32_16x16x32_bf16 v[88:91], v[216:219], v[124:127], v[88:91]\n\t"
    "v_mfma_f32_16x16x32_bf16 v[92:95], v[220:223], v[124:127], v[92:95]\n\t"
    "s_waitcnt lgkmcnt(4)\n\t"
    "v_mfma_f32_16x16x32_bf16 v[80:83], v[224:227], v[96:99], v[80:83]\n\t"
    "ds_read_b128 v[120:123], %[da] offset:28672\n\t"
    "v_mfma_f32_16x16x32_bf16 v[84:87], v[228:231], v[96:99], v[84:87]\n\t"
    "ds_read_b128 v[124:127], %[da] offset:30720\n\t"
    "v_mfma_f32_16x16x32_bf16 v[88:91], v[224:227], v[100:103], v[88:91]\n\t"
    "v_mfma_f32_16x16x32_bf16 v[92:95], v[228:231], v[100:103], v[92:95]\n\t"
    "s_waitcnt lgkmcnt(4)\n\t"
    "v_mfma_f32_16x16x32_bf16 v[80:83], v[232:235], v[104:107], v[80:83]\n\t"
    "v_mfma_f32_16x16x32_bf16 v[84:87], v[236:239], v[104:107], v[84:87]\n\t"
    "v_mfma_f32_16x16x32_bf16 v[88:91], v[232:235], v[108:111], v[88:91]\n\t"
    "v_mfma_f32_16x16x32_bf16 v[92:95], v[236:239], v[108:111], v[92:95]\n\t"
    "s_waitcnt lgkmcnt(2)\n\t"
    "v_mfma_f32_16x16x32_bf16 v[80:83], v[240:243], v[112:115], v[80:83]\n\t"
    "v_mfma_f32_16x16x32_bf16 v[84:87], v[244:247], v[112:115], v[84:87]\n\t"
    "v_mfma_f32_16x16x32_bf16 v[88:91], v[240:243], v[116:119], v[88:91]\n\t"
    "v_mfma_f32_16x16x32_bf16 v[92:95], v[244:247], v[116:119], v[92:95]\n\t"
    "s_waitcnt lgkmcnt(0)\n\t"
    "v_mfma_f32_16x16x32_bf16 v[80:83], v[248:251], v[120:123], v[80:83]\n\t"
    "v_mfma_f32_16x16x32_bf16 v[84:87], v[252:255], v[120:123], v[84:87]\n\t"
    "v_mfma_f32_16x16x32_bf16 v[88:91], v[248:251], v[124:127], v[88:91]\n\t"
    "v_mfma_f32_16x16x32_bf16 v[92:95], v[252:255], v[124:127], v[92:95]\n\t"
    "s_nop 6\n\t"
    "s_branch 9f\n\t"
    "3:\n\t"
    "9:"
    : "=&{v[80:95]}"(pd)
    : [ea] "v"(ea), [da] "v"(da), [x] "s"(x), "{v[128:159]}"(W[0]), "{v[160:191]}"(W[1]), "{v[192:223]}"(W[2]), "{v[224:255]}"(W[3]), [lb] "s"(lb), [s0] "s"(s0), [s1] "s"(s1), [av0] "v"(av[0]), [av1] "v"(av[1]), [av2] "v"(av[2]), [av3] "v"(av[3]), [av4] "v"(av[4]), [av5] "v"(av[5]), [av6] "v"(av[6]), [av7] "v"(av[7])
    : "v96", "v97", "v98", "v99", "v100", "v101", "v102", "v103", "v104", "v105", "v106", "v107", "v108", "v109", "v110", "v111", "v112", "v113", "v114", "v115", "v116", "v117", "v118", "v119", "v120", "v121", "v122", "v123", "v124", "v125", "v126", "v127", "vcc", "scc", "m0", "memory");
}
__device__ __forceinline__ void lean_st0(const u32x32 (&W)[4], u32x16& pd, unsigned ea, unsigned da, unsigned x, unsigned pv, unsigned cn, unsigned sl, unsigned dv, const char* db, const char* pg) {
  unsigned st;
  asm volatile("ds_read_b128 v[96:99], %[ea] offset:0\n\t"
    "ds_read_b128 v[100:103], %[ea] offset:2048\n\t"
    "ds_read_b128 v[104:107], %[da] offset:0\n\t"
    "ds_read_b128 v[108:111], %[da] offset:2048\n\t"
    "ds_read_b128 v[112:115], %[ea] offset:4096\n\t"
    "ds_read_b128 v[116:119], %[ea] offset:6144\n\t"
    "s_waitcnt lgkmcnt(4)\n\t"
    "v_mfma_f32_16x16x32_bf16 v[80:83], v[128:131], v[96:99], 0\n\t"
    "ds_read_b128 v[120:123], %[da] offset:4096\n\t"
    "v_mfma_f32_16x16x32_bf16 v[84:87], v[132:135], v[96:99], 0\n\t"
    "ds_read_b128 v[124:127], %[da] offset:6144\n\t"
    "v_mfma_f32_16x16x32_bf16 v[88:91], v[128:131], v[100:103], 0\n\t"
    "v_mfma_f32_16x16x32_bf16 v[92:95], v[132:135], v[100:103], 0\n\t"
    "s_barrier\n\t"
    "s_waitcnt lgkmcnt(4)\n\t"
    "v_mfma_f32_16x16x32_bf16 v[80:83], v[136:139], v[104:107], v[80:83]\n\t"
    "ds_read_b128 v[96:99], %[ea] offset:8192\n\t"
    "v_mfma_f32_16x16x32_bf16 v[84:87], v[140:143], v[104:107], v[84:87]\n\t"
    "ds_read_b128 v[100:103], %[ea] offset:10240\n\t"
    "v_mfma_f32_16x16x32_bf16 v[88:91], v[136:139], v[108:111], v[88:91]\n\t"
    "v_mfma_f32_16x16x32_bf16 v[92:95], v[140:143], v[108:111], v[92:95]\n\t"
    "s_waitcnt lgkmcnt(4)\n\t"
    "v_mfma_f32_16x16x32_bf16 v[80:83], v[144:147], v[112:115], v[80:83]\n\t"
    "ds_read_b128 v[104:107], %[da] offset:8192\n\t"
    "v_mfma_f32_16x16x32_bf16 v[84:87], v[148:151], v[112:115], v[84:87]\n\t"
    "ds_read_b128 v[108:111], %[da] offset:10240\n\t"
    "v_mfma_f32_16x16x32_bf16 v[88:91], v[144:147], v[116:119], v[88:91]\n\t"
    "v_mfma_f32_16x16x32_bf16 v[92:95], v[148:151], v[116:119], v[92:95]\n\t"
    "s_waitcnt lgkmcnt(4)\n\t"
    "v_mfma_f32_16x16x32_bf16 v[80:83], v[152:155], v[120:123], v[80:83]\n\t"
    "ds_read_b128 v[112:115], %[ea] offset:12288\n\t"
    "v_mfma_f32_16x16x32_bf16 v[84:87], v[156:159], v[120:123], v[84:87]\n\t"
    "ds_read_b128 v[116:119], %[ea] offset:14336\n\t"
    "v_mfma_f32_16x16x32_bf16 v[88:91], v[152:155], v[124:127], v[88:91]\n\t"
    "v_mfma_f32_16x16x32_bf16 v[92:95], v[156:159], v[124:127], v[92:95]\n\t"
    "s_bitcmp1_b32 %[x], 2\n\t"
    "s_cbranch_scc1 7f\n\t"
    "8:\n\t"
    "s_waitcnt lgkmcnt(4)\n\t"
    "v_mfma_f32_16x16x32_bf16 v[80:83], v[160:163], v[96:99], v[80:83]\n\t"
    "ds_read_b128 v[120:123], %[da] offset:12288\n\t"
    "v_mfma_f32_16x16x32_bf16 v[84:87], v[164:167], v[96:99], v[84:87]\n\t"
    "ds_read_b128 v[124:127], %[da] offset:14336\n\t"
    "v_mfma_f32_16x16x32_bf16 v[88:91], v[160:163], v[100:103], v[88:91]\n\t"
    "v_mfma_f32_16x16x32_bf16 v[92:95], v[164:167], v[100:103], v[92:95]\n\t"
    "s_barrier\n\t"
    "s_waitcnt lgkmcnt(4)\n\t"
    "v_mfma_f32_16x16x32_bf16 v[80:83], v[168:171], v[104:107], v[80:83]\n\t"
    "ds_read_b128 v[96:99], %[ea] offset:16384\n\t"
    "v_mfma_f32_16x16x32_bf16 v[84:87], v[172:175], v[104:107], v[84:87]\n\t"
    "ds_read_b128 v[100:103], %[ea] offset:18432\n\t"
    "v_mfma_f32_16x16x32_bf16 v[88:91], v[168:171], v[108:111], v[88:91]\n\t"
    "v_mfma_f32_16x16x32_bf16 v[92:95], v[172:175], v[108:111], v[92:95]\n\t"
    "s_waitcnt lgkmcnt(4)\n\t"
    "v_mfma_f32_16x16x32_bf16 v[80:83], v[176:179], v[112:115], v[80:83]\n\t"
    "ds_read_b128 v[104:107], %[da] offset:16384\n\t"
    "v_mfma_f32_16x16x32_bf16 v[84:87], v[180:183], v[112:115], v[84:87]\n\t"
    "ds_read_b128 v[108:111], %[da] offset:18432\n\t"
    "v_mfma_f32_16x16x32_bf16 v[88:91], v[176:179], v[116:119], v[88:91]\n\t"
    "v_mfma_f32_16x16x32_bf16 v[92:95], v[180:183], v[116:119], v[92:95]\n\t"
    "s_waitcnt lgkmcnt(4)\n\t"
    "v_mfma_f32_16x16x32_bf16 v[80:83], v[184:187], v[120:123], v[80:83]\n\t"
    "ds_read_b128 v[112:115], %[ea] offset:20480\n\t"
    "v_mfma_f32_16x16x32_bf16 v[84:87], v[188:191], v[120:123], v[84:87]\n\t"
    "ds_read_b128 v[116:119], %[ea] offset:22528\n\t"
    "v_mfma_f32_16x16x32_bf16 v[88:91], v[184:187], v[124:127], v[88:91]\n\t"
    "v_mfma_f32_16x16x32_bf16 v[92:95], v[188:191], v[124:127], v[92:95]\n\t"
    "s_waitcnt lgkmcnt(4)\n\t"
    "v_mfma_f32_16x16x32_bf16 v[80:83], v[192:195], v[96:99], v[80:83]\n\t"
    "ds_read_b128 v[120:123], %[da] offset:20480\n\t"
    "v_mfma_f32_16x16x32_bf16 v[84:87], v[196:199], v[96:99], v[84:87]\n\t"
    "ds_read_b128 v[124:127], %[da] offset:22528\n\t"
    "v_mfma_f32_16x16x32_bf16 v[88:91], v[192:195], v[100:103], v[88:91]\n\t"
    "v_mfma_f32_16x16x32_bf16 v[92:95], v[196:199], v[100:103], v[92:95]\n\t"
    "s_barrier\n\t"
    "s_and_b32 %[st], %[x], 3\n\t"
    "s_cbranch_scc1 3f\n\t"
    "4:\n\t"
    "s_waitcnt lgkmcnt(4)\n\t"
    "v_mfma_f32_16x16x32_bf16 v[80:83], v[200:203], v[104:107], v[80:83]\n\t"
    "ds_read_b128 v[96:99], %[ea] offset:24576\n\t"
    "v_mfma_f32_16x16x32_bf16 v[84:87], v[204:207], v[104:107], v[84:87]\n\t"
    "ds_read_b128 v[100:103], %[ea] offset:26624\n\t"
    "v_mfma_f32_16x16x32_bf16 v[88:91], v[200:203], v[108:111], v[88:91]\n\t"
    "v_mfma_f32_16x16x32_bf16 v[92:95], v[204:207], v[108:111], v[92:95]\n\t"
    "s_waitcnt lgkmcnt(4)\n\t"
    "v_mfma_f32_16x16x32_bf16 v[80:83], v[208:211], v[112:115], v[80:83]\n\t"
    "ds_read_b128 v[104:107], %[da] offset:24576\n\t"
    "v_mfma_f32_16x16x32_bf16 v[84:87], v[212:215], v[112:115], v[84:87]\n\t"
    "ds_read_b128 v[108:111], %[da] offset:26624\n\t"
    "v_mfma_f32_16x16x32_bf16 v[88:91], v[208:211], v[116:119], v[88:91]\n\t"
    "v_mfma_f32_16x16x32_bf16 v[92:95], v[212:215], v[116:119], v[92:95]\n\t"
    "s_waitcnt lgkmcnt(4)\n\t"
    "v_mfma_f32_16x16x32_bf16 v[80:83], v[216:219], v[120:123], v[80:83]\n\t"
    "ds_read_b128 v[112:115], %[ea] offset:28672\n\t"
    "v_mfma_f32_16x16x32_bf16 v[84:87], v[220:223], v[120:123], v[84:87]\n\t"
    "ds_read_b128 v[116:119], %[ea] offset:30720\n\t"
    "v_mfma_f32_16x16x32_bf16 v[88:91], v[216:219], v[124:127], v[88:91]\n\t"
    "v_mfma_f32_16x16x32_bf16 v[92:95], v[220:223], v[124:127], v[92:95]\n\t"
    "s_waitcnt lgkmcnt(4)\n\t"
    "v_mfma_f32_16x16x32_bf16 v[80:83], v[224:227], v[96:99], v[80:83]\n\t"
    "ds_read_b128 v[120:123], %[da] offset:28672\n\t"
    "v_mfma_f32_16x16x32_bf16 v[84:87], v[228:231], v[96:99], v[84:87]\n\t"
    "ds_read_b128 v[124:127], %[da] offset:30720\n\t"
    "v_mfma_f32_16x16x32_bf16 v[88:91], v[224:227], v[100:103], v[88:91]\n\t"
    "v_mfma_f32_16x16x32_bf16 v[92:95], v[228:231], v[100:103], v[92:95]\n\t"
    "s_waitcnt lgkmcnt(4)\n\t"
    "v_mfma_f32_16x16x32_bf16 v[80:83], v[232:235], v[104:107], v[80:83]\n\t"
    "v_mfma_f32_16x16x32_bf16 v[84:87], v[236:239], v[104:107], v[84:87]\n\t"
    "v_mfma_f32_16x16x32_bf16 v[88:91], v[232:235], v[108:111], v[88:91]\n\t"
    "v_mfma_f32_16x16x32_bf16 v[92:95], v[236:239], v[108:111], v[92:95]\n\t"
    "s_waitcnt lgkmcnt(2)\n\t"
    "v_mfma_f32_16x16x32_bf16 v[80:83], v[240:243], v[112:115], v[80:83]\n\t"
    "v_mfma_f32_16x16x32_bf16 v[84:87], v[244:247], v[112:115], v[84:87]\n\t"
    "v_mfma_f32_16x16x32_bf16 v[88:91], v[240:243], v[116:119], v[88:91]\n\t"
    "v_mfma_f32_16x16x32_bf16 v[92:95], v[244:247], v[116:119], v[92:95]\n\t"
    "s_waitcnt lgkmcnt(0)\n\t"
    "v_mfma_f32_16x16x32_bf16 v[80:83], v[248:251], v[120:123], v[80:83]\n\t"
    "v_mfma_f32_16x16x32_bf16 v[84:87], v[252:255], v[120:123], v[84:87]\n\t"
    "v_mfma_f32_16x16x32_bf16 v[88:91], v[248:251], v[124:127], v[88:91]\n\t"
    "v_mfma_f32_16x16x32_bf16 v[92:95], v[252:255], v[124:127], v[92:95]\n\t"
    "s_nop 6\n\t"
    "s_branch 9f\n\t"
    "3:\n\t"
    "s_bitcmp0_b32 %[x], 0\n\t"
    "s_cbranch_scc1 5f\n\t"
    "7:\n\t"
    "v_mov_b32 v76, %[cn]\n\t"
    "v_mov_b32 v77, 1\n\t"
    "v_mov_b32 v78, %[pv]\n\t"
    "v_mov_b32 v79, 0\n\t"
    "s_waitcnt vmcnt(0)\n\t"
    "s_mov_b64 exec, 1\n\t"
    "ds_add_rtn_u32 v77, v76, v77\n\t"
    "s_waitcnt lgkmcnt(0)\n\t"
    "v_readfirstlane_b32 %[st], v77\n\t"
    "s_and_b32 %[st], %[st], 3\n\t"
    "s_cmp_lg_u32 %[st], 3\n\t"
    "s_cbranch_scc1 6f\n\t"
    "global_store_dword v79, v78, %[pg] sc1\n\t"
    "6:\n\t"
    "s_mov_b64 exec, -1\n\t"
    "s_bitcmp1_b32 %[x], 2\n\t"
    "s_cbranch_scc1 8b\n\t"
    "5:\n\t"
    "s_bitcmp0_b32 %[x], 1\n\t"
    "s_cbranch_scc1 4b\n\t"
    "s_mov_b32 m0, %[sl]\n\t"
    "s_nop 0\n\t"
    "global_load_lds_dword %[dv], %[db] sc1\n\t"
    "s_branch 4b\n\t"
    "9:"
    : "=&{v[80:95]}"(pd), [st] "=&s"(st)
    : [ea] "v"(ea), [da] "v"(da), [x] "s"(x), "{v[128:159]}"(W[0]), "{v[160:191]}"(W[1]), "{v[192:223]}"(W[2]), "{v[224:255]}"(W[3]), [pv] "s"(pv), [cn] "s"(cn), [sl] "s"(sl), [dv] "v"(dv), [db] "s"(db), [pg] "s"(pg)
    : "v96", "v97", "v98", "v99", "v100", "v101", "v102", "v103", "v104", "v105", "v106", "v107", "v108", "v109", "v110", "v111", "v112", "v113", "v114", "v115", "v116", "v117", "v118", "v119", "v120", "v121", "v122", "v123", "v124", "v125", "v126", "v127", "v76", "v77", "v78", "v79", "vcc", "scc", "m0", "memory");
}
#define LEAN_QB 1
__device__ __forceinline__ void fin_ld(const u32x16& pd, unsigned rw, unsigned fa, unsigned fv) {
  asm volatile("ds_write_b128 %[rw], v[80:83] offset:0\n\t"
    "ds_write_b128 %[rw], v[84:87] offset:1024\n\t"
    "ds_write_b128 %[rw], v[88:91] offset:2048\n\t"
    "ds_write_b128 %[rw], v[92:95] offset:3072\n\t"
    "ds_write_b32 %[fa], %[fv]"
    :
    : [rw] "v"(rw), "{v[80:95]}"(pd), [fa] "v"(fa), [fv] "v"(fv)
    : "memory");
}
__device__ __forceinline__ void fin_st0(const u32x16& pd, unsigned rw, unsigned fa, unsigned fv, const char* cb, unsigned co0, unsigned co1) {
  asm volatile("1:\n\t"
    "ds_read_b32 v39, %[fa]\n\t"
    "ds_read_b128 v[48:51], %[rw] offset:0\n\t"
    "ds_read_b128 v[52:55], %[rw] offset:1024\n\t"
    "ds_read_b128 v[56:59], %[rw] offset:2048\n\t"
    "ds_read_b128 v[60:63], %[rw] offset:3072\n\t"
    "s_waitcnt lgkmcnt(2)\n\t"
    "v_cmp_ne_u32 vcc, v39, %[fv]\n\t"
    "s_cbranch_vccz 2f\n\t"
    "s_waitcnt lgkmcnt(0)\n\t"
    "s_branch 1b\n\t"
    "2:\n\t"
    "v_add_f32 v48, v48, v80\n\t"
    "v_add_f32 v49, v49, v81\n\t"
    "v_add_f32 v50, v50, v82\n\t"
    "v_add_f32 v51, v51, v83\n\t"
    "v_add_f32 v52, v52, v84\n\t"
    "v_add_f32 v53, v53, v85\n\t"
    "v_add_f32 v54, v54, v86\n\t"
    "v_add_f32 v55, v55, v87\n\t"
    "v_cvt_pk_bf16_f32 v40, v48, v49\n\t"
    "v_cvt_pk_bf16_f32 v41, v50, v51\n\t"
    "v_cvt_pk_bf16_f32 v42, v52, v53\n\t"
    "v_cvt_pk_bf16_f32 v43, v54, v55\n\t"
    "global_store_dwordx4 %[co0], v[40:43], %[cb] sc1\n\t"
    "s_waitcnt lgkmcnt(0)\n\t"
    "v_add_f32 v56, v56, v88\n\t"
    "v_add_f32 v57, v57, v89\n\t"
    "v_add_f32 v58, v58, v90\n\t"
    "v_add_f32 v59, v59, v91\n\t"
    "v_add_f32 v60, v60, v92\n\t"
    "v_add_f32 v61, v61, v93\n\t"
    "v_add_f32 v62, v62, v94\n\t"
    "v_add_f32 v63, v63, v95\n\t"
    "v_cvt_pk_bf16_f32 v44, v56, v57\n\t"
    "v_cvt_pk_bf16_f32 v45, v58, v59\n\t"
    "v_cvt_pk_bf16_f32 v46, v60, v61\n\t"
    "v_cvt_pk_bf16_f32 v47, v62, v63\n\t"
    "global_store_dwordx4 %[co1], v[44:47], %[cb] sc1\n\t"
    "s_nop 1"
    :
    : [rw] "v"(rw), "{v[80:95]}"(pd), [fa] "v"(fa), [fv] "v"(fv), [cb] "s"(cb), [co0] "v"(co0), [co1] "v"(co1)
    : "v39", "v40", "v41", "v42", "v43", "v44", "v45", "v46", "v47", "v48", "v49", "v50", "v51", "v52", "v53", "v54", "v55", "v56", "v57", "v58", "v59", "v60", "v61", "v62", "v63", "vcc", "memory");
}
__device__ __forceinline__ void fin_st1(const u32x16& pd, unsigned rw, unsigned fa, unsigned fv, const char* cb, unsigned co0, unsigned co1) {
  asm volatile("1:\n\t"
    "ds_read_b32 v39, %[fa]\n\t"
    "ds_read_b128 v[48:51], %[rw] offset:0\n\t"
    "ds_read_b128 v[52:55], %[rw] offset:1024\n\t"
    "ds_read_b128 v[56:59], %[rw] offset:2048\n\t"
    "ds_read_b128 v[60:63], %[rw] offset:3072\n\t"
    "s_waitcnt lgkmcnt(2)\n\t"
    "v_cmp_ne_u32 vcc, v39, %[fv]\n\t"
    "s_cbranch_vccz 2f\n\t"
    "s_waitcnt lgkmcnt(0)\n\t"
    "s_branch 1b\n\t"
    "2:\n\t"
    "v_add_f32 v48, v48, v80\n\t"
    "v_add_f32 v49, v49, v81\n\t"
    "v_add_f32 v50, v50, v82\n\t"
    "v_add_f32 v51, v51, v83\n\t"
    "v_add_f32 v52, v52, v84\n\t"
    "v_add_f32 v53, v53, v85\n\t"
    "v_add_f32 v54, v54, v86\n\t"
    "v_add_f32 v55, v55, v87\n\t"
    "s_waitcnt lgkmcnt(0)\n\t"
    "v_add_f32 v56, v56, v88\n\t"
    "v_add_f32 v57, v57, v89\n\t"
    "v_add_f32 v58, v58, v90\n\t"
    "v_add_f32 v59, v59, v91\n\t"
    "v_add_f32 v60, v60, v92\n\t"
    "v_add_f32 v61, v61, v93\n\t"
    "v_add_f32 v62, v62, v94\n\t"
    "v_add_f32 v63, v63, v95\n\t"
    "v_mul_f32 v40, %[nl], v48\n\t"
    "v_mul_f32 v41, %[nl], v49\n\t"
    "v_mul_f32 v42, %[nl], v50\n\t"
    "v_mul_f32 v43, %[nl], v51\n\t"
    "v_mul_f32 v44, %[nl], v56\n\t"
    "v_mul_f32 v45, %[nl], v57\n\t"
    "v_mul_f32 v46, %[nl], v58\n\t"
    "v_mul_f32 v47, %[nl], v59\n\t"
    "v_exp_f32 v40, v40\n\t"
    "v_exp_f32 v41, v41\n\t"
    "v_exp_f32 v42, v42\n\t"
    "v_exp_f32 v43, v43\n\t"
    "v_exp_f32 v44, v44\n\t"
    "v_exp_f32 v45, v45\n\t"
    "v_exp_f32 v46, v46\n\t"
    "v_exp_f32 v47, v47\n\t"
    "v_add_f32 v40, 1.0, v40\n\t"
    "v_add_f32 v41, 1.0, v41\n\t"
    "v_add_f32 v42, 1.0, v42\n\t"
    "v_add_f32 v43, 1.0, v43\n\t"
    "v_add_f32 v44, 1.0, v44\n\t"
    "v_add_f32 v45, 1.0, v45\n\t"
    "v_add_f32 v46, 1.0, v46\n\t"
    "v_add_f32 v47, 1.0, v47\n\t"
    "v_rcp_f32 v40, v40\n\t"
    "v_rcp_f32 v41, v41\n\t"
    "v_rcp_f32 v42, v42\n\t"
    "v_rcp_f32 v43, v43\n\t"
    "v_rcp_f32 v44, v44\n\t"
    "v_rcp_f32 v45, v45\n\t"
    "v_rcp_f32 v46, v46\n\t"
    "v_rcp_f32 v47, v47\n\t"
    "v_mul_f32 v40, v48, v40\n\t"
    "v_mul_f32 v41, v49, v41\n\t"
    "v_mul_f32 v42, v50, v42\n\t"
    "v_mul_f32 v43, v51, v43\n\t"
    "v_mul_f32 v44, v56, v44\n\t"
    "v_mul_f32 v45, v57, v45\n\t"
    "v_mul_f32 v46, v58, v46\n\t"
    "v_mul_f32 v47, v59, v47\n\t"
    "v_mul_f32 v40, v40, v52\n\t"
    "v_mul_f32 v41, v41, v53\n\t"
    "v_mul_f32 v42, v42, v54\n\t"
    "v_mul_f32 v43, v43, v55\n\t"
    "v_mul_f32 v44, v44, v60\n\t"
    "v_mul_f32 v45, v45, v61\n\t"
    "v_mul_f32 v46, v46, v62\n\t"
    "v_mul_f32 v47, v47, v63\n\t"
    "v_cvt_pk_bf16_f32 v48, v40, v41\n\t"
    "v_cvt_pk_bf16_f32 v49, v42, v43\n\t"
    "v_cvt_pk_bf16_f32 v50, v44, v45\n\t"
    "v_cvt_pk_bf16_f32 v51, v46, v47\n\t"
    "s_nop 1\n\t"
    "v_permlane16_swap_b32 v48, v50\n\t"
    "v_permlane16_swap_b32 v49, v51\n\t"
    "global_store_dwordx4 %[co0], v[48:51], %[cb] sc1\n\t"
    "s_nop 1"
    :
    : [rw] "v"(rw), "{v[80:95]}"(pd), [fa] "v"(fa), [fv] "v"(fv), [cb] "s"(cb), [co0] "v"(co0), [nl] "s"(-LOG2E)
    : "v39", "v40", "v41", "v42", "v43", "v44", "v45", "v46", "v47", "v48", "v49", "v50", "v51", "v52", "v53", "v54", "v55", "v56", "v57", "v58", "v59", "v60", "v61", "v62", "v63", "vcc", "memory");
}
__device__ __forceinline__ void fin_st2(const u32x16& pd, unsigned rw, unsigned fa, unsigned fv, const char* cb, unsigned co0, unsigned co1) {
  asm volatile("s_nop 1\n\t"
    "v_cvt_pk_bf16_f32 v40, v80, v81\n\t"
    "v_cvt_pk_bf16_f32 v41, v82, v83\n\t"
    "v_cvt_pk_bf16_f32 v42, v84, v85\n\t"
    "v_cvt_pk_bf16_f32 v43, v86, v87\n\t"
    "global_store_dwordx4 %[co0], v[40:43], %[cb] sc1\n\t"
    "v_cvt_pk_bf16_f32 v44, v88, v89\n\t"
    "v_cvt_pk_bf16_f32 v45, v90, v91\n\t"
    "v_cvt_pk_bf16_f32 v46, v92, v93\n\t"
    "v_cvt_pk_bf16_f32 v47, v94, v95\n\t"
    "global_store_dwordx4 %[co1], v[44:47], %[cb] sc1\n\t"
    "1:\n\t"
    "ds_read_b32 v39, %[fa]\n\t"
    "ds_read_b128 v[48:51], %[rw] offset:0\n\t"
    "ds_read_b128 v[52:55], %[rw] offset:1024\n\t"
    "ds_read_b128 v[56:59], %[rw] offset:2048\n\t"
    "ds_read_b128 v[60:63], %[rw] offset:3072\n\t"
    "s_waitcnt lgkmcnt(2)\n\t"
    "v_cmp_ne_u32 vcc, v39, %[fv]\n\t"
    "s_cbranch_vccz 2f\n\t"
    "s_waitcnt lgkmcnt(0)\n\t"
    "s_branch 1b\n\t"
    "2:\n\t"
    "v_cvt_pk_bf16_f32 v48, v48, v49\n\t"
    "v_cvt_pk_bf16_f32 v49, v50, v51\n\t"
    "v_cvt_pk_bf16_f32 v50, v52, v53\n\t"
    "v_cvt_pk_bf16_f32 v51, v54, v55\n\t"
    "global_store_dwordx4 %[co0], v[48:51], %[cb] offset:2048 sc1\n\t"
    "s_waitcnt lgkmcnt(0)\n\t"
    "v_cvt_pk_bf16_f32 v56, v56, v57\n\t"
    "v_cvt_pk_bf16_f32 v57, v58, v59\n\t"
    "v_cvt_pk_bf16_f32 v58, v60, v61\n\t"
    "v_cvt_pk_bf16_f32 v59, v62, v63\n\t"
    "global_store_dwordx4 %[co1], v[56:59], %[cb] offset:2048 sc1\n\t"
    "s_nop 1"
    :
    : [rw] "v"(rw), "{v[80:95]}"(pd), [fa] "v"(fa), [fv] "v"(fv), [cb] "s"(cb), [co0] "v"(co0), [co1] "v"(co1)
    : "v39", "v40", "v41", "v42", "v43", "v44", "v45", "v46", "v47", "v48", "v49", "v50", "v51", "v52", "v53", "v54", "v55", "v56", "v57", "v58", "v59", "v60", "v61", "v62", "v63", "vcc", "memory");
}
template <int R> __device__ __forceinline__ void gw_load(u32x32& w, unsigned a0, unsigned a1);
template <> __device__ __forceinline__ void gw_load<0>(u32x32& w, unsigned a0, unsigned a1) {
  asm volatile("ds_read_b64_tr_b16 v[128:129], %1 offset:0\n\t"
    "ds_read_b64_tr_b16 v[130:131], %1 offset:512\n\t"
    "ds_read_b64_tr_b16 v[132:133], %2 offset:0\n\t"
    "ds_read_b64_tr_b16 v[134:135], %2 offset:512\n\t"
    "ds_read_b64_tr_b16 v[136:137], %1 offset:4096\n\t"
    "ds_read_b64_tr_b16 v[138:139], %1 offset:4608\n\t"
    "ds_read_b64_tr_b16 v[140:141], %2 offset:4096\n\t"
    "ds_read_b64_tr_b16 v[142:143], %2 offset:4608\n\t"
    "s_waitcnt lgkmcnt(0)\n\t"
    "ds_read_b64_tr_b16 v[144:145], %1 offset:16384\n\t"
    "ds_read_b64_tr_b16 v[146:147], %1 offset:16896\n\t"
    "ds_read_b64_tr_b16 v[148:149], %2 offset:16384\n\t"
    "ds_read_b64_tr_b16 v[150:151], %2 offset:16896\n\t"
    "ds_read_b64_tr_b16 v[152:153], %1 offset:20480\n\t"
    "ds_read_b64_tr_b16 v[154:155], %1 offset:20992\n\t"
    "ds_read_b64_tr_b16 v[156:157], %2 offset:20480\n\t"
    "ds_read_b64_tr_b16 v[158:159], %2 offset:20992\n\t"
    "s_waitcnt lgkmcnt(0)"
    : "=&{v[128:159]}"(w) : "v"(a0), "v"(a1) : "memory");
}
template <> __device__ __forceinline__ void gw_load<1>(u32x32& w, unsigned a0, unsigned a1) {
  asm volatile("ds_read_b64_tr_b16 v[160:161], %1 offset:0\n\t"
    "ds_read_b64_tr_b16 v[162:163], %1 offset:512\n\t"
    "ds_read_b64_tr_b16 v[164:165], %2 offset:0\n\t"
    "ds_read_b64_tr_b16 v[166:167], %2 offset:512\n\t"
    "ds_read_b64_tr_b16 v[168:169], %1 offset:4096\n\t"
    "ds_read_b64_tr_b16 v[170:171], %1 offset:4608\n\t"
    "ds_read_b64_tr_b16 v[172:173], %2 offset:4096\n\t"
    "ds_read_b64_tr_b16 v[174:175], %2 offset:4608\n\t"
    "s_waitcnt lgkmcnt(0)\n\t"
    "ds_read_b64_tr_b16 v[176:177], %1 offset:16384\n\t"
    "ds_read_b64_tr_b16 v[178:179], %1 offset:16896\n\t"
    "ds_read_b64_tr_b16 v[180:181], %2 offset:16384\n\t"
    "ds_read_b64_tr_b16 v[182:183], %2 offset:16896\n\t"
    "ds_read_b64_tr_b16 v[184:185], %1 offset:20480\n\t"
    "ds_read_b64_tr_b16 v[186:187], %1 offset:20992\n\t"
    "ds_read_b64_tr_b16 v[188:189], %2 offset:20480\n\t"
    "ds_read_b64_tr_b16 v[190:191], %2 offset:20992\n\t"
    "s_waitcnt lgkmcnt(0)"
    : "=&{v[160:191]}"(w) : "v"(a0), "v"(a1) : "memory");
}
template <> __device__ __forceinline__ void gw_load<2>(u32x32& w, unsigned a0, unsigned a1) {
  asm volatile("ds_read_b64_tr_b16 v[192:193], %1 offset:0\n\t"
    "ds_read_b64_tr_b16 v[194:195], %1 offset:512\n\t"
    "ds_read_b64_tr_b16 v[196:197], %2 offset:0\n\t"
    "ds_read_b64_tr_b16 v[198:199], %2 offset:512\n\t"
    "ds_read_b64_tr_b16 v[200:201], %1 offset:4096\n\t"
    "ds_read_b64_tr_b16 v[202:203], %1 offset:4608\n\t"
    "ds_read_b64_tr_b16 v[204:205], %2 offset:4096\n\t"
    "ds_read_b64_tr_b16 v[206:207], %2 offset:4608\n\t"
    "s_waitcnt lgkmcnt(0)\n\t"
    "ds_read_b64_tr_b16 v[208:209], %1 offset:16384\n\t"
    "ds_read_b64_tr_b16 v[210:211], %1 offset:16896\n\t"
    "ds_read_b64_tr_b16 v[212:213], %2 offset:16384\n\t"
    "ds_read_b64_tr_b16 v[214:215], %2 offset:16896\n\t"
    "ds_read_b64_tr_b16 v[216:217], %1 offset:20480\n\t"
    "ds_read_b64_tr_b16 v[218:219], %1 offset:20992\n\t"
    "ds_read_b64_tr_b16 v[220:221], %2 offset:20480\n\t"
    "ds_read_b64_tr_b16 v[222:223], %2 offset:20992\n\t"
    "s_waitcnt lgkmcnt(0)"
    : "=&{v[192:223]}"(w) : "v"(a0), "v"(a1) : "memory");
}
template <> __device__ __forceinline__ void gw_load<3>(u32x32& w, unsigned a0, unsigned a1) {
  asm volatile("ds_read_b64_tr_b16 v[224:225], %1 offset:0\n\t"
    "ds_read_b64_tr_b16 v[226:227], %1 offset:512\n\t"
    "ds_read_b64_tr_b16 v[228:229], %2 offset:0\n\t"
    "ds_read_b64_tr_b16 v[230:231], %2 offset:512\n\t"
    "ds_read_b64_tr_b16 v[232:233], %1 offset:4096\n\t"
    "ds_read_b64_tr_b16 v[234:235], %1 offset:4608\n\t"
    "ds_read_b64_tr_b16 v[236:237], %2 offset:4096\n\t"
    "ds_read_b64_tr_b16 v[238:239], %2 offset:4608\n\t"
    "s_waitcnt lgkmcnt(0)\n\t"
    "ds_read_b64_tr_b16 v[240:241], %1 offset:16384\n\t"
    "ds_read_b64_tr_b16 v[242:243], %1 offset:16896\n\t"
    "ds_read_b64_tr_b16 v[244:245], %2 offset:16384\n\t"
    "ds_read_b64_tr_b16 v[246:247], %2 offset:16896\n\t"
    "ds_read_b64_tr_b16 v[248:249], %1 offset:20480\n\t"
    "ds_read_b64_tr_b16 v[250:251], %1 offset:20992\n\t"
    "ds_read_b64_tr_b16 v[252:253], %2 offset:20480\n\t"
    "ds_read_b64_tr_b16 v[254:255], %2 offset:20992\n\t"
    "s_waitcnt lgkmcnt(0)"
    : "=&{v[224:255]}"(w) : "v"(a0), "v"(a1) : "memory");
}
template <int I, int E, class F> __device__ __forceinline__ void sfor(F&& f) { if constexpr (I < E) { f(std::integral_constant<int, I>{}); sfor<I + 1, E>(f); } }
#define WALL 1
template <int ROLE, int KQ, class F>
__device__ __forceinline__ void wload(char* smem, int wave_id_, const u16* W0, const u16* W1, int ldb, u32x32 (&bw)[4], int rot, F&& hook) {
  constexpr int ABUF = 65536;
  const int tid = opaque_tid(), lane = tid & 63, wave = WAVE_ID;
  const int kq = KQ == 2 ? wave >> 2 : 0, nq = KQ == 2 ? wave & 3 : wave;
  const int g = lane >> 4, r16 = lane & 15, q4 = r16 >> 2, p4 = lane & 3;
  const unsigned ldsb = __builtin_amdgcn_readfirstlane((unsigned)(size_t)(LAS char*)smem);
  {
    const int srow = 8 * wave + (lane >> 3), ch = (lane & 7) ^ bswz(srow);
    const unsigned voff = (unsigned)(srow * ldb + ch * 8) * 2;
    const char* w0 = uni((const char*)W0); const char* w1 = uni((const char*)(ROLE == R_F1 ? W1 : W0 + 64));
    const size_t kb = (size_t)64 * ldb * 2;
    auto wsrc = [&](int r, int i) {
      const int kbi = KQ == 2 ? (ROLE == R_PART2 ? 0 : (i >> 2) * 8) + ((2 * r + ((i >> 1) & 1)) ^ rot) : 2 * r + (i >> 2);
      return KQ == 2 ? ((i & 1) ? w1 : w0) + kbi * kb + (ROLE == R_PART2 ? (i >> 2) * 256 : 0) : w0 + kbi * kb + (i & 3) * 128;
    };
    auto wstage = [&](int r) {
      const unsigned ld = ldsb + (r & 1) * ABUF + wave * 1024;
#pragma unroll
      for (int i = 0; i < 8; ++i) {
        unsigned keep;
        asm volatile("s_mov_b32 %0, m0\n\ts_mov_b32 m0, %3\n\ts_nop 0\n\tglobal_load_lds_dwordx4 %1, %2" WNT_ "\n\ts_mov_b32 m0, %0"
                     : "=&s"(keep) : "v"(voff), "s"(wsrc(r, i)), "s"(ld + i * 8192) : "memory");
      }
    };
    const int brow = 8 * g + q4, bs = bswz(brow);
    unsigned la[2];
#pragma unroll
    for (int j = 0; j < 2; ++j) {
      int blk, c16, sub;
      if (ROLE == R_F1) { blk = j; c16 = nq * 2 + (p4 >> 1); sub = (p4 & 1) * 8; }
      else { blk = nq >> 1; c16 = (nq & 1) * 4 + p4; sub = j * 8; }
      int set = kq;
      if (ROLE == R_PART2 && TLP) { set = nq >> 1; blk = nq & 1; c16 = kq * 4 + p4; }
      la[j] = (unsigned)((KQ == 2 ? set * 4 + blk : blk) * 8192 + brow * 128 + ((c16 ^ bs) << 4) + sub);
    }
    wstage(0); wstage(1);
    u32x4 raw[2][8];
#pragma unroll
    for (int r = 0; r < 2; ++r)
#pragma unroll
      for (int i = 0; i < 8; ++i) asm volatile("global_load_dwordx4 %0, %1, %2" WNT_ : "=&v"(raw[r][i]) : "v"(voff), "s"(wsrc(r + 2, i)) : "memory");
    sfor<0, 4>([&](auto r_c) __attribute__((always_inline)) {
      constexpr int r = decltype(r_c)::value;
      if (r == 0) WAIT_V(24); else if (r == 1) WAIT_V(16);
      if (r == 3) BAR(); else if (r < 2) { __builtin_amdgcn_s_barrier(); asm volatile("" ::: "memory"); }
      gw_load<r>(bw[r], ldsb + (r & 1) * ABUF + la[0], ldsb + (r & 1) * ABUF + la[1]);
      if (r == 1 || r == 2) {
        u32x4 (&q)[8] = raw[(r + 1) & 1];
        if (r == 1) asm volatile("s_waitcnt vmcnt(8)" : "+v"(q[0]), "+v"(q[1]), "+v"(q[2]), "+v"(q[3]), "+v"(q[4]), "+v"(q[5]), "+v"(q[6]), "+v"(q[7]) :: "memory");
        else asm volatile("s_waitcnt vmcnt(0)" : "+v"(q[0]), "+v"(q[1]), "+v"(q[2]), "+v"(q[3]), "+v"(q[4]), "+v"(q[5]), "+v"(q[6]), "+v"(q[7]) :: "memory");
#pragma unroll
        for (int i = 0; i < 8; ++i) *(u32x4*)(smem + ((r + 1) & 1) * ABUF + wave * 1024 + i * 8192 + lane * 16) = q[i];
        if (r == 1) BAR();
      }
      if (r == 3) { hook(); BAR(); }
    });
  }
}
#define DSR2(d0, d1, a0_, a1_, off) asm volatile("ds_read_b128 %0, %2 offset:%4\n\tds_read_b128 %1, %3 offset:%4" : "=&v"(d0), "=&v"(d1) : "v"(a0_), "v"(a1_), "i"(off))
template <int N> __device__ __forceinline__ void waitl(u32x4& a, u32x4& b) { asm volatile("s_waitcnt lgkmcnt(%2)" : "+v"(a), "+v"(b) : "i"(N)); }
__device__ __forceinline__ void glds1(const char* sbase, unsigned voff, unsigned lds) {
  unsigned keep;
  asm volatile("s_mov_b32 %0, m0\n\ts_mov_b32 m0, %3\n\ts_nop 0\n\tglobal_load_lds_dwordx4 %1, %2 sc1\n\ts_mov_b32 m0, %0"
               : "=&s"(keep) : "v"(voff), "s"(sbase), "s"(lds) : "memory");
}
#define XS(i) do { } while (0)
#define XFL(b_) do { } while (0)
#define XA_ 0
#define XF_ 0
#define XE_ 0
#define XP_ 0
#define PUBK 9
#define LATE 0
#define NOWP 2
#define NEARM 14
#define PNEAR 3
#define LATEQ 0
#define DEFP 1
#define ROTM 0
#define ROTK 7
#define W2 1
#define PDEEP 4
template <int ROLE>
__device__ __forceinline__ void gemm7(char* smem, int wave_id_, const u16* W0, const u16* W1, int ldb, const char* A_, size_t a_blk, const Dep& dep,
                                      char* C_, size_t c_blk, unsigned* myprog, unsigned* tmo, int slot, unsigned long long* mts,
                                      bool gl, int nimm = 0, int nseg = NSEG, int wm0 = 0, int pofs = 0) {
  volatile LAS unsigned* wsl = (volatile LAS unsigned*)(smem + L_SLOT + 512);
  if (opaque_tid() < 144) *(volatile LAS unsigned*)(smem + L_SLOT + opaque_tid() * 4) = 0u;
  const int tid = opaque_tid(), lane = tid & 63, wave = WAVE_ID;
  const int kq = wave >> 2, nq = wave & 3;
  const unsigned ldsb = __builtin_amdgcn_readfirstlane((unsigned)(size_t)(LAS char*)smem);
  const int rot = (ROTM >> ROLE) & 1 ? slot & ROTK : 0;
  const char* A = uni(A_) + nq * 1024;
  constexpr bool NEAR = (NEARM >> ROLE) & 1;
  constexpr int AH = NEAR ? 2 : 3, PD = NEAR ? PNEAR : PDEEP;
  constexpr bool P2C = ROLE == R_PART2;
  int pw = 0;
  int wm = wm0, nh = 0;
  auto lpoll = [&](int b, bool hs = false) {
    unsigned w;
    if (wave == 4) {
      for (int spins = 0;; ++spins) {
        unsigned v;
        if (hs) { WAIT_V(0); v = *(volatile LAS unsigned*)(smem + L_SLOT + 768 + lane_id() * 4); }
        else asm volatile("global_load_dword %0, %1, %2 sc1\n\ts_waitcnt vmcnt(0)" : "=&v"(v) : "v"(dep.voff), "s"(dep.base) : "memory");
        w = dep_eval(v, dep);
        if ((int)w > b) break;
        if (!hs) __builtin_amdgcn_s_sleep(2);
        hs = false;
        if (give_up(spins, tmo, 9)) { w = NSEG; break; }
      }
      w = w < 255u ? w : 255u;
      if (lane == 0) wsl[0] = ((unsigned)(b + 1) << 8) | w;
    } else {
      for (;;) {
        w = __builtin_amdgcn_readfirstlane(wsl[0]);
        if ((w >> 8) == (unsigned)(b + 1)) break;
        __builtin_amdgcn_s_sleep(1);
      }
      w &= 255u;
    }
    return (int)w;
  };
  auto pubw = [&](int n) {
    WAIT_V(0);
    if (lane == 0) {
      const unsigned o = __hip_atomic_fetch_add((LAS unsigned*)(smem + L_SLOT + 516), 1u, __ATOMIC_RELAXED, __HIP_MEMORY_SCOPE_WORKGROUP);
      if ((o & 3u) == 3u) {
        st4_sc1(myprog, (unsigned)(n + pofs));
      }
    }
    asm volatile("s_waitcnt lgkmcnt(0)" ::: "memory");
  };
  auto hsrc = [&](int hh) { return A + (size_t)(hh >> 1) * a_blk + ((hh ^ (rot >> 2)) & 1) * 16384; };
  auto hlds = [&](int hh) { return ldsb + (unsigned)(((hh >> 1) & 1) * 65536 + (hh & 1) * 16384 + nq * 1024); };
#define POFF(i_) (((i_) & 1) * 32768 + ((i_) >> 1) * 4096)
#define PIECE(s_, l_, i_) asm volatile("s_add_u32 m0, %2, %3\n\ts_nop 0\n\tglobal_load_lds_dwordx4 %0, %1 sc1" :: "v"(av[i_]), "s"(s_), "s"(l_), "i"(POFF(i_)) : "memory", "scc", "m0")
  u32x32 BW[4];
  KTS(1);
  wload<ROLE, 2>(smem, wave_id_, W0, W1, ldb, BW, rot, [&]() {
    if (ROLE == R_QKV && nimm > 0) {
      if (kq) {
        wm = lpoll(0);
        const unsigned l16 = lane_id() * 16;
#pragma unroll
        for (int i = 0; i < 16; ++i) glds1(hsrc(i >> 3) + (i & 1) * 32768 + ((((i & 7) >> 1) ^ rot) & 3) * 4096, l16, hlds(i >> 3) + POFF(i & 7));
      }
      nh = 2;
    }
  });
  KTS(2);
  if (ROLE == R_QKV && nimm > 0 && !kq) wm = (int)(__builtin_amdgcn_readfirstlane(wsl[0]) & 255u);
  if (ROLE == R_QKV && opaque_tid() == 0) STS((char*)tmo - OFF_TMO, 128 + slot, 0);
  auto body = [&](auto ldr_c) __attribute__((always_inline)) {
    constexpr bool LDR = decltype(ldr_c)::value;
    if (XP_ == 0 ? !LDR : (XP_ == 1 ? LDR : false)) __builtin_amdgcn_s_setprio(1);
    unsigned av[8];
#pragma unroll
    for (int i = 0; i < 8; ++i) av[i] = lane * 16 + (P2C ? 0 : (i & 1) * 32768) + (((i >> 1) ^ rot) & 3) * 4096;
    auto issue_all = [&](int hh) {
      if (LDR) {
        const char* s_ = hsrc(hh); const unsigned l_ = hlds(hh);
#pragma unroll
        for (int i = 0; i < 8; ++i) if (!(P2C && (i & 1))) PIECE(s_, l_, i);
      }
    };
    int b = 0;
    bool hs = false;
    while (b < nseg) {
      const int ln = lane_id(), g = ln >> 4, r16 = ln & 15;
      const unsigned e0 = ldsb + (unsigned)((P2C ? 0 : kq * 32768) + (b & 1) * 65536 + (gl ? (g >> 1) * 512 + r16 * 32 + (g & 1) * 16 : r16 * 128 + ((g ^ (r16 & 7)) << 4)));
      const unsigned d0 = e0 ^ (gl ? 1024u : 64u);
      const unsigned rw = ldsb + L_RED + nq * 4096 + ln * 16;
      const unsigned co0 = ROLE == R_F1 ? (unsigned)((g & 1) * 2048 + nq * 512 + r16 * 32 + (g >> 1) * 16)
                         : ((ROLE == R_QKV ? TLQ : TLP) ? (unsigned)(nq * (P2C ? 4096 : 2048) + r16 * 64 + g * 16)
                         : (ROLE == R_QKV ? (unsigned)(r16 * 256 + (((4 * nq + g) ^ r16) << 4)) : (unsigned)(r16 * PROW + (32 * nq + 8 * g) * 2)));
      const unsigned co1 = co0 + ((ROLE == R_QKV ? TLQ : TLP) ? 1024u : (ROLE == R_QKV ? 4096u : 16u * PROW));
      u32x16 pv_;
      bool smp = false, lt0 = false;
      if ((wm < nseg ? wm : nseg) >= b + PD && b >= nimm && nh <= 2 * b + AH) {
        __builtin_amdgcn_s_barrier(); asm volatile("" ::: "memory");
        for (; nh < 2 * b + AH; ++nh) issue_all(nh);
        int lf = (wm < nseg ? wm : nseg) - (AH - 1), cp = nseg - (AH - 1);
        if (!LDR) KTS(128 + b);
        const unsigned sa = ldsb + L_SLOT + ln * 4, ad = dep.add - (AH - 1);
        if constexpr (LDR) (NEAR ? (P2C ? near_ld2 : near_ld0) : (P2C ? fast_ld2 : fast_ld0))(BW, pv_, e0, d0, b, lf, cp, sa, dep.mul, ad, rw, (unsigned)a_blk, hsrc(2 * b + AH), hsrc(2 * b + AH + 1), hlds(2 * b + AH), hlds(2 * b + AH + 1), av);
        else {
          const char* c0_ = uni((const char*)tmo - OFF_TMO + OFF_DUMP); const char* c1_ = uni(C_ + (size_t)b * c_blk);
          const int sf = b + pofs - (pw > 0 ? 1 : 0);
          const char* pg = uni((const char*)myprog);
          (NEAR ? (ROLE == R_F1 ? near_st1 : (P2C ? near_st2 : near_st0)) : (ROLE == R_F1 ? fast_st1 : (P2C ? fast_st2 : fast_st0)))(BW, pv_, e0, d0, b, lf, cp, sa, dep.mul, ad, rw, (unsigned)c_blk, c0_, c1_, co0, co1, pofs, sf, (unsigned)wave,
                                                                   ldsb + L_SLOT, dep.voff, dep.base, pg);
        }
        if (!LDR) KTS(64 + b);
        wm = lf + (AH - 1) > wm ? lf + (AH - 1) : wm;
        nh = 2 * b + AH;
        --b;
        __builtin_amdgcn_s_barrier(); asm volatile("" ::: "memory");
      } else {
        const bool pl = wm <= b;
        XS(0);
        if (pl && LDR) wm = lpoll(b, hs);
        XS(1);
        const bool cold = nh < 2 * b + 2;
        { const int upto = nh == 2 * b + 3 ? 2 * b + 4 : 2 * b + 2; for (; nh < upto; ++nh) issue_all(nh); }
        smp = nh == 2 * b + 2 && b + 1 < nseg && wm0 < nseg;
        if (LDR && wave == 4 && smp)
          asm volatile("s_mov_b32 m0, %0\n\ts_nop 0\n\tglobal_load_lds_dword %1, %2 sc1" :: "s"(ldsb + L_SLOT + (b & 1) * 256), "v"(dep.voff), "s"(dep.base) : "memory", "m0");
        XS(2);
        if (LDR && LEAN_QB) { if (nh >= 2 * b + 4) { if (P2C) WAIT_V(14); else WAIT_V(28); } else { if (P2C) WAIT_V(6); else WAIT_V(12); } }
        else if (LDR) { if (nh >= 2 * b + 4) { if (P2C) WAIT_V(12); else WAIT_V(24); } else { if (P2C) WAIT_V(4); else WAIT_V(8); } }
        BAR();
        XS(3);
        if (pl && !LDR) wm = (int)(__builtin_amdgcn_readfirstlane(wsl[0]) & 255u);
        const bool dm = nh == 2 * b + 2 && wm > b + 1 && b + 1 < nseg;
        if (ROLE == R_QKV && b < 2 && opaque_tid() == 0) STS((char*)tmo - OFF_TMO, 128 + slot, 1 + b);
        lt0 = smp && !dm;
        if constexpr (LDR) {
          if (dm) (P2C ? lean_dm1 : lean_dm0)(BW, pv_, e0, d0, 0u, hlds(nh), hsrc(nh), hsrc(nh + 1), av);
          else (P2C ? lean_ld1 : lean_ld0)(BW, pv_, e0, d0, (unsigned)(nh - 2 * b - 2), hlds(nh), hsrc(nh), hsrc(nh + 1), av, ldsb + L_SLOT + (b & 1) * 256 + ln * 4, dep.mul, dep.add, LATEQ && lt0 ? b + 1 : 0x7FFFFFFF,
                                           __builtin_amdgcn_readfirstlane(lt0 && wave == 4 ? ldsb + L_SLOT + 768 : 0u), dep.voff, dep.base);
        }
        else lean_st0(BW, pv_, e0, d0, __builtin_amdgcn_readfirstlane(pw > 0 ? (cold ? 4u : 1u) : 0u), (unsigned)(pw + pofs), ldsb + L_SLOT + 516, ldsb + L_SLOT + 256,
                     dep.voff, dep.base, uni((const char*)myprog));
        if (dm) nh += 2;
        XS(5);
      }
      if constexpr (LDR) fin_ld(pv_, rw, ldsb + L_SLOT + 528 + nq * 4, (unsigned)(b + 1));
      else (ROLE == R_F1 ? fin_st1 : (P2C ? fin_st2 : fin_st0))(pv_, rw, ldsb + L_SLOT + 528 + nq * 4, (unsigned)(b + 1), uni(C_ + (size_t)b * c_blk), co0, co1);
      XS(6);
      if (smp) {
        const int w = (int)dep_eval(*(volatile LAS unsigned*)(smem + L_SLOT + (b & 1) * 256 + lane_id() * 4), dep);
        if (LATEQ && lt0 && w > b + 1) nh += 2;
        wm = w > wm ? w : wm;
      }
      bool now = (NOWP == 2 ? nh < 2 * b + 4 && wm < b + 1 + PD : wm <= b + 1) || b + 1 >= nseg || b < nimm;
      if (!LDR && now) {
        if (DEFP && b >= nimm && b + 1 < nseg && (wm > b + 1 || (__builtin_amdgcn_readfirstlane(wsl[0]) >> 8) == (unsigned)(b + 2))) now = false;
        else pubw(b + 1);
      }
      XS(7);
      XFL(b);
      pw = now ? 0 : b + 1;
      hs = W2 && lt0;
      ++b;
    }
    __builtin_amdgcn_s_setprio(0);
  };
  if (kq) body(std::true_type{}); else body(std::false_type{});
#undef PIECE
#undef POFF
  WAIT_V(0);
  BAR();
  KTS(3);
}
__device__ __forceinline__ void fin_role(char* smem, int wave_id_, int fidx, const float* mod, const char* P2, const u16* x1, u16* out, unsigned* cnt, unsigned* tmo,
                                         unsigned long long* mts) {
  const int tid = opaque_tid(), lane = tid & 63, wave = WAVE_ID;
  row_table(smem + L_GB, mod, cnt, tmo, tid);
  volatile LAS unsigned* wsl = (volatile LAS unsigned*)(smem + L_SLOT + 512);
  int wf = 0, par = 0;
  for (int nf = fidx >> 3; nf < NSEG; nf += 2) {
    if (wf <= nf) {
      if (wave == 0) {
        unsigned w;
        for (int spins = 0;; ++spins) {
          const unsigned v = rowmin16(ld4_sc1(cnt + (PF2 + ((lane & 31) < 28 ? (lane & 31) : 0)) * 32));
          w = min(rdl(v, 0), rdl(v, 16));
          if ((int)w > nf) break;
          __builtin_amdgcn_s_sleep(2);
          if (give_up(spins, tmo, 9)) { w = NSEG; break; }
        }
        if (lane == 0) wsl[par] = w;
      }
      BAR();
      wf = (int)__builtin_amdgcn_readfirstlane(wsl[par]);
      par ^= 1;
    }
    int ln = lane; asm volatile("" : "+v"(ln));
    if (wave == 0) WTS(256 + 8 + fidx, nf / 2, 0);
    fin_half(P2, x1, smem + L_GB + 12288, out, nf * 32 + 4 * (fidx & 7) + (wave >> 1), wave & 1, ln);
    if (wave == 0) WTS(256 + 8 + fidx, nf / 2, 2);
  }
}
#define LD8(r, vo, sb) asm volatile("global_load_dwordx4 %0, %8, %12 sc1\n\tglobal_load_dwordx4 %1, %9, %12 sc1\n\t" \
    "global_load_dwordx4 %2, %10, %12 sc1\n\tglobal_load_dwordx4 %3, %11, %12 sc1\n\t" \
    "global_load_dwordx4 %4, %8, %13 sc1\n\tglobal_load_dwordx4 %5, %9, %13 sc1\n\t" \
    "global_load_dwordx4 %6, %10, %13 sc1\n\tglobal_load_dwordx4 %7, %11, %13 sc1" \
    : "=&v"(r[0][0]), "=&v"(r[0][1]), "=&v"(r[0][2]), "=&v"(r[0][3]), "=&v"(r[1][0]), "=&v"(r[1][1]), "=&v"(r[1][2]), "=&v"(r[1][3]) \
    : "v"(vo), "v"(vo ^ (TLQ ? 2048u : 64u)), "v"(vo ^ (TLQ ? 4096u : 128u)), "v"(vo ^ (TLQ ? 6144u : 192u)), "s"(sb), "s"((sb) + (TLQ ? 1024 : 4096)) : "memory")
#define TIE8(r) "+v"(r[0][0]), "+v"(r[0][1]), "+v"(r[0][2]), "+v"(r[0][3]), "+v"(r[1][0]), "+v"(r[1][1]), "+v"(r[1][2]), "+v"(r[1][3])
#define SADL 44
#define SASL 8
__device__ __forceinline__ void sa_role(char* smem, int wave_id_, int s0, int hbase, const char* __restrict__ Q, const char* __restrict__ Kx, const char* __restrict__ V,
                                        const char* kvm, char* __restrict__ O, unsigned* cnt, unsigned* tmo, int slot, unsigned long long* mts) {
  constexpr float LS = LOG2E * SCALE;
  const int tid = opaque_tid(), wave = WAVE_ID;
  const int h = hbase + wave;
  char* vl = smem + wave * 17408;
  const unsigned ldsw = __builtin_amdgcn_readfirstlane((unsigned)(size_t)(LAS char*)vl);
  unsigned avail = 0;
  for (int s = s0, it = 1; s < NSEG; s += 4, ++it) {
    u32x4 qv[2][4], kv[2][4];
    int lane = tid & 63; asm volatile("" : "+v"(lane));
    const int g = lane >> 4, r16 = lane & 15, q4 = r16 >> 2, p4 = lane & 3;
    auto request = [&](int sr) {
      const size_t o = ((size_t)sr * NH + h) * 8192;
      const char* qs = uni(Q + o); const char* ks = uni(Kx + o); const char* vs = uni(V + o);
      const unsigned fo = TLQ ? (unsigned)(r16 * 64 + g * 16) : (unsigned)(r16 * 256 + ((g ^ r16) << 4));
      LD8(qv, fo, qs); LD8(kv, fo, ks);
      head_to_lds(vs, lane, ldsw);
    };
    {
      for (int spins = 0; avail <= (unsigned)s; ++spins) {
        avail = qkv_avail<0>(cnt + (PQKV + h) * 32);
        if (avail > (unsigned)s) break;
        __builtin_amdgcn_s_sleep(32);
        if (give_up(spins, tmo, 10)) break;
      }
      request(s);
      asm volatile("s_waitcnt vmcnt(0)" : TIE8(qv), TIE8(kv) :: "memory");
    }
    if (wave == 0) WTS(slot, s, 0);
    f32x4 s00 = f32x4{0, 0, 0, 0}, s01 = s00, s11 = s00;
#pragma unroll
    for (int ks = 0; ks < 4; ++ks) {
      s00 = MFMA32(asbf8(kv[0][ks]), asbf8(qv[0][ks]), s00);
      s01 = MFMA32(asbf8(kv[0][ks]), asbf8(qv[1][ks]), s01);
      s11 = MFMA32(asbf8(kv[1][ks]), asbf8(qv[1][ks]), s11);
    }
    float mx[2], sum[2];
    u32x4 pseg[2];
    {
#pragma unroll
      for (int r = 0; r < 4; ++r) { const bool ok = 4 * g + r <= r16; s00[r] = ok ? s00[r] : -3.0e38f; s11[r] = ok ? s11[r] : -3.0e38f; }
      mx[0] = red16_32_max(max3(max3(s00[0], s00[1], s00[2]), s00[3], -1.0e38f));
      mx[1] = red16_32_max(max3(max3(fmaxf(fmaxf(s01[0], s01[1]), fmaxf(s01[2], s01[3])), s11[0], -1.0e38f), max3(s11[1], s11[2], s11[3]), -1.0e38f));
      const float n0 = -mx[0] * LS, n1 = -mx[1] * LS;
#pragma unroll
      for (int r = 0; r < 4; ++r) {
        s00[r] = __builtin_amdgcn_exp2f(s00[r] * LS + n0); s01[r] = __builtin_amdgcn_exp2f(s01[r] * LS + n1); s11[r] = __builtin_amdgcn_exp2f(s11[r] * LS + n1);
      }
      sum[0] = (s00[0] + s00[1]) + (s00[2] + s00[3]);
      sum[1] = ((s01[0] + s01[1]) + (s01[2] + s01[3])) + ((s11[0] + s11[1]) + (s11[2] + s11[3]));
      pseg[0] = u32x4{pack2(s00[0], s00[1]), pack2(s00[2], s00[3]), 0u, 0u};
      pseg[1] = u32x4{pack2(s01[0], s01[1]), pack2(s01[2], s01[3]), pack2(s11[0], s11[1]), pack2(s11[2], s11[3])};
    }
    f32x4 o[8][2];
#pragma unroll
    for (int dt = 0; dt < 8; ++dt) {
      const int row0 = 4 * g + q4;
      const unsigned xo = (unsigned)(row0 * 256 + (((4 * (dt >> 1) + p4) ^ row0) << 4) + (dt & 1) * 8);
      const u32x2 lo = trread(vl, xo), hi = trread(vl, xo + 4096);
      o[dt][0] = MFMA32(asbf8(u32x4{lo.x, lo.y, 0u, 0u}), asbf8(pseg[0]), (f32x4{0, 0, 0, 0}));
      o[dt][1] = MFMA32(asbf8(u32x4{lo.x, lo.y, hi.x, hi.y}), asbf8(pseg[1]), (f32x4{0, 0, 0, 0}));
    }
#pragma unroll
    for (int dt = 0; dt < 8; ++dt) asm volatile("" : "+v"(o[dt][0]), "+v"(o[dt][1]));
    asm volatile("s_waitcnt lgkmcnt(0)" ::: "memory");
    for (int spins = 0;; ++spins) {
      if (__all(ld4_sc1(cnt + (PROG + h) * 32 + ((lane & 4) ? 5 : (lane & 3))) + (s >= SDIR ? 1u : 0u) > (unsigned)s)) break;
      __builtin_amdgcn_s_sleep(SASL);
      if (give_up(spins, tmo, 6)) break;
    }
    if (s >= SDIR) delay64<SADL>();
    const char* mk = kvm + ((size_t)s * NH + h) * 4096;
    u32x4 kmv[4]; u32x2 vmv[8];
    {
      const char* pk = mk + (g * 16 + (r16 & 1) + ((r16 & 6) << 1)) * 16;
      const char* pv = mk + 2048 + (8 * q4 + p4) * 16 + (g & 1) * 8;
      for (int spins = 0;; ++spins) {
      asm volatile("global_load_dwordx4 %0, %12, off sc1\n\tglobal_load_dwordx4 %1, %12, off offset:32 sc1\n\t"
                   "global_load_dwordx4 %2, %12, off offset:1024 sc1\n\tglobal_load_dwordx4 %3, %12, off offset:1056 sc1\n\t"
                   "global_load_dwordx2 %4, %13, off sc1\n\tglobal_load_dwordx2 %5, %13, off offset:64 sc1\n\t"
                   "global_load_dwordx2 %6, %13, off offset:512 sc1\n\tglobal_load_dwordx2 %7, %13, off offset:576 sc1\n\t"
                   "global_load_dwordx2 %8, %13, off offset:1024 sc1\n\tglobal_load_dwordx2 %9, %13, off offset:1088 sc1\n\t"
                   "global_load_dwordx2 %10, %13, off offset:1536 sc1\n\tglobal_load_dwordx2 %11, %13, off offset:1600 sc1\n\ts_waitcnt vmcnt(0)"
                   : "=&v"(kmv[0]), "=&v"(kmv[1]), "=&v"(kmv[2]), "=&v"(kmv[3]), "=&v"(vmv[0]), "=&v"(vmv[1]), "=&v"(vmv[2]), "=&v"(vmv[3]),
                     "=&v"(vmv[4]), "=&v"(vmv[5]), "=&v"(vmv[6]), "=&v"(vmv[7])
                   : "v"(pk), "v"(pv) : "memory");
      unsigned mx_ = 0;
#pragma unroll
      for (int i = 0; i < 4; ++i) mx_ = max(max(mx_, max(kmv[i].x, kmv[i].y)), max(kmv[i].z, kmv[i].w));
#pragma unroll
      for (int i = 0; i < 8; ++i) mx_ = max(mx_, max(vmv[i].x, vmv[i].y));
      if (__all(mx_ != SENT)) break;
      __builtin_amdgcn_s_sleep(2);
      if (give_up(spins, tmo, 6)) break;
      }
      WTM(slot, s, 1);
    }
    u32x2 pm[2]; float cs[2], inv[2];
#pragma unroll
    for (int b = 0; b < 2; ++b) {
      f32x4 t = f32x4{0, 0, 0, 0};
#pragma unroll
      for (int ks = 0; ks < 4; ++ks) t = MFMA32(asbf8(kmv[ks]), asbf8(qv[b][ks]), t);
      float m = fmaxf(fmaxf(t[0], t[1]), fmaxf(fmaxf(t[2], t[3]), mx[b]));
      { float x, y; sw16(m, x, y); m = max2(x, y); }
      const float n = -m * LS;
      const float f = __builtin_amdgcn_exp2f(mx[b] * LS + n);
#pragma unroll
      for (int r = 0; r < 4; ++r) t[r] = g < 2 ? __builtin_amdgcn_exp2f(t[r] * LS + n) : 0.0f;
      inv[b] = __builtin_amdgcn_rcpf(red16_32_add(sum[b] * f + ((t[0] + t[1]) + (t[2] + t[3]))));
      cs[b] = f * inv[b];
      pm[b] = u32x2{pack2(t[0] * inv[b], t[1] * inv[b]), pack2(t[2] * inv[b], t[3] * inv[b])};
    }
    const char* od = uni(O + ((size_t)s * 64 + 2 * h) * 4096);
#pragma unroll
    for (int j = 0; j < 4; ++j) {
      u32x2 v0 = vmv[2 * j], v1 = vmv[2 * j + 1];
      if (g >= 2) { v0 = u32x2{0, 0}; v1 = v0; }
#pragma unroll
      for (int b = 0; b < 2; ++b) {
        const f32x4 r0 = __builtin_amdgcn_mfma_f32_16x16x16bf16_1k(ass4(v0), ass4(pm[b]), o[2 * j][b] * cs[b], 0, 0, 0);
        const f32x4 r1 = __builtin_amdgcn_mfma_f32_16x16x16bf16_1k(ass4(v1), ass4(pm[b]), o[2 * j + 1][b] * cs[b], 0, 0, 0);
        st16s(od, (unsigned)((j >> 1) * 4096 + (16 * b + r16) * 128 + (((4 * (j & 1) + g) ^ (r16 & 7)) << 4)),
              u32x4{pack2(r0[0], r0[1]), pack2(r0[2], r0[3]), pack2(r1[0], r1[1]), pack2(r1[2], r1[3])});
      }
    }
    WAIT_V(0);
    __builtin_amdgcn_s_barrier();
    if (wave == 0 && lane == 0) st4_sc1(cnt + (PSA + (hbase >> 1) + s0) * 32, (unsigned)it);
    WTM(slot, s, 2);
  }
}
#undef LD8
#undef TIE8

constexpr int Q_MEM = 0;
constexpr int Q_K   = 16384;
constexpr int Q_V   = 24576;
constexpr int Q_QK  = 32768;
constexpr int Q_TM  = 36864;
constexpr int Q_PQ  = 40960;
constexpr int Q_RING = 32768;
constexpr int NCHAIN = 96;
#define NQQ 6
#define NQK 4
#define E_AG 9
#define RDYI -1
#define ROTW 2
#define DQU 6
#define DQM 18
#define DQD 1
#define ORSEL (xg >> 1)
#define D_Q 92
#define D_O 8
#define D_A 0
#define D_A2 20
#define CL0 1
#define TSW 0
#define E_B 6
#define E_O 1700
#define KDEL 5
#define VPOS 0

typedef __bf16 bf16x16 __attribute__((ext_vector_type(16)));
typedef unsigned u32x8 __attribute__((ext_vector_type(8)));
__device__ __forceinline__ f32x4 SMF(u32x4 a, u32x8 b, f32x4 c, int idx) {
  return __builtin_amdgcn_smfmac_f32_16x16x64_bf16(__builtin_bit_cast(bf16x8, a), __builtin_bit_cast(bf16x16, b), c, idx, 0, 0);
}
__device__ __forceinline__ float fold32(float a, float b) {
  const auto p = __builtin_amdgcn_permlane32_swap(__float_as_uint(a), __float_as_uint(b), false, false);
  return __uint_as_float(p[0]) + __uint_as_float(p[1]);
}
__device__ __forceinline__ f32x2 rowsum(f32x4 x) { return f32x2{fold32(x[0], x[2]), fold32(x[1], x[3])}; }
__device__ __forceinline__ unsigned nosent1(unsigned v) { return v < 0xFFFFFFFEu ? v : 0xFFFFFFFEu; }
__device__ __forceinline__ void st4s(const char* sb, unsigned vo, unsigned v) { asm volatile("global_store_dword %0, %1, %2 sc1\n\ts_nop 0" :: "v"(vo), "v"(v), "s"(sb) : "memory"); }
__device__ __forceinline__ void st8s(const char* sb, unsigned vo, u32x2 v) { asm volatile("global_store_dwordx2 %0, %1, %2 sc1\n\ts_nop 1" :: "v"(vo), "v"(v), "s"(sb) : "memory"); }
__device__ __forceinline__ void gldsw(const char* sbase, unsigned voff, unsigned lds) {
  unsigned keep;
  asm volatile("s_mov_b32 %0, m0\n\ts_mov_b32 m0, %3\n\ts_nop 0\n\tglobal_load_lds_dwordx4 %1, %2" CNT_ "\n\ts_mov_b32 m0, %0"
               : "=&s"(keep) : "v"(voff), "s"(sbase), "s"(lds) : "memory");
}
__device__ __forceinline__ void wait_clk(unsigned t0, unsigned d) {
  while ((unsigned)__builtin_amdgcn_s_memtime() - t0 < d) __builtin_amdgcn_s_sleep(1);
}
__device__ __forceinline__ unsigned bell_time(const char* a, unsigned vo, unsigned tag, unsigned cap) {
  unsigned v;
  for (unsigned spins = 0;; ++spins) {
    asm volatile("global_load_dword %0, %1, %2 sc1\n\ts_waitcnt vmcnt(0)" : "=&v"(v) : "v"(vo), "s"(a) : "memory");
    if (__all((v >> 24) >= tag) || spins >= cap) break;
  }
  const unsigned now = (unsigned)__builtin_amdgcn_s_memrealtime() + 250u;
  const unsigned age = rowmin16((now - v) & 0xFFFFFFu);
  return now - min(min(rdl(age, 0), rdl(age, 16)), 500u);
}
__device__ __forceinline__ void wait_rt(unsigned t0, int d) {
  while ((int)((unsigned)__builtin_amdgcn_s_memrealtime() - t0) < d) { }
}
#define WBAR() do { __builtin_amdgcn_s_barrier(); asm volatile("" ::: "memory"); } while (0)
#define SB() __builtin_amdgcn_sched_barrier(0)
#define TN(k, v) do { } while (0)
template <bool HALF>
__device__ __forceinline__ void stage_proj(const char* src, unsigned lds, int tid, int wave) {
  const int row = tid >> 4, f = 2 * ((row & 1) | (((row >> 2) & 3) << 1));
#pragma unroll
  for (int j = 0; j < 4; ++j) {
    const int pc = (tid & 15) ^ f;
    gldsw(src + (size_t)j * 32 * HD * 2, (unsigned)(HALF ? (row + (pc >> 3) * 128) * HD * 2 + ((pc & 7) << 4) : row * HD * 2 + (pc << 4)), lds + (wave * 64 + 512 * j) * 16);
  }
}
__device__ __forceinline__ unsigned proj_base(int T, int lane) {
  const int g = lane >> 4, q4 = (lane & 15) >> 2, p4 = lane & 3, fs = 2 * ((q4 & 1) | ((g & 1) << 1) | ((q4 >> 1) << 2));
  return (unsigned)((16 * (g >> 1) + 4 * (g & 1) + (q4 & 1) + 8 * (q4 >> 1)) * 256 + (((2 * T) ^ fs) << 4) + p4 * 8);
}
__device__ __forceinline__ f32x4 zero4() { float z = 0.0f; asm volatile("" : "+v"(z)); return f32x4{z, z, z, z}; }
__device__ __forceinline__ unsigned lds_word(const char* p) { return __builtin_amdgcn_readfirstlane(*(volatile LAS unsigned*)p); }

#define MFMA16(a, b, c) __builtin_amdgcn_mfma_f32_16x16x16bf16_1k((a), (b), (c), 0, 0, 0)
#define UF(x) __uint_as_float(x)
__device__ __forceinline__ unsigned umax3(unsigned a, unsigned b, unsigned c) { return max(max(a, b), c); }
__device__ __forceinline__ f32x2 logit2(float a, float b) {
  return f32x2{a, b};
}
constexpr float LSC = LOG2E * SCALE;

struct QKW { u32x32 w0, w1, w2, w3, w4, w5; };
template <int W_> __device__ __forceinline__ void qk_step(const QKW& W, const char* ag, const char* vs, const char* v2, const char* od, const char* kv, unsigned sn, unsigned& cap, unsigned& sp,
    unsigned long long& tc, unsigned long long* ts, unsigned& d0, unsigned& d1);
template <> __device__ __forceinline__ void qk_step<0>(const QKW& W, const char* ag, const char* vs, const char* v2, const char* od, const char* kv, unsigned sn, unsigned& cap, unsigned& sp,
    unsigned long long& tc, unsigned long long* ts, unsigned& d0, unsigned& d1) {
  unsigned st, bg;
  asm volatile("v_mbcnt_lo_u32_b32 v60, -1, 0\n\t"
    "v_mbcnt_hi_u32_b32 v60, -1, v60\n\t"
    "v_lshlrev_b32 v5, 4, v60\n\t"
    "s_mov_b32 %[sp], 0\n\t"
    "s_setprio 2\n\t"
    "global_load_dwordx4 v[24:27], v5, %[ag] sc1\n\t"
    "global_load_dwordx4 v[28:31], v5, %[ag] offset:1024 sc1\n\t"
    "s_memtime %[tc]\n\t"
    "v_lshrrev_b32 v32, 4, v60\n\t"
    "v_and_b32 v33, 15, v60\n\t"
    "v_lshrrev_b32 v34, 2, v33\n\t"
    "v_and_b32 v35, 3, v60\n\t"
    "v_and_b32 v61, 1, v60\n\t"
    "v_lshlrev_b32 v61, 3, v61\n\t"
    "v_lshl_or_b32 v7, v34, 4, v61\n\t"
    "v_lshl_or_b32 v7, v32, 7, v7\n\t"
    "v_xor_b32 v10, v32, v33\n\t"
    "v_lshlrev_b32 v10, 4, v10\n\t"
    "v_lshl_or_b32 v10, v33, 8, v10\n\t"
    "v_xor_b32 v11, 0x40, v10\n\t"
    "v_xor_b32 v12, 0x80, v10\n\t"
    "v_xor_b32 v13, 0xc0, v10\n\t"
    "v_lshl_or_b32 v34, v32, 2, v34\n\t"
    "v_lshrrev_b32 v35, 1, v35\n\t"
    "v_or_b32 v35, 0, v35\n\t"
    "v_xor_b32 v35, v35, v34\n\t"
    "v_lshl_or_b32 v14, v35, 4, v61\n\t"
    "v_lshl_or_b32 v14, v34, 8, v14\n\t"
    "v_and_b32 v61, 3, v60\n\t"
    "v_lshlrev_b32 v61, 6, v61\n\t"
    "v_lshl_or_b32 v4, v32, 4, v61\n\t"
    "v_and_b32 v61, 4, v60\n\t"
    "v_lshl_or_b32 v4, v61, 10, v4\n\t"
    "v_and_b32 v61, 8, v60\n\t"
    "v_lshl_or_b32 v4, v61, 14, v4\n\t"
    "v_or_b32 v4, 0, v4\n\t"
    "v_and_b32 v61, 2, v60\n\t"
    "v_mul_u32_u24 v61, 0x5555, v61\n\t"
    "v_or_b32 v6, %[c44], v61\n\t"
    "v_lshlrev_b32 v61, 2, v32\n\t"
    "v_lshl_or_b32 v15, v33, 4, v61\n\t"
    "3:\n\t"
    "v_mov_b32 v16, 0\n\t"
    "v_mov_b32 v17, 0\n\t"
    "v_mov_b32 v18, 0\n\t"
    "v_mov_b32 v19, 0\n\t"
    "v_mov_b32 v20, 0\n\t"
    "v_mov_b32 v21, 0\n\t"
    "v_mov_b32 v22, 0\n\t"
    "v_mov_b32 v23, 0\n\t"
    "s_waitcnt vmcnt(0) lgkmcnt(0)\n\t"
    "ds_write_b128 v5, v[24:27] offset:0\n\t"
    "ds_write_b128 v5, v[28:31] offset:1024\n\t"
    "v_smfmac_f32_16x16x64_bf16 v[16:19], v[24:27], v[64:71], v6\n\t"
    "v_max3_u32 v60, v24, v25, v26\n\t"
    "v_max3_u32 v61, v27, v28, v29\n\t"
    "v_smfmac_f32_16x16x64_bf16 v[20:23], v[24:27], v[72:79], v6\n\t"
    "v_max3_u32 v60, v60, v30, v31\n\t"
    "v_max_u32 v60, v60, v61\n\t"
    "v_cmp_eq_u32 vcc, -1, v60\n\t"
    "s_cbranch_vccnz 1f\n\t"
    "2:\n\t"
    "s_waitcnt lgkmcnt(0)\n\t"
    "s_barrier\n\t"
    "ds_read_b128 v[32:35], v5 offset:2048\n\t"
    "ds_read_b128 v[36:39], v5 offset:3072\n\t"
    "v_smfmac_f32_16x16x64_bf16 v[16:19], v[28:31], v[80:87], v6\n\t"
    "ds_read_b128 v[40:43], v5 offset:4096\n\t"
    "ds_read_b128 v[44:47], v5 offset:5120\n\t"
    "v_smfmac_f32_16x16x64_bf16 v[20:23], v[28:31], v[88:95], v6\n\t"
    "s_waitcnt lgkmcnt(3)\n\t"
    "v_smfmac_f32_16x16x64_bf16 v[16:19], v[32:35], v[96:103], v6\n\t"
    "ds_read_b128 v[48:51], v5 offset:6144\n\t"
    "v_smfmac_f32_16x16x64_bf16 v[20:23], v[32:35], v[104:111], v6\n\t"
    "ds_read_b128 v[52:55], v5 offset:7168\n\t"
    "s_waitcnt lgkmcnt(4)\n\t"
    "v_smfmac_f32_16x16x64_bf16 v[16:19], v[36:39], v[112:119], v6\n\t"
    "ds_read_b128 v[24:27], v5 offset:8192\n\t"
    "v_smfmac_f32_16x16x64_bf16 v[20:23], v[36:39], v[120:127], v6\n\t"
    "ds_read_b128 v[28:31], v5 offset:9216\n\t"
    "s_waitcnt lgkmcnt(5)\n\t"
    "v_smfmac_f32_16x16x64_bf16 v[16:19], v[40:43], v[128:135], v6\n\t"
    "ds_read_b128 v[56:59], v5 offset:10240\n\t"
    "v_smfmac_f32_16x16x64_bf16 v[20:23], v[40:43], v[136:143], v6\n\t"
    "ds_read_b128 v[60:63], v5 offset:11264\n\t"
    "ds_read_b128 v[36:39], v11 offset:20480\n\t"
    "s_waitcnt lgkmcnt(7)\n\t"
    "v_smfmac_f32_16x16x64_bf16 v[16:19], v[44:47], v[144:151], v6\n\t"
    "ds_read_b128 v[40:43], v12 offset:16384\n\t"
    "v_smfmac_f32_16x16x64_bf16 v[20:23], v[44:47], v[152:159], v6\n\t"
    "ds_read_b128 v[44:47], v12 offset:20480\n\t"
    "s_waitcnt lgkmcnt(8)\n\t"
    "v_smfmac_f32_16x16x64_bf16 v[16:19], v[48:51], v[160:167], v6\n\t"
    "v_smfmac_f32_16x16x64_bf16 v[20:23], v[48:51], v[168:175], v6\n\t"
    "ds_read_b128 v[48:51], v13 offset:16384\n\t"
    "s_waitcnt lgkmcnt(8)\n\t"
    "v_smfmac_f32_16x16x64_bf16 v[16:19], v[52:55], v[176:183], v6\n\t"
    "v_smfmac_f32_16x16x64_bf16 v[20:23], v[52:55], v[184:191], v6\n\t"
    "ds_read_b128 v[52:55], v13 offset:20480\n\t"
    "s_waitcnt lgkmcnt(8)\n\t"
    "v_smfmac_f32_16x16x64_bf16 v[16:19], v[24:27], v[192:199], v6\n\t"
    "ds_read_b32 v8, v15 offset:45056\n\t"
    "ds_read_b128 v[32:35], v5 offset:40960\n\t"
    "s_setprio 3\n\t"
    "v_smfmac_f32_16x16x64_bf16 v[20:23], v[24:27], v[200:207], v6\n\t"
    "ds_read_b128 v[24:27], v10 offset:16384\n\t"
    "s_waitcnt lgkmcnt(10)\n\t"
    "v_smfmac_f32_16x16x64_bf16 v[16:19], v[28:31], v[208:215], v6\n\t"
    "s_waitcnt lgkmcnt(9)\n\t"
    "v_smfmac_f32_16x16x64_bf16 v[16:19], v[56:59], v[224:231], v6\n\t"
    "s_waitcnt lgkmcnt(8)\n\t"
    "v_smfmac_f32_16x16x64_bf16 v[16:19], v[60:63], v[240:247], v6\n\t"
    "v_smfmac_f32_16x16x64_bf16 v[20:23], v[28:31], v[216:223], v6\n\t"
    "s_waitcnt lgkmcnt(1)\n\t"
    "21:\n\t"
    "v_cmp_ne_u32 vcc, %[sn], v8\n\t"
    "s_cbranch_vccnz 22f\n\t"
    "v_mul_f32 v32, %[lsc], v32\n\t"
    "v_mul_f32 v33, %[lsc], v33\n\t"
    "v_smfmac_f32_16x16x64_bf16 v[20:23], v[56:59], v[232:239], v6\n\t"
    "v_mul_f32 v34, %[lsc], v34\n\t"
    "v_mul_f32 v35, %[lsc], v35\n\t"
    "v_add_f32 v16, v16, v18\n\t"
    "v_add_f32 v17, v17, v19\n\t"
    "v_smfmac_f32_16x16x64_bf16 v[20:23], v[60:63], v[248:255], v6\n\t"
    "v_fma_f32 v16, v16, %[lsc], v32\n\t"
    "v_fma_f32 v17, v17, %[lsc], v33\n\t"
    "v_cvt_pk_bf16_f32 v16, v16, v17\n\t"
    "ds_write_b32 v15, v16 offset:32768\n\t"
    "s_mov_b32 %[bg], %[big]\n\t"
    "s_nop 2\n\t"
    "v_add_f32 v20, v20, v22\n\t"
    "v_add_f32 v21, v21, v23\n\t"
    "v_fma_f32 v20, v20, %[lsc], v34\n\t"
    "v_fma_f32 v21, v21, %[lsc], v35\n\t"
    "v_cvt_pk_bf16_f32 v20, v20, v21\n\t"
    "ds_write_b32 v15, v20 offset:33024\n\t"
    "ds_read_b128 v[28:31], v10 offset:20480\n\t"
    "ds_read_b128 v[32:35], v11 offset:16384\n\t"
    "s_waitcnt lgkmcnt(2)\n\t"
    "s_barrier\n\t"
    "ds_read_b64_tr_b16 v[16:17], v7 offset:32768\n\t"
    "ds_read_b64_tr_b16 v[18:19], v7 offset:32832\n\t"
    "ds_read_b64_tr_b16 v[20:21], v7 offset:33280\n\t"
    "ds_read_b64_tr_b16 v[22:23], v7 offset:33344\n\t"
    "ds_read_b64_tr_b16 v[56:57], v7 offset:33792\n\t"
    "ds_read_b64_tr_b16 v[58:59], v7 offset:33856\n\t"
    "ds_read_b64_tr_b16 v[60:61], v7 offset:34304\n\t"
    "ds_read_b64_tr_b16 v[62:63], v7 offset:34368\n\t"
    "global_load_dwordx4 v[6:9], v5, %[vs] sc1\n\t"
    "global_load_dwordx4 v[10:13], v5, %[vs] offset:1024 sc1\n\t"
    "s_waitcnt lgkmcnt(6)\n\t"
    "v_mfma_f32_16x16x32_bf16 v[24:27], v[24:27], v[16:19], 0\n\t"
    "v_mfma_f32_16x16x32_bf16 v[28:31], v[28:31], v[16:19], 0\n\t"
    "s_waitcnt lgkmcnt(4)\n\t"
    "v_mfma_f32_16x16x32_bf16 v[24:27], v[32:35], v[20:23], v[24:27]\n\t"
    "v_mfma_f32_16x16x32_bf16 v[28:31], v[36:39], v[20:23], v[28:31]\n\t"
    "v_xor_b32 v39, 32, v14\n\t"
    "ds_read_b64_tr_b16 v[32:33], v14 offset:24576\n\t"
    "ds_read_b64_tr_b16 v[34:35], v14 offset:28672\n\t"
    "ds_read_b64_tr_b16 v[36:37], v39 offset:24576\n\t"
    "ds_read_b64_tr_b16 v[38:39], v39 offset:28672\n\t"
    "s_waitcnt lgkmcnt(6)\n\t"
    "v_mfma_f32_16x16x32_bf16 v[24:27], v[40:43], v[56:59], v[24:27]\n\t"
    "v_mfma_f32_16x16x32_bf16 v[28:31], v[44:47], v[56:59], v[28:31]\n\t"
    "s_waitcnt lgkmcnt(4)\n\t"
    "v_mfma_f32_16x16x32_bf16 v[24:27], v[48:51], v[60:63], v[24:27]\n\t"
    "v_mfma_f32_16x16x32_bf16 v[28:31], v[52:55], v[60:63], v[28:31]\n\t"
    "v_mov_b32 v52, %[one]\n\t"
    "v_mov_b32 v53, %[one]\n\t"
    "v_mov_b32 v54, %[one]\n\t"
    "v_mov_b32 v55, %[one]\n\t"
    "v_mov_b32 v45, 0\n\t"
    "v_mov_b32 v47, 0\n\t"
    "v_mov_b32 v48, %[one]\n\t"
    "v_max3_f32 v14, |v24|, |v25|, |v26|\n\t"
    "v_max3_f32 v40, |v27|, |v28|, |v29|\n\t"
    "v_max3_f32 v14, v14, |v30|, |v31|\n\t"
    "v_max_f32 v14, v14, v40\n\t"
    "v_cmp_nge_f32 vcc, %[big], v14\n\t"
    "v_mov_b32 v14, 0\n\t"
    "s_cbranch_vccnz 12f\n\t"
    "13:\n\t"
    "v_exp_f32 v24, v24\n\t"
    "v_exp_f32 v25, v25\n\t"
    "v_exp_f32 v26, v26\n\t"
    "v_exp_f32 v27, v27\n\t"
    "v_exp_f32 v28, v28\n\t"
    "v_exp_f32 v29, v29\n\t"
    "v_exp_f32 v30, v30\n\t"
    "v_exp_f32 v31, v31\n\t"
    "v_cvt_pk_bf16_f32 v40, v24, v25\n\t"
    "v_cvt_pk_bf16_f32 v41, v26, v27\n\t"
    "v_cvt_pk_bf16_f32 v42, v28, v29\n\t"
    "v_cvt_pk_bf16_f32 v43, v30, v31\n\t"
    "v_mov_b32 v49, 0\n\t"
    "v_mov_b32 v51, 0\n\t"
    "s_waitcnt lgkmcnt(0)\n\t"
    "v_mfma_f32_16x16x32_bf16 v[52:55], v[52:55], v[40:43], 0\n\t"
    "v_mfma_f32_16x16x32_bf16 v[24:27], v[32:35], v[40:43], 0\n\t"
    "v_mfma_f32_16x16x32_bf16 v[28:31], v[36:39], v[40:43], 0\n\t"
    "s_waitcnt vmcnt(1)\n\t"
    "5:\n\t"
    "v_mfma_f32_16x16x32_bf16 v[32:35], v[6:9], v[16:19], 0\n\t"
    "v_mfma_f32_16x16x32_bf16 v[36:39], v[6:9], v[20:23], 0\n\t"
    "s_waitcnt vmcnt(0)\n\t"
    "v_mfma_f32_16x16x32_bf16 v[32:35], v[10:13], v[56:59], v[32:35]\n\t"
    "v_mfma_f32_16x16x32_bf16 v[36:39], v[10:13], v[60:63], v[36:39]\n\t"
    "ds_read_b32 v44, v15 offset:34816\n\t"
    "ds_read_b32 v46, v15 offset:35072\n\t"
    "v_max3_u32 v40, v6, v7, v8\n\t"
    "v_max3_u32 v41, v9, v10, v11\n\t"
    "v_max3_u32 v40, v40, v12, v13\n\t"
    "v_max_u32 v40, v40, v41\n\t"
    "v_cmp_eq_u32 vcc, -1, v40\n\t"
    "s_cbranch_vccnz 4f\n\t"
    "6:\n\t"
    "s_nop 0\n\t"
    "v_add_f32 v32, v32, v38\n\t"
    "v_add_f32 v33, v33, v39\n\t"
    "v_exp_f32 v34, v32\n\t"
    "v_max_f32 v40, v32, v33\n\t"
    "v_exp_f32 v35, v33\n\t"
    "v_cmp_nge_f32 vcc, %[bg], v40\n\t"
    "s_cbranch_vccnz 10f\n\t"
    "11:\n\t"
    "v_cvt_pk_bf16_f32 v50, v34, v35\n\t"
    "17:\n\t"
    "s_waitcnt vmcnt(0) lgkmcnt(0)\n\t"
    "v_max_u32 v40, v44, v46\n\t"
    "v_cmp_eq_u32 vcc, -1, v40\n\t"
    "s_cbranch_vccnz 18f\n\t"
    "19:\n\t"
    "v_mfma_f32_16x16x16_bf16 v[52:55], v[48:49], v[50:51], v[52:55]\n\t"
    "v_mfma_f32_16x16x16_bf16 v[24:27], v[44:45], v[50:51], v[24:27]\n\t"
    "v_mfma_f32_16x16x16_bf16 v[28:31], v[46:47], v[50:51], v[28:31]\n\t"
    "s_nop 5\n\t"
    "v_rcp_f32 v42, v52\n\t"
    "s_nop 0\n\t"
    "v_pk_mul_f32 v[24:25], v[24:25], v[42:43] op_sel_hi:[1,0]\n\t"
    "v_pk_mul_f32 v[26:27], v[26:27], v[42:43] op_sel_hi:[1,0]\n\t"
    "v_pk_mul_f32 v[28:29], v[28:29], v[42:43] op_sel_hi:[1,0]\n\t"
    "v_pk_mul_f32 v[30:31], v[30:31], v[42:43] op_sel_hi:[1,0]\n\t"
    "v_cvt_pk_bf16_f32 v36, v24, v25\n\t"
    "v_cvt_pk_bf16_f32 v37, v26, v27\n\t"
    "v_cvt_pk_bf16_f32 v38, v28, v29\n\t"
    "v_cvt_pk_bf16_f32 v39, v30, v31\n\t"
    "v_min_u32 v36, -2, v36\n\t"
    "v_min_u32 v37, -2, v37\n\t"
    "v_min_u32 v38, -2, v38\n\t"
    "v_min_u32 v39, -2, v39\n\t"
    "global_store_dwordx4 v4, v[36:39], %[od] sc1\n\t"
    "s_barrier\n\t"
    "s_mov_b32 exec_hi, 0\n\t"
    "global_store_dwordx4 v5, v[6:9], %[kv] offset:0 sc1\n\t"
    "s_mov_b64 exec, -1\n\t"
    "s_nop 0\n\t"
    "s_waitcnt lgkmcnt(0)\n\t"
    "s_branch 9f\n\t"
    "1:\n\t"
    "s_add_u32 %[sp], %[sp], 1\n\t"
    "s_cmp_ge_u32 %[sp], %[cap]\n\t"
    "s_cbranch_scc1 7f\n\t"
    "global_load_dwordx4 v[24:27], v5, %[ag] sc1\n\t"
    "global_load_dwordx4 v[28:31], v5, %[ag] offset:1024 sc1\n\t"
    "s_waitcnt lgkmcnt(0)\n\t"
    "s_memtime %[tc]\n\t"
    "s_nop 7\n\t"
    "s_branch 3b\n\t"
    "7:\n\t"
    "s_mov_b32 %[cap], 0\n\t"
    "s_branch 2b\n\t"
    "4:\n\t"
    "s_add_u32 %[sp], %[sp], 0x10000\n\t"
    "s_lshr_b32 %[st], %[sp], 16\n\t"
    "s_cmp_ge_u32 %[st], %[cap]\n\t"
    "s_cbranch_scc1 8f\n\t"
    "global_load_dwordx4 v[6:9], v5, %[vs] sc1\n\t"
    "global_load_dwordx4 v[10:13], v5, %[vs] offset:1024 sc1\n\t"
    "s_waitcnt vmcnt(0) lgkmcnt(0)\n\t"
    "s_branch 5b\n\t"
    "8:\n\t"
    "s_mov_b32 %[cap], 0\n\t"
    "s_branch 6b\n\t"
    "18:\n\t"
    "s_add_u32 %[sp], %[sp], 0x10000\n\t"
    "s_lshr_b32 %[st], %[sp], 16\n\t"
    "s_cmp_ge_u32 %[st], %[cap]\n\t"
    "s_cbranch_scc1 20f\n\t"
    "ds_read_b32 v44, v15 offset:34816\n\t"
    "ds_read_b32 v46, v15 offset:35072\n\t"
    "s_waitcnt vmcnt(0) lgkmcnt(0)\n\t"
    "s_branch 17b\n\t"
    "20:\n\t"
    "s_mov_b32 %[cap], 0\n\t"
    "s_branch 19b\n\t"
    "22:\n\t"
    "ds_read_b32 v8, v15 offset:45056\n\t"
    "ds_read_b128 v[32:35], v5 offset:40960\n\t"
    "s_waitcnt lgkmcnt(0)\n\t"
    "s_branch 21b\n\t"
    "10:\n\t"
    "v_max_f32 v40, v40, v14\n\t"
    "v_mov_b32 v42, v40\n\t"
    "s_nop 1\n\t"
    "v_permlane16_swap_b32 v40, v42\n\t"
    "v_max_f32 v40, v40, v42\n\t"
    "v_mov_b32 v42, v40\n\t"
    "s_nop 1\n\t"
    "v_permlane32_swap_b32 v40, v42\n\t"
    "v_max_f32 v40, v40, v42\n\t"
    "v_sub_f32 v34, v32, v40\n\t"
    "v_sub_f32 v35, v33, v40\n\t"
    "v_sub_f32 v40, v14, v40\n\t"
    "v_exp_f32 v40, v40\n\t"
    "v_exp_f32 v34, v34\n\t"
    "v_exp_f32 v35, v35\n\t"
    "v_mul_f32 v24, v24, v40\n\t"
    "v_mul_f32 v25, v25, v40\n\t"
    "v_mul_f32 v26, v26, v40\n\t"
    "v_mul_f32 v27, v27, v40\n\t"
    "v_mul_f32 v28, v28, v40\n\t"
    "v_mul_f32 v29, v29, v40\n\t"
    "v_mul_f32 v30, v30, v40\n\t"
    "v_mul_f32 v31, v31, v40\n\t"
    "v_mul_f32 v52, v52, v40\n\t"
    "v_mul_f32 v53, v53, v40\n\t"
    "v_mul_f32 v54, v54, v40\n\t"
    "v_mul_f32 v55, v55, v40\n\t"
    "s_branch 11b\n\t"
    "12:\n\t"
    "v_max3_f32 v14, v24, v25, v26\n\t"
    "v_max3_f32 v40, v27, v28, v29\n\t"
    "v_max3_f32 v14, v14, v30, v31\n\t"
    "v_max3_f32 v14, v14, v40, %[neg]\n\t"
    "v_mov_b32 v40, v14\n\t"
    "s_nop 1\n\t"
    "v_permlane16_swap_b32 v14, v40\n\t"
    "v_max_f32 v14, v14, v40\n\t"
    "v_mov_b32 v40, v14\n\t"
    "s_nop 1\n\t"
    "v_permlane32_swap_b32 v14, v40\n\t"
    "v_max_f32 v14, v14, v40\n\t"
    "v_sub_f32 v24, v24, v14\n\t"
    "v_sub_f32 v25, v25, v14\n\t"
    "v_sub_f32 v26, v26, v14\n\t"
    "v_sub_f32 v27, v27, v14\n\t"
    "v_sub_f32 v28, v28, v14\n\t"
    "v_sub_f32 v29, v29, v14\n\t"
    "v_sub_f32 v30, v30, v14\n\t"
    "v_sub_f32 v31, v31, v14\n\t"
    "s_mov_b32 %[bg], 0x7fc00000\n\t"
    "s_branch 13b\n\t"
    "9:"
    : [tc] "=&s"(tc), [sp] "=&s"(sp), [cap] "+s"(cap), [st] "=&s"(st), [bg] "=&s"(bg)
    : [ag] "s"(ag), [c44] "s"(0x4444u), [lsc] "s"(LSC), [sn] "s"(sn), [kv] "s"(kv), [vs] "s"(vs), [od] "s"(od), [neg] "s"(-1.0e38f), [one] "s"(0x3F803F80u), [big] "s"(24.0f), "{v[64:95]}"(W.w0), "{v[96:127]}"(W.w1), "{v[128:159]}"(W.w2), "{v[160:191]}"(W.w3), "{v[192:223]}"(W.w4), "{v[224:255]}"(W.w5)
    : "v4", "v5", "v6", "v7", "v8", "v9", "v10", "v11", "v12", "v13", "v14", "v15", "v16", "v17", "v18", "v19", "v20", "v21", "v22", "v23", "v24", "v25", "v26", "v27", "v28", "v29", "v30", "v31", "v32", "v33", "v34", "v35", "v36", "v37", "v38", "v39", "v40", "v41", "v42", "v43", "v44", "v45", "v46", "v47", "v48", "v49", "v50", "v51", "v52", "v53", "v54", "v55", "v56", "v57", "v58", "v59", "v60", "v61", "v62", "v63", "vcc", "scc", "memory");
}
template <> __device__ __forceinline__ void qk_step<1>(const QKW& W, const char* ag, const char* vs, const char* v2, const char* od, const char* kv, unsigned sn, unsigned& cap, unsigned& sp,
    unsigned long long& tc, unsigned long long* ts, unsigned& d0, unsigned& d1) {
  unsigned st, bg;
  asm volatile("v_mbcnt_lo_u32_b32 v60, -1, 0\n\t"
    "v_mbcnt_hi_u32_b32 v60, -1, v60\n\t"
    "v_lshlrev_b32 v5, 4, v60\n\t"
    "s_mov_b32 %[sp], 0\n\t"
    "s_setprio 2\n\t"
    "global_load_dwordx4 v[24:27], v5, %[ag] sc1\n\t"
    "global_load_dwordx4 v[28:31], v5, %[ag] offset:1024 sc1\n\t"
    "s_memtime %[tc]\n\t"
    "v_lshrrev_b32 v32, 4, v60\n\t"
    "v_and_b32 v33, 15, v60\n\t"
    "v_lshrrev_b32 v34, 2, v33\n\t"
    "v_and_b32 v35, 3, v60\n\t"
    "v_and_b32 v61, 1, v60\n\t"
    "v_lshlrev_b32 v61, 3, v61\n\t"
    "v_lshl_or_b32 v7, v34, 4, v61\n\t"
    "v_lshl_or_b32 v7, v32, 7, v7\n\t"
    "v_xor_b32 v10, v32, v33\n\t"
    "v_lshlrev_b32 v10, 4, v10\n\t"
    "v_lshl_or_b32 v10, v33, 8, v10\n\t"
    "v_xor_b32 v11, 0x40, v10\n\t"
    "v_xor_b32 v12, 0x80, v10\n\t"
    "v_xor_b32 v13, 0xc0, v10\n\t"
    "v_lshl_or_b32 v34, v32, 2, v34\n\t"
    "v_lshrrev_b32 v35, 1, v35\n\t"
    "v_or_b32 v35, 4, v35\n\t"
    "v_xor_b32 v35, v35, v34\n\t"
    "v_lshl_or_b32 v14, v35, 4, v61\n\t"
    "v_lshl_or_b32 v14, v34, 8, v14\n\t"
    "v_and_b32 v61, 3, v60\n\t"
    "v_lshlrev_b32 v61, 6, v61\n\t"
    "v_lshl_or_b32 v4, v32, 4, v61\n\t"
    "v_and_b32 v61, 4, v60\n\t"
    "v_lshl_or_b32 v4, v61, 10, v4\n\t"
    "v_and_b32 v61, 8, v60\n\t"
    "v_lshl_or_b32 v4, v61, 14, v4\n\t"
    "v_or_b32 v4, 256, v4\n\t"
    "v_and_b32 v61, 2, v60\n\t"
    "v_mul_u32_u24 v61, 0x5555, v61\n\t"
    "v_or_b32 v6, %[c44], v61\n\t"
    "v_lshlrev_b32 v61, 2, v32\n\t"
    "v_lshl_or_b32 v15, v33, 4, v61\n\t"
    "3:\n\t"
    "v_mov_b32 v16, 0\n\t"
    "v_mov_b32 v17, 0\n\t"
    "v_mov_b32 v18, 0\n\t"
    "v_mov_b32 v19, 0\n\t"
    "v_mov_b32 v20, 0\n\t"
    "v_mov_b32 v21, 0\n\t"
    "v_mov_b32 v22, 0\n\t"
    "v_mov_b32 v23, 0\n\t"
    "s_waitcnt vmcnt(0) lgkmcnt(0)\n\t"
    "ds_write_b128 v5, v[24:27] offset:2048\n\t"
    "ds_write_b128 v5, v[28:31] offset:3072\n\t"
    "v_smfmac_f32_16x16x64_bf16 v[16:19], v[24:27], v[96:103], v6\n\t"
    "v_max3_u32 v60, v24, v25, v26\n\t"
    "v_max3_u32 v61, v27, v28, v29\n\t"
    "v_smfmac_f32_16x16x64_bf16 v[20:23], v[24:27], v[104:111], v6\n\t"
    "v_max3_u32 v60, v60, v30, v31\n\t"
    "v_max_u32 v60, v60, v61\n\t"
    "v_cmp_eq_u32 vcc, -1, v60\n\t"
    "s_cbranch_vccnz 1f\n\t"
    "2:\n\t"
    "s_waitcnt lgkmcnt(0)\n\t"
    "s_barrier\n\t"
    "ds_read_b128 v[32:35], v5 offset:0\n\t"
    "ds_read_b128 v[36:39], v5 offset:1024\n\t"
    "v_smfmac_f32_16x16x64_bf16 v[16:19], v[28:31], v[112:119], v6\n\t"
    "ds_read_b128 v[40:43], v5 offset:4096\n\t"
    "ds_read_b128 v[44:47], v5 offset:5120\n\t"
    "v_smfmac_f32_16x16x64_bf16 v[20:23], v[28:31], v[120:127], v6\n\t"
    "s_waitcnt lgkmcnt(3)\n\t"
    "v_smfmac_f32_16x16x64_bf16 v[16:19], v[32:35], v[64:71], v6\n\t"
    "ds_read_b128 v[48:51], v5 offset:6144\n\t"
    "v_smfmac_f32_16x16x64_bf16 v[20:23], v[32:35], v[72:79], v6\n\t"
    "ds_read_b128 v[52:55], v5 offset:7168\n\t"
    "s_waitcnt lgkmcnt(4)\n\t"
    "v_smfmac_f32_16x16x64_bf16 v[16:19], v[36:39], v[80:87], v6\n\t"
    "ds_read_b128 v[24:27], v5 offset:8192\n\t"
    "v_smfmac_f32_16x16x64_bf16 v[20:23], v[36:39], v[88:95], v6\n\t"
    "ds_read_b128 v[28:31], v5 offset:9216\n\t"
    "s_waitcnt lgkmcnt(5)\n\t"
    "v_smfmac_f32_16x16x64_bf16 v[16:19], v[40:43], v[128:135], v6\n\t"
    "ds_read_b128 v[56:59], v5 offset:10240\n\t"
    "v_smfmac_f32_16x16x64_bf16 v[20:23], v[40:43], v[136:143], v6\n\t"
    "ds_read_b128 v[60:63], v5 offset:11264\n\t"
    "ds_read_b128 v[36:39], v11 offset:20480\n\t"
    "s_waitcnt lgkmcnt(7)\n\t"
    "v_smfmac_f32_16x16x64_bf16 v[16:19], v[44:47], v[144:151], v6\n\t"
    "ds_read_b128 v[40:43], v12 offset:16384\n\t"
    "v_smfmac_f32_16x16x64_bf16 v[20:23], v[44:47], v[152:159], v6\n\t"
    "ds_read_b128 v[44:47], v12 offset:20480\n\t"
    "s_waitcnt lgkmcnt(8)\n\t"
    "v_smfmac_f32_16x16x64_bf16 v[16:19], v[48:51], v[160:167], v6\n\t"
    "v_smfmac_f32_16x16x64_bf16 v[20:23], v[48:51], v[168:175], v6\n\t"
    "ds_read_b128 v[48:51], v13 offset:16384\n\t"
    "s_waitcnt lgkmcnt(8)\n\t"
    "v_smfmac_f32_16x16x64_bf16 v[16:19], v[52:55], v[176:183], v6\n\t"
    "v_smfmac_f32_16x16x64_bf16 v[20:23], v[52:55], v[184:191], v6\n\t"
    "ds_read_b128 v[52:55], v13 offset:20480\n\t"
    "s_waitcnt lgkmcnt(8)\n\t"
    "v_smfmac_f32_16x16x64_bf16 v[16:19], v[24:27], v[192:199], v6\n\t"
    "ds_read_b32 v8, v15 offset:45312\n\t"
    "ds_read_b128 v[32:35], v5 offset:41984\n\t"
    "s_setprio 3\n\t"
    "v_smfmac_f32_16x16x64_bf16 v[20:23], v[24:27], v[200:207], v6\n\t"
    "ds_read_b128 v[24:27], v10 offset:16384\n\t"
    "s_waitcnt lgkmcnt(10)\n\t"
    "v_smfmac_f32_16x16x64_bf16 v[16:19], v[28:31], v[208:215], v6\n\t"
    "s_waitcnt lgkmcnt(9)\n\t"
    "v_smfmac_f32_16x16x64_bf16 v[16:19], v[56:59], v[224:231], v6\n\t"
    "s_waitcnt lgkmcnt(8)\n\t"
    "v_smfmac_f32_16x16x64_bf16 v[16:19], v[60:63], v[240:247], v6\n\t"
    "v_smfmac_f32_16x16x64_bf16 v[20:23], v[28:31], v[216:223], v6\n\t"
    "s_waitcnt lgkmcnt(1)\n\t"
    "21:\n\t"
    "v_cmp_ne_u32 vcc, %[sn], v8\n\t"
    "s_cbranch_vccnz 22f\n\t"
    "v_mul_f32 v32, %[lsc], v32\n\t"
    "v_mul_f32 v33, %[lsc], v33\n\t"
    "v_smfmac_f32_16x16x64_bf16 v[20:23], v[56:59], v[232:239], v6\n\t"
    "v_mul_f32 v34, %[lsc], v34\n\t"
    "v_mul_f32 v35, %[lsc], v35\n\t"
    "v_add_f32 v16, v16, v18\n\t"
    "v_add_f32 v17, v17, v19\n\t"
    "v_smfmac_f32_16x16x64_bf16 v[20:23], v[60:63], v[248:255], v6\n\t"
    "v_fma_f32 v16, v16, %[lsc], v32\n\t"
    "v_fma_f32 v17, v17, %[lsc], v33\n\t"
    "v_cvt_pk_bf16_f32 v16, v16, v17\n\t"
    "ds_write_b32 v15, v16 offset:33280\n\t"
    "s_mov_b32 %[bg], %[big]\n\t"
    "s_nop 2\n\t"
    "v_add_f32 v20, v20, v22\n\t"
    "v_add_f32 v21, v21, v23\n\t"
    "v_fma_f32 v20, v20, %[lsc], v34\n\t"
    "v_fma_f32 v21, v21, %[lsc], v35\n\t"
    "v_cvt_pk_bf16_f32 v20, v20, v21\n\t"
    "ds_write_b32 v15, v20 offset:33536\n\t"
    "ds_read_b128 v[28:31], v10 offset:20480\n\t"
    "ds_read_b128 v[32:35], v11 offset:16384\n\t"
    "s_waitcnt lgkmcnt(2)\n\t"
    "s_barrier\n\t"
    "ds_read_b64_tr_b16 v[16:17], v7 offset:32768\n\t"
    "ds_read_b64_tr_b16 v[18:19], v7 offset:32832\n\t"
    "ds_read_b64_tr_b16 v[20:21], v7 offset:33280\n\t"
    "ds_read_b64_tr_b16 v[22:23], v7 offset:33344\n\t"
    "ds_read_b64_tr_b16 v[56:57], v7 offset:33792\n\t"
    "ds_read_b64_tr_b16 v[58:59], v7 offset:33856\n\t"
    "ds_read_b64_tr_b16 v[60:61], v7 offset:34304\n\t"
    "ds_read_b64_tr_b16 v[62:63], v7 offset:34368\n\t"
    "global_load_dwordx4 v[6:9], v5, %[vs] sc1\n\t"
    "global_load_dwordx4 v[10:13], v5, %[vs] offset:1024 sc1\n\t"
    "s_waitcnt lgkmcnt(6)\n\t"
    "v_mfma_f32_16x16x32_bf16 v[24:27], v[24:27], v[16:19], 0\n\t"
    "v_mfma_f32_16x16x32_bf16 v[28:31], v[28:31], v[16:19], 0\n\t"
    "s_waitcnt lgkmcnt(4)\n\t"
    "v_mfma_f32_16x16x32_bf16 v[24:27], v[32:35], v[20:23], v[24:27]\n\t"
    "v_mfma_f32_16x16x32_bf16 v[28:31], v[36:39], v[20:23], v[28:31]\n\t"
    "v_xor_b32 v39, 32, v14\n\t"
    "ds_read_b64_tr_b16 v[32:33], v14 offset:24576\n\t"
    "ds_read_b64_tr_b16 v[34:35], v14 offset:28672\n\t"
    "ds_read_b64_tr_b16 v[36:37], v39 offset:24576\n\t"
    "ds_read_b64_tr_b16 v[38:39], v39 offset:28672\n\t"
    "s_waitcnt lgkmcnt(6)\n\t"
    "v_mfma_f32_16x16x32_bf16 v[24:27], v[40:43], v[56:59], v[24:27]\n\t"
    "v_mfma_f32_16x16x32_bf16 v[28:31], v[44:47], v[56:59], v[28:31]\n\t"
    "s_waitcnt lgkmcnt(4)\n\t"
    "v_mfma_f32_16x16x32_bf16 v[24:27], v[48:51], v[60:63], v[24:27]\n\t"
    "v_mfma_f32_16x16x32_bf16 v[28:31], v[52:55], v[60:63], v[28:31]\n\t"
    "v_mov_b32 v52, %[one]\n\t"
    "v_mov_b32 v53, %[one]\n\t"
    "v_mov_b32 v54, %[one]\n\t"
    "v_mov_b32 v55, %[one]\n\t"
    "v_mov_b32 v45, 0\n\t"
    "v_mov_b32 v47, 0\n\t"
    "v_mov_b32 v48, %[one]\n\t"
    "v_max3_f32 v14, |v24|, |v25|, |v26|\n\t"
    "v_max3_f32 v40, |v27|, |v28|, |v29|\n\t"
    "v_max3_f32 v14, v14, |v30|, |v31|\n\t"
    "v_max_f32 v14, v14, v40\n\t"
    "v_cmp_nge_f32 vcc, %[big], v14\n\t"
    "v_mov_b32 v14, 0\n\t"
    "s_cbranch_vccnz 12f\n\t"
    "13:\n\t"
    "v_exp_f32 v24, v24\n\t"
    "v_exp_f32 v25, v25\n\t"
    "v_exp_f32 v26, v26\n\t"
    "v_exp_f32 v27, v27\n\t"
    "v_exp_f32 v28, v28\n\t"
    "v_exp_f32 v29, v29\n\t"
    "v_exp_f32 v30, v30\n\t"
    "v_exp_f32 v31, v31\n\t"
    "v_cvt_pk_bf16_f32 v40, v24, v25\n\t"
    "v_cvt_pk_bf16_f32 v41, v26, v27\n\t"
    "v_cvt_pk_bf16_f32 v42, v28, v29\n\t"
    "v_cvt_pk_bf16_f32 v43, v30, v31\n\t"
    "v_mov_b32 v49, 0\n\t"
    "v_mov_b32 v51, 0\n\t"
    "s_waitcnt lgkmcnt(0)\n\t"
    "v_mfma_f32_16x16x32_bf16 v[52:55], v[52:55], v[40:43], 0\n\t"
    "v_mfma_f32_16x16x32_bf16 v[24:27], v[32:35], v[40:43], 0\n\t"
    "v_mfma_f32_16x16x32_bf16 v[28:31], v[36:39], v[40:43], 0\n\t"
    "s_waitcnt vmcnt(1)\n\t"
    "5:\n\t"
    "v_mfma_f32_16x16x32_bf16 v[32:35], v[6:9], v[16:19], 0\n\t"
    "v_mfma_f32_16x16x32_bf16 v[36:39], v[6:9], v[20:23], 0\n\t"
    "s_waitcnt vmcnt(0)\n\t"
    "v_mfma_f32_16x16x32_bf16 v[32:35], v[10:13], v[56:59], v[32:35]\n\t"
    "v_mfma_f32_16x16x32_bf16 v[36:39], v[10:13], v[60:63], v[36:39]\n\t"
    "ds_read_b32 v44, v15 offset:35328\n\t"
    "ds_read_b32 v46, v15 offset:35584\n\t"
    "v_max3_u32 v40, v6, v7, v8\n\t"
    "v_max3_u32 v41, v9, v10, v11\n\t"
    "v_max3_u32 v40, v40, v12, v13\n\t"
    "v_max_u32 v40, v40, v41\n\t"
    "v_cmp_eq_u32 vcc, -1, v40\n\t"
    "s_cbranch_vccnz 4f\n\t"
    "6:\n\t"
    "s_nop 0\n\t"
    "v_add_f32 v32, v32, v38\n\t"
    "v_add_f32 v33, v33, v39\n\t"
    "v_exp_f32 v34, v32\n\t"
    "v_max_f32 v40, v32, v33\n\t"
    "v_exp_f32 v35, v33\n\t"
    "v_cmp_nge_f32 vcc, %[bg], v40\n\t"
    "s_cbranch_vccnz 10f\n\t"
    "11:\n\t"
    "v_cvt_pk_bf16_f32 v50, v34, v35\n\t"
    "17:\n\t"
    "s_waitcnt vmcnt(0) lgkmcnt(0)\n\t"
    "v_max_u32 v40, v44, v46\n\t"
    "v_cmp_eq_u32 vcc, -1, v40\n\t"
    "s_cbranch_vccnz 18f\n\t"
    "19:\n\t"
    "v_mfma_f32_16x16x16_bf16 v[52:55], v[48:49], v[50:51], v[52:55]\n\t"
    "v_mfma_f32_16x16x16_bf16 v[24:27], v[44:45], v[50:51], v[24:27]\n\t"
    "v_mfma_f32_16x16x16_bf16 v[28:31], v[46:47], v[50:51], v[28:31]\n\t"
    "s_nop 5\n\t"
    "v_rcp_f32 v42, v52\n\t"
    "s_nop 0\n\t"
    "v_pk_mul_f32 v[24:25], v[24:25], v[42:43] op_sel_hi:[1,0]\n\t"
    "v_pk_mul_f32 v[26:27], v[26:27], v[42:43] op_sel_hi:[1,0]\n\t"
    "v_pk_mul_f32 v[28:29], v[28:29], v[42:43] op_sel_hi:[1,0]\n\t"
    "v_pk_mul_f32 v[30:31], v[30:31], v[42:43] op_sel_hi:[1,0]\n\t"
    "v_cvt_pk_bf16_f32 v36, v24, v25\n\t"
    "v_cvt_pk_bf16_f32 v37, v26, v27\n\t"
    "v_cvt_pk_bf16_f32 v38, v28, v29\n\t"
    "v_cvt_pk_bf16_f32 v39, v30, v31\n\t"
    "v_min_u32 v36, -2, v36\n\t"
    "v_min_u32 v37, -2, v37\n\t"
    "v_min_u32 v38, -2, v38\n\t"
    "v_min_u32 v39, -2, v39\n\t"
    "global_store_dwordx4 v4, v[36:39], %[od] sc1\n\t"
    "s_barrier\n\t"
    "s_mov_b32 exec_lo, 0\n\t"
    "global_store_dwordx4 v5, v[6:9], %[kv] offset:0 sc1\n\t"
    "s_mov_b64 exec, -1\n\t"
    "s_nop 0\n\t"
    "s_waitcnt lgkmcnt(0)\n\t"
    "s_branch 9f\n\t"
    "1:\n\t"
    "s_add_u32 %[sp], %[sp], 1\n\t"
    "s_cmp_ge_u32 %[sp], %[cap]\n\t"
    "s_cbranch_scc1 7f\n\t"
    "global_load_dwordx4 v[24:27], v5, %[ag] sc1\n\t"
    "global_load_dwordx4 v[28:31], v5, %[ag] offset:1024 sc1\n\t"
    "s_waitcnt lgkmcnt(0)\n\t"
    "s_memtime %[tc]\n\t"
    "s_nop 7\n\t"
    "s_branch 3b\n\t"
    "7:\n\t"
    "s_mov_b32 %[cap], 0\n\t"
    "s_branch 2b\n\t"
    "4:\n\t"
    "s_add_u32 %[sp], %[sp], 0x10000\n\t"
    "s_lshr_b32 %[st], %[sp], 16\n\t"
    "s_cmp_ge_u32 %[st], %[cap]\n\t"
    "s_cbranch_scc1 8f\n\t"
    "global_load_dwordx4 v[6:9], v5, %[vs] sc1\n\t"
    "global_load_dwordx4 v[10:13], v5, %[vs] offset:1024 sc1\n\t"
    "s_waitcnt vmcnt(0) lgkmcnt(0)\n\t"
    "s_branch 5b\n\t"
    "8:\n\t"
    "s_mov_b32 %[cap], 0\n\t"
    "s_branch 6b\n\t"
    "18:\n\t"
    "s_add_u32 %[sp], %[sp], 0x10000\n\t"
    "s_lshr_b32 %[st], %[sp], 16\n\t"
    "s_cmp_ge_u32 %[st], %[cap]\n\t"
    "s_cbranch_scc1 20f\n\t"
    "ds_read_b32 v44, v15 offset:35328\n\t"
    "ds_read_b32 v46, v15 offset:35584\n\t"
    "s_waitcnt vmcnt(0) lgkmcnt(0)\n\t"
    "s_branch 17b\n\t"
    "20:\n\t"
    "s_mov_b32 %[cap], 0\n\t"
    "s_branch 19b\n\t"
    "22:\n\t"
    "ds_read_b32 v8, v15 offset:45312\n\t"
    "ds_read_b128 v[32:35], v5 offset:41984\n\t"
    "s_waitcnt lgkmcnt(0)\n\t"
    "s_branch 21b\n\t"
    "10:\n\t"
    "v_max_f32 v40, v40, v14\n\t"
    "v_mov_b32 v42, v40\n\t"
    "s_nop 1\n\t"
    "v_permlane16_swap_b32 v40, v42\n\t"
    "v_max_f32 v40, v40, v42\n\t"
    "v_mov_b32 v42, v40\n\t"
    "s_nop 1\n\t"
    "v_permlane32_swap_b32 v40, v42\n\t"
    "v_max_f32 v40, v40, v42\n\t"
    "v_sub_f32 v34, v32, v40\n\t"
    "v_sub_f32 v35, v33, v40\n\t"
    "v_sub_f32 v40, v14, v40\n\t"
    "v_exp_f32 v40, v40\n\t"
    "v_exp_f32 v34, v34\n\t"
    "v_exp_f32 v35, v35\n\t"
    "v_mul_f32 v24, v24, v40\n\t"
    "v_mul_f32 v25, v25, v40\n\t"
    "v_mul_f32 v26, v26, v40\n\t"
    "v_mul_f32 v27, v27, v40\n\t"
    "v_mul_f32 v28, v28, v40\n\t"
    "v_mul_f32 v29, v29, v40\n\t"
    "v_mul_f32 v30, v30, v40\n\t"
    "v_mul_f32 v31, v31, v40\n\t"
    "v_mul_f32 v52, v52, v40\n\t"
    "v_mul_f32 v53, v53, v40\n\t"
    "v_mul_f32 v54, v54, v40\n\t"
    "v_mul_f32 v55, v55, v40\n\t"
    "s_branch 11b\n\t"
    "12:\n\t"
    "v_max3_f32 v14, v24, v25, v26\n\t"
    "v_max3_f32 v40, v27, v28, v29\n\t"
    "v_max3_f32 v14, v14, v30, v31\n\t"
    "v_max3_f32 v14, v14, v40, %[neg]\n\t"
    "v_mov_b32 v40, v14\n\t"
    "s_nop 1\n\t"
    "v_permlane16_swap_b32 v14, v40\n\t"
    "v_max_f32 v14, v14, v40\n\t"
    "v_mov_b32 v40, v14\n\t"
    "s_nop 1\n\t"
    "v_permlane32_swap_b32 v14, v40\n\t"
    "v_max_f32 v14, v14, v40\n\t"
    "v_sub_f32 v24, v24, v14\n\t"
    "v_sub_f32 v25, v25, v14\n\t"
    "v_sub_f32 v26, v26, v14\n\t"
    "v_sub_f32 v27, v27, v14\n\t"
    "v_sub_f32 v28, v28, v14\n\t"
    "v_sub_f32 v29, v29, v14\n\t"
    "v_sub_f32 v30, v30, v14\n\t"
    "v_sub_f32 v31, v31, v14\n\t"
    "s_mov_b32 %[bg], 0x7fc00000\n\t"
    "s_branch 13b\n\t"
    "9:"
    : [tc] "=&s"(tc), [sp] "=&s"(sp), [cap] "+s"(cap), [st] "=&s"(st), [bg] "=&s"(bg)
    : [ag] "s"(ag), [c44] "s"(0x4444u), [lsc] "s"(LSC), [sn] "s"(sn), [kv] "s"(kv), [vs] "s"(vs), [od] "s"(od), [neg] "s"(-1.0e38f), [one] "s"(0x3F803F80u), [big] "s"(24.0f), "{v[64:95]}"(W.w0), "{v[96:127]}"(W.w1), "{v[128:159]}"(W.w2), "{v[160:191]}"(W.w3), "{v[192:223]}"(W.w4), "{v[224:255]}"(W.w5)
    : "v4", "v5", "v6", "v7", "v8", "v9", "v10", "v11", "v12", "v13", "v14", "v15", "v16", "v17", "v18", "v19", "v20", "v21", "v22", "v23", "v24", "v25", "v26", "v27", "v28", "v29", "v30", "v31", "v32", "v33", "v34", "v35", "v36", "v37", "v38", "v39", "v40", "v41", "v42", "v43", "v44", "v45", "v46", "v47", "v48", "v49", "v50", "v51", "v52", "v53", "v54", "v55", "v56", "v57", "v58", "v59", "v60", "v61", "v62", "v63", "vcc", "scc", "memory");
}
template <> __device__ __forceinline__ void qk_step<2>(const QKW& W, const char* ag, const char* vs, const char* v2, const char* od, const char* kv, unsigned sn, unsigned& cap, unsigned& sp,
    unsigned long long& tc, unsigned long long* ts, unsigned& d0, unsigned& d1) {
  unsigned st, bg;
  asm volatile("v_mbcnt_lo_u32_b32 v60, -1, 0\n\t"
    "v_mbcnt_hi_u32_b32 v60, -1, v60\n\t"
    "v_lshlrev_b32 v5, 4, v60\n\t"
    "s_mov_b32 %[sp], 0\n\t"
    "s_setprio 2\n\t"
    "global_load_dwordx4 v[24:27], v5, %[ag] sc1\n\t"
    "global_load_dwordx4 v[28:31], v5, %[ag] offset:1024 sc1\n\t"
    "s_memtime %[tc]\n\t"
    "v_lshrrev_b32 v32, 4, v60\n\t"
    "v_and_b32 v33, 15, v60\n\t"
    "v_lshrrev_b32 v34, 2, v33\n\t"
    "v_and_b32 v35, 3, v60\n\t"
    "v_and_b32 v61, 1, v60\n\t"
    "v_lshlrev_b32 v61, 3, v61\n\t"
    "v_lshl_or_b32 v7, v34, 4, v61\n\t"
    "v_lshl_or_b32 v7, v32, 7, v7\n\t"
    "v_xor_b32 v10, v32, v33\n\t"
    "v_lshlrev_b32 v10, 4, v10\n\t"
    "v_lshl_or_b32 v10, v33, 8, v10\n\t"
    "v_xor_b32 v11, 0x40, v10\n\t"
    "v_xor_b32 v12, 0x80, v10\n\t"
    "v_xor_b32 v13, 0xc0, v10\n\t"
    "v_lshl_or_b32 v34, v32, 2, v34\n\t"
    "v_lshrrev_b32 v35, 1, v35\n\t"
    "v_or_b32 v35, 8, v35\n\t"
    "v_xor_b32 v35, v35, v34\n\t"
    "v_lshl_or_b32 v14, v35, 4, v61\n\t"
    "v_lshl_or_b32 v14, v34, 8, v14\n\t"
    "v_and_b32 v61, 3, v60\n\t"
    "v_lshlrev_b32 v61, 6, v61\n\t"
    "v_lshl_or_b32 v4, v32, 4, v61\n\t"
    "v_and_b32 v61, 4, v60\n\t"
    "v_lshl_or_b32 v4, v61, 10, v4\n\t"
    "v_and_b32 v61, 8, v60\n\t"
    "v_lshl_or_b32 v4, v61, 14, v4\n\t"
    "v_or_b32 v4, 512, v4\n\t"
    "v_and_b32 v61, 2, v60\n\t"
    "v_mul_u32_u24 v61, 0x5555, v61\n\t"
    "v_or_b32 v6, %[c44], v61\n\t"
    "v_lshlrev_b32 v61, 2, v32\n\t"
    "v_lshl_or_b32 v15, v33, 4, v61\n\t"
    "3:\n\t"
    "v_mov_b32 v16, 0\n\t"
    "v_mov_b32 v17, 0\n\t"
    "v_mov_b32 v18, 0\n\t"
    "v_mov_b32 v19, 0\n\t"
    "v_mov_b32 v20, 0\n\t"
    "v_mov_b32 v21, 0\n\t"
    "v_mov_b32 v22, 0\n\t"
    "v_mov_b32 v23, 0\n\t"
    "s_waitcnt vmcnt(0) lgkmcnt(0)\n\t"
    "ds_write_b128 v5, v[24:27] offset:4096\n\t"
    "ds_write_b128 v5, v[28:31] offset:5120\n\t"
    "v_smfmac_f32_16x16x64_bf16 v[16:19], v[24:27], v[128:135], v6\n\t"
    "v_max3_u32 v60, v24, v25, v26\n\t"
    "v_max3_u32 v61, v27, v28, v29\n\t"
    "v_smfmac_f32_16x16x64_bf16 v[20:23], v[24:27], v[136:143], v6\n\t"
    "v_max3_u32 v60, v60, v30, v31\n\t"
    "v_max_u32 v60, v60, v61\n\t"
    "v_cmp_eq_u32 vcc, -1, v60\n\t"
    "s_cbranch_vccnz 1f\n\t"
    "2:\n\t"
    "s_waitcnt lgkmcnt(0)\n\t"
    "s_barrier\n\t"
    "ds_read_b128 v[32:35], v5 offset:0\n\t"
    "ds_read_b128 v[36:39], v5 offset:1024\n\t"
    "v_smfmac_f32_16x16x64_bf16 v[16:19], v[28:31], v[144:151], v6\n\t"
    "ds_read_b128 v[40:43], v5 offset:2048\n\t"
    "ds_read_b128 v[44:47], v5 offset:3072\n\t"
    "v_smfmac_f32_16x16x64_bf16 v[20:23], v[28:31], v[152:159], v6\n\t"
    "s_waitcnt lgkmcnt(3)\n\t"
    "v_smfmac_f32_16x16x64_bf16 v[16:19], v[32:35], v[64:71], v6\n\t"
    "ds_read_b128 v[48:51], v5 offset:6144\n\t"
    "v_smfmac_f32_16x16x64_bf16 v[20:23], v[32:35], v[72:79], v6\n\t"
    "ds_read_b128 v[52:55], v5 offset:7168\n\t"
    "s_waitcnt lgkmcnt(4)\n\t"
    "v_smfmac_f32_16x16x64_bf16 v[16:19], v[36:39], v[80:87], v6\n\t"
    "ds_read_b128 v[24:27], v5 offset:8192\n\t"
    "v_smfmac_f32_16x16x64_bf16 v[20:23], v[36:39], v[88:95], v6\n\t"
    "ds_read_b128 v[28:31], v5 offset:9216\n\t"
    "s_waitcnt lgkmcnt(5)\n\t"
    "v_smfmac_f32_16x16x64_bf16 v[16:19], v[40:43], v[96:103], v6\n\t"
    "ds_read_b128 v[56:59], v5 offset:10240\n\t"
    "v_smfmac_f32_16x16x64_bf16 v[20:23], v[40:43], v[104:111], v6\n\t"
    "ds_read_b128 v[60:63], v5 offset:11264\n\t"
    "ds_read_b128 v[36:39], v11 offset:20480\n\t"
    "s_waitcnt lgkmcnt(7)\n\t"
    "v_smfmac_f32_16x16x64_bf16 v[16:19], v[44:47], v[112:119], v6\n\t"
    "ds_read_b128 v[40:43], v12 offset:16384\n\t"
    "v_smfmac_f32_16x16x64_bf16 v[20:23], v[44:47], v[120:127], v6\n\t"
    "ds_read_b128 v[44:47], v12 offset:20480\n\t"
    "s_waitcnt lgkmcnt(8)\n\t"
    "v_smfmac_f32_16x16x64_bf16 v[16:19], v[48:51], v[160:167], v6\n\t"
    "v_smfmac_f32_16x16x64_bf16 v[20:23], v[48:51], v[168:175], v6\n\t"
    "ds_read_b128 v[48:51], v13 offset:16384\n\t"
    "s_waitcnt lgkmcnt(8)\n\t"
    "v_smfmac_f32_16x16x64_bf16 v[16:19], v[52:55], v[176:183], v6\n\t"
    "v_smfmac_f32_16x16x64_bf16 v[20:23], v[52:55], v[184:191], v6\n\t"
    "ds_read_b128 v[52:55], v13 offset:20480\n\t"
    "s_waitcnt lgkmcnt(8)\n\t"
    "v_smfmac_f32_16x16x64_bf16 v[16:19], v[24:27], v[192:199], v6\n\t"
    "ds_read_b32 v8, v15 offset:45568\n\t"
    "ds_read_b128 v[32:35], v5 offset:43008\n\t"
    "s_setprio 3\n\t"
    "v_smfmac_f32_16x16x64_bf16 v[20:23], v[24:27], v[200:207], v6\n\t"
    "ds_read_b128 v[24:27], v10 offset:16384\n\t"
    "s_waitcnt lgkmcnt(10)\n\t"
    "v_smfmac_f32_16x16x64_bf16 v[16:19], v[28:31], v[208:215], v6\n\t"
    "s_waitcnt lgkmcnt(9)\n\t"
    "v_smfmac_f32_16x16x64_bf16 v[16:19], v[56:59], v[224:231], v6\n\t"
    "s_waitcnt lgkmcnt(8)\n\t"
    "v_smfmac_f32_16x16x64_bf16 v[16:19], v[60:63], v[240:247], v6\n\t"
    "v_smfmac_f32_16x16x64_bf16 v[20:23], v[28:31], v[216:223], v6\n\t"
    "s_waitcnt lgkmcnt(1)\n\t"
    "21:\n\t"
    "v_cmp_ne_u32 vcc, %[sn], v8\n\t"
    "s_cbranch_vccnz 22f\n\t"
    "v_mul_f32 v32, %[lsc], v32\n\t"
    "v_mul_f32 v33, %[lsc], v33\n\t"
    "v_smfmac_f32_16x16x64_bf16 v[20:23], v[56:59], v[232:239], v6\n\t"
    "v_mul_f32 v34, %[lsc], v34\n\t"
    "v_mul_f32 v35, %[lsc], v35\n\t"
    "v_add_f32 v16, v16, v18\n\t"
    "v_add_f32 v17, v17, v19\n\t"
    "v_smfmac_f32_16x16x64_bf16 v[20:23], v[60:63], v[248:255], v6\n\t"
    "v_fma_f32 v16, v16, %[lsc], v32\n\t"
    "v_fma_f32 v17, v17, %[lsc], v33\n\t"
    "v_cvt_pk_bf16_f32 v16, v16, v17\n\t"
    "ds_write_b32 v15, v16 offset:33792\n\t"
    "s_mov_b32 %[bg], %[big]\n\t"
    "s_nop 2\n\t"
    "v_add_f32 v20, v20, v22\n\t"
    "v_add_f32 v21, v21, v23\n\t"
    "v_fma_f32 v20, v20, %[lsc], v34\n\t"
    "v_fma_f32 v21, v21, %[lsc], v35\n\t"
    "v_cvt_pk_bf16_f32 v20, v20, v21\n\t"
    "ds_write_b32 v15, v20 offset:34048\n\t"
    "ds_read_b128 v[28:31], v10 offset:20480\n\t"
    "ds_read_b128 v[32:35], v11 offset:16384\n\t"
    "s_waitcnt lgkmcnt(2)\n\t"
    "s_barrier\n\t"
    "ds_read_b64_tr_b16 v[16:17], v7 offset:32768\n\t"
    "ds_read_b64_tr_b16 v[18:19], v7 offset:32832\n\t"
    "ds_read_b64_tr_b16 v[20:21], v7 offset:33280\n\t"
    "ds_read_b64_tr_b16 v[22:23], v7 offset:33344\n\t"
    "ds_read_b64_tr_b16 v[56:57], v7 offset:33792\n\t"
    "ds_read_b64_tr_b16 v[58:59], v7 offset:33856\n\t"
    "ds_read_b64_tr_b16 v[60:61], v7 offset:34304\n\t"
    "ds_read_b64_tr_b16 v[62:63], v7 offset:34368\n\t"
    "global_load_dwordx4 v[6:9], v5, %[vs] sc1\n\t"
    "global_load_dwordx4 v[10:13], v5, %[vs] offset:1024 sc1\n\t"
    "s_waitcnt lgkmcnt(6)\n\t"
    "v_mfma_f32_16x16x32_bf16 v[24:27], v[24:27], v[16:19], 0\n\t"
    "v_mfma_f32_16x16x32_bf16 v[28:31], v[28:31], v[16:19], 0\n\t"
    "s_waitcnt lgkmcnt(4)\n\t"
    "v_mfma_f32_16x16x32_bf16 v[24:27], v[32:35], v[20:23], v[24:27]\n\t"
    "v_mfma_f32_16x16x32_bf16 v[28:31], v[36:39], v[20:23], v[28:31]\n\t"
    "v_xor_b32 v39, 32, v14\n\t"
    "ds_read_b64_tr_b16 v[32:33], v14 offset:24576\n\t"
    "ds_read_b64_tr_b16 v[34:35], v14 offset:28672\n\t"
    "ds_read_b64_tr_b16 v[36:37], v39 offset:24576\n\t"
    "ds_read_b64_tr_b16 v[38:39], v39 offset:28672\n\t"
    "s_waitcnt lgkmcnt(6)\n\t"
    "v_mfma_f32_16x16x32_bf16 v[24:27], v[40:43], v[56:59], v[24:27]\n\t"
    "v_mfma_f32_16x16x32_bf16 v[28:31], v[44:47], v[56:59], v[28:31]\n\t"
    "s_waitcnt lgkmcnt(4)\n\t"
    "v_mfma_f32_16x16x32_bf16 v[24:27], v[48:51], v[60:63], v[24:27]\n\t"
    "v_mfma_f32_16x16x32_bf16 v[28:31], v[52:55], v[60:63], v[28:31]\n\t"
    "v_mov_b32 v52, %[one]\n\t"
    "v_mov_b32 v53, %[one]\n\t"
    "v_mov_b32 v54, %[one]\n\t"
    "v_mov_b32 v55, %[one]\n\t"
    "v_mov_b32 v45, 0\n\t"
    "v_mov_b32 v47, 0\n\t"
    "v_mov_b32 v48, %[one]\n\t"
    "v_max3_f32 v14, |v24|, |v25|, |v26|\n\t"
    "v_max3_f32 v40, |v27|, |v28|, |v29|\n\t"
    "v_max3_f32 v14, v14, |v30|, |v31|\n\t"
    "v_max_f32 v14, v14, v40\n\t"
    "v_cmp_nge_f32 vcc, %[big], v14\n\t"
    "v_mov_b32 v14, 0\n\t"
    "s_cbranch_vccnz 12f\n\t"
    "13:\n\t"
    "v_lshrrev_b32 v46, 2, v5\n\t"
    "global_load_dword v44, v46, %[v2] sc1\n\t"
    "global_load_dword v46, v46, %[v2] offset:256 sc1\n\t"
    "v_exp_f32 v24, v24\n\t"
    "v_exp_f32 v25, v25\n\t"
    "v_exp_f32 v26, v26\n\t"
    "v_exp_f32 v27, v27\n\t"
    "v_exp_f32 v28, v28\n\t"
    "v_exp_f32 v29, v29\n\t"
    "v_exp_f32 v30, v30\n\t"
    "v_exp_f32 v31, v31\n\t"
    "v_cvt_pk_bf16_f32 v40, v24, v25\n\t"
    "v_cvt_pk_bf16_f32 v41, v26, v27\n\t"
    "v_cvt_pk_bf16_f32 v42, v28, v29\n\t"
    "v_cvt_pk_bf16_f32 v43, v30, v31\n\t"
    "v_mov_b32 v49, 0\n\t"
    "v_mov_b32 v51, 0\n\t"
    "s_waitcnt lgkmcnt(0)\n\t"
    "v_mfma_f32_16x16x32_bf16 v[52:55], v[52:55], v[40:43], 0\n\t"
    "v_mfma_f32_16x16x32_bf16 v[24:27], v[32:35], v[40:43], 0\n\t"
    "v_mfma_f32_16x16x32_bf16 v[28:31], v[36:39], v[40:43], 0\n\t"
    "s_waitcnt vmcnt(3)\n\t"
    "5:\n\t"
    "v_mfma_f32_16x16x32_bf16 v[32:35], v[6:9], v[16:19], 0\n\t"
    "v_mfma_f32_16x16x32_bf16 v[36:39], v[6:9], v[20:23], 0\n\t"
    "s_waitcnt vmcnt(2)\n\t"
    "v_mfma_f32_16x16x32_bf16 v[32:35], v[10:13], v[56:59], v[32:35]\n\t"
    "v_mfma_f32_16x16x32_bf16 v[36:39], v[10:13], v[60:63], v[36:39]\n\t"
    "v_max3_u32 v40, v6, v7, v8\n\t"
    "v_max3_u32 v41, v9, v10, v11\n\t"
    "v_max3_u32 v40, v40, v12, v13\n\t"
    "v_max_u32 v40, v40, v41\n\t"
    "v_cmp_eq_u32 vcc, -1, v40\n\t"
    "s_cbranch_vccnz 4f\n\t"
    "6:\n\t"
    "s_nop 0\n\t"
    "s_nop 0\n\t"
    "v_add_f32 v32, v32, v38\n\t"
    "v_add_f32 v33, v33, v39\n\t"
    "v_exp_f32 v34, v32\n\t"
    "v_max_f32 v40, v32, v33\n\t"
    "v_exp_f32 v35, v33\n\t"
    "v_cmp_nge_f32 vcc, %[bg], v40\n\t"
    "s_cbranch_vccnz 10f\n\t"
    "11:\n\t"
    "v_cvt_pk_bf16_f32 v50, v34, v35\n\t"
    "17:\n\t"
    "s_waitcnt vmcnt(0) lgkmcnt(0)\n\t"
    "v_max_u32 v40, v44, v46\n\t"
    "v_cmp_eq_u32 vcc, -1, v40\n\t"
    "s_cbranch_vccnz 18f\n\t"
    "19:\n\t"
    "v_mfma_f32_16x16x16_bf16 v[52:55], v[48:49], v[50:51], v[52:55]\n\t"
    "v_mfma_f32_16x16x16_bf16 v[24:27], v[44:45], v[50:51], v[24:27]\n\t"
    "v_mfma_f32_16x16x16_bf16 v[28:31], v[46:47], v[50:51], v[28:31]\n\t"
    "s_nop 5\n\t"
    "v_rcp_f32 v42, v52\n\t"
    "s_nop 0\n\t"
    "v_pk_mul_f32 v[24:25], v[24:25], v[42:43] op_sel_hi:[1,0]\n\t"
    "v_pk_mul_f32 v[26:27], v[26:27], v[42:43] op_sel_hi:[1,0]\n\t"
    "v_pk_mul_f32 v[28:29], v[28:29], v[42:43] op_sel_hi:[1,0]\n\t"
    "v_pk_mul_f32 v[30:31], v[30:31], v[42:43] op_sel_hi:[1,0]\n\t"
    "v_cvt_pk_bf16_f32 v36, v24, v25\n\t"
    "v_cvt_pk_bf16_f32 v37, v26, v27\n\t"
    "v_cvt_pk_bf16_f32 v38, v28, v29\n\t"
    "v_cvt_pk_bf16_f32 v39, v30, v31\n\t"
    "v_min_u32 v36, -2, v36\n\t"
    "v_min_u32 v37, -2, v37\n\t"
    "v_min_u32 v38, -2, v38\n\t"
    "v_min_u32 v39, -2, v39\n\t"
    "global_store_dwordx4 v4, v[36:39], %[od] sc1\n\t"
    "ds_write_b32 v15, v44 offset:35840\n\t"
    "ds_write_b32 v15, v46 offset:36096\n\t"
    "s_waitcnt lgkmcnt(0)\n\t"
    "s_barrier\n\t"
    "s_mov_b32 exec_hi, 0\n\t"
    "global_store_dwordx4 v5, v[10:13], %[kv] offset:1024 sc1\n\t"
    "s_mov_b64 exec, -1\n\t"
    "s_nop 0\n\t"
    "s_waitcnt lgkmcnt(0)\n\t"
    "s_branch 9f\n\t"
    "1:\n\t"
    "s_add_u32 %[sp], %[sp], 1\n\t"
    "s_cmp_ge_u32 %[sp], %[cap]\n\t"
    "s_cbranch_scc1 7f\n\t"
    "global_load_dwordx4 v[24:27], v5, %[ag] sc1\n\t"
    "global_load_dwordx4 v[28:31], v5, %[ag] offset:1024 sc1\n\t"
    "s_waitcnt lgkmcnt(0)\n\t"
    "s_memtime %[tc]\n\t"
    "s_nop 7\n\t"
    "s_branch 3b\n\t"
    "7:\n\t"
    "s_mov_b32 %[cap], 0\n\t"
    "s_branch 2b\n\t"
    "4:\n\t"
    "s_add_u32 %[sp], %[sp], 0x10000\n\t"
    "s_lshr_b32 %[st], %[sp], 16\n\t"
    "s_cmp_ge_u32 %[st], %[cap]\n\t"
    "s_cbranch_scc1 8f\n\t"
    "global_load_dwordx4 v[6:9], v5, %[vs] sc1\n\t"
    "global_load_dwordx4 v[10:13], v5, %[vs] offset:1024 sc1\n\t"
    "s_waitcnt vmcnt(0) lgkmcnt(0)\n\t"
    "s_branch 5b\n\t"
    "8:\n\t"
    "s_mov_b32 %[cap], 0\n\t"
    "s_branch 6b\n\t"
    "18:\n\t"
    "s_add_u32 %[sp], %[sp], 0x10000\n\t"
    "s_lshr_b32 %[st], %[sp], 16\n\t"
    "s_cmp_ge_u32 %[st], %[cap]\n\t"
    "s_cbranch_scc1 20f\n\t"
    "v_lshrrev_b32 v46, 2, v5\n\t"
    "global_load_dword v44, v46, %[v2] sc1\n\t"
    "global_load_dword v46, v46, %[v2] offset:256 sc1\n\t"
    "s_waitcnt vmcnt(0) lgkmcnt(0)\n\t"
    "s_branch 17b\n\t"
    "20:\n\t"
    "s_mov_b32 %[cap], 0\n\t"
    "s_branch 19b\n\t"
    "22:\n\t"
    "ds_read_b32 v8, v15 offset:45568\n\t"
    "ds_read_b128 v[32:35], v5 offset:43008\n\t"
    "s_waitcnt lgkmcnt(0)\n\t"
    "s_branch 21b\n\t"
    "10:\n\t"
    "v_max_f32 v40, v40, v14\n\t"
    "v_mov_b32 v42, v40\n\t"
    "s_nop 1\n\t"
    "v_permlane16_swap_b32 v40, v42\n\t"
    "v_max_f32 v40, v40, v42\n\t"
    "v_mov_b32 v42, v40\n\t"
    "s_nop 1\n\t"
    "v_permlane32_swap_b32 v40, v42\n\t"
    "v_max_f32 v40, v40, v42\n\t"
    "v_sub_f32 v34, v32, v40\n\t"
    "v_sub_f32 v35, v33, v40\n\t"
    "v_sub_f32 v40, v14, v40\n\t"
    "v_exp_f32 v40, v40\n\t"
    "v_exp_f32 v34, v34\n\t"
    "v_exp_f32 v35, v35\n\t"
    "v_mul_f32 v24, v24, v40\n\t"
    "v_mul_f32 v25, v25, v40\n\t"
    "v_mul_f32 v26, v26, v40\n\t"
    "v_mul_f32 v27, v27, v40\n\t"
    "v_mul_f32 v28, v28, v40\n\t"
    "v_mul_f32 v29, v29, v40\n\t"
    "v_mul_f32 v30, v30, v40\n\t"
    "v_mul_f32 v31, v31, v40\n\t"
    "v_mul_f32 v52, v52, v40\n\t"
    "v_mul_f32 v53, v53, v40\n\t"
    "v_mul_f32 v54, v54, v40\n\t"
    "v_mul_f32 v55, v55, v40\n\t"
    "s_branch 11b\n\t"
    "12:\n\t"
    "v_max3_f32 v14, v24, v25, v26\n\t"
    "v_max3_f32 v40, v27, v28, v29\n\t"
    "v_max3_f32 v14, v14, v30, v31\n\t"
    "v_max3_f32 v14, v14, v40, %[neg]\n\t"
    "v_mov_b32 v40, v14\n\t"
    "s_nop 1\n\t"
    "v_permlane16_swap_b32 v14, v40\n\t"
    "v_max_f32 v14, v14, v40\n\t"
    "v_mov_b32 v40, v14\n\t"
    "s_nop 1\n\t"
    "v_permlane32_swap_b32 v14, v40\n\t"
    "v_max_f32 v14, v14, v40\n\t"
    "v_sub_f32 v24, v24, v14\n\t"
    "v_sub_f32 v25, v25, v14\n\t"
    "v_sub_f32 v26, v26, v14\n\t"
    "v_sub_f32 v27, v27, v14\n\t"
    "v_sub_f32 v28, v28, v14\n\t"
    "v_sub_f32 v29, v29, v14\n\t"
    "v_sub_f32 v30, v30, v14\n\t"
    "v_sub_f32 v31, v31, v14\n\t"
    "s_mov_b32 %[bg], 0x7fc00000\n\t"
    "s_branch 13b\n\t"
    "9:"
    : [tc] "=&s"(tc), [sp] "=&s"(sp), [cap] "+s"(cap), [st] "=&s"(st), [bg] "=&s"(bg)
    : [ag] "s"(ag), [c44] "s"(0x4444u), [lsc] "s"(LSC), [sn] "s"(sn), [kv] "s"(kv), [vs] "s"(vs), [od] "s"(od), [neg] "s"(-1.0e38f), [one] "s"(0x3F803F80u), [big] "s"(24.0f), [v2] "s"(v2), "{v[64:95]}"(W.w0), "{v[96:127]}"(W.w1), "{v[128:159]}"(W.w2), "{v[160:191]}"(W.w3), "{v[192:223]}"(W.w4), "{v[224:255]}"(W.w5)
    : "v4", "v5", "v6", "v7", "v8", "v9", "v10", "v11", "v12", "v13", "v14", "v15", "v16", "v17", "v18", "v19", "v20", "v21", "v22", "v23", "v24", "v25", "v26", "v27", "v28", "v29", "v30", "v31", "v32", "v33", "v34", "v35", "v36", "v37", "v38", "v39", "v40", "v41", "v42", "v43", "v44", "v45", "v46", "v47", "v48", "v49", "v50", "v51", "v52", "v53", "v54", "v55", "v56", "v57", "v58", "v59", "v60", "v61", "v62", "v63", "vcc", "scc", "memory");
}
template <> __device__ __forceinline__ void qk_step<3>(const QKW& W, const char* ag, const char* vs, const char* v2, const char* od, const char* kv, unsigned sn, unsigned& cap, unsigned& sp,
    unsigned long long& tc, unsigned long long* ts, unsigned& d0, unsigned& d1) {
  unsigned st, bg;
  asm volatile("v_mbcnt_lo_u32_b32 v60, -1, 0\n\t"
    "v_mbcnt_hi_u32_b32 v60, -1, v60\n\t"
    "v_lshlrev_b32 v5, 4, v60\n\t"
    "s_mov_b32 %[sp], 0\n\t"
    "s_setprio 2\n\t"
    "global_load_dwordx4 v[24:27], v5, %[ag] sc1\n\t"
    "global_load_dwordx4 v[28:31], v5, %[ag] offset:1024 sc1\n\t"
    "s_memtime %[tc]\n\t"
    "v_lshrrev_b32 v32, 4, v60\n\t"
    "v_and_b32 v33, 15, v60\n\t"
    "v_lshrrev_b32 v34, 2, v33\n\t"
    "v_and_b32 v35, 3, v60\n\t"
    "v_and_b32 v61, 1, v60\n\t"
    "v_lshlrev_b32 v61, 3, v61\n\t"
    "v_lshl_or_b32 v7, v34, 4, v61\n\t"
    "v_lshl_or_b32 v7, v32, 7, v7\n\t"
    "v_xor_b32 v10, v32, v33\n\t"
    "v_lshlrev_b32 v10, 4, v10\n\t"
    "v_lshl_or_b32 v10, v33, 8, v10\n\t"
    "v_xor_b32 v11, 0x40, v10\n\t"
    "v_xor_b32 v12, 0x80, v10\n\t"
    "v_xor_b32 v13, 0xc0, v10\n\t"
    "v_lshl_or_b32 v34, v32, 2, v34\n\t"
    "v_lshrrev_b32 v35, 1, v35\n\t"
    "v_or_b32 v35, 12, v35\n\t"
    "v_xor_b32 v35, v35, v34\n\t"
    "v_lshl_or_b32 v14, v35, 4, v61\n\t"
    "v_lshl_or_b32 v14, v34, 8, v14\n\t"
    "v_and_b32 v61, 3, v60\n\t"
    "v_lshlrev_b32 v61, 6, v61\n\t"
    "v_lshl_or_b32 v4, v32, 4, v61\n\t"
    "v_and_b32 v61, 4, v60\n\t"
    "v_lshl_or_b32 v4, v61, 10, v4\n\t"
    "v_and_b32 v61, 8, v60\n\t"
    "v_lshl_or_b32 v4, v61, 14, v4\n\t"
    "v_or_b32 v4, 768, v4\n\t"
    "v_and_b32 v61, 2, v60\n\t"
    "v_mul_u32_u24 v61, 0x5555, v61\n\t"
    "v_or_b32 v6, %[c44], v61\n\t"
    "v_lshlrev_b32 v61, 2, v32\n\t"
    "v_lshl_or_b32 v15, v33, 4, v61\n\t"
    "3:\n\t"
    "v_mov_b32 v16, 0\n\t"
    "v_mov_b32 v17, 0\n\t"
    "v_mov_b32 v18, 0\n\t"
    "v_mov_b32 v19, 0\n\t"
    "v_mov_b32 v20, 0\n\t"
    "v_mov_b32 v21, 0\n\t"
    "v_mov_b32 v22, 0\n\t"
    "v_mov_b32 v23, 0\n\t"
    "s_waitcnt vmcnt(0) lgkmcnt(0)\n\t"
    "ds_write_b128 v5, v[24:27] offset:6144\n\t"
    "ds_write_b128 v5, v[28:31] offset:7168\n\t"
    "v_smfmac_f32_16x16x64_bf16 v[16:19], v[24:27], v[160:167], v6\n\t"
    "v_max3_u32 v60, v24, v25, v26\n\t"
    "v_max3_u32 v61, v27, v28, v29\n\t"
    "v_smfmac_f32_16x16x64_bf16 v[20:23], v[24:27], v[168:175], v6\n\t"
    "v_max3_u32 v60, v60, v30, v31\n\t"
    "v_max_u32 v60, v60, v61\n\t"
    "v_cmp_eq_u32 vcc, -1, v60\n\t"
    "s_cbranch_vccnz 1f\n\t"
    "2:\n\t"
    "s_waitcnt lgkmcnt(0)\n\t"
    "s_barrier\n\t"
    "ds_read_b128 v[32:35], v5 offset:0\n\t"
    "ds_read_b128 v[36:39], v5 offset:1024\n\t"
    "v_smfmac_f32_16x16x64_bf16 v[16:19], v[28:31], v[176:183], v6\n\t"
    "ds_read_b128 v[40:43], v5 offset:2048\n\t"
    "ds_read_b128 v[44:47], v5 offset:3072\n\t"
    "v_smfmac_f32_16x16x64_bf16 v[20:23], v[28:31], v[184:191], v6\n\t"
    "s_waitcnt lgkmcnt(3)\n\t"
    "v_smfmac_f32_16x16x64_bf16 v[16:19], v[32:35], v[64:71], v6\n\t"
    "ds_read_b128 v[48:51], v5 offset:4096\n\t"
    "v_smfmac_f32_16x16x64_bf16 v[20:23], v[32:35], v[72:79], v6\n\t"
    "ds_read_b128 v[52:55], v5 offset:5120\n\t"
    "s_waitcnt lgkmcnt(4)\n\t"
    "v_smfmac_f32_16x16x64_bf16 v[16:19], v[36:39], v[80:87], v6\n\t"
    "ds_read_b128 v[24:27], v5 offset:8192\n\t"
    "v_smfmac_f32_16x16x64_bf16 v[20:23], v[36:39], v[88:95], v6\n\t"
    "ds_read_b128 v[28:31], v5 offset:9216\n\t"
    "s_waitcnt lgkmcnt(5)\n\t"
    "v_smfmac_f32_16x16x64_bf16 v[16:19], v[40:43], v[96:103], v6\n\t"
    "ds_read_b128 v[56:59], v5 offset:10240\n\t"
    "v_smfmac_f32_16x16x64_bf16 v[20:23], v[40:43], v[104:111], v6\n\t"
    "ds_read_b128 v[60:63], v5 offset:11264\n\t"
    "ds_read_b128 v[36:39], v11 offset:20480\n\t"
    "s_waitcnt lgkmcnt(7)\n\t"
    "v_smfmac_f32_16x16x64_bf16 v[16:19], v[44:47], v[112:119], v6\n\t"
    "ds_read_b128 v[40:43], v12 offset:16384\n\t"
    "v_smfmac_f32_16x16x64_bf16 v[20:23], v[44:47], v[120:127], v6\n\t"
    "ds_read_b128 v[44:47], v12 offset:20480\n\t"
    "s_waitcnt lgkmcnt(8)\n\t"
    "v_smfmac_f32_16x16x64_bf16 v[16:19], v[48:51], v[128:135], v6\n\t"
    "v_smfmac_f32_16x16x64_bf16 v[20:23], v[48:51], v[136:143], v6\n\t"
    "ds_read_b128 v[48:51], v13 offset:16384\n\t"
    "s_waitcnt lgkmcnt(8)\n\t"
    "v_smfmac_f32_16x16x64_bf16 v[16:19], v[52:55], v[144:151], v6\n\t"
    "v_smfmac_f32_16x16x64_bf16 v[20:23], v[52:55], v[152:159], v6\n\t"
    "ds_read_b128 v[52:55], v13 offset:20480\n\t"
    "s_waitcnt lgkmcnt(8)\n\t"
    "v_smfmac_f32_16x16x64_bf16 v[16:19], v[24:27], v[192:199], v6\n\t"
    "ds_read_b32 v8, v15 offset:45824\n\t"
    "ds_read_b128 v[32:35], v5 offset:44032\n\t"
    "s_setprio 3\n\t"
    "v_smfmac_f32_16x16x64_bf16 v[20:23], v[24:27], v[200:207], v6\n\t"
    "ds_read_b128 v[24:27], v10 offset:16384\n\t"
    "s_waitcnt lgkmcnt(10)\n\t"
    "v_smfmac_f32_16x16x64_bf16 v[16:19], v[28:31], v[208:215], v6\n\t"
    "s_waitcnt lgkmcnt(9)\n\t"
    "v_smfmac_f32_16x16x64_bf16 v[16:19], v[56:59], v[224:231], v6\n\t"
    "s_waitcnt lgkmcnt(8)\n\t"
    "v_smfmac_f32_16x16x64_bf16 v[16:19], v[60:63], v[240:247], v6\n\t"
    "v_smfmac_f32_16x16x64_bf16 v[20:23], v[28:31], v[216:223], v6\n\t"
    "s_waitcnt lgkmcnt(1)\n\t"
    "21:\n\t"
    "v_cmp_ne_u32 vcc, %[sn], v8\n\t"
    "s_cbranch_vccnz 22f\n\t"
    "v_mul_f32 v32, %[lsc], v32\n\t"
    "v_mul_f32 v33, %[lsc], v33\n\t"
    "v_smfmac_f32_16x16x64_bf16 v[20:23], v[56:59], v[232:239], v6\n\t"
    "v_mul_f32 v34, %[lsc], v34\n\t"
    "v_mul_f32 v35, %[lsc], v35\n\t"
    "v_add_f32 v16, v16, v18\n\t"
    "v_add_f32 v17, v17, v19\n\t"
    "v_smfmac_f32_16x16x64_bf16 v[20:23], v[60:63], v[248:255], v6\n\t"
    "v_fma_f32 v16, v16, %[lsc], v32\n\t"
    "v_fma_f32 v17, v17, %[lsc], v33\n\t"
    "v_cvt_pk_bf16_f32 v16, v16, v17\n\t"
    "ds_write_b32 v15, v16 offset:34304\n\t"
    "s_mov_b32 %[bg], %[big]\n\t"
    "s_nop 2\n\t"
    "v_add_f32 v20, v20, v22\n\t"
    "v_add_f32 v21, v21, v23\n\t"
    "v_fma_f32 v20, v20, %[lsc], v34\n\t"
    "v_fma_f32 v21, v21, %[lsc], v35\n\t"
    "v_cvt_pk_bf16_f32 v20, v20, v21\n\t"
    "ds_write_b32 v15, v20 offset:34560\n\t"
    "ds_read_b128 v[28:31], v10 offset:20480\n\t"
    "ds_read_b128 v[32:35], v11 offset:16384\n\t"
    "s_waitcnt lgkmcnt(2)\n\t"
    "s_barrier\n\t"
    "ds_read_b64_tr_b16 v[16:17], v7 offset:32768\n\t"
    "ds_read_b64_tr_b16 v[18:19], v7 offset:32832\n\t"
    "ds_read_b64_tr_b16 v[20:21], v7 offset:33280\n\t"
    "ds_read_b64_tr_b16 v[22:23], v7 offset:33344\n\t"
    "ds_read_b64_tr_b16 v[56:57], v7 offset:33792\n\t"
    "ds_read_b64_tr_b16 v[58:59], v7 offset:33856\n\t"
    "ds_read_b64_tr_b16 v[60:61], v7 offset:34304\n\t"
    "ds_read_b64_tr_b16 v[62:63], v7 offset:34368\n\t"
    "global_load_dwordx4 v[6:9], v5, %[vs] sc1\n\t"
    "global_load_dwordx4 v[10:13], v5, %[vs] offset:1024 sc1\n\t"
    "s_waitcnt lgkmcnt(6)\n\t"
    "v_mfma_f32_16x16x32_bf16 v[24:27], v[24:27], v[16:19], 0\n\t"
    "v_mfma_f32_16x16x32_bf16 v[28:31], v[28:31], v[16:19], 0\n\t"
    "s_waitcnt lgkmcnt(4)\n\t"
    "v_mfma_f32_16x16x32_bf16 v[24:27], v[32:35], v[20:23], v[24:27]\n\t"
    "v_mfma_f32_16x16x32_bf16 v[28:31], v[36:39], v[20:23], v[28:31]\n\t"
    "v_xor_b32 v39, 32, v14\n\t"
    "ds_read_b64_tr_b16 v[32:33], v14 offset:24576\n\t"
    "ds_read_b64_tr_b16 v[34:35], v14 offset:28672\n\t"
    "ds_read_b64_tr_b16 v[36:37], v39 offset:24576\n\t"
    "ds_read_b64_tr_b16 v[38:39], v39 offset:28672\n\t"
    "s_waitcnt lgkmcnt(6)\n\t"
    "v_mfma_f32_16x16x32_bf16 v[24:27], v[40:43], v[56:59], v[24:27]\n\t"
    "v_mfma_f32_16x16x32_bf16 v[28:31], v[44:47], v[56:59], v[28:31]\n\t"
    "s_waitcnt lgkmcnt(4)\n\t"
    "v_mfma_f32_16x16x32_bf16 v[24:27], v[48:51], v[60:63], v[24:27]\n\t"
    "v_mfma_f32_16x16x32_bf16 v[28:31], v[52:55], v[60:63], v[28:31]\n\t"
    "v_mov_b32 v52, %[one]\n\t"
    "v_mov_b32 v53, %[one]\n\t"
    "v_mov_b32 v54, %[one]\n\t"
    "v_mov_b32 v55, %[one]\n\t"
    "v_mov_b32 v45, 0\n\t"
    "v_mov_b32 v47, 0\n\t"
    "v_mov_b32 v48, %[one]\n\t"
    "v_max3_f32 v14, |v24|, |v25|, |v26|\n\t"
    "v_max3_f32 v40, |v27|, |v28|, |v29|\n\t"
    "v_max3_f32 v14, v14, |v30|, |v31|\n\t"
    "v_max_f32 v14, v14, v40\n\t"
    "v_cmp_nge_f32 vcc, %[big], v14\n\t"
    "v_mov_b32 v14, 0\n\t"
    "s_cbranch_vccnz 12f\n\t"
    "13:\n\t"
    "v_lshrrev_b32 v46, 2, v5\n\t"
    "global_load_dword v44, v46, %[v2] sc1\n\t"
    "global_load_dword v46, v46, %[v2] offset:256 sc1\n\t"
    "v_exp_f32 v24, v24\n\t"
    "v_exp_f32 v25, v25\n\t"
    "v_exp_f32 v26, v26\n\t"
    "v_exp_f32 v27, v27\n\t"
    "v_exp_f32 v28, v28\n\t"
    "v_exp_f32 v29, v29\n\t"
    "v_exp_f32 v30, v30\n\t"
    "v_exp_f32 v31, v31\n\t"
    "v_cvt_pk_bf16_f32 v40, v24, v25\n\t"
    "v_cvt_pk_bf16_f32 v41, v26, v27\n\t"
    "v_cvt_pk_bf16_f32 v42, v28, v29\n\t"
    "v_cvt_pk_bf16_f32 v43, v30, v31\n\t"
    "v_mov_b32 v49, 0\n\t"
    "v_mov_b32 v51, 0\n\t"
    "s_waitcnt lgkmcnt(0)\n\t"
    "v_mfma_f32_16x16x32_bf16 v[52:55], v[52:55], v[40:43], 0\n\t"
    "v_mfma_f32_16x16x32_bf16 v[24:27], v[32:35], v[40:43], 0\n\t"
    "v_mfma_f32_16x16x32_bf16 v[28:31], v[36:39], v[40:43], 0\n\t"
    "s_waitcnt vmcnt(3)\n\t"
    "5:\n\t"
    "v_mfma_f32_16x16x32_bf16 v[32:35], v[6:9], v[16:19], 0\n\t"
    "v_mfma_f32_16x16x32_bf16 v[36:39], v[6:9], v[20:23], 0\n\t"
    "s_waitcnt vmcnt(2)\n\t"
    "v_mfma_f32_16x16x32_bf16 v[32:35], v[10:13], v[56:59], v[32:35]\n\t"
    "v_mfma_f32_16x16x32_bf16 v[36:39], v[10:13], v[60:63], v[36:39]\n\t"
    "v_max3_u32 v40, v6, v7, v8\n\t"
    "v_max3_u32 v41, v9, v10, v11\n\t"
    "v_max3_u32 v40, v40, v12, v13\n\t"
    "v_max_u32 v40, v40, v41\n\t"
    "v_cmp_eq_u32 vcc, -1, v40\n\t"
    "s_cbranch_vccnz 4f\n\t"
    "6:\n\t"
    "s_nop 0\n\t"
    "s_nop 0\n\t"
    "v_add_f32 v32, v32, v38\n\t"
    "v_add_f32 v33, v33, v39\n\t"
    "v_exp_f32 v34, v32\n\t"
    "v_max_f32 v40, v32, v33\n\t"
    "v_exp_f32 v35, v33\n\t"
    "v_cmp_nge_f32 vcc, %[bg], v40\n\t"
    "s_cbranch_vccnz 10f\n\t"
    "11:\n\t"
    "v_cvt_pk_bf16_f32 v50, v34, v35\n\t"
    "17:\n\t"
    "s_waitcnt vmcnt(0) lgkmcnt(0)\n\t"
    "v_max_u32 v40, v44, v46\n\t"
    "v_cmp_eq_u32 vcc, -1, v40\n\t"
    "s_cbranch_vccnz 18f\n\t"
    "19:\n\t"
    "v_mfma_f32_16x16x16_bf16 v[52:55], v[48:49], v[50:51], v[52:55]\n\t"
    "v_mfma_f32_16x16x16_bf16 v[24:27], v[44:45], v[50:51], v[24:27]\n\t"
    "v_mfma_f32_16x16x16_bf16 v[28:31], v[46:47], v[50:51], v[28:31]\n\t"
    "s_nop 5\n\t"
    "v_rcp_f32 v42, v52\n\t"
    "s_nop 0\n\t"
    "v_pk_mul_f32 v[24:25], v[24:25], v[42:43] op_sel_hi:[1,0]\n\t"
    "v_pk_mul_f32 v[26:27], v[26:27], v[42:43] op_sel_hi:[1,0]\n\t"
    "v_pk_mul_f32 v[28:29], v[28:29], v[42:43] op_sel_hi:[1,0]\n\t"
    "v_pk_mul_f32 v[30:31], v[30:31], v[42:43] op_sel_hi:[1,0]\n\t"
    "v_cvt_pk_bf16_f32 v36, v24, v25\n\t"
    "v_cvt_pk_bf16_f32 v37, v26, v27\n\t"
    "v_cvt_pk_bf16_f32 v38, v28, v29\n\t"
    "v_cvt_pk_bf16_f32 v39, v30, v31\n\t"
    "v_min_u32 v36, -2, v36\n\t"
    "v_min_u32 v37, -2, v37\n\t"
    "v_min_u32 v38, -2, v38\n\t"
    "v_min_u32 v39, -2, v39\n\t"
    "global_store_dwordx4 v4, v[36:39], %[od] sc1\n\t"
    "ds_write_b32 v15, v44 offset:36352\n\t"
    "ds_write_b32 v15, v46 offset:36608\n\t"
    "s_waitcnt lgkmcnt(0)\n\t"
    "s_barrier\n\t"
    "s_mov_b32 exec_lo, 0\n\t"
    "global_store_dwordx4 v5, v[10:13], %[kv] offset:1024 sc1\n\t"
    "s_mov_b64 exec, -1\n\t"
    "s_nop 0\n\t"
    "s_waitcnt lgkmcnt(0)\n\t"
    "s_branch 9f\n\t"
    "1:\n\t"
    "s_add_u32 %[sp], %[sp], 1\n\t"
    "s_cmp_ge_u32 %[sp], %[cap]\n\t"
    "s_cbranch_scc1 7f\n\t"
    "global_load_dwordx4 v[24:27], v5, %[ag] sc1\n\t"
    "global_load_dwordx4 v[28:31], v5, %[ag] offset:1024 sc1\n\t"
    "s_waitcnt lgkmcnt(0)\n\t"
    "s_memtime %[tc]\n\t"
    "s_nop 7\n\t"
    "s_branch 3b\n\t"
    "7:\n\t"
    "s_mov_b32 %[cap], 0\n\t"
    "s_branch 2b\n\t"
    "4:\n\t"
    "s_add_u32 %[sp], %[sp], 0x10000\n\t"
    "s_lshr_b32 %[st], %[sp], 16\n\t"
    "s_cmp_ge_u32 %[st], %[cap]\n\t"
    "s_cbranch_scc1 8f\n\t"
    "global_load_dwordx4 v[6:9], v5, %[vs] sc1\n\t"
    "global_load_dwordx4 v[10:13], v5, %[vs] offset:1024 sc1\n\t"
    "s_waitcnt vmcnt(0) lgkmcnt(0)\n\t"
    "s_branch 5b\n\t"
    "8:\n\t"
    "s_mov_b32 %[cap], 0\n\t"
    "s_branch 6b\n\t"
    "18:\n\t"
    "s_add_u32 %[sp], %[sp], 0x10000\n\t"
    "s_lshr_b32 %[st], %[sp], 16\n\t"
    "s_cmp_ge_u32 %[st], %[cap]\n\t"
    "s_cbranch_scc1 20f\n\t"
    "v_lshrrev_b32 v46, 2, v5\n\t"
    "global_load_dword v44, v46, %[v2] sc1\n\t"
    "global_load_dword v46, v46, %[v2] offset:256 sc1\n\t"
    "s_waitcnt vmcnt(0) lgkmcnt(0)\n\t"
    "s_branch 17b\n\t"
    "20:\n\t"
    "s_mov_b32 %[cap], 0\n\t"
    "s_branch 19b\n\t"
    "22:\n\t"
    "ds_read_b32 v8, v15 offset:45824\n\t"
    "ds_read_b128 v[32:35], v5 offset:44032\n\t"
    "s_waitcnt lgkmcnt(0)\n\t"
    "s_branch 21b\n\t"
    "10:\n\t"
    "v_max_f32 v40, v40, v14\n\t"
    "v_mov_b32 v42, v40\n\t"
    "s_nop 1\n\t"
    "v_permlane16_swap_b32 v40, v42\n\t"
    "v_max_f32 v40, v40, v42\n\t"
    "v_mov_b32 v42, v40\n\t"
    "s_nop 1\n\t"
    "v_permlane32_swap_b32 v40, v42\n\t"
    "v_max_f32 v40, v40, v42\n\t"
    "v_sub_f32 v34, v32, v40\n\t"
    "v_sub_f32 v35, v33, v40\n\t"
    "v_sub_f32 v40, v14, v40\n\t"
    "v_exp_f32 v40, v40\n\t"
    "v_exp_f32 v34, v34\n\t"
    "v_exp_f32 v35, v35\n\t"
    "v_mul_f32 v24, v24, v40\n\t"
    "v_mul_f32 v25, v25, v40\n\t"
    "v_mul_f32 v26, v26, v40\n\t"
    "v_mul_f32 v27, v27, v40\n\t"
    "v_mul_f32 v28, v28, v40\n\t"
    "v_mul_f32 v29, v29, v40\n\t"
    "v_mul_f32 v30, v30, v40\n\t"
    "v_mul_f32 v31, v31, v40\n\t"
    "v_mul_f32 v52, v52, v40\n\t"
    "v_mul_f32 v53, v53, v40\n\t"
    "v_mul_f32 v54, v54, v40\n\t"
    "v_mul_f32 v55, v55, v40\n\t"
    "s_branch 11b\n\t"
    "12:\n\t"
    "v_max3_f32 v14, v24, v25, v26\n\t"
    "v_max3_f32 v40, v27, v28, v29\n\t"
    "v_max3_f32 v14, v14, v30, v31\n\t"
    "v_max3_f32 v14, v14, v40, %[neg]\n\t"
    "v_mov_b32 v40, v14\n\t"
    "s_nop 1\n\t"
    "v_permlane16_swap_b32 v14, v40\n\t"
    "v_max_f32 v14, v14, v40\n\t"
    "v_mov_b32 v40, v14\n\t"
    "s_nop 1\n\t"
    "v_permlane32_swap_b32 v14, v40\n\t"
    "v_max_f32 v14, v14, v40\n\t"
    "v_sub_f32 v24, v24, v14\n\t"
    "v_sub_f32 v25, v25, v14\n\t"
    "v_sub_f32 v26, v26, v14\n\t"
    "v_sub_f32 v27, v27, v14\n\t"
    "v_sub_f32 v28, v28, v14\n\t"
    "v_sub_f32 v29, v29, v14\n\t"
    "v_sub_f32 v30, v30, v14\n\t"
    "v_sub_f32 v31, v31, v14\n\t"
    "s_mov_b32 %[bg], 0x7fc00000\n\t"
    "s_branch 13b\n\t"
    "9:"
    : [tc] "=&s"(tc), [sp] "=&s"(sp), [cap] "+s"(cap), [st] "=&s"(st), [bg] "=&s"(bg)
    : [ag] "s"(ag), [c44] "s"(0x4444u), [lsc] "s"(LSC), [sn] "s"(sn), [kv] "s"(kv), [vs] "s"(vs), [od] "s"(od), [neg] "s"(-1.0e38f), [one] "s"(0x3F803F80u), [big] "s"(24.0f), [v2] "s"(v2), "{v[64:95]}"(W.w0), "{v[96:127]}"(W.w1), "{v[128:159]}"(W.w2), "{v[160:191]}"(W.w3), "{v[192:223]}"(W.w4), "{v[224:255]}"(W.w5)
    : "v4", "v5", "v6", "v7", "v8", "v9", "v10", "v11", "v12", "v13", "v14", "v15", "v16", "v17", "v18", "v19", "v20", "v21", "v22", "v23", "v24", "v25", "v26", "v27", "v28", "v29", "v30", "v31", "v32", "v33", "v34", "v35", "v36", "v37", "v38", "v39", "v40", "v41", "v42", "v43", "v44", "v45", "v46", "v47", "v48", "v49", "v50", "v51", "v52", "v53", "v54", "v55", "v56", "v57", "v58", "v59", "v60", "v61", "v62", "v63", "vcc", "scc", "memory");
}
template <> __device__ __forceinline__ void qk_step<4>(const QKW& W, const char* ag, const char* vs, const char* v2, const char* od, const char* kv, unsigned sn, unsigned& cap, unsigned& sp,
    unsigned long long& tc, unsigned long long* ts, unsigned& d0, unsigned& d1) {
  unsigned st, bg;
  asm volatile("v_mbcnt_lo_u32_b32 v60, -1, 0\n\t"
    "v_mbcnt_hi_u32_b32 v60, -1, v60\n\t"
    "v_lshlrev_b32 v6, 4, v60\n\t"
    "s_mov_b32 %[sp], 0\n\t"
    "s_setprio 3\n\t"
    "global_load_dwordx4 v[24:27], v6, %[ag] sc1\n\t"
    "global_load_dwordx4 v[28:31], v6, %[ag] offset:-4096 sc1\n\t"
    "s_memtime %[tc]\n\t"
    "v_lshrrev_b32 v56, 4, v60\n\t"
    "v_and_b32 v57, 15, v60\n\t"
    "v_mov_b32 v20, 0\n\t"
    "v_mov_b32 v21, 0\n\t"
    "v_mov_b32 v22, 0\n\t"
    "v_mov_b32 v23, 0\n\t"
    "v_mov_b32 v11, %[sn]\n\t"
    "v_and_b32 v61, 2, v60\n\t"
    "v_mul_u32_u24 v61, 0x5555, v61\n\t"
    "v_or_b32 v8, %[c44], v61\n\t"
    "v_lshlrev_b32 v61, 2, v56\n\t"
    "v_lshl_or_b32 v9, v57, 4, v61\n\t"
    "3:\n\t"
    "v_mov_b32 v12, 0\n\t"
    "v_mov_b32 v13, 0\n\t"
    "v_mov_b32 v14, 0\n\t"
    "v_mov_b32 v15, 0\n\t"
    "v_mov_b32 v16, 0\n\t"
    "v_mov_b32 v17, 0\n\t"
    "v_mov_b32 v18, 0\n\t"
    "v_mov_b32 v19, 0\n\t"
    "s_waitcnt vmcnt(0) lgkmcnt(0)\n\t"
    "ds_write_b128 v6, v[24:27] offset:12288\n\t"
    "ds_write_b128 v6, v[28:31] offset:8192\n\t"
    "v_smfmac_f32_16x16x64_bf16 v[12:15], v[24:27], v[64:71], v8\n\t"
    "v_max3_u32 v60, v24, v25, v26\n\t"
    "v_max3_u32 v61, v27, v28, v29\n\t"
    "v_smfmac_f32_16x16x64_bf16 v[16:19], v[24:27], v[72:79], v8\n\t"
    "v_max3_u32 v60, v60, v30, v31\n\t"
    "v_max_u32 v60, v60, v61\n\t"
    "v_cmp_eq_u32 vcc, -1, v60\n\t"
    "s_cbranch_vccnz 1f\n\t"
    "2:\n\t"
    "s_waitcnt lgkmcnt(0)\n\t"
    "s_barrier\n\t"
    "ds_read_b128 v[32:35], v6 offset:13312\n\t"
    "ds_read_b128 v[36:39], v6 offset:14336\n\t"
    "ds_read_b128 v[40:43], v6 offset:15360\n\t"
    "s_waitcnt lgkmcnt(2)\n\t"
    "v_smfmac_f32_16x16x64_bf16 v[12:15], v[32:35], v[80:87], v8\n\t"
    "v_smfmac_f32_16x16x64_bf16 v[16:19], v[32:35], v[88:95], v8\n\t"
    "s_waitcnt lgkmcnt(1)\n\t"
    "v_smfmac_f32_16x16x64_bf16 v[12:15], v[36:39], v[96:103], v8\n\t"
    "v_smfmac_f32_16x16x64_bf16 v[16:19], v[36:39], v[104:111], v8\n\t"
    "s_waitcnt lgkmcnt(0)\n\t"
    "v_smfmac_f32_16x16x64_bf16 v[12:15], v[40:43], v[112:119], v8\n\t"
    "v_smfmac_f32_16x16x64_bf16 v[16:19], v[40:43], v[120:127], v8\n\t"
    "s_nop 6\n\t"
    "v_add_f32 v12, v12, v14\n\t"
    "v_add_f32 v13, v13, v15\n\t"
    "v_add_f32 v14, v16, v18\n\t"
    "v_add_f32 v15, v17, v19\n\t"
    "ds_write_b128 v6, v[12:15] offset:40960\n\t"
    "ds_write_b32 v9, v11 offset:45056\n\t"
    "s_setprio 0\n\t"
    "s_memrealtime s[36:37]\n\t"
    "s_waitcnt lgkmcnt(0)\n\t"
    "s_mov_b32 s38, s36\n\t"
    "s_min_u32 s37, %[d0], %[d1]\n\t"
    "s_add_u32 s36, s36, s37\n\t"
    "s_and_b32 s36, s36, 0xffffff\n\t"
    "s_or_b32 s36, s36, %[sn]\n\t"
    "v_lshlrev_b32 v10, 10, v6\n\t"
    "v_mov_b32 v5, s36\n\t"
    "s_mov_b64 exec, 15\n\t"
    "global_store_dword v10, v5, %[vs] offset:-4096 sc1\n\t"
    "s_mov_b64 exec, -1\n\t"
    "ds_read_b128 v[44:47], v6 offset:0\n\t"
    "ds_read_b128 v[48:51], v6 offset:1024\n\t"
    "ds_read_b128 v[52:55], v6 offset:2048\n\t"
    "ds_read_b128 v[56:59], v6 offset:3072\n\t"
    "ds_read_b128 v[60:63], v6 offset:4096\n\t"
    "s_sleep 2\n\t"
    "v_smfmac_f32_16x16x64_bf16 v[20:23], v[28:31], v[192:199], v8\n\t"
    "ds_read_b128 v[28:31], v6 offset:5120\n\t"
    "v_smfmac_f32_16x16x64_bf16 v[20:23], v[24:27], v[224:231], v8\n\t"
    "ds_read_b128 v[24:27], v6 offset:6144\n\t"
    "v_smfmac_f32_16x16x64_bf16 v[20:23], v[32:35], v[240:247], v8\n\t"
    "ds_read_b128 v[32:35], v6 offset:7168\n\t"
    "v_smfmac_f32_16x16x64_bf16 v[20:23], v[36:39], v[232:239], v8\n\t"
    "ds_read_b128 v[36:39], v6 offset:9216\n\t"
    "v_smfmac_f32_16x16x64_bf16 v[20:23], v[40:43], v[248:255], v8\n\t"
    "ds_read_b128 v[40:43], v6 offset:10240\n\t"
    "s_waitcnt lgkmcnt(9)\n\t"
    "v_smfmac_f32_16x16x64_bf16 v[20:23], v[44:47], v[128:135], v8\n\t"
    "ds_read_b128 v[44:47], v6 offset:11264\n\t"
    "s_barrier\n\t"
    "s_setprio 0\n\t"
    "s_waitcnt lgkmcnt(9)\n\t"
    "v_smfmac_f32_16x16x64_bf16 v[20:23], v[48:51], v[144:151], v8\n\t"
    "s_waitcnt lgkmcnt(8)\n\t"
    "v_smfmac_f32_16x16x64_bf16 v[20:23], v[52:55], v[136:143], v8\n\t"
    "s_waitcnt lgkmcnt(7)\n\t"
    "v_smfmac_f32_16x16x64_bf16 v[20:23], v[56:59], v[152:159], v8\n\t"
    "s_waitcnt lgkmcnt(6)\n\t"
    "v_smfmac_f32_16x16x64_bf16 v[20:23], v[60:63], v[160:167], v8\n\t"
    "s_waitcnt lgkmcnt(5)\n\t"
    "v_smfmac_f32_16x16x64_bf16 v[20:23], v[28:31], v[176:183], v8\n\t"
    "s_waitcnt lgkmcnt(4)\n\t"
    "v_smfmac_f32_16x16x64_bf16 v[20:23], v[24:27], v[168:175], v8\n\t"
    "s_waitcnt lgkmcnt(3)\n\t"
    "v_smfmac_f32_16x16x64_bf16 v[20:23], v[32:35], v[184:191], v8\n\t"
    "s_waitcnt lgkmcnt(2)\n\t"
    "v_smfmac_f32_16x16x64_bf16 v[20:23], v[36:39], v[208:215], v8\n\t"
    "s_waitcnt lgkmcnt(1)\n\t"
    "v_smfmac_f32_16x16x64_bf16 v[20:23], v[40:43], v[200:207], v8\n\t"
    "s_waitcnt lgkmcnt(0)\n\t"
    "v_smfmac_f32_16x16x64_bf16 v[20:23], v[44:47], v[216:223], v8\n\t"
    "s_nop 7\n\t"
    "v_add_f32 v20, v20, v22\n\t"
    "v_add_f32 v21, v21, v23\n\t"
    "v_cvt_pk_bf16_f32 v20, v20, v21\n\t"
    "v_min_u32 v20, -2, v20\n\t"
    "ds_write_b32 v9, v20 offset:34816\n\t"
    "s_waitcnt lgkmcnt(0)\n\t"
    "s_setprio 0\n\t"
    "s_barrier\n\t"
    "s_memrealtime s[36:37]\n\t"
    "s_mov_b32 %[d1], %[d0]\n\t"
    "s_waitcnt lgkmcnt(0)\n\t"
    "s_sub_u32 %[d0], s36, s38\n\t"
    "s_min_u32 %[d0], %[d0], 200\n\t"
    "s_and_b32 s36, s36, 0xffffff\n\t"
    "s_or_b32 s36, s36, %[sn]\n\t"
    "v_lshlrev_b32 v10, 10, v6\n\t"
    "v_mov_b32 v5, s36\n\t"
    "s_mov_b64 exec, 15\n\t"
    "global_store_dword v10, v5, %[vs] offset:0 sc1\n\t"
    "s_mov_b64 exec, -1\n\t"
    "s_waitcnt lgkmcnt(0)\n\t"
    "s_branch 9f\n\t"
    "1:\n\t"
    "s_add_u32 %[sp], %[sp], 1\n\t"
    "s_cmp_ge_u32 %[sp], %[cap]\n\t"
    "s_cbranch_scc1 7f\n\t"
    "global_load_dwordx4 v[24:27], v6, %[ag] sc1\n\t"
    "global_load_dwordx4 v[28:31], v6, %[ag] offset:-4096 sc1\n\t"
    "s_waitcnt lgkmcnt(0)\n\t"
    "s_memtime %[tc]\n\t"
    "s_nop 7\n\t"
    "s_branch 3b\n\t"
    "7:\n\t"
    "s_mov_b32 %[cap], 0\n\t"
    "s_branch 2b\n\t"
    "9:"
    : [tc] "=&s"(tc), [sp] "=&s"(sp), [cap] "+s"(cap), [d0] "+s"(d0), [d1] "+s"(d1)
    : [ag] "s"(ag), [c44] "s"(0x4444u), [lsc] "s"(LSC), [sn] "s"(sn), [vs] "s"(vs), "{v[64:95]}"(W.w0), "{v[96:127]}"(W.w1), "{v[128:159]}"(W.w2), "{v[160:191]}"(W.w3), "{v[192:223]}"(W.w4), "{v[224:255]}"(W.w5)
    : "v5", "v6", "v7", "v8", "v9", "v10", "v11", "v12", "v13", "v14", "v15", "v16", "v17", "v18", "v19", "v20", "v21", "v22", "v23", "v24", "v25", "v26", "v27", "v28", "v29", "v30", "v31", "v32", "v33", "v34", "v35", "v36", "v37", "v38", "v39", "v40", "v41", "v42", "v43", "v44", "v45", "v46", "v47", "v48", "v49", "v50", "v51", "v52", "v53", "v54", "v55", "v56", "v57", "v58", "v59", "v60", "v61", "v62", "v63", "vcc", "scc", "memory", "s36", "s37", "s38");
}
template <> __device__ __forceinline__ void qk_step<5>(const QKW& W, const char* ag, const char* vs, const char* v2, const char* od, const char* kv, unsigned sn, unsigned& cap, unsigned& sp,
    unsigned long long& tc, unsigned long long* ts, unsigned& d0, unsigned& d1) {
  unsigned st, bg;
  asm volatile("v_mbcnt_lo_u32_b32 v60, -1, 0\n\t"
    "v_mbcnt_hi_u32_b32 v60, -1, v60\n\t"
    "v_lshlrev_b32 v6, 4, v60\n\t"
    "s_mov_b32 %[sp], 0\n\t"
    "s_setprio 3\n\t"
    "global_load_dwordx4 v[24:27], v6, %[ag] sc1\n\t"
    "global_load_dwordx4 v[28:31], v6, %[ag] offset:-4096 sc1\n\t"
    "s_memtime %[tc]\n\t"
    "v_lshrrev_b32 v56, 4, v60\n\t"
    "v_and_b32 v57, 15, v60\n\t"
    "v_mov_b32 v20, 0\n\t"
    "v_mov_b32 v21, 0\n\t"
    "v_mov_b32 v22, 0\n\t"
    "v_mov_b32 v23, 0\n\t"
    "v_mov_b32 v11, %[sn]\n\t"
    "v_and_b32 v61, 2, v60\n\t"
    "v_mul_u32_u24 v61, 0x5555, v61\n\t"
    "v_or_b32 v8, %[c44], v61\n\t"
    "v_lshlrev_b32 v61, 2, v56\n\t"
    "v_lshl_or_b32 v9, v57, 4, v61\n\t"
    "3:\n\t"
    "v_mov_b32 v12, 0\n\t"
    "v_mov_b32 v13, 0\n\t"
    "v_mov_b32 v14, 0\n\t"
    "v_mov_b32 v15, 0\n\t"
    "v_mov_b32 v16, 0\n\t"
    "v_mov_b32 v17, 0\n\t"
    "v_mov_b32 v18, 0\n\t"
    "v_mov_b32 v19, 0\n\t"
    "s_waitcnt vmcnt(0) lgkmcnt(0)\n\t"
    "ds_write_b128 v6, v[24:27] offset:13312\n\t"
    "ds_write_b128 v6, v[28:31] offset:9216\n\t"
    "v_smfmac_f32_16x16x64_bf16 v[12:15], v[24:27], v[80:87], v8\n\t"
    "v_max3_u32 v60, v24, v25, v26\n\t"
    "v_max3_u32 v61, v27, v28, v29\n\t"
    "v_smfmac_f32_16x16x64_bf16 v[16:19], v[24:27], v[88:95], v8\n\t"
    "v_max3_u32 v60, v60, v30, v31\n\t"
    "v_max_u32 v60, v60, v61\n\t"
    "v_cmp_eq_u32 vcc, -1, v60\n\t"
    "s_cbranch_vccnz 1f\n\t"
    "2:\n\t"
    "s_waitcnt lgkmcnt(0)\n\t"
    "s_barrier\n\t"
    "ds_read_b128 v[32:35], v6 offset:12288\n\t"
    "ds_read_b128 v[36:39], v6 offset:14336\n\t"
    "ds_read_b128 v[40:43], v6 offset:15360\n\t"
    "s_waitcnt lgkmcnt(2)\n\t"
    "v_smfmac_f32_16x16x64_bf16 v[12:15], v[32:35], v[64:71], v8\n\t"
    "v_smfmac_f32_16x16x64_bf16 v[16:19], v[32:35], v[72:79], v8\n\t"
    "s_waitcnt lgkmcnt(1)\n\t"
    "v_smfmac_f32_16x16x64_bf16 v[12:15], v[36:39], v[96:103], v8\n\t"
    "v_smfmac_f32_16x16x64_bf16 v[16:19], v[36:39], v[104:111], v8\n\t"
    "s_waitcnt lgkmcnt(0)\n\t"
    "v_smfmac_f32_16x16x64_bf16 v[12:15], v[40:43], v[112:119], v8\n\t"
    "v_smfmac_f32_16x16x64_bf16 v[16:19], v[40:43], v[120:127], v8\n\t"
    "s_nop 6\n\t"
    "v_add_f32 v12, v12, v14\n\t"
    "v_add_f32 v13, v13, v15\n\t"
    "v_add_f32 v14, v16, v18\n\t"
    "v_add_f32 v15, v17, v19\n\t"
    "ds_write_b128 v6, v[12:15] offset:41984\n\t"
    "ds_write_b32 v9, v11 offset:45312\n\t"
    "s_setprio 0\n\t"
    "ds_read_b128 v[44:47], v6 offset:0\n\t"
    "ds_read_b128 v[48:51], v6 offset:1024\n\t"
    "ds_read_b128 v[52:55], v6 offset:2048\n\t"
    "ds_read_b128 v[56:59], v6 offset:3072\n\t"
    "ds_read_b128 v[60:63], v6 offset:4096\n\t"
    "s_sleep 2\n\t"
    "v_smfmac_f32_16x16x64_bf16 v[20:23], v[28:31], v[208:215], v8\n\t"
    "ds_read_b128 v[28:31], v6 offset:5120\n\t"
    "v_smfmac_f32_16x16x64_bf16 v[20:23], v[24:27], v[240:247], v8\n\t"
    "ds_read_b128 v[24:27], v6 offset:6144\n\t"
    "v_smfmac_f32_16x16x64_bf16 v[20:23], v[32:35], v[224:231], v8\n\t"
    "ds_read_b128 v[32:35], v6 offset:7168\n\t"
    "v_smfmac_f32_16x16x64_bf16 v[20:23], v[36:39], v[232:239], v8\n\t"
    "ds_read_b128 v[36:39], v6 offset:8192\n\t"
    "v_smfmac_f32_16x16x64_bf16 v[20:23], v[40:43], v[248:255], v8\n\t"
    "ds_read_b128 v[40:43], v6 offset:10240\n\t"
    "s_waitcnt lgkmcnt(9)\n\t"
    "v_smfmac_f32_16x16x64_bf16 v[20:23], v[44:47], v[128:135], v8\n\t"
    "ds_read_b128 v[44:47], v6 offset:11264\n\t"
    "s_barrier\n\t"
    "s_setprio 0\n\t"
    "s_waitcnt lgkmcnt(9)\n\t"
    "v_smfmac_f32_16x16x64_bf16 v[20:23], v[48:51], v[144:151], v8\n\t"
    "s_waitcnt lgkmcnt(8)\n\t"
    "v_smfmac_f32_16x16x64_bf16 v[20:23], v[52:55], v[136:143], v8\n\t"
    "s_waitcnt lgkmcnt(7)\n\t"
    "v_smfmac_f32_16x16x64_bf16 v[20:23], v[56:59], v[152:159], v8\n\t"
    "s_waitcnt lgkmcnt(6)\n\t"
    "v_smfmac_f32_16x16x64_bf16 v[20:23], v[60:63], v[160:167], v8\n\t"
    "s_waitcnt lgkmcnt(5)\n\t"
    "v_smfmac_f32_16x16x64_bf16 v[20:23], v[28:31], v[176:183], v8\n\t"
    "s_waitcnt lgkmcnt(4)\n\t"
    "v_smfmac_f32_16x16x64_bf16 v[20:23], v[24:27], v[168:175], v8\n\t"
    "s_waitcnt lgkmcnt(3)\n\t"
    "v_smfmac_f32_16x16x64_bf16 v[20:23], v[32:35], v[184:191], v8\n\t"
    "s_waitcnt lgkmcnt(2)\n\t"
    "v_smfmac_f32_16x16x64_bf16 v[20:23], v[36:39], v[192:199], v8\n\t"
    "s_waitcnt lgkmcnt(1)\n\t"
    "v_smfmac_f32_16x16x64_bf16 v[20:23], v[40:43], v[200:207], v8\n\t"
    "s_waitcnt lgkmcnt(0)\n\t"
    "v_smfmac_f32_16x16x64_bf16 v[20:23], v[44:47], v[216:223], v8\n\t"
    "s_nop 7\n\t"
    "v_add_f32 v20, v20, v22\n\t"
    "v_add_f32 v21, v21, v23\n\t"
    "v_cvt_pk_bf16_f32 v20, v20, v21\n\t"
    "v_min_u32 v20, -2, v20\n\t"
    "ds_write_b32 v9, v20 offset:35072\n\t"
    "v_mov_b32 v32, -1\n\t"
    "v_mov_b32 v33, -1\n\t"
    "v_mov_b32 v34, -1\n\t"
    "v_mov_b32 v35, -1\n\t"
    "s_waitcnt lgkmcnt(0)\n\t"
    "s_setprio 0\n\t"
    "s_barrier\n\t"
    "ds_read_b128 v[24:27], v6 offset:34816\n\t"
    "ds_read_b128 v[28:31], v6 offset:35840\n\t"
    "ds_write_b128 v6, v[32:35] offset:34816\n\t"
    "s_waitcnt lgkmcnt(0)\n\t"
    "global_store_dwordx4 v6, v[24:27], %[vs] sc1\n\t"
    "global_store_dwordx4 v6, v[28:31], %[vs] offset:1024 sc1\n\t"
    "s_nop 1\n\t"
    "s_waitcnt lgkmcnt(0)\n\t"
    "s_branch 9f\n\t"
    "1:\n\t"
    "s_add_u32 %[sp], %[sp], 1\n\t"
    "s_cmp_ge_u32 %[sp], %[cap]\n\t"
    "s_cbranch_scc1 7f\n\t"
    "global_load_dwordx4 v[24:27], v6, %[ag] sc1\n\t"
    "global_load_dwordx4 v[28:31], v6, %[ag] offset:-4096 sc1\n\t"
    "s_waitcnt lgkmcnt(0)\n\t"
    "s_memtime %[tc]\n\t"
    "s_nop 7\n\t"
    "s_branch 3b\n\t"
    "7:\n\t"
    "s_mov_b32 %[cap], 0\n\t"
    "s_branch 2b\n\t"
    "9:"
    : [tc] "=&s"(tc), [sp] "=&s"(sp), [cap] "+s"(cap)
    : [ag] "s"(ag), [c44] "s"(0x4444u), [lsc] "s"(LSC), [sn] "s"(sn), [vs] "s"(vs), "{v[64:95]}"(W.w0), "{v[96:127]}"(W.w1), "{v[128:159]}"(W.w2), "{v[160:191]}"(W.w3), "{v[192:223]}"(W.w4), "{v[224:255]}"(W.w5)
    : "v6", "v7", "v8", "v9", "v10", "v11", "v12", "v13", "v14", "v15", "v16", "v17", "v18", "v19", "v20", "v21", "v22", "v23", "v24", "v25", "v26", "v27", "v28", "v29", "v30", "v31", "v32", "v33", "v34", "v35", "v36", "v37", "v38", "v39", "v40", "v41", "v42", "v43", "v44", "v45", "v46", "v47", "v48", "v49", "v50", "v51", "v52", "v53", "v54", "v55", "v56", "v57", "v58", "v59", "v60", "v61", "v62", "v63", "vcc", "scc", "memory");
}
template <> __device__ __forceinline__ void qk_step<6>(const QKW& W, const char* ag, const char* vs, const char* v2, const char* od, const char* kv, unsigned sn, unsigned& cap, unsigned& sp,
    unsigned long long& tc, unsigned long long* ts, unsigned& d0, unsigned& d1) {
  unsigned st, bg;
  asm volatile("v_mbcnt_lo_u32_b32 v60, -1, 0\n\t"
    "v_mbcnt_hi_u32_b32 v60, -1, v60\n\t"
    "v_lshlrev_b32 v6, 4, v60\n\t"
    "s_mov_b32 %[sp], 0\n\t"
    "s_setprio 3\n\t"
    "global_load_dwordx4 v[24:27], v6, %[ag] sc1\n\t"
    "global_load_dwordx4 v[28:31], v6, %[ag] offset:-4096 sc1\n\t"
    "s_memtime %[tc]\n\t"
    "v_lshrrev_b32 v56, 4, v60\n\t"
    "v_and_b32 v57, 15, v60\n\t"
    "v_mov_b32 v20, 0\n\t"
    "v_mov_b32 v21, 0\n\t"
    "v_mov_b32 v22, 0\n\t"
    "v_mov_b32 v23, 0\n\t"
    "v_mov_b32 v11, %[sn]\n\t"
    "v_and_b32 v61, 2, v60\n\t"
    "v_mul_u32_u24 v61, 0x5555, v61\n\t"
    "v_or_b32 v8, %[c44], v61\n\t"
    "v_lshlrev_b32 v61, 2, v56\n\t"
    "v_lshl_or_b32 v9, v57, 4, v61\n\t"
    "3:\n\t"
    "v_mov_b32 v12, 0\n\t"
    "v_mov_b32 v13, 0\n\t"
    "v_mov_b32 v14, 0\n\t"
    "v_mov_b32 v15, 0\n\t"
    "v_mov_b32 v16, 0\n\t"
    "v_mov_b32 v17, 0\n\t"
    "v_mov_b32 v18, 0\n\t"
    "v_mov_b32 v19, 0\n\t"
    "s_waitcnt vmcnt(0) lgkmcnt(0)\n\t"
    "ds_write_b128 v6, v[24:27] offset:14336\n\t"
    "ds_write_b128 v6, v[28:31] offset:10240\n\t"
    "v_smfmac_f32_16x16x64_bf16 v[12:15], v[24:27], v[96:103], v8\n\t"
    "v_max3_u32 v60, v24, v25, v26\n\t"
    "v_max3_u32 v61, v27, v28, v29\n\t"
    "v_smfmac_f32_16x16x64_bf16 v[16:19], v[24:27], v[104:111], v8\n\t"
    "v_max3_u32 v60, v60, v30, v31\n\t"
    "v_max_u32 v60, v60, v61\n\t"
    "v_cmp_eq_u32 vcc, -1, v60\n\t"
    "s_cbranch_vccnz 1f\n\t"
    "2:\n\t"
    "s_waitcnt lgkmcnt(0)\n\t"
    "s_barrier\n\t"
    "ds_read_b128 v[32:35], v6 offset:12288\n\t"
    "ds_read_b128 v[36:39], v6 offset:13312\n\t"
    "ds_read_b128 v[40:43], v6 offset:15360\n\t"
    "s_waitcnt lgkmcnt(2)\n\t"
    "v_smfmac_f32_16x16x64_bf16 v[12:15], v[32:35], v[64:71], v8\n\t"
    "v_smfmac_f32_16x16x64_bf16 v[16:19], v[32:35], v[72:79], v8\n\t"
    "s_waitcnt lgkmcnt(1)\n\t"
    "v_smfmac_f32_16x16x64_bf16 v[12:15], v[36:39], v[80:87], v8\n\t"
    "v_smfmac_f32_16x16x64_bf16 v[16:19], v[36:39], v[88:95], v8\n\t"
    "s_waitcnt lgkmcnt(0)\n\t"
    "v_smfmac_f32_16x16x64_bf16 v[12:15], v[40:43], v[112:119], v8\n\t"
    "v_smfmac_f32_16x16x64_bf16 v[16:19], v[40:43], v[120:127], v8\n\t"
    "s_nop 6\n\t"
    "v_add_f32 v12, v12, v14\n\t"
    "v_add_f32 v13, v13, v15\n\t"
    "v_add_f32 v14, v16, v18\n\t"
    "v_add_f32 v15, v17, v19\n\t"
    "ds_write_b128 v6, v[12:15] offset:43008\n\t"
    "ds_write_b32 v9, v11 offset:45568\n\t"
    "s_setprio 0\n\t"
    "ds_read_b128 v[44:47], v6 offset:0\n\t"
    "ds_read_b128 v[48:51], v6 offset:1024\n\t"
    "ds_read_b128 v[52:55], v6 offset:2048\n\t"
    "ds_read_b128 v[56:59], v6 offset:3072\n\t"
    "ds_read_b128 v[60:63], v6 offset:4096\n\t"
    "s_sleep 2\n\t"
    "v_smfmac_f32_16x16x64_bf16 v[20:23], v[28:31], v[200:207], v8\n\t"
    "ds_read_b128 v[28:31], v6 offset:5120\n\t"
    "v_smfmac_f32_16x16x64_bf16 v[20:23], v[24:27], v[232:239], v8\n\t"
    "ds_read_b128 v[24:27], v6 offset:6144\n\t"
    "v_smfmac_f32_16x16x64_bf16 v[20:23], v[32:35], v[224:231], v8\n\t"
    "ds_read_b128 v[32:35], v6 offset:7168\n\t"
    "v_smfmac_f32_16x16x64_bf16 v[20:23], v[36:39], v[240:247], v8\n\t"
    "ds_read_b128 v[36:39], v6 offset:8192\n\t"
    "v_smfmac_f32_16x16x64_bf16 v[20:23], v[40:43], v[248:255], v8\n\t"
    "ds_read_b128 v[40:43], v6 offset:9216\n\t"
    "s_waitcnt lgkmcnt(9)\n\t"
    "v_smfmac_f32_16x16x64_bf16 v[20:23], v[44:47], v[128:135], v8\n\t"
    "ds_read_b128 v[44:47], v6 offset:11264\n\t"
    "s_barrier\n\t"
    "s_setprio 0\n\t"
    "s_waitcnt lgkmcnt(9)\n\t"
    "v_smfmac_f32_16x16x64_bf16 v[20:23], v[48:51], v[144:151], v8\n\t"
    "s_waitcnt lgkmcnt(8)\n\t"
    "v_smfmac_f32_16x16x64_bf16 v[20:23], v[52:55], v[136:143], v8\n\t"
    "s_waitcnt lgkmcnt(7)\n\t"
    "v_smfmac_f32_16x16x64_bf16 v[20:23], v[56:59], v[152:159], v8\n\t"
    "s_waitcnt lgkmcnt(6)\n\t"
    "v_smfmac_f32_16x16x64_bf16 v[20:23], v[60:63], v[160:167], v8\n\t"
    "s_waitcnt lgkmcnt(5)\n\t"
    "v_smfmac_f32_16x16x64_bf16 v[20:23], v[28:31], v[176:183], v8\n\t"
    "s_waitcnt lgkmcnt(4)\n\t"
    "v_smfmac_f32_16x16x64_bf16 v[20:23], v[24:27], v[168:175], v8\n\t"
    "s_waitcnt lgkmcnt(3)\n\t"
    "v_smfmac_f32_16x16x64_bf16 v[20:23], v[32:35], v[184:191], v8\n\t"
    "s_waitcnt lgkmcnt(2)\n\t"
    "v_smfmac_f32_16x16x64_bf16 v[20:23], v[36:39], v[192:199], v8\n\t"
    "s_waitcnt lgkmcnt(1)\n\t"
    "v_smfmac_f32_16x16x64_bf16 v[20:23], v[40:43], v[208:215], v8\n\t"
    "s_waitcnt lgkmcnt(0)\n\t"
    "v_smfmac_f32_16x16x64_bf16 v[20:23], v[44:47], v[216:223], v8\n\t"
    "s_nop 7\n\t"
    "v_add_f32 v20, v20, v22\n\t"
    "v_add_f32 v21, v21, v23\n\t"
    "v_cvt_pk_bf16_f32 v20, v20, v21\n\t"
    "v_min_u32 v20, -2, v20\n\t"
    "ds_write_b32 v9, v20 offset:35328\n\t"
    "s_waitcnt lgkmcnt(0)\n\t"
    "s_setprio 0\n\t"
    "s_barrier\n\t"
    "s_waitcnt lgkmcnt(0)\n\t"
    "s_branch 9f\n\t"
    "1:\n\t"
    "s_add_u32 %[sp], %[sp], 1\n\t"
    "s_cmp_ge_u32 %[sp], %[cap]\n\t"
    "s_cbranch_scc1 7f\n\t"
    "global_load_dwordx4 v[24:27], v6, %[ag] sc1\n\t"
    "global_load_dwordx4 v[28:31], v6, %[ag] offset:-4096 sc1\n\t"
    "s_waitcnt lgkmcnt(0)\n\t"
    "s_memtime %[tc]\n\t"
    "s_nop 7\n\t"
    "s_branch 3b\n\t"
    "7:\n\t"
    "s_mov_b32 %[cap], 0\n\t"
    "s_branch 2b\n\t"
    "9:"
    : [tc] "=&s"(tc), [sp] "=&s"(sp), [cap] "+s"(cap)
    : [ag] "s"(ag), [c44] "s"(0x4444u), [lsc] "s"(LSC), [sn] "s"(sn), "{v[64:95]}"(W.w0), "{v[96:127]}"(W.w1), "{v[128:159]}"(W.w2), "{v[160:191]}"(W.w3), "{v[192:223]}"(W.w4), "{v[224:255]}"(W.w5)
    : "v6", "v7", "v8", "v9", "v10", "v11", "v12", "v13", "v14", "v15", "v16", "v17", "v18", "v19", "v20", "v21", "v22", "v23", "v24", "v25", "v26", "v27", "v28", "v29", "v30", "v31", "v32", "v33", "v34", "v35", "v36", "v37", "v38", "v39", "v40", "v41", "v42", "v43", "v44", "v45", "v46", "v47", "v48", "v49", "v50", "v51", "v52", "v53", "v54", "v55", "v56", "v57", "v58", "v59", "v60", "v61", "v62", "v63", "vcc", "scc", "memory");
}
template <> __device__ __forceinline__ void qk_step<7>(const QKW& W, const char* ag, const char* vs, const char* v2, const char* od, const char* kv, unsigned sn, unsigned& cap, unsigned& sp,
    unsigned long long& tc, unsigned long long* ts, unsigned& d0, unsigned& d1) {
  unsigned st, bg;
  asm volatile("v_mbcnt_lo_u32_b32 v60, -1, 0\n\t"
    "v_mbcnt_hi_u32_b32 v60, -1, v60\n\t"
    "v_lshlrev_b32 v6, 4, v60\n\t"
    "s_mov_b32 %[sp], 0\n\t"
    "s_setprio 3\n\t"
    "global_load_dwordx4 v[24:27], v6, %[ag] sc1\n\t"
    "global_load_dwordx4 v[28:31], v6, %[ag] offset:-4096 sc1\n\t"
    "s_memtime %[tc]\n\t"
    "v_lshrrev_b32 v56, 4, v60\n\t"
    "v_and_b32 v57, 15, v60\n\t"
    "v_mov_b32 v20, 0\n\t"
    "v_mov_b32 v21, 0\n\t"
    "v_mov_b32 v22, 0\n\t"
    "v_mov_b32 v23, 0\n\t"
    "v_mov_b32 v11, %[sn]\n\t"
    "v_and_b32 v61, 2, v60\n\t"
    "v_mul_u32_u24 v61, 0x5555, v61\n\t"
    "v_or_b32 v8, %[c44], v61\n\t"
    "v_lshlrev_b32 v61, 2, v56\n\t"
    "v_lshl_or_b32 v9, v57, 4, v61\n\t"
    "3:\n\t"
    "v_mov_b32 v12, 0\n\t"
    "v_mov_b32 v13, 0\n\t"
    "v_mov_b32 v14, 0\n\t"
    "v_mov_b32 v15, 0\n\t"
    "v_mov_b32 v16, 0\n\t"
    "v_mov_b32 v17, 0\n\t"
    "v_mov_b32 v18, 0\n\t"
    "v_mov_b32 v19, 0\n\t"
    "s_waitcnt vmcnt(0) lgkmcnt(0)\n\t"
    "ds_write_b128 v6, v[24:27] offset:15360\n\t"
    "ds_write_b128 v6, v[28:31] offset:11264\n\t"
    "v_smfmac_f32_16x16x64_bf16 v[12:15], v[24:27], v[112:119], v8\n\t"
    "v_max3_u32 v60, v24, v25, v26\n\t"
    "v_max3_u32 v61, v27, v28, v29\n\t"
    "v_smfmac_f32_16x16x64_bf16 v[16:19], v[24:27], v[120:127], v8\n\t"
    "v_max3_u32 v60, v60, v30, v31\n\t"
    "v_max_u32 v60, v60, v61\n\t"
    "v_cmp_eq_u32 vcc, -1, v60\n\t"
    "s_cbranch_vccnz 1f\n\t"
    "2:\n\t"
    "s_waitcnt lgkmcnt(0)\n\t"
    "s_barrier\n\t"
    "ds_read_b128 v[32:35], v6 offset:12288\n\t"
    "ds_read_b128 v[36:39], v6 offset:13312\n\t"
    "ds_read_b128 v[40:43], v6 offset:14336\n\t"
    "s_waitcnt lgkmcnt(2)\n\t"
    "v_smfmac_f32_16x16x64_bf16 v[12:15], v[32:35], v[64:71], v8\n\t"
    "v_smfmac_f32_16x16x64_bf16 v[16:19], v[32:35], v[72:79], v8\n\t"
    "s_waitcnt lgkmcnt(1)\n\t"
    "v_smfmac_f32_16x16x64_bf16 v[12:15], v[36:39], v[80:87], v8\n\t"
    "v_smfmac_f32_16x16x64_bf16 v[16:19], v[36:39], v[88:95], v8\n\t"
    "s_waitcnt lgkmcnt(0)\n\t"
    "v_smfmac_f32_16x16x64_bf16 v[12:15], v[40:43], v[96:103], v8\n\t"
    "v_smfmac_f32_16x16x64_bf16 v[16:19], v[40:43], v[104:111], v8\n\t"
    "s_nop 6\n\t"
    "v_add_f32 v12, v12, v14\n\t"
    "v_add_f32 v13, v13, v15\n\t"
    "v_add_f32 v14, v16, v18\n\t"
    "v_add_f32 v15, v17, v19\n\t"
    "ds_write_b128 v6, v[12:15] offset:44032\n\t"
    "ds_write_b32 v9, v11 offset:45824\n\t"
    "s_setprio 0\n\t"
    "ds_read_b128 v[44:47], v6 offset:0\n\t"
    "ds_read_b128 v[48:51], v6 offset:1024\n\t"
    "ds_read_b128 v[52:55], v6 offset:2048\n\t"
    "ds_read_b128 v[56:59], v6 offset:3072\n\t"
    "ds_read_b128 v[60:63], v6 offset:4096\n\t"
    "s_sleep 2\n\t"
    "v_smfmac_f32_16x16x64_bf16 v[20:23], v[28:31], v[216:223], v8\n\t"
    "ds_read_b128 v[28:31], v6 offset:5120\n\t"
    "v_smfmac_f32_16x16x64_bf16 v[20:23], v[24:27], v[248:255], v8\n\t"
    "ds_read_b128 v[24:27], v6 offset:6144\n\t"
    "v_smfmac_f32_16x16x64_bf16 v[20:23], v[32:35], v[224:231], v8\n\t"
    "ds_read_b128 v[32:35], v6 offset:7168\n\t"
    "v_smfmac_f32_16x16x64_bf16 v[20:23], v[36:39], v[240:247], v8\n\t"
    "ds_read_b128 v[36:39], v6 offset:8192\n\t"
    "v_smfmac_f32_16x16x64_bf16 v[20:23], v[40:43], v[232:239], v8\n\t"
    "ds_read_b128 v[40:43], v6 offset:9216\n\t"
    "s_waitcnt lgkmcnt(9)\n\t"
    "v_smfmac_f32_16x16x64_bf16 v[20:23], v[44:47], v[128:135], v8\n\t"
    "ds_read_b128 v[44:47], v6 offset:10240\n\t"
    "s_barrier\n\t"
    "s_setprio 0\n\t"
    "s_waitcnt lgkmcnt(9)\n\t"
    "v_smfmac_f32_16x16x64_bf16 v[20:23], v[48:51], v[144:151], v8\n\t"
    "s_waitcnt lgkmcnt(8)\n\t"
    "v_smfmac_f32_16x16x64_bf16 v[20:23], v[52:55], v[136:143], v8\n\t"
    "s_waitcnt lgkmcnt(7)\n\t"
    "v_smfmac_f32_16x16x64_bf16 v[20:23], v[56:59], v[152:159], v8\n\t"
    "s_waitcnt lgkmcnt(6)\n\t"
    "v_smfmac_f32_16x16x64_bf16 v[20:23], v[60:63], v[160:167], v8\n\t"
    "s_waitcnt lgkmcnt(5)\n\t"
    "v_smfmac_f32_16x16x64_bf16 v[20:23], v[28:31], v[176:183], v8\n\t"
    "s_waitcnt lgkmcnt(4)\n\t"
    "v_smfmac_f32_16x16x64_bf16 v[20:23], v[24:27], v[168:175], v8\n\t"
    "s_waitcnt lgkmcnt(3)\n\t"
    "v_smfmac_f32_16x16x64_bf16 v[20:23], v[32:35], v[184:191], v8\n\t"
    "s_waitcnt lgkmcnt(2)\n\t"
    "v_smfmac_f32_16x16x64_bf16 v[20:23], v[36:39], v[192:199], v8\n\t"
    "s_waitcnt lgkmcnt(1)\n\t"
    "v_smfmac_f32_16x16x64_bf16 v[20:23], v[40:43], v[208:215], v8\n\t"
    "s_waitcnt lgkmcnt(0)\n\t"
    "v_smfmac_f32_16x16x64_bf16 v[20:23], v[44:47], v[200:207], v8\n\t"
    "s_nop 7\n\t"
    "v_add_f32 v20, v20, v22\n\t"
    "v_add_f32 v21, v21, v23\n\t"
    "v_cvt_pk_bf16_f32 v20, v20, v21\n\t"
    "v_min_u32 v20, -2, v20\n\t"
    "ds_write_b32 v9, v20 offset:35584\n\t"
    "s_waitcnt lgkmcnt(0)\n\t"
    "s_setprio 0\n\t"
    "s_barrier\n\t"
    "s_waitcnt lgkmcnt(0)\n\t"
    "s_branch 9f\n\t"
    "1:\n\t"
    "s_add_u32 %[sp], %[sp], 1\n\t"
    "s_cmp_ge_u32 %[sp], %[cap]\n\t"
    "s_cbranch_scc1 7f\n\t"
    "global_load_dwordx4 v[24:27], v6, %[ag] sc1\n\t"
    "global_load_dwordx4 v[28:31], v6, %[ag] offset:-4096 sc1\n\t"
    "s_waitcnt lgkmcnt(0)\n\t"
    "s_memtime %[tc]\n\t"
    "s_nop 7\n\t"
    "s_branch 3b\n\t"
    "7:\n\t"
    "s_mov_b32 %[cap], 0\n\t"
    "s_branch 2b\n\t"
    "9:"
    : [tc] "=&s"(tc), [sp] "=&s"(sp), [cap] "+s"(cap)
    : [ag] "s"(ag), [c44] "s"(0x4444u), [lsc] "s"(LSC), [sn] "s"(sn), "{v[64:95]}"(W.w0), "{v[96:127]}"(W.w1), "{v[128:159]}"(W.w2), "{v[160:191]}"(W.w3), "{v[192:223]}"(W.w4), "{v[224:255]}"(W.w5)
    : "v6", "v7", "v8", "v9", "v10", "v11", "v12", "v13", "v14", "v15", "v16", "v17", "v18", "v19", "v20", "v21", "v22", "v23", "v24", "v25", "v26", "v27", "v28", "v29", "v30", "v31", "v32", "v33", "v34", "v35", "v36", "v37", "v38", "v39", "v40", "v41", "v42", "v43", "v44", "v45", "v46", "v47", "v48", "v49", "v50", "v51", "v52", "v53", "v54", "v55", "v56", "v57", "v58", "v59", "v60", "v61", "v62", "v63", "vcc", "scc", "memory");
}
template <int I> __device__ __forceinline__ void qk_wload(u32x32& w, unsigned a0, unsigned a1);
template <> __device__ __forceinline__ void qk_wload<0>(u32x32& w, unsigned a0, unsigned a1) {
  asm volatile("ds_read_b64_tr_b16 v[64:65], %1 offset:0\n\t"
    "ds_read_b64_tr_b16 v[66:67], %1 offset:512\n\t"
    "ds_read_b64_tr_b16 v[68:69], %1 offset:8192\n\t"
    "ds_read_b64_tr_b16 v[70:71], %1 offset:8704\n\t"
    "ds_read_b64_tr_b16 v[72:73], %2 offset:0\n\t"
    "ds_read_b64_tr_b16 v[74:75], %2 offset:512\n\t"
    "ds_read_b64_tr_b16 v[76:77], %2 offset:8192\n\t"
    "ds_read_b64_tr_b16 v[78:79], %2 offset:8704\n\t"
    "s_waitcnt lgkmcnt(0)\n\t"
    "ds_read_b64_tr_b16 v[80:81], %1 offset:16384\n\t"
    "ds_read_b64_tr_b16 v[82:83], %1 offset:16896\n\t"
    "ds_read_b64_tr_b16 v[84:85], %1 offset:24576\n\t"
    "ds_read_b64_tr_b16 v[86:87], %1 offset:25088\n\t"
    "ds_read_b64_tr_b16 v[88:89], %2 offset:16384\n\t"
    "ds_read_b64_tr_b16 v[90:91], %2 offset:16896\n\t"
    "ds_read_b64_tr_b16 v[92:93], %2 offset:24576\n\t"
    "ds_read_b64_tr_b16 v[94:95], %2 offset:25088\n\t"
    "s_waitcnt lgkmcnt(0)"
    : "=&{v[64:95]}"(w) : "v"(a0), "v"(a1) : "memory");
}
template <> __device__ __forceinline__ void qk_wload<1>(u32x32& w, unsigned a0, unsigned a1) {
  asm volatile("ds_read_b64_tr_b16 v[96:97], %1 offset:0\n\t"
    "ds_read_b64_tr_b16 v[98:99], %1 offset:512\n\t"
    "ds_read_b64_tr_b16 v[100:101], %1 offset:8192\n\t"
    "ds_read_b64_tr_b16 v[102:103], %1 offset:8704\n\t"
    "ds_read_b64_tr_b16 v[104:105], %2 offset:0\n\t"
    "ds_read_b64_tr_b16 v[106:107], %2 offset:512\n\t"
    "ds_read_b64_tr_b16 v[108:109], %2 offset:8192\n\t"
    "ds_read_b64_tr_b16 v[110:111], %2 offset:8704\n\t"
    "s_waitcnt lgkmcnt(0)\n\t"
    "ds_read_b64_tr_b16 v[112:113], %1 offset:16384\n\t"
    "ds_read_b64_tr_b16 v[114:115], %1 offset:16896\n\t"
    "ds_read_b64_tr_b16 v[116:117], %1 offset:24576\n\t"
    "ds_read_b64_tr_b16 v[118:119], %1 offset:25088\n\t"
    "ds_read_b64_tr_b16 v[120:121], %2 offset:16384\n\t"
    "ds_read_b64_tr_b16 v[122:123], %2 offset:16896\n\t"
    "ds_read_b64_tr_b16 v[124:125], %2 offset:24576\n\t"
    "ds_read_b64_tr_b16 v[126:127], %2 offset:25088\n\t"
    "s_waitcnt lgkmcnt(0)"
    : "=&{v[96:127]}"(w) : "v"(a0), "v"(a1) : "memory");
}
template <> __device__ __forceinline__ void qk_wload<2>(u32x32& w, unsigned a0, unsigned a1) {
  asm volatile("ds_read_b64_tr_b16 v[128:129], %1 offset:0\n\t"
    "ds_read_b64_tr_b16 v[130:131], %1 offset:512\n\t"
    "ds_read_b64_tr_b16 v[132:133], %1 offset:8192\n\t"
    "ds_read_b64_tr_b16 v[134:135], %1 offset:8704\n\t"
    "ds_read_b64_tr_b16 v[136:137], %2 offset:0\n\t"
    "ds_read_b64_tr_b16 v[138:139], %2 offset:512\n\t"
    "ds_read_b64_tr_b16 v[140:141], %2 offset:8192\n\t"
    "ds_read_b64_tr_b16 v[142:143], %2 offset:8704\n\t"
    "s_waitcnt lgkmcnt(0)\n\t"
    "ds_read_b64_tr_b16 v[144:145], %1 offset:16384\n\t"
    "ds_read_b64_tr_b16 v[146:147], %1 offset:16896\n\t"
    "ds_read_b64_tr_b16 v[148:149], %1 offset:24576\n\t"
    "ds_read_b64_tr_b16 v[150:151], %1 offset:25088\n\t"
    "ds_read_b64_tr_b16 v[152:153], %2 offset:16384\n\t"
    "ds_read_b64_tr_b16 v[154:155], %2 offset:16896\n\t"
    "ds_read_b64_tr_b16 v[156:157], %2 offset:24576\n\t"
    "ds_read_b64_tr_b16 v[158:159], %2 offset:25088\n\t"
    "s_waitcnt lgkmcnt(0)"
    : "=&{v[128:159]}"(w) : "v"(a0), "v"(a1) : "memory");
}
template <> __device__ __forceinline__ void qk_wload<3>(u32x32& w, unsigned a0, unsigned a1) {
  asm volatile("ds_read_b64_tr_b16 v[160:161], %1 offset:0\n\t"
    "ds_read_b64_tr_b16 v[162:163], %1 offset:512\n\t"
    "ds_read_b64_tr_b16 v[164:165], %1 offset:8192\n\t"
    "ds_read_b64_tr_b16 v[166:167], %1 offset:8704\n\t"
    "ds_read_b64_tr_b16 v[168:169], %2 offset:0\n\t"
    "ds_read_b64_tr_b16 v[170:171], %2 offset:512\n\t"
    "ds_read_b64_tr_b16 v[172:173], %2 offset:8192\n\t"
    "ds_read_b64_tr_b16 v[174:175], %2 offset:8704\n\t"
    "s_waitcnt lgkmcnt(0)\n\t"
    "ds_read_b64_tr_b16 v[176:177], %1 offset:16384\n\t"
    "ds_read_b64_tr_b16 v[178:179], %1 offset:16896\n\t"
    "ds_read_b64_tr_b16 v[180:181], %1 offset:24576\n\t"
    "ds_read_b64_tr_b16 v[182:183], %1 offset:25088\n\t"
    "ds_read_b64_tr_b16 v[184:185], %2 offset:16384\n\t"
    "ds_read_b64_tr_b16 v[186:187], %2 offset:16896\n\t"
    "ds_read_b64_tr_b16 v[188:189], %2 offset:24576\n\t"
    "ds_read_b64_tr_b16 v[190:191], %2 offset:25088\n\t"
    "s_waitcnt lgkmcnt(0)"
    : "=&{v[160:191]}"(w) : "v"(a0), "v"(a1) : "memory");
}
template <> __device__ __forceinline__ void qk_wload<4>(u32x32& w, unsigned a0, unsigned a1) {
  asm volatile("ds_read_b64_tr_b16 v[192:193], %1 offset:0\n\t"
    "ds_read_b64_tr_b16 v[194:195], %1 offset:512\n\t"
    "ds_read_b64_tr_b16 v[196:197], %1 offset:8192\n\t"
    "ds_read_b64_tr_b16 v[198:199], %1 offset:8704\n\t"
    "ds_read_b64_tr_b16 v[200:201], %2 offset:0\n\t"
    "ds_read_b64_tr_b16 v[202:203], %2 offset:512\n\t"
    "ds_read_b64_tr_b16 v[204:205], %2 offset:8192\n\t"
    "ds_read_b64_tr_b16 v[206:207], %2 offset:8704\n\t"
    "s_waitcnt lgkmcnt(0)\n\t"
    "ds_read_b64_tr_b16 v[208:209], %1 offset:16384\n\t"
    "ds_read_b64_tr_b16 v[210:211], %1 offset:16896\n\t"
    "ds_read_b64_tr_b16 v[212:213], %1 offset:24576\n\t"
    "ds_read_b64_tr_b16 v[214:215], %1 offset:25088\n\t"
    "ds_read_b64_tr_b16 v[216:217], %2 offset:16384\n\t"
    "ds_read_b64_tr_b16 v[218:219], %2 offset:16896\n\t"
    "ds_read_b64_tr_b16 v[220:221], %2 offset:24576\n\t"
    "ds_read_b64_tr_b16 v[222:223], %2 offset:25088\n\t"
    "s_waitcnt lgkmcnt(0)"
    : "=&{v[192:223]}"(w) : "v"(a0), "v"(a1) : "memory");
}
template <> __device__ __forceinline__ void qk_wload<5>(u32x32& w, unsigned a0, unsigned a1) {
  asm volatile("ds_read_b64_tr_b16 v[224:225], %1 offset:0\n\t"
    "ds_read_b64_tr_b16 v[226:227], %1 offset:512\n\t"
    "ds_read_b64_tr_b16 v[228:229], %1 offset:8192\n\t"
    "ds_read_b64_tr_b16 v[230:231], %1 offset:8704\n\t"
    "ds_read_b64_tr_b16 v[232:233], %2 offset:0\n\t"
    "ds_read_b64_tr_b16 v[234:235], %2 offset:512\n\t"
    "ds_read_b64_tr_b16 v[236:237], %2 offset:8192\n\t"
    "ds_read_b64_tr_b16 v[238:239], %2 offset:8704\n\t"
    "s_waitcnt lgkmcnt(0)\n\t"
    "ds_read_b64_tr_b16 v[240:241], %1 offset:16384\n\t"
    "ds_read_b64_tr_b16 v[242:243], %1 offset:16896\n\t"
    "ds_read_b64_tr_b16 v[244:245], %1 offset:24576\n\t"
    "ds_read_b64_tr_b16 v[246:247], %1 offset:25088\n\t"
    "ds_read_b64_tr_b16 v[248:249], %2 offset:16384\n\t"
    "ds_read_b64_tr_b16 v[250:251], %2 offset:16896\n\t"
    "ds_read_b64_tr_b16 v[252:253], %2 offset:24576\n\t"
    "ds_read_b64_tr_b16 v[254:255], %2 offset:25088\n\t"
    "s_waitcnt lgkmcnt(0)"
    : "=&{v[224:255]}"(w) : "v"(a0), "v"(a1) : "memory");
}
#define OREP 2
#define AREP 2
constexpr int V_P1 = 131072, V_PV = 149504, V_P2 = 157696;
struct VRW { u32x32 k0, k1, o0, o1, o2, o3; };
__device__ __forceinline__ void vr_v0(const VRW& W, const char* ag, const char* vd, unsigned wl, unsigned wa, unsigned wb, unsigned ra, unsigned& cap, unsigned& sp, unsigned long long& tc, unsigned long long* ts) {
  asm volatile("s_mov_b32 %[sp], 0\n\t"
    "v_mbcnt_lo_u32_b32 v20, -1, 0\n\t"
    "v_mbcnt_hi_u32_b32 v20, -1, v20\n\t"
    "v_lshlrev_b32 v21, 4, v20\n\t"
    "global_load_dwordx4 v[24:27], v21, %[ag] sc1\n\t"
    "global_load_dwordx4 v[28:31], v21, %[ag] offset:1024 sc1\n\t"
    "s_memtime %[tc]\n\t"
    "v_lshl_add_u32 v6, v20, 4, %[wl]\n\t"
    "ds_read_b128 v[32:35], v6 offset:0\n\t"
    "ds_read_b128 v[36:39], v6 offset:1024\n\t"
    "ds_read_b128 v[40:43], v6 offset:2048\n\t"
    "ds_read_b128 v[44:47], v6 offset:3072\n\t"
    "ds_read_b128 v[48:51], v6 offset:4096\n\t"
    "ds_read_b128 v[52:55], v6 offset:5120\n\t"
    "ds_read_b128 v[56:59], v6 offset:6144\n\t"
    "ds_read_b128 v[60:63], v6 offset:7168\n\t"
    "s_setprio 2\n\t"
    "v_and_b32 v6, 2, v20\n\t"
    "v_mul_u32_u24 v6, 0x5555, v6\n\t"
    "v_or_b32 v4, %[c44], v6\n\t"
    "v_and_b32 v6, 15, v20\n\t"
    "v_lshl_add_u32 v5, v6, 2, %[wa]\n\t"
    "v_and_b32 v6, 48, v20\n\t"
    "v_mad_u32_u24 v5, v6, 36, v5\n\t"
    "v_mul_u32_u24 v7, 36, v6\n\t"
    "v_and_b32 v6, 4, v20\n\t"
    "v_mul_u32_u24 v6, 72, v6\n\t"
    "v_add_u32 v7, v7, v6\n\t"
    "v_and_b32 v6, 8, v20\n\t"
    "v_lshl_add_u32 v7, v6, 4, v7\n\t"
    "v_and_b32 v6, 3, v20\n\t"
    "v_lshl_add_u32 v7, v6, 3, v7\n\t"
    "v_add_u32 v7, %[ra], v7\n\t"
    "3:\n\t"
    "v_mov_b32 v8, 0\n\t"
    "v_mov_b32 v9, 0\n\t"
    "v_mov_b32 v10, 0\n\t"
    "v_mov_b32 v11, 0\n\t"
    "v_mov_b32 v12, 0\n\t"
    "v_mov_b32 v13, 0\n\t"
    "v_mov_b32 v14, 0\n\t"
    "v_mov_b32 v15, 0\n\t"
    "v_mov_b32 v16, 0\n\t"
    "v_mov_b32 v17, 0\n\t"
    "v_mov_b32 v18, 0\n\t"
    "v_mov_b32 v19, 0\n\t"
    "v_mov_b32 v20, 0\n\t"
    "v_mov_b32 v21, 0\n\t"
    "v_mov_b32 v22, 0\n\t"
    "v_mov_b32 v23, 0\n\t"
    "s_waitcnt vmcnt(0) lgkmcnt(0)\n\t"
    "v_smfmac_f32_16x16x64_bf16 v[8:11], v[24:27], v[64:71], v4\n\t"
    "v_smfmac_f32_16x16x64_bf16 v[8:11], v[28:31], v[72:79], v4\n\t"
    "v_max3_u32 v6, v24, v25, v26\n\t"
    "v_max3_u32 v6, v6, v27, v28\n\t"
    "v_smfmac_f32_16x16x64_bf16 v[12:15], v[24:27], v[80:87], v4\n\t"
    "v_max3_u32 v6, v6, v29, v30\n\t"
    "v_max_u32 v6, v6, v31\n\t"
    "v_smfmac_f32_16x16x64_bf16 v[12:15], v[28:31], v[88:95], v4\n\t"
    "v_cmp_eq_u32 vcc, -1, v6\n\t"
    "s_cbranch_vccnz 1f\n\t"
    "2:\n\t"
    "v_smfmac_f32_16x16x64_bf16 v[16:19], v[24:27], v[96:103], v4\n\t"
    "v_add_f32 v8, v8, v10\n\t"
    "v_add_f32 v9, v9, v11\n\t"
    "ds_write2_b32 v5, v8, v9 offset0:0 offset1:72\n\t"
    "v_smfmac_f32_16x16x64_bf16 v[16:19], v[28:31], v[104:111], v4\n\t"
    "v_mov_b32 v8, 0\n\t"
    "v_mov_b32 v9, 0\n\t"
    "v_smfmac_f32_16x16x64_bf16 v[20:23], v[24:27], v[112:119], v4\n\t"
    "v_add_f32 v12, v12, v14\n\t"
    "v_add_f32 v13, v13, v15\n\t"
    "ds_write2_b32 v5, v12, v13 offset0:16 offset1:88\n\t"
    "v_smfmac_f32_16x16x64_bf16 v[20:23], v[28:31], v[120:127], v4\n\t"
    "v_mov_b32 v10, 0\n\t"
    "v_mov_b32 v11, 0\n\t"
    "v_add_f32 v16, v16, v18\n\t"
    "v_add_f32 v17, v17, v19\n\t"
    "ds_write2_b32 v5, v16, v17 offset0:32 offset1:104\n\t"
    "v_mov_b32 v12, 0\n\t"
    "v_mov_b32 v13, 0\n\t"
    "v_mov_b32 v14, 0\n\t"
    "v_mov_b32 v15, 0\n\t"
    "v_add_f32 v20, v20, v22\n\t"
    "v_add_f32 v21, v21, v23\n\t"
    "ds_write2_b32 v5, v20, v21 offset0:48 offset1:120\n\t"
    "s_setprio 1\n\t"
    "v_smfmac_f32_16x16x64_bf16 v[8:11], v[24:27], v[32:39], v4\n\t"
    "v_smfmac_f32_16x16x64_bf16 v[8:11], v[28:31], v[40:47], v4\n\t"
    "v_smfmac_f32_16x16x64_bf16 v[12:15], v[24:27], v[48:55], v4\n\t"
    "v_smfmac_f32_16x16x64_bf16 v[12:15], v[28:31], v[56:63], v4\n\t"
    "v_mbcnt_lo_u32_b32 v17, -1, 0\n\t"
    "v_mbcnt_hi_u32_b32 v17, -1, v17\n\t"
    "v_lshl_add_u32 v16, v17, 3, %[wb]\n\t"
    "v_lshlrev_b32 v17, 2, v17\n\t"
    "s_nop 1\n\t"
    "v_add_f32 v8, v8, v10\n\t"
    "v_add_f32 v9, v9, v11\n\t"
    "v_add_f32 v12, v12, v14\n\t"
    "v_add_f32 v13, v13, v15\n\t"
    "ds_write2st64_b64 v16, v[8:9], v[12:13] offset1:1\n\t"
    "s_setprio 3\n\t"
    "s_waitcnt lgkmcnt(1)\n\t"
    "s_barrier\n\t"
    "ds_read_b64 v[32:33], v7 offset:0\n\t"
    "ds_read_b64 v[34:35], v7 offset:2304\n\t"
    "ds_read_b64 v[36:37], v7 offset:4608\n\t"
    "ds_read_b64 v[38:39], v7 offset:6912\n\t"
    "ds_read_b64 v[40:41], v7 offset:9216\n\t"
    "ds_read_b64 v[42:43], v7 offset:11520\n\t"
    "ds_read_b64 v[44:45], v7 offset:13824\n\t"
    "ds_read_b64 v[46:47], v7 offset:16128\n\t"
    "s_waitcnt lgkmcnt(6)\n\t"
    "v_pk_add_f32 v[32:33], v[32:33], v[34:35]\n\t"
    "s_waitcnt lgkmcnt(4)\n\t"
    "v_pk_add_f32 v[36:37], v[36:37], v[38:39]\n\t"
    "v_pk_add_f32 v[32:33], v[32:33], v[36:37]\n\t"
    "s_waitcnt lgkmcnt(2)\n\t"
    "v_pk_add_f32 v[40:41], v[40:41], v[42:43]\n\t"
    "s_waitcnt lgkmcnt(0)\n\t"
    "v_pk_add_f32 v[44:45], v[44:45], v[46:47]\n\t"
    "v_pk_add_f32 v[40:41], v[40:41], v[44:45]\n\t"
    "v_pk_add_f32 v[32:33], v[32:33], v[40:41]\n\t"
    "v_cvt_pk_bf16_f32 v6, v32, v33\n\t"
    "v_min_u32 v6, -2, v6\n\t"
    "global_store_dword v17, v6, %[vd] sc1\n\t"
    "s_barrier\n\t"
    "s_setprio 2\n\t"
    "s_waitcnt lgkmcnt(0)\n\t"
    "s_branch 9f\n\t"
    "1:\n\t"
    "s_add_u32 %[sp], %[sp], 1\n\t"
    "s_cmp_ge_u32 %[sp], %[cap]\n\t"
    "s_cbranch_scc1 7f\n\t"
    "v_mbcnt_lo_u32_b32 v20, -1, 0\n\t"
    "v_mbcnt_hi_u32_b32 v20, -1, v20\n\t"
    "v_lshlrev_b32 v21, 4, v20\n\t"
    "global_load_dwordx4 v[24:27], v21, %[ag] sc1\n\t"
    "global_load_dwordx4 v[28:31], v21, %[ag] offset:1024 sc1\n\t"
    "s_waitcnt lgkmcnt(0)\n\t"
    "s_memtime %[tc]\n\t"
    "s_nop 7\n\t"
    "s_branch 3b\n\t"
    "7:\n\t"
    "s_mov_b32 %[cap], 0\n\t"
    "s_branch 2b\n\t"
    "9:"
    : [tc] "=&s"(tc), [sp] "=&s"(sp), [cap] "+s"(cap)
    : [ag] "s"(ag), [vd] "s"(vd), [wl] "s"(wl), [wa] "s"(wa), [wb] "s"(wb), [ra] "s"(ra), [c44] "s"(0x4444u), "{v[64:95]}"(W.k0), "{v[96:127]}"(W.k1), "{v[128:159]}"(W.o0), "{v[160:191]}"(W.o1), "{v[192:223]}"(W.o2), "{v[224:255]}"(W.o3)
    : "v4", "v5", "v6", "v7", "v8", "v9", "v10", "v11", "v12", "v13", "v14", "v15", "v16", "v17", "v18", "v19", "v20", "v21", "v22", "v23", "v24", "v25", "v26", "v27", "v28", "v29", "v30", "v31", "v32", "v33", "v34", "v35", "v36", "v37", "v38", "v39", "v40", "v41", "v42", "v43", "v44", "v45", "v46", "v47", "v48", "v49", "v50", "v51", "v52", "v53", "v54", "v55", "v56", "v57", "v58", "v59", "v60", "v61", "v62", "v63", "vcc", "scc", "memory");
}
__device__ __forceinline__ void vr_v1(const VRW& W, const char* ag, const char* vd, unsigned wl, unsigned wa, unsigned wb, unsigned ra, unsigned& cap, unsigned& sp, unsigned long long& tc, unsigned long long* ts) {
  asm volatile("s_mov_b32 %[sp], 0\n\t"
    "v_mbcnt_lo_u32_b32 v20, -1, 0\n\t"
    "v_mbcnt_hi_u32_b32 v20, -1, v20\n\t"
    "v_lshlrev_b32 v21, 4, v20\n\t"
    "global_load_dwordx4 v[24:27], v21, %[ag] sc1\n\t"
    "global_load_dwordx4 v[28:31], v21, %[ag] offset:1024 sc1\n\t"
    "s_memtime %[tc]\n\t"
    "v_lshl_add_u32 v6, v20, 4, %[wl]\n\t"
    "ds_read_b128 v[32:35], v6 offset:0\n\t"
    "ds_read_b128 v[36:39], v6 offset:1024\n\t"
    "ds_read_b128 v[40:43], v6 offset:2048\n\t"
    "ds_read_b128 v[44:47], v6 offset:3072\n\t"
    "ds_read_b128 v[48:51], v6 offset:4096\n\t"
    "ds_read_b128 v[52:55], v6 offset:5120\n\t"
    "ds_read_b128 v[56:59], v6 offset:6144\n\t"
    "ds_read_b128 v[60:63], v6 offset:7168\n\t"
    "s_setprio 3\n\t"
    "v_and_b32 v6, 2, v20\n\t"
    "v_mul_u32_u24 v6, 0x5555, v6\n\t"
    "v_or_b32 v4, %[c44], v6\n\t"
    "v_and_b32 v6, 15, v20\n\t"
    "v_lshl_add_u32 v5, v6, 2, %[wa]\n\t"
    "v_and_b32 v6, 48, v20\n\t"
    "v_mad_u32_u24 v5, v6, 36, v5\n\t"
    "v_and_b32 v6, 32, v20\n\t"
    "v_mul_u32_u24 v7, 120, v6\n\t"
    "v_lshl_add_u32 v7, v20, 3, v7\n\t"
    "v_add_u32 v7, %[ra], v7\n\t"
    "3:\n\t"
    "v_mov_b32 v8, 0\n\t"
    "v_mov_b32 v9, 0\n\t"
    "v_mov_b32 v10, 0\n\t"
    "v_mov_b32 v11, 0\n\t"
    "v_mov_b32 v12, 0\n\t"
    "v_mov_b32 v13, 0\n\t"
    "v_mov_b32 v14, 0\n\t"
    "v_mov_b32 v15, 0\n\t"
    "v_mov_b32 v16, 0\n\t"
    "v_mov_b32 v17, 0\n\t"
    "v_mov_b32 v18, 0\n\t"
    "v_mov_b32 v19, 0\n\t"
    "v_mov_b32 v20, 0\n\t"
    "v_mov_b32 v21, 0\n\t"
    "v_mov_b32 v22, 0\n\t"
    "v_mov_b32 v23, 0\n\t"
    "s_waitcnt vmcnt(0) lgkmcnt(0)\n\t"
    "v_smfmac_f32_16x16x64_bf16 v[8:11], v[24:27], v[64:71], v4\n\t"
    "v_smfmac_f32_16x16x64_bf16 v[8:11], v[28:31], v[72:79], v4\n\t"
    "v_max3_u32 v6, v24, v25, v26\n\t"
    "v_max3_u32 v6, v6, v27, v28\n\t"
    "v_smfmac_f32_16x16x64_bf16 v[12:15], v[24:27], v[80:87], v4\n\t"
    "v_max3_u32 v6, v6, v29, v30\n\t"
    "v_max_u32 v6, v6, v31\n\t"
    "v_smfmac_f32_16x16x64_bf16 v[12:15], v[28:31], v[88:95], v4\n\t"
    "v_cmp_eq_u32 vcc, -1, v6\n\t"
    "s_cbranch_vccnz 1f\n\t"
    "2:\n\t"
    "v_smfmac_f32_16x16x64_bf16 v[16:19], v[24:27], v[96:103], v4\n\t"
    "v_add_f32 v8, v8, v10\n\t"
    "v_add_f32 v9, v9, v11\n\t"
    "ds_write2_b32 v5, v8, v9 offset0:0 offset1:72\n\t"
    "v_smfmac_f32_16x16x64_bf16 v[16:19], v[28:31], v[104:111], v4\n\t"
    "v_mov_b32 v8, 0\n\t"
    "v_mov_b32 v9, 0\n\t"
    "v_smfmac_f32_16x16x64_bf16 v[20:23], v[24:27], v[112:119], v4\n\t"
    "v_add_f32 v12, v12, v14\n\t"
    "v_add_f32 v13, v13, v15\n\t"
    "ds_write2_b32 v5, v12, v13 offset0:16 offset1:88\n\t"
    "v_smfmac_f32_16x16x64_bf16 v[20:23], v[28:31], v[120:127], v4\n\t"
    "v_mov_b32 v10, 0\n\t"
    "v_mov_b32 v11, 0\n\t"
    "v_add_f32 v16, v16, v18\n\t"
    "v_add_f32 v17, v17, v19\n\t"
    "ds_write2_b32 v5, v16, v17 offset0:32 offset1:104\n\t"
    "v_mov_b32 v12, 0\n\t"
    "v_mov_b32 v13, 0\n\t"
    "v_mov_b32 v14, 0\n\t"
    "v_mov_b32 v15, 0\n\t"
    "v_add_f32 v20, v20, v22\n\t"
    "v_add_f32 v21, v21, v23\n\t"
    "ds_write2_b32 v5, v20, v21 offset0:48 offset1:120\n\t"
    "s_waitcnt lgkmcnt(0)\n\t"
    "s_barrier\n\t"
    "v_smfmac_f32_16x16x64_bf16 v[8:11], v[24:27], v[32:39], v4\n\t"
    "v_smfmac_f32_16x16x64_bf16 v[8:11], v[28:31], v[40:47], v4\n\t"
    "v_smfmac_f32_16x16x64_bf16 v[12:15], v[24:27], v[48:55], v4\n\t"
    "v_smfmac_f32_16x16x64_bf16 v[12:15], v[28:31], v[56:63], v4\n\t"
    "v_mbcnt_lo_u32_b32 v17, -1, 0\n\t"
    "v_mbcnt_hi_u32_b32 v17, -1, v17\n\t"
    "v_lshl_add_u32 v16, v17, 3, %[wb]\n\t"
    "v_lshlrev_b32 v17, 2, v17\n\t"
    "s_nop 1\n\t"
    "v_add_f32 v8, v8, v10\n\t"
    "v_add_f32 v9, v9, v11\n\t"
    "v_add_f32 v12, v12, v14\n\t"
    "v_add_f32 v13, v13, v15\n\t"
    "ds_write2st64_b64 v16, v[8:9], v[12:13] offset1:1\n\t"
    "s_waitcnt lgkmcnt(0)\n\t"
    "s_barrier\n\t"
    "ds_read_b64 v[32:33], v7 offset:0\n\t"
    "ds_read_b64 v[34:35], v7 offset:1024\n\t"
    "ds_read_b64 v[36:37], v7 offset:2048\n\t"
    "ds_read_b64 v[38:39], v7 offset:3072\n\t"
    "s_waitcnt lgkmcnt(2)\n\t"
    "v_pk_add_f32 v[32:33], v[32:33], v[34:35]\n\t"
    "s_waitcnt lgkmcnt(0)\n\t"
    "v_pk_add_f32 v[36:37], v[36:37], v[38:39]\n\t"
    "v_pk_add_f32 v[32:33], v[32:33], v[36:37]\n\t"
    "s_nop 1\n\t"
    "v_permlane32_swap_b32 v32, v34\n\t"
    "v_permlane32_swap_b32 v33, v35\n\t"
    "s_mov_b32 exec_hi, 0\n\t"
    "v_pk_add_f32 v[32:33], v[32:33], v[34:35]\n\t"
    "v_cvt_pk_bf16_f32 v6, v32, v33\n\t"
    "v_min_u32 v6, -2, v6\n\t"
    "global_store_dword v17, v6, %[vd] sc1\n\t"
    "s_mov_b32 exec_hi, -1\n\t"
    "s_setprio 2\n\t"
    "s_waitcnt lgkmcnt(0)\n\t"
    "s_branch 9f\n\t"
    "1:\n\t"
    "s_add_u32 %[sp], %[sp], 1\n\t"
    "s_cmp_ge_u32 %[sp], %[cap]\n\t"
    "s_cbranch_scc1 7f\n\t"
    "v_mbcnt_lo_u32_b32 v20, -1, 0\n\t"
    "v_mbcnt_hi_u32_b32 v20, -1, v20\n\t"
    "v_lshlrev_b32 v21, 4, v20\n\t"
    "global_load_dwordx4 v[24:27], v21, %[ag] sc1\n\t"
    "global_load_dwordx4 v[28:31], v21, %[ag] offset:1024 sc1\n\t"
    "s_waitcnt lgkmcnt(0)\n\t"
    "s_memtime %[tc]\n\t"
    "s_nop 7\n\t"
    "s_branch 3b\n\t"
    "7:\n\t"
    "s_mov_b32 %[cap], 0\n\t"
    "s_branch 2b\n\t"
    "9:"
    : [tc] "=&s"(tc), [sp] "=&s"(sp), [cap] "+s"(cap)
    : [ag] "s"(ag), [vd] "s"(vd), [wl] "s"(wl), [wa] "s"(wa), [wb] "s"(wb), [ra] "s"(ra), [c44] "s"(0x4444u), "{v[64:95]}"(W.k0), "{v[96:127]}"(W.k1), "{v[128:159]}"(W.o0), "{v[160:191]}"(W.o1), "{v[192:223]}"(W.o2), "{v[224:255]}"(W.o3)
    : "v4", "v5", "v6", "v7", "v8", "v9", "v10", "v11", "v12", "v13", "v14", "v15", "v16", "v17", "v18", "v19", "v20", "v21", "v22", "v23", "v24", "v25", "v26", "v27", "v28", "v29", "v30", "v31", "v32", "v33", "v34", "v35", "v36", "v37", "v38", "v39", "v40", "v41", "v42", "v43", "v44", "v45", "v46", "v47", "v48", "v49", "v50", "v51", "v52", "v53", "v54", "v55", "v56", "v57", "v58", "v59", "v60", "v61", "v62", "v63", "vcc", "scc", "memory");
}
__device__ __forceinline__ void vr_r(const VRW& W, const char* os, const char* ad, unsigned wa, unsigned& cap, unsigned& sp, unsigned long long* ts) {
  asm volatile("v_mbcnt_lo_u32_b32 v4, -1, 0\n\t"
    "v_mbcnt_hi_u32_b32 v4, -1, v4\n\t"
    "v_lshlrev_b32 v5, 4, v4\n\t"
    "global_load_dwordx4 v[12:15], v5, %[os] offset:0 sc1\n\t"
    "global_load_dwordx4 v[16:19], v5, %[os] offset:1024 sc1\n\t"
    "global_load_dwordx4 v[20:23], v5, %[os] offset:2048 sc1\n\t"
    "global_load_dwordx4 v[24:27], v5, %[os] offset:3072 sc1\n\t"
    "s_mov_b32 %[sp], 0\n\t"
    "v_and_b32 v10, 1, v4\n\t"
    "v_mul_u32_u24 v10, 0xaaaa, v10\n\t"
    "v_or_b32 v6, %[c44], v10\n\t"
    "v_and_b32 v10, 15, v4\n\t"
    "v_lshl_add_u32 v7, v10, 2, %[wa]\n\t"
    "v_and_b32 v10, 48, v4\n\t"
    "v_mad_u32_u24 v7, v10, 12, v7\n\t"
    "v_and_b32 v10, 16, v4\n\t"
    "v_mul_u32_u24 v8, 24, v10\n\t"
    "v_and_b32 v10, 4, v4\n\t"
    "v_mad_u32_u24 v8, v10, 48, v8\n\t"
    "v_and_b32 v10, 32, v4\n\t"
    "v_lshl_add_u32 v8, v10, 1, v8\n\t"
    "v_and_b32 v10, 8, v4\n\t"
    "v_lshl_add_u32 v8, v10, 2, v8\n\t"
    "v_and_b32 v10, 3, v4\n\t"
    "v_lshl_add_u32 v8, v10, 3, v8\n\t"
    "v_add_u32 v8, 157696, v8\n\t"
    "v_and_b32 v10, 32, v4\n\t"
    "v_add_u32 v9, v10, v4\n\t"
    "v_lshlrev_b32 v9, 2, v9\n\t"
    "3:\n\t"
    "v_mov_b32 v28, 0\n\t"
    "v_mov_b32 v29, 0\n\t"
    "v_mov_b32 v30, 0\n\t"
    "v_mov_b32 v31, 0\n\t"
    "v_mov_b32 v32, 0\n\t"
    "v_mov_b32 v33, 0\n\t"
    "v_mov_b32 v34, 0\n\t"
    "v_mov_b32 v35, 0\n\t"
    "v_mov_b32 v36, 0\n\t"
    "v_mov_b32 v37, 0\n\t"
    "v_mov_b32 v38, 0\n\t"
    "v_mov_b32 v39, 0\n\t"
    "v_mov_b32 v40, 0\n\t"
    "v_mov_b32 v41, 0\n\t"
    "v_mov_b32 v42, 0\n\t"
    "v_mov_b32 v43, 0\n\t"
    "v_mov_b32 v10, 0\n\t"
    "s_waitcnt vmcnt(3)\n\t"
    "v_smfmac_f32_16x16x64_bf16 v[28:31], v[12:15], v[128:135], v6\n\t"
    "v_max3_u32 v10, v10, v12, v13\n\t"
    "v_smfmac_f32_16x16x64_bf16 v[32:35], v[12:15], v[136:143], v6\n\t"
    "v_max3_u32 v10, v10, v14, v15\n\t"
    "v_smfmac_f32_16x16x64_bf16 v[36:39], v[12:15], v[144:151], v6\n\t"
    "v_smfmac_f32_16x16x64_bf16 v[40:43], v[12:15], v[152:159], v6\n\t"
    "s_waitcnt vmcnt(2)\n\t"
    "v_smfmac_f32_16x16x64_bf16 v[28:31], v[16:19], v[160:167], v6\n\t"
    "v_max3_u32 v10, v10, v16, v17\n\t"
    "v_smfmac_f32_16x16x64_bf16 v[32:35], v[16:19], v[168:175], v6\n\t"
    "v_max3_u32 v10, v10, v18, v19\n\t"
    "v_smfmac_f32_16x16x64_bf16 v[36:39], v[16:19], v[176:183], v6\n\t"
    "v_smfmac_f32_16x16x64_bf16 v[40:43], v[16:19], v[184:191], v6\n\t"
    "s_waitcnt vmcnt(1)\n\t"
    "v_smfmac_f32_16x16x64_bf16 v[28:31], v[20:23], v[192:199], v6\n\t"
    "v_max3_u32 v10, v10, v20, v21\n\t"
    "v_smfmac_f32_16x16x64_bf16 v[32:35], v[20:23], v[200:207], v6\n\t"
    "v_max3_u32 v10, v10, v22, v23\n\t"
    "v_smfmac_f32_16x16x64_bf16 v[36:39], v[20:23], v[208:215], v6\n\t"
    "v_smfmac_f32_16x16x64_bf16 v[40:43], v[20:23], v[216:223], v6\n\t"
    "s_waitcnt vmcnt(0)\n\t"
    "v_smfmac_f32_16x16x64_bf16 v[28:31], v[24:27], v[224:231], v6\n\t"
    "v_max3_u32 v10, v10, v24, v25\n\t"
    "v_smfmac_f32_16x16x64_bf16 v[32:35], v[24:27], v[232:239], v6\n\t"
    "v_max3_u32 v10, v10, v26, v27\n\t"
    "v_smfmac_f32_16x16x64_bf16 v[36:39], v[24:27], v[240:247], v6\n\t"
    "v_cmp_eq_u32 vcc, -1, v10\n\t"
    "v_smfmac_f32_16x16x64_bf16 v[40:43], v[24:27], v[248:255], v6\n\t"
    "s_cbranch_vccnz 4f\n\t"
    "5:\n\t"
    "s_cmp_ge_u32 %[wa], 159232\n\t"
    "s_nop 1\n\t"
    "v_add_f32 v28, v28, v29\n\t"
    "v_add_f32 v32, v32, v33\n\t"
    "v_add_f32 v36, v38, v39\n\t"
    "v_add_f32 v28, v28, v36\n\t"
    "v_add_f32 v40, v42, v43\n\t"
    "v_add_f32 v32, v32, v40\n\t"
    "ds_write2_b32 v7, v28, v32 offset1:16\n\t"
    "s_waitcnt lgkmcnt(0)\n\t"
    "s_barrier\n\t"
    "s_cbranch_scc1 9f\n\t"
    "ds_read_b64 v[44:45], v8 offset:0\n\t"
    "ds_read_b64 v[46:47], v8 offset:768\n\t"
    "ds_read_b64 v[48:49], v8 offset:1536\n\t"
    "ds_read_b64 v[50:51], v8 offset:2304\n\t"
    "ds_read_b64 v[52:53], v8 offset:3072\n\t"
    "ds_read_b64 v[54:55], v8 offset:3840\n\t"
    "ds_read_b64 v[56:57], v8 offset:4608\n\t"
    "ds_read_b64 v[58:59], v8 offset:5376\n\t"
    "s_waitcnt lgkmcnt(6)\n\t"
    "v_pk_add_f32 v[44:45], v[44:45], v[46:47]\n\t"
    "s_waitcnt lgkmcnt(4)\n\t"
    "v_pk_add_f32 v[48:49], v[48:49], v[50:51]\n\t"
    "v_pk_add_f32 v[44:45], v[44:45], v[48:49]\n\t"
    "s_waitcnt lgkmcnt(2)\n\t"
    "v_pk_add_f32 v[52:53], v[52:53], v[54:55]\n\t"
    "s_waitcnt lgkmcnt(0)\n\t"
    "v_pk_add_f32 v[56:57], v[56:57], v[58:59]\n\t"
    "v_pk_add_f32 v[52:53], v[52:53], v[56:57]\n\t"
    "v_pk_add_f32 v[44:45], v[44:45], v[52:53]\n\t"
    "v_cvt_pk_bf16_f32 v10, v44, v45\n\t"
    "v_min_u32 v10, -2, v10\n\t"
    "global_store_dword v9, v10, %[ad] sc1\n\t"
    "s_waitcnt lgkmcnt(0)\n\t"
    "s_branch 9f\n\t"
    "4:\n\t"
    "s_add_u32 %[sp], %[sp], 1\n\t"
    "s_cmp_ge_u32 %[sp], %[cap]\n\t"
    "s_cbranch_scc1 7f\n\t"
    "global_load_dwordx4 v[12:15], v5, %[os] offset:0 sc1\n\t"
    "global_load_dwordx4 v[16:19], v5, %[os] offset:1024 sc1\n\t"
    "global_load_dwordx4 v[20:23], v5, %[os] offset:2048 sc1\n\t"
    "global_load_dwordx4 v[24:27], v5, %[os] offset:3072 sc1\n\t"
    "s_nop 3\n\t"
    "s_branch 3b\n\t"
    "7:\n\t"
    "s_mov_b32 %[cap], 0\n\t"
    "s_branch 5b\n\t"
    "9:"
    : [sp] "=&s"(sp), [cap] "+s"(cap)
    : [os] "s"(os), [ad] "s"(ad), [wa] "s"(wa), [c44] "s"(0x4444u), "{v[64:95]}"(W.k0), "{v[96:127]}"(W.k1), "{v[128:159]}"(W.o0), "{v[160:191]}"(W.o1), "{v[192:223]}"(W.o2), "{v[224:255]}"(W.o3)
    : "v4", "v5", "v6", "v7", "v8", "v9", "v10", "v11", "v12", "v13", "v14", "v15", "v16", "v17", "v18", "v19", "v20", "v21", "v22", "v23", "v24", "v25", "v26", "v27", "v28", "v29", "v30", "v31", "v32", "v33", "v34", "v35", "v36", "v37", "v38", "v39", "v40", "v41", "v42", "v43", "v44", "v45", "v46", "v47", "v48", "v49", "v50", "v51", "v52", "v53", "v54", "v55", "v56", "v57", "v58", "v59", "vcc", "scc", "memory");
}
__device__ __forceinline__ void kr_load_k0(VRW& W, unsigned a) {
  asm volatile("ds_read_b64_tr_b16 v[64:65], %[a] offset:0\n\t"
    "ds_read_b64_tr_b16 v[66:67], %[a] offset:256\n\t"
    "ds_read_b64_tr_b16 v[68:69], %[a] offset:4096\n\t"
    "ds_read_b64_tr_b16 v[70:71], %[a] offset:4352\n\t"
    "ds_read_b64_tr_b16 v[80:81], %[a] offset:32\n\t"
    "ds_read_b64_tr_b16 v[82:83], %[a] offset:288\n\t"
    "ds_read_b64_tr_b16 v[84:85], %[a] offset:4128\n\t"
    "ds_read_b64_tr_b16 v[86:87], %[a] offset:4384\n\t"
    "s_waitcnt lgkmcnt(0)\n\t"
    "ds_read_b64_tr_b16 v[96:97], %[a] offset:64\n\t"
    "ds_read_b64_tr_b16 v[98:99], %[a] offset:320\n\t"
    "ds_read_b64_tr_b16 v[100:101], %[a] offset:4160\n\t"
    "ds_read_b64_tr_b16 v[102:103], %[a] offset:4416\n\t"
    "ds_read_b64_tr_b16 v[112:113], %[a] offset:96\n\t"
    "ds_read_b64_tr_b16 v[114:115], %[a] offset:352\n\t"
    "ds_read_b64_tr_b16 v[116:117], %[a] offset:4192\n\t"
    "ds_read_b64_tr_b16 v[118:119], %[a] offset:4448\n\t"
    "s_waitcnt lgkmcnt(0)"
    : "=&{v[64:95]}"(W.k0), "=&{v[96:127]}"(W.k1) : [a] "v"(a) : "memory");
}
__device__ __forceinline__ void kr_load_k1(VRW& W, unsigned a) {
  asm volatile("ds_read_b64_tr_b16 v[72:73], %[a] offset:0\n\t"
    "ds_read_b64_tr_b16 v[74:75], %[a] offset:256\n\t"
    "ds_read_b64_tr_b16 v[76:77], %[a] offset:4096\n\t"
    "ds_read_b64_tr_b16 v[78:79], %[a] offset:4352\n\t"
    "ds_read_b64_tr_b16 v[88:89], %[a] offset:32\n\t"
    "ds_read_b64_tr_b16 v[90:91], %[a] offset:288\n\t"
    "ds_read_b64_tr_b16 v[92:93], %[a] offset:4128\n\t"
    "ds_read_b64_tr_b16 v[94:95], %[a] offset:4384\n\t"
    "s_waitcnt lgkmcnt(0)\n\t"
    "ds_read_b64_tr_b16 v[104:105], %[a] offset:64\n\t"
    "ds_read_b64_tr_b16 v[106:107], %[a] offset:320\n\t"
    "ds_read_b64_tr_b16 v[108:109], %[a] offset:4160\n\t"
    "ds_read_b64_tr_b16 v[110:111], %[a] offset:4416\n\t"
    "ds_read_b64_tr_b16 v[120:121], %[a] offset:96\n\t"
    "ds_read_b64_tr_b16 v[122:123], %[a] offset:352\n\t"
    "ds_read_b64_tr_b16 v[124:125], %[a] offset:4192\n\t"
    "ds_read_b64_tr_b16 v[126:127], %[a] offset:4448\n\t"
    "s_waitcnt lgkmcnt(0)"
    : "+&{v[64:95]}"(W.k0), "+&{v[96:127]}"(W.k1) : [a] "v"(a) : "memory");
}
__device__ __forceinline__ void kr_load_o0(VRW& W, unsigned a) {
  asm volatile("ds_read_b64_tr_b16 v[128:129], %[a] offset:0\n\t"
    "ds_read_b64_tr_b16 v[130:131], %[a] offset:128\n\t"
    "ds_read_b64_tr_b16 v[132:133], %[a] offset:1024\n\t"
    "ds_read_b64_tr_b16 v[134:135], %[a] offset:1152\n\t"
    "ds_read_b64_tr_b16 v[136:137], %[a] offset:32\n\t"
    "ds_read_b64_tr_b16 v[138:139], %[a] offset:160\n\t"
    "ds_read_b64_tr_b16 v[140:141], %[a] offset:1056\n\t"
    "ds_read_b64_tr_b16 v[142:143], %[a] offset:1184\n\t"
    "s_waitcnt lgkmcnt(0)\n\t"
    "ds_read_b64_tr_b16 v[144:145], %[a] offset:4096\n\t"
    "ds_read_b64_tr_b16 v[146:147], %[a] offset:4224\n\t"
    "ds_read_b64_tr_b16 v[148:149], %[a] offset:5120\n\t"
    "ds_read_b64_tr_b16 v[150:151], %[a] offset:5248\n\t"
    "ds_read_b64_tr_b16 v[152:153], %[a] offset:4128\n\t"
    "ds_read_b64_tr_b16 v[154:155], %[a] offset:4256\n\t"
    "ds_read_b64_tr_b16 v[156:157], %[a] offset:5152\n\t"
    "ds_read_b64_tr_b16 v[158:159], %[a] offset:5280\n\t"
    "s_waitcnt lgkmcnt(0)"
    : "=&{v[128:159]}"(W.o0) : [a] "v"(a) : "memory");
}
__device__ __forceinline__ void kr_load_o1(VRW& W, unsigned a) {
  asm volatile("ds_read_b64_tr_b16 v[160:161], %[a] offset:0\n\t"
    "ds_read_b64_tr_b16 v[162:163], %[a] offset:128\n\t"
    "ds_read_b64_tr_b16 v[164:165], %[a] offset:1024\n\t"
    "ds_read_b64_tr_b16 v[166:167], %[a] offset:1152\n\t"
    "ds_read_b64_tr_b16 v[168:169], %[a] offset:32\n\t"
    "ds_read_b64_tr_b16 v[170:171], %[a] offset:160\n\t"
    "ds_read_b64_tr_b16 v[172:173], %[a] offset:1056\n\t"
    "ds_read_b64_tr_b16 v[174:175], %[a] offset:1184\n\t"
    "s_waitcnt lgkmcnt(0)\n\t"
    "ds_read_b64_tr_b16 v[176:177], %[a] offset:4096\n\t"
    "ds_read_b64_tr_b16 v[178:179], %[a] offset:4224\n\t"
    "ds_read_b64_tr_b16 v[180:181], %[a] offset:5120\n\t"
    "ds_read_b64_tr_b16 v[182:183], %[a] offset:5248\n\t"
    "ds_read_b64_tr_b16 v[184:185], %[a] offset:4128\n\t"
    "ds_read_b64_tr_b16 v[186:187], %[a] offset:4256\n\t"
    "ds_read_b64_tr_b16 v[188:189], %[a] offset:5152\n\t"
    "ds_read_b64_tr_b16 v[190:191], %[a] offset:5280\n\t"
    "s_waitcnt lgkmcnt(0)"
    : "=&{v[160:191]}"(W.o1) : [a] "v"(a) : "memory");
}
__device__ __forceinline__ void kr_load_o2(VRW& W, unsigned a) {
  asm volatile("ds_read_b64_tr_b16 v[192:193], %[a] offset:0\n\t"
    "ds_read_b64_tr_b16 v[194:195], %[a] offset:128\n\t"
    "ds_read_b64_tr_b16 v[196:197], %[a] offset:1024\n\t"
    "ds_read_b64_tr_b16 v[198:199], %[a] offset:1152\n\t"
    "ds_read_b64_tr_b16 v[200:201], %[a] offset:32\n\t"
    "ds_read_b64_tr_b16 v[202:203], %[a] offset:160\n\t"
    "ds_read_b64_tr_b16 v[204:205], %[a] offset:1056\n\t"
    "ds_read_b64_tr_b16 v[206:207], %[a] offset:1184\n\t"
    "s_waitcnt lgkmcnt(0)\n\t"
    "ds_read_b64_tr_b16 v[208:209], %[a] offset:4096\n\t"
    "ds_read_b64_tr_b16 v[210:211], %[a] offset:4224\n\t"
    "ds_read_b64_tr_b16 v[212:213], %[a] offset:5120\n\t"
    "ds_read_b64_tr_b16 v[214:215], %[a] offset:5248\n\t"
    "ds_read_b64_tr_b16 v[216:217], %[a] offset:4128\n\t"
    "ds_read_b64_tr_b16 v[218:219], %[a] offset:4256\n\t"
    "ds_read_b64_tr_b16 v[220:221], %[a] offset:5152\n\t"
    "ds_read_b64_tr_b16 v[222:223], %[a] offset:5280\n\t"
    "s_waitcnt lgkmcnt(0)"
    : "=&{v[192:223]}"(W.o2) : [a] "v"(a) : "memory");
}
__device__ __forceinline__ void kr_load_o3(VRW& W, unsigned a) {
  asm volatile("ds_read_b64_tr_b16 v[224:225], %[a] offset:0\n\t"
    "ds_read_b64_tr_b16 v[226:227], %[a] offset:128\n\t"
    "ds_read_b64_tr_b16 v[228:229], %[a] offset:1024\n\t"
    "ds_read_b64_tr_b16 v[230:231], %[a] offset:1152\n\t"
    "ds_read_b64_tr_b16 v[232:233], %[a] offset:32\n\t"
    "ds_read_b64_tr_b16 v[234:235], %[a] offset:160\n\t"
    "ds_read_b64_tr_b16 v[236:237], %[a] offset:1056\n\t"
    "ds_read_b64_tr_b16 v[238:239], %[a] offset:1184\n\t"
    "s_waitcnt lgkmcnt(0)\n\t"
    "ds_read_b64_tr_b16 v[240:241], %[a] offset:4096\n\t"
    "ds_read_b64_tr_b16 v[242:243], %[a] offset:4224\n\t"
    "ds_read_b64_tr_b16 v[244:245], %[a] offset:5120\n\t"
    "ds_read_b64_tr_b16 v[246:247], %[a] offset:5248\n\t"
    "ds_read_b64_tr_b16 v[248:249], %[a] offset:4128\n\t"
    "ds_read_b64_tr_b16 v[250:251], %[a] offset:4256\n\t"
    "ds_read_b64_tr_b16 v[252:253], %[a] offset:5152\n\t"
    "ds_read_b64_tr_b16 v[254:255], %[a] offset:5280\n\t"
    "s_waitcnt lgkmcnt(0)"
    : "=&{v[224:255]}"(W.o3) : [a] "v"(a) : "memory");
}
__device__ __forceinline__ void qk_body(char* smem, int wave_id_, int xg, int h, int wg, const u16* __restrict__ Wq, const u16* __restrict__ Wv,
                                        const char* __restrict__ Kx, const char* __restrict__ V, char* ws, unsigned* tmo, unsigned* cnt,
                                        const char* xb, const char* ob) {
  const int wave = WAVE_ID;
  const int ty = wave >> 2, w4 = wave & 3;
  const char* oh = ob + (size_t)(h >> 2) * 16384 + (h & 3) * 1024;
  const char* wq = uni((const char*)(Wq + h * DH)); const char* wv = uni((const char*)(Wv + h * DH));
  auto stage2 = [&](int i) {
    const int tid = wave * 64 + lane_id();
    if (i < 6) {
      stage_proj<false>(wq + (size_t)i * 128 * HD * 2, Q_RING + ((2 * i) & 3) * 32768, tid, wave);
      if (i < 2) stage_proj<false>(wq + (size_t)(i + 6) * 128 * HD * 2, Q_RING + ((2 * i + 1) & 3) * 32768, tid, wave);
      else stage_proj<true>(wv + (size_t)(i - 2) * 256 * HD * 2, Q_RING + ((2 * i + 1) & 3) * 32768, tid, wave);
    }
  };
  auto rearm = [&](int ps) {
    unsigned z = SENT; asm volatile("" : "+v"(z));
    const unsigned l16 = lane_id() * 16;
#pragma unroll
    for (int r = 0; r < 2 * OREP; ++r) st16s(oh + (size_t)(ps * 4 + (r >> 1)) * 131072, (r & 1) * 4096u + l16, u32x4{z, z, z, z});
  };
  const char* dbw = xb + (size_t)48 * 16384 + 4096 + h * 128;
  QKW W;
  stage2(0); stage2(1);
#define WLD(i, w) do { if ((i) < 5) WAIT_V(8); else WAIT_V(0); \
    WBAR(); { const int l_ = lane_id(); const unsigned o_ = Q_RING + ty * 32768 + ((i) & 1) * 65536; const bool v_ = ty && (i) >= 2; \
      qk_wload<i>(w, proj_base(v_ ? w4 : 2 * w4, l_) + o_, proj_base(v_ ? w4 + 4 : 2 * w4 + 1, l_) + o_); } \
    WBAR(); stage2((i) + 2); if (wave == 0) STS(ws, wg, 1 + (i)); if ((i) - 4 == RDYI && opaque_tid() == 64) st4_sc1(cnt + (PQKV + h) * 32 + 3, 1u); } while (0)
  WLD(0, W.w0); WLD(1, W.w1); WLD(2, W.w2); WLD(3, W.w3); WLD(4, W.w4); WLD(5, W.w5);
#undef WLD
  if (wave == 4) *(volatile LAS u32x2*)(smem + Q_TM) = u32x2{0u, 0u};
  if (wave == 5) *(u32x4*)(smem + Q_QK + 2048 + lane_id() * 16) = u32x4{SENT, SENT, SENT, SENT};
  if (wave == 6) *(u32x4*)(smem + Q_PQ + 4096 + lane_id() * 16) = u32x4{0u, 0u, 0u, 0u};
  BAR();
  if (wave == 0) STS(ws, wg, 0);
  unsigned avail = 0;
  auto stage_kv = [&](int s) {
    if (s < NSEG - 1) {
      if (avail <= (unsigned)s) {
        for (int spins = 0;; ++spins) {
          avail = qkv_avail<1>(cnt + (PQKV + h) * 32);
          if (avail > (unsigned)s) break;
          __builtin_amdgcn_s_sleep(8);
          if (give_up(spins, tmo, 5)) break;
        }
      }
      const char* ks = uni(Kx + ((size_t)s * NH + h) * 8192); const char* vs = uni(V + ((size_t)s * NH + h) * 8192);
      const int lane = lane_id();
      head_to_lds(ks, lane, Q_K); head_to_lds(vs, lane, Q_V);
    }
  };
  auto loop = [&](auto w_c) __attribute__((always_inline)) {
    constexpr int WV = decltype(w_c)::value;
    if (WV == 6) stage_kv(0);
    unsigned t0 = 0, cap = 60000, d0 = 105, d1 = 200;
    int dq = D_Q;
    int sl = 0;
    for (int s = 0; s < NSEG; ++s) {
      TSDECL;
      const char* ag = xb + (size_t)((sl * 4 + (AREP == 4 ? xg : xg >> 1)) * 2 + (WV >> 2)) * 16384 + (WV < 4 ? WV * 2048 : WV * 1024);
      const char* kv = ws + OFF_KVM + ((size_t)s * NH + h) * 4096;
      const char* vs = WV == 4 ? dbw : WV == 5 ? kv + 2048 : xb + (size_t)(24 + sl * 8 + (h >> 2)) * 16384 + (h & 3) * 2048;
      const char* v2 = xb + (size_t)(52 + sl * 4 + (h >> 3)) * 16384 + (h & 7) * 1024 + (WV & 1) * 512;
      const char* od = oh + (size_t)sl * 524288;
      const unsigned sn = (unsigned)(s + 1) << 24;
      if (s > 0) wait_rt(t0, dq);
      TS(0);
      unsigned long long tc; unsigned sp;
      qk_step<WV>(W, ag, vs, v2, od, kv, sn, cap, sp, tc, nullptr, d0, d1);
      dq = (sp & 0xFFFFu) ? min(dq + DQU, D_Q + DQM) : max(dq - DQD, D_Q);
      if (WV < 4 || WV == 5) { WAIT_V(0); if (lane_id() == 0) st4_sc1(cnt + (PROG + h) * 32 + WV, (unsigned)(s + 1)); }
      if (WV == 6 && s + 1 < NSEG) stage_kv(s + 1);
      if (WV == 7 && s > 0 && s < NSEG - 1) rearm(sl == 0 ? 2 : sl - 1);
      if (s < NSEG - 1) {
        if (WV == 4) {
          delay64<E_B>();
          t0 = bell_time(xb + (size_t)(48 + xg) * 16384 + 4096, (lane_id() & 31) * 128, (unsigned)(s + 1), cap);
          *(volatile LAS u32x2*)(smem + Q_TM) = u32x2{(unsigned)(s + 1), t0};
        } else {
          for (;;) {
            const u32x2 v = *(volatile LAS u32x2*)(smem + Q_TM);
            t0 = __builtin_amdgcn_readfirstlane(v.y);
            if (__builtin_amdgcn_readfirstlane(v.x) == (unsigned)(s + 1)) break;
            __builtin_amdgcn_s_sleep(1);
          }
        }
      }
      sl = sl == 2 ? 0 : sl + 1;
    }
    if (cap == 0 && lane_id() == 0) st4_sc1(tmo, 1u);
  };
  switch (wave) {
    case 0: loop(std::integral_constant<int, 0>{}); break;
    case 1: loop(std::integral_constant<int, 1>{}); break;
    case 2: loop(std::integral_constant<int, 2>{}); break;
    case 3: loop(std::integral_constant<int, 3>{}); break;
    case 4: loop(std::integral_constant<int, 4>{}); break;
    case 5: loop(std::integral_constant<int, 5>{}); break;
    case 6: loop(std::integral_constant<int, 6>{}); break;
    default: loop(std::integral_constant<int, 7>{}); break;
  }
  __builtin_amdgcn_s_setprio(0);
}

__device__ __forceinline__ void vr_body(char* smem, int wave_id_, int xg, int h, int c, int wg, const u16* __restrict__ Wk, const u16* __restrict__ Wv,
                                        const u16* __restrict__ Wo, char* ws, unsigned* tmo, unsigned* cnt, const char* xb, const char* ob) {
  const int wave = WAVE_ID;
  const unsigned po = (unsigned)(h >> 4) * 16384u + ((h >> 1) & 7) * 1024 + (h & 1) * 512 + c * 128;
  const unsigned ko_ = (unsigned)(24 + (h >> 2)) * 16384u + (h & 3) * 2048 + c * 1024;
  const unsigned vo_ = (unsigned)(52 + (h >> 3)) * 16384u + (h & 7) * 1024 + c * 512;
  auto rearm = [&](int ps) {
    const int lane = lane_id();
    unsigned z = SENT; asm volatile("" : "+v"(z));
    const u32x4 sv = u32x4{z, z, z, z};
    static_assert(AREP == 2, "");
    st16s(xb, lane < 32 ? (unsigned)(ps * 8 + (lane >> 4) * 2) * 16384u + po + ((lane >> 3) & 1) * 256 + (lane & 7) * 16 : (unsigned)ps * 65536u + vo_ + (lane & 31) * 16, sv);
    st16s(xb, (unsigned)ps * 131072u + ko_ + lane * 16, sv);
  };
  const int hg = (wave + ROTW * ((2 * h + c) & 3)) & 7;
  VRW W;
  {
    const int lane = lane_id();
    const int g = lane >> 4, q4 = (lane & 15) >> 2, p4 = lane & 3, fr = 4 * g + (q4 & 1) + 32 * (q4 >> 1);
    const unsigned lw = wave * 16384;
    const char* sk = uni((const char*)(Wk + (size_t)(128 * wave) * HD + h * DH + 64 * c));
    const char* sv_ = uni((const char*)(Wv + (size_t)(128 * wave) * HD + h * DH + 64 + 32 * c));
    const char* so = uni((const char*)(Wo + (size_t)(512 * hg) * DM + 32 * h));
    const int kp = lane >> 2, sr = 32 * (kp >> 3) + 16 * ((kp >> 2) & 1) + (kp & 3);
    auto issue = [&](int p) {
#pragma unroll
      for (int i = 0; i < 8; ++i) {
        const unsigned d = lw + (p & 1) * 8192 + i * 1024;
        if (p < 2) gldsw(sk + (size_t)(8 * p + i) * 8 * HD * 2, (unsigned)((lane >> 3) * HD * 2 + (lane & 7) * 16), d);
        else if (p < 6) glds16s<0>(so + (size_t)(128 * (p - 2) + 64 * (i & 1) + 4 * (i >> 1)) * DM * 2, (unsigned)(sr * DM * 2 + p4 * 16), d);
        else glds16s<0>(sv_ + (size_t)i * 16 * HD * 2, (unsigned)(kp * HD * 2 + p4 * 16), d);
      }
    };
#define VTS(p) do { } while (0)
    const int fm = 16 * (g >> 1) + 4 * (g & 1) + (q4 & 1) + 8 * (q4 >> 1);
    const unsigned ak = lw + fm * 128 + p4 * 8, ao = lw + fr * 64 + p4 * 8;
    issue(0); issue(1);
    WAIT_V(8); kr_load_k0(W, ak); issue(2);
    WAIT_V(8); kr_load_k1(W, ak + 8192); issue(3);
    WAIT_V(8); kr_load_o0(W, ao); issue(4);
    WAIT_V(8); kr_load_o1(W, ao + 8192); issue(5);
    WAIT_V(8); kr_load_o2(W, ao); issue(6);
    WAIT_V(8); kr_load_o3(W, ao + 8192);
    WAIT_V(0);
    u32x8 x[4];
#pragma unroll
    for (int j = 0; j < 4; ++j) {
      const unsigned a = lw + fm * 64 + p4 * 8 + (j & 1) * 4096 + (j >> 1) * 32;
      const u32x2 t0 = trread(smem, a), t1 = trread(smem, a + 128), t2 = trread(smem, a + 2048), t3 = trread(smem, a + 2176);
      x[j] = u32x8{t0.x, t0.y, t1.x, t1.y, t2.x, t2.y, t3.x, t3.y};
    }
    char* d = smem + lw + 8192 + lane * 16;
#pragma unroll
    for (int j = 0; j < 4; ++j) { *(u32x4*)(d + j * 2048) = u32x4{x[j][0], x[j][1], x[j][2], x[j][3]}; *(u32x4*)(d + j * 2048 + 1024) = u32x4{x[j][4], x[j][5], x[j][6], x[j][7]}; }
    VTS(4);
  }
  BAR();
  if (wave == 0) STS(ws, wg, 0);
  __builtin_amdgcn_s_setprio(2);
  auto loop = [&](auto cl_c) __attribute__((always_inline)) {
    constexpr int CL = decltype(cl_c)::value;
    unsigned cap = 60000, tr = 0;
    int sl = 0;
    for (int s = 0; s < NSEG; ++s) {
      TSDECL;
      const int sn = sl == 2 ? 0 : sl + 1;
      const char* ag = xb + (size_t)((sl * 4 + (AREP == 4 ? xg : xg >> 1)) * 2 + (wave >> 2)) * 16384 + (wave & 3) * 2048;
      const char* vd = CL == 0 ? xb + (size_t)sl * 131072 + ko_ + (wave & 3) * 256 : xb + (size_t)sl * 65536 + vo_ + (wave & 3) * 128;
      const char* os = ob + (size_t)((sl * 4 + ORSEL) * 8 + hg) * 16384 + c * 4096;
      const char* ad = xb + (size_t)((sn * 4 + (wave & 3)) * 2) * 16384 + po;
      if (s > 0) delay64<E_AG>();
      TS(0);
      unsigned long long tc; unsigned sp;
      unsigned long long* tsp = nullptr;
      if (CL == 0) vr_v0(W, ag, vd, wave * 16384 + 8192, V_P1 + wave * 2304, V_PV + wave * 1024, V_P1 + (wave & 3) * 32, cap, sp, tc, tsp);
      else vr_v1(W, ag, vd, wave * 16384 + 8192, V_P1 + wave * 2304, V_PV + wave * 1024, V_PV + (wave & 3) * 256, cap, sp, tc, tsp);
      TN(12, sp + 1);
      if (s == NSEG - 1) break;
      if (wave == 7 && s > 0) rearm(sl == 0 ? 2 : sl - 1);
      wait_clk((unsigned)tc, E_O);
      TS(3);
      const unsigned tb = bell_time(xb + (size_t)(48 + xg) * 16384 + hg * 512, (lane_id() & 3) * 128, (unsigned)(s + 1), cap);
      wait_rt(tb, D_O);
      TS(4);
      vr_r(W, os, ad, V_P2 + wave * 768, cap, sp, tsp);
      TN(14, (sp & 0xFFFF) + 1);
      if (wave == 1) TSFLUSH(0x6078);
      sl = sn;
    }
    if (cap == 0 && lane_id() == 0) st4_sc1(tmo, 2u);
  };
  if ((wave >> 2) == CL0) loop(std::integral_constant<int, 0>{}); else loop(std::integral_constant<int, 1>{});
  __builtin_amdgcn_s_setprio(0);
}
#define GEMMX gemm7
#define PF0 0
#define NIMM 3
enum { T_NONE, T_QV, T_KR, T_WO, T_F1, T_F2, T_F2C, T_FIN, T_LN, T_SA };
__device__ const unsigned ROLES[256][6] = {
  {0x1, 0x0, 0x0, 0x0, 0x0, 0x0},
  {0x11, 0x0, 0x0, 0x0, 0x0, 0x0},
  {0x21, 0x0, 0x0, 0x0, 0x0, 0x0},
  {0x31, 0x0, 0x0, 0x0, 0x0, 0x0},
  {0x41, 0x0, 0x0, 0x0, 0x0, 0x0},
  {0x51, 0x0, 0x0, 0x0, 0x0, 0x0},
  {0x61, 0x0, 0x0, 0x0, 0x0, 0x0},
  {0x71, 0x0, 0x0, 0x0, 0x0, 0x0},
  {0x81, 0x0, 0x0, 0x0, 0x0, 0x0},
  {0x91, 0x0, 0x0, 0x0, 0x0, 0x0},
  {0xa1, 0x0, 0x0, 0x0, 0x0, 0x0},
  {0xb1, 0x0, 0x0, 0x0, 0x0, 0x0},
  {0x2, 0x0, 0x0, 0x0, 0x0, 0x0},
  {0x12, 0x0, 0x0, 0x0, 0x0, 0x0},
  {0x22, 0x0, 0x0, 0x0, 0x0, 0x0},
  {0x32, 0x0, 0x0, 0x0, 0x0, 0x0},
  {0x42, 0x0, 0x0, 0x0, 0x0, 0x0},
  {0x52, 0x0, 0x0, 0x0, 0x0, 0x0},
  {0x62, 0x0, 0x0, 0x0, 0x0, 0x0},
  {0x72, 0x0, 0x0, 0x0, 0x0, 0x0},
  {0x82, 0x0, 0x0, 0x0, 0x0, 0x0},
  {0x92, 0x0, 0x0, 0x0, 0x0, 0x0},
  {0xa2, 0x0, 0x0, 0x0, 0x0, 0x0},
  {0xb2, 0x0, 0x0, 0x0, 0x0, 0x0},
  {0xc2, 0x0, 0x0, 0x0, 0x0, 0x0},
  {0xd2, 0x0, 0x0, 0x0, 0x0, 0x0},
  {0xe2, 0x0, 0x0, 0x0, 0x0, 0x0},
  {0xf2, 0x0, 0x0, 0x0, 0x0, 0x0},
  {0x102, 0x0, 0x0, 0x0, 0x0, 0x0},
  {0x112, 0x0, 0x0, 0x0, 0x0, 0x0},
  {0x122, 0x0, 0x0, 0x0, 0x0, 0x0},
  {0x132, 0x0, 0x0, 0x0, 0x0, 0x0},
  {0xc1, 0x0, 0x0, 0x0, 0x0, 0x0},
  {0xd1, 0x0, 0x0, 0x0, 0x0, 0x0},
  {0xe1, 0x0, 0x0, 0x0, 0x0, 0x0},
  {0xf1, 0x0, 0x0, 0x0, 0x0, 0x0},
  {0x101, 0x0, 0x0, 0x0, 0x0, 0x0},
  {0x111, 0x0, 0x0, 0x0, 0x0, 0x0},
  {0x121, 0x0, 0x0, 0x0, 0x0, 0x0},
  {0x131, 0x0, 0x0, 0x0, 0x0, 0x0},
  {0x142, 0x0, 0x0, 0x0, 0x0, 0x0},
  {0x152, 0x0, 0x0, 0x0, 0x0, 0x0},
  {0x162, 0x0, 0x0, 0x0, 0x0, 0x0},
  {0x172, 0x0, 0x0, 0x0, 0x0, 0x0},
  {0x182, 0x0, 0x0, 0x0, 0x0, 0x0},
  {0x192, 0x0, 0x0, 0x0, 0x0, 0x0},
  {0x1a2, 0x0, 0x0, 0x0, 0x0, 0x0},
  {0x1b2, 0x0, 0x0, 0x0, 0x0, 0x0},
  {0x1c2, 0x0, 0x0, 0x0, 0x0, 0x0},
  {0x1d2, 0x0, 0x0, 0x0, 0x0, 0x0},
  {0x1e2, 0x0, 0x0, 0x0, 0x0, 0x0},
  {0x1f2, 0x0, 0x0, 0x0, 0x0, 0x0},
  {0x202, 0x0, 0x0, 0x0, 0x0, 0x0},
  {0x212, 0x0, 0x0, 0x0, 0x0, 0x0},
  {0x222, 0x0, 0x0, 0x0, 0x0, 0x0},
  {0x232, 0x0, 0x0, 0x0, 0x0, 0x0},
  {0x242, 0x0, 0x0, 0x0, 0x0, 0x0},
  {0x252, 0x0, 0x0, 0x0, 0x0, 0x0},
  {0x262, 0x0, 0x0, 0x0, 0x0, 0x0},
  {0x272, 0x0, 0x0, 0x0, 0x0, 0x0},
  {0x282, 0x0, 0x0, 0x0, 0x0, 0x0},
  {0x292, 0x0, 0x0, 0x0, 0x0, 0x0},
  {0x2a2, 0x0, 0x0, 0x0, 0x0, 0x0},
  {0x2b2, 0x0, 0x0, 0x0, 0x0, 0x0},
  {0x141, 0x0, 0x0, 0x0, 0x0, 0x0},
  {0x151, 0x0, 0x0, 0x0, 0x0, 0x0},
  {0x161, 0x0, 0x0, 0x0, 0x0, 0x0},
  {0x171, 0x0, 0x0, 0x0, 0x0, 0x0},
  {0x181, 0x0, 0x0, 0x0, 0x0, 0x0},
  {0x191, 0x0, 0x0, 0x0, 0x0, 0x0},
  {0x1a1, 0x0, 0x0, 0x0, 0x0, 0x0},
  {0x1b1, 0x0, 0x0, 0x0, 0x0, 0x0},
  {0x1c1, 0x0, 0x0, 0x0, 0x0, 0x0},
  {0x1d1, 0x0, 0x0, 0x0, 0x0, 0x0},
  {0x1e1, 0x0, 0x0, 0x0, 0x0, 0x0},
  {0x1f1, 0x0, 0x0, 0x0, 0x0, 0x0},
  {0x2c2, 0x0, 0x0, 0x0, 0x0, 0x0},
  {0x2d2, 0x0, 0x0, 0x0, 0x0, 0x0},
  {0x2e2, 0x0, 0x0, 0x0, 0x0, 0x0},
  {0x2f2, 0x0, 0x0, 0x0, 0x0, 0x0},
  {0x302, 0x0, 0x0, 0x0, 0x0, 0x0},
  {0x312, 0x0, 0x0, 0x0, 0x0, 0x0},
  {0x322, 0x0, 0x0, 0x0, 0x0, 0x0},
  {0x332, 0x0, 0x0, 0x0, 0x0, 0x0},
  {0x342, 0x0, 0x0, 0x0, 0x0, 0x0},
  {0x352, 0x0, 0x0, 0x0, 0x0, 0x0},
  {0x362, 0x0, 0x0, 0x0, 0x0, 0x0},
  {0x372, 0x0, 0x0, 0x0, 0x0, 0x0},
  {0x382, 0x0, 0x0, 0x0, 0x0, 0x0},
  {0x392, 0x0, 0x0, 0x0, 0x0, 0x0},
  {0x3a2, 0x0, 0x0, 0x0, 0x0, 0x0},
  {0x3b2, 0x0, 0x0, 0x0, 0x0, 0x0},
  {0x3c2, 0x0, 0x0, 0x0, 0x0, 0x0},
  {0x3d2, 0x0, 0x0, 0x0, 0x0, 0x0},
  {0x3e2, 0x0, 0x0, 0x0, 0x0, 0x0},
  {0x3f2, 0x0, 0x0, 0x0, 0x0, 0x0},
  {0x100003, 0x5402600, 0x0, 0x0, 0x0, 0x0},
  {0x100013, 0x5402604, 0x0, 0x0, 0x0, 0x0},
  {0x100023, 0x5402608, 0x0, 0x0, 0x0, 0x0},
  {0x100033, 0x540260c, 0x0, 0x0, 0x0, 0x0},
  {0x100043, 0x5402610, 0x0, 0x0, 0x0, 0x0},
  {0x100053, 0x5402614, 0x0, 0x0, 0x0, 0x0},
  {0x100063, 0x5402618, 0x0, 0x0, 0x0, 0x0},
  {0x100073, 0x540261c, 0x0, 0x0, 0x0, 0x0},
  {0x100083, 0x5402620, 0x0, 0x0, 0x0, 0x0},
  {0x100093, 0x5402624, 0x0, 0x0, 0x0, 0x0},
  {0x1000a3, 0x5402628, 0x0, 0x0, 0x0, 0x0},
  {0x1000b3, 0x540262c, 0x0, 0x0, 0x0, 0x0},
  {0x1000c3, 0x5402630, 0x0, 0x0, 0x0, 0x0},
  {0x1000d3, 0x5402634, 0x0, 0x0, 0x0, 0x0},
  {0x1000e3, 0x5402638, 0x0, 0x0, 0x0, 0x0},
  {0x1000f3, 0x540263c, 0x0, 0x0, 0x0, 0x0},
  {0x100103, 0x5402640, 0x0, 0x0, 0x0, 0x0},
  {0x100113, 0x5402644, 0x0, 0x0, 0x0, 0x0},
  {0x100123, 0x5402648, 0x0, 0x0, 0x0, 0x0},
  {0x100133, 0x540264c, 0x0, 0x0, 0x0, 0x0},
  {0x100143, 0x5402650, 0x0, 0x0, 0x0, 0x0},
  {0x100153, 0x5402654, 0x0, 0x0, 0x0, 0x0},
  {0x100163, 0x5402658, 0x0, 0x0, 0x0, 0x0},
  {0x100173, 0x540265c, 0x0, 0x0, 0x0, 0x0},
  {0x100183, 0x5402660, 0x0, 0x0, 0x0, 0x0},
  {0x100193, 0x5402664, 0x0, 0x0, 0x0, 0x0},
  {0x1001a3, 0x5402668, 0x0, 0x0, 0x0, 0x0},
  {0x1001b3, 0x540266c, 0x0, 0x0, 0x0, 0x0},
  {0x1001c3, 0x5402670, 0x0, 0x0, 0x0, 0x0},
  {0x1001d3, 0x5402674, 0x0, 0x0, 0x0, 0x0},
  {0x1001e3, 0x5402678, 0x0, 0x0, 0x0, 0x0},
  {0x1001f3, 0x540267c, 0x0, 0x0, 0x0, 0x0},
  {0x100064, 0x4260000, 0x0, 0x0, 0x0, 0x0},
  {0x100074, 0x4260004, 0x0, 0x0, 0x0, 0x0},
  {0x100084, 0x4260008, 0x0, 0x0, 0x0, 0x0},
  {0x100094, 0x426000c, 0x0, 0x0, 0x0, 0x0},
  {0x1000a4, 0x4260010, 0x0, 0x0, 0x0, 0x0},
  {0x1000d4, 0x4260014, 0x0, 0x0, 0x0, 0x0},
  {0x100034, 0x4260018, 0x0, 0x0, 0x0, 0x0},
  {0x1000b4, 0x426001c, 0x0, 0x0, 0x0, 0x0},
  {0x200000, 0x400001, 0x1402e02, 0x0, 0x0, 0x0},
  {0x200007, 0x400005, 0x1402e06, 0x0, 0x0, 0x0},
  {0x200017, 0x400009, 0x1402e0a, 0x0, 0x0, 0x0},
  {0x200027, 0x40000d, 0x1402e0e, 0x0, 0x0, 0x0},
  {0x200037, 0x400011, 0x140341e, 0x0, 0x0, 0x0},
  {0x100025, 0x340015, 0x0, 0x0, 0x0, 0x0},
  {0x100035, 0x340019, 0x0, 0x0, 0x0, 0x0},
  {0x100045, 0x34001d, 0x0, 0x0, 0x0, 0x0},
  {0x100004, 0x2e0002, 0x0, 0x0, 0x0, 0x0},
  {0x100014, 0x2e0006, 0x0, 0x0, 0x0, 0x0},
  {0x100024, 0x2e000a, 0x0, 0x0, 0x0, 0x0},
  {0x1000c4, 0x2e000e, 0x0, 0x0, 0x0, 0x0},
  {0x100044, 0x2e0012, 0x0, 0x0, 0x0, 0x0},
  {0x100054, 0x2e0016, 0x0, 0x0, 0x0, 0x0},
  {0x100005, 0x34001a, 0x0, 0x0, 0x0, 0x0},
  {0x100015, 0x34001e, 0x0, 0x0, 0x0, 0x0},
  {0x201055, 0x1403415, 0x140341d, 0x0, 0x0, 0x0},
  {0x202065, 0x1403419, 0x140341a, 0x0, 0x0, 0x0},
  {0x3029, 0x0, 0x0, 0x0, 0x0, 0x0},
  {0x4039, 0x0, 0x0, 0x0, 0x0, 0x0},
  {0x105008, 0x1402e12, 0x0, 0x0, 0x0, 0x0},
  {0x106018, 0x1402e16, 0x0, 0x0, 0x0, 0x0},
  {0x7009, 0x0, 0x0, 0x0, 0x0, 0x0},
  {0x8019, 0x0, 0x0, 0x0, 0x0, 0x0},
  {0x100144, 0x4260020, 0x0, 0x0, 0x0, 0x0},
  {0x100154, 0x4260024, 0x0, 0x0, 0x0, 0x0},
  {0x100164, 0x4260028, 0x0, 0x0, 0x0, 0x0},
  {0x100174, 0x426002c, 0x0, 0x0, 0x0, 0x0},
  {0x100184, 0x4260030, 0x0, 0x0, 0x0, 0x0},
  {0x100194, 0x4260034, 0x0, 0x0, 0x0, 0x0},
  {0x100134, 0x4260038, 0x0, 0x0, 0x0, 0x0},
  {0x1001b4, 0x426003c, 0x0, 0x0, 0x0, 0x0},
  {0x200000, 0x400021, 0x1402e22, 0x0, 0x0, 0x0},
  {0x200047, 0x400025, 0x1402e26, 0x0, 0x0, 0x0},
  {0x200057, 0x400029, 0x1402e2a, 0x0, 0x0, 0x0},
  {0x200067, 0x40002d, 0x1402e2e, 0x0, 0x0, 0x0},
  {0x200077, 0x400031, 0x140343e, 0x0, 0x0, 0x0},
  {0x1000a5, 0x340035, 0x0, 0x0, 0x0, 0x0},
  {0x1000b5, 0x340039, 0x0, 0x0, 0x0, 0x0},
  {0x1000c5, 0x34003d, 0x0, 0x0, 0x0, 0x0},
  {0x1000e4, 0x2e0022, 0x0, 0x0, 0x0, 0x0},
  {0x1000f4, 0x2e0026, 0x0, 0x0, 0x0, 0x0},
  {0x100104, 0x2e002a, 0x0, 0x0, 0x0, 0x0},
  {0x100114, 0x2e002e, 0x0, 0x0, 0x0, 0x0},
  {0x100124, 0x2e0032, 0x0, 0x0, 0x0, 0x0},
  {0x1001a4, 0x2e0036, 0x0, 0x0, 0x0, 0x0},
  {0x100085, 0x34003a, 0x0, 0x0, 0x0, 0x0},
  {0x100095, 0x34003e, 0x0, 0x0, 0x0, 0x0},
  {0x2090d5, 0x1403435, 0x140343d, 0x0, 0x0, 0x0},
  {0x20a0e5, 0x1403439, 0x140343a, 0x0, 0x0, 0x0},
  {0xb069, 0x0, 0x0, 0x0, 0x0, 0x0},
  {0xc079, 0x0, 0x0, 0x0, 0x0, 0x0},
  {0x10d028, 0x1402e32, 0x0, 0x0, 0x0, 0x0},
  {0x10e038, 0x1402e36, 0x0, 0x0, 0x0, 0x0},
  {0xf049, 0x0, 0x0, 0x0, 0x0, 0x0},
  {0x10059, 0x0, 0x0, 0x0, 0x0, 0x0},
  {0x100224, 0x4260040, 0x0, 0x0, 0x0, 0x0},
  {0x100284, 0x4260044, 0x0, 0x0, 0x0, 0x0},
  {0x100244, 0x4260048, 0x0, 0x0, 0x0, 0x0},
  {0x100254, 0x426004c, 0x0, 0x0, 0x0, 0x0},
  {0x100264, 0x4260050, 0x0, 0x0, 0x0, 0x0},
  {0x100274, 0x4260054, 0x0, 0x0, 0x0, 0x0},
  {0x100234, 0x4260058, 0x0, 0x0, 0x0, 0x0},
  {0x100294, 0x426005c, 0x0, 0x0, 0x0, 0x0},
  {0x200000, 0x400041, 0x1402e42, 0x0, 0x0, 0x0},
  {0x200087, 0x400045, 0x1402e46, 0x0, 0x0, 0x0},
  {0x200097, 0x400049, 0x1402e4a, 0x0, 0x0, 0x0},
  {0x2000a7, 0x40004d, 0x1402e4e, 0x0, 0x0, 0x0},
  {0x2000b7, 0x400051, 0x140345e, 0x0, 0x0, 0x0},
  {0x100125, 0x340055, 0x0, 0x0, 0x0, 0x0},
  {0x100135, 0x340059, 0x0, 0x0, 0x0, 0x0},
  {0x100145, 0x34005d, 0x0, 0x0, 0x0, 0x0},
  {0x1001c4, 0x2e0042, 0x0, 0x0, 0x0, 0x0},
  {0x1001d4, 0x2e0046, 0x0, 0x0, 0x0, 0x0},
  {0x1001e4, 0x2e004a, 0x0, 0x0, 0x0, 0x0},
  {0x1001f4, 0x2e004e, 0x0, 0x0, 0x0, 0x0},
  {0x100204, 0x2e0052, 0x0, 0x0, 0x0, 0x0},
  {0x100214, 0x2e0056, 0x0, 0x0, 0x0, 0x0},
  {0x100105, 0x34005a, 0x0, 0x0, 0x0, 0x0},
  {0x100115, 0x34005e, 0x0, 0x0, 0x0, 0x0},
  {0x211155, 0x1403455, 0x140345d, 0x0, 0x0, 0x0},
  {0x212165, 0x1403459, 0x140345a, 0x0, 0x0, 0x0},
  {0x130a9, 0x0, 0x0, 0x0, 0x0, 0x0},
  {0x140b9, 0x0, 0x0, 0x0, 0x0, 0x0},
  {0x115048, 0x1402e52, 0x0, 0x0, 0x0, 0x0},
  {0x116058, 0x1402e56, 0x0, 0x0, 0x0, 0x0},
  {0x17089, 0x0, 0x0, 0x0, 0x0, 0x0},
  {0x18099, 0x0, 0x0, 0x0, 0x0, 0x0},
  {0x100304, 0x4260060, 0x0, 0x0, 0x0, 0x0},
  {0x100314, 0x4260064, 0x0, 0x0, 0x0, 0x0},
  {0x100324, 0x4260068, 0x0, 0x0, 0x0, 0x0},
  {0x100374, 0x426006c, 0x0, 0x0, 0x0, 0x0},
  {0x100344, 0x4260070, 0x0, 0x0, 0x0, 0x0},
  {0x100354, 0x4260074, 0x0, 0x0, 0x0, 0x0},
  {0x1002b4, 0x4260078, 0x0, 0x0, 0x0, 0x0},
  {0x100334, 0x426007c, 0x0, 0x0, 0x0, 0x0},
  {0x200000, 0x400061, 0x1402e62, 0x0, 0x0, 0x0},
  {0x2000c7, 0x400065, 0x1402e66, 0x0, 0x0, 0x0},
  {0x2000d7, 0x400069, 0x1402e6a, 0x0, 0x0, 0x0},
  {0x2000e7, 0x40006d, 0x1402e6e, 0x0, 0x0, 0x0},
  {0x2000f7, 0x400071, 0x140347e, 0x0, 0x0, 0x0},
  {0x100175, 0x340075, 0x0, 0x0, 0x0, 0x0},
  {0x100006, 0x340079, 0x0, 0x0, 0x0, 0x0},
  {0x100016, 0x34007d, 0x0, 0x0, 0x0, 0x0},
  {0x1002a4, 0x2e0062, 0x0, 0x0, 0x0, 0x0},
  {0x100364, 0x2e0066, 0x0, 0x0, 0x0, 0x0},
  {0x1002c4, 0x2e006a, 0x0, 0x0, 0x0, 0x0},
  {0x1002d4, 0x2e006e, 0x0, 0x0, 0x0, 0x0},
  {0x1002e4, 0x2e0072, 0x0, 0x0, 0x0, 0x0},
  {0x1002f4, 0x2e0076, 0x0, 0x0, 0x0, 0x0},
  {0x100075, 0x34007a, 0x0, 0x0, 0x0, 0x0},
  {0x1000f5, 0x34007e, 0x0, 0x0, 0x0, 0x0},
  {0x219026, 0x1403475, 0x140347d, 0x0, 0x0, 0x0},
  {0x21a036, 0x1403479, 0x140347a, 0x0, 0x0, 0x0},
  {0x1b0e9, 0x0, 0x0, 0x0, 0x0, 0x0},
  {0x1c0f9, 0x0, 0x0, 0x0, 0x0, 0x0},
  {0x11d068, 0x1402e72, 0x0, 0x0, 0x0, 0x0},
  {0x11e078, 0x1402e76, 0x0, 0x0, 0x0, 0x0},
  {0x1f0c9, 0x0, 0x0, 0x0, 0x0, 0x0},
  {0x200d9, 0x0, 0x0, 0x0, 0x0, 0x0},
};
__global__ void __launch_bounds__(512, 2) k_mega(const u16* __restrict__ Wq, const u16* __restrict__ Wk, const u16* __restrict__ Wv,
                                                 const u16* __restrict__ Wo, const u16* __restrict__ w1, const u16* __restrict__ w3,
                                                 const u16* __restrict__ w2, const u16* __restrict__ x, const u16* __restrict__ cvec,
                                                 const u16* __restrict__ wmod, char* ws, u16* out) {
  extern __shared__ __attribute__((aligned(16))) char smem[];
  int wave_id_ = __builtin_amdgcn_readfirstlane((int)threadIdx.x >> 6);
  asm volatile("" : "+s"(wave_id_));
  unsigned* tmo = (unsigned*)(ws + OFF_TMO); unsigned* tick = (unsigned*)(ws + OFF_CNT);
  unsigned long long* mts = nullptr;
  const float* mod = (const float*)(ws + OFF_MOD);
  volatile LAS unsigned* tslot = (volatile LAS unsigned*)(smem + L_SLOT + 768);
  if (opaque_tid() == 0) {
    unsigned xcc; asm volatile("s_getreg_b32 %0, hwreg(HW_REG_XCC_ID)" : "=s"(xcc));
    const int4* cq = (const int4*)(ws + OFF_CAL + 4 * NCALW * 4);
    const int4 ca = cq[0], cb = cq[1], cc = cq[2], cd = cq[3];
    unsigned sl = 0xFFFFu;
    for (int i = 0; i < 8; ++i) {
      const unsigned xg = (xcc + i) & 7;
      const unsigned t = __hip_atomic_fetch_add(tick + (TICK + xg) * 32, 1u, __ATOMIC_RELAXED, __HIP_MEMORY_SCOPE_AGENT);
      if (t < 32) { sl = xg * 32 + t; break; }
    }
    const int sum = ca.x + ca.y + ca.z + ca.w + cb.x + cb.y + cb.z + cb.w + cc.x + cc.y + cc.z + cc.w + cd.x + cd.y + cd.z + cd.w;
    *tslot = sl | ((unsigned)((sum > 0 ? 0 : 1) ^ XPARC) << 16);
  }
  __syncthreads();
  const unsigned tsl = __builtin_amdgcn_readfirstlane(*tslot);
  __syncthreads();
  const int slot = (int)(tsl & 0xFFFFu);
  unsigned* cnt = (unsigned*)(xch_base(ws) + ((size_t)4 << 21) + (size_t)((tsl >> 16) << 13));
  if (slot >= 256) return;
  const int xg = slot >> 5;
  const unsigned rw = ROLES[slot][0];
  const int role = rw & 15, idx = (rw >> 4) & 255;
  const char* cb = uni((const char*)cnt);
  if (role == T_QV || role == T_KR) {
    char* xch = xch_base(ws);
    const int* cal = (const int*)(ws + OFF_CAL);
    auto chunk = [&](int c) { int sum = 0; for (int w = 0; w < NCALW; ++w) sum += cal[c * NCALW + w]; return uni(xch + ((size_t)c << 21) + (XPARF(sum) << 13)); };
    const char* xb = chunk(3);
    const char* ob = chunk(0);
    const int wg = role == T_QV ? (idx >> 3) * 32 + (idx & 7) : (idx >> 4) * 32 + 8 + (idx & 15);
    if (role == T_QV) qk_body(smem, wave_id_, xg, idx, wg, Wq, Wv, ws + OFF_K, ws + OFF_V, ws, tmo, cnt, xb, ob);
    else vr_body(smem, wave_id_, xg, idx >> 1, idx & 1, wg, Wk, Wv, Wo, ws, tmo, cnt, xb, ob);
    return;
  }
  if ((rw >> 12) & 255) l1_role(smem, wave_id_, ((rw >> 12) & 255) - 1, x, mod, cvec, wmod, ws + OFF_H1, cnt, tmo, mts);
  if (rw >> 24) {
    const int tid = opaque_tid();
    const size_t o = (size_t)(tid >> 4) * HD + ((rw >> 24) - 1) * DH + (tid & 15) * 8;
    u32x4 acc = u32x4{0u, 0u, 0u, 0u};
#pragma unroll 1
    for (int i = PF0; i < 4; ++i) {
      u32x4 v[8];
#pragma unroll
      for (int j = 0; j < 8; ++j) v[j] = ldnt<4>(Wv + o + (size_t)(i * 256 + 32 * j) * HD);
#pragma unroll
      for (int j = 0; j < 8; ++j) acc ^= v[j];
    }
    asm volatile("" :: "v"(acc));
  }
  const int ntask = (rw >> 20) & 15;
  if (ntask == 0 && role == T_WO) { if (wave_id_ == 0) wait_k0(cnt, idx, tmo); __syncthreads(); }
  for (int it = 0; it < ntask; ++it) {
    const unsigned tk = ROLES[slot][1 + it];
    const int m = tk & 3, h = (tk >> 2) & 31, bo = (tk >> 8) & 255, ns = ((tk >> 16) & 255) - bo, part = (tk >> 24) & 3;
    const int lane = lane_id();
    const Dep d{cb, (unsigned)(PL1 + (lane & 31)) * 128, 8u, (unsigned)-bo};
    if (wave_id_ == 0) {
      if ((tk >> 26) & 1) wait_k0(cnt, h, tmo);
      if (bo) wait_l1(cnt, 8u, tmo);
    }
    __syncthreads();
    gemm7<R_QKV>(smem, wave_id_, (m == 0 ? Wq : (m == 1 ? Wk : Wv)) + h * DH, nullptr, HD, ws + OFF_H1 + (size_t)bo * 65536, 65536, d,
                    ws + (m == 0 ? OFF_Q : (m == 1 ? OFF_K : OFF_V)) + h * 8192 + (size_t)bo * 32 * 8192, 32 * 8192,
                    cnt + (PQKV + h) * 32 + 4 * part + m, tmo, part ? 319 : (h >> 3) * 32 + m * 8 + (h & 7), mts, false,
                    m == 0 || bo ? 0 : NIMM, ns, bo ? ns : 0, bo * 257);
  }
  __syncthreads();
  if (role == T_WO || role == T_F2) {
    const bool wo = role == T_WO;
    const int ks = idx >> 3, cn = idx & 7;
    const int lane = lane_id();
    const Dep d{cb, (unsigned)(wo ? PSA + 4 * ks + (lane & 3) : PF1 + 16 * ks + (lane & 15)) * 128, wo ? 4u : 1u, wo ? (unsigned)(lane & 3) : 0u};
    gemm7<R_PART>(smem, wave_id_, (wo ? Wo : w2) + (size_t)ks * 1024 * DM + cn * 128, nullptr, DM, ws + (wo ? OFF_O : OFF_G) + ks * 16 * 4096, (wo ? 64 : 56) * 4096, d,
                  ws + (wo ? OFF_P : OFF_P2) + (ks * DM + cn * 128) * (TLP ? 64 : 2), (size_t)32 * PROW, cnt + ((wo ? PWO : PF2) + idx) * 32, tmo,
                  wo ? 128 + ks * 32 + cn : 128 + (idx / 7) * 32 + 22 + idx % 7, mts, !wo);
  } else if (role == T_F2C) {
    const int lane = lane_id();
    const Dep d{cb, (unsigned)(PF1 + 48 + (lane & 7)) * 128, 1u, 0u};
    gemm7<R_PART2>(smem, wave_id_, w2 + (size_t)3072 * DM + idx * 256, nullptr, DM, ws + OFF_G + 48 * 4096, 56 * 4096, d,
                   ws + OFF_P2 + (3 * DM + idx * 256) * (TLP ? 64 : 2), (size_t)32 * PROW, cnt + (PF2 + 24 + idx) * 32, tmo, 128 + 96 + 25 + idx, mts, true);
  } else if (role == T_F1) {
    const int lane = lane_id();
    const Dep d{cb, (unsigned)((PLN + (lane >> 3)) * 128 + (lane & 7) * 4), 1u, 0u};
    gemm7<R_F1>(smem, wave_id_, w1 + 64 * idx, w3 + 64 * idx, FF, ws + OFF_H2, 65536, d, ws + OFF_G + idx * 4096, 56 * 4096, cnt + (PF1 + idx) * 32, tmo,
                128 + (idx / 14) * 32 + 8 + idx % 14, mts, false);
  } else if (role == T_FIN) {
    fin_role(smem, wave_id_, idx, mod, ws + OFF_P2, (const u16*)(ws + OFF_X1), out, cnt, tmo, mts);
  } else if (role == T_LN) {
    row_role(smem, wave_id_, 8 * (idx >> 1), idx & 1, mod, ws + OFF_P, x, (u16*)(ws + OFF_X1), ws + OFF_H2, cnt, tmo, 256 + (idx >> 1), mts);
  } else if (role == T_SA) {
    sa_role(smem, wave_id_, idx & 3, 8 * (idx >> 2), ws + OFF_Q, ws + OFF_K, ws + OFF_V, ws + OFF_KVM, ws + OFF_O, cnt, tmo, 128 + (idx >> 2) * 32 + 30 + (idx & 1), mts);
  }
}

#define PPF 16
#define PPV 0
constexpr int NARM = 8 + 157 + (NSEG - SDIR) * 8;
#define NCS 6
__global__ void __launch_bounds__(256) k_prep(const u16* __restrict__ c, const u16* __restrict__ wmod, const u16* __restrict__ mem0,
                                              float* __restrict__ mod, char* __restrict__ ws, unsigned* __restrict__ cnt,
                                              const u16* __restrict__ Wq, const u16* __restrict__ Wk, const u16* __restrict__ Wv) {
  const int t = threadIdx.x;
  int b = blockIdx.x;
  char* xch = xch_base(ws);
  if (b < NCALB) {
    if (t >= 64) return;
    const int ci = b % 3, w = b / 3, chunk = ci == 0 ? 0 : ci + 2;
    char* base = xch + ((size_t)chunk << 21) + 127 * 16384 + w * 512;
    st16_sc1(base + (t >> 5) * 8192 + (t & 31) * 16, u32x4{SENT, SENT, SENT, SENT});
    asm volatile("s_waitcnt vmcnt(0)" ::: "memory");
    unsigned best[2] = {1u << 30, 1u << 30};
    const char* sb = uni(base);
#pragma unroll
    for (int r = 1; r < NCS; ++r) {
      unsigned long long t0, t1; unsigned v;
      asm volatile("s_memtime %0\n\ts_waitcnt lgkmcnt(0)\n\ts_load_dword %2, %3, %4 glc\n\ts_waitcnt lgkmcnt(0)\n\ts_memtime %1\n\ts_waitcnt lgkmcnt(0)"
                   : "=&s"(t0), "=&s"(t1), "=&s"(v) : "s"(sb), "i"((r & 1) * 8192 + (r >> 1) * 128) : "memory");
      if (r > 1) best[r & 1] = min(best[r & 1], (unsigned)(t1 - t0));
    }
    unsigned xcc; asm volatile("s_getreg_b32 %0, hwreg(HW_REG_XCC_ID)" : "=s"(xcc));
    const int d = max(-250, min(250, (int)best[1] - (int)best[0]));
    if (t == 0) ((int*)(ws + OFF_CAL))[chunk * NCALW + w] = (xcc & 4) ? -d : d;
    return;
  }
  b -= NCALB;
  if (b < NMODB) {
    __shared__ float red[32][65];
    const int cgp = b & 31, ksl = b >> 5;
    const int cg = t & 7, r0 = t >> 3, n0 = cgp * 64 + cg * 8;
    float acc[8] = {0, 0, 0, 0, 0, 0, 0, 0};
    u32x4 w[4]; u16 cv16[4];
#pragma unroll
    for (int i = 0; i < 4; ++i) {
      const int k = ksl * 128 + r0 + 32 * i;
      w[i] = ldnt<1>(wmod + (size_t)k * (6 * DM) + n0);
      cv16[i] = c[k];
    }
#pragma unroll
    for (int i = 0; i < 4; ++i) {
      const float cv = bf2f(cv16[i]);
      const float sv = bfr(cv / (1.0f + __expf(-cv)));
      acc[0] += sv * bflo(w[i].x); acc[1] += sv * bfhi(w[i].x); acc[2] += sv * bflo(w[i].y); acc[3] += sv * bfhi(w[i].y);
      acc[4] += sv * bflo(w[i].z); acc[5] += sv * bfhi(w[i].z); acc[6] += sv * bflo(w[i].w); acc[7] += sv * bfhi(w[i].w);
    }
#pragma unroll
    for (int e = 0; e < 8; ++e) red[r0][cg * 8 + e] = acc[e];
    __syncthreads();
    if (t < 64) {
      float s = 0;
#pragma unroll
      for (int r = 0; r < 32; ++r) s += red[r][t];
      mod[ksl * (2 * DM) + cgp * 64 + t] = s;
    }
    return;
  }
  b -= NMODB;
  if (b >= NARM) {
    unsigned xcc; asm volatile("s_getreg_b32 %0, hwreg(HW_REG_XCC_ID)" : "=s"(xcc));
    const int j = (b - NARM) >> 3, xq = xcc & 3;
    const bool wk = xcc & 4;
    u32x4 acc = u32x4{0u, 0u, 0u, 0u};
#pragma unroll
    for (int i = 0; i < PPF / 2; ++i) {
      const int n = (j >> 1) * PPF + 2 * i + (t >> 7);
      const int row = wk ? (n >> 1) + ((n & 1) << 9) : n + ((j & 1) ? 768 : 0);
      acc ^= __builtin_nontemporal_load((const u32x4*)((wk ? ((j & 1) ? Wv : Wk) : Wq) + (size_t)row * HD + xq * 1024 + (t & 127) * 8));
    }
    if (!wk) {
#pragma unroll
      for (int i = 0; i < PPV / 2; ++i) {
        const int n = j * PPV + 2 * i + (t >> 7);
        acc ^= __builtin_nontemporal_load((const u32x4*)(Wk + (size_t)((n & 7) * 128 + (n >> 3)) * HD + xq * 1024 + (t & 127) * 8));
      }
    }
    asm volatile("" :: "v"(acc));
    return;
  }
  if (b >= 8) {
    b -= 8;
    char* p = b < 96 ? xch + (size_t)b * 16384 : (b < 152 ? xch + ((size_t)3 << 21) + (size_t)(b - 96 + 8) * 16384 : xch + ((size_t)4 << 21) + (size_t)(b - 152) * 16384);
    if (b >= 157) p = ws + OFF_KVM + (size_t)SDIR * NH * 4096 + (size_t)(b - 157) * 16384;
    const unsigned v = b < 96 + 40 || (b >= 140 && b < 152) || b >= 157 ? SENT : 0u;
#pragma unroll
    for (int i = 0; i < 4; ++i) *(u32x4*)(p + i * 4096 + t * 16) = u32x4{v, v, v, v};
    return;
  }
  {
    const int rep = b & 3, par = b >> 2;
    if (b == 2 && t < 64) *(u32x4*)(cnt + t * 4) = u32x4{0u, 0u, 0u, 0u};
    if (b == 1 && t == 0) *(unsigned*)(ws + OFF_TMO) = 0;
    for (int i = 0; i < 4; ++i) {
      const int u = t + 256 * i, pairidx = u >> 6, lane = u & 63, g = lane >> 4, hf = (lane >> 1) & 1, row = ((lane >> 1) & 6) + (lane & 1);
      const int k0 = 64 * pairidx + 16 * g + 8 * hf;
      u32x4 v = *(const u32x4*)(mem0 + row * DM + k0);
      v.x = nosent(v.x); v.y = nosent(v.y); v.z = nosent(v.z); v.w = nosent(v.w);
      *(u32x4*)(xch + ((size_t)3 << 21) + (size_t)(rep * 2 + (u >> 9)) * 16384 + par * 8192 + (u & 511) * 16) = v;
    }
  }
}
extern "C" void kernel_launch(void* const* d_in, const int* in_sizes, int n_in, void* d_out, int out_size, void* d_ws, size_t ws_size,
                              hipStream_t stream) {
  const u16* x    = (const u16*)d_in[0];
  const u16* mem0 = (const u16*)d_in[1];
  const u16* Wq   = (const u16*)d_in[2];
  const u16* Wk   = (const u16*)d_in[3];
  const u16* Wv   = (const u16*)d_in[4];
  const u16* Wo   = (const u16*)d_in[5];
  const u16* w1   = (const u16*)d_in[8];
  const u16* w3   = (const u16*)d_in[9];
  const u16* w2   = (const u16*)d_in[10];
  const u16* c    = (const u16*)d_in[11];
  const u16* wmod = (const u16*)d_in[12];
#define WSHIFT 0
  char* ws = (char*)d_ws + ((size_t)WSHIFT << 20);
  k_prep<<<NCALB + NMODB + NARM + (PPF > 0 ? 256 : 0), 256, 0, stream>>>(c, wmod, mem0, (float*)(ws + OFF_MOD), ws, (unsigned*)(ws + OFF_CNT), Wq, Wk, Wv);
  hipFuncSetAttribute((const void*)k_mega, hipFuncAttributeMaxDynamicSharedMemorySize, MEGA_LDS);
  k_mega<<<256, 512, MEGA_LDS, stream>>>(Wq, Wk, Wv, Wo, w1, w3, w2, x, c, wmod, ws, (u16*)d_out);
}
```

```cpp
#include <hip/hip_runtime.h>
#include <hip/hip_bf16.h>
#include <stdint.h>
#include <type_traits>
#pragma clang diagnostic ignored "-Winline-asm"
#pragma clang diagnostic ignored "-Wunused-value"

typedef unsigned short u16;
typedef short s16x4 __attribute__((ext_vector_type(4)));
typedef __bf16 bf16x8 __attribute__((ext_vector_type(8)));
typedef float f32x4 __attribute__((ext_vector_type(4)));
typedef float f32x2 __attribute__((ext_vector_type(2)));
typedef unsigned u32x4 __attribute__((ext_vector_type(4)));
typedef unsigned u32x2 __attribute__((ext_vector_type(2)));
#define LAS __attribute__((address_space(3)))

constexpr int SEQ = 2048, DM = 1024, NH = 32, DH = 128, HD = 4096, NMEM = 8, SEG = 32, NSEG = 64, FF = 3584;
constexpr unsigned SENT = 0xFFFFFFFFu;

__device__ __forceinline__ int lane_id() { int l; asm volatile("v_mbcnt_lo_u32_b32 %0, -1, 0\n\tv_mbcnt_hi_u32_b32 %0, -1, %0" : "=v"(l)); return l; }
#define WAVE_ID wave_id_
#define opaque_tid() (WAVE_ID * 64 + lane_id())
__device__ __forceinline__ float bf2f(u16 v) { return __builtin_bit_cast(float, (unsigned)v << 16); }
__device__ __forceinline__ float bflo(unsigned v) { return __builtin_bit_cast(float, v << 16); }
__device__ __forceinline__ float bfhi(unsigned v) { return __builtin_bit_cast(float, v & 0xffff0000u); }
__device__ __forceinline__ float bfr(float x) { return (float)(__bf16)x; }
__device__ __forceinline__ unsigned pack2(float a, float b) {
  typedef __bf16 bf16x2 __attribute__((ext_vector_type(2)));
  bf16x2 v; v[0] = (__bf16)a; v[1] = (__bf16)b; return __builtin_bit_cast(unsigned, v);
}
__device__ __forceinline__ unsigned nosent(unsigned v) { return v == SENT ? 0x7FC07FC0u : v; }
__device__ __forceinline__ bf16x8 asbf8(u32x4 v) { return __builtin_bit_cast(bf16x8, v); }
__device__ __forceinline__ s16x4 ass4(u32x2 v) { return __builtin_bit_cast(s16x4, v); }
__device__ __forceinline__ u32x2 trread(const char* lds_base, unsigned off) {
  s16x4 v = __builtin_amdgcn_ds_read_tr16_b64_v4i16((LAS s16x4*)(lds_base + off));
  return __builtin_bit_cast(u32x2, v);
}
__device__ __forceinline__ void glds16(const void* gsrc, unsigned lds_dst) {
  unsigned keep;
  asm volatile("s_mov_b32 %0, m0\n\ts_mov_b32 m0, %2\n\ts_nop 0\n\tglobal_load_lds_dwordx4 %1, off\n\ts_mov_b32 m0, %0"
               : "=&s"(keep) : "v"(gsrc), "s"(lds_dst) : "memory");
}
#define CNT_ " nt"
#define WNT_ " nt"
__device__ __forceinline__ void glds16_w(const void* gsrc, unsigned lds_dst) {
  unsigned keep;
  asm volatile("s_mov_b32 %0, m0\n\ts_mov_b32 m0, %2\n\ts_nop 0\n\tglobal_load_lds_dwordx4 %1, off" CNT_ "\n\ts_mov_b32 m0, %0"
               : "=&s"(keep) : "v"(gsrc), "s"(lds_dst) : "memory");
}
__device__ __forceinline__ void glds16_sc1(const void* gsrc, unsigned lds_dst) {
  unsigned keep;
  asm volatile("s_mov_b32 %0, m0\n\ts_mov_b32 m0, %2\n\ts_nop 0\n\tglobal_load_lds_dwordx4 %1, off sc1\n\ts_mov_b32 m0, %0"
               : "=&s"(keep) : "v"(gsrc), "s"(lds_dst) : "memory");
}
#define NTM 17
template <int B> __device__ __forceinline__ u32x4 ldnt(const void* p) { if (NTM & B) return __builtin_nontemporal_load((const u32x4*)p); else return *(const u32x4*)p; }
#define MFMA32(a, b, c) __builtin_amdgcn_mfma_f32_16x16x32_bf16((a), (b), (c), 0, 0, 0)

__device__ __forceinline__ void st16_sc1(void* p, u32x4 v) { asm volatile("global_store_dwordx4 %0, %1, off sc1\n\ts_nop 1" :: "v"(p), "v"(v) : "memory"); }
__device__ __forceinline__ void st8_sc1(void* p, u32x2 v) { asm volatile("global_store_dwordx2 %0, %1, off sc1\n\ts_nop 1" :: "v"(p), "v"(v) : "memory"); }
template <bool WT> __device__ __forceinline__ void st8(void* p, u32x2 v) { if (WT) st8_sc1(p, v); else *(u32x2*)p = v; }
template <bool WT> __device__ __forceinline__ void st16(void* p, u32x4 v) { if (WT) st16_sc1(p, v); else *(u32x4*)p = v; }
__device__ __forceinline__ u32x4 ld16_sc1(const void* p) {
  u32x4 r; asm volatile("global_load_dwordx4 %0, %1, off sc1\n\ts_waitcnt vmcnt(0)" : "=&v"(r) : "v"(p) : "memory"); return r;
}
__device__ __forceinline__ void ld16x4_sc1(const void* p, u32x4& a, u32x4& b, u32x4& c, u32x4& d) {
  asm volatile("global_load_dwordx4 %0, %4, off sc1\n\tglobal_load_dwordx4 %1, %4, off offset:1024 sc1\n\t"
               "global_load_dwordx4 %2, %4, off offset:2048 sc1\n\tglobal_load_dwordx4 %3, %4, off offset:3072 sc1\n\ts_waitcnt vmcnt(0)"
               : "=&v"(a), "=&v"(b), "=&v"(c), "=&v"(d) : "v"(p) : "memory");
}
typedef __amdgpu_buffer_rsrc_t rsrc_t;
__device__ __forceinline__ rsrc_t mkrs(const void* p) { return __builtin_amdgcn_make_buffer_rsrc((void*)p, 0, 0x7FFFFFFF, 0x00020000); }
__device__ __forceinline__ u32x4 bld16(rsrc_t r, unsigned off) { return __builtin_amdgcn_raw_buffer_load_b128(r, off, 0, 16); }
__device__ __forceinline__ u32x2 bld8(rsrc_t r, unsigned off) { return __builtin_amdgcn_raw_buffer_load_b64(r, off, 0, 16); }
__device__ __forceinline__ bool ok4(u32x4 v) { return v.x != SENT && v.y != SENT && v.z != SENT && v.w != SENT; }
constexpr int SPIN_CAP = 2000000;
__device__ __forceinline__ bool give_up(int spins, unsigned* tmo, unsigned code) {
  if ((spins & 4095) != 4095) return false;
  if (spins > SPIN_CAP) { __hip_atomic_store(tmo, code, __ATOMIC_RELAXED, __HIP_MEMORY_SCOPE_AGENT); return true; }
  return __hip_atomic_load(tmo, __ATOMIC_RELAXED, __HIP_MEMORY_SCOPE_AGENT) != 0u;
}
template <bool ACQ = true>
__device__ __forceinline__ void wait_cnt(unsigned* c, unsigned n, unsigned* tmo) {
  for (int spins = 0;; ++spins) {
    const unsigned v = __hip_atomic_load(c, __ATOMIC_RELAXED, __HIP_MEMORY_SCOPE_AGENT);
    if (v >= n) break;
    __builtin_amdgcn_s_sleep(8);
    if (give_up(spins, tmo, 5)) break;
  }
  if (ACQ) {
    __builtin_amdgcn_fence(__ATOMIC_ACQUIRE, "agent");
    asm volatile("s_waitcnt vmcnt(0)" ::: "memory");
  }
}

__device__ __forceinline__ const char* uni(const char* p) {
  const unsigned long long v = (unsigned long long)p;
  const unsigned lo = __builtin_amdgcn_readfirstlane((unsigned)v), hi = __builtin_amdgcn_readfirstlane((unsigned)(v >> 32));
  return (const char*)(((unsigned long long)hi << 32) | lo);
}
#define TSCLK "s_memrealtime"
#define TC(k) do { } while (0)
#define TS(k) do { } while (0)
#define TSDECL do { } while (0)
#define TSFLUSH(mask) do { } while (0)

constexpr size_t OFF_MOD  = 0;
constexpr size_t OFF_MOD2 = 65536;
constexpr size_t OFF_TMO  = 98304;
constexpr size_t OFF_CAL  = 98304 + 256;
constexpr int    NCALW    = 16;
constexpr size_t OFF_CNT  = 131072;
constexpr int    NCNT     = 128;
constexpr size_t OFF_H1   = 262144;
constexpr size_t OFF_Q    = OFF_H1 + (size_t)SEQ * DM * 2;
constexpr size_t OFF_K    = OFF_Q + (size_t)SEQ * HD * 2;
constexpr size_t OFF_V    = OFF_K + (size_t)SEQ * HD * 2;
constexpr size_t OFF_O    = OFF_V + (size_t)SEQ * HD * 2;
constexpr size_t OFF_X1   = OFF_O + (size_t)SEQ * HD * 2;
constexpr size_t OFF_H2   = OFF_X1 + (size_t)SEQ * DM * 2;
constexpr size_t OFF_G    = OFF_H2 + (size_t)SEQ * DM * 2;
constexpr size_t OFF_XCH  = (size_t)100 << 20;
constexpr size_t OFF_P    = (size_t)112 << 20;
constexpr size_t OFF_P2   = (size_t)128 << 20;
constexpr size_t OFF_DUMP = (size_t)144 << 20;
constexpr int PROW = 4 * DM * 2;
#define TLQ 1
#define TLP 1
static_assert(OFF_G + (size_t)SEQ * FF * 2 <= OFF_XCH, "layout");
__device__ __forceinline__ char* xch_base(char* ws) { return (char*)(((size_t)(ws + OFF_XCH) + ((2u << 20) - 1)) & ~(size_t)((2u << 20) - 1)); }
#define XPAR 0
#define XPARF(sum) ((size_t)(((sum) > 0 ? 0 : 1) ^ XPAR))
constexpr int TICK = 0, ARMED = 8;
constexpr int PROG = 16;
constexpr int PQKV = 64;
constexpr int PL1 = 96;
constexpr int PWO = 192;
constexpr int PLN = 224;
constexpr int PF2 = 320;
constexpr int PF1 = 448;
constexpr int L_RED = 131072;
constexpr int L_SLOT = L_RED + 16384;
constexpr int L_GB = L_SLOT + 1024;
constexpr int MEGA_LDS = 163840;
static_assert(L_GB + 12288 + 2048 <= MEGA_LDS, "lds");
constexpr int NMODB = 256;
constexpr int NCALB = 3 * NCALW;
#define XPARC 0

__device__ __forceinline__ int bswz(int row) { return (((row >> 1) & 1) | (((row >> 3) & 1) << 1)) << 1; }
template <int OFF, bool SC1 = false>
__device__ __forceinline__ void glds16s(const char* sbase, unsigned voff, unsigned lds_base) {
  unsigned keep;
  if (SC1)
    asm volatile("s_mov_b32 %0, m0\n\ts_add_u32 m0, %3, %4\n\ts_nop 0\n\tglobal_load_lds_dwordx4 %1, %2 sc1\n\ts_mov_b32 m0, %0"
                 : "=&s"(keep) : "v"(voff), "s"(sbase), "s"(lds_base), "i"(OFF) : "memory", "scc");
  else
    asm volatile("s_mov_b32 %0, m0\n\ts_add_u32 m0, %3, %4\n\ts_nop 0\n\tglobal_load_lds_dwordx4 %1, %2\n\ts_mov_b32 m0, %0"
                 : "=&s"(keep) : "v"(voff), "s"(sbase), "s"(lds_base), "i"(OFF) : "memory", "scc");
}
template <int OFF>
__device__ __forceinline__ void glds16x2s(const char* sbase, unsigned voff, unsigned lds_base) {
  unsigned keep;
  asm volatile("s_mov_b32 %0, m0\n\ts_add_u32 m0, %3, %4\n\ts_nop 0\n\tglobal_load_lds_dwordx4 %1, %2 sc1\n\tglobal_load_lds_dwordx4 %1, %2 offset:1024 sc1\n\ts_mov_b32 m0, %0"
               : "=&s"(keep) : "v"(voff), "s"(sbase), "s"(lds_base), "i"(OFF) : "memory", "scc");
}
__device__ __forceinline__ void st16s(const char* sb, unsigned vo, u32x4 v) { asm volatile("global_store_dwordx4 %0, %1, %2 sc1\n\ts_nop 1" :: "v"(vo), "v"(v), "s"(sb) : "memory"); }
__device__ __forceinline__ void head_to_lds(const char* src, int lane, unsigned lds) {
  if (TLQ) {
    const int c = (lane & 15) ^ (lane >> 4);
    const unsigned v0 = (unsigned)((c >> 2) * 2048 + (lane >> 4) * 64 + (c & 3) * 16);
#pragma unroll
    for (int i = 0; i < 8; ++i) glds16s<0, true>(src + i * 256, v0 ^ ((i & 3) * 2048), lds + i * 1024);
  } else {
#pragma unroll
    for (int i = 0; i < 4; ++i) glds16x2s<0>(src + i * 2048, (unsigned)lane * 16, lds + i * 2048);
  }
}
#define WAIT_V(n) asm volatile("s_waitcnt vmcnt(" #n ")" ::: "memory")
#define BAR() do { asm volatile("s_waitcnt lgkmcnt(0)" ::: "memory"); __builtin_amdgcn_s_barrier(); asm volatile("" ::: "memory"); } while (0)
__device__ __forceinline__ void st4_sc1(void* p, unsigned v) { asm volatile("global_store_dword %0, %1, off sc1" :: "v"(p), "v"(v) : "memory"); }
__device__ __forceinline__ unsigned ld4_sc1(const void* p) {
  unsigned r; asm volatile("global_load_dword %0, %1, off sc1\n\ts_waitcnt vmcnt(0)" : "=&v"(r) : "v"(p) : "memory"); return r;
}
template <int CTRL> __device__ __forceinline__ unsigned dppu(unsigned x) { return (unsigned)__builtin_amdgcn_update_dpp(0, (int)x, CTRL, 0xf, 0xf, false); }
__device__ __forceinline__ unsigned rowmin16(unsigned v) {
  v = min(v, dppu<0x128>(v)); v = min(v, dppu<0x124>(v)); v = min(v, dppu<0x122>(v)); v = min(v, dppu<0x121>(v));
  return v;
}
__device__ __forceinline__ unsigned rdl(unsigned v, int l) { return (unsigned)__builtin_amdgcn_readlane((int)v, l); }
constexpr float SCALE = 0.08838834764831845f;
constexpr float LOG2E = 1.4426950408889634f;
template <int N> __device__ __forceinline__ void delay64() {
  if (N >= 16) { __builtin_amdgcn_s_sleep(15); delay64<(N >= 16 ? N - 15 : 0)>(); } else if (N > 0) __builtin_amdgcn_s_sleep(N);
}
__device__ __forceinline__ void sw16(float x, float& a, float& b) {
  a = x; b = x; asm volatile("s_nop 1\n\tv_permlane16_swap_b32 %0, %1" : "+v"(a), "+v"(b));
}
__device__ __forceinline__ void sw32(float x, float& a, float& b) {
  a = x; b = x; asm volatile("s_nop 1\n\tv_permlane32_swap_b32 %0, %1" : "+v"(a), "+v"(b));
}
__device__ __forceinline__ float red16_32_max(float x) {
  float a, b; sw16(x, a, b); x = fmaxf(a, b); sw32(x, a, b); return fmaxf(a, b);
}
__device__ __forceinline__ float red16_32_add(float x) {
  float a, b; sw16(x, a, b); x = a + b; sw32(x, a, b); return a + b;
}
__device__ __forceinline__ unsigned cvtpk(float a, float b) { unsigned r; asm("v_cvt_pk_bf16_f32 %0, %1, %2" : "=v"(r) : "v"(a), "v"(b)); return r; }
__device__ __forceinline__ float max3(float a, float b, float c) { float r; asm("v_max3_f32 %0, %1, %2, %3" : "=v"(r) : "v"(a), "v"(b), "v"(c)); return r; }
__device__ __forceinline__ float max2(float a, float b) { float r; asm("v_max_f32 %0, %1, %2" : "=v"(r) : "v"(a), "v"(b)); return r; }
__device__ __forceinline__ unsigned ror8u(unsigned x) { return (unsigned)__builtin_amdgcn_update_dpp(0, (int)x, 0x128, 0xf, 0xf, false); }
__device__ __forceinline__ float ror8(float x) { return __builtin_bit_cast(float, ror8u(__builtin_bit_cast(unsigned, x))); }
__device__ __forceinline__ float add_ror8(float x) {
  return x + __builtin_bit_cast(float, __builtin_amdgcn_update_dpp(0, __builtin_bit_cast(int, x), 0x128, 0xf, 0xf, false));
}

template <int M0> __device__ __forceinline__ unsigned qkv_avail(const unsigned* line) {
  const unsigned v = ld4_sc1(line + (lane_id() & 15));
  unsigned r = NSEG;
#pragma unroll
  for (int m = M0; m < 3; ++m) {
    unsigned a = rdl(v, m);
#pragma unroll
    for (int p = 1; p < 3; ++p) { const unsigned w = rdl(v, 4 * p + m); if (a >= (w >> 8)) a = max(a, w & 255u); }
    r = min(r, a);
  }
  return r;
}
__device__ __forceinline__ void wait_l1(const unsigned* cnt, unsigned n, unsigned* tmo) {
  const int lane = lane_id();
  for (int spins = 0;; ++spins) {
    const unsigned v = ld4_sc1(cnt + (PL1 + (lane & 31)) * 32);
    if (__all(v >= n)) break;
    __builtin_amdgcn_s_sleep(8);
    if (give_up(spins, tmo, 12)) break;
  }
}
#define GKV 1
__device__ __forceinline__ void wait_k0(const unsigned* cnt, int h, unsigned* tmo) {
  for (int spins = 0;; ++spins) {
    const int l = lane_id();
    const unsigned v = ld4_sc1(cnt + (PQKV + h) * 32 + (l == 0 ? 3 : 1 + (l & 1)));
    if (__all(v >= (l == 0 ? 1u : (unsigned)GKV))) break;
    __builtin_amdgcn_s_sleep(32);
    if (give_up(spins, tmo, 13)) break;
  }
}
#define KTON 0
#define WCLK(slot, b) do { } while (0)
#define WTM(slot, b, k) do { } while (0)
#define WTS(slot, b, k) do { } while (0)
#define KTS(k_) do { } while (0)
#define STS(ws_, slot_, k_) do { } while (0)
typedef __bf16 bf16x2 __attribute__((ext_vector_type(2)));
__device__ __forceinline__ f32x2 pkfma(f32x2 a, f32x2 b, f32x2 c) { return __builtin_elementwise_fma(a, b, c); }
__device__ __forceinline__ float wavesum(float v) {
  v += __builtin_bit_cast(float, dppu<0x128>(__builtin_bit_cast(unsigned, v))); v += __builtin_bit_cast(float, dppu<0x124>(__builtin_bit_cast(unsigned, v)));
  v += __builtin_bit_cast(float, dppu<0x122>(__builtin_bit_cast(unsigned, v))); v += __builtin_bit_cast(float, dppu<0x121>(__builtin_bit_cast(unsigned, v)));
  return red16_32_add(v);
}
__device__ __forceinline__ void bst16(rsrc_t r, unsigned off, unsigned soff, u32x4 v) { __builtin_amdgcn_raw_buffer_store_b128(v, r, off + soff, 0, 16); }
__device__ __forceinline__ float adds(unsigned v, unsigned sel, float c) { return __builtin_amdgcn_fdot2_f32_bf16(__builtin_bit_cast(bf16x2, v), __builtin_bit_cast(bf16x2, sel), c, false); }
__device__ __forceinline__ void sum4(const u32x4 (&p)[4], f32x2 (&a)[4]) {
  unsigned slo = 0x00003F80u, shi = 0x3F800000u;
  asm("" : "+s"(slo), "+s"(shi));
#pragma unroll
  for (int e = 0; e < 4; ++e) {
    float lo = bflo(p[0][e]), hi = bfhi(p[0][e]);
#pragma unroll
    for (int q = 1; q < 4; ++q) { lo = adds(p[q][e], slo, lo); hi = adds(p[q][e], shi, hi); }
    a[e] = f32x2{lo, hi};
  }
}
struct RowSet { u32x4 p[2][4]; u32x4 rv[2]; };
#define XAUX 16
__device__ __forceinline__ void row_issue2(RowSet& S, rsrc_t rp, rsrc_t rr, int row, unsigned l16) {
#pragma unroll
  for (int i = 0; i < 2; ++i) {
#pragma unroll
    for (int q = 0; q < 4; ++q) S.p[i][q] = TLP ? __builtin_amdgcn_raw_buffer_load_b128(rp, (l16 >> 6) * 2048 + (l16 & 48) + i * 32768, (unsigned)((row >> 5) * 32 * PROW + q * 65536 + (row & 31) * 64), 16)
                                                 : __builtin_amdgcn_raw_buffer_load_b128(rp, l16 + i * 1024, (unsigned)(row * PROW + q * 2048), 16);
    S.rv[i] = __builtin_amdgcn_raw_buffer_load_b128(rr, l16 + i * 1024, (unsigned)(row * DM) * 2, XAUX);
  }
}
__device__ __forceinline__ void row_a(const RowSet& S, f32x2 (&a)[2][4], u32x4 (&xv)[2]) {
#pragma unroll
  for (int i = 0; i < 2; ++i) { sum4(S.p[i], a[i]); xv[i] = S.rv[i]; }
#pragma unroll
  for (int i = 0; i < 2; ++i) asm volatile("" : "+v"(a[i][0]), "+v"(a[i][1]), "+v"(a[i][2]), "+v"(a[i][3]), "+v"(xv[i]));
}
__device__ __forceinline__ u32x4 resid8(const f32x2 (&a)[4], u32x4 xv, const char* mt, f32x2 (&v)[4]) {
  const f32x4 g0 = *(const f32x4*)mt, g1 = *(const f32x4*)(mt + 1024);
  u32x4 o;
  o[0] = [&] { const f32x2 s = pkfma(f32x2{g0[0], g0[1]}, a[0], f32x2{bflo(xv[0]), bfhi(xv[0])}); return pack2(s[0], s[1]); }();
  o[1] = [&] { const f32x2 s = pkfma(f32x2{g0[2], g0[3]}, a[1], f32x2{bflo(xv[1]), bfhi(xv[1])}); return pack2(s[0], s[1]); }();
  o[2] = [&] { const f32x2 s = pkfma(f32x2{g1[0], g1[1]}, a[2], f32x2{bflo(xv[2]), bfhi(xv[2])}); return pack2(s[0], s[1]); }();
  o[3] = [&] { const f32x2 s = pkfma(f32x2{g1[2], g1[3]}, a[3], f32x2{bflo(xv[3]), bfhi(xv[3])}); return pack2(s[0], s[1]); }();
#pragma unroll
  for (int e = 0; e < 4; ++e) v[e] = f32x2{bflo(o[e]), bfhi(o[e])};
  return o;
}
__device__ __forceinline__ u32x4 norm8(const f32x2 (&v)[4], const char* mt, float mb, float rden) {
  const f32x4 h0 = *(const f32x4*)(mt + 4096), h1 = *(const f32x4*)(mt + 4096 + 1024), c0 = *(const f32x4*)(mt + 8192), c1 = *(const f32x4*)(mt + 8192 + 1024);
  const f32x2 m2 = f32x2{mb, mb}, d2 = f32x2{rden, rden};
  const f32x2 r0 = pkfma(v[0] - m2, f32x2{c0[0], c0[1]} * d2, f32x2{h0[0], h0[1]}), r1 = pkfma(v[1] - m2, f32x2{c0[2], c0[3]} * d2, f32x2{h0[2], h0[3]});
  const f32x2 r2 = pkfma(v[2] - m2, f32x2{c1[0], c1[1]} * d2, f32x2{h1[0], h1[1]}), r3 = pkfma(v[3] - m2, f32x2{c1[2], c1[3]} * d2, f32x2{h1[2], h1[3]});
  return u32x4{pack2(r0[0], r0[1]), pack2(r1[0], r1[1]), pack2(r2[0], r2[1]), pack2(r3[0], r3[1])};
}
__device__ __forceinline__ void row_b(const f32x2 (&a)[2][4], const u32x4 (&xv)[2], const char* ml, rsrc_t rx1, rsrc_t rh2, int row, int lane) {
  f32x2 v[2][4];
  unsigned mo = lane * 16; asm volatile("" : "+v"(mo));
  ml += mo;
#pragma unroll
  for (int i = 0; i < 2; ++i) bst16(rx1, mo + i * 1024, (unsigned)(row * DM) * 2, resid8(a[i], xv[i], ml + i * 2048, v[i]));
  const f32x2 s2 = ((v[0][0] + v[0][1]) + (v[0][2] + v[0][3])) + ((v[1][0] + v[1][1]) + (v[1][2] + v[1][3]));
  const float mean = wavesum(s2[0] + s2[1]) * (1.0f / DM);
  f32x2 q2 = f32x2{0, 0};
#pragma unroll
  for (int i = 0; i < 8; ++i) { const f32x2 d = v[i >> 2][i & 3] - f32x2{mean, mean}; q2 = pkfma(d, d, q2); }
  const float q = wavesum(q2[0] + q2[1]);
  const float mb = bfr(mean), vb = bfr(q * (1.0f / DM));
  const float rden = 1.0f / bfr(sqrtf(bfr(vb + 1e-5f)));
  const int rb = row & 31;
  const unsigned hd = (unsigned)((((lane & 7) ^ (rb & 7)) << 4) + (lane >> 3) * 4096);
#pragma unroll
  for (int i = 0; i < 2; ++i) bst16(rh2, hd, (unsigned)((row >> 5) * 65536 + rb * 128 + i * 32768), norm8(v[i], ml + i * 2048, mb, rden));
}
__device__ __forceinline__ void half_load(const char* P, const u16* res, int row, int hf, int lane, f32x2 (&a)[4], u32x4& rv) {
  const rsrc_t rp = mkrs(P), rr = mkrs(res);
  const unsigned po = TLP ? (unsigned)((row >> 5) * 32 * PROW + (hf * 16 + (lane >> 2)) * 2048 + (row & 31) * 64 + (lane & 3) * 16) : (unsigned)(row * PROW + hf * 1024 + lane * 16), ro = (unsigned)(row * DM * 2 + hf * 1024 + lane * 16);
  u32x4 p[4];
#pragma unroll
  for (int q = 0; q < 4; ++q) p[q] = bld16(rp, po + q * (TLP ? 65536 : 2048));
  if (res) rv = bld16(rr, ro);
  sum4(p, a);
}
__device__ __forceinline__ void fin_half(const char* P, const u16* res, const char* gbt, u16* __restrict__ o1, int row, int hf, int lane) {
  f32x2 a[4]; u32x4 rv;
  half_load(P, res, row, hf, lane, a, rv);
  const u32x4 gv = *(const u32x4*)(gbt + hf * 1024 + lane * 16);
  u32x4 o;
#pragma unroll
  for (int e = 0; e < 4; ++e) { const f32x2 s = pkfma(f32x2{bflo(gv[e]), bfhi(gv[e])}, a[e], f32x2{bflo(rv[e]), bfhi(rv[e])}); o[e] = pack2(s[0], s[1]); }
  st16_sc1(o1 + (size_t)row * DM + hf * 512 + lane * 8, o);
}
__device__ __forceinline__ void ln_half(const u32x4 (&p)[4], u32x4 rv, const f32x4 (&tb)[6], volatile LAS float* xs, const char* x1, unsigned xo, const char* h2, unsigned ho,
                                        int wave, int lane) {
  f32x2 a[4], v[4];
  sum4(p, a);
  u32x4 o;
#pragma unroll
  for (int e = 0; e < 4; ++e) {
    const f32x2 s = pkfma(f32x2{tb[e >> 1][2 * (e & 1)], tb[e >> 1][2 * (e & 1) + 1]}, a[e], f32x2{bflo(rv[e]), bfhi(rv[e])});
    o[e] = pack2(s[0], s[1]);
    v[e] = f32x2{bflo(o[e]), bfhi(o[e])};
  }
  const float c = __builtin_bit_cast(float, __builtin_amdgcn_readfirstlane(__builtin_bit_cast(int, v[0][0])));
  f32x2 s2 = f32x2{0, 0}, q2 = s2;
#pragma unroll
  for (int i = 0; i < 4; ++i) { const f32x2 d = v[i] - f32x2{c, c}; s2 += d; q2 = pkfma(d, d, q2); }
  float s = s2[0] + s2[1], q = q2[0] + q2[1];
#define DA(x, k) x += __builtin_bit_cast(float, dppu<k>(__builtin_bit_cast(unsigned, x)))
  DA(s, 0x128); DA(q, 0x128); DA(s, 0x124); DA(q, 0x124); DA(s, 0x122); DA(q, 0x122); DA(s, 0x121); DA(q, 0x121);
#undef DA
  {
    asm volatile("s_nop 1\n\tv_permlane16_swap_b32 %0, %1" : "+v"(s), "+v"(q));
    s += q; q = s;
    asm volatile("s_nop 1\n\tv_permlane32_swap_b32 %0, %1" : "+v"(s), "+v"(q));
    s += q;
  }
  if (lane < 48 && (lane & 15) == 0) xs[wave * 4 + (lane >> 4)] = lane == 32 ? c : s;
  st16s(x1, xo, o);
  BAR();
  const f32x4 u0 = *(const volatile LAS f32x4*)(xs + (wave & 6) * 4), u1 = *(const volatile LAS f32x4*)(xs + (wave | 1) * 4);
  const float m0 = u0[0] * (2.0f / DM), m1 = u1[0] * (2.0f / DM);
  const float dm = (u0[2] - u1[2]) + (m0 - m1);
  const float mean = ((u0[2] + m0) + (u1[2] + m1)) * 0.5f;
  const float qq = ((u0[1] - u0[0] * m0) + (u1[1] - u1[0] * m1)) + dm * dm * (DM / 4);
  const float mb = bfr(mean), vb = bfr(fmaxf(qq, 0.0f) * (1.0f / DM));
  const float rden = __builtin_amdgcn_rcpf(bfr(__builtin_amdgcn_sqrtf(bfr(vb + 1e-5f))));
  const f32x2 m2 = f32x2{mb, mb}, d2 = f32x2{rden, rden};
  u32x4 r;
#pragma unroll
  for (int e = 0; e < 4; ++e) {
    const f32x2 t = pkfma(v[e] - m2, f32x2{tb[4 + (e >> 1)][2 * (e & 1)], tb[4 + (e >> 1)][2 * (e & 1) + 1]} * d2, f32x2{tb[2 + (e >> 1)][2 * (e & 1)], tb[2 + (e >> 1)][2 * (e & 1) + 1]});
    r[e] = pack2(t[0], t[1]);
  }
  st16s(h2, ho, r);
}
__device__ __forceinline__ void row_table(char* tab, const float* mod, const unsigned* cnt, unsigned* tmo, int tid) {
  wait_l1(cnt, 9u, tmo);
  const rsrc_t rm = mkrs((const char*)mod + OFF_MOD2);
#pragma unroll
  for (int i = 0; i < 2; ++i) {
    const int u = tid + 512 * i, a = u >> 8, c = u & 255;
    f32x4 v = __builtin_bit_cast(f32x4, bld16(rm, (unsigned)u * 16));
    v = f32x4{bfr(v[0]), bfr(v[1]), bfr(v[2]), bfr(v[3])};
    if (a == 2) v = f32x4{bfr(1.0f + v[0]), bfr(1.0f + v[1]), bfr(1.0f + v[2]), bfr(1.0f + v[3])};
    if (a < 3) *(f32x4*)(tab + a * 4096 + ((c >> 7) * 2 + (c & 1)) * 1024 + ((c >> 1) & 63) * 16) = v;
    else *(u32x2*)(tab + 12288 + c * 8) = u32x2{pack2(v[0], v[1]), pack2(v[2], v[3])};
  }
  __syncthreads();
}
#define LS(i) do { } while (0)
#define LNSL 2
#define LNB 56
__device__ __forceinline__ void row_role(char* smem, int wave_id_, int rbase, int par, const float* mod, const char* P, const u16* x, u16* x1, char* h2c,
                                         unsigned* cnt, unsigned* tmo, int slot, unsigned long long* mts) {
  const int tid = opaque_tid(), lane = tid & 63, wave = WAVE_ID;
  row_table(smem, mod, cnt, tmo, tid);
  const int rb = rbase + wave + 32 * par;
  const rsrc_t rx1 = mkrs(x1), rh2 = mkrs(h2c), rc = mkrs(cnt), rp = mkrs(P), rr = mkrs(x);
  const unsigned l16 = lane * 16;
  const unsigned pwo = (unsigned)(PWO + (lane & 31)) * 128;
  unsigned* myp = cnt + (PLN + (rb >> 3)) * 32 + wave;
  auto eval = [&](unsigned v) { v = rowmin16(v); return (int)((min(min(rdl(v, 0), rdl(v, 16)), (unsigned)LNB) + 1u - par) >> 1); };
  auto wmk = [&](int n) { return (unsigned)min(2 * n + par, LNB); };
  if (par && lane == 0) st4_sc1(myp, 1u);
  const int hf = wave & 1, r4 = rbase + 4 * par + (wave >> 1);
  {
  u32x4 xa[NSEG - LNB + 1];
#pragma unroll
  for (int i = 0; i < NSEG - LNB; ++i) xa[i] = __builtin_amdgcn_raw_buffer_load_b128(rr, (unsigned)(hf * 1024 + lane * 16), (unsigned)(((LNB + i) * 32 + r4) * DM) * 2, 0);
#pragma unroll
  for (int i = 0; i < NSEG - LNB; ++i) *(u32x4*)(smem + 20480 + (i * 8 + wave) * 1024 + lane * 16) = xa[i];
  if (tid == 0) *(volatile LAS unsigned*)(smem + 16384 + 256) = 0u;
  }
  int b = 0, wa = 0;
  f32x2 a[2][4]; u32x4 xv[2];
  while (b < LNB / 2) {
    if (b >= wa) {
      for (int spins = 0;; ++spins) {
        wa = eval(ld4_sc1((const char*)cnt + pwo));
        if (b < wa) break;
        __builtin_amdgcn_s_sleep(2);
        if (give_up(spins, tmo, 11)) { wa = LNB / 2; break; }
      }
    }
    if ((b & 3) == 0 || b >= 28) { WTM(slot, 2 * b + par, 0); }
    if (wa >= b + 8) {
      RowSet S[4];
      const int b0 = b;
      asm volatile("; deep" ::: "memory");
#pragma unroll
      for (int i = 0; i < 4; ++i) row_issue2(S[i], rp, rr, (b + i) * 64 + rb, l16);
      while (b + 7 < wa) {
        const unsigned smp = __builtin_amdgcn_raw_buffer_load_b32(rc, pwo, 0, 16);
#pragma unroll
        for (int i = 0; i < 4; ++i) {
          row_a(S[i], a, xv);
          WAIT_V(42);
          if (lane == 0 && b > b0) st4_sc1(myp, wmk((b + i - 3)));
          __builtin_amdgcn_sched_barrier(0);
          row_issue2(S[i], rp, rr, (b + 4 + i) * 64 + rb, l16);
          __builtin_amdgcn_sched_barrier(0);
          row_b(a, xv, smem, rx1, rh2, (b + i) * 64 + rb, lane);
          __builtin_amdgcn_sched_barrier(0);
        }
        const int w = eval(smp);
        wa = w > wa ? w : wa;
        b += 4;
      }
#pragma unroll
      for (int i = 0; i < 4; ++i) {
        row_a(S[i], a, xv);
        __builtin_amdgcn_sched_barrier(0);
        row_b(a, xv, smem, rx1, rh2, (b + i) * 64 + rb, lane);
        WAIT_V(4);
        if (lane == 0) st4_sc1(myp, wmk((b + i)));
        __builtin_amdgcn_sched_barrier(0);
      }
      WAIT_V(0);
      b += 4;
      if (lane == 0) st4_sc1(myp, wmk(b));
      continue;
    }
    if (wa < b + 2) {
      RowSet T;
      asm volatile("; single" ::: "memory");
      row_issue2(T, rp, rr, b * 64 + rb, l16);
      row_a(T, a, xv);
      row_b(a, xv, smem, rx1, rh2, b * 64 + rb, lane);
      WAIT_V(0);
      ++b;
      if (lane == 0) st4_sc1(myp, wmk(b));
      WTM(slot, 2 * (b - 1) + par, 2);
      continue;
    }
    RowSet S0, S1;
    const int b0 = b;
    asm volatile("; multi" ::: "memory");
    row_issue2(S0, rp, rr, b * 64 + rb, l16);
    row_issue2(S1, rp, rr, (b + 1) * 64 + rb, l16);
    while (b + 3 < wa) {
      const unsigned smp = __builtin_amdgcn_raw_buffer_load_b32(rc, pwo, 0, 16);
      row_a(S0, a, xv);
      WAIT_V(15);
      if (lane == 0 && b > b0) st4_sc1(myp, wmk((b - 1)));
      __builtin_amdgcn_sched_barrier(0);
      row_issue2(S0, rp, rr, (b + 2) * 64 + rb, l16);
      __builtin_amdgcn_sched_barrier(0);
      row_b(a, xv, smem, rx1, rh2, b * 64 + rb, lane);
      __builtin_amdgcn_sched_barrier(0);
      row_a(S1, a, xv);
      WAIT_V(15);
      if (lane == 0 && b > b0) st4_sc1(myp, wmk(b));
      __builtin_amdgcn_sched_barrier(0);
      row_issue2(S1, rp, rr, (b + 3) * 64 + rb, l16);
      __builtin_amdgcn_sched_barrier(0);
      row_b(a, xv, smem, rx1, rh2, (b + 1) * 64 + rb, lane);
      __builtin_amdgcn_sched_barrier(0);
      const int w = eval(smp);
      wa = w > wa ? w : wa;
      b += 2;
    }
    row_a(S0, a, xv);
    row_b(a, xv, smem, rx1, rh2, b * 64 + rb, lane);
    row_a(S1, a, xv);
    WAIT_V(4 - 4);
    if (lane == 0) st4_sc1(myp, wmk((b + 1)));
    WTM(slot, 2 * b + par, 2);
    row_b(a, xv, smem, rx1, rh2, (b + 1) * 64 + rb, lane);
    WAIT_V(0);
    b += 2;
    if (lane == 0) st4_sc1(myp, wmk(b));
    WTM(slot, 2 * (b - 1) + par, 2);
  }
  static_assert(20480 + (NSEG - LNB) * 8192 <= MEGA_LDS && LNB % 2 == 0, "");
  const unsigned pw2 = (unsigned)(PWO + ((lane >> 2) & 3) * 8 + hf * 4 + (lane & 3)) * 128;
  static_assert(TLP, "");
  unsigned pv = 0;
  u32x4 p[4];
  bool req = false;
  const char* pb = uni(P); const char* cb = uni((const char*)cnt);
  auto publish = [&](int n, int ln) {
    unsigned o = 0;
    if (ln == 0) o = __hip_atomic_fetch_add((LAS unsigned*)(smem + 16384 + 256), 1u, __ATOMIC_RELAXED, __HIP_MEMORY_SCOPE_WORKGROUP);
    if ((__builtin_amdgcn_readfirstlane(o) & 7u) == 7u && ln < 8) st4_sc1(myp - wave + ln, (unsigned)n);
  };
  BAR();
  f32x4 tb[6];
#pragma unroll
  for (int i = 0; i < 6; ++i) tb[i] = *(const f32x4*)(smem + (i >> 1) * 4096 + hf * 2048 + (i & 1) * 1024 + lane * 16);
  const unsigned po = (unsigned)((hf * 16 + (lane >> 2)) * 2048 + r4 * 64 + (lane & 3) * 16), xo = (unsigned)(r4 * DM * 2 + hf * 1024 + lane * 16);
  const unsigned ho = (unsigned)(r4 * 128 + (((lane & 7) ^ (r4 & 7)) << 4) + (lane >> 3) * 4096 + hf * 32768);
  for (int bb = LNB; bb < NSEG; ++bb) {
    const int ln = lane;
    const u32x4 rv = *(const u32x4*)(smem + 20480 + ((bb - LNB) * 8 + wave) * 1024 + ln * 16);
    if (!req) {
      for (int spins = 0; !__all(pv > (unsigned)bb); ++spins) {
        pv = ld4_sc1((const char*)cnt + pw2);
        if (__all(pv > (unsigned)bb)) break;
        __builtin_amdgcn_s_sleep(LNSL);
        if (give_up(spins, tmo, 11)) break;
      }
    }
    WTM(slot, bb, 0);
    LS(0);
    {
      asm volatile("global_load_dwordx4 %0, %5, %9 sc1\n\tglobal_load_dwordx4 %1, %6, %9 sc1\n\tglobal_load_dwordx4 %2, %7, %9 sc1\n\tglobal_load_dwordx4 %3, %8, %9 sc1\n\t"
                   "global_load_dword %4, %10, %11 sc1\n\ts_waitcnt vmcnt(0)"
                   : "=&v"(p[0]), "=&v"(p[1]), "=&v"(p[2]), "=&v"(p[3]), "=&v"(pv)
                   : "v"(po), "v"(po + 65536), "v"(po + 131072), "v"(po + 196608), "s"(pb + (size_t)bb * 32 * PROW), "v"(pw2), "s"(cb) : "memory");
    }
    LS(1);
    if (req) publish(bb, ln);
    ln_half(p, rv, tb, (volatile LAS float*)(smem + 16384 + (bb & 1) * 128), uni((const char*)x1) + (size_t)bb * 65536, xo, uni(h2c) + (size_t)bb * 65536, ho, wave, ln);
    LS(2);
    req = bb + 1 < NSEG && __all(pv > (unsigned)(bb + 1));
    if (!req) {
      if (bb + 1 < NSEG) pv = ld4_sc1((const char*)cnt + pw2);
      WAIT_V(0);
      LS(3);
      publish(bb + 1, ln);
    }
    WTM(slot, bb, 2);
  }
}
__device__ __forceinline__ void l1_role(char* smem, int wave_id_, int c, const u16* __restrict__ x, const float* __restrict__ mod, const u16* __restrict__ cvec,
                                        const u16* __restrict__ wmod, char* __restrict__ h1c, unsigned* cnt, unsigned* tmo, unsigned long long* mts) {
  const int tid = opaque_tid(), lane = tid & 63, wave = WAVE_ID;
  u32x4 xr[8][2];
  const u16* src = x + (size_t)(8 * c + wave) * DM + lane * 8;
  xr[0][0] = ldnt<2>(src); xr[0][1] = ldnt<2>(src + 512);
  {
    f32x4 m[8];
#pragma unroll
    for (int k = 0; k < 8; ++k) m[k] = *(const f32x4*)(mod + k * (2 * DM) + tid * 4);
    const float sva = bf2f(cvec[tid]), svb = bf2f(cvec[tid + 512]);
    f32x4 v = ((m[0] + m[1]) + (m[2] + m[3])) + ((m[4] + m[5]) + (m[6] + m[7]));
    v = f32x4{bfr(v[0]), bfr(v[1]), bfr(v[2]), bfr(v[3])};
    const int a = tid >> 8, cc = tid & 255;
    if (a) v = f32x4{bfr(1.0f + v[0]), bfr(1.0f + v[1]), bfr(1.0f + v[2]), bfr(1.0f + v[3])};
    *(f32x4*)(smem + 4096 + a * 4096 + ((cc >> 7) * 2 + (cc & 1)) * 1024 + ((cc >> 1) & 63) * 16) = v;
    float* sv = (float*)(smem + 12288);
    sv[tid] = bfr(sva / (1.0f + __expf(-sva))); sv[tid + 512] = bfr(svb / (1.0f + __expf(-svb)));
  }
  __syncthreads();
#pragma unroll
  for (int p = 0; p < 8; ++p) {
    f32x2 v[2][4];
#pragma unroll
    for (int i = 0; i < 2; ++i)
#pragma unroll
      for (int e = 0; e < 4; ++e) v[i][e] = f32x2{bflo(xr[p][i][e]), bfhi(xr[p][i][e])};
    const f32x2 s2 = ((v[0][0] + v[0][1]) + (v[0][2] + v[0][3])) + ((v[1][0] + v[1][1]) + (v[1][2] + v[1][3]));
    const float mean = wavesum(s2[0] + s2[1]) * (1.0f / DM);
    f32x2 q2 = f32x2{0, 0};
#pragma unroll
    for (int i = 0; i < 8; ++i) { const f32x2 d = v[i >> 2][i & 3] - f32x2{mean, mean}; q2 = pkfma(d, d, q2); }
    const float q = wavesum(q2[0] + q2[1]);
    if (p == 0 && wave == 0) STS((char*)cnt - OFF_CNT, 128 + (c >> 3) * 32 + 24 + (c & 7), 0);
    const float mb = bfr(mean), vb = bfr(q * (1.0f / DM));
    const float rden = 1.0f / bfr(sqrtf(bfr(vb + 1e-5f)));
    const int row = 256 * p + 8 * c + wave, rb = row & 31;
    char* dst = h1c + (size_t)(row >> 5) * 65536 + (lane >> 3) * 4096 + rb * 128 + (((lane & 7) ^ (rb & 7)) << 4);
    const char* ml = smem + lane * 16;
#pragma unroll
    for (int i = 0; i < 2; ++i) st16_sc1(dst + i * 32768, norm8(v[i], ml + i * 2048, mb, rden));
    WAIT_V(0);
    __builtin_amdgcn_s_barrier();
    if (tid == 0) st4_sc1(cnt + (PL1 + c) * 32, (unsigned)(p + 1));
    if (p == 0) {
#pragma unroll
      for (int pp = 1; pp < 8; ++pp) { xr[pp][0] = ldnt<2>(src + (size_t)pp * 256 * DM); xr[pp][1] = ldnt<2>(src + (size_t)pp * 256 * DM + 512); }
    }
    if (wave == 0) WTS(c, p, 0);
    if (p == 0 && wave == 0) STS((char*)cnt - OFF_CNT, 128 + (c >> 3) * 32 + 24 + (c & 7), 1);
  }
  wait_k0(cnt, c, tmo);
  {
    const int cg = tid & 15, r0 = tid >> 4;
    const u16* wp = wmod + (size_t)r0 * (6 * DM) + 2 * DM + 128 * c + cg * 8;
    const float* sv = (const float*)(smem + 12288) + r0;
    f32x2 acc[4] = {f32x2{0, 0}, f32x2{0, 0}, f32x2{0, 0}, f32x2{0, 0}};
#pragma unroll 1
    for (int o = 0; o < 4; ++o) {
      u32x4 w[8];
#pragma unroll
      for (int i = 0; i < 8; ++i) w[i] = ldnt<1>(wp + (size_t)(o * 8 + i) * 32 * (6 * DM));
#pragma unroll
      for (int i = 0; i < 8; ++i) {
        const float s = sv[(o * 8 + i) * 32];
#pragma unroll
        for (int e = 0; e < 4; ++e) acc[e] = pkfma(f32x2{s, s}, f32x2{bflo(w[i][e]), bfhi(w[i][e])}, acc[e]);
      }
    }
    float* red = (float*)(smem + 16384);
    *(f32x4*)(red + r0 * 128 + cg * 8) = f32x4{acc[0][0], acc[0][1], acc[1][0], acc[1][1]};
    *(f32x4*)(red + r0 * 128 + cg * 8 + 4) = f32x4{acc[2][0], acc[2][1], acc[3][0], acc[3][1]};
    __syncthreads();
    if (tid < 128) {
      float s = 0;
#pragma unroll
      for (int r = 0; r < 32; ++r) s += red[r * 128 + tid];
      st4_sc1((char*)mod + OFF_MOD2 + (128 * c + tid) * 4, __builtin_bit_cast(unsigned, s));
    }
    WAIT_V(0);
    __builtin_amdgcn_s_barrier();
    if (tid == 0) st4_sc1(cnt + (PL1 + c) * 32, 9u);
  }
}
enum { R_QKV = 0, R_PART = 1, R_F1 = 2, R_PART2 = 3 };
struct Dep {
  const char* base; unsigned voff; unsigned mul, add;
};
__device__ __forceinline__ unsigned dep_eval(unsigned v, const Dep& d) {
  v = rowmin16(v * d.mul + d.add);
  return min(min(rdl(v, 0), rdl(v, 16)), min(rdl(v, 32), rdl(v, 48)));
}
typedef unsigned u32x32 __attribute__((ext_vector_type(32)));
typedef unsigned u32x16 __attribute__((ext_vector_type(16)));
__device__ __forceinline__ void fast_ld0(const u32x32 (&W)[4], u32x16& pd, unsigned ea, unsigned da, int& b, int& lf, int cp, unsigned sa, unsigned mu, unsigned ad,
    unsigned rw, unsigned ab, const char* p1, const char* p0, unsigned l1, unsigned l0, const unsigned (&av)[8]) {
  asm volatile("s_setprio 0\n\t"
    "s_waitcnt vmcnt(12)\n\t"
    "s_barrier\n\t"
    "ds_read_b128 v[96:99], %[ea] offset:0\n\t"
    "ds_read_b128 v[100:103], %[ea] offset:2048\n\t"
    "ds_read_b128 v[104:107], %[da] offset:0\n\t"
    "ds_read_b128 v[108:111], %[da] offset:2048\n\t"
    "ds_read_b128 v[112:115], %[ea] offset:4096\n\t"
    "ds_read_b128 v[116:119], %[ea] offset:6144\n\t"
    "s_branch 2f\n\t"
    "1:\n\t"
    "s_waitcnt vmcnt(12)\n\t"
    "s_nop 1\n\t"
    "s_barrier\n\t"
    "2:\n\t"
    "s_waitcnt lgkmcnt(4)\n\t"
    "v_mfma_f32_16x16x32_bf16 v[64:67], v[128:131], v[96:99], 0\n\t"
    "ds_read_b128 v[120:123], %[da] offset:4096\n\t"
    "s_add_u32 m0, %[l1], 0\n\t"
    "ds_write_b128 %[rw], v[80:83] offset:0\n\t"
    "v_mfma_f32_16x16x32_bf16 v[68:71], v[132:135], v[96:99], 0\n\t"
    "ds_read_b128 v[124:127], %[da] offset:6144\n\t"
    "global_load_lds_dwordx4 %[av0], s[36:37] sc1\n\t"
    "ds_write_b128 %[rw], v[84:87] offset:1024\n\t"
    "v_mfma_f32_16x16x32_bf16 v[72:75], v[128:131], v[100:103], 0\n\t"
    "s_add_u32 m0, %[l1], 32768\n\t"
    "ds_write_b128 %[rw], v[88:91] offset:2048\n\t"
    "v_mfma_f32_16x16x32_bf16 v[76:79], v[132:135], v[100:103], 0\n\t"
    "global_load_lds_dwordx4 %[av1], s[36:37] sc1\n\t"
    "ds_write_b128 %[rw], v[92:95] offset:3072\n\t"
    "s_waitcnt lgkmcnt(8)\n\t"
    "v_mfma_f32_16x16x32_bf16 v[64:67], v[136:139], v[104:107], v[64:67]\n\t"
    "ds_read_b128 v[96:99], %[ea] offset:8192\n\t"
    "s_add_u32 m0, %[l1], 4096\n\t"
    "v_mfma_f32_16x16x32_bf16 v[68:71], v[140:143], v[104:107], v[68:71]\n\t"
    "ds_read_b128 v[100:103], %[ea] offset:10240\n\t"
    "global_load_lds_dwordx4 %[av2], s[36:37] sc1\n\t"
    "v_mfma_f32_16x16x32_bf16 v[72:75], v[136:139], v[108:111], v[72:75]\n\t"
    "s_add_u32 m0, %[l1], 36864\n\t"
    "v_mfma_f32_16x16x32_bf16 v[76:79], v[140:143], v[108:111], v[76:79]\n\t"
    "global_load_lds_dwordx4 %[av3], s[36:37] sc1\n\t"
    "s_waitcnt lgkmcnt(8)\n\t"
    "v_mfma_f32_16x16x32_bf16 v[64:67], v[144:147], v[112:115], v[64:67]\n\t"
    "ds_read_b128 v[104:107], %[da] offset:8192\n\t"
    "s_add_u32 m0, %[l1], 8192\n\t"
    "v_mfma_f32_16x16x32_bf16 v[68:71], v[148:151], v[112:115], v[68:71]\n\t"
    "ds_read_b128 v[108:111], %[da] offset:10240\n\t"
    "global_load_lds_dwordx4 %[av4], s[36:37] sc1\n\t"
    "v_mfma_f32_16x16x32_bf16 v[72:75], v[144:147], v[116:119], v[72:75]\n\t"
    "s_add_u32 m0, %[l1], 40960\n\t"
    "v_mfma_f32_16x16x32_bf16 v[76:79], v[148:151], v[116:119], v[76:79]\n\t"
    "global_load_lds_dwordx4 %[av5], s[36:37] sc1\n\t"
    "s_waitcnt lgkmcnt(7)\n\t"
    "v_mfma_f32_16x16x32_bf16 v[64:67], v[152:155], v[120:123], v[64:67]\n\t"
    "ds_read_b128 v[112:115], %[ea] offset:12288\n\t"
    "s_add_u32 m0, %[l1], 12288\n\t"
    "v_mfma_f32_16x16x32_bf16 v[68:71], v[156:159], v[120:123], v[68:71]\n\t"
    "ds_read_b128 v[116:119], %[ea] offset:14336\n\t"
    "global_load_lds_dwordx4 %[av6], s[36:37] sc1\n\t"
    "v_mfma_f32_16x16x32_bf16 v[72:75], v[152:155], v[124:127], v[72:75]\n\t"
    "s_add_u32 m0, %[l1], 45056\n\t"
    "v_mfma_f32_16x16x32_bf16 v[76:79], v[156:159], v[124:127], v[76:79]\n\t"
    "global_load_lds_dwordx4 %[av7], s[36:37] sc1\n\t"
    "s_waitcnt lgkmcnt(4)\n\t"
    "v_mfma_f32_16x16x32_bf16 v[64:67], v[160:163], v[96:99], v[64:67]\n\t"
    "ds_read_b128 v[120:123], %[da] offset:12288\n\t"
    "v_mfma_f32_16x16x32_bf16 v[68:71], v[164:167], v[96:99], v[68:71]\n\t"
    "ds_read_b128 v[124:127], %[da] offset:14336\n\t"
    "v_mfma_f32_16x16x32_bf16 v[72:75], v[160:163], v[100:103], v[72:75]\n\t"
    "v_mfma_f32_16x16x32_bf16 v[76:79], v[164:167], v[100:103], v[76:79]\n\t"
    "s_waitcnt lgkmcnt(4)\n\t"
    "v_mfma_f32_16x16x32_bf16 v[64:67], v[168:171], v[104:107], v[64:67]\n\t"
    "ds_read_b128 v[96:99], %[ea] offset:16384\n\t"
    "v_mfma_f32_16x16x32_bf16 v[68:71], v[172:175], v[104:107], v[68:71]\n\t"
    "ds_read_b128 v[100:103], %[ea] offset:18432\n\t"
    "v_mfma_f32_16x16x32_bf16 v[72:75], v[168:171], v[108:111], v[72:75]\n\t"
    "v_mfma_f32_16x16x32_bf16 v[76:79], v[172:175], v[108:111], v[76:79]\n\t"
    "s_waitcnt lgkmcnt(4)\n\t"
    "v_mfma_f32_16x16x32_bf16 v[64:67], v[176:179], v[112:115], v[64:67]\n\t"
    "ds_read_b128 v[104:107], %[da] offset:16384\n\t"
    "v_mfma_f32_16x16x32_bf16 v[68:71], v[180:183], v[112:115], v[68:71]\n\t"
    "ds_read_b128 v[108:111], %[da] offset:18432\n\t"
    "v_mfma_f32_16x16x32_bf16 v[72:75], v[176:179], v[116:119], v[72:75]\n\t"
    "v_mfma_f32_16x16x32_bf16 v[76:79], v[180:183], v[116:119], v[76:79]\n\t"
    "s_waitcnt lgkmcnt(4)\n\t"
    "v_mfma_f32_16x16x32_bf16 v[64:67], v[184:187], v[120:123], v[64:67]\n\t"
    "ds_read_b128 v[112:115], %[ea] offset:20480\n\t"
    "v_mfma_f32_16x16x32_bf16 v[68:71], v[188:191], v[120:123], v[68:71]\n\t"
    "ds_read_b128 v[116:119], %[ea] offset:22528\n\t"
    "v_mfma_f32_16x16x32_bf16 v[72:75], v[184:187], v[124:127], v[72:75]\n\t"
    "v_mfma_f32_16x16x32_bf16 v[76:79], v[188:191], v[124:127], v[76:79]\n\t"
    "s_waitcnt vmcnt(12)\n\t"
    "s_barrier\n\t"
    "s_waitcnt lgkmcnt(4)\n\t"
    "v_mfma_f32_16x16x32_bf16 v[64:67], v[192:195], v[96:99], v[64:67]\n\t"
    "ds_read_b128 v[120:123], %[da] offset:20480\n\t"
    "s_add_u32 m0, %[l0], 0\n\t"
    "v_mfma_f32_16x16x32_bf16 v[68:71], v[196:199], v[96:99], v[68:71]\n\t"
    "ds_read_b128 v[124:127], %[da] offset:22528\n\t"
    "global_load_lds_dwordx4 %[av0], s[38:39] sc1\n\t"
    "v_mfma_f32_16x16x32_bf16 v[72:75], v[192:195], v[100:103], v[72:75]\n\t"
    "s_add_u32 m0, %[l0], 32768\n\t"
    "v_mfma_f32_16x16x32_bf16 v[76:79], v[196:199], v[100:103], v[76:79]\n\t"
    "global_load_lds_dwordx4 %[av1], s[38:39] sc1\n\t"
    "s_waitcnt lgkmcnt(4)\n\t"
    "v_mfma_f32_16x16x32_bf16 v[64:67], v[200:203], v[104:107], v[64:67]\n\t"
    "ds_read_b128 v[96:99], %[ea] offset:24576\n\t"
    "s_add_u32 m0, %[l0], 4096\n\t"
    "v_mfma_f32_16x16x32_bf16 v[68:71], v[204:207], v[104:107], v[68:71]\n\t"
    "ds_read_b128 v[100:103], %[ea] offset:26624\n\t"
    "global_load_lds_dwordx4 %[av2], s[38:39] sc1\n\t"
    "v_mfma_f32_16x16x32_bf16 v[72:75], v[200:203], v[108:111], v[72:75]\n\t"
    "s_add_u32 m0, %[l0], 36864\n\t"
    "v_mfma_f32_16x16x32_bf16 v[76:79], v[204:207], v[108:111], v[76:79]\n\t"
    "global_load_lds_dwordx4 %[av3], s[38:39] sc1\n\t"
    "s_add_u32 s36, s36, %[ab]\n\t"
    "s_addc_u32 s37, s37, 0\n\t"
    "s_waitcnt lgkmcnt(4)\n\t"
    "v_mfma_f32_16x16x32_bf16 v[64:67], v[208:211], v[112:115], v[64:67]\n\t"
    "ds_read_b128 v[104:107], %[da] offset:24576\n\t"
    "s_add_u32 m0, %[l0], 8192\n\t"
    "v_mfma_f32_16x16x32_bf16 v[68:71], v[212:215], v[112:115], v[68:71]\n\t"
    "ds_read_b128 v[108:111], %[da] offset:26624\n\t"
    "global_load_lds_dwordx4 %[av4], s[38:39] sc1\n\t"
    "v_mfma_f32_16x16x32_bf16 v[72:75], v[208:211], v[116:119], v[72:75]\n\t"
    "ds_read_b32 v39, %[sa]\n\t"
    "s_add_u32 m0, %[l0], 40960\n\t"
    "v_mfma_f32_16x16x32_bf16 v[76:79], v[212:215], v[116:119], v[76:79]\n\t"
    "v_xor_b32 %[sa], 0x100, %[sa]\n\t"
    "global_load_lds_dwordx4 %[av5], s[38:39] sc1\n\t"
    "s_xor_b32 %[l1], %[l1], 0x10000\n\t"
    "s_waitcnt lgkmcnt(5)\n\t"
    "v_mfma_f32_16x16x32_bf16 v[64:67], v[216:219], v[120:123], v[64:67]\n\t"
    "ds_read_b128 v[112:115], %[ea] offset:28672\n\t"
    "s_add_u32 m0, %[l0], 12288\n\t"
    "v_mfma_f32_16x16x32_bf16 v[68:71], v[220:223], v[120:123], v[68:71]\n\t"
    "ds_read_b128 v[116:119], %[ea] offset:30720\n\t"
    "global_load_lds_dwordx4 %[av6], s[38:39] sc1\n\t"
    "v_mfma_f32_16x16x32_bf16 v[72:75], v[216:219], v[124:127], v[72:75]\n\t"
    "v_xor_b32 %[ea], 0x10000, %[ea]\n\t"
    "s_add_u32 m0, %[l0], 45056\n\t"
    "v_mfma_f32_16x16x32_bf16 v[76:79], v[220:223], v[124:127], v[76:79]\n\t"
    "global_load_lds_dwordx4 %[av7], s[38:39] sc1\n\t"
    "s_waitcnt lgkmcnt(5)\n\t"
    "v_mfma_f32_16x16x32_bf16 v[64:67], v[224:227], v[96:99], v[64:67]\n\t"
    "ds_read_b128 v[120:123], %[da] offset:28672\n\t"
    "v_mfma_f32_16x16x32_bf16 v[68:71], v[228:231], v[96:99], v[68:71]\n\t"
    "ds_read_b128 v[124:127], %[da] offset:30720\n\t"
    "v_mfma_f32_16x16x32_bf16 v[72:75], v[224:227], v[100:103], v[72:75]\n\t"
    "v_xor_b32 %[da], 0x10000, %[da]\n\t"
    "v_mfma_f32_16x16x32_bf16 v[76:79], v[228:231], v[100:103], v[76:79]\n\t"
    "s_waitcnt lgkmcnt(5)\n\t"
    "v_mfma_f32_16x16x32_bf16 v[64:67], v[232:235], v[104:107], v[64:67]\n\t"
    "ds_read_b128 v[96:99], %[ea] offset:0\n\t"
    "v_mfma_f32_16x16x32_bf16 v[68:71], v[236:239], v[104:107], v[68:71]\n\t"
    "ds_read_b128 v[100:103], %[ea] offset:2048\n\t"
    "v_mfma_f32_16x16x32_bf16 v[72:75], v[232:235], v[108:111], v[72:75]\n\t"
    "v_mfma_f32_16x16x32_bf16 v[76:79], v[236:239], v[108:111], v[76:79]\n\t"
    "s_waitcnt lgkmcnt(4)\n\t"
    "v_mfma_f32_16x16x32_bf16 v[64:67], v[240:243], v[112:115], v[64:67]\n\t"
    "ds_read_b128 v[104:107], %[da] offset:0\n\t"
    "v_mad_u32_u24 v39, v39, %[mu], %[ad]\n\t"
    "v_mfma_f32_16x16x32_bf16 v[68:71], v[244:247], v[112:115], v[68:71]\n\t"
    "ds_read_b128 v[108:111], %[da] offset:2048\n\t"
    "v_cmp_ge_i32 vcc, %[lf], v39\n\t"
    "s_cmp_eq_u64 vcc, 0\n\t"
    "s_addc_u32 %[lf], %[lf], 0\n\t"
    "v_mfma_f32_16x16x32_bf16 v[72:75], v[240:243], v[116:119], v[72:75]\n\t"
    "v_mfma_f32_16x16x32_bf16 v[76:79], v[244:247], v[116:119], v[76:79]\n\t"
    "v_cmp_ge_i32 vcc, %[lf], v39\n\t"
    "s_cmp_eq_u64 vcc, 0\n\t"
    "s_addc_u32 %[lf], %[lf], 0\n\t"
    "s_waitcnt lgkmcnt(4)\n\t"
    "v_mfma_f32_16x16x32_bf16 v[80:83], v[248:251], v[120:123], v[64:67]\n\t"
    "ds_read_b128 v[112:115], %[ea] offset:4096\n\t"
    "s_min_i32 %[lf], %[lf], %[cp]\n\t"
    "s_add_u32 %[b], %[b], 1\n\t"
    "v_mfma_f32_16x16x32_bf16 v[84:87], v[252:255], v[120:123], v[68:71]\n\t"
    "ds_read_b128 v[116:119], %[ea] offset:6144\n\t"
    "v_mfma_f32_16x16x32_bf16 v[88:91], v[248:251], v[124:127], v[72:75]\n\t"
    "v_mfma_f32_16x16x32_bf16 v[92:95], v[252:255], v[124:127], v[76:79]\n\t"
    "s_add_u32 s38, s38, %[ab]\n\t"
    "s_addc_u32 s39, s39, 0\n\t"
    "s_xor_b32 %[l0], %[l0], 0x10000\n\t"
    "s_cmp_lt_i32 %[b], %[lf]\n\t"
    "s_cbranch_scc1 1b\n\t"
    "s_waitcnt lgkmcnt(0)\n\t"
    "s_nop 4"
    : "=&{v[80:95]}"(pd), [ea] "+&v"(ea), [da] "+&v"(da), [b] "+&s"(b), [lf] "+&s"(lf), "+{s[36:37]}"(p1), "+{s[38:39]}"(p0), [sa] "+&v"(sa), [l1] "+&s"(l1), [l0] "+&s"(l0)
    : [rw] "v"(rw), [ab] "s"(ab), [cp] "s"(cp), [mu] "s"(mu), [ad] "v"(ad), "{v[128:159]}"(W[0]), "{v[160:191]}"(W[1]), "{v[192:223]}"(W[2]), "{v[224:255]}"(W[3]), [av0] "v"(av[0]), [av1] "v"(av[1]), [av2] "v"(av[2]), [av3] "v"(av[3]), [av4] "v"(av[4]), [av5] "v"(av[5]), [av6] "v"(av[6]), [av7] "v"(av[7])
    : "v39", "v64", "v65", "v66", "v67", "v68", "v69", "v70", "v71", "v72", "v73", "v74", "v75", "v76", "v77", "v78", "v79", "v96", "v97", "v98", "v99", "v100", "v101", "v102", "v103", "v104", "v105", "v106", "v107", "v108", "v109", "v110", "v111", "v112", "v113", "v114", "v115", "v116", "v117", "v118", "v119", "v120", "v121", "v122", "v123", "v124", "v125", "v126", "v127", "vcc", "scc", "m0", "memory");
}
__device__ __forceinline__ void fast_ld2(const u32x32 (&W)[4], u32x16& pd, unsigned ea, unsigned da, int& b, int& lf, int cp, unsigned sa, unsigned mu, unsigned ad,
    unsigned rw, unsigned ab, const char* p1, const char* p0, unsigned l1, unsigned l0, const unsigned (&av)[8]) {
  asm volatile("s_setprio 0\n\t"
    "s_waitcnt vmcnt(6)\n\t"
    "s_barrier\n\t"
    "ds_read_b128 v[96:99], %[ea] offset:0\n\t"
    "ds_read_b128 v[100:103], %[ea] offset:2048\n\t"
    "ds_read_b128 v[104:107], %[da] offset:0\n\t"
    "ds_read_b128 v[108:111], %[da] offset:2048\n\t"
    "ds_read_b128 v[112:115], %[ea] offset:4096\n\t"
    "ds_read_b128 v[116:119], %[ea] offset:6144\n\t"
    "s_branch 2f\n\t"
    "1:\n\t"
    "s_waitcnt vmcnt(6)\n\t"
    "s_nop 1\n\t"
    "s_barrier\n\t"
    "2:\n\t"
    "s_waitcnt lgkmcnt(4)\n\t"
    "v_mfma_f32_16x16x32_bf16 v[64:67], v[128:131], v[96:99], 0\n\t"
    "ds_read_b128 v[120:123], %[da] offset:4096\n\t"
    "s_add_u32 m0, %[l1], 0\n\t"
    "ds_write_b128 %[rw], v[80:83] offset:0\n\t"
    "v_mfma_f32_16x16x32_bf16 v[68:71], v[132:135], v[96:99], 0\n\t"
    "ds_read_b128 v[124:127], %[da] offset:6144\n\t"
    "global_load_lds_dwordx4 %[av0], s[36:37] sc1\n\t"
    "ds_write_b128 %[rw], v[84:87] offset:1024\n\t"
    "v_mfma_f32_16x16x32_bf16 v[72:75], v[128:131], v[100:103], 0\n\t"
    "ds_write_b128 %[rw], v[88:91] offset:2048\n\t"
    "v_mfma_f32_16x16x32_bf16 v[76:79], v[132:135], v[100:103], 0\n\t"
    "ds_write_b128 %[rw], v[92:95] offset:3072\n\t"
    "s_waitcnt lgkmcnt(8)\n\t"
    "v_mfma_f32_16x16x32_bf16 v[64:67], v[136:139], v[104:107], v[64:67]\n\t"
    "ds_read_b128 v[96:99], %[ea] offset:8192\n\t"
    "s_add_u32 m0, %[l1], 4096\n\t"
    "v_mfma_f32_16x16x32_bf16 v[68:71], v[140:143], v[104:107], v[68:71]\n\t"
    "ds_read_b128 v[100:103], %[ea] offset:10240\n\t"
    "global_load_lds_dwordx4 %[av2], s[36:37] sc1\n\t"
    "v_mfma_f32_16x16x32_bf16 v[72:75], v[136:139], v[108:111], v[72:75]\n\t"
    "v_mfma_f32_16x16x32_bf16 v[76:79], v[140:143], v[108:111], v[76:79]\n\t"
    "s_waitcnt lgkmcnt(8)\n\t"
    "v_mfma_f32_16x16x32_bf16 v[64:67], v[144:147], v[112:115], v[64:67]\n\t"
    "ds_read_b128 v[104:107], %[da] offset:8192\n\t"
    "s_add_u32 m0, %[l1], 8192\n\t"
    "v_mfma_f32_16x16x32_bf16 v[68:71], v[148:151], v[112:115], v[68:71]\n\t"
    "ds_read_b128 v[108:111], %[da] offset:10240\n\t"
    "global_load_lds_dwordx4 %[av4], s[36:37] sc1\n\t"
    "v_mfma_f32_16x16x32_bf16 v[72:75], v[144:147], v[116:119], v[72:75]\n\t"
    "v_mfma_f32_16x16x32_bf16 v[76:79], v[148:151], v[116:119], v[76:79]\n\t"
    "s_waitcnt lgkmcnt(7)\n\t"
    "v_mfma_f32_16x16x32_bf16 v[64:67], v[152:155], v[120:123], v[64:67]\n\t"
    "ds_read_b128 v[112:115], %[ea] offset:12288\n\t"
    "s_add_u32 m0, %[l1], 12288\n\t"
    "v_mfma_f32_16x16x32_bf16 v[68:71], v[156:159], v[120:123], v[68:71]\n\t"
    "ds_read_b128 v[116:119], %[ea] offset:14336\n\t"
    "global_load_lds_dwordx4 %[av6], s[36:37] sc1\n\t"
    "v_mfma_f32_16x16x32_bf16 v[72:75], v[152:155], v[124:127], v[72:75]\n\t"
    "v_mfma_f32_16x16x32_bf16 v[76:79], v[156:159], v[124:127], v[76:79]\n\t"
    "s_waitcnt lgkmcnt(4)\n\t"
    "v_mfma_f32_16x16x32_bf16 v[64:67], v[160:163], v[96:99], v[64:67]\n\t"
    "ds_read_b128 v[120:123], %[da] offset:12288\n\t"
    "v_mfma_f32_16x16x32_bf16 v[68:71], v[164:167], v[96:99], v[68:71]\n\t"
    "ds_read_b128 v[124:127], %[da] offset:14336\n\t"
    "v_mfma_f32_16x16x32_bf16 v[72:75], v[160:163], v[100:103], v[72:75]\n\t"
    "v_mfma_f32_16x16x32_bf16 v[76:79], v[164:167], v[100:103], v[76:79]\n\t"
    "s_waitcnt lgkmcnt(4)\n\t"
    "v_mfma_f32_16x16x32_bf16 v[64:67], v[168:171], v[104:107], v[64:67]\n\t"
    "ds_read_b128 v[96:99], %[ea] offset:16384\n\t"
    "v_mfma_f32_16x16x32_bf16 v[68:71], v[172:175], v[104:107], v[68:71]\n\t"
    "ds_read_b128 v[100:103], %[ea] offset:18432\n\t"
    "v_mfma_f32_16x16x32_bf16 v[72:75], v[168:171], v[108:111], v[72:75]\n\t"
    "v_mfma_f32_16x16x32_bf16 v[76:79], v[172:175], v[108:111], v[76:79]\n\t"
    "s_waitcnt lgkmcnt(4)\n\t"
    "v_mfma_f32_16x16x32_bf16 v[64:67], v[176:179], v[112:115], v[64:67]\n\t"
    "ds_read_b128 v[104:107], %[da] offset:16384\n\t"
    "v_mfma_f32_16x16x32_bf16 v[68:71], v[180:183], v[112:115], v[68:71]\n\t"
    "ds_read_b128 v[108:111], %[da] offset:18432\n\t"
    "v_mfma_f32_16x16x32_bf16 v[72:75], v[176:179], v[116:119], v[72:75]\n\t"
    "v_mfma_f32_16x16x32_bf16 v[76:79], v[180:183], v[116:119], v[76:79]\n\t"
    "s_waitcnt lgkmcnt(4)\n\t"
    "v_mfma_f32_16x16x32_bf16 v[64:67], v[184:187], v[120:123], v[64:67]\n\t"
    "ds_read_b128 v[112:115], %[ea] offset:20480\n\t"
    "v_mfma_f32_16x16x32_bf16 v[68:71], v[188:191], v[120:123], v[68:71]\n\t"
    "ds_read_b128 v[116:119], %[ea] offset:22528\n\t"
    "v_mfma_f32_16x16x32_bf16 v[72:75], v[184:187], v[124:127], v[72:75]\n\t"
    "v_mfma_f32_16x16x32_bf16 v[76:79], v[188:191], v[124:127], v[76:79]\n\t"
    "s_waitcnt vmcnt(6)\n\t"
    "s_barrier\n\t"
    "s_waitcnt lgkmcnt(4)\n\t"
    "v_mfma_f32_16x16x32_bf16 v[64:67], v[192:195], v[96:99], v[64:67]\n\t"
    "ds_read_b128 v[120:123], %[da] offset:20480\n\t"
    "s_add_u32 m0, %[l0], 0\n\t"
    "v_mfma_f32_16x16x32_bf16 v[68:71], v[196:199], v[96:99], v[68:71]\n\t"
    "ds_read_b128 v[124:127], %[da] offset:22528\n\t"
    "global_load_lds_dwordx4 %[av0], s[38:39] sc1\n\t"
    "v_mfma_f32_16x16x32_bf16 v[72:75], v[192:195], v[100:103], v[72:75]\n\t"
    "v_mfma_f32_16x16x32_bf16 v[76:79], v[196:199], v[100:103], v[76:79]\n\t"
    "s_waitcnt lgkmcnt(4)\n\t"
    "v_mfma_f32_16x16x32_bf16 v[64:67], v[200:203], v[104:107], v[64:67]\n\t"
    "ds_read_b128 v[96:99], %[ea] offset:24576\n\t"
    "s_add_u32 m0, %[l0], 4096\n\t"
    "v_mfma_f32_16x16x32_bf16 v[68:71], v[204:207], v[104:107], v[68:71]\n\t"
    "ds_read_b128 v[100:103], %[ea] offset:26624\n\t"
    "global_load_lds_dwordx4 %[av2], s[38:39] sc1\n\t"
    "v_mfma_f32_16x16x32_bf16 v[72:75], v[200:203], v[108:111], v[72:75]\n\t"
    "v_mfma_f32_16x16x32_bf16 v[76:79], v[204:207], v[108:111], v[76:79]\n\t"
    "s_add_u32 s36, s36, %[ab]\n\t"
    "s_addc_u32 s37, s37, 0\n\t"
    "s_waitcnt lgkmcnt(4)\n\t"
    "v_mfma_f32_16x16x32_bf16 v[64:67], v[208:211], v[112:115], v[64:67]\n\t"
    "ds_read_b128 v[104:107], %[da] offset:24576\n\t"
    "s_add_u32 m0, %[l0], 8192\n\t"
    "v_mfma_f32_16x16x32_bf16 v[68:71], v[212:215], v[112:115], v[68:71]\n\t"
    "ds_read_b128 v[108:111], %[da] offset:26624\n\t"
    "global_load_lds_dwordx4 %[av4], s[38:39] sc1\n\t"
    "v_mfma_f32_16x16x32_bf16 v[72:75], v[208:211], v[116:119], v[72:75]\n\t"
    "ds_read_b32 v39, %[sa]\n\t"
    "v_mfma_f32_16x16x32_bf16 v[76:79], v[212:215], v[116:119], v[76:79]\n\t"
    "v_xor_b32 %[sa], 0x100, %[sa]\n\t"
    "s_xor_b32 %[l1], %[l1], 0x10000\n\t"
    "s_waitcnt lgkmcnt(5)\n\t"
    "v_mfma_f32_16x16x32_bf16 v[64:67], v[216:219], v[120:123], v[64:67]\n\t"
    "ds_read_b128 v[112:115], %[ea] offset:28672\n\t"
    "s_add_u32 m0, %[l0], 12288\n\t"
    "v_mfma_f32_16x16x32_bf16 v[68:71], v[220:223], v[120:123], v[68:71]\n\t"
    "ds_read_b128 v[116:119], %[ea] offset:30720\n\t"
    "global_load_lds_dwordx4 %[av6], s[38:39] sc1\n\t"
    "v_mfma_f32_16x16x32_bf16 v[72:75], v[216:219], v[124:127], v[72:75]\n\t"
    "v_xor_b32 %[ea], 0x10000, %[ea]\n\t"
    "v_mfma_f32_16x16x32_bf16 v[76:79], v[220:223], v[124:127], v[76:79]\n\t"
    "s_waitcnt lgkmcnt(5)\n\t"
    "v_mfma_f32_16x16x32_bf16 v[64:67], v[224:227], v[96:99], v[64:67]\n\t"
    "ds_read_b128 v[120:123], %[da] offset:28672\n\t"
    "v_mfma_f32_16x16x32_bf16 v[68:71], v[228:231], v[96:99], v[68:71]\n\t"
    "ds_read_b128 v[124:127], %[da] offset:30720\n\t"
    "v_mfma_f32_16x16x32_bf16 v[72:75], v[224:227], v[100:103], v[72:75]\n\t"
    "v_xor_b32 %[da], 0x10000, %[da]\n\t"
    "v_mfma_f32_16x16x32_bf16 v[76:79], v[228:231], v[100:103], v[76:79]\n\t"
    "s_waitcnt lgkmcnt(5)\n\t"
    "v_mfma_f32_16x16x32_bf16 v[64:67], v[232:235], v[104:107], v[64:67]\n\t"
    "ds_read_b128 v[96:99], %[ea] offset:0\n\t"
    "v_mfma_f32_16x16x32_bf16 v[68:71], v[236:239], v[104:107], v[68:71]\n\t"
    "ds_read_b128 v[100:103], %[ea] offset:2048\n\t"
    "v_mfma_f32_16x16x32_bf16 v[72:75], v[232:235], v[108:111], v[72:75]\n\t"
    "v_mfma_f32_16x16x32_bf16 v[76:79], v[236:239], v[108:111], v[76:79]\n\t"
    "s_waitcnt lgkmcnt(4)\n\t"
    "v_mfma_f32_16x16x32_bf16 v[64:67], v[240:243], v[112:115], v[64:67]\n\t"
    "ds_read_b128 v[104:107], %[da] offset:0\n\t"
    "v_mad_u32_u24 v39, v39, %[mu], %[ad]\n\t"
    "v_mfma_f32_16x16x32_bf16 v[68:71], v[244:247], v[112:115], v[68:71]\n\t"
    "ds_read_b128 v[108:111], %[da] offset:2048\n\t"
    "v_cmp_ge_i32 vcc, %[lf], v39\n\t"
    "s_cmp_eq_u64 vcc, 0\n\t"
    "s_addc_u32 %[lf], %[lf], 0\n\t"
    "v_mfma_f32_16x16x32_bf16 v[72:75], v[240:243], v[116:119], v[72:75]\n\t"
    "v_mfma_f32_16x16x32_bf16 v[76:79], v[244:247], v[116:119], v[76:79]\n\t"
    "v_cmp_ge_i32 vcc, %[lf], v39\n\t"
    "s_cmp_eq_u64 vcc, 0\n\t"
    "s_addc_u32 %[lf], %[lf], 0\n\t"
    "s_waitcnt lgkmcnt(4)\n\t"
    "v_mfma_f32_16x16x32_bf16 v[80:83], v[248:251], v[120:123], v[64:67]\n\t"
    "ds_read_b128 v[112:115], %[ea] offset:4096\n\t"
    "s_min_i32 %[lf], %[lf], %[cp]\n\t"
    "s_add_u32 %[b], %[b], 1\n\t"
    "v_mfma_f32_16x16x32_bf16 v[84:87], v[252:255], v[120:123], v[68:71]\n\t"
    "ds_read_b128 v[116:119], %[ea] offset:6144\n\t"
    "v_mfma_f32_16x16x32_bf16 v[88:91], v[248:251], v[124:127], v[72:75]\n\t"
    "v_mfma_f32_16x16x32_bf16 v[92:95], v[252:255], v[124:127], v[76:79]\n\t"
    "s_add_u32 s38, s38, %[ab]\n\t"
    "s_addc_u32 s39, s39, 0\n\t"
    "s_xor_b32 %[l0], %[l0], 0x10000\n\t"
    "s_cmp_lt_i32 %[b], %[lf]\n\t"
    "s_cbranch_scc1 1b\n\t"
    "s_waitcnt lgkmcnt(0)\n\t"
    "s_nop 4"
    : "=&{v[80:95]}"(pd), [ea] "+&v"(ea), [da] "+&v"(da), [b] "+&s"(b), [lf] "+&s"(lf), "+{s[36:37]}"(p1), "+{s[38:39]}"(p0), [sa] "+&v"(sa), [l1] "+&s"(l1), [l0] "+&s"(l0)
    : [rw] "v"(rw), [ab] "s"(ab), [cp] "s"(cp), [mu] "s"(mu), [ad] "v"(ad), "{v[128:159]}"(W[0]), "{v[160:191]}"(W[1]), "{v[192:223]}"(W[2]), "{v[224:255]}"(W[3]), [av0] "v"(av[0]), [av1] "v"(av[1]), [av2] "v"(av[2]), [av3] "v"(av[3]), [av4] "v"(av[4]), [av5] "v"(av[5]), [av6] "v"(av[6]), [av7] "v"(av[7])
    : "v39", "v64", "v65", "v66", "v67", "v68", "v69", "v70", "v71", "v72", "v73", "v74", "v75", "v76", "v77", "v78", "v79", "v96", "v97", "v98", "v99", "v100", "v101", "v102", "v103", "v104", "v105", "v106", "v107", "v108", "v109", "v110", "v111", "v112", "v113", "v114", "v115", "v116", "v117", "v118", "v119", "v120", "v121", "v122", "v123", "v124", "v125", "v126", "v127", "vcc", "scc", "m0", "memory");
}
__device__ __forceinline__ void fast_st0(const u32x32 (&W)[4], u32x16& pd, unsigned ea, unsigned da, int& b, int& lf, int cp, unsigned sa, unsigned mu, unsigned ad,
    unsigned rw, unsigned ab, const char* p1, const char* p0, unsigned co0, unsigned co1, int po, int pf, unsigned w0, unsigned sl, unsigned dv, const char* db, const char* pg) {
  unsigned st;
  asm volatile("s_barrier\n\t"
    "ds_read_b128 v[96:99], %[ea] offset:0\n\t"
    "ds_read_b128 v[100:103], %[ea] offset:2048\n\t"
    "ds_read_b128 v[104:107], %[da] offset:0\n\t"
    "ds_read_b128 v[108:111], %[da] offset:2048\n\t"
    "ds_read_b128 v[112:115], %[ea] offset:4096\n\t"
    "ds_read_b128 v[116:119], %[ea] offset:6144\n\t"
    "s_branch 2f\n\t"
    "1:\n\t"
    "s_barrier\n\t"
    "2:\n\t"
    "s_waitcnt lgkmcnt(4)\n\t"
    "v_mfma_f32_16x16x32_bf16 v[64:67], v[128:131], v[96:99], 0\n\t"
    "ds_read_b128 v[120:123], %[da] offset:4096\n\t"
    "v_mfma_f32_16x16x32_bf16 v[68:71], v[132:135], v[96:99], 0\n\t"
    "ds_read_b128 v[124:127], %[da] offset:6144\n\t"
    "v_mfma_f32_16x16x32_bf16 v[72:75], v[128:131], v[100:103], 0\n\t"
    "v_mfma_f32_16x16x32_bf16 v[76:79], v[132:135], v[100:103], 0\n\t"
    "s_waitcnt lgkmcnt(4)\n\t"
    "v_mfma_f32_16x16x32_bf16 v[64:67], v[136:139], v[104:107], v[64:67]\n\t"
    "ds_read_b128 v[96:99], %[ea] offset:8192\n\t"
    "v_mfma_f32_16x16x32_bf16 v[68:71], v[140:143], v[104:107], v[68:71]\n\t"
    "ds_read_b128 v[100:103], %[ea] offset:10240\n\t"
    "v_mfma_f32_16x16x32_bf16 v[72:75], v[136:139], v[108:111], v[72:75]\n\t"
    "v_mfma_f32_16x16x32_bf16 v[76:79], v[140:143], v[108:111], v[76:79]\n\t"
    "s_waitcnt lgkmcnt(4)\n\t"
    "v_mfma_f32_16x16x32_bf16 v[64:67], v[144:147], v[112:115], v[64:67]\n\t"
    "ds_read_b128 v[104:107], %[da] offset:8192\n\t"
    "v_mfma_f32_16x16x32_bf16 v[68:71], v[148:151], v[112:115], v[68:71]\n\t"
    "ds_read_b128 v[108:111], %[da] offset:10240\n\t"
    "v_mfma_f32_16x16x32_bf16 v[72:75], v[144:147], v[116:119], v[72:75]\n\t"
    "v_mfma_f32_16x16x32_bf16 v[76:79], v[148:151], v[116:119], v[76:79]\n\t"
    "s_waitcnt lgkmcnt(4)\n\t"
    "v_mfma_f32_16x16x32_bf16 v[64:67], v[152:155], v[120:123], v[64:67]\n\t"
    "ds_read_b128 v[112:115], %[ea] offset:12288\n\t"
    "v_mfma_f32_16x16x32_bf16 v[68:71], v[156:159], v[120:123], v[68:71]\n\t"
    "ds_read_b128 v[116:119], %[ea] offset:14336\n\t"
    "v_mfma_f32_16x16x32_bf16 v[72:75], v[152:155], v[124:127], v[72:75]\n\t"
    "v_mfma_f32_16x16x32_bf16 v[76:79], v[156:159], v[124:127], v[76:79]\n\t"
    "s_waitcnt lgkmcnt(4)\n\t"
    "v_mfma_f32_16x16x32_bf16 v[64:67], v[160:163], v[96:99], v[64:67]\n\t"
    "ds_read_b128 v[120:123], %[da] offset:12288\n\t"
    "v_mfma_f32_16x16x32_bf16 v[68:71], v[164:167], v[96:99], v[68:71]\n\t"
    "ds_read_b128 v[124:127], %[da] offset:14336\n\t"
    "v_mfma_f32_16x16x32_bf16 v[72:75], v[160:163], v[100:103], v[72:75]\n\t"
    "v_mfma_f32_16x16x32_bf16 v[76:79], v[164:167], v[100:103], v[76:79]\n\t"
    "s_waitcnt lgkmcnt(4)\n\t"
    "v_mfma_f32_16x16x32_bf16 v[64:67], v[168:171], v[104:107], v[64:67]\n\t"
    "ds_read_b128 v[96:99], %[ea] offset:16384\n\t"
    "v_mfma_f32_16x16x32_bf16 v[68:71], v[172:175], v[104:107], v[68:71]\n\t"
    "ds_read_b128 v[100:103], %[ea] offset:18432\n\t"
    "v_mfma_f32_16x16x32_bf16 v[72:75], v[168:171], v[108:111], v[72:75]\n\t"
    "v_mfma_f32_16x16x32_bf16 v[76:79], v[172:175], v[108:111], v[76:79]\n\t"
    "s_waitcnt lgkmcnt(4)\n\t"
    "v_mfma_f32_16x16x32_bf16 v[64:67], v[176:179], v[112:115], v[64:67]\n\t"
    "ds_read_b128 v[104:107], %[da] offset:16384\n\t"
    "v_mfma_f32_16x16x32_bf16 v[68:71], v[180:183], v[112:115], v[68:71]\n\t"
    "ds_read_b128 v[108:111], %[da] offset:18432\n\t"
    "v_mfma_f32_16x16x32_bf16 v[72:75], v[176:179], v[116:119], v[72:75]\n\t"
    "v_mfma_f32_16x16x32_bf16 v[76:79], v[180:183], v[116:119], v[76:79]\n\t"
    "s_waitcnt lgkmcnt(4)\n\t"
    "v_mfma_f32_16x16x32_bf16 v[64:67], v[184:187], v[120:123], v[64:67]\n\t"
    "ds_read_b128 v[112:115], %[ea] offset:20480\n\t"
    "v_mfma_f32_16x16x32_bf16 v[68:71], v[188:191], v[120:123], v[68:71]\n\t"
    "ds_read_b128 v[116:119], %[ea] offset:22528\n\t"
    "v_mfma_f32_16x16x32_bf16 v[72:75], v[184:187], v[124:127], v[72:75]\n\t"
    "v_mfma_f32_16x16x32_bf16 v[76:79], v[188:191], v[124:127], v[76:79]\n\t"
    "s_waitcnt vmcnt(0)\n\t"
    "s_barrier\n\t"
    "s_waitcnt lgkmcnt(4)\n\t"
    "v_mfma_f32_16x16x32_bf16 v[64:67], v[192:195], v[96:99], v[64:67]\n\t"
    "ds_read_b128 v[120:123], %[da] offset:20480\n\t"
    "v_mfma_f32_16x16x32_bf16 v[68:71], v[196:199], v[96:99], v[68:71]\n\t"
    "ds_read_b128 v[124:127], %[da] offset:22528\n\t"
    "v_mfma_f32_16x16x32_bf16 v[72:75], v[192:195], v[100:103], v[72:75]\n\t"
    "ds_read_b128 v[48:51], %[rw] offset:0\n\t"
    "v_mfma_f32_16x16x32_bf16 v[76:79], v[196:199], v[100:103], v[76:79]\n\t"
    "ds_read_b128 v[52:55], %[rw] offset:1024\n\t"
    "s_cmp_eq_u32 %[w0], 0\n\t"
    "s_cbranch_scc1 5f\n\t"
    "6:\n\t"
    "s_waitcnt lgkmcnt(6)\n\t"
    "v_mfma_f32_16x16x32_bf16 v[64:67], v[200:203], v[104:107], v[64:67]\n\t"
    "ds_read_b128 v[96:99], %[ea] offset:24576\n\t"
    "v_mfma_f32_16x16x32_bf16 v[68:71], v[204:207], v[104:107], v[68:71]\n\t"
    "ds_read_b128 v[100:103], %[ea] offset:26624\n\t"
    "v_mfma_f32_16x16x32_bf16 v[72:75], v[200:203], v[108:111], v[72:75]\n\t"
    "ds_read_b128 v[56:59], %[rw] offset:2048\n\t"
    "v_mfma_f32_16x16x32_bf16 v[76:79], v[204:207], v[108:111], v[76:79]\n\t"
    "ds_read_b128 v[60:63], %[rw] offset:3072\n\t"
    "s_waitcnt lgkmcnt(0)\n\t"
    "v_mfma_f32_16x16x32_bf16 v[64:67], v[208:211], v[112:115], v[64:67]\n\t"
    "ds_read_b128 v[104:107], %[da] offset:24576\n\t"
    "v_add_f32 v48, v48, v80\n\t"
    "v_add_f32 v49, v49, v81\n\t"
    "v_mfma_f32_16x16x32_bf16 v[68:71], v[212:215], v[112:115], v[68:71]\n\t"
    "ds_read_b128 v[108:111], %[da] offset:26624\n\t"
    "v_add_f32 v50, v50, v82\n\t"
    "v_mfma_f32_16x16x32_bf16 v[72:75], v[208:211], v[116:119], v[72:75]\n\t"
    "ds_read_b32 v39, %[sa]\n\t"
    "v_add_f32 v51, v51, v83\n\t"
    "v_mfma_f32_16x16x32_bf16 v[76:79], v[212:215], v[116:119], v[76:79]\n\t"
    "v_xor_b32 %[sa], 0x100, %[sa]\n\t"
    "v_add_f32 v52, v52, v84\n\t"
    "v_add_f32 v53, v53, v85\n\t"
    "v_mfma_f32_16x16x32_bf16 v[64:67], v[216:219], v[120:123], v[64:67]\n\t"
    "ds_read_b128 v[112:115], %[ea] offset:28672\n\t"
    "v_add_f32 v54, v54, v86\n\t"
    "v_mfma_f32_16x16x32_bf16 v[68:71], v[220:223], v[120:123], v[68:71]\n\t"
    "ds_read_b128 v[116:119], %[ea] offset:30720\n\t"
    "v_add_f32 v55, v55, v87\n\t"
    "v_mfma_f32_16x16x32_bf16 v[72:75], v[216:219], v[124:127], v[72:75]\n\t"
    "v_xor_b32 %[ea], 0x10000, %[ea]\n\t"
    "v_add_f32 v56, v56, v88\n\t"
    "v_add_f32 v57, v57, v89\n\t"
    "v_mfma_f32_16x16x32_bf16 v[76:79], v[220:223], v[124:127], v[76:79]\n\t"
    "v_add_f32 v58, v58, v90\n\t"
    "v_mfma_f32_16x16x32_bf16 v[64:67], v[224:227], v[96:99], v[64:67]\n\t"
    "ds_read_b128 v[120:123], %[da] offset:28672\n\t"
    "v_add_f32 v59, v59, v91\n\t"
    "v_mfma_f32_16x16x32_bf16 v[68:71], v[228:231], v[96:99], v[68:71]\n\t"
    "ds_read_b128 v[124:127], %[da] offset:30720\n\t"
    "v_add_f32 v60, v60, v92\n\t"
    "v_mfma_f32_16x16x32_bf16 v[72:75], v[224:227], v[100:103], v[72:75]\n\t"
    "v_xor_b32 %[da], 0x10000, %[da]\n\t"
    "v_add_f32 v61, v61, v93\n\t"
    "v_add_f32 v62, v62, v94\n\t"
    "v_mfma_f32_16x16x32_bf16 v[76:79], v[228:231], v[100:103], v[76:79]\n\t"
    "v_add_f32 v63, v63, v95\n\t"
    "s_waitcnt lgkmcnt(5)\n\t"
    "v_mfma_f32_16x16x32_bf16 v[64:67], v[232:235], v[104:107], v[64:67]\n\t"
    "ds_read_b128 v[96:99], %[ea] offset:0\n\t"
    "v_cvt_pk_bf16_f32 v40, v48, v49\n\t"
    "v_mfma_f32_16x16x32_bf16 v[68:71], v[236:239], v[104:107], v[68:71]\n\t"
    "ds_read_b128 v[100:103], %[ea] offset:2048\n\t"
    "v_cvt_pk_bf16_f32 v41, v50, v51\n\t"
    "v_cvt_pk_bf16_f32 v42, v52, v53\n\t"
    "v_mfma_f32_16x16x32_bf16 v[72:75], v[232:235], v[108:111], v[72:75]\n\t"
    "v_cvt_pk_bf16_f32 v43, v54, v55\n\t"
    "v_mfma_f32_16x16x32_bf16 v[76:79], v[236:239], v[108:111], v[76:79]\n\t"
    "global_store_dwordx4 %[co0], v[40:43], s[36:37] sc1\n\t"
    "s_waitcnt lgkmcnt(4)\n\t"
    "v_mfma_f32_16x16x32_bf16 v[64:67], v[240:243], v[112:115], v[64:67]\n\t"
    "ds_read_b128 v[104:107], %[da] offset:0\n\t"
    "v_mad_u32_u24 v39, v39, %[mu], %[ad]\n\t"
    "v_cvt_pk_bf16_f32 v44, v56, v57\n\t"
    "v_cvt_pk_bf16_f32 v45, v58, v59\n\t"
    "v_mfma_f32_16x16x32_bf16 v[68:71], v[244:247], v[112:115], v[68:71]\n\t"
    "ds_read_b128 v[108:111], %[da] offset:2048\n\t"
    "v_cmp_ge_i32 vcc, %[lf], v39\n\t"
    "s_cmp_eq_u64 vcc, 0\n\t"
    "s_addc_u32 %[lf], %[lf], 0\n\t"
    "v_cvt_pk_bf16_f32 v46, v60, v61\n\t"
    "v_mfma_f32_16x16x32_bf16 v[72:75], v[240:243], v[116:119], v[72:75]\n\t"
    "v_cvt_pk_bf16_f32 v47, v62, v63\n\t"
    "v_mfma_f32_16x16x32_bf16 v[76:79], v[244:247], v[116:119], v[76:79]\n\t"
    "v_cmp_ge_i32 vcc, %[lf], v39\n\t"
    "s_cmp_eq_u64 vcc, 0\n\t"
    "s_addc_u32 %[lf], %[lf], 0\n\t"
    "global_store_dwordx4 %[co1], v[44:47], s[36:37] sc1\n\t"
    "s_waitcnt lgkmcnt(4)\n\t"
    "v_mfma_f32_16x16x32_bf16 v[80:83], v[248:251], v[120:123], v[64:67]\n\t"
    "ds_read_b128 v[112:115], %[ea] offset:4096\n\t"
    "s_min_i32 %[lf], %[lf], %[cp]\n\t"
    "s_add_u32 %[b], %[b], 1\n\t"
    "v_mfma_f32_16x16x32_bf16 v[84:87], v[252:255], v[120:123], v[68:71]\n\t"
    "ds_read_b128 v[116:119], %[ea] offset:6144\n\t"
    "v_mfma_f32_16x16x32_bf16 v[88:91], v[248:251], v[124:127], v[72:75]\n\t"
    "v_mfma_f32_16x16x32_bf16 v[92:95], v[252:255], v[124:127], v[76:79]\n\t"
    "s_mov_b64 s[36:37], s[38:39]\n\t"
    "s_add_u32 s38, s38, %[ab]\n\t"
    "s_addc_u32 s39, s39, 0\n\t"
    "s_cmp_lt_i32 %[b], %[lf]\n\t"
    "s_cbranch_scc1 1b\n\t"
    "s_waitcnt lgkmcnt(0)\n\t"
    "s_nop 4\n\t"
    "s_branch 9f\n\t"
    "5:\n\t"
    "s_add_u32 %[st], %[b], %[po]\n\t"
    "s_max_i32 %[st], %[st], %[p0]\n\t"
    "v_mov_b32 v39, %[st]\n\t"
    "v_mov_b32 v38, 0\n\t"
    "s_mov_b64 exec, 1\n\t"
    "global_store_dword v38, v39, %[pg] sc1\n\t"
    "s_mov_b64 exec, -1\n\t"
    "s_mov_b32 %[p0], %[p1]\n\t"
    "s_xor_b32 %[sl], %[sl], 0x100\n\t"
    "s_cmp_ge_i32 %[lf], %[cp]\n\t"
    "s_cbranch_scc1 6b\n\t"
    "s_mov_b32 m0, %[sl]\n\t"
    "s_nop 0\n\t"
    "global_load_lds_dword %[dv], %[db] sc1\n\t"
    "s_branch 6b\n\t"
    "9:"
    : "=&{v[80:95]}"(pd), [ea] "+&v"(ea), [da] "+&v"(da), [b] "+&s"(b), [lf] "+&s"(lf), "+{s[36:37]}"(p1), "+{s[38:39]}"(p0), [sa] "+&v"(sa), [st] "=&s"(st), [sl] "+&s"(sl), [p0] "+&s"(pf)
    : [rw] "v"(rw), [ab] "s"(ab), [cp] "s"(cp), [mu] "s"(mu), [ad] "v"(ad), "{v[128:159]}"(W[0]), "{v[160:191]}"(W[1]), "{v[192:223]}"(W[2]), "{v[224:255]}"(W[3]), [co0] "v"(co0), [w0] "s"(w0), [dv] "v"(dv), [db] "s"(db), [pg] "s"(pg), [po] "s"(po - 1), [p1] "s"(b + po), [co1] "v"(co1)
    : "v38", "v39", "v40", "v41", "v42", "v43", "v44", "v45", "v46", "v47", "v48", "v49", "v50", "v51", "v52", "v53", "v54", "v55", "v56", "v57", "v58", "v59", "v60", "v61", "v62", "v63", "v64", "v65", "v66", "v67", "v68", "v69", "v70", "v71", "v72", "v73", "v74", "v75", "v76", "v77", "v78", "v79", "v96", "v97", "v98", "v99", "v100", "v101", "v102", "v103", "v104", "v105", "v106", "v107", "v108", "v109", "v110", "v111", "v112", "v113", "v114", "v115", "v116", "v117", "v118", "v119", "v120", "v121", "v122", "v123", "v124", "v125", "v126", "v127", "vcc", "scc", "m0", "memory");
}
__device__ __forceinline__ void fast_st1(const u32x32 (&W)[4], u32x16& pd, unsigned ea, unsigned da, int& b, int& lf, int cp, unsigned sa, unsigned mu, unsigned ad,
    unsigned rw, unsigned ab, const char* p1, const char* p0, unsigned co0, unsigned co1, int po, int pf, unsigned w0, unsigned sl, unsigned dv, const char* db, const char* pg) {
  unsigned st;
  asm volatile("s_barrier\n\t"
    "ds_read_b128 v[96:99], %[ea] offset:0\n\t"
    "ds_read_b128 v[100:103], %[ea] offset:2048\n\t"
    "ds_read_b128 v[104:107], %[da] offset:0\n\t"
    "ds_read_b128 v[108:111], %[da] offset:2048\n\t"
    "ds_read_b128 v[112:115], %[ea] offset:4096\n\t"
    "ds_read_b128 v[116:119], %[ea] offset:6144\n\t"
    "s_branch 2f\n\t"
    "1:\n\t"
    "s_barrier\n\t"
    "2:\n\t"
    "s_waitcnt lgkmcnt(4)\n\t"
    "v_mfma_f32_16x16x32_bf16 v[64:67], v[128:131], v[96:99], 0\n\t"
    "ds_read_b128 v[120:123], %[da] offset:4096\n\t"
    "v_mfma_f32_16x16x32_bf16 v[68:71], v[132:135], v[96:99], 0\n\t"
    "ds_read_b128 v[124:127], %[da] offset:6144\n\t"
    "v_mfma_f32_16x16x32_bf16 v[72:75], v[128:131], v[100:103], 0\n\t"
    "v_mfma_f32_16x16x32_bf16 v[76:79], v[132:135], v[100:103], 0\n\t"
    "s_waitcnt lgkmcnt(4)\n\t"
    "v_mfma_f32_16x16x32_bf16 v[64:67], v[136:139], v[104:107], v[64:67]\n\t"
    "ds_read_b128 v[96:99], %[ea] offset:8192\n\t"
    "v_mfma_f32_16x16x32_bf16 v[68:71], v[140:143], v[104:107], v[68:71]\n\t"
    "ds_read_b128 v[100:103], %[ea] offset:10240\n\t"
    "v_mfma_f32_16x16x32_bf16 v[72:75], v[136:139], v[108:111], v[72:75]\n\t"
    "v_mfma_f32_16x16x32_bf16 v[76:79], v[140:143], v[108:111], v[76:79]\n\t"
    "s_waitcnt lgkmcnt(4)\n\t"
    "v_mfma_f32_16x16x32_bf16 v[64:67], v[144:147], v[112:115], v[64:67]\n\t"
    "ds_read_b128 v[104:107], %[da] offset:8192\n\t"
    "v_mfma_f32_16x16x32_bf16 v[68:71], v[148:151], v[112:115], v[68:71]\n\t"
    "ds_read_b128 v[108:111], %[da] offset:10240\n\t"
    "v_mfma_f32_16x16x32_bf16 v[72:75], v[144:147], v[116:119], v[72:75]\n\t"
    "v_mfma_f32_16x16x32_bf16 v[76:79], v[148:151], v[116:119], v[76:79]\n\t"
    "s_waitcnt lgkmcnt(4)\n\t"
    "v_mfma_f32_16x16x32_bf16 v[64:67], v[152:155], v[120:123], v[64:67]\n\t"
    "ds_read_b128 v[112:115], %[ea] offset:12288\n\t"
    "v_mfma_f32_16x16x32_bf16 v[68:71], v[156:159], v[120:123], v[68:71]\n\t"
    "ds_read_b128 v[116:119], %[ea] offset:14336\n\t"
    "v_mfma_f32_16x16x32_bf16 v[72:75], v[152:155], v[124:127], v[72:75]\n\t"
    "v_mfma_f32_16x16x32_bf16 v[76:79], v[156:159], v[124:127], v[76:79]\n\t"
    "s_waitcnt lgkmcnt(4)\n\t"
    "v_mfma_f32_16x16x32_bf16 v[64:67], v[160:163], v[96:99], v[64:67]\n\t"
    "ds_read_b128 v[120:123], %[da] offset:12288\n\t"
    "v_mfma_f32_16x16x32_bf16 v[68:71], v[164:167], v[96:99], v[68:71]\n\t"
    "ds_read_b128 v[124:127], %[da] offset:14336\n\t"
    "v_mfma_f32_16x16x32_bf16 v[72:75], v[160:163], v[100:103], v[72:75]\n\t"
    "v_mfma_f32_16x16x32_bf16 v[76:79], v[164:167], v[100:103], v[76:79]\n\t"
    "s_waitcnt lgkmcnt(4)\n\t"
    "v_mfma_f32_16x16x32_bf16 v[64:67], v[168:171], v[104:107], v[64:67]\n\t"
    "ds_read_b128 v[96:99], %[ea] offset:16384\n\t"
    "v_mfma_f32_16x16x32_bf16 v[68:71], v[172:175], v[104:107], v[68:71]\n\t"
    "ds_read_b128 v[100:103], %[ea] offset:18432\n\t"
    "v_mfma_f32_16x16x32_bf16 v[72:75], v[168:171], v[108:111], v[72:75]\n\t"
    "v_mfma_f32_16x16x32_bf16 v[76:79], v[172:175], v[108:111], v[76:79]\n\t"
    "s_waitcnt lgkmcnt(4)\n\t"
    "v_mfma_f32_16x16x32_bf16 v[64:67], v[176:179], v[112:115], v[64:67]\n\t"
    "ds_read_b128 v[104:107], %[da] offset:16384\n\t"
    "v_mfma_f32_16x16x32_bf16 v[68:71], v[180:183], v[112:115], v[68:71]\n\t"
    "ds_read_b128 v[108:111], %[da] offset:18432\n\t"
    "v_mfma_f32_16x16x32_bf16 v[72:75], v[176:179], v[116:119], v[72:75]\n\t"
    "v_mfma_f32_16x16x32_bf16 v[76:79], v[180:183], v[116:119], v[76:79]\n\t"
    "s_waitcnt lgkmcnt(4)\n\t"
    "v_mfma_f32_16x16x32_bf16 v[64:67], v[184:187], v[120:123], v[64:67]\n\t"
    "ds_read_b128 v[112:115], %[ea] offset:20480\n\t"
    "v_mfma_f32_16x16x32_bf16 v[68:71], v[188:191], v[120:123], v[68:71]\n\t"
    "ds_read_b128 v[116:119], %[ea] offset:22528\n\t"
    "v_mfma_f32_16x16x32_bf16 v[72:75], v[184:187], v[124:127], v[72:75]\n\t"
    "v_mfma_f32_16x16x32_bf16 v[76:79], v[188:191], v[124:127], v[76:79]\n\t"
    "s_waitcnt vmcnt(0)\n\t"
    "s_barrier\n\t"
    "s_waitcnt lgkmcnt(4)\n\t"
    "v_mfma_f32_16x16x32_bf16 v[64:67], v[192:195], v[96:99], v[64:67]\n\t"
    "ds_read_b128 v[120:123], %[da] offset:20480\n\t"
    "v_mfma_f32_16x16x32_bf16 v[68:71], v[196:199], v[96:99], v[68:71]\n\t"
    "ds_read_b128 v[124:127], %[da] offset:22528\n\t"
    "v_mfma_f32_16x16x32_bf16 v[72:75], v[192:195], v[100:103], v[72:75]\n\t"
    "ds_read_b128 v[48:51], %[rw] offset:0\n\t"
    "v_mfma_f32_16x16x32_bf16 v[76:79], v[196:199], v[100:103], v[76:79]\n\t"
    "ds_read_b128 v[52:55], %[rw] offset:1024\n\t"
    "s_cmp_eq_u32 %[w0], 0\n\t"
    "s_cbranch_scc1 5f\n\t"
    "6:\n\t"
    "s_waitcnt lgkmcnt(6)\n\t"
    "v_mfma_f32_16x16x32_bf16 v[64:67], v[200:203], v[104:107], v[64:67]\n\t"
    "ds_read_b128 v[96:99], %[ea] offset:24576\n\t"
    "v_mfma_f32_16x16x32_bf16 v[68:71], v[204:207], v[104:107], v[68:71]\n\t"
    "ds_read_b128 v[100:103], %[ea] offset:26624\n\t"
    "v_mfma_f32_16x16x32_bf16 v[72:75], v[200:203], v[108:111], v[72:75]\n\t"
    "ds_read_b128 v[56:59], %[rw] offset:2048\n\t"
    "v_mfma_f32_16x16x32_bf16 v[76:79], v[204:207], v[108:111], v[76:79]\n\t"
    "ds_read_b128 v[60:63], %[rw] offset:3072\n\t"
    "s_waitcnt lgkmcnt(0)\n\t"
    "v_mfma_f32_16x16x32_bf16 v[64:67], v[208:211], v[112:115], v[64:67]\n\t"
    "ds_read_b128 v[104:107], %[da] offset:24576\n\t"
    "v_add_f32 v48, v48, v80\n\t"
    "v_add_f32 v49, v49, v81\n\t"
    "v_add_f32 v50, v50, v82\n\t"
    "v_mfma_f32_16x16x32_bf16 v[68:71], v[212:215], v[112:115], v[68:71]\n\t"
    "ds_read_b128 v[108:111], %[da] offset:26624\n\t"
    "v_add_f32 v51, v51, v83\n\t"
    "v_add_f32 v52, v52, v84\n\t"
    "v_add_f32 v53, v53, v85\n\t"
    "v_mfma_f32_16x16x32_bf16 v[72:75], v[208:211], v[116:119], v[72:75]\n\t"
    "ds_read_b32 v39, %[sa]\n\t"
    "v_add_f32 v54, v54, v86\n\t"
    "v_add_f32 v55, v55, v87\n\t"
    "v_add_f32 v56, v56, v88\n\t"
    "v_mfma_f32_16x16x32_bf16 v[76:79], v[212:215], v[116:119], v[76:79]\n\t"
    "v_xor_b32 %[sa], 0x100, %[sa]\n\t"
    "v_add_f32 v57, v57, v89\n\t"
    "v_add_f32 v58, v58, v90\n\t"
    "v_add_f32 v59, v59, v91\n\t"
    "v_mfma_f32_16x16x32_bf16 v[64:67], v[216:219], v[120:123], v[64:67]\n\t"
    "ds_read_b128 v[112:115], %[ea] offset:28672\n\t"
    "v_add_f32 v60, v60, v92\n\t"
    "v_add_f32 v61, v61, v93\n\t"
    "v_add_f32 v62, v62, v94\n\t"
    "v_mfma_f32_16x16x32_bf16 v[68:71], v[220:223], v[120:123], v[68:71]\n\t"
    "ds_read_b128 v[116:119], %[ea] offset:30720\n\t"
    "v_add_f32 v63, v63, v95\n\t"
    "v_mul_f32 v40, %[nl], v48\n\t"
    "v_mul_f32 v41, %[nl], v49\n\t"
    "v_mfma_f32_16x16x32_bf16 v[72:75], v[216:219], v[124:127], v[72:75]\n\t"
    "v_xor_b32 %[ea], 0x10000, %[ea]\n\t"
    "v_mul_f32 v42, %[nl], v50\n\t"
    "v_mul_f32 v43, %[nl], v51\n\t"
    "v_mul_f32 v44, %[nl], v56\n\t"
    "v_mfma_f32_16x16x32_bf16 v[76:79], v[220:223], v[124:127], v[76:79]\n\t"
    "v_mul_f32 v45, %[nl], v57\n\t"
    "v_mul_f32 v46, %[nl], v58\n\t"
    "v_mul_f32 v47, %[nl], v59\n\t"
    "v_mfma_f32_16x16x32_bf16 v[64:67], v[224:227], v[96:99], v[64:67]\n\t"
    "ds_read_b128 v[120:123], %[da] offset:28672\n\t"
    "v_exp_f32 v40, v40\n\t"
    "v_exp_f32 v41, v41\n\t"
    "v_exp_f32 v42, v42\n\t"
    "v_mfma_f32_16x16x32_bf16 v[68:71], v[228:231], v[96:99], v[68:71]\n\t"
    "ds_read_b128 v[124:127], %[da] offset:30720\n\t"
    "v_exp_f32 v43, v43\n\t"
    "v_exp_f32 v44, v44\n\t"
    "v_exp_f32 v45, v45\n\t"
    "v_mfma_f32_16x16x32_bf16 v[72:75], v[224:227], v[100:103], v[72:75]\n\t"
    "v_xor_b32 %[da], 0x10000, %[da]\n\t"
    "v_exp_f32 v46, v46\n\t"
    "v_exp_f32 v47, v47\n\t"
    "v_add_f32 v40, 1.0, v40\n\t"
    "v_mfma_f32_16x16x32_bf16 v[76:79], v[228:231], v[100:103], v[76:79]\n\t"
    "v_add_f32 v41, 1.0, v41\n\t"
    "v_add_f32 v42, 1.0, v42\n\t"
    "v_add_f32 v43, 1.0, v43\n\t"
    "s_waitcnt lgkmcnt(5)\n\t"
    "v_mfma_f32_16x16x32_bf16 v[64:67], v[232:235], v[104:107], v[64:67]\n\t"
    "ds_read_b128 v[96:99], %[ea] offset:0\n\t"
    "v_add_f32 v44, 1.0, v44\n\t"
    "v_add_f32 v45, 1.0, v45\n\t"
    "v_add_f32 v46, 1.0, v46\n\t"
    "v_mfma_f32_16x16x32_bf16 v[68:71], v[236:239], v[104:107], v[68:71]\n\t"
    "ds_read_b128 v[100:103], %[ea] offset:2048\n\t"
    "v_add_f32 v47, 1.0, v47\n\t"
    "v_rcp_f32 v40, v40\n\t"
    "v_rcp_f32 v41, v41\n\t"
    "v_mfma_f32_16x16x32_bf16 v[72:75], v[232:235], v[108:111], v[72:75]\n\t"
    "v_rcp_f32 v42, v42\n\t"
    "v_rcp_f32 v43, v43\n\t"
    "v_rcp_f32 v44, v44\n\t"
    "v_mfma_f32_16x16x32_bf16 v[76:79], v[236:239], v[108:111], v[76:79]\n\t"
    "v_rcp_f32 v45, v45\n\t"
    "v_rcp_f32 v46, v46\n\t"
    "v_rcp_f32 v47, v47\n\t"
    "s_waitcnt lgkmcnt(4)\n\t"
    "v_mfma_f32_16x16x32_bf16 v[64:67], v[240:243], v[112:115], v[64:67]\n\t"
    "ds_read_b128 v[104:107], %[da] offset:0\n\t"
    "v_mad_u32_u24 v39, v39, %[mu], %[ad]\n\t"
    "v_mul_f32 v40, v48, v40\n\t"
    "v_mul_f32 v41, v49, v41\n\t"
    "v_mul_f32 v42, v50, v42\n\t"
    "v_mfma_f32_16x16x32_bf16 v[68:71], v[244:247], v[112:115], v[68:71]\n\t"
    "ds_read_b128 v[108:111], %[da] offset:2048\n\t"
    "v_cmp_ge_i32 vcc, %[lf], v39\n\t"
    "s_cmp_eq_u64 vcc, 0\n\t"
    "s_addc_u32 %[lf], %[lf], 0\n\t"
    "v_mul_f32 v43, v51, v43\n\t"
    "v_mul_f32 v44, v56, v44\n\t"
    "v_mul_f32 v45, v57, v45\n\t"
    "v_mfma_f32_16x16x32_bf16 v[72:75], v[240:243], v[116:119], v[72:75]\n\t"
    "v_mul_f32 v46, v58, v46\n\t"
    "v_mul_f32 v47, v59, v47\n\t"
    "v_mul_f32 v40, v40, v52\n\t"
    "v_mfma_f32_16x16x32_bf16 v[76:79], v[244:247], v[116:119], v[76:79]\n\t"
    "v_cmp_ge_i32 vcc, %[lf], v39\n\t"
    "s_cmp_eq_u64 vcc, 0\n\t"
    "s_addc_u32 %[lf], %[lf], 0\n\t"
    "v_mul_f32 v41, v41, v53\n\t"
    "v_mul_f32 v42, v42, v54\n\t"
    "v_mul_f32 v43, v43, v55\n\t"
    "s_waitcnt lgkmcnt(4)\n\t"
    "v_mfma_f32_16x16x32_bf16 v[80:83], v[248:251], v[120:123], v[64:67]\n\t"
    "ds_read_b128 v[112:115], %[ea] offset:4096\n\t"
    "s_min_i32 %[lf], %[lf], %[cp]\n\t"
    "s_add_u32 %[b], %[b], 1\n\t"
    "v_mul_f32 v44, v44, v60\n\t"
    "v_mul_f32 v45, v45, v61\n\t"
    "v_mul_f32 v46, v46, v62\n\t"
    "v_mfma_f32_16x16x32_bf16 v[84:87], v[252:255], v[120:123], v[68:71]\n\t"
    "ds_read_b128 v[116:119], %[ea] offset:6144\n\t"
    "v_mul_f32 v47, v47, v63\n\t"
    "v_cvt_pk_bf16_f32 v48, v40, v41\n\t"
    "v_cvt_pk_bf16_f32 v49, v42, v43\n\t"
    "v_mfma_f32_16x16x32_bf16 v[88:91], v[248:251], v[124:127], v[72:75]\n\t"
    "v_cvt_pk_bf16_f32 v50, v44, v45\n\t"
    "v_cvt_pk_bf16_f32 v51, v46, v47\n\t"
    "s_nop 1\n\t"
    "v_mfma_f32_16x16x32_bf16 v[92:95], v[252:255], v[124:127], v[76:79]\n\t"
    "v_permlane16_swap_b32 v48, v50\n\t"
    "v_permlane16_swap_b32 v49, v51\n\t"
    "global_store_dwordx4 %[co0], v[48:51], s[36:37] sc1\n\t"
    "s_mov_b64 s[36:37], s[38:39]\n\t"
    "s_add_u32 s38, s38, %[ab]\n\t"
    "s_addc_u32 s39, s39, 0\n\t"
    "s_cmp_lt_i32 %[b], %[lf]\n\t"
    "s_cbranch_scc1 1b\n\t"
    "s_waitcnt lgkmcnt(0)\n\t"
    "s_nop 4\n\t"
    "s_branch 9f\n\t"
    "5:\n\t"
    "s_add_u32 %[st], %[b], %[po]\n\t"
    "s_max_i32 %[st], %[st], %[p0]\n\t"
    "v_mov_b32 v39, %[st]\n\t"
    "v_mov_b32 v38, 0\n\t"
    "s_mov_b64 exec, 1\n\t"
    "global_store_dword v38, v39, %[pg] sc1\n\t"
    "s_mov_b64 exec, -1\n\t"
    "s_mov_b32 %[p0], %[p1]\n\t"
    "s_xor_b32 %[sl], %[sl], 0x100\n\t"
    "s_cmp_ge_i32 %[lf], %[cp]\n\t"
    "s_cbranch_scc1 6b\n\t"
    "s_mov_b32 m0, %[sl]\n\t"
    "s_nop 0\n\t"
    "global_load_lds_dword %[dv], %[db] sc1\n\t"
    "s_branch 6b\n\t"
    "9:"
    : "=&{v[80:95]}"(pd), [ea] "+&v"(ea), [da] "+&v"(da), [b] "+&s"(b), [lf] "+&s"(lf), "+{s[36:37]}"(p1), "+{s[38:39]}"(p0), [sa] "+&v"(sa), [st] "=&s"(st), [sl] "+&s"(sl), [p0] "+&s"(pf)
    : [rw] "v"(rw), [ab] "s"(ab), [cp] "s"(cp), [mu] "s"(mu), [ad] "v"(ad), "{v[128:159]}"(W[0]), "{v[160:191]}"(W[1]), "{v[192:223]}"(W[2]), "{v[224:255]}"(W[3]), [co0] "v"(co0), [w0] "s"(w0), [dv] "v"(dv), [db] "s"(db), [pg] "s"(pg), [po] "s"(po - 1), [p1] "s"(b + po), [nl] "s"(-LOG2E)
    : "v38", "v39", "v40", "v41", "v42", "v43", "v44", "v45", "v46", "v47", "v48", "v49", "v50", "v51", "v52", "v53", "v54", "v55", "v56", "v57", "v58", "v59", "v60", "v61", "v62", "v63", "v64", "v65", "v66", "v67", "v68", "v69", "v70", "v71", "v72", "v73", "v74", "v75", "v76", "v77", "v78", "v79", "v96", "v97", "v98", "v99", "v100", "v101", "v102", "v103", "v104", "v105", "v106", "v107", "v108", "v109", "v110", "v111", "v112", "v113", "v114", "v115", "v116", "v117", "v118", "v119", "v120", "v121", "v122", "v123", "v124", "v125", "v126", "v127", "vcc", "scc", "m0", "memory");
}
__device__ __forceinline__ void fast_st2(const u32x32 (&W)[4], u32x16& pd, unsigned ea, unsigned da, int& b, int& lf, int cp, unsigned sa, unsigned mu, unsigned ad,
    unsigned rw, unsigned ab, const char* p1, const char* p0, unsigned co0, unsigned co1, int po, int pf, unsigned w0, unsigned sl, unsigned dv, const char* db, const char* pg) {
  unsigned st;
  asm volatile("s_barrier\n\t"
    "ds_read_b128 v[96:99], %[ea] offset:0\n\t"
    "ds_read_b128 v[100:103], %[ea] offset:2048\n\t"
    "ds_read_b128 v[104:107], %[da] offset:0\n\t"
    "ds_read_b128 v[108:111], %[da] offset:2048\n\t"
    "ds_read_b128 v[112:115], %[ea] offset:4096\n\t"
    "ds_read_b128 v[116:119], %[ea] offset:6144\n\t"
    "s_branch 2f\n\t"
    "1:\n\t"
    "s_barrier\n\t"
    "2:\n\t"
    "s_waitcnt lgkmcnt(4)\n\t"
    "v_mfma_f32_16x16x32_bf16 v[64:67], v[128:131], v[96:99], 0\n\t"
    "ds_read_b128 v[120:123], %[da] offset:4096\n\t"
    "v_mfma_f32_16x16x32_bf16 v[68:71], v[132:135], v[96:99], 0\n\t"
    "ds_read_b128 v[124:127], %[da] offset:6144\n\t"
    "v_mfma_f32_16x16x32_bf16 v[72:75], v[128:131], v[100:103], 0\n\t"
    "v_mfma_f32_16x16x32_bf16 v[76:79], v[132:135], v[100:103], 0\n\t"
    "s_waitcnt lgkmcnt(4)\n\t"
    "v_mfma_f32_16x16x32_bf16 v[64:67], v[136:139], v[104:107], v[64:67]\n\t"
    "ds_read_b128 v[96:99], %[ea] offset:8192\n\t"
    "v_mfma_f32_16x16x32_bf16 v[68:71], v[140:143], v[104:107], v[68:71]\n\t"
    "ds_read_b128 v[100:103], %[ea] offset:10240\n\t"
    "v_mfma_f32_16x16x32_bf16 v[72:75], v[136:139], v[108:111], v[72:75]\n\t"
    "v_mfma_f32_16x16x32_bf16 v[76:79], v[140:143], v[108:111], v[76:79]\n\t"
    "s_waitcnt lgkmcnt(4)\n\t"
    "v_mfma_f32_16x16x32_bf16 v[64:67], v[144:147], v[112:115], v[64:67]\n\t"
    "ds_read_b128 v[104:107], %[da] offset:8192\n\t"
    "v_mfma_f32_16x16x32_bf16 v[68:71], v[148:151], v[112:115], v[68:71]\n\t"
    "ds_read_b128 v[108:111], %[da] offset:10240\n\t"
    "v_mfma_f32_16x16x32_bf16 v[72:75], v[144:147], v[116:119], v[72:75]\n\t"
    "v_mfma_f32_16x16x32_bf16 v[76:79], v[148:151], v[116:119], v[76:79]\n\t"
    "s_waitcnt lgkmcnt(4)\n\t"
    "v_mfma_f32_16x16x32_bf16 v[64:67], v[152:155], v[120:123], v[64:67]\n\t"
    "ds_read_b128 v[112:115], %[ea] offset:12288\n\t"
    "v_mfma_f32_16x16x32_bf16 v[68:71], v[156:159], v[120:123], v[68:71]\n\t"
    "ds_read_b128 v[116:119], %[ea] offset:14336\n\t"
    "v_mfma_f32_16x16x32_bf16 v[72:75], v[152:155], v[124:127], v[72:75]\n\t"
    "v_mfma_f32_16x16x32_bf16 v[76:79], v[156:159], v[124:127], v[76:79]\n\t"
    "s_waitcnt lgkmcnt(4)\n\t"
    "v_mfma_f32_16x16x32_bf16 v[64:67], v[160:163], v[96:99], v[64:67]\n\t"
    "ds_read_b128 v[120:123], %[da] offset:12288\n\t"
    "v_mfma_f32_16x16x32_bf16 v[68:71], v[164:167], v[96:99], v[68:71]\n\t"
    "ds_read_b128 v[124:127], %[da] offset:14336\n\t"
    "v_mfma_f32_16x16x32_bf16 v[72:75], v[160:163], v[100:103], v[72:75]\n\t"
    "v_mfma_f32_16x16x32_bf16 v[76:79], v[164:167], v[100:103], v[76:79]\n\t"
    "s_waitcnt lgkmcnt(4)\n\t"
    "v_mfma_f32_16x16x32_bf16 v[64:67], v[168:171], v[104:107], v[64:67]\n\t"
    "ds_read_b128 v[96:99], %[ea] offset:16384\n\t"
    "v_mfma_f32_16x16x32_bf16 v[68:71], v[172:175], v[104:107], v[68:71]\n\t"
    "ds_read_b128 v[100:103], %[ea] offset:18432\n\t"
    "v_mfma_f32_16x16x32_bf16 v[72:75], v[168:171], v[108:111], v[72:75]\n\t"
    "v_mfma_f32_16x16x32_bf16 v[76:79], v[172:175], v[108:111], v[76:79]\n\t"
    "s_waitcnt lgkmcnt(4)\n\t"
    "v_mfma_f32_16x16x32_bf16 v[64:67], v[176:179], v[112:115], v[64:67]\n\t"
    "ds_read_b128 v[104:107], %[da] offset:16384\n\t"
    "v_mfma_f32_16x16x32_bf16 v[68:71], v[180:183], v[112:115], v[68:71]\n\t"
    "ds_read_b128 v[108:111], %[da] offset:18432\n\t"
    "v_mfma_f32_16x16x32_bf16 v[72:75], v[176:179], v[116:119], v[72:75]\n\t"
    "v_mfma_f32_16x16x32_bf16 v[76:79], v[180:183], v[116:119], v[76:79]\n\t"
    "s_waitcnt lgkmcnt(4)\n\t"
    "v_mfma_f32_16x16x32_bf16 v[64:67], v[184:187], v[120:123], v[64:67]\n\t"
    "ds_read_b128 v[112:115], %[ea] offset:20480\n\t"
    "v_mfma_f32_16x16x32_bf16 v[68:71], v[188:191], v[120:123], v[68:71]\n\t"
    "ds_read_b128 v[116:119], %[ea] offset:22528\n\t"
    "v_mfma_f32_16x16x32_bf16 v[72:75], v[184:187], v[124:127], v[72:75]\n\t"
    "v_mfma_f32_16x16x32_bf16 v[76:79], v[188:191], v[124:127], v[76:79]\n\t"
    "s_waitcnt vmcnt(0)\n\t"
    "s_barrier\n\t"
    "s_waitcnt lgkmcnt(4)\n\t"
    "v_mfma_f32_16x16x32_bf16 v[64:67], v[192:195], v[96:99], v[64:67]\n\t"
    "ds_read_b128 v[120:123], %[da] offset:20480\n\t"
    "v_mfma_f32_16x16x32_bf16 v[68:71], v[196:199], v[96:99], v[68:71]\n\t"
    "ds_read_b128 v[124:127], %[da] offset:22528\n\t"
    "v_mfma_f32_16x16x32_bf16 v[72:75], v[192:195], v[100:103], v[72:75]\n\t"
    "ds_read_b128 v[48:51], %[rw] offset:0\n\t"
    "v_mfma_f32_16x16x32_bf16 v[76:79], v[196:199], v[100:103], v[76:79]\n\t"
    "ds_read_b128 v[52:55], %[rw] offset:1024\n\t"
    "s_cmp_eq_u32 %[w0], 0\n\t"
    "s_cbranch_scc1 5f\n\t"
    "6:\n\t"
    "s_waitcnt lgkmcnt(6)\n\t"
    "v_mfma_f32_16x16x32_bf16 v[64:67], v[200:203], v[104:107], v[64:67]\n\t"
    "ds_read_b128 v[96:99], %[ea] offset:24576\n\t"
    "v_mfma_f32_16x16x32_bf16 v[68:71], v[204:207], v[104:107], v[68:71]\n\t"
    "ds_read_b128 v[100:103], %[ea] offset:26624\n\t"
    "v_mfma_f32_16x16x32_bf16 v[72:75], v[200:203], v[108:111], v[72:75]\n\t"
    "ds_read_b128 v[56:59], %[rw] offset:2048\n\t"
    "v_mfma_f32_16x16x32_bf16 v[76:79], v[204:207], v[108:111], v[76:79]\n\t"
    "ds_read_b128 v[60:63], %[rw] offset:3072\n\t"
    "s_waitcnt lgkmcnt(0)\n\t"
    "v_mfma_f32_16x16x32_bf16 v[64:67], v[208:211], v[112:115], v[64:67]\n\t"
    "ds_read_b128 v[104:107], %[da] offset:24576\n\t"
    "v_cvt_pk_bf16_f32 v40, v80, v81\n\t"
    "v_mfma_f32_16x16x32_bf16 v[68:71], v[212:215], v[112:115], v[68:71]\n\t"
    "ds_read_b128 v[108:111], %[da] offset:26624\n\t"
    "v_cvt_pk_bf16_f32 v41, v82, v83\n\t"
    "v_mfma_f32_16x16x32_bf16 v[72:75], v[208:211], v[116:119], v[72:75]\n\t"
    "ds_read_b32 v39, %[sa]\n\t"
    "v_cvt_pk_bf16_f32 v42, v84, v85\n\t"
    "v_mfma_f32_16x16x32_bf16 v[76:79], v[212:215], v[116:119], v[76:79]\n\t"
    "v_xor_b32 %[sa], 0x100, %[sa]\n\t"
    "v_cvt_pk_bf16_f32 v43, v86, v87\n\t"
    "v_mfma_f32_16x16x32_bf16 v[64:67], v[216:219], v[120:123], v[64:67]\n\t"
    "ds_read_b128 v[112:115], %[ea] offset:28672\n\t"
    "global_store_dwordx4 %[co0], v[40:43], s[36:37] sc1\n\t"
    "v_mfma_f32_16x16x32_bf16 v[68:71], v[220:223], v[120:123], v[68:71]\n\t"
    "ds_read_b128 v[116:119], %[ea] offset:30720\n\t"
    "v_cvt_pk_bf16_f32 v44, v88, v89\n\t"
    "v_mfma_f32_16x16x32_bf16 v[72:75], v[216:219], v[124:127], v[72:75]\n\t"
    "v_xor_b32 %[ea], 0x10000, %[ea]\n\t"
    "v_cvt_pk_bf16_f32 v45, v90, v91\n\t"
    "v_mfma_f32_16x16x32_bf16 v[76:79], v[220:223], v[124:127], v[76:79]\n\t"
    "v_cvt_pk_bf16_f32 v46, v92, v93\n\t"
    "v_mfma_f32_16x16x32_bf16 v[64:67], v[224:227], v[96:99], v[64:67]\n\t"
    "ds_read_b128 v[120:123], %[da] offset:28672\n\t"
    "v_cvt_pk_bf16_f32 v47, v94, v95\n\t"
    "v_mfma_f32_16x16x32_bf16 v[68:71], v[228:231], v[96:99], v[68:71]\n\t"
    "ds_read_b128 v[124:127], %[da] offset:30720\n\t"
    "global_store_dwordx4 %[co1], v[44:47], s[36:37] sc1\n\t"
    "v_mfma_f32_16x16x32_bf16 v[72:75], v[224:227], v[100:103], v[72:75]\n\t"
    "v_xor_b32 %[da], 0x10000, %[da]\n\t"
    "v_cvt_pk_bf16_f32 v48, v48, v49\n\t"
    "v_mfma_f32_16x16x32_bf16 v[76:79], v[228:231], v[100:103], v[76:79]\n\t"
    "v_cvt_pk_bf16_f32 v49, v50, v51\n\t"
    "s_waitcnt lgkmcnt(5)\n\t"
    "v_mfma_f32_16x16x32_bf16 v[64:67], v[232:235], v[104:107], v[64:67]\n\t"
    "ds_read_b128 v[96:99], %[ea] offset:0\n\t"
    "v_cvt_pk_bf16_f32 v50, v52, v53\n\t"
    "v_mfma_f32_16x16x32_bf16 v[68:71], v[236:239], v[104:107], v[68:71]\n\t"
    "ds_read_b128 v[100:103], %[ea] offset:2048\n\t"
    "v_cvt_pk_bf16_f32 v51, v54, v55\n\t"
    "v_mfma_f32_16x16x32_bf16 v[72:75], v[232:235], v[108:111], v[72:75]\n\t"
    "global_store_dwordx4 %[co0], v[48:51], s[36:37] offset:2048 sc1\n\t"
    "v_mfma_f32_16x16x32_bf16 v[76:79], v[236:239], v[108:111], v[76:79]\n\t"
    "v_cvt_pk_bf16_f32 v56, v56, v57\n\t"
    "s_waitcnt lgkmcnt(4)\n\t"
    "v_mfma_f32_16x16x32_bf16 v[64:67], v[240:243], v[112:115], v[64:67]\n\t"
    "ds_read_b128 v[104:107], %[da] offset:0\n\t"
    "v_mad_u32_u24 v39, v39, %[mu], %[ad]\n\t"
    "v_cvt_pk_bf16_f32 v57, v58, v59\n\t"
    "v_mfma_f32_16x16x32_bf16 v[68:71], v[244:247], v[112:115], v[68:71]\n\t"
    "ds_read_b128 v[108:111], %[da] offset:2048\n\t"
    "v_cmp_ge_i32 vcc, %[lf], v39\n\t"
    "s_cmp_eq_u64 vcc, 0\n\t"
    "s_addc_u32 %[lf], %[lf], 0\n\t"
    "v_cvt_pk_bf16_f32 v58, v60, v61\n\t"
    "v_mfma_f32_16x16x32_bf16 v[72:75], v[240:243], v[116:119], v[72:75]\n\t"
    "v_cvt_pk_bf16_f32 v59, v62, v63\n\t"
    "v_mfma_f32_16x16x32_bf16 v[76:79], v[244:247], v[116:119], v[76:79]\n\t"
    "v_cmp_ge_i32 vcc, %[lf], v39\n\t"
    "s_cmp_eq_u64 vcc, 0\n\t"
    "s_addc_u32 %[lf], %[lf], 0\n\t"
    "global_store_dwordx4 %[co1], v[56:59], s[36:37] offset:2048 sc1\n\t"
    "s_waitcnt lgkmcnt(4)\n\t"
    "v_mfma_f32_16x16x32_bf16 v[80:83], v[248:251], v[120:123], v[64:67]\n\t"
    "ds_read_b128 v[112:115], %[ea] offset:4096\n\t"
    "s_min_i32 %[lf], %[lf], %[cp]\n\t"
    "s_add_u32 %[b], %[b], 1\n\t"
    "v_mfma_f32_16x16x32_bf16 v[84:87], v[252:255], v[120:123], v[68:71]\n\t"
    "ds_read_b128 v[116:119], %[ea] offset:6144\n\t"
    "v_mfma_f32_16x16x32_bf16 v[88:91], v[248:251], v[124:127], v[72:75]\n\t"
    "v_mfma_f32_16x16x32_bf16 v[92:95], v[252:255], v[124:127], v[76:79]\n\t"
    "s_mov_b64 s[36:37], s[38:39]\n\t"
    "s_add_u32 s38, s38, %[ab]\n\t"
    "s_addc_u32 s39, s39, 0\n\t"
    "s_cmp_lt_i32 %[b], %[lf]\n\t"
    "s_cbranch_scc1 1b\n\t"
    "s_waitcnt lgkmcnt(0)\n\t"
    "s_nop 4\n\t"
    "s_branch 9f\n\t"
    "5:\n\t"
    "s_add_u32 %[st], %[b], %[po]\n\t"
    "s_max_i32 %[st], %[st], %[p0]\n\t"
    "v_mov_b32 v39, %[st]\n\t"
    "v_mov_b32 v38, 0\n\t"
    "s_mov_b64 exec, 1\n\t"
    "global_store_dword v38, v39, %[pg] sc1\n\t"
    "s_mov_b64 exec, -1\n\t"
    "s_mov_b32 %[p0], %[p1]\n\t"
    "s_xor_b32 %[sl], %[sl], 0x100\n\t"
    "s_cmp_ge_i32 %[lf], %[cp]\n\t"
    "s_cbranch_scc1 6b\n\t"
    "s_mov_b32 m0, %[sl]\n\t"
    "s_nop 0\n\t"
    "global_load_lds_dword %[dv], %[db] sc1\n\t"
    "s_branch 6b\n\t"
    "9:"
    : "=&{v[80:95]}"(pd), [ea] "+&v"(ea), [da] "+&v"(da), [b] "+&s"(b), [lf] "+&s"(lf), "+{s[36:37]}"(p1), "+{s[38:39]}"(p0), [sa] "+&v"(sa), [st] "=&s"(st), [sl] "+&s"(sl), [p0] "+&s"(pf)
    : [rw] "v"(rw), [ab] "s"(ab), [cp] "s"(cp), [mu] "s"(mu), [ad] "v"(ad), "{v[128:159]}"(W[0]), "{v[160:191]}"(W[1]), "{v[192:223]}"(W[2]), "{v[224:255]}"(W[3]), [co0] "v"(co0), [w0] "s"(w0), [dv] "v"(dv), [db] "s"(db), [pg] "s"(pg), [po] "s"(po - 1), [p1] "s"(b + po), [co1] "v"(co1)
    : "v38", "v39", "v40", "v41", "v42", "v43", "v44", "v45", "v46", "v47", "v48", "v49", "v50", "v51", "v52", "v53", "v54", "v55", "v56", "v57", "v58", "v59", "v60", "v61", "v62", "v63", "v64", "v65", "v66", "v67", "v68", "v69", "v70", "v71", "v72", "v73", "v74", "v75", "v76", "v77", "v78", "v79", "v96", "v97", "v98", "v99", "v100", "v101", "v102", "v103", "v104", "v105", "v106", "v107", "v108", "v109", "v110", "v111", "v112", "v113", "v114", "v115", "v116", "v117", "v118", "v119", "v120", "v121", "v122", "v123", "v124", "v125", "v126", "v127", "vcc", "scc", "m0", "memory");
}
__device__ __forceinline__ void near_ld0(const u32x32 (&W)[4], u32x16& pd, unsigned ea, unsigned da, int& b, int& lf, int cp, unsigned sa, unsigned mu, unsigned ad,
    unsigned rw, unsigned ab, const char* p1, const char* p0, unsigned l1, unsigned l0, const unsigned (&av)[8]) {
  asm volatile("s_setprio 0\n\t"
    "s_waitcnt vmcnt(4)\n\t"
    "s_barrier\n\t"
    "ds_read_b128 v[96:99], %[ea] offset:0\n\t"
    "ds_read_b128 v[100:103], %[ea] offset:2048\n\t"
    "ds_read_b128 v[104:107], %[da] offset:0\n\t"
    "ds_read_b128 v[108:111], %[da] offset:2048\n\t"
    "ds_read_b128 v[112:115], %[ea] offset:4096\n\t"
    "ds_read_b128 v[116:119], %[ea] offset:6144\n\t"
    "s_branch 2f\n\t"
    "1:\n\t"
    "s_waitcnt vmcnt(4)\n\t"
    "s_nop 1\n\t"
    "s_barrier\n\t"
    "2:\n\t"
    "s_waitcnt lgkmcnt(4)\n\t"
    "v_mfma_f32_16x16x32_bf16 v[64:67], v[128:131], v[96:99], 0\n\t"
    "ds_read_b128 v[120:123], %[da] offset:4096\n\t"
    "s_add_u32 m0, %[l1], 0\n\t"
    "ds_write_b128 %[rw], v[80:83] offset:0\n\t"
    "v_mfma_f32_16x16x32_bf16 v[68:71], v[132:135], v[96:99], 0\n\t"
    "ds_read_b128 v[124:127], %[da] offset:6144\n\t"
    "global_load_lds_dwordx4 %[av0], s[36:37] sc1\n\t"
    "ds_write_b128 %[rw], v[84:87] offset:1024\n\t"
    "v_mfma_f32_16x16x32_bf16 v[72:75], v[128:131], v[100:103], 0\n\t"
    "s_add_u32 m0, %[l1], 32768\n\t"
    "ds_write_b128 %[rw], v[88:91] offset:2048\n\t"
    "v_mfma_f32_16x16x32_bf16 v[76:79], v[132:135], v[100:103], 0\n\t"
    "global_load_lds_dwordx4 %[av1], s[36:37] sc1\n\t"
    "ds_write_b128 %[rw], v[92:95] offset:3072\n\t"
    "s_waitcnt lgkmcnt(8)\n\t"
    "v_mfma_f32_16x16x32_bf16 v[64:67], v[136:139], v[104:107], v[64:67]\n\t"
    "ds_read_b128 v[96:99], %[ea] offset:8192\n\t"
    "s_add_u32 m0, %[l1], 4096\n\t"
    "v_mfma_f32_16x16x32_bf16 v[68:71], v[140:143], v[104:107], v[68:71]\n\t"
    "ds_read_b128 v[100:103], %[ea] offset:10240\n\t"
    "global_load_lds_dwordx4 %[av2], s[36:37] sc1\n\t"
    "v_mfma_f32_16x16x32_bf16 v[72:75], v[136:139], v[108:111], v[72:75]\n\t"
    "s_add_u32 m0, %[l1], 36864\n\t"
    "v_mfma_f32_16x16x32_bf16 v[76:79], v[140:143], v[108:111], v[76:79]\n\t"
    "global_load_lds_dwordx4 %[av3], s[36:37] sc1\n\t"
    "s_waitcnt lgkmcnt(2)\n\t"
    "v_mfma_f32_16x16x32_bf16 v[64:67], v[144:147], v[112:115], v[64:67]\n\t"
    "ds_read_b128 v[104:107], %[da] offset:8192\n\t"
    "s_add_u32 m0, %[l1], 8192\n\t"
    "v_mfma_f32_16x16x32_bf16 v[68:71], v[148:151], v[112:115], v[68:71]\n\t"
    "ds_read_b128 v[108:111], %[da] offset:10240\n\t"
    "global_load_lds_dwordx4 %[av4], s[36:37] sc1\n\t"
    "v_mfma_f32_16x16x32_bf16 v[72:75], v[144:147], v[116:119], v[72:75]\n\t"
    "s_add_u32 m0, %[l1], 40960\n\t"
    "v_mfma_f32_16x16x32_bf16 v[76:79], v[148:151], v[116:119], v[76:79]\n\t"
    "global_load_lds_dwordx4 %[av5], s[36:37] sc1\n\t"
    "s_waitcnt vmcnt(6)\n\t"
    "s_barrier\n\t"
    "v_mfma_f32_16x16x32_bf16 v[64:67], v[152:155], v[120:123], v[64:67]\n\t"
    "ds_read_b128 v[112:115], %[ea] offset:12288\n\t"
    "s_add_u32 m0, %[l1], 12288\n\t"
    "v_mfma_f32_16x16x32_bf16 v[68:71], v[156:159], v[120:123], v[68:71]\n\t"
    "ds_read_b128 v[116:119], %[ea] offset:14336\n\t"
    "global_load_lds_dwordx4 %[av6], s[36:37] sc1\n\t"
    "v_mfma_f32_16x16x32_bf16 v[72:75], v[152:155], v[124:127], v[72:75]\n\t"
    "s_add_u32 m0, %[l1], 45056\n\t"
    "v_mfma_f32_16x16x32_bf16 v[76:79], v[156:159], v[124:127], v[76:79]\n\t"
    "global_load_lds_dwordx4 %[av7], s[36:37] sc1\n\t"
    "s_waitcnt lgkmcnt(4)\n\t"
    "v_mfma_f32_16x16x32_bf16 v[64:67], v[160:163], v[96:99], v[64:67]\n\t"
    "ds_read_b128 v[120:123], %[da] offset:12288\n\t"
    "s_add_u32 m0, %[l0], 0\n\t"
    "v_mfma_f32_16x16x32_bf16 v[68:71], v[164:167], v[96:99], v[68:71]\n\t"
    "ds_read_b128 v[124:127], %[da] offset:14336\n\t"
    "global_load_lds_dwordx4 %[av0], s[38:39] sc1\n\t"
    "v_mfma_f32_16x16x32_bf16 v[72:75], v[160:163], v[100:103], v[72:75]\n\t"
    "s_add_u32 m0, %[l0], 32768\n\t"
    "v_mfma_f32_16x16x32_bf16 v[76:79], v[164:167], v[100:103], v[76:79]\n\t"
    "global_load_lds_dwordx4 %[av1], s[38:39] sc1\n\t"
    "s_waitcnt lgkmcnt(4)\n\t"
    "v_mfma_f32_16x16x32_bf16 v[64:67], v[168:171], v[104:107], v[64:67]\n\t"
    "ds_read_b128 v[96:99], %[ea] offset:16384\n\t"
    "s_add_u32 m0, %[l0], 4096\n\t"
    "v_mfma_f32_16x16x32_bf16 v[68:71], v[172:175], v[104:107], v[68:71]\n\t"
    "ds_read_b128 v[100:103], %[ea] offset:18432\n\t"
    "global_load_lds_dwordx4 %[av2], s[38:39] sc1\n\t"
    "v_mfma_f32_16x16x32_bf16 v[72:75], v[168:171], v[108:111], v[72:75]\n\t"
    "s_add_u32 m0, %[l0], 36864\n\t"
    "v_mfma_f32_16x16x32_bf16 v[76:79], v[172:175], v[108:111], v[76:79]\n\t"
    "global_load_lds_dwordx4 %[av3], s[38:39] sc1\n\t"
    "s_waitcnt lgkmcnt(4)\n\t"
    "v_mfma_f32_16x16x32_bf16 v[64:67], v[176:179], v[112:115], v[64:67]\n\t"
    "ds_read_b128 v[104:107], %[da] offset:16384\n\t"
    "s_add_u32 m0, %[l0], 8192\n\t"
    "v_mfma_f32_16x16x32_bf16 v[68:71], v[180:183], v[112:115], v[68:71]\n\t"
    "ds_read_b128 v[108:111], %[da] offset:18432\n\t"
    "global_load_lds_dwordx4 %[av4], s[38:39] sc1\n\t"
    "v_mfma_f32_16x16x32_bf16 v[72:75], v[176:179], v[116:119], v[72:75]\n\t"
    "s_add_u32 m0, %[l0], 40960\n\t"
    "v_mfma_f32_16x16x32_bf16 v[76:79], v[180:183], v[116:119], v[76:79]\n\t"
    "global_load_lds_dwordx4 %[av5], s[38:39] sc1\n\t"
    "s_waitcnt lgkmcnt(4)\n\t"
    "v_mfma_f32_16x16x32_bf16 v[64:67], v[184:187], v[120:123], v[64:67]\n\t"
    "ds_read_b128 v[112:115], %[ea] offset:20480\n\t"
    "s_add_u32 m0, %[l0], 12288\n\t"
    "v_mfma_f32_16x16x32_bf16 v[68:71], v[188:191], v[120:123], v[68:71]\n\t"
    "ds_read_b128 v[116:119], %[ea] offset:22528\n\t"
    "global_load_lds_dwordx4 %[av6], s[38:39] sc1\n\t"
    "v_mfma_f32_16x16x32_bf16 v[72:75], v[184:187], v[124:127], v[72:75]\n\t"
    "s_add_u32 m0, %[l0], 45056\n\t"
    "v_mfma_f32_16x16x32_bf16 v[76:79], v[188:191], v[124:127], v[76:79]\n\t"
    "global_load_lds_dwordx4 %[av7], s[38:39] sc1\n\t"
    "s_waitcnt lgkmcnt(4)\n\t"
    "v_mfma_f32_16x16x32_bf16 v[64:67], v[192:195], v[96:99], v[64:67]\n\t"
    "ds_read_b128 v[120:123], %[da] offset:20480\n\t"
    "v_mfma_f32_16x16x32_bf16 v[68:71], v[196:199], v[96:99], v[68:71]\n\t"
    "ds_read_b128 v[124:127], %[da] offset:22528\n\t"
    "v_mfma_f32_16x16x32_bf16 v[72:75], v[192:195], v[100:103], v[72:75]\n\t"
    "v_mfma_f32_16x16x32_bf16 v[76:79], v[196:199], v[100:103], v[76:79]\n\t"
    "s_waitcnt lgkmcnt(4)\n\t"
    "v_mfma_f32_16x16x32_bf16 v[64:67], v[200:203], v[104:107], v[64:67]\n\t"
    "ds_read_b128 v[96:99], %[ea] offset:24576\n\t"
    "v_mfma_f32_16x16x32_bf16 v[68:71], v[204:207], v[104:107], v[68:71]\n\t"
    "ds_read_b128 v[100:103], %[ea] offset:26624\n\t"
    "v_mfma_f32_16x16x32_bf16 v[72:75], v[200:203], v[108:111], v[72:75]\n\t"
    "v_mfma_f32_16x16x32_bf16 v[76:79], v[204:207], v[108:111], v[76:79]\n\t"
    "s_add_u32 s36, s36, %[ab]\n\t"
    "s_addc_u32 s37, s37, 0\n\t"
    "s_waitcnt lgkmcnt(4)\n\t"
    "v_mfma_f32_16x16x32_bf16 v[64:67], v[208:211], v[112:115], v[64:67]\n\t"
    "ds_read_b128 v[104:107], %[da] offset:24576\n\t"
    "v_mfma_f32_16x16x32_bf16 v[68:71], v[212:215], v[112:115], v[68:71]\n\t"
    "ds_read_b128 v[108:111], %[da] offset:26624\n\t"
    "v_mfma_f32_16x16x32_bf16 v[72:75], v[208:211], v[116:119], v[72:75]\n\t"
    "v_mfma_f32_16x16x32_bf16 v[76:79], v[212:215], v[116:119], v[76:79]\n\t"
    "s_xor_b32 %[l1], %[l1], 0x10000\n\t"
    "s_waitcnt lgkmcnt(4)\n\t"
    "v_mfma_f32_16x16x32_bf16 v[64:67], v[216:219], v[120:123], v[64:67]\n\t"
    "ds_read_b128 v[112:115], %[ea] offset:28672\n\t"
    "v_mfma_f32_16x16x32_bf16 v[68:71], v[220:223], v[120:123], v[68:71]\n\t"
    "ds_read_b128 v[116:119], %[ea] offset:30720\n\t"
    "v_mfma_f32_16x16x32_bf16 v[72:75], v[216:219], v[124:127], v[72:75]\n\t"
    "v_xor_b32 %[ea], 0x10000, %[ea]\n\t"
    "v_mfma_f32_16x16x32_bf16 v[76:79], v[220:223], v[124:127], v[76:79]\n\t"
    "s_waitcnt vmcnt(12)\n\t"
    "s_barrier\n\t"
    "s_waitcnt lgkmcnt(4)\n\t"
    "v_mfma_f32_16x16x32_bf16 v[64:67], v[224:227], v[96:99], v[64:67]\n\t"
    "ds_read_b32 v39, %[sa]\n\t"
    "ds_read_b128 v[120:123], %[da] offset:28672\n\t"
    "v_mfma_f32_16x16x32_bf16 v[68:71], v[228:231], v[96:99], v[68:71]\n\t"
    "ds_read_b128 v[124:127], %[da] offset:30720\n\t"
    "v_mfma_f32_16x16x32_bf16 v[72:75], v[224:227], v[100:103], v[72:75]\n\t"
    "v_xor_b32 %[da], 0x10000, %[da]\n\t"
    "v_mfma_f32_16x16x32_bf16 v[76:79], v[228:231], v[100:103], v[76:79]\n\t"
    "s_waitcnt lgkmcnt(5)\n\t"
    "v_mfma_f32_16x16x32_bf16 v[64:67], v[232:235], v[104:107], v[64:67]\n\t"
    "ds_read_b128 v[96:99], %[ea] offset:0\n\t"
    "v_mfma_f32_16x16x32_bf16 v[68:71], v[236:239], v[104:107], v[68:71]\n\t"
    "ds_read_b128 v[100:103], %[ea] offset:2048\n\t"
    "v_mfma_f32_16x16x32_bf16 v[72:75], v[232:235], v[108:111], v[72:75]\n\t"
    "v_mfma_f32_16x16x32_bf16 v[76:79], v[236:239], v[108:111], v[76:79]\n\t"
    "s_waitcnt lgkmcnt(4)\n\t"
    "v_mfma_f32_16x16x32_bf16 v[64:67], v[240:243], v[112:115], v[64:67]\n\t"
    "ds_read_b128 v[104:107], %[da] offset:0\n\t"
    "v_mad_u32_u24 v39, v39, %[mu], %[ad]\n\t"
    "v_mfma_f32_16x16x32_bf16 v[68:71], v[244:247], v[112:115], v[68:71]\n\t"
    "ds_read_b128 v[108:111], %[da] offset:2048\n\t"
    "v_cmp_ge_i32 vcc, %[lf], v39\n\t"
    "s_cmp_eq_u64 vcc, 0\n\t"
    "s_addc_u32 %[lf], %[lf], 0\n\t"
    "v_mfma_f32_16x16x32_bf16 v[72:75], v[240:243], v[116:119], v[72:75]\n\t"
    "v_mfma_f32_16x16x32_bf16 v[76:79], v[244:247], v[116:119], v[76:79]\n\t"
    "v_cmp_ge_i32 vcc, %[lf], v39\n\t"
    "s_cmp_eq_u64 vcc, 0\n\t"
    "s_addc_u32 %[lf], %[lf], 0\n\t"
    "s_waitcnt lgkmcnt(4)\n\t"
    "v_mfma_f32_16x16x32_bf16 v[80:83], v[248:251], v[120:123], v[64:67]\n\t"
    "ds_read_b128 v[112:115], %[ea] offset:4096\n\t"
    "s_min_i32 %[lf], %[lf], %[cp]\n\t"
    "s_add_u32 %[b], %[b], 1\n\t"
    "v_mfma_f32_16x16x32_bf16 v[84:87], v[252:255], v[120:123], v[68:71]\n\t"
    "ds_read_b128 v[116:119], %[ea] offset:6144\n\t"
    "v_mfma_f32_16x16x32_bf16 v[88:91], v[248:251], v[124:127], v[72:75]\n\t"
    "v_mfma_f32_16x16x32_bf16 v[92:95], v[252:255], v[124:127], v[76:79]\n\t"
    "s_add_u32 s38, s38, %[ab]\n\t"
    "s_addc_u32 s39, s39, 0\n\t"
    "s_xor_b32 %[l0], %[l0], 0x10000\n\t"
    "s_cmp_lt_i32 %[b], %[lf]\n\t"
    "s_cbranch_scc1 1b\n\t"
    "s_waitcnt lgkmcnt(0)\n\t"
    "s_nop 4"
    : "=&{v[80:95]}"(pd), [ea] "+&v"(ea), [da] "+&v"(da), [b] "+&s"(b), [lf] "+&s"(lf), "+{s[36:37]}"(p1), "+{s[38:39]}"(p0), [sa] "+&v"(sa), [l1] "+&s"(l1), [l0] "+&s"(l0)
    : [rw] "v"(rw), [ab] "s"(ab), [cp] "s"(cp), [mu] "s"(mu), [ad] "v"(ad), "{v[128:159]}"(W[0]), "{v[160:191]}"(W[1]), "{v[192:223]}"(W[2]), "{v[224:255]}"(W[3]), [av0] "v"(av[0]), [av1] "v"(av[1]), [av2] "v"(av[2]), [av3] "v"(av[3]), [av4] "v"(av[4]), [av5] "v"(av[5]), [av6] "v"(av[6]), [av7] "v"(av[7])
    : "v39", "v64", "v65", "v66", "v67", "v68", "v69", "v70", "v71", "v72", "v73", "v74", "v75", "v76", "v77", "v78", "v79", "v96", "v97", "v98", "v99", "v100", "v101", "v102", "v103", "v104", "v105", "v106", "v107", "v108", "v109", "v110", "v111", "v112", "v113", "v114", "v115", "v116", "v117", "v118", "v119", "v120", "v121", "v122", "v123", "v124", "v125", "v126", "v127", "vcc", "scc", "m0", "memory");
}
__device__ __forceinline__ void near_ld2(const u32x32 (&W)[4], u32x16& pd, unsigned ea, unsigned da, int& b, int& lf, int cp, unsigned sa, unsigned mu, unsigned ad,
    unsigned rw, unsigned ab, const char* p1, const char* p0, unsigned l1, unsigned l0, const unsigned (&av)[8]) {
  asm volatile("s_setprio 0\n\t"
    "s_waitcnt vmcnt(2)\n\t"
    "s_barrier\n\t"
    "ds_read_b128 v[96:99], %[ea] offset:0\n\t"
    "ds_read_b128 v[100:103], %[ea] offset:2048\n\t"
    "ds_read_b128 v[104:107], %[da] offset:0\n\t"
    "ds_read_b128 v[108:111], %[da] offset:2048\n\t"
    "ds_read_b128 v[112:115], %[ea] offset:4096\n\t"
    "ds_read_b128 v[116:119], %[ea] offset:6144\n\t"
    "s_branch 2f\n\t"
    "1:\n\t"
    "s_waitcnt vmcnt(2)\n\t"
    "s_nop 1\n\t"
    "s_barrier\n\t"
    "2:\n\t"
    "s_waitcnt lgkmcnt(4)\n\t"
    "v_mfma_f32_16x16x32_bf16 v[64:67], v[128:131], v[96:99], 0\n\t"
    "ds_read_b128 v[120:123], %[da] offset:4096\n\t"
    "s_add_u32 m0, %[l1], 0\n\t"
    "ds_write_b128 %[rw], v[80:83] offset:0\n\t"
    "v_mfma_f32_16x16x32_bf16 v[68:71], v[132:135], v[96:99], 0\n\t"
    "ds_read_b128 v[124:127], %[da] offset:6144\n\t"
    "global_load_lds_dwordx4 %[av0], s[36:37] sc1\n\t"
    "ds_write_b128 %[rw], v[84:87] offset:1024\n\t"
    "v_mfma_f32_16x16x32_bf16 v[72:75], v[128:131], v[100:103], 0\n\t"
    "ds_write_b128 %[rw], v[88:91] offset:2048\n\t"
    "v_mfma_f32_16x16x32_bf16 v[76:79], v[132:135], v[100:103], 0\n\t"
    "ds_write_b128 %[rw], v[92:95] offset:3072\n\t"
    "s_waitcnt lgkmcnt(8)\n\t"
    "v_mfma_f32_16x16x32_bf16 v[64:67], v[136:139], v[104:107], v[64:67]\n\t"
    "ds_read_b128 v[96:99], %[ea] offset:8192\n\t"
    "s_add_u32 m0, %[l1], 4096\n\t"
    "v_mfma_f32_16x16x32_bf16 v[68:71], v[140:143], v[104:107], v[68:71]\n\t"
    "ds_read_b128 v[100:103], %[ea] offset:10240\n\t"
    "global_load_lds_dwordx4 %[av2], s[36:37] sc1\n\t"
    "v_mfma_f32_16x16x32_bf16 v[72:75], v[136:139], v[108:111], v[72:75]\n\t"
    "v_mfma_f32_16x16x32_bf16 v[76:79], v[140:143], v[108:111], v[76:79]\n\t"
    "s_waitcnt lgkmcnt(2)\n\t"
    "v_mfma_f32_16x16x32_bf16 v[64:67], v[144:147], v[112:115], v[64:67]\n\t"
    "ds_read_b128 v[104:107], %[da] offset:8192\n\t"
    "s_add_u32 m0, %[l1], 8192\n\t"
    "v_mfma_f32_16x16x32_bf16 v[68:71], v[148:151], v[112:115], v[68:71]\n\t"
    "ds_read_b128 v[108:111], %[da] offset:10240\n\t"
    "global_load_lds_dwordx4 %[av4], s[36:37] sc1\n\t"
    "v_mfma_f32_16x16x32_bf16 v[72:75], v[144:147], v[116:119], v[72:75]\n\t"
    "v_mfma_f32_16x16x32_bf16 v[76:79], v[148:151], v[116:119], v[76:79]\n\t"
    "s_waitcnt vmcnt(3)\n\t"
    "s_barrier\n\t"
    "v_mfma_f32_16x16x32_bf16 v[64:67], v[152:155], v[120:123], v[64:67]\n\t"
    "ds_read_b128 v[112:115], %[ea] offset:12288\n\t"
    "s_add_u32 m0, %[l1], 12288\n\t"
    "v_mfma_f32_16x16x32_bf16 v[68:71], v[156:159], v[120:123], v[68:71]\n\t"
    "ds_read_b128 v[116:119], %[ea] offset:14336\n\t"
    "global_load_lds_dwordx4 %[av6], s[36:37] sc1\n\t"
    "v_mfma_f32_16x16x32_bf16 v[72:75], v[152:155], v[124:127], v[72:75]\n\t"
    "v_mfma_f32_16x16x32_bf16 v[76:79], v[156:159], v[124:127], v[76:79]\n\t"
    "s_waitcnt lgkmcnt(4)\n\t"
    "v_mfma_f32_16x16x32_bf16 v[64:67], v[160:163], v[96:99], v[64:67]\n\t"
    "ds_read_b128 v[120:123], %[da] offset:12288\n\t"
    "s_add_u32 m0, %[l0], 0\n\t"
    "v_mfma_f32_16x16x32_bf16 v[68:71], v[164:167], v[96:99], v[68:71]\n\t"
    "ds_read_b128 v[124:127], %[da] offset:14336\n\t"
    "global_load_lds_dwordx4 %[av0], s[38:39] sc1\n\t"
    "v_mfma_f32_16x16x32_bf16 v[72:75], v[160:163], v[100:103], v[72:75]\n\t"
    "v_mfma_f32_16x16x32_bf16 v[76:79], v[164:167], v[100:103], v[76:79]\n\t"
    "s_waitcnt lgkmcnt(4)\n\t"
    "v_mfma_f32_16x16x32_bf16 v[64:67], v[168:171], v[104:107], v[64:67]\n\t"
    "ds_read_b128 v[96:99], %[ea] offset:16384\n\t"
    "s_add_u32 m0, %[l0], 4096\n\t"
    "v_mfma_f32_16x16x32_bf16 v[68:71], v[172:175], v[104:107], v[68:71]\n\t"
    "ds_read_b128 v[100:103], %[ea] offset:18432\n\t"
    "global_load_lds_dwordx4 %[av2], s[38:39] sc1\n\t"
    "v_mfma_f32_16x16x32_bf16 v[72:75], v[168:171], v[108:111], v[72:75]\n\t"
    "v_mfma_f32_16x16x32_bf16 v[76:79], v[172:175], v[108:111], v[76:79]\n\t"
    "s_waitcnt lgkmcnt(4)\n\t"
    "v_mfma_f32_16x16x32_bf16 v[64:67], v[176:179], v[112:115], v[64:67]\n\t"
    "ds_read_b128 v[104:107], %[da] offset:16384\n\t"
    "s_add_u32 m0, %[l0], 8192\n\t"
    "v_mfma_f32_16x16x32_bf16 v[68:71], v[180:183], v[112:115], v[68:71]\n\t"
    "ds_read_b128 v[108:111], %[da] offset:18432\n\t"
    "global_load_lds_dwordx4 %[av4], s[38:39] sc1\n\t"
    "v_mfma_f32_16x16x32_bf16 v[72:75], v[176:179], v[116:119], v[72:75]\n\t"
    "v_mfma_f32_16x16x32_bf16 v[76:79], v[180:183], v[116:119], v[76:79]\n\t"
    "s_waitcnt lgkmcnt(4)\n\t"
    "v_mfma_f32_16x16x32_bf16 v[64:67], v[184:187], v[120:123], v[64:67]\n\t"
    "ds_read_b128 v[112:115], %[ea] offset:20480\n\t"
    "s_add_u32 m0, %[l0], 12288\n\t"
    "v_mfma_f32_16x16x32_bf16 v[68:71], v[188:191], v[120:123], v[68:71]\n\t"
    "ds_read_b128 v[116:119], %[ea] offset:22528\n\t"
    "global_load_lds_dwordx4 %[av6], s[38:39] sc1\n\t"
    "v_mfma_f32_16x16x32_bf16 v[72:75], v[184:187], v[124:127], v[72:75]\n\t"
    "v_mfma_f32_16x16x32_bf16 v[76:79], v[188:191], v[124:127], v[76:79]\n\t"
    "s_waitcnt lgkmcnt(4)\n\t"
    "v_mfma_f32_16x16x32_bf16 v[64:67], v[192:195], v[96:99], v[64:67]\n\t"
    "ds_read_b128 v[120:123], %[da] offset:20480\n\t"
    "v_mfma_f32_16x16x32_bf16 v[68:71], v[196:199], v[96:99], v[68:71]\n\t"
    "ds_read_b128 v[124:127], %[da] offset:22528\n\t"
    "v_mfma_f32_16x16x32_bf16 v[72:75], v[192:195], v[100:103], v[72:75]\n\t"
    "v_mfma_f32_16x16x32_bf16 v[76:79], v[196:199], v[100:103], v[76:79]\n\t"
    "s_waitcnt lgkmcnt(4)\n\t"
    "v_mfma_f32_16x16x32_bf16 v[64:67], v[200:203], v[104:107], v[64:67]\n\t"
    "ds_read_b128 v[96:99], %[ea] offset:24576\n\t"
    "v_mfma_f32_16x16x32_bf16 v[68:71], v[204:207], v[104:107], v[68:71]\n\t"
    "ds_read_b128 v[100:103], %[ea] offset:26624\n\t"
    "v_mfma_f32_16x16x32_bf16 v[72:75], v[200:203], v[108:111], v[72:75]\n\t"
    "v_mfma_f32_16x16x32_bf16 v[76:79], v[204:207], v[108:111], v[76:79]\n\t"
    "s_add_u32 s36, s36, %[ab]\n\t"
    "s_addc_u32 s37, s37, 0\n\t"
    "s_waitcnt lgkmcnt(4)\n\t"
    "v_mfma_f32_16x16x32_bf16 v[64:67], v[208:211], v[112:115], v[64:67]\n\t"
    "ds_read_b128 v[104:107], %[da] offset:24576\n\t"
    "v_mfma_f32_16x16x32_bf16 v[68:71], v[212:215], v[112:115], v[68:71]\n\t"
    "ds_read_b128 v[108:111], %[da] offset:26624\n\t"
    "v_mfma_f32_16x16x32_bf16 v[72:75], v[208:211], v[116:119], v[72:75]\n\t"
    "v_mfma_f32_16x16x32_bf16 v[76:79], v[212:215], v[116:119], v[76:79]\n\t"
    "s_xor_b32 %[l1], %[l1], 0x10000\n\t"
    "s_waitcnt lgkmcnt(4)\n\t"
    "v_mfma_f32_16x16x32_bf16 v[64:67], v[216:219], v[120:123], v[64:67]\n\t"
    "ds_read_b128 v[112:115], %[ea] offset:28672\n\t"
    "v_mfma_f32_16x16x32_bf16 v[68:71], v[220:223], v[120:123], v[68:71]\n\t"
    "ds_read_b128 v[116:119], %[ea] offset:30720\n\t"
    "v_mfma_f32_16x16x32_bf16 v[72:75], v[216:219], v[124:127], v[72:75]\n\t"
    "v_xor_b32 %[ea], 0x10000, %[ea]\n\t"
    "v_mfma_f32_16x16x32_bf16 v[76:79], v[220:223], v[124:127], v[76:79]\n\t"
    "s_waitcnt vmcnt(6)\n\t"
    "s_barrier\n\t"
    "s_waitcnt lgkmcnt(4)\n\t"
    "v_mfma_f32_16x16x32_bf16 v[64:67], v[224:227], v[96:99], v[64:67]\n\t"
    "ds_read_b32 v39, %[sa]\n\t"
    "ds_read_b128 v[120:123], %[da] offset:28672\n\t"
    "v_mfma_f32_16x16x32_bf16 v[68:71], v[228:231], v[96:99], v[68:71]\n\t"
    "ds_read_b128 v[124:127], %[da] offset:30720\n\t"
    "v_mfma_f32_16x16x32_bf16 v[72:75], v[224:227], v[100:103], v[72:75]\n\t"
    "v_xor_b32 %[da], 0x10000, %[da]\n\t"
    "v_mfma_f32_16x16x32_bf16 v[76:79], v[228:231], v[100:103], v[76:79]\n\t"
    "s_waitcnt lgkmcnt(5)\n\t"
    "v_mfma_f32_16x16x32_bf16 v[64:67], v[232:235], v[104:107], v[64:67]\n\t"
    "ds_read_b128 v[96:99], %[ea] offset:0\n\t"
    "v_mfma_f32_16x16x32_bf16 v[68:71], v[236:239], v[104:107], v[68:71]\n\t"
    "ds_read_b128 v[100:103], %[ea] offset:2048\n\t"
    "v_mfma_f32_16x16x32_bf16 v[72:75], v[232:235], v[108:111], v[72:75]\n\t"
    "v_mfma_f32_16x16x32_bf16 v[76:79], v[236:239], v[108:111], v[76:79]\n\t"
    "s_waitcnt lgkmcnt(4)\n\t"
    "v_mfma_f32_16x16x32_bf16 v[64:67], v[240:243], v[112:115], v[64:67]\n\t"
    "ds_read_b128 v[104:107], %[da] offset:0\n\t"
    "v_mad_u32_u24 v39, v39, %[mu], %[ad]\n\t"
    "v_mfma_f32_16x16x32_bf16 v[68:71], v[244:247], v[112:115], v[68:71]\n\t"
    "ds_read_b128 v[108:111], %[da] offset:2048\n\t"
    "v_cmp_ge_i32 vcc, %[lf], v39\n\t"
    "s_cmp_eq_u64 vcc, 0\n\t"
    "s_addc_u32 %[lf], %[lf], 0\n\t"
    "v_mfma_f32_16x16x32_bf16 v[72:75], v[240:243], v[116:119], v[72:75]\n\t"
    "v_mfma_f32_16x16x32_bf16 v[76:79], v[244:247], v[116:119], v[76:79]\n\t"
    "v_cmp_ge_i32 vcc, %[lf], v39\n\t"
    "s_cmp_eq_u64 vcc, 0\n\t"
    "s_addc_u32 %[lf], %[lf], 0\n\t"
    "s_waitcnt lgkmcnt(4)\n\t"
    "v_mfma_f32_16x16x32_bf16 v[80:83], v[248:251], v[120:123], v[64:67]\n\t"
    "ds_read_b128 v[112:115], %[ea] offset:4096\n\t"
    "s_min_i32 %[lf], %[lf], %[cp]\n\t"
    "s_add_u32 %[b], %[b], 1\n\t"
    "v_mfma_f32_16x16x32_bf16 v[84:87], v[252:255], v[120:123], v[68:71]\n\t"
    "ds_read_b128 v[116:119], %[ea] offset:6144\n\t"
    "v_mfma_f32_16x16x32_bf16 v[88:91], v[248:251], v[124:127], v[72:75]\n\t"
    "v_mfma_f32_16x16x32_bf16 v[92:95], v[252:255], v[124:127], v[76:79]\n\t"
    "s_add_u32 s38, s38, %[ab]\n\t"
    "s_addc_u32 s39, s39, 0\n\t"
    "s_xor_b32 %[l0], %[l0], 0x10000\n\t"
    "s_cmp_lt_i32 %[b], %[lf]\n\t"
    "s_cbranch_scc1 1b\n\t"
    "s_waitcnt lgkmcnt(0)\n\t"
    "s_nop 4"
    : "=&{v[80:95]}"(pd), [ea] "+&v"(ea), [da] "+&v"(da), [b] "+&s"(b), [lf] "+&s"(lf), "+{s[36:37]}"(p1), "+{s[38:39]}"(p0), [sa] "+&v"(sa), [l1] "+&s"(l1), [l0] "+&s"(l0)
    : [rw] "v"(rw), [ab] "s"(ab), [cp] "s"(cp), [mu] "s"(mu), [ad] "v"(ad), "{v[128:159]}"(W[0]), "{v[160:191]}"(W[1]), "{v[192:223]}"(W[2]), "{v[224:255]}"(W[3]), [av0] "v"(av[0]), [av1] "v"(av[1]), [av2] "v"(av[2]), [av3] "v"(av[3]), [av4] "v"(av[4]), [av5] "v"(av[5]), [av6] "v"(av[6]), [av7] "v"(av[7])
    : "v39", "v64", "v65", "v66", "v67", "v68", "v69", "v70", "v71", "v72", "v73", "v74", "v75", "v76", "v77", "v78", "v79", "v96", "v97", "v98", "v99", "v100", "v101", "v102", "v103", "v104", "v105", "v106", "v107", "v108", "v109", "v110", "v111", "v112", "v113", "v114", "v115", "v116", "v117", "v118", "v119", "v120", "v121", "v122", "v123", "v124", "v125", "v126", "v127", "vcc", "scc", "m0", "memory");
}
__device__ __forceinline__ void near_st0(const u32x32 (&W)[4], u32x16& pd, unsigned ea, unsigned da, int& b, int& lf, int cp, unsigned sa, unsigned mu, unsigned ad,
    unsigned rw, unsigned ab, const char* p1, const char* p0, unsigned co0, unsigned co1, int po, int pf, unsigned w0, unsigned sl, unsigned dv, const char* db, const char* pg) {
  unsigned st;
  asm volatile("s_barrier\n\t"
    "ds_read_b128 v[96:99], %[ea] offset:0\n\t"
    "ds_read_b128 v[100:103], %[ea] offset:2048\n\t"
    "ds_read_b128 v[104:107], %[da] offset:0\n\t"
    "ds_read_b128 v[108:111], %[da] offset:2048\n\t"
    "ds_read_b128 v[112:115], %[ea] offset:4096\n\t"
    "ds_read_b128 v[116:119], %[ea] offset:6144\n\t"
    "s_branch 2f\n\t"
    "1:\n\t"
    "s_barrier\n\t"
    "2:\n\t"
    "s_waitcnt lgkmcnt(4)\n\t"
    "v_mfma_f32_16x16x32_bf16 v[64:67], v[128:131], v[96:99], 0\n\t"
    "ds_read_b128 v[120:123], %[da] offset:4096\n\t"
    "v_mfma_f32_16x16x32_bf16 v[68:71], v[132:135], v[96:99], 0\n\t"
    "ds_read_b128 v[124:127], %[da] offset:6144\n\t"
    "v_mfma_f32_16x16x32_bf16 v[72:75], v[128:131], v[100:103], 0\n\t"
    "v_mfma_f32_16x16x32_bf16 v[76:79], v[132:135], v[100:103], 0\n\t"
    "s_waitcnt lgkmcnt(4)\n\t"
    "v_mfma_f32_16x16x32_bf16 v[64:67], v[136:139], v[104:107], v[64:67]\n\t"
    "ds_read_b128 v[96:99], %[ea] offset:8192\n\t"
    "v_mfma_f32_16x16x32_bf16 v[68:71], v[140:143], v[104:107], v[68:71]\n\t"
    "ds_read_b128 v[100:103], %[ea] offset:10240\n\t"
    "v_mfma_f32_16x16x32_bf16 v[72:75], v[136:139], v[108:111], v[72:75]\n\t"
    "v_mfma_f32_16x16x32_bf16 v[76:79], v[140:143], v[108:111], v[76:79]\n\t"
    "s_waitcnt lgkmcnt(4)\n\t"
    "v_mfma_f32_16x16x32_bf16 v[64:67], v[144:147], v[112:115], v[64:67]\n\t"
    "ds_read_b128 v[104:107], %[da] offset:8192\n\t"
    "v_mfma_f32_16x16x32_bf16 v[68:71], v[148:151], v[112:115], v[68:71]\n\t"
    "ds_read_b128 v[108:111], %[da] offset:10240\n\t"
    "v_mfma_f32_16x16x32_bf16 v[72:75], v[144:147], v[116:119], v[72:75]\n\t"
    "v_mfma_f32_16x16x32_bf16 v[76:79], v[148:151], v[116:119], v[76:79]\n\t"
    "s_waitcnt vmcnt(0)\n\t"
    "s_barrier\n\t"
    "s_waitcnt lgkmcnt(4)\n\t"
    "v_mfma_f32_16x16x32_bf16 v[64:67], v[152:155], v[120:123], v[64:67]\n\t"
    "ds_read_b128 v[112:115], %[ea] offset:12288\n\t"
    "v_mfma_f32_16x16x32_bf16 v[68:71], v[156:159], v[120:123], v[68:71]\n\t"
    "ds_read_b128 v[116:119], %[ea] offset:14336\n\t"
    "v_mfma_f32_16x16x32_bf16 v[72:75], v[152:155], v[124:127], v[72:75]\n\t"
    "ds_read_b128 v[48:51], %[rw] offset:0\n\t"
    "v_mfma_f32_16x16x32_bf16 v[76:79], v[156:159], v[124:127], v[76:79]\n\t"
    "ds_read_b128 v[52:55], %[rw] offset:1024\n\t"
    "s_cmp_eq_u32 %[w0], 0\n\t"
    "s_cbranch_scc1 5f\n\t"
    "6:\n\t"
    "s_waitcnt lgkmcnt(6)\n\t"
    "v_mfma_f32_16x16x32_bf16 v[64:67], v[160:163], v[96:99], v[64:67]\n\t"
    "ds_read_b128 v[120:123], %[da] offset:12288\n\t"
    "v_mfma_f32_16x16x32_bf16 v[68:71], v[164:167], v[96:99], v[68:71]\n\t"
    "ds_read_b128 v[124:127], %[da] offset:14336\n\t"
    "v_mfma_f32_16x16x32_bf16 v[72:75], v[160:163], v[100:103], v[72:75]\n\t"
    "ds_read_b128 v[56:59], %[rw] offset:2048\n\t"
    "v_mfma_f32_16x16x32_bf16 v[76:79], v[164:167], v[100:103], v[76:79]\n\t"
    "ds_read_b128 v[60:63], %[rw] offset:3072\n\t"
    "s_waitcnt lgkmcnt(0)\n\t"
    "v_mfma_f32_16x16x32_bf16 v[64:67], v[168:171], v[104:107], v[64:67]\n\t"
    "ds_read_b128 v[96:99], %[ea] offset:16384\n\t"
    "v_add_f32 v48, v48, v80\n\t"
    "v_add_f32 v49, v49, v81\n\t"
    "v_mfma_f32_16x16x32_bf16 v[68:71], v[172:175], v[104:107], v[68:71]\n\t"
    "ds_read_b128 v[100:103], %[ea] offset:18432\n\t"
    "v_add_f32 v50, v50, v82\n\t"
    "v_mfma_f32_16x16x32_bf16 v[72:75], v[168:171], v[108:111], v[72:75]\n\t"
    "v_add_f32 v51, v51, v83\n\t"
    "v_mfma_f32_16x16x32_bf16 v[76:79], v[172:175], v[108:111], v[76:79]\n\t"
    "v_add_f32 v52, v52, v84\n\t"
    "v_add_f32 v53, v53, v85\n\t"
    "v_mfma_f32_16x16x32_bf16 v[64:67], v[176:179], v[112:115], v[64:67]\n\t"
    "ds_read_b128 v[104:107], %[da] offset:16384\n\t"
    "v_add_f32 v54, v54, v86\n\t"
    "v_mfma_f32_16x16x32_bf16 v[68:71], v[180:183], v[112:115], v[68:71]\n\t"
    "ds_read_b128 v[108:111], %[da] offset:18432\n\t"
    "v_add_f32 v55, v55, v87\n\t"
    "v_mfma_f32_16x16x32_bf16 v[72:75], v[176:179], v[116:119], v[72:75]\n\t"
    "v_add_f32 v56, v56, v88\n\t"
    "v_add_f32 v57, v57, v89\n\t"
    "v_mfma_f32_16x16x32_bf16 v[76:79], v[180:183], v[116:119], v[76:79]\n\t"
    "v_add_f32 v58, v58, v90\n\t"
    "v_mfma_f32_16x16x32_bf16 v[64:67], v[184:187], v[120:123], v[64:67]\n\t"
    "ds_read_b128 v[112:115], %[ea] offset:20480\n\t"
    "v_add_f32 v59, v59, v91\n\t"
    "v_mfma_f32_16x16x32_bf16 v[68:71], v[188:191], v[120:123], v[68:71]\n\t"
    "ds_read_b128 v[116:119], %[ea] offset:22528\n\t"
    "v_add_f32 v60, v60, v92\n\t"
    "v_mfma_f32_16x16x32_bf16 v[72:75], v[184:187], v[124:127], v[72:75]\n\t"
    "v_add_f32 v61, v61, v93\n\t"
    "v_add_f32 v62, v62, v94\n\t"
    "v_mfma_f32_16x16x32_bf16 v[76:79], v[188:191], v[124:127], v[76:79]\n\t"
    "v_add_f32 v63, v63, v95\n\t"
    "s_waitcnt lgkmcnt(4)\n\t"
    "v_mfma_f32_16x16x32_bf16 v[64:67], v[192:195], v[96:99], v[64:67]\n\t"
    "ds_read_b128 v[120:123], %[da] offset:20480\n\t"
    "v_cvt_pk_bf16_f32 v40, v48, v49\n\t"
    "v_mfma_f32_16x16x32_bf16 v[68:71], v[196:199], v[96:99], v[68:71]\n\t"
    "ds_read_b128 v[124:127], %[da] offset:22528\n\t"
    "v_cvt_pk_bf16_f32 v41, v50, v51\n\t"
    "v_cvt_pk_bf16_f32 v42, v52, v53\n\t"
    "v_mfma_f32_16x16x32_bf16 v[72:75], v[192:195], v[100:103], v[72:75]\n\t"
    "v_cvt_pk_bf16_f32 v43, v54, v55\n\t"
    "v_mfma_f32_16x16x32_bf16 v[76:79], v[196:199], v[100:103], v[76:79]\n\t"
    "global_store_dwordx4 %[co0], v[40:43], s[36:37] sc1\n\t"
    "s_waitcnt lgkmcnt(4)\n\t"
    "v_mfma_f32_16x16x32_bf16 v[64:67], v[200:203], v[104:107], v[64:67]\n\t"
    "ds_read_b128 v[96:99], %[ea] offset:24576\n\t"
    "v_cvt_pk_bf16_f32 v44, v56, v57\n\t"
    "v_cvt_pk_bf16_f32 v45, v58, v59\n\t"
    "v_mfma_f32_16x16x32_bf16 v[68:71], v[204:207], v[104:107], v[68:71]\n\t"
    "ds_read_b128 v[100:103], %[ea] offset:26624\n\t"
    "v_cvt_pk_bf16_f32 v46, v60, v61\n\t"
    "v_mfma_f32_16x16x32_bf16 v[72:75], v[200:203], v[108:111], v[72:75]\n\t"
    "v_cvt_pk_bf16_f32 v47, v62, v63\n\t"
    "v_mfma_f32_16x16x32_bf16 v[76:79], v[204:207], v[108:111], v[76:79]\n\t"
    "global_store_dwordx4 %[co1], v[44:47], s[36:37] sc1\n\t"
    "s_waitcnt lgkmcnt(4)\n\t"
    "v_mfma_f32_16x16x32_bf16 v[64:67], v[208:211], v[112:115], v[64:67]\n\t"
    "ds_read_b128 v[104:107], %[da] offset:24576\n\t"
    "v_mfma_f32_16x16x32_bf16 v[68:71], v[212:215], v[112:115], v[68:71]\n\t"
    "ds_read_b128 v[108:111], %[da] offset:26624\n\t"
    "v_mfma_f32_16x16x32_bf16 v[72:75], v[208:211], v[116:119], v[72:75]\n\t"
    "v_mfma_f32_16x16x32_bf16 v[76:79], v[212:215], v[116:119], v[76:79]\n\t"
    "s_waitcnt lgkmcnt(4)\n\t"
    "v_mfma_f32_16x16x32_bf16 v[64:67], v[216:219], v[120:123], v[64:67]\n\t"
    "ds_read_b128 v[112:115], %[ea] offset:28672\n\t"
    "v_mfma_f32_16x16x32_bf16 v[68:71], v[220:223], v[120:123], v[68:71]\n\t"
    "ds_read_b128 v[116:119], %[ea] offset:30720\n\t"
    "v_mfma_f32_16x16x32_bf16 v[72:75], v[216:219], v[124:127], v[72:75]\n\t"
    "v_xor_b32 %[ea], 0x10000, %[ea]\n\t"
    "v_mfma_f32_16x16x32_bf16 v[76:79], v[220:223], v[124:127], v[76:79]\n\t"
    "s_waitcnt vmcnt(2)\n\t"
    "s_barrier\n\t"
    "s_waitcnt lgkmcnt(4)\n\t"
    "v_mfma_f32_16x16x32_bf16 v[64:67], v[224:227], v[96:99], v[64:67]\n\t"
    "ds_read_b32 v39, %[sa]\n\t"
    "ds_read_b128 v[120:123], %[da] offset:28672\n\t"
    "v_mfma_f32_16x16x32_bf16 v[68:71], v[228:231], v[96:99], v[68:71]\n\t"
    "ds_read_b128 v[124:127], %[da] offset:30720\n\t"
    "v_mfma_f32_16x16x32_bf16 v[72:75], v[224:227], v[100:103], v[72:75]\n\t"
    "v_xor_b32 %[da], 0x10000, %[da]\n\t"
    "v_mfma_f32_16x16x32_bf16 v[76:79], v[228:231], v[100:103], v[76:79]\n\t"
    "s_waitcnt lgkmcnt(5)\n\t"
    "v_mfma_f32_16x16x32_bf16 v[64:67], v[232:235], v[104:107], v[64:67]\n\t"
    "ds_read_b128 v[96:99], %[ea] offset:0\n\t"
    "v_mfma_f32_16x16x32_bf16 v[68:71], v[236:239], v[104:107], v[68:71]\n\t"
    "ds_read_b128 v[100:103], %[ea] offset:2048\n\t"
    "v_mfma_f32_16x16x32_bf16 v[72:75], v[232:235], v[108:111], v[72:75]\n\t"
    "v_mfma_f32_16x16x32_bf16 v[76:79], v[236:239], v[108:111], v[76:79]\n\t"
    "s_waitcnt lgkmcnt(4)\n\t"
    "v_mfma_f32_16x16x32_bf16 v[64:67], v[240:243], v[112:115], v[64:67]\n\t"
    "ds_read_b128 v[104:107], %[da] offset:0\n\t"
    "v_mad_u32_u24 v39, v39, %[mu], %[ad]\n\t"
    "v_mfma_f32_16x16x32_bf16 v[68:71], v[244:247], v[112:115], v[68:71]\n\t"
    "ds_read_b128 v[108:111], %[da] offset:2048\n\t"
    "v_cmp_ge_i32 vcc, %[lf], v39\n\t"
    "s_cmp_eq_u64 vcc, 0\n\t"
    "s_addc_u32 %[lf], %[lf], 0\n\t"
    "v_mfma_f32_16x16x32_bf16 v[72:75], v[240:243], v[116:119], v[72:75]\n\t"
    "v_mfma_f32_16x16x32_bf16 v[76:79], v[244:247], v[116:119], v[76:79]\n\t"
    "v_cmp_ge_i32 vcc, %[lf], v39\n\t"
    "s_cmp_eq_u64 vcc, 0\n\t"
    "s_addc_u32 %[lf], %[lf], 0\n\t"
    "s_waitcnt lgkmcnt(4)\n\t"
    "v_mfma_f32_16x16x32_bf16 v[80:83], v[248:251], v[120:123], v[64:67]\n\t"
    "ds_read_b128 v[112:115], %[ea] offset:4096\n\t"
    "s_min_i32 %[lf], %[lf], %[cp]\n\t"
    "s_add_u32 %[b], %[b], 1\n\t"
    "v_mfma_f32_16x16x32_bf16 v[84:87], v[252:255], v[120:123], v[68:71]\n\t"
    "ds_read_b128 v[116:119], %[ea] offset:6144\n\t"
    "v_mfma_f32_16x16x32_bf16 v[88:91], v[248:251], v[124:127], v[72:75]\n\t"
    "v_mfma_f32_16x16x32_bf16 v[92:95], v[252:255], v[124:127], v[76:79]\n\t"
    "s_mov_b64 s[36:37], s[38:39]\n\t"
    "s_add_u32 s38, s38, %[ab]\n\t"
    "s_addc_u32 s39, s39, 0\n\t"
    "s_cmp_lt_i32 %[b], %[lf]\n\t"
    "s_cbranch_scc1 1b\n\t"
    "s_waitcnt lgkmcnt(0)\n\t"
    "s_nop 4\n\t"
    "s_branch 9f\n\t"
    "5:\n\t"
    "s_add_u32 %[st], %[b], %[po]\n\t"
    "s_max_i32 %[st], %[st], %[p0]\n\t"
    "v_mov_b32 v39, %[st]\n\t"
    "v_mov_b32 v38, 0\n\t"
    "s_mov_b64 exec, 1\n\t"
    "global_store_dword v38, v39, %[pg] sc1\n\t"
    "s_mov_b64 exec, -1\n\t"
    "s_mov_b32 %[p0], %[p1]\n\t"
    "s_cmp_ge_i32 %[lf], %[cp]\n\t"
    "s_cbranch_scc1 6b\n\t"
    "s_mov_b32 m0, %[sl]\n\t"
    "s_nop 0\n\t"
    "global_load_lds_dword %[dv], %[db] sc1\n\t"
    "s_branch 6b\n\t"
    "9:"
    : "=&{v[80:95]}"(pd), [ea] "+&v"(ea), [da] "+&v"(da), [b] "+&s"(b), [lf] "+&s"(lf), "+{s[36:37]}"(p1), "+{s[38:39]}"(p0), [sa] "+&v"(sa), [st] "=&s"(st), [sl] "+&s"(sl), [p0] "+&s"(pf)
    : [rw] "v"(rw), [ab] "s"(ab), [cp] "s"(cp), [mu] "s"(mu), [ad] "v"(ad), "{v[128:159]}"(W[0]), "{v[160:191]}"(W[1]), "{v[192:223]}"(W[2]), "{v[224:255]}"(W[3]), [co0] "v"(co0), [w0] "s"(w0), [dv] "v"(dv), [db] "s"(db), [pg] "s"(pg), [po] "s"(po - 1), [p1] "s"(b + po), [co1] "v"(co1)
    : "v38", "v39", "v40", "v41", "v42", "v43", "v44", "v45", "v46", "v47", "v48", "v49", "v50", "v51", "v52", "v53", "v54", "v55", "v56", "v57", "v58", "v59", "v60", "v61", "v62", "v63", "v64", "v65", "v66", "v67", "v68", "v69", "v70", "v71", "v72", "v73", "v74", "v75", "v76", "v77", "v78", "v79", "v96", "v97", "v98", "v99", "v100", "v101", "v102", "v103", "v104", "v105", "v106", "v107", "v108", "v109", "v110", "v111", "v112", "v113", "v114", "v115", "v116", "v117", "v118", "v119", "v120", "v121", "v122", "v123", "v124", "v125", "v126", "v127", "vcc", "scc", "m0", "memory");
}
__device__ __forceinline__ void near_st1(const u32x32 (&W)[4], u32x16& pd, unsigned ea, unsigned da, int& b, int& lf, int cp, unsigned sa, unsigned mu, unsigned ad,
    unsigned rw, unsigned ab, const char* p1, const char* p0, unsigned co0, unsigned co1, int po, int pf, unsigned w0, unsigned sl, unsigned dv, const char* db, const char* pg) {
  unsigned st;
  asm volatile("s_barrier\n\t"
    "ds_read_b128 v[96:99], %[ea] offset:0\n\t"
    "ds_read_b128 v[100:103], %[ea] offset:2048\n\t"
    "ds_read_b128 v[104:107], %[da] offset:0\n\t"
    "ds_read_b128 v[108:111], %[da] offset:2048\n\t"
    "ds_read_b128 v[112:115], %[ea] offset:4096\n\t"
    "ds_read_b128 v[116:119], %[ea] offset:6144\n\t"
    "s_branch 2f\n\t"
    "1:\n\t"
    "s_barrier\n\t"
    "2:\n\t"
    "s_waitcnt lgkmcnt(4)\n\t"
    "v_mfma_f32_16x16x32_bf16 v[64:67], v[128:131], v[96:99], 0\n\t"
    "ds_read_b128 v[120:123], %[da] offset:4096\n\t"
    "v_mfma_f32_16x16x32_bf16 v[68:71], v[132:135], v[96:99], 0\n\t"
    "ds_read_b128 v[124:127], %[da] offset:6144\n\t"
    "v_mfma_f32_16x16x32_bf16 v[72:75], v[128:131], v[100:103], 0\n\t"
    "v_mfma_f32_16x16x32_bf16 v[76:79], v[132:135], v[100:103], 0\n\t"
    "s_waitcnt lgkmcnt(4)\n\t"
    "v_mfma_f32_16x16x32_bf16 v[64:67], v[136:139], v[104:107], v[64:67]\n\t"
    "ds_read_b128 v[96:99], %[ea] offset:8192\n\t"
    "v_mfma_f32_16x16x32_bf16 v[68:71], v[140:143], v[104:107], v[68:71]\n\t"
    "ds_read_b128 v[100:103], %[ea] offset:10240\n\t"
    "v_mfma_f32_16x16x32_bf16 v[72:75], v[136:139], v[108:111], v[72:75]\n\t"
    "v_mfma_f32_16x16x32_bf16 v[76:79], v[140:143], v[108:111], v[76:79]\n\t"
    "s_waitcnt lgkmcnt(4)\n\t"
    "v_mfma_f32_16x16x32_bf16 v[64:67], v[144:147], v[112:115], v[64:67]\n\t"
    "ds_read_b128 v[104:107], %[da] offset:8192\n\t"
    "v_mfma_f32_16x16x32_bf16 v[68:71], v[148:151], v[112:115], v[68:71]\n\t"
    "ds_read_b128 v[108:111], %[da] offset:10240\n\t"
    "v_mfma_f32_16x16x32_bf16 v[72:75], v[144:147], v[116:119], v[72:75]\n\t"
    "v_mfma_f32_16x16x32_bf16 v[76:79], v[148:151], v[116:119], v[76:79]\n\t"
    "s_waitcnt vmcnt(0)\n\t"
    "s_barrier\n\t"
    "s_waitcnt lgkmcnt(4)\n\t"
    "v_mfma_f32_16x16x32_bf16 v[64:67], v[152:155], v[120:123], v[64:67]\n\t"
    "ds_read_b128 v[112:115], %[ea] offset:12288\n\t"
    "v_mfma_f32_16x16x32_bf16 v[68:71], v[156:159], v[120:123], v[68:71]\n\t"
    "ds_read_b128 v[116:119], %[ea] offset:14336\n\t"
    "v_mfma_f32_16x16x32_bf16 v[72:75], v[152:155], v[124:127], v[72:75]\n\t"
    "ds_read_b128 v[48:51], %[rw] offset:0\n\t"
    "v_mfma_f32_16x16x32_bf16 v[76:79], v[156:159], v[124:127], v[76:79]\n\t"
    "ds_read_b128 v[52:55], %[rw] offset:1024\n\t"
    "s_cmp_eq_u32 %[w0], 0\n\t"
    "s_cbranch_scc1 5f\n\t"
    "6:\n\t"
    "s_waitcnt lgkmcnt(6)\n\t"
    "v_mfma_f32_16x16x32_bf16 v[64:67], v[160:163], v[96:99], v[64:67]\n\t"
    "ds_read_b128 v[120:123], %[da] offset:12288\n\t"
    "v_mfma_f32_16x16x32_bf16 v[68:71], v[164:167], v[96:99], v[68:71]\n\t"
    "ds_read_b128 v[124:127], %[da] offset:14336\n\t"
    "v_mfma_f32_16x16x32_bf16 v[72:75], v[160:163], v[100:103], v[72:75]\n\t"
    "ds_read_b128 v[56:59], %[rw] offset:2048\n\t"
    "v_mfma_f32_16x16x32_bf16 v[76:79], v[164:167], v[100:103], v[76:79]\n\t"
    "ds_read_b128 v[60:63], %[rw] offset:3072\n\t"
    "s_waitcnt lgkmcnt(0)\n\t"
    "v_mfma_f32_16x16x32_bf16 v[64:67], v[168:171], v[104:107], v[64:67]\n\t"
    "ds_read_b128 v[96:99], %[ea] offset:16384\n\t"
    "v_add_f32 v48, v48, v80\n\t"
    "v_add_f32 v49, v49, v81\n\t"
    "v_add_f32 v50, v50, v82\n\t"
    "v_mfma_f32_16x16x32_bf16 v[68:71], v[172:175], v[104:107], v[68:71]\n\t"
    "ds_read_b128 v[100:103], %[ea] offset:18432\n\t"
    "v_add_f32 v51, v51, v83\n\t"
    "v_add_f32 v52, v52, v84\n\t"
    "v_add_f32 v53, v53, v85\n\t"
    "v_mfma_f32_16x16x32_bf16 v[72:75], v[168:171], v[108:111], v[72:75]\n\t"
    "v_add_f32 v54, v54, v86\n\t"
    "v_add_f32 v55, v55, v87\n\t"
    "v_add_f32 v56, v56, v88\n\t"
    "v_mfma_f32_16x16x32_bf16 v[76:79], v[172:175], v[108:111], v[76:79]\n\t"
    "v_add_f32 v57, v57, v89\n\t"
    "v_add_f32 v58, v58, v90\n\t"
    "v_add_f32 v59, v59, v91\n\t"
    "v_mfma_f32_16x16x32_bf16 v[64:67], v[176:179], v[112:115], v[64:67]\n\t"
    "ds_read_b128 v[104:107], %[da] offset:16384\n\t"
    "v_add_f32 v60, v60, v92\n\t"
    "v_add_f32 v61, v61, v93\n\t"
    "v_add_f32 v62, v62, v94\n\t"
    "v_mfma_f32_16x16x32_bf16 v[68:71], v[180:183], v[112:115], v[68:71]\n\t"
    "ds_read_b128 v[108:111], %[da] offset:18432\n\t"
    "v_add_f32 v63, v63, v95\n\t"
    "v_mul_f32 v40, %[nl], v48\n\t"
    "v_mul_f32 v41, %[nl], v49\n\t"
    "v_mfma_f32_16x16x32_bf16 v[72:75], v[176:179], v[116:119], v[72:75]\n\t"
    "v_mul_f32 v42, %[nl], v50\n\t"
    "v_mul_f32 v43, %[nl], v51\n\t"
    "v_mul_f32 v44, %[nl], v56\n\t"
    "v_mfma_f32_16x16x32_bf16 v[76:79], v[180:183], v[116:119], v[76:79]\n\t"
    "v_mul_f32 v45, %[nl], v57\n\t"
    "v_mul_f32 v46, %[nl], v58\n\t"
    "v_mul_f32 v47, %[nl], v59\n\t"
    "v_mfma_f32_16x16x32_bf16 v[64:67], v[184:187], v[120:123], v[64:67]\n\t"
    "ds_read_b128 v[112:115], %[ea] offset:20480\n\t"
    "v_exp_f32 v40, v40\n\t"
    "v_exp_f32 v41, v41\n\t"
    "v_exp_f32 v42, v42\n\t"
    "v_mfma_f32_16x16x32_bf16 v[68:71], v[188:191], v[120:123], v[68:71]\n\t"
    "ds_read_b128 v[116:119], %[ea] offset:22528\n\t"
    "v_exp_f32 v43, v43\n\t"
    "v_exp_f32 v44, v44\n\t"
    "v_exp_f32 v45, v45\n\t"
    "v_mfma_f32_16x16x32_bf16 v[72:75], v[184:187], v[124:127], v[72:75]\n\t"
    "v_exp_f32 v46, v46\n\t"
    "v_exp_f32 v47, v47\n\t"
    "v_add_f32 v40, 1.0, v40\n\t"
    "v_mfma_f32_16x16x32_bf16 v[76:79], v[188:191], v[124:127], v[76:79]\n\t"
    "v_add_f32 v41, 1.0, v41\n\t"
    "v_add_f32 v42, 1.0, v42\n\t"
    "v_add_f32 v43, 1.0, v43\n\t"
    "s_waitcnt lgkmcnt(4)\n\t"
    "v_mfma_f32_16x16x32_bf16 v[64:67], v[192:195], v[96:99], v[64:67]\n\t"
    "ds_read_b128 v[120:123], %[da] offset:20480\n\t"
    "v_add_f32 v44, 1.0, v44\n\t"
    "v_add_f32 v45, 1.0, v45\n\t"
    "v_add_f32 v46, 1.0, v46\n\t"
    "v_mfma_f32_16x16x32_bf16 v[68:71], v[196:199], v[96:99], v[68:71]\n\t"
    "ds_read_b128 v[124:127], %[da] offset:22528\n\t"
    "v_add_f32 v47, 1.0, v47\n\t"
    "v_rcp_f32 v40, v40\n\t"
    "v_rcp_f32 v41, v41\n\t"
    "v_mfma_f32_16x16x32_bf16 v[72:75], v[192:195], v[100:103], v[72:75]\n\t"
    "v_rcp_f32 v42, v42\n\t"
    "v_rcp_f32 v43, v43\n\t"
    "v_rcp_f32 v44, v44\n\t"
    "v_mfma_f32_16x16x32_bf16 v[76:79], v[196:199], v[100:103], v[76:79]\n\t"
    "v_rcp_f32 v45, v45\n\t"
    "v_rcp_f32 v46, v46\n\t"
    "v_rcp_f32 v47, v47\n\t"
    "s_waitcnt lgkmcnt(4)\n\t"
    "v_mfma_f32_16x16x32_bf16 v[64:67], v[200:203], v[104:107], v[64:67]\n\t"
    "ds_read_b128 v[96:99], %[ea] offset:24576\n\t"
    "v_mul_f32 v40, v48, v40\n\t"
    "v_mul_f32 v41, v49, v41\n\t"
    "v_mul_f32 v42, v50, v42\n\t"
    "v_mfma_f32_16x16x32_bf16 v[68:71], v[204:207], v[104:107], v[68:71]\n\t"
    "ds_read_b128 v[100:103], %[ea] offset:26624\n\t"
    "v_mul_f32 v43, v51, v43\n\t"
    "v_mul_f32 v44, v56, v44\n\t"
    "v_mul_f32 v45, v57, v45\n\t"
    "v_mfma_f32_16x16x32_bf16 v[72:75], v[200:203], v[108:111], v[72:75]\n\t"
    "v_mul_f32 v46, v58, v46\n\t"
    "v_mul_f32 v47, v59, v47\n\t"
    "v_mul_f32 v40, v40, v52\n\t"
    "v_mfma_f32_16x16x32_bf16 v[76:79], v[204:207], v[108:111], v[76:79]\n\t"
    "v_mul_f32 v41, v41, v53\n\t"
    "v_mul_f32 v42, v42, v54\n\t"
    "v_mul_f32 v43, v43, v55\n\t"
    "s_waitcnt lgkmcnt(4)\n\t"
    "v_mfma_f32_16x16x32_bf16 v[64:67], v[208:211], v[112:115], v[64:67]\n\t"
    "ds_read_b128 v[104:107], %[da] offset:24576\n\t"
    "v_mul_f32 v44, v44, v60\n\t"
    "v_mul_f32 v45, v45, v61\n\t"
    "v_mul_f32 v46, v46, v62\n\t"
    "v_mfma_f32_16x16x32_bf16 v[68:71], v[212:215], v[112:115], v[68:71]\n\t"
    "ds_read_b128 v[108:111], %[da] offset:26624\n\t"
    "v_mul_f32 v47, v47, v63\n\t"
    "v_cvt_pk_bf16_f32 v48, v40, v41\n\t"
    "v_cvt_pk_bf16_f32 v49, v42, v43\n\t"
    "v_mfma_f32_16x16x32_bf16 v[72:75], v[208:211], v[116:119], v[72:75]\n\t"
    "v_cvt_pk_bf16_f32 v50, v44, v45\n\t"
    "v_cvt_pk_bf16_f32 v51, v46, v47\n\t"
    "s_nop 1\n\t"
    "v_mfma_f32_16x16x32_bf16 v[76:79], v[212:215], v[116:119], v[76:79]\n\t"
    "v_permlane16_swap_b32 v48, v50\n\t"
    "v_permlane16_swap_b32 v49, v51\n\t"
    "global_store_dwordx4 %[co0], v[48:51], s[36:37] sc1\n\t"
    "s_waitcnt lgkmcnt(4)\n\t"
    "v_mfma_f32_16x16x32_bf16 v[64:67], v[216:219], v[120:123], v[64:67]\n\t"
    "ds_read_b128 v[112:115], %[ea] offset:28672\n\t"
    "v_mfma_f32_16x16x32_bf16 v[68:71], v[220:223], v[120:123], v[68:71]\n\t"
    "ds_read_b128 v[116:119], %[ea] offset:30720\n\t"
    "v_mfma_f32_16x16x32_bf16 v[72:75], v[216:219], v[124:127], v[72:75]\n\t"
    "v_xor_b32 %[ea], 0x10000, %[ea]\n\t"
    "v_mfma_f32_16x16x32_bf16 v[76:79], v[220:223], v[124:127], v[76:79]\n\t"
    "s_waitcnt vmcnt(1)\n\t"
    "s_barrier\n\t"
    "s_waitcnt lgkmcnt(4)\n\t"
    "v_mfma_f32_16x16x32_bf16 v[64:67], v[224:227], v[96:99], v[64:67]\n\t"
    "ds_read_b32 v39, %[sa]\n\t"
    "ds_read_b128 v[120:123], %[da] offset:28672\n\t"
    "v_mfma_f32_16x16x32_bf16 v[68:71], v[228:231], v[96:99], v[68:71]\n\t"
    "ds_read_b128 v[124:127], %[da] offset:30720\n\t"
    "v_mfma_f32_16x16x32_bf16 v[72:75], v[224:227], v[100:103], v[72:75]\n\t"
    "v_xor_b32 %[da], 0x10000, %[da]\n\t"
    "v_mfma_f32_16x16x32_bf16 v[76:79], v[228:231], v[100:103], v[76:79]\n\t"
    "s_waitcnt lgkmcnt(5)\n\t"
    "v_mfma_f32_16x16x32_bf16 v[64:67], v[232:235], v[104:107], v[64:67]\n\t"
    "ds_read_b128 v[96:99], %[ea] offset:0\n\t"
    "v_mfma_f32_16x16x32_bf16 v[68:71], v[236:239], v[104:107], v[68:71]\n\t"
    "ds_read_b128 v[100:103], %[ea] offset:2048\n\t"
    "v_mfma_f32_16x16x32_bf16 v[72:75], v[232:235], v[108:111], v[72:75]\n\t"
    "v_mfma_f32_16x16x32_bf16 v[76:79], v[236:239], v[108:111], v[76:79]\n\t"
    "s_waitcnt lgkmcnt(4)\n\t"
    "v_mfma_f32_16x16x32_bf16 v[64:67], v[240:243], v[112:115], v[64:67]\n\t"
    "ds_read_b128 v[104:107], %[da] offset:0\n\t"
    "v_mad_u32_u24 v39, v39, %[mu], %[ad]\n\t"
    "v_mfma_f32_16x16x32_bf16 v[68:71], v[244:247], v[112:115], v[68:71]\n\t"
    "ds_read_b128 v[108:111], %[da] offset:2048\n\t"
    "v_cmp_ge_i32 vcc, %[lf], v39\n\t"
    "s_cmp_eq_u64 vcc, 0\n\t"
    "s_addc_u32 %[lf], %[lf], 0\n\t"
    "v_mfma_f32_16x16x32_bf16 v[72:75], v[240:243], v[116:119], v[72:75]\n\t"
    "v_mfma_f32_16x16x32_bf16 v[76:79], v[244:247], v[116:119], v[76:79]\n\t"
    "v_cmp_ge_i32 vcc, %[lf], v39\n\t"
    "s_cmp_eq_u64 vcc, 0\n\t"
    "s_addc_u32 %[lf], %[lf], 0\n\t"
    "s_waitcnt lgkmcnt(4)\n\t"
    "v_mfma_f32_16x16x32_bf16 v[80:83], v[248:251], v[120:123], v[64:67]\n\t"
    "ds_read_b128 v[112:115], %[ea] offset:4096\n\t"
    "s_min_i32 %[lf], %[lf], %[cp]\n\t"
    "s_add_u32 %[b], %[b], 1\n\t"
    "v_mfma_f32_16x16x32_bf16 v[84:87], v[252:255], v[120:123], v[68:71]\n\t"
    "ds_read_b128 v[116:119], %[ea] offset:6144\n\t"
    "v_mfma_f32_16x16x32_bf16 v[88:91], v[248:251], v[124:127], v[72:75]\n\t"
    "v_mfma_f32_16x16x32_bf16 v[92:95], v[252:255], v[124:127], v[76:79]\n\t"
    "s_mov_b64 s[36:37], s[38:39]\n\t"
    "s_add_u32 s38, s38, %[ab]\n\t"
    "s_addc_u32 s39, s39, 0\n\t"
    "s_cmp_lt_i32 %[b], %[lf]\n\t"
    "s_cbranch_scc1 1b\n\t"
    "s_waitcnt lgkmcnt(0)\n\t"
    "s_nop 4\n\t"
    "s_branch 9f\n\t"
    "5:\n\t"
    "s_add_u32 %[st], %[b], %[po]\n\t"
    "s_max_i32 %[st], %[st], %[p0]\n\t"
    "v_mov_b32 v39, %[st]\n\t"
    "v_mov_b32 v38, 0\n\t"
    "s_mov_b64 exec, 1\n\t"
    "global_store_dword v38, v39, %[pg] sc1\n\t"
    "s_mov_b64 exec, -1\n\t"
    "s_mov_b32 %[p0], %[p1]\n\t"
    "s_cmp_ge_i32 %[lf], %[cp]\n\t"
    "s_cbranch_scc1 6b\n\t"
    "s_mov_b32 m0, %[sl]\n\t"
    "s_nop 0\n\t"
    "global_load_lds_dword %[dv], %[db] sc1\n\t"
    "s_branch 6b\n\t"
    "9:"
    : "=&{v[80:95]}"(pd), [ea] "+&v"(ea), [da] "+&v"(da), [b] "+&s"(b), [lf] "+&s"(lf), "+{s[36:37]}"(p1), "+{s[38:39]}"(p0), [sa] "+&v"(sa), [st] "=&s"(st), [sl] "+&s"(sl), [p0] "+&s"(pf)
    : [rw] "v"(rw), [ab] "s"(ab), [cp] "s"(cp), [mu] "s"(mu), [ad] "v"(ad), "{v[128:159]}"(W[0]), "{v[160:191]}"(W[1]), "{v[192:223]}"(W[2]), "{v[224:255]}"(W[3]), [co0] "v"(co0), [w0] "s"(w0), [dv] "v"(dv), [db] "s"(db), [pg] "s"(pg), [po] "s"(po - 1), [p1] "s"(b + po), [nl] "s"(-LOG2E)
    : "v38", "v39", "v40", "v41", "v42", "v43", "v44", "v45", "v46", "v47", "v48", "v49", "v50", "v51", "v52", "v53", "v54", "v55", "v56", "v57", "v58", "v59", "v60", "v61", "v62", "v63", "v64", "v65", "v66", "v67", "v68", "v69", "v70", "v71", "v72", "v73", "v74", "v75", "v76", "v77", "v78", "v79", "v96", "v97", "v98", "v99", "v100", "v101", "v102", "v103", "v104", "v105", "v106", "v107", "v108", "v109", "v110", "v111", "v112", "v113", "v114", "v115", "v116", "v117", "v118", "v119", "v120", "v121", "v122", "v123", "v124", "v125", "v126", "v127", "vcc", "scc", "m0", "memory");
}
__device__ __forceinline__ void near_st2(const u32x32 (&W)[4], u32x16& pd, unsigned ea, unsigned da, int& b, int& lf, int cp, unsigned sa, unsigned mu, unsigned ad,
    unsigned rw, unsigned ab, const char* p1, const char* p0, unsigned co0, unsigned co1, int po, int pf, unsigned w0, unsigned sl, unsigned dv, const char* db, const char* pg) {
  unsigned st;
  asm volatile("s_barrier\n\t"
    "ds_read_b128 v[96:99], %[ea] offset:0\n\t"
    "ds_read_b128 v[100:103], %[ea] offset:2048\n\t"
    "ds_read_b128 v[104:107], %[da] offset:0\n\t"
    "ds_read_b128 v[108:111], %[da] offset:2048\n\t"
    "ds_read_b128 v[112:115], %[ea] offset:4096\n\t"
    "ds_read_b128 v[116:119], %[ea] offset:6144\n\t"
    "s_branch 2f\n\t"
    "1:\n\t"
    "s_barrier\n\t"
    "2:\n\t"
    "s_waitcnt lgkmcnt(4)\n\t"
    "v_mfma_f32_16x16x32_bf16 v[64:67], v[128:131], v[96:99], 0\n\t"
    "ds_read_b128 v[120:123], %[da] offset:4096\n\t"
    "v_mfma_f32_16x16x32_bf16 v[68:71], v[132:135], v[96:99], 0\n\t"
    "ds_read_b128 v[124:127], %[da] offset:6144\n\t"
    "v_mfma_f32_16x16x32_bf16 v[72:75], v[128:131], v[100:103], 0\n\t"
    "v_mfma_f32_16x16x32_bf16 v[76:79], v[132:135], v[100:103], 0\n\t"
    "s_waitcnt lgkmcnt(4)\n\t"
    "v_mfma_f32_16x16x32_bf16 v[64:67], v[136:139], v[104:107], v[64:67]\n\t"
    "ds_read_b128 v[96:99], %[ea] offset:8192\n\t"
    "v_mfma_f32_16x16x32_bf16 v[68:71], v[140:143], v[104:107], v[68:71]\n\t"
    "ds_read_b128 v[100:103], %[ea] offset:10240\n\t"
    "v_mfma_f32_16x16x32_bf16 v[72:75], v[136:139], v[108:111], v[72:75]\n\t"
    "v_mfma_f32_16x16x32_bf16 v[76:79], v[140:143], v[108:111], v[76:79]\n\t"
    "s_waitcnt lgkmcnt(4)\n\t"
    "v_mfma_f32_16x16x32_bf16 v[64:67], v[144:147], v[112:115], v[64:67]\n\t"
    "ds_read_b128 v[104:107], %[da] offset:8192\n\t"
    "v_mfma_f32_16x16x32_bf16 v[68:71], v[148:151], v[112:115], v[68:71]\n\t"
    "ds_read_b128 v[108:111], %[da] offset:10240\n\t"
    "v_mfma_f32_16x16x32_bf16 v[72:75], v[144:147], v[116:119], v[72:75]\n\t"
    "v_mfma_f32_16x16x32_bf16 v[76:79], v[148:151], v[116:119], v[76:79]\n\t"
    "s_waitcnt vmcnt(0)\n\t"
    "s_barrier\n\t"
    "s_waitcnt lgkmcnt(4)\n\t"
    "v_mfma_f32_16x16x32_bf16 v[64:67], v[152:155], v[120:123], v[64:67]\n\t"
    "ds_read_b128 v[112:115], %[ea] offset:12288\n\t"
    "v_mfma_f32_16x16x32_bf16 v[68:71], v[156:159], v[120:123], v[68:71]\n\t"
    "ds_read_b128 v[116:119], %[ea] offset:14336\n\t"
    "v_mfma_f32_16x16x32_bf16 v[72:75], v[152:155], v[124:127], v[72:75]\n\t"
    "ds_read_b128 v[48:51], %[rw] offset:0\n\t"
    "v_mfma_f32_16x16x32_bf16 v[76:79], v[156:159], v[124:127], v[76:79]\n\t"
    "ds_read_b128 v[52:55], %[rw] offset:1024\n\t"
    "s_cmp_eq_u32 %[w0], 0\n\t"
    "s_cbranch_scc1 5f\n\t"
    "6:\n\t"
    "s_waitcnt lgkmcnt(6)\n\t"
    "v_mfma_f32_16x16x32_bf16 v[64:67], v[160:163], v[96:99], v[64:67]\n\t"
    "ds_read_b128 v[120:123], %[da] offset:12288\n\t"
    "v_mfma_f32_16x16x32_bf16 v[68:71], v[164:167], v[96:99], v[68:71]\n\t"
    "ds_read_b128 v[124:127], %[da] offset:14336\n\t"
    "v_mfma_f32_16x16x32_bf16 v[72:75], v[160:163], v[100:103], v[72:75]\n\t"
    "ds_read_b128 v[56:59], %[rw] offset:2048\n\t"
    "v_mfma_f32_16x16x32_bf16 v[76:79], v[164:167], v[100:103], v[76:79]\n\t"
    "ds_read_b128 v[60:63], %[rw] offset:3072\n\t"
    "s_waitcnt lgkmcnt(0)\n\t"
    "v_mfma_f32_16x16x32_bf16 v[64:67], v[168:171], v[104:107], v[64:67]\n\t"
    "ds_read_b128 v[96:99], %[ea] offset:16384\n\t"
    "v_cvt_pk_bf16_f32 v40, v80, v81\n\t"
    "v_mfma_f32_16x16x32_bf16 v[68:71], v[172:175], v[104:107], v[68:71]\n\t"
    "ds_read_b128 v[100:103], %[ea] offset:18432\n\t"
    "v_cvt_pk_bf16_f32 v41, v82, v83\n\t"
    "v_mfma_f32_16x16x32_bf16 v[72:75], v[168:171], v[108:111], v[72:75]\n\t"
    "v_cvt_pk_bf16_f32 v42, v84, v85\n\t"
    "v_mfma_f32_16x16x32_bf16 v[76:79], v[172:175], v[108:111], v[76:79]\n\t"
    "v_cvt_pk_bf16_f32 v43, v86, v87\n\t"
    "v_mfma_f32_16x16x32_bf16 v[64:67], v[176:179], v[112:115], v[64:67]\n\t"
    "ds_read_b128 v[104:107], %[da] offset:16384\n\t"
    "global_store_dwordx4 %[co0], v[40:43], s[36:37] sc1\n\t"
    "v_mfma_f32_16x16x32_bf16 v[68:71], v[180:183], v[112:115], v[68:71]\n\t"
    "ds_read_b128 v[108:111], %[da] offset:18432\n\t"
    "v_cvt_pk_bf16_f32 v44, v88, v89\n\t"
    "v_mfma_f32_16x16x32_bf16 v[72:75], v[176:179], v[116:119], v[72:75]\n\t"
    "v_cvt_pk_bf16_f32 v45, v90, v91\n\t"
    "v_mfma_f32_16x16x32_bf16 v[76:79], v[180:183], v[116:119], v[76:79]\n\t"
    "v_cvt_pk_bf16_f32 v46, v92, v93\n\t"
    "v_mfma_f32_16x16x32_bf16 v[64:67], v[184:187], v[120:123], v[64:67]\n\t"
    "ds_read_b128 v[112:115], %[ea] offset:20480\n\t"
    "v_cvt_pk_bf16_f32 v47, v94, v95\n\t"
    "v_mfma_f32_16x16x32_bf16 v[68:71], v[188:191], v[120:123], v[68:71]\n\t"
    "ds_read_b128 v[116:119], %[ea] offset:22528\n\t"
    "global_store_dwordx4 %[co1], v[44:47], s[36:37] sc1\n\t"
    "v_mfma_f32_16x16x32_bf16 v[72:75], v[184:187], v[124:127], v[72:75]\n\t"
    "v_cvt_pk_bf16_f32 v48, v48, v49\n\t"
    "v_mfma_f32_16x16x32_bf16 v[76:79], v[188:191], v[124:127], v[76:79]\n\t"
    "v_cvt_pk_bf16_f32 v49, v50, v51\n\t"
    "s_waitcnt lgkmcnt(4)\n\t"
    "v_mfma_f32_16x16x32_bf16 v[64:67], v[192:195], v[96:99], v[64:67]\n\t"
    "ds_read_b128 v[120:123], %[da] offset:20480\n\t"
    "v_cvt_pk_bf16_f32 v50, v52, v53\n\t"
    "v_mfma_f32_16x16x32_bf16 v[68:71], v[196:199], v[96:99], v[68:71]\n\t"
    "ds_read_b128 v[124:127], %[da] offset:22528\n\t"
    "v_cvt_pk_bf16_f32 v51, v54, v55\n\t"
    "v_mfma_f32_16x16x32_bf16 v[72:75], v[192:195], v[100:103], v[72:75]\n\t"
    "global_store_dwordx4 %[co0], v[48:51], s[36:37] offset:2048 sc1\n\t"
    "v_mfma_f32_16x16x32_bf16 v[76:79], v[196:199], v[100:103], v[76:79]\n\t"
    "v_cvt_pk_bf16_f32 v56, v56, v57\n\t"
    "s_waitcnt lgkmcnt(4)\n\t"
    "v_mfma_f32_16x16x32_bf16 v[64:67], v[200:203], v[104:107], v[64:67]\n\t"
    "ds_read_b128 v[96:99], %[ea] offset:24576\n\t"
    "v_cvt_pk_bf16_f32 v57, v58, v59\n\t"
    "v_mfma_f32_16x16x32_bf16 v[68:71], v[204:207], v[104:107], v[68:71]\n\t"
    "ds_read_b128 v[100:103], %[ea] offset:26624\n\t"
    "v_cvt_pk_bf16_f32 v58, v60, v61\n\t"
    "v_mfma_f32_16x16x32_bf16 v[72:75], v[200:203], v[108:111], v[72:75]\n\t"
    "v_cvt_pk_bf16_f32 v59, v62, v63\n\t"
    "v_mfma_f32_16x16x32_bf16 v[76:79], v[204:207], v[108:111], v[76:79]\n\t"
    "global_store_dwordx4 %[co1], v[56:59], s[36:37] offset:2048 sc1\n\t"
    "s_waitcnt lgkmcnt(4)\n\t"
    "v_mfma_f32_16x16x32_bf16 v[64:67], v[208:211], v[112:115], v[64:67]\n\t"
    "ds_read_b128 v[104:107], %[da] offset:24576\n\t"
    "v_mfma_f32_16x16x32_bf16 v[68:71], v[212:215], v[112:115], v[68:71]\n\t"
    "ds_read_b128 v[108:111], %[da] offset:26624\n\t"
    "v_mfma_f32_16x16x32_bf16 v[72:75], v[208:211], v[116:119], v[72:75]\n\t"
    "v_mfma_f32_16x16x32_bf16 v[76:79], v[212:215], v[116:119], v[76:79]\n\t"
    "s_waitcnt lgkmcnt(4)\n\t"
    "v_mfma_f32_16x16x32_bf16 v[64:67], v[216:219], v[120:123], v[64:67]\n\t"
    "ds_read_b128 v[112:115], %[ea] offset:28672\n\t"
    "v_mfma_f32_16x16x32_bf16 v[68:71], v[220:223], v[120:123], v[68:71]\n\t"
    "ds_read_b128 v[116:119], %[ea] offset:30720\n\t"
    "v_mfma_f32_16x16x32_bf16 v[72:75], v[216:219], v[124:127], v[72:75]\n\t"
    "v_xor_b32 %[ea], 0x10000, %[ea]\n\t"
    "v_mfma_f32_16x16x32_bf16 v[76:79], v[220:223], v[124:127], v[76:79]\n\t"
    "s_waitcnt vmcnt(4)\n\t"
    "s_barrier\n\t"
    "s_waitcnt lgkmcnt(4)\n\t"
    "v_mfma_f32_16x16x32_bf16 v[64:67], v[224:227], v[96:99], v[64:67]\n\t"
    "ds_read_b32 v39, %[sa]\n\t"
    "ds_read_b128 v[120:123], %[da] offset:28672\n\t"
    "v_mfma_f32_16x16x32_bf16 v[68:71], v[228:231], v[96:99], v[68:71]\n\t"
    "ds_read_b128 v[124:127], %[da] offset:30720\n\t"
    "v_mfma_f32_16x16x32_bf16 v[72:75], v[224:227], v[100:103], v[72:75]\n\t"
    "v_xor_b32 %[da], 0x10000, %[da]\n\t"
    "v_mfma_f32_16x16x32_bf16 v[76:79], v[228:231], v[100:103], v[76:79]\n\t"
    "s_waitcnt lgkmcnt(5)\n\t"
    "v_mfma_f32_16x16x32_bf16 v[64:67], v[232:235], v[104:107], v[64:67]\n\t"
    "ds_read_b128 v[96:99], %[ea] offset:0\n\t"
    "v_mfma_f32_16x16x32_bf16 v[68:71], v[236:239], v[104:107], v[68:71]\n\t"
    "ds_read_b128 v[100:103], %[ea] offset:2048\n\t"
    "v_mfma_f32_16x16x32_bf16 v[72:75], v[232:235], v[108:111], v[72:75]\n\t"
    "v_mfma_f32_16x16x32_bf16 v[76:79], v[236:239], v[108:111], v[76:79]\n\t"
    "s_waitcnt lgkmcnt(4)\n\t"
    "v_mfma_f32_16x16x32_bf16 v[64:67], v[240:243], v[112:115], v[64:67]\n\t"
    "ds_read_b128 v[104:107], %[da] offset:0\n\t"
    "v_mad_u32_u24 v39, v39, %[mu], %[ad]\n\t"
    "v_mfma_f32_16x16x32_bf16 v[68:71], v[244:247], v[112:115], v[68:71]\n\t"
    "ds_read_b128 v[108:111], %[da] offset:2048\n\t"
    "v_cmp_ge_i32 vcc, %[lf], v39\n\t"
    "s_cmp_eq_u64 vcc, 0\n\t"
    "s_addc_u32 %[lf], %[lf], 0\n\t"
    "v_mfma_f32_16x16x32_bf16 v[72:75], v[240:243], v[116:119], v[72:75]\n\t"
    "v_mfma_f32_16x16x32_bf16 v[76:79], v[244:247], v[116:119], v[76:79]\n\t"
    "v_cmp_ge_i32 vcc, %[lf], v39\n\t"
    "s_cmp_eq_u64 vcc, 0\n\t"
    "s_addc_u32 %[lf], %[lf], 0\n\t"
    "s_waitcnt lgkmcnt(4)\n\t"
    "v_mfma_f32_16x16x32_bf16 v[80:83], v[248:251], v[120:123], v[64:67]\n\t"
    "ds_read_b128 v[112:115], %[ea] offset:4096\n\t"
    "s_min_i32 %[lf], %[lf], %[cp]\n\t"
    "s_add_u32 %[b], %[b], 1\n\t"
    "v_mfma_f32_16x16x32_bf16 v[84:87], v[252:255], v[120:123], v[68:71]\n\t"
    "ds_read_b128 v[116:119], %[ea] offset:6144\n\t"
    "v_mfma_f32_16x16x32_bf16 v[88:91], v[248:251], v[124:127], v[72:75]\n\t"
    "v_mfma_f32_16x16x32_bf16 v[92:95], v[252:255], v[124:127], v[76:79]\n\t"
    "s_mov_b64 s[36:37], s[38:39]\n\t"
    "s_add_u32 s38, s38, %[ab]\n\t"
    "s_addc_u32 s39, s39, 0\n\t"
    "s_cmp_lt_i32 %[b], %[lf]\n\t"
    "s_cbranch_scc1 1b\n\t"
    "s_waitcnt lgkmcnt(0)\n\t"
    "s_nop 4\n\t"
    "s_branch 9f\n\t"
    "5:\n\t"
    "s_add_u32 %[st], %[b], %[po]\n\t"
    "s_max_i32 %[st], %[st], %[p0]\n\t"
    "v_mov_b32 v39, %[st]\n\t"
    "v_mov_b32 v38, 0\n\t"
    "s_mov_b64 exec, 1\n\t"
    "global_store_dword v38, v39, %[pg] sc1\n\t"
    "s_mov_b64 exec, -1\n\t"
    "s_mov_b32 %[p0], %[p1]\n\t"
    "s_cmp_ge_i32 %[lf], %[cp]\n\t"
    "s_cbranch_scc1 6b\n\t"
    "s_mov_b32 m0, %[sl]\n\t"
    "s_nop 0\n\t"
    "global_load_lds_dword %[dv], %[db] sc1\n\t"
    "s_branch 6b\n\t"
    "9:"
    : "=&{v[80:95]}"(pd), [ea] "+&v"(ea), [da] "+&v"(da), [b] "+&s"(b), [lf] "+&s"(lf), "+{s[36:37]}"(p1), "+{s[38:39]}"(p0), [sa] "+&v"(sa), [st] "=&s"(st), [sl] "+&s"(sl), [p0] "+&s"(pf)
    : [rw] "v"(rw), [ab] "s"(ab), [cp] "s"(cp), [mu] "s"(mu), [ad] "v"(ad), "{v[128:159]}"(W[0]), "{v[160:191]}"(W[1]), "{v[192:223]}"(W[2]), "{v[224:255]}"(W[3]), [co0] "v"(co0), [w0] "s"(w0), [dv] "v"(dv), [db] "s"(db), [pg] "s"(pg), [po] "s"(po - 1), [p1] "s"(b + po), [co1] "v"(co1)
    : "v38", "v39", "v40", "v41", "v42", "v43", "v44", "v45", "v46", "v47", "v48", "v49", "v50", "v51", "v52", "v53", "v54", "v55", "v56", "v57", "v58", "v59", "v60", "v61", "v62", "v63", "v64", "v65", "v66", "v67", "v68", "v69", "v70", "v71", "v72", "v73", "v74", "v75", "v76", "v77", "v78", "v79", "v96", "v97", "v98", "v99", "v100", "v101", "v102", "v103", "v104", "v105", "v106", "v107", "v108", "v109", "v110", "v111", "v112", "v113", "v114", "v115", "v116", "v117", "v118", "v119", "v120", "v121", "v122", "v123", "v124", "v125", "v126", "v127", "vcc", "scc", "m0", "memory");
}
__device__ __forceinline__ void lean_ld0(const u32x32 (&W)[4], u32x16& pd, unsigned ea, unsigned da, unsigned x, unsigned lb, const char* s0, const char* s1, const unsigned (&av)[8], unsigned sa, unsigned mu, unsigned ad, int th, unsigned s2, unsigned dv, const char* db) {
  asm volatile("s_setprio 2\n\t"
    "ds_read_b128 v[96:99], %[ea] offset:0\n\t"
    "ds_read_b128 v[100:103], %[ea] offset:2048\n\t"
    "ds_read_b128 v[104:107], %[da] offset:0\n\t"
    "ds_read_b128 v[108:111], %[da] offset:2048\n\t"
    "ds_read_b128 v[112:115], %[ea] offset:4096\n\t"
    "ds_read_b128 v[116:119], %[ea] offset:6144\n\t"
    "s_waitcnt lgkmcnt(4)\n\t"
    "v_mfma_f32_16x16x32_bf16 v[80:83], v[128:131], v[96:99], 0\n\t"
    "ds_read_b128 v[120:123], %[da] offset:4096\n\t"
    "v_mfma_f32_16x16x32_bf16 v[84:87], v[132:135], v[96:99], 0\n\t"
    "ds_read_b128 v[124:127], %[da] offset:6144\n\t"
    "v_mfma_f32_16x16x32_bf16 v[88:91], v[128:131], v[100:103], 0\n\t"
    "v_mfma_f32_16x16x32_bf16 v[92:95], v[132:135], v[100:103], 0\n\t"
    "s_waitcnt vmcnt(24)\n\t"
    "s_cmp_lt_u32 %[x], 2\n\t"
    "s_cbranch_scc0 41f\n\t"
    "s_waitcnt vmcnt(8)\n\t"
    "41:\n\t"
    "s_barrier\n\t"
    "s_waitcnt lgkmcnt(4)\n\t"
    "v_mfma_f32_16x16x32_bf16 v[80:83], v[136:139], v[104:107], v[80:83]\n\t"
    "ds_read_b128 v[96:99], %[ea] offset:8192\n\t"
    "v_mfma_f32_16x16x32_bf16 v[84:87], v[140:143], v[104:107], v[84:87]\n\t"
    "ds_read_b128 v[100:103], %[ea] offset:10240\n\t"
    "v_mfma_f32_16x16x32_bf16 v[88:91], v[136:139], v[108:111], v[88:91]\n\t"
    "v_mfma_f32_16x16x32_bf16 v[92:95], v[140:143], v[108:111], v[92:95]\n\t"
    "s_waitcnt lgkmcnt(4)\n\t"
    "v_mfma_f32_16x16x32_bf16 v[80:83], v[144:147], v[112:115], v[80:83]\n\t"
    "ds_read_b128 v[104:107], %[da] offset:8192\n\t"
    "v_mfma_f32_16x16x32_bf16 v[84:87], v[148:151], v[112:115], v[84:87]\n\t"
    "ds_read_b128 v[108:111], %[da] offset:10240\n\t"
    "v_mfma_f32_16x16x32_bf16 v[88:91], v[144:147], v[116:119], v[88:91]\n\t"
    "v_mfma_f32_16x16x32_bf16 v[92:95], v[148:151], v[116:119], v[92:95]\n\t"
    "s_waitcnt lgkmcnt(4)\n\t"
    "v_mfma_f32_16x16x32_bf16 v[80:83], v[152:155], v[120:123], v[80:83]\n\t"
    "ds_read_b128 v[112:115], %[ea] offset:12288\n\t"
    "v_mfma_f32_16x16x32_bf16 v[84:87], v[156:159], v[120:123], v[84:87]\n\t"
    "ds_read_b128 v[116:119], %[ea] offset:14336\n\t"
    "v_mfma_f32_16x16x32_bf16 v[88:91], v[152:155], v[124:127], v[88:91]\n\t"
    "v_mfma_f32_16x16x32_bf16 v[92:95], v[156:159], v[124:127], v[92:95]\n\t"
    "s_waitcnt lgkmcnt(4)\n\t"
    "v_mfma_f32_16x16x32_bf16 v[80:83], v[160:163], v[96:99], v[80:83]\n\t"
    "ds_read_b128 v[120:123], %[da] offset:12288\n\t"
    "v_mfma_f32_16x16x32_bf16 v[84:87], v[164:167], v[96:99], v[84:87]\n\t"
    "ds_read_b128 v[124:127], %[da] offset:14336\n\t"
    "v_mfma_f32_16x16x32_bf16 v[88:91], v[160:163], v[100:103], v[88:91]\n\t"
    "v_mfma_f32_16x16x32_bf16 v[92:95], v[164:167], v[100:103], v[92:95]\n\t"
    "s_waitcnt vmcnt(20)\n\t"
    "s_cmp_lt_u32 %[x], 2\n\t"
    "s_cbranch_scc0 45f\n\t"
    "s_waitcnt vmcnt(4)\n\t"
    "45:\n\t"
    "s_barrier\n\t"
    "s_waitcnt lgkmcnt(4)\n\t"
    "v_mfma_f32_16x16x32_bf16 v[80:83], v[168:171], v[104:107], v[80:83]\n\t"
    "ds_read_b128 v[96:99], %[ea] offset:16384\n\t"
    "v_mfma_f32_16x16x32_bf16 v[84:87], v[172:175], v[104:107], v[84:87]\n\t"
    "ds_read_b128 v[100:103], %[ea] offset:18432\n\t"
    "v_mfma_f32_16x16x32_bf16 v[88:91], v[168:171], v[108:111], v[88:91]\n\t"
    "v_mfma_f32_16x16x32_bf16 v[92:95], v[172:175], v[108:111], v[92:95]\n\t"
    "s_waitcnt lgkmcnt(4)\n\t"
    "v_mfma_f32_16x16x32_bf16 v[80:83], v[176:179], v[112:115], v[80:83]\n\t"
    "ds_read_b128 v[104:107], %[da] offset:16384\n\t"
    "v_mfma_f32_16x16x32_bf16 v[84:87], v[180:183], v[112:115], v[84:87]\n\t"
    "ds_read_b128 v[108:111], %[da] offset:18432\n\t"
    "v_mfma_f32_16x16x32_bf16 v[88:91], v[176:179], v[116:119], v[88:91]\n\t"
    "v_mfma_f32_16x16x32_bf16 v[92:95], v[180:183], v[116:119], v[92:95]\n\t"
    "s_waitcnt lgkmcnt(4)\n\t"
    "v_mfma_f32_16x16x32_bf16 v[80:83], v[184:187], v[120:123], v[80:83]\n\t"
    "ds_read_b128 v[112:115], %[ea] offset:20480\n\t"
    "v_mfma_f32_16x16x32_bf16 v[84:87], v[188:191], v[120:123], v[84:87]\n\t"
    "ds_read_b128 v[116:119], %[ea] offset:22528\n\t"
    "v_mfma_f32_16x16x32_bf16 v[88:91], v[184:187], v[124:127], v[88:91]\n\t"
    "v_mfma_f32_16x16x32_bf16 v[92:95], v[188:191], v[124:127], v[92:95]\n\t"
    "s_waitcnt lgkmcnt(4)\n\t"
    "v_mfma_f32_16x16x32_bf16 v[80:83], v[192:195], v[96:99], v[80:83]\n\t"
    "ds_read_b128 v[120:123], %[da] offset:20480\n\t"
    "v_mfma_f32_16x16x32_bf16 v[84:87], v[196:199], v[96:99], v[84:87]\n\t"
    "ds_read_b128 v[124:127], %[da] offset:22528\n\t"
    "v_mfma_f32_16x16x32_bf16 v[88:91], v[192:195], v[100:103], v[88:91]\n\t"
    "v_mfma_f32_16x16x32_bf16 v[92:95], v[196:199], v[100:103], v[92:95]\n\t"
    "s_waitcnt vmcnt(16)\n\t"
    "s_cmp_lt_u32 %[x], 2\n\t"
    "s_cbranch_scc0 49f\n\t"
    "s_waitcnt vmcnt(0)\n\t"
    "49:\n\t"
    "s_barrier\n\t"
    "ds_read_b32 v79, %[sa]\n\t"
    "s_waitcnt lgkmcnt(5)\n\t"
    "v_mfma_f32_16x16x32_bf16 v[80:83], v[200:203], v[104:107], v[80:83]\n\t"
    "ds_read_b128 v[96:99], %[ea] offset:24576\n\t"
    "v_mfma_f32_16x16x32_bf16 v[84:87], v[204:207], v[104:107], v[84:87]\n\t"
    "ds_read_b128 v[100:103], %[ea] offset:26624\n\t"
    "v_mfma_f32_16x16x32_bf16 v[88:91], v[200:203], v[108:111], v[88:91]\n\t"
    "v_mfma_f32_16x16x32_bf16 v[92:95], v[204:207], v[108:111], v[92:95]\n\t"
    "s_waitcnt lgkmcnt(5)\n\t"
    "v_mfma_f32_16x16x32_bf16 v[80:83], v[208:211], v[112:115], v[80:83]\n\t"
    "ds_read_b128 v[104:107], %[da] offset:24576\n\t"
    "v_mfma_f32_16x16x32_bf16 v[84:87], v[212:215], v[112:115], v[84:87]\n\t"
    "ds_read_b128 v[108:111], %[da] offset:26624\n\t"
    "v_mfma_f32_16x16x32_bf16 v[88:91], v[208:211], v[116:119], v[88:91]\n\t"
    "v_mfma_f32_16x16x32_bf16 v[92:95], v[212:215], v[116:119], v[92:95]\n\t"
    "s_cmp_eq_u32 %[s2], 0\n\t"
    "s_cbranch_scc1 7f\n\t"
    "s_mov_b32 m0, %[s2]\n\t"
    "s_nop 0\n\t"
    "global_load_lds_dword %[dv], %[db] sc1\n\t"
    "7:\n\t"
    "s_waitcnt lgkmcnt(4)\n\t"
    "v_mfma_f32_16x16x32_bf16 v[80:83], v[216:219], v[120:123], v[80:83]\n\t"
    "ds_read_b128 v[112:115], %[ea] offset:28672\n\t"
    "v_mfma_f32_16x16x32_bf16 v[84:87], v[220:223], v[120:123], v[84:87]\n\t"
    "ds_read_b128 v[116:119], %[ea] offset:30720\n\t"
    "v_mfma_f32_16x16x32_bf16 v[88:91], v[216:219], v[124:127], v[88:91]\n\t"
    "v_mad_u32_u24 v79, v79, %[mu], %[ad]\n\t"
    "v_cmp_ge_i32 vcc, %[th], v79\n\t"
    "v_mfma_f32_16x16x32_bf16 v[92:95], v[220:223], v[124:127], v[92:95]\n\t"
    "s_cbranch_vccz 5f\n\t"
    "s_waitcnt lgkmcnt(4)\n\t"
    "v_mfma_f32_16x16x32_bf16 v[80:83], v[224:227], v[96:99], v[80:83]\n\t"
    "ds_read_b128 v[120:123], %[da] offset:28672\n\t"
    "v_mfma_f32_16x16x32_bf16 v[84:87], v[228:231], v[96:99], v[84:87]\n\t"
    "ds_read_b128 v[124:127], %[da] offset:30720\n\t"
    "v_mfma_f32_16x16x32_bf16 v[88:91], v[224:227], v[100:103], v[88:91]\n\t"
    "v_mfma_f32_16x16x32_bf16 v[92:95], v[228:231], v[100:103], v[92:95]\n\t"
    "s_waitcnt lgkmcnt(4)\n\t"
    "v_mfma_f32_16x16x32_bf16 v[80:83], v[232:235], v[104:107], v[80:83]\n\t"
    "v_mfma_f32_16x16x32_bf16 v[84:87], v[236:239], v[104:107], v[84:87]\n\t"
    "v_mfma_f32_16x16x32_bf16 v[88:91], v[232:235], v[108:111], v[88:91]\n\t"
    "v_mfma_f32_16x16x32_bf16 v[92:95], v[236:239], v[108:111], v[92:95]\n\t"
    "s_waitcnt lgkmcnt(2)\n\t"
    "v_mfma_f32_16x16x32_bf16 v[80:83], v[240:243], v[112:115], v[80:83]\n\t"
    "v_mfma_f32_16x16x32_bf16 v[84:87], v[244:247], v[112:115], v[84:87]\n\t"
    "v_mfma_f32_16x16x32_bf16 v[88:91], v[240:243], v[116:119], v[88:91]\n\t"
    "v_mfma_f32_16x16x32_bf16 v[92:95], v[244:247], v[116:119], v[92:95]\n\t"
    "s_waitcnt lgkmcnt(0)\n\t"
    "v_mfma_f32_16x16x32_bf16 v[80:83], v[248:251], v[120:123], v[80:83]\n\t"
    "v_mfma_f32_16x16x32_bf16 v[84:87], v[252:255], v[120:123], v[84:87]\n\t"
    "v_mfma_f32_16x16x32_bf16 v[88:91], v[248:251], v[124:127], v[88:91]\n\t"
    "v_mfma_f32_16x16x32_bf16 v[92:95], v[252:255], v[124:127], v[92:95]\n\t"
    "s_branch 6f\n\t"
    "5:\n\t"
    "s_waitcnt lgkmcnt(4)\n\t"
    "v_mfma_f32_16x16x32_bf16 v[80:83], v[224:227], v[96:99], v[80:83]\n\t"
    "ds_read_b128 v[120:123], %[da] offset:28672\n\t"
    "s_add_u32 m0, %[lb], 0\n\t"
    "s_nop 0\n\t"
    "global_load_lds_dwordx4 %[av0], %[s0] sc1\n\t"
    "v_mfma_f32_16x16x32_bf16 v[84:87], v[228:231], v[96:99], v[84:87]\n\t"
    "ds_read_b128 v[124:127], %[da] offset:30720\n\t"
    "s_add_u32 m0, %[lb], 32768\n\t"
    "s_nop 0\n\t"
    "global_load_lds_dwordx4 %[av1], %[s0] sc1\n\t"
    "v_mfma_f32_16x16x32_bf16 v[88:91], v[224:227], v[100:103], v[88:91]\n\t"
    "s_add_u32 m0, %[lb], 4096\n\t"
    "s_nop 0\n\t"
    "global_load_lds_dwordx4 %[av2], %[s0] sc1\n\t"
    "v_mfma_f32_16x16x32_bf16 v[92:95], v[228:231], v[100:103], v[92:95]\n\t"
    "s_add_u32 m0, %[lb], 36864\n\t"
    "s_nop 0\n\t"
    "global_load_lds_dwordx4 %[av3], %[s0] sc1\n\t"
    "s_waitcnt lgkmcnt(4)\n\t"
    "v_mfma_f32_16x16x32_bf16 v[80:83], v[232:235], v[104:107], v[80:83]\n\t"
    "s_add_u32 m0, %[lb], 8192\n\t"
    "s_nop 0\n\t"
    "global_load_lds_dwordx4 %[av4], %[s0] sc1\n\t"
    "v_mfma_f32_16x16x32_bf16 v[84:87], v[236:239], v[104:107], v[84:87]\n\t"
    "s_add_u32 m0, %[lb], 40960\n\t"
    "s_nop 0\n\t"
    "global_load_lds_dwordx4 %[av5], %[s0] sc1\n\t"
    "v_mfma_f32_16x16x32_bf16 v[88:91], v[232:235], v[108:111], v[88:91]\n\t"
    "s_add_u32 m0, %[lb], 12288\n\t"
    "s_nop 0\n\t"
    "global_load_lds_dwordx4 %[av6], %[s0] sc1\n\t"
    "v_mfma_f32_16x16x32_bf16 v[92:95], v[236:239], v[108:111], v[92:95]\n\t"
    "s_add_u32 m0, %[lb], 45056\n\t"
    "s_nop 0\n\t"
    "global_load_lds_dwordx4 %[av7], %[s0] sc1\n\t"
    "s_waitcnt lgkmcnt(2)\n\t"
    "v_mfma_f32_16x16x32_bf16 v[80:83], v[240:243], v[112:115], v[80:83]\n\t"
    "s_add_u32 m0, %[lb], 16384\n\t"
    "s_nop 0\n\t"
    "global_load_lds_dwordx4 %[av0], %[s1] sc1\n\t"
    "v_mfma_f32_16x16x32_bf16 v[84:87], v[244:247], v[112:115], v[84:87]\n\t"
    "s_add_u32 m0, %[lb], 49152\n\t"
    "s_nop 0\n\t"
    "global_load_lds_dwordx4 %[av1], %[s1] sc1\n\t"
    "v_mfma_f32_16x16x32_bf16 v[88:91], v[240:243], v[116:119], v[88:91]\n\t"
    "s_add_u32 m0, %[lb], 20480\n\t"
    "s_nop 0\n\t"
    "global_load_lds_dwordx4 %[av2], %[s1] sc1\n\t"
    "v_mfma_f32_16x16x32_bf16 v[92:95], v[244:247], v[116:119], v[92:95]\n\t"
    "s_add_u32 m0, %[lb], 53248\n\t"
    "s_nop 0\n\t"
    "global_load_lds_dwordx4 %[av3], %[s1] sc1\n\t"
    "s_waitcnt lgkmcnt(0)\n\t"
    "v_mfma_f32_16x16x32_bf16 v[80:83], v[248:251], v[120:123], v[80:83]\n\t"
    "s_add_u32 m0, %[lb], 24576\n\t"
    "s_nop 0\n\t"
    "global_load_lds_dwordx4 %[av4], %[s1] sc1\n\t"
    "v_mfma_f32_16x16x32_bf16 v[84:87], v[252:255], v[120:123], v[84:87]\n\t"
    "s_add_u32 m0, %[lb], 57344\n\t"
    "s_nop 0\n\t"
    "global_load_lds_dwordx4 %[av5], %[s1] sc1\n\t"
    "v_mfma_f32_16x16x32_bf16 v[88:91], v[248:251], v[124:127], v[88:91]\n\t"
    "s_add_u32 m0, %[lb], 28672\n\t"
    "s_nop 0\n\t"
    "global_load_lds_dwordx4 %[av6], %[s1] sc1\n\t"
    "v_mfma_f32_16x16x32_bf16 v[92:95], v[252:255], v[124:127], v[92:95]\n\t"
    "s_add_u32 m0, %[lb], 61440\n\t"
    "s_nop 0\n\t"
    "global_load_lds_dwordx4 %[av7], %[s1] sc1\n\t"
    "6:\n\t"
    "s_nop 6\n\t"
    "s_branch 9f\n\t"
    "3:\n\t"
    "9:"
    : "=&{v[80:95]}"(pd)
    : [ea] "v"(ea), [da] "v"(da), [x] "s"(x), "{v[128:159]}"(W[0]), "{v[160:191]}"(W[1]), "{v[192:223]}"(W[2]), "{v[224:255]}"(W[3]), [lb] "s"(lb), [s0] "s"(s0), [s1] "s"(s1), [av0] "v"(av[0]), [av1] "v"(av[1]), [av2] "v"(av[2]), [av3] "v"(av[3]), [av4] "v"(av[4]), [av5] "v"(av[5]), [av6] "v"(av[6]), [av7] "v"(av[7]), [sa] "v"(sa), [mu] "s"(mu), [ad] "v"(ad), [th] "s"(th), [s2] "s"(s2), [dv] "v"(dv), [db] "s"(db)
    : "v96", "v97", "v98", "v99", "v100", "v101", "v102", "v103", "v104", "v105", "v106", "v107", "v108", "v109", "v110", "v111", "v112", "v113", "v114", "v115", "v116", "v117", "v118", "v119", "v120", "v121", "v122", "v123", "v124", "v125", "v126", "v127", "v79", "vcc", "scc", "m0", "memory");
}
__device__ __forceinline__ void lean_dm0(const u32x32 (&W)[4], u32x16& pd, unsigned ea, unsigned da, unsigned x, unsigned lb, const char* s0, const char* s1, const unsigned (&av)[8]) {
  asm volatile("s_setprio 2\n\t"
    "ds_read_b128 v[96:99], %[ea] offset:0\n\t"
    "ds_read_b128 v[100:103], %[ea] offset:2048\n\t"
    "ds_read_b128 v[104:107], %[da] offset:0\n\t"
    "ds_read_b128 v[108:111], %[da] offset:2048\n\t"
    "ds_read_b128 v[112:115], %[ea] offset:4096\n\t"
    "ds_read_b128 v[116:119], %[ea] offset:6144\n\t"
    "s_waitcnt lgkmcnt(4)\n\t"
    "v_mfma_f32_16x16x32_bf16 v[80:83], v[128:131], v[96:99], 0\n\t"
    "ds_read_b128 v[120:123], %[da] offset:4096\n\t"
    "s_add_u32 m0, %[lb], 0\n\t"
    "v_mfma_f32_16x16x32_bf16 v[84:87], v[132:135], v[96:99], 0\n\t"
    "ds_read_b128 v[124:127], %[da] offset:6144\n\t"
    "global_load_lds_dwordx4 %[av0], %[s0] sc1\n\t"
    "v_mfma_f32_16x16x32_bf16 v[88:91], v[128:131], v[100:103], 0\n\t"
    "s_add_u32 m0, %[lb], 32768\n\t"
    "v_mfma_f32_16x16x32_bf16 v[92:95], v[132:135], v[100:103], 0\n\t"
    "global_load_lds_dwordx4 %[av1], %[s0] sc1\n\t"
    "s_waitcnt vmcnt(10)\n\t"
    "s_barrier\n\t"
    "s_waitcnt lgkmcnt(4)\n\t"
    "v_mfma_f32_16x16x32_bf16 v[80:83], v[136:139], v[104:107], v[80:83]\n\t"
    "ds_read_b128 v[96:99], %[ea] offset:8192\n\t"
    "s_add_u32 m0, %[lb], 4096\n\t"
    "v_mfma_f32_16x16x32_bf16 v[84:87], v[140:143], v[104:107], v[84:87]\n\t"
    "ds_read_b128 v[100:103], %[ea] offset:10240\n\t"
    "global_load_lds_dwordx4 %[av2], %[s0] sc1\n\t"
    "v_mfma_f32_16x16x32_bf16 v[88:91], v[136:139], v[108:111], v[88:91]\n\t"
    "s_add_u32 m0, %[lb], 36864\n\t"
    "v_mfma_f32_16x16x32_bf16 v[92:95], v[140:143], v[108:111], v[92:95]\n\t"
    "global_load_lds_dwordx4 %[av3], %[s0] sc1\n\t"
    "s_waitcnt lgkmcnt(4)\n\t"
    "v_mfma_f32_16x16x32_bf16 v[80:83], v[144:147], v[112:115], v[80:83]\n\t"
    "ds_read_b128 v[104:107], %[da] offset:8192\n\t"
    "s_add_u32 m0, %[lb], 8192\n\t"
    "v_mfma_f32_16x16x32_bf16 v[84:87], v[148:151], v[112:115], v[84:87]\n\t"
    "ds_read_b128 v[108:111], %[da] offset:10240\n\t"
    "global_load_lds_dwordx4 %[av4], %[s0] sc1\n\t"
    "v_mfma_f32_16x16x32_bf16 v[88:91], v[144:147], v[116:119], v[88:91]\n\t"
    "s_add_u32 m0, %[lb], 40960\n\t"
    "v_mfma_f32_16x16x32_bf16 v[92:95], v[148:151], v[116:119], v[92:95]\n\t"
    "global_load_lds_dwordx4 %[av5], %[s0] sc1\n\t"
    "s_waitcnt lgkmcnt(4)\n\t"
    "v_mfma_f32_16x16x32_bf16 v[80:83], v[152:155], v[120:123], v[80:83]\n\t"
    "ds_read_b128 v[112:115], %[ea] offset:12288\n\t"
    "s_add_u32 m0, %[lb], 12288\n\t"
    "v_mfma_f32_16x16x32_bf16 v[84:87], v[156:159], v[120:123], v[84:87]\n\t"
    "ds_read_b128 v[116:119], %[ea] offset:14336\n\t"
    "global_load_lds_dwordx4 %[av6], %[s0] sc1\n\t"
    "v_mfma_f32_16x16x32_bf16 v[88:91], v[152:155], v[124:127], v[88:91]\n\t"
    "s_add_u32 m0, %[lb], 45056\n\t"
    "v_mfma_f32_16x16x32_bf16 v[92:95], v[156:159], v[124:127], v[92:95]\n\t"
    "global_load_lds_dwordx4 %[av7], %[s0] sc1\n\t"
    "s_waitcnt lgkmcnt(4)\n\t"
    "v_mfma_f32_16x16x32_bf16 v[80:83], v[160:163], v[96:99], v[80:83]\n\t"
    "ds_read_b128 v[120:123], %[da] offset:12288\n\t"
    "s_add_u32 m0, %[lb], 16384\n\t"
    "v_mfma_f32_16x16x32_bf16 v[84:87], v[164:167], v[96:99], v[84:87]\n\t"
    "ds_read_b128 v[124:127], %[da] offset:14336\n\t"
    "global_load_lds_dwordx4 %[av0], %[s1] sc1\n\t"
    "v_mfma_f32_16x16x32_bf16 v[88:91], v[160:163], v[100:103], v[88:91]\n\t"
    "s_add_u32 m0, %[lb], 49152\n\t"
    "v_mfma_f32_16x16x32_bf16 v[92:95], v[164:167], v[100:103], v[92:95]\n\t"
    "global_load_lds_dwordx4 %[av1], %[s1] sc1\n\t"
    "s_waitcnt vmcnt(14)\n\t"
    "s_barrier\n\t"
    "s_waitcnt lgkmcnt(4)\n\t"
    "v_mfma_f32_16x16x32_bf16 v[80:83], v[168:171], v[104:107], v[80:83]\n\t"
    "ds_read_b128 v[96:99], %[ea] offset:16384\n\t"
    "s_add_u32 m0, %[lb], 20480\n\t"
    "v_mfma_f32_16x16x32_bf16 v[84:87], v[172:175], v[104:107], v[84:87]\n\t"
    "ds_read_b128 v[100:103], %[ea] offset:18432\n\t"
    "global_load_lds_dwordx4 %[av2], %[s1] sc1\n\t"
    "v_mfma_f32_16x16x32_bf16 v[88:91], v[168:171], v[108:111], v[88:91]\n\t"
    "s_add_u32 m0, %[lb], 53248\n\t"
    "v_mfma_f32_16x16x32_bf16 v[92:95], v[172:175], v[108:111], v[92:95]\n\t"
    "global_load_lds_dwordx4 %[av3], %[s1] sc1\n\t"
    "s_waitcnt lgkmcnt(4)\n\t"
    "v_mfma_f32_16x16x32_bf16 v[80:83], v[176:179], v[112:115], v[80:83]\n\t"
    "ds_read_b128 v[104:107], %[da] offset:16384\n\t"
    "s_add_u32 m0, %[lb], 24576\n\t"
    "v_mfma_f32_16x16x32_bf16 v[84:87], v[180:183], v[112:115], v[84:87]\n\t"
    "ds_read_b128 v[108:111], %[da] offset:18432\n\t"
    "global_load_lds_dwordx4 %[av4], %[s1] sc1\n\t"
    "v_mfma_f32_16x16x32_bf16 v[88:91], v[176:179], v[116:119], v[88:91]\n\t"
    "s_add_u32 m0, %[lb], 57344\n\t"
    "v_mfma_f32_16x16x32_bf16 v[92:95], v[180:183], v[116:119], v[92:95]\n\t"
    "global_load_lds_dwordx4 %[av5], %[s1] sc1\n\t"
    "s_waitcnt lgkmcnt(4)\n\t"
    "v_mfma_f32_16x16x32_bf16 v[80:83], v[184:187], v[120:123], v[80:83]\n\t"
    "ds_read_b128 v[112:115], %[ea] offset:20480\n\t"
    "s_add_u32 m0, %[lb], 28672\n\t"
    "v_mfma_f32_16x16x32_bf16 v[84:87], v[188:191], v[120:123], v[84:87]\n\t"
    "ds_read_b128 v[116:119], %[ea] offset:22528\n\t"
    "global_load_lds_dwordx4 %[av6], %[s1] sc1\n\t"
    "v_mfma_f32_16x16x32_bf16 v[88:91], v[184:187], v[124:127], v[88:91]\n\t"
    "s_add_u32 m0, %[lb], 61440\n\t"
    "v_mfma_f32_16x16x32_bf16 v[92:95], v[188:191], v[124:127], v[92:95]\n\t"
    "global_load_lds_dwordx4 %[av7], %[s1] sc1\n\t"
    "s_waitcnt lgkmcnt(4)\n\t"
    "v_mfma_f32_16x16x32_bf16 v[80:83], v[192:195], v[96:99], v[80:83]\n\t"
    "ds_read_b128 v[120:123], %[da] offset:20480\n\t"
    "v_mfma_f32_16x16x32_bf16 v[84:87], v[196:199], v[96:99], v[84:87]\n\t"
    "ds_read_b128 v[124:127], %[da] offset:22528\n\t"
    "v_mfma_f32_16x16x32_bf16 v[88:91], v[192:195], v[100:103], v[88:91]\n\t"
    "v_mfma_f32_16x16x32_bf16 v[92:95], v[196:199], v[100:103], v[92:95]\n\t"
    "s_waitcnt vmcnt(16)\n\t"
    "s_barrier\n\t"
    "s_waitcnt lgkmcnt(4)\n\t"
    "v_mfma_f32_16x16x32_bf16 v[80:83], v[200:203], v[104:107], v[80:83]\n\t"
    "ds_read_b128 v[96:99], %[ea] offset:24576\n\t"
    "v_mfma_f32_16x16x32_bf16 v[84:87], v[204:207], v[104:107], v[84:87]\n\t"
    "ds_read_b128 v[100:103], %[ea] offset:26624\n\t"
    "v_mfma_f32_16x16x32_bf16 v[88:91], v[200:203], v[108:111], v[88:91]\n\t"
    "v_mfma_f32_16x16x32_bf16 v[92:95], v[204:207], v[108:111], v[92:95]\n\t"
    "s_waitcnt lgkmcnt(4)\n\t"
    "v_mfma_f32_16x16x32_bf16 v[80:83], v[208:211], v[112:115], v[80:83]\n\t"
    "ds_read_b128 v[104:107], %[da] offset:24576\n\t"
    "v_mfma_f32_16x16x32_bf16 v[84:87], v[212:215], v[112:115], v[84:87]\n\t"
    "ds_read_b128 v[108:111], %[da] offset:26624\n\t"
    "v_mfma_f32_16x16x32_bf16 v[88:91], v[208:211], v[116:119], v[88:91]\n\t"
    "v_mfma_f32_16x16x32_bf16 v[92:95], v[212:215], v[116:119], v[92:95]\n\t"
    "s_waitcnt lgkmcnt(4)\n\t"
    "v_mfma_f32_16x16x32_bf16 v[80:83], v[216:219], v[120:123], v[80:83]\n\t"
    "ds_read_b128 v[112:115], %[ea] offset:28672\n\t"
    "v_mfma_f32_16x16x32_bf16 v[84:87], v[220:223], v[120:123], v[84:87]\n\t"
    "ds_read_b128 v[116:119], %[ea] offset:30720\n\t"
    "v_mfma_f32_16x16x32_bf16 v[88:91], v[216:219], v[124:127], v[88:91]\n\t"
    "v_mfma_f32_16x16x32_bf16 v[92:95], v[220:223], v[124:127], v[92:95]\n\t"
    "s_waitcnt lgkmcnt(4)\n\t"
    "v_mfma_f32_16x16x32_bf16 v[80:83], v[224:227], v[96:99], v[80:83]\n\t"
    "ds_read_b128 v[120:123], %[da] offset:28672\n\t"
    "v_mfma_f32_16x16x32_bf16 v[84:87], v[228:231], v[96:99], v[84:87]\n\t"
    "ds_read_b128 v[124:127], %[da] offset:30720\n\t"
    "v_mfma_f32_16x16x32_bf16 v[88:91], v[224:227], v[100:103], v[88:91]\n\t"
    "v_mfma_f32_16x16x32_bf16 v[92:95], v[228:231], v[100:103], v[92:95]\n\t"
    "s_waitcnt lgkmcnt(4)\n\t"
    "v_mfma_f32_16x16x32_bf16 v[80:83], v[232:235], v[104:107], v[80:83]\n\t"
    "v_mfma_f32_16x16x32_bf16 v[84:87], v[236:239], v[104:107], v[84:87]\n\t"
    "v_mfma_f32_16x16x32_bf16 v[88:91], v[232:235], v[108:111], v[88:91]\n\t"
    "v_mfma_f32_16x16x32_bf16 v[92:95], v[236:239], v[108:111], v[92:95]\n\t"
    "s_waitcnt lgkmcnt(2)\n\t"
    "v_mfma_f32_16x16x32_bf16 v[80:83], v[240:243], v[112:115], v[80:83]\n\t"
    "v_mfma_f32_16x16x32_bf16 v[84:87], v[244:247], v[112:115], v[84:87]\n\t"
    "v_mfma_f32_16x16x32_bf16 v[88:91], v[240:243], v[116:119], v[88:91]\n\t"
    "v_mfma_f32_16x16x32_bf16 v[92:95], v[244:247], v[116:119], v[92:95]\n\t"
    "s_waitcnt lgkmcnt(0)\n\t"
    "v_mfma_f32_16x16x32_bf16 v[80:83], v[248:251], v[120:123], v[80:83]\n\t"
    "v_mfma_f32_16x16x32_bf16 v[84:87], v[252:255], v[120:123], v[84:87]\n\t"
    "v_mfma_f32_16x16x32_bf16 v[88:91], v[248:251], v[124:127], v[88:91]\n\t"
    "v_mfma_f32_16x16x32_bf16 v[92:95], v[252:255], v[124:127], v[92:95]\n\t"
    "s_nop 6\n\t"
    "s_branch 9f\n\t"
    "3:\n\t"
    "9:"
    : "=&{v[80:95]}"(pd)
    : [ea] "v"(ea), [da] "v"(da), [x] "s"(x), "{v[128:159]}"(W[0]), "{v[160:191]}"(W[1]), "{v[192:223]}"(W[2]), "{v[224:255]}"(W[3]), [lb] "s"(lb), [s0] "s"(s0), [s1] "s"(s1), [av0] "v"(av[0]), [av1] "v"(av[1]), [av2] "v"(av[2]), [av3] "v"(av[3]), [av4] "v"(av[4]), [av5] "v"(av[5]), [av6] "v"(av[6]), [av7] "v"(av[7])
    : "v96", "v97", "v98", "v99", "v100", "v101", "v102", "v103", "v104", "v105", "v106", "v107", "v108", "v109", "v110", "v111", "v112", "v113", "v114", "v115", "v116", "v117", "v118", "v119", "v120", "v121", "v122", "v123", "v124", "v125", "v126", "v127", "vcc", "scc", "m0", "memory");
}
__device__ __forceinline__ void lean_ld1(const u32x32 (&W)[4], u32x16& pd, unsigned ea, unsigned da, unsigned x, unsigned lb, const char* s0, const char* s1, const unsigned (&av)[8], unsigned sa, unsigned mu, unsigned ad, int th, unsigned s2, unsigned dv, const char* db) {
  asm volatile("s_setprio 2\n\t"
    "ds_read_b128 v[96:99], %[ea] offset:0\n\t"
    "ds_read_b128 v[100:103], %[ea] offset:2048\n\t"
    "ds_read_b128 v[104:107], %[da] offset:0\n\t"
    "ds_read_b128 v[108:111], %[da] offset:2048\n\t"
    "ds_read_b128 v[112:115], %[ea] offset:4096\n\t"
    "ds_read_b128 v[116:119], %[ea] offset:6144\n\t"
    "s_waitcnt lgkmcnt(4)\n\t"
    "v_mfma_f32_16x16x32_bf16 v[80:83], v[128:131], v[96:99], 0\n\t"
    "ds_read_b128 v[120:123], %[da] offset:4096\n\t"
    "v_mfma_f32_16x16x32_bf16 v[84:87], v[132:135], v[96:99], 0\n\t"
    "ds_read_b128 v[124:127], %[da] offset:6144\n\t"
    "v_mfma_f32_16x16x32_bf16 v[88:91], v[128:131], v[100:103], 0\n\t"
    "v_mfma_f32_16x16x32_bf16 v[92:95], v[132:135], v[100:103], 0\n\t"
    "s_waitcnt vmcnt(12)\n\t"
    "s_cmp_lt_u32 %[x], 2\n\t"
    "s_cbranch_scc0 41f\n\t"
    "s_waitcnt vmcnt(4)\n\t"
    "41:\n\t"
    "s_barrier\n\t"
    "s_waitcnt lgkmcnt(4)\n\t"
    "v_mfma_f32_16x16x32_bf16 v[80:83], v[136:139], v[104:107], v[80:83]\n\t"
    "ds_read_b128 v[96:99], %[ea] offset:8192\n\t"
    "v_mfma_f32_16x16x32_bf16 v[84:87], v[140:143], v[104:107], v[84:87]\n\t"
    "ds_read_b128 v[100:103], %[ea] offset:10240\n\t"
    "v_mfma_f32_16x16x32_bf16 v[88:91], v[136:139], v[108:111], v[88:91]\n\t"
    "v_mfma_f32_16x16x32_bf16 v[92:95], v[140:143], v[108:111], v[92:95]\n\t"
    "s_waitcnt lgkmcnt(4)\n\t"
    "v_mfma_f32_16x16x32_bf16 v[80:83], v[144:147], v[112:115], v[80:83]\n\t"
    "ds_read_b128 v[104:107], %[da] offset:8192\n\t"
    "v_mfma_f32_16x16x32_bf16 v[84:87], v[148:151], v[112:115], v[84:87]\n\t"
    "ds_read_b128 v[108:111], %[da] offset:10240\n\t"
    "v_mfma_f32_16x16x32_bf16 v[88:91], v[144:147], v[116:119], v[88:91]\n\t"
    "v_mfma_f32_16x16x32_bf16 v[92:95], v[148:151], v[116:119], v[92:95]\n\t"
    "s_waitcnt lgkmcnt(4)\n\t"
    "v_mfma_f32_16x16x32_bf16 v[80:83], v[152:155], v[120:123], v[80:83]\n\t"
    "ds_read_b128 v[112:115], %[ea] offset:12288\n\t"
    "v_mfma_f32_16x16x32_bf16 v[84:87], v[156:159], v[120:123], v[84:87]\n\t"
    "ds_read_b128 v[116:119], %[ea] offset:14336\n\t"
    "v_mfma_f32_16x16x32_bf16 v[88:91], v[152:155], v[124:127], v[88:91]\n\t"
    "v_mfma_f32_16x16x32_bf16 v[92:95], v[156:159], v[124:127], v[92:95]\n\t"
    "s_waitcnt lgkmcnt(4)\n\t"
    "v_mfma_f32_16x16x32_bf16 v[80:83], v[160:163], v[96:99], v[80:83]\n\t"
    "ds_read_b128 v[120:123], %[da] offset:12288\n\t"
    "v_mfma_f32_16x16x32_bf16 v[84:87], v[164:167], v[96:99], v[84:87]\n\t"
    "ds_read_b128 v[124:127], %[da] offset:14336\n\t"
    "v_mfma_f32_16x16x32_bf16 v[88:91], v[160:163], v[100:103], v[88:91]\n\t"
    "v_mfma_f32_16x16x32_bf16 v[92:95], v[164:167], v[100:103], v[92:95]\n\t"
    "s_waitcnt vmcnt(10)\n\t"
    "s_cmp_lt_u32 %[x], 2\n\t"
    "s_cbranch_scc0 45f\n\t"
    "s_waitcnt vmcnt(2)\n\t"
    "45:\n\t"
    "s_barrier\n\t"
    "s_waitcnt lgkmcnt(4)\n\t"
    "v_mfma_f32_16x16x32_bf16 v[80:83], v[168:171], v[104:107], v[80:83]\n\t"
    "ds_read_b128 v[96:99], %[ea] offset:16384\n\t"
    "v_mfma_f32_16x16x32_bf16 v[84:87], v[172:175], v[104:107], v[84:87]\n\t"
    "ds_read_b128 v[100:103], %[ea] offset:18432\n\t"
    "v_mfma_f32_16x16x32_bf16 v[88:91], v[168:171], v[108:111], v[88:91]\n\t"
    "v_mfma_f32_16x16x32_bf16 v[92:95], v[172:175], v[108:111], v[92:95]\n\t"
    "s_waitcnt lgkmcnt(4)\n\t"
    "v_mfma_f32_16x16x32_bf16 v[80:83], v[176:179], v[112:115], v[80:83]\n\t"
    "ds_read_b128 v[104:107], %[da] offset:16384\n\t"
    "v_mfma_f32_16x16x32_bf16 v[84:87], v[180:183], v[112:115], v[84:87]\n\t"
    "ds_read_b128 v[108:111], %[da] offset:18432\n\t"
    "v_mfma_f32_16x16x32_bf16 v[88:91], v[176:179], v[116:119], v[88:91]\n\t"
    "v_mfma_f32_16x16x32_bf16 v[92:95], v[180:183], v[116:119], v[92:95]\n\t"
    "s_waitcnt lgkmcnt(4)\n\t"
    "v_mfma_f32_16x16x32_bf16 v[80:83], v[184:187], v[120:123], v[80:83]\n\t"
    "ds_read_b128 v[112:115], %[ea] offset:20480\n\t"
    "v_mfma_f32_16x16x32_bf16 v[84:87], v[188:191], v[120:123], v[84:87]\n\t"
    "ds_read_b128 v[116:119], %[ea] offset:22528\n\t"
    "v_mfma_f32_16x16x32_bf16 v[88:91], v[184:187], v[124:127], v[88:91]\n\t"
    "v_mfma_f32_16x16x32_bf16 v[92:95], v[188:191], v[124:127], v[92:95]\n\t"
    "s_waitcnt lgkmcnt(4)\n\t"
    "v_mfma_f32_16x16x32_bf16 v[80:83], v[192:195], v[96:99], v[80:83]\n\t"
    "ds_read_b128 v[120:123], %[da] offset:20480\n\t"
    "v_mfma_f32_16x16x32_bf16 v[84:87], v[196:199], v[96:99], v[84:87]\n\t"
    "ds_read_b128 v[124:127], %[da] offset:22528\n\t"
    "v_mfma_f32_16x16x32_bf16 v[88:91], v[192:195], v[100:103], v[88:91]\n\t"
    "v_mfma_f32_16x16x32_bf16 v[92:95], v[196:199], v[100:103], v[92:95]\n\t"
    "s_waitcnt vmcnt(8)\n\t"
    "s_cmp_lt_u32 %[x], 2\n\t"
    "s_cbranch_scc0 49f\n\t"
    "s_waitcnt vmcnt(0)\n\t"
    "49:\n\t"
    "s_barrier\n\t"
    "ds_read_b32 v79, %[sa]\n\t"
    "s_waitcnt lgkmcnt(5)\n\t"
    "v_mfma_f32_16x16x32_bf16 v[80:83], v[200:203], v[104:107], v[80:83]\n\t"
    "ds_read_b128 v[96:99], %[ea] offset:24576\n\t"
    "v_mfma_f32_16x16x32_bf16 v[84:87], v[204:207], v[104:107], v[84:87]\n\t"
    "ds_read_b128 v[100:103], %[ea] offset:26624\n\t"
    "v_mfma_f32_16x16x32_bf16 v[88:91], v[200:203], v[108:111], v[88:91]\n\t"
    "v_mfma_f32_16x16x32_bf16 v[92:95], v[204:207], v[108:111], v[92:95]\n\t"
    "s_waitcnt lgkmcnt(5)\n\t"
    "v_mfma_f32_16x16x32_bf16 v[80:83], v[208:211], v[112:115], v[80:83]\n\t"
    "ds_read_b128 v[104:107], %[da] offset:24576\n\t"
    "v_mfma_f32_16x16x32_bf16 v[84:87], v[212:215], v[112:115], v[84:87]\n\t"
    "ds_read_b128 v[108:111], %[da] offset:26624\n\t"
    "v_mfma_f32_16x16x32_bf16 v[88:91], v[208:211], v[116:119], v[88:91]\n\t"
    "v_mfma_f32_16x16x32_bf16 v[92:95], v[212:215], v[116:119], v[92:95]\n\t"
    "s_cmp_eq_u32 %[s2], 0\n\t"
    "s_cbranch_scc1 7f\n\t"
    "s_mov_b32 m0, %[s2]\n\t"
    "s_nop 0\n\t"
    "global_load_lds_dword %[dv], %[db] sc1\n\t"
    "7:\n\t"
    "s_waitcnt lgkmcnt(4)\n\t"
    "v_mfma_f32_16x16x32_bf16 v[80:83], v[216:219], v[120:123], v[80:83]\n\t"
    "ds_read_b128 v[112:115], %[ea] offset:28672\n\t"
    "v_mfma_f32_16x16x32_bf16 v[84:87], v[220:223], v[120:123], v[84:87]\n\t"
    "ds_read_b128 v[116:119], %[ea] offset:30720\n\t"
    "v_mfma_f32_16x16x32_bf16 v[88:91], v[216:219], v[124:127], v[88:91]\n\t"
    "v_mad_u32_u24 v79, v79, %[mu], %[ad]\n\t"
    "v_cmp_ge_i32 vcc, %[th], v79\n\t"
    "v_mfma_f32_16x16x32_bf16 v[92:95], v[220:223], v[124:127], v[92:95]\n\t"
    "s_cbranch_vccz 5f\n\t"
    "s_waitcnt lgkmcnt(4)\n\t"
    "v_mfma_f32_16x16x32_bf16 v[80:83], v[224:227], v[96:99], v[80:83]\n\t"
    "ds_read_b128 v[120:123], %[da] offset:28672\n\t"
    "v_mfma_f32_16x16x32_bf16 v[84:87], v[228:231], v[96:99], v[84:87]\n\t"
    "ds_read_b128 v[124:127], %[da] offset:30720\n\t"
    "v_mfma_f32_16x16x32_bf16 v[88:91], v[224:227], v[100:103], v[88:91]\n\t"
    "v_mfma_f32_16x16x32_bf16 v[92:95], v[228:231], v[100:103], v[92:95]\n\t"
    "s_waitcnt lgkmcnt(4)\n\t"
    "v_mfma_f32_16x16x32_bf16 v[80:83], v[232:235], v[104:107], v[80:83]\n\t"
    "v_mfma_f32_16x16x32_bf16 v[84:87], v[236:239], v[104:107], v[84:87]\n\t"
    "v_mfma_f32_16x16x32_bf16 v[88:91], v[232:235], v[108:111], v[88:91]\n\t"
    "v_mfma_f32_16x16x32_bf16 v[92:95], v[236:239], v[108:111], v[92:95]\n\t"
    "s_waitcnt lgkmcnt(2)\n\t"
    "v_mfma_f32_16x16x32_bf16 v[80:83], v[240:243], v[112:115], v[80:83]\n\t"
    "v_mfma_f32_16x16x32_bf16 v[84:87], v[244:247], v[112:115], v[84:87]\n\t"
    "v_mfma_f32_16x16x32_bf16 v[88:91], v[240:243], v[116:119], v[88:91]\n\t"
    "v_mfma_f32_16x16x32_bf16 v[92:95], v[244:247], v[116:119], v[92:95]\n\t"
    "s_waitcnt lgkmcnt(0)\n\t"
    "v_mfma_f32_16x16x32_bf16 v[80:83], v[248:251], v[120:123], v[80:83]\n\t"
    "v_mfma_f32_16x16x32_bf16 v[84:87], v[252:255], v[120:123], v[84:87]\n\t"
    "v_mfma_f32_16x16x32_bf16 v[88:91], v[248:251], v[124:127], v[88:91]\n\t"
    "v_mfma_f32_16x16x32_bf16 v[92:95], v[252:255], v[124:127], v[92:95]\n\t"
    "s_branch 6f\n\t"
    "5:\n\t"
    "s_waitcnt lgkmcnt(4)\n\t"
    "v_mfma_f32_16x16x32_bf16 v[80:83], v[224:227], v[96:99], v[80:83]\n\t"
    "ds_read_b128 v[120:123], %[da] offset:28672\n\t"
    "s_add_u32 m0, %[lb], 0\n\t"
    "s_nop 0\n\t"
    "global_load_lds_dwordx4 %[av0], %[s0] sc1\n\t"
    "v_mfma_f32_16x16x32_bf16 v[84:87], v[228:231], v[96:99], v[84:87]\n\t"
    "ds_read_b128 v[124:127], %[da] offset:30720\n\t"
    "v_mfma_f32_16x16x32_bf16 v[88:91], v[224:227], v[100:103], v[88:91]\n\t"
    "s_add_u32 m0, %[lb], 4096\n\t"
    "s_nop 0\n\t"
    "global_load_lds_dwordx4 %[av2], %[s0] sc1\n\t"
    "v_mfma_f32_16x16x32_bf16 v[92:95], v[228:231], v[100:103], v[92:95]\n\t"
    "s_waitcnt lgkmcnt(4)\n\t"
    "v_mfma_f32_16x16x32_bf16 v[80:83], v[232:235], v[104:107], v[80:83]\n\t"
    "s_add_u32 m0, %[lb], 8192\n\t"
    "s_nop 0\n\t"
    "global_load_lds_dwordx4 %[av4], %[s0] sc1\n\t"
    "v_mfma_f32_16x16x32_bf16 v[84:87], v[236:239], v[104:107], v[84:87]\n\t"
    "v_mfma_f32_16x16x32_bf16 v[88:91], v[232:235], v[108:111], v[88:91]\n\t"
    "s_add_u32 m0, %[lb], 12288\n\t"
    "s_nop 0\n\t"
    "global_load_lds_dwordx4 %[av6], %[s0] sc1\n\t"
    "v_mfma_f32_16x16x32_bf16 v[92:95], v[236:239], v[108:111], v[92:95]\n\t"
    "s_waitcnt lgkmcnt(2)\n\t"
    "v_mfma_f32_16x16x32_bf16 v[80:83], v[240:243], v[112:115], v[80:83]\n\t"
    "s_add_u32 m0, %[lb], 16384\n\t"
    "s_nop 0\n\t"
    "global_load_lds_dwordx4 %[av0], %[s1] sc1\n\t"
    "v_mfma_f32_16x16x32_bf16 v[84:87], v[244:247], v[112:115], v[84:87]\n\t"
    "v_mfma_f32_16x16x32_bf16 v[88:91], v[240:243], v[116:119], v[88:91]\n\t"
    "s_add_u32 m0, %[lb], 20480\n\t"
    "s_nop 0\n\t"
    "global_load_lds_dwordx4 %[av2], %[s1] sc1\n\t"
    "v_mfma_f32_16x16x32_bf16 v[92:95], v[244:247], v[116:119], v[92:95]\n\t"
    "s_waitcnt lgkmcnt(0)\n\t"
    "v_mfma_f32_16x16x32_bf16 v[80:83], v[248:251], v[120:123], v[80:83]\n\t"
    "s_add_u32 m0, %[lb], 24576\n\t"
    "s_nop 0\n\t"
    "global_load_lds_dwordx4 %[av4], %[s1] sc1\n\t"
    "v_mfma_f32_16x16x32_bf16 v[84:87], v[252:255], v[120:123], v[84:87]\n\t"
    "v_mfma_f32_16x16x32_bf16 v[88:91], v[248:251], v[124:127], v[88:91]\n\t"
    "s_add_u32 m0, %[lb], 28672\n\t"
    "s_nop 0\n\t"
    "global_load_lds_dwordx4 %[av6], %[s1] sc1\n\t"
    "v_mfma_f32_16x16x32_bf16 v[92:95], v[252:255], v[124:127], v[92:95]\n\t"
    "6:\n\t"
    "s_nop 6\n\t"
    "s_branch 9f\n\t"
    "3:\n\t"
    "9:"
    : "=&{v[80:95]}"(pd)
    : [ea] "v"(ea), [da] "v"(da), [x] "s"(x), "{v[128:159]}"(W[0]), "{v[160:191]}"(W[1]), "{v[192:223]}"(W[2]), "{v[224:255]}"(W[3]), [lb] "s"(lb), [s0] "s"(s0), [s1] "s"(s1), [av0] "v"(av[0]), [av1] "v"(av[1]), [av2] "v"(av[2]), [av3] "v"(av[3]), [av4] "v"(av[4]), [av5] "v"(av[5]), [av6] "v"(av[6]), [av7] "v"(av[7]), [sa] "v"(sa), [mu] "s"(mu), [ad] "v"(ad), [th] "s"(th), [s2] "s"(s2), [dv] "v"(dv), [db] "s"(db)
    : "v96", "v97", "v98", "v99", "v100", "v101", "v102", "v103", "v104", "v105", "v106", "v107", "v108", "v109", "v110", "v111", "v112", "v113", "v114", "v115", "v116", "v117", "v118", "v119", "v120", "v121", "v122", "v123", "v124", "v125", "v126", "v127", "v79", "vcc", "scc", "m0", "memory");
}
__device__ __forceinline__ void lean_dm1(const u32x32 (&W)[4], u32x16& pd, unsigned ea, unsigned da, unsigned x, unsigned lb, const char* s0, const char* s1, const unsigned (&av)[8]) {
  asm volatile("s_setprio 2\n\t"
    "ds_read_b128 v[96:99], %[ea] offset:0\n\t"
    "ds_read_b128 v[100:103], %[ea] offset:2048\n\t"
    "ds_read_b128 v[104:107], %[da] offset:0\n\t"
    "ds_read_b128 v[108:111], %[da] offset:2048\n\t"
    "ds_read_b128 v[112:115], %[ea] offset:4096\n\t"
    "ds_read_b128 v[116:119], %[ea] offset:6144\n\t"
    "s_waitcnt lgkmcnt(4)\n\t"
    "v_mfma_f32_16x16x32_bf16 v[80:83], v[128:131], v[96:99], 0\n\t"
    "ds_read_b128 v[120:123], %[da] offset:4096\n\t"
    "s_add_u32 m0, %[lb], 0\n\t"
    "v_mfma_f32_16x16x32_bf16 v[84:87], v[132:135], v[96:99], 0\n\t"
    "ds_read_b128 v[124:127], %[da] offset:6144\n\t"
    "global_load_lds_dwordx4 %[av0], %[s0] sc1\n\t"
    "v_mfma_f32_16x16x32_bf16 v[88:91], v[128:131], v[100:103], 0\n\t"
    "v_mfma_f32_16x16x32_bf16 v[92:95], v[132:135], v[100:103], 0\n\t"
    "s_waitcnt vmcnt(5)\n\t"
    "s_barrier\n\t"
    "s_waitcnt lgkmcnt(4)\n\t"
    "v_mfma_f32_16x16x32_bf16 v[80:83], v[136:139], v[104:107], v[80:83]\n\t"
    "ds_read_b128 v[96:99], %[ea] offset:8192\n\t"
    "s_add_u32 m0, %[lb], 4096\n\t"
    "v_mfma_f32_16x16x32_bf16 v[84:87], v[140:143], v[104:107], v[84:87]\n\t"
    "ds_read_b128 v[100:103], %[ea] offset:10240\n\t"
    "global_load_lds_dwordx4 %[av2], %[s0] sc1\n\t"
    "v_mfma_f32_16x16x32_bf16 v[88:91], v[136:139], v[108:111], v[88:91]\n\t"
    "v_mfma_f32_16x16x32_bf16 v[92:95], v[140:143], v[108:111], v[92:95]\n\t"
    "s_waitcnt lgkmcnt(4)\n\t"
    "v_mfma_f32_16x16x32_bf16 v[80:83], v[144:147], v[112:115], v[80:83]\n\t"
    "ds_read_b128 v[104:107], %[da] offset:8192\n\t"
    "s_add_u32 m0, %[lb], 8192\n\t"
    "v_mfma_f32_16x16x32_bf16 v[84:87], v[148:151], v[112:115], v[84:87]\n\t"
    "ds_read_b128 v[108:111], %[da] offset:10240\n\t"
    "global_load_lds_dwordx4 %[av4], %[s0] sc1\n\t"
    "v_mfma_f32_16x16x32_bf16 v[88:91], v[144:147], v[116:119], v[88:91]\n\t"
    "v_mfma_f32_16x16x32_bf16 v[92:95], v[148:151], v[116:119], v[92:95]\n\t"
    "s_waitcnt lgkmcnt(4)\n\t"
    "v_mfma_f32_16x16x32_bf16 v[80:83], v[152:155], v[120:123], v[80:83]\n\t"
    "ds_read_b128 v[112:115], %[ea] offset:12288\n\t"
    "s_add_u32 m0, %[lb], 12288\n\t"
    "v_mfma_f32_16x16x32_bf16 v[84:87], v[156:159], v[120:123], v[84:87]\n\t"
    "ds_read_b128 v[116:119], %[ea] offset:14336\n\t"
    "global_load_lds_dwordx4 %[av6], %[s0] sc1\n\t"
    "v_mfma_f32_16x16x32_bf16 v[88:91], v[152:155], v[124:127], v[88:91]\n\t"
    "v_mfma_f32_16x16x32_bf16 v[92:95], v[156:159], v[124:127], v[92:95]\n\t"
    "s_waitcnt lgkmcnt(4)\n\t"
    "v_mfma_f32_16x16x32_bf16 v[80:83], v[160:163], v[96:99], v[80:83]\n\t"
    "ds_read_b128 v[120:123], %[da] offset:12288\n\t"
    "s_add_u32 m0, %[lb], 16384\n\t"
    "v_mfma_f32_16x16x32_bf16 v[84:87], v[164:167], v[96:99], v[84:87]\n\t"
    "ds_read_b128 v[124:127], %[da] offset:14336\n\t"
    "global_load_lds_dwordx4 %[av0], %[s1] sc1\n\t"
    "v_mfma_f32_16x16x32_bf16 v[88:91], v[160:163], v[100:103], v[88:91]\n\t"
    "v_mfma_f32_16x16x32_bf16 v[92:95], v[164:167], v[100:103], v[92:95]\n\t"
    "s_waitcnt vmcnt(7)\n\t"
    "s_barrier\n\t"
    "s_waitcnt lgkmcnt(4)\n\t"
    "v_mfma_f32_16x16x32_bf16 v[80:83], v[168:171], v[104:107], v[80:83]\n\t"
    "ds_read_b128 v[96:99], %[ea] offset:16384\n\t"
    "s_add_u32 m0, %[lb], 20480\n\t"
    "v_mfma_f32_16x16x32_bf16 v[84:87], v[172:175], v[104:107], v[84:87]\n\t"
    "ds_read_b128 v[100:103], %[ea] offset:18432\n\t"
    "global_load_lds_dwordx4 %[av2], %[s1] sc1\n\t"
    "v_mfma_f32_16x16x32_bf16 v[88:91], v[168:171], v[108:111], v[88:91]\n\t"
    "v_mfma_f32_16x16x32_bf16 v[92:95], v[172:175], v[108:111], v[92:95]\n\t"
    "s_waitcnt lgkmcnt(4)\n\t"
    "v_mfma_f32_16x16x32_bf16 v[80:83], v[176:179], v[112:115], v[80:83]\n\t"
    "ds_read_b128 v[104:107], %[da] offset:16384\n\t"
    "s_add_u32 m0, %[lb], 24576\n\t"
    "v_mfma_f32_16x16x32_bf16 v[84:87], v[180:183], v[112:115], v[84:87]\n\t"
    "ds_read_b128 v[108:111], %[da] offset:18432\n\t"
    "global_load_lds_dwordx4 %[av4], %[s1] sc1\n\t"
    "v_mfma_f32_16x16x32_bf16 v[88:91], v[176:179], v[116:119], v[88:91]\n\t"
    "v_mfma_f32_16x16x32_bf16 v[92:95], v[180:183], v[116:119], v[92:95]\n\t"
    "s_waitcnt lgkmcnt(4)\n\t"
    "v_mfma_f32_16x16x32_bf16 v[80:83], v[184:187], v[120:123], v[80:83]\n\t"
    "ds_read_b128 v[112:115], %[ea] offset:20480\n\t"
    "s_add_u32 m0, %[lb], 28672\n\t"
    "v_mfma_f32_16x16x32_bf16 v[84:87], v[188:191], v[120:123], v[84:87]\n\t"
    "ds_read_b128 v[116:119], %[ea] offset:22528\n\t"
    "global_load_lds_dwordx4 %[av6], %[s1] sc1\n\t"
    "v_mfma_f32_16x16x32_bf16 v[88:91], v[184:187], v[124:127], v[88:91]\n\t"
    "v_mfma_f32_16x16x32_bf16 v[92:95], v[188:191], v[124:127], v[92:95]\n\t"
    "s_waitcnt lgkmcnt(4)\n\t"
    "v_mfma_f32_16x16x32_bf16 v[80:83], v[192:195], v[96:99], v[80:83]\n\t"
    "ds_read_b128 v[120:123], %[da] offset:20480\n\t"
    "v_mfma_f32_16x16x32_bf16 v[84:87], v[196:199], v[96:99], v[84:87]\n\t"
    "ds_read_b128 v[124:127], %[da] offset:22528\n\t"
    "v_mfma_f32_16x16x32_bf16 v[88:91], v[192:195], v[100:103], v[88:91]\n\t"
    "v_mfma_f32_16x16x32_bf16 v[92:95], v[196:199], v[100:103], v[92:95]\n\t"
    "s_waitcnt vmcnt(8)\n\t"
    "s_barrier\n\t"
    "s_waitcnt lgkmcnt(4)\n\t"
    "v_mfma_f32_16x16x32_bf16 v[80:83], v[200:203], v[104:107], v[80:83]\n\t"
    "ds_read_b128 v[96:99], %[ea] offset:24576\n\t"
    "v_mfma_f32_16x16x32_bf16 v[84:87], v[204:207], v[104:107], v[84:87]\n\t"
    "ds_read_b128 v[100:103], %[ea] offset:26624\n\t"
    "v_mfma_f32_16x16x32_bf16 v[88:91], v[200:203], v[108:111], v[88:91]\n\t"
    "v_mfma_f32_16x16x32_bf16 v[92:95], v[204:207], v[108:111], v[92:95]\n\t"
    "s_waitcnt lgkmcnt(4)\n\t"
    "v_mfma_f32_16x16x32_bf16 v[80:83], v[208:211], v[112:115], v[80:83]\n\t"
    "ds_read_b128 v[104:107], %[da] offset:24576\n\t"
    "v_mfma_f32_16x16x32_bf16 v[84:87], v[212:215], v[112:115], v[84:87]\n\t"
    "ds_read_b128 v[108:111], %[da] offset:26624\n\t"
    "v_mfma_f32_16x16x32_bf16 v[88:91], v[208:211], v[116:119], v[88:91]\n\t"
    "v_mfma_f32_16x16x32_bf16 v[92:95], v[212:215], v[116:119], v[92:95]\n\t"
    "s_waitcnt lgkmcnt(4)\n\t"
    "v_mfma_f32_16x16x32_bf16 v[80:83], v[216:219], v[120:123], v[80:83]\n\t"
    "ds_read_b128 v[112:115], %[ea] offset:28672\n\t"
    "v_mfma_f32_16x16x32_bf16 v[84:87], v[220:223], v[120:123], v[84:87]\n\t"
    "ds_read_b128 v[116:119], %[ea] offset:30720\n\t"
    "v_mfma_f32_16x16x32_bf16 v[88:91], v[216:219], v[124:127], v[88:91]\n\t"
    "v_mfma_f32_16x16x32_bf16 v[92:95], v[220:223], v[124:127], v[92:95]\n\t"
    "s_waitcnt lgkmcnt(4)\n\t"
    "v_mfma_f32_16x16x32_bf16 v[80:83], v[224:227], v[96:99], v[80:83]\n\t"
    "ds_read_b128 v[120:123], %[da] offset:28672\n\t"
    "v_mfma_f32_16x16x32_bf16 v[84:87], v[228:231], v[96:99], v[84:87]\n\t"
    "ds_read_b128 v[124:127], %[da] offset:30720\n\t"
    "v_mfma_f32_16x16x32_bf16 v[88:91], v[224:227], v[100:103], v[88:91]\n\t"
    "v_mfma_f32_16x16x32_bf16 v[92:95], v[228:231], v[100:103], v[92:95]\n\t"
    "s_waitcnt lgkmcnt(4)\n\t"
    "v_mfma_f32_16x16x32_bf16 v[80:83], v[232:235], v[104:107], v[80:83]\n\t"
    "v_mfma_f32_16x16x32_bf16 v[84:87], v[236:239], v[104:107], v[84:87]\n\t"
    "v_mfma_f32_16x16x32_bf16 v[88:91], v[232:235], v[108:111], v[88:91]\n\t"
    "v_mfma_f32_16x16x32_bf16 v[92:95], v[236:239], v[108:111], v[92:95]\n\t"
    "s_waitcnt lgkmcnt(2)\n\t"
    "v_mfma_f32_16x16x32_bf16 v[80:83], v[240:243], v[112:115], v[80:83]\n\t"
    "v_mfma_f32_16x16x32_bf16 v[84:87], v[244:247], v[112:115], v[84:87]\n\t"
    "v_mfma_f32_16x16x32_bf16 v[88:91], v[240:243], v[116:119], v[88:91]\n\t"
    "v_mfma_f32_16x16x32_bf16 v[92:95], v[244:247], v[116:119], v[92:95]\n\t"
    "s_waitcnt lgkmcnt(0)\n\t"
    "v_mfma_f32_16x16x32_bf16 v[80:83], v[248:251], v[120:123], v[80:83]\n\t"
    "v_mfma_f32_16x16x32_bf16 v[84:87], v[252:255], v[120:123], v[84:87]\n\t"
    "v_mfma_f32_16x16x32_bf16 v[88:91], v[248:251], v[124:127], v[88:91]\n\t"
    "v_mfma_f32_16x16x32_bf16 v[92:95], v[252:255], v[124:127], v[92:95]\n\t"
    "s_nop 6\n\t"
    "s_branch 9f\n\t"
    "3:\n\t"
    "9:"
    : "=&{v[80:95]}"(pd)
    : [ea] "v"(ea), [da] "v"(da), [x] "s"(x), "{v[128:159]}"(W[0]), "{v[160:191]}"(W[1]), "{v[192:223]}"(W[2]), "{v[224:255]}"(W[3]), [lb] "s"(lb), [s0] "s"(s0), [s1] "s"(s1), [av0] "v"(av[0]), [av1] "v"(av[1]), [av2] "v"(av[2]), [av3] "v"(av[3]), [av4] "v"(av[4]), [av5] "v"(av[5]), [av6] "v"(av[6]), [av7] "v"(av[7])
    : "v96", "v97", "v98", "v99", "v100", "v101", "v102", "v103", "v104", "v105", "v106", "v107", "v108", "v109", "v110", "v111", "v112", "v113", "v114", "v115", "v116", "v117", "v118", "v119", "v120", "v121", "v122", "v123", "v124", "v125", "v126", "v127", "vcc", "scc", "m0", "memory");
}
__device__ __forceinline__ void lean_st0(const u32x32 (&W)[4], u32x16& pd, unsigned ea, unsigned da, unsigned x, unsigned pv, unsigned cn, unsigned sl, unsigned dv, const char* db, const char* pg) {
  unsigned st;
  asm volatile("ds_read_b128 v[96:99], %[ea] offset:0\n\t"
    "ds_read_b128 v[100:103], %[ea] offset:2048\n\t"
    "ds_read_b128 v[104:107], %[da] offset:0\n\t"
    "ds_read_b128 v[108:111], %[da] offset:2048\n\t"
    "ds_read_b128 v[112:115], %[ea] offset:4096\n\t"
    "ds_read_b128 v[116:119], %[ea] offset:6144\n\t"
    "s_waitcnt lgkmcnt(4)\n\t"
    "v_mfma_f32_16x16x32_bf16 v[80:83], v[128:131], v[96:99], 0\n\t"
    "ds_read_b128 v[120:123], %[da] offset:4096\n\t"
    "v_mfma_f32_16x16x32_bf16 v[84:87], v[132:135], v[96:99], 0\n\t"
    "ds_read_b128 v[124:127], %[da] offset:6144\n\t"
    "v_mfma_f32_16x16x32_bf16 v[88:91], v[128:131], v[100:103], 0\n\t"
    "v_mfma_f32_16x16x32_bf16 v[92:95], v[132:135], v[100:103], 0\n\t"
    "s_barrier\n\t"
    "s_waitcnt lgkmcnt(4)\n\t"
    "v_mfma_f32_16x16x32_bf16 v[80:83], v[136:139], v[104:107], v[80:83]\n\t"
    "ds_read_b128 v[96:99], %[ea] offset:8192\n\t"
    "v_mfma_f32_16x16x32_bf16 v[84:87], v[140:143], v[104:107], v[84:87]\n\t"
    "ds_read_b128 v[100:103], %[ea] offset:10240\n\t"
    "v_mfma_f32_16x16x32_bf16 v[88:91], v[136:139], v[108:111], v[88:91]\n\t"
    "v_mfma_f32_16x16x32_bf16 v[92:95], v[140:143], v[108:111], v[92:95]\n\t"
    "s_waitcnt lgkmcnt(4)\n\t"
    "v_mfma_f32_16x16x32_bf16 v[80:83], v[144:147], v[112:115], v[80:83]\n\t"
    "ds_read_b128 v[104:107], %[da] offset:8192\n\t"
    "v_mfma_f32_16x16x32_bf16 v[84:87], v[148:151], v[112:115], v[84:87]\n\t"
    "ds_read_b128 v[108:111], %[da] offset:10240\n\t"
    "v_mfma_f32_16x16x32_bf16 v[88:91], v[144:147], v[116:119], v[88:91]\n\t"
    "v_mfma_f32_16x16x32_bf16 v[92:95], v[148:151], v[116:119], v[92:95]\n\t"
    "s_waitcnt lgkmcnt(4)\n\t"
    "v_mfma_f32_16x16x32_bf16 v[80:83], v[152:155], v[120:123], v[80:83]\n\t"
    "ds_read_b128 v[112:115], %[ea] offset:12288\n\t"
    "v_mfma_f32_16x16x32_bf16 v[84:87], v[156:159], v[120:123], v[84:87]\n\t"
    "ds_read_b128 v[116:119], %[ea] offset:14336\n\t"
    "v_mfma_f32_16x16x32_bf16 v[88:91], v[152:155], v[124:127], v[88:91]\n\t"
    "v_mfma_f32_16x16x32_bf16 v[92:95], v[156:159], v[124:127], v[92:95]\n\t"
    "s_bitcmp1_b32 %[x], 2\n\t"
    "s_cbranch_scc1 7f\n\t"
    "8:\n\t"
    "s_waitcnt lgkmcnt(4)\n\t"
    "v_mfma_f32_16x16x32_bf16 v[80:83], v[160:163], v[96:99], v[80:83]\n\t"
    "ds_read_b128 v[120:123], %[da] offset:12288\n\t"
    "v_mfma_f32_16x16x32_bf16 v[84:87], v[164:167], v[96:99], v[84:87]\n\t"
    "ds_read_b128 v[124:127], %[da] offset:14336\n\t"
    "v_mfma_f32_16x16x32_bf16 v[88:91], v[160:163], v[100:103], v[88:91]\n\t"
    "v_mfma_f32_16x16x32_bf16 v[92:95], v[164:167], v[100:103], v[92:95]\n\t"
    "s_barrier\n\t"
    "s_waitcnt lgkmcnt(4)\n\t"
    "v_mfma_f32_16x16x32_bf16 v[80:83], v[168:171], v[104:107], v[80:83]\n\t"
    "ds_read_b128 v[96:99], %[ea] offset:16384\n\t"
    "v_mfma_f32_16x16x32_bf16 v[84:87], v[172:175], v[104:107], v[84:87]\n\t"
    "ds_read_b128 v[100:103], %[ea] offset:18432\n\t"
    "v_mfma_f32_16x16x32_bf16 v[88:91], v[168:171], v[108:111], v[88:91]\n\t"
    "v_mfma_f32_16x16x32_bf16 v[92:95], v[172:175], v[108:111], v[92:95]\n\t"
    "s_waitcnt lgkmcnt(4)\n\t"
    "v_mfma_f32_16x16x32_bf16 v[80:83], v[176:179], v[112:115], v[80:83]\n\t"
    "ds_read_b128 v[104:107], %[da] offset:16384\n\t"
    "v_mfma_f32_16x16x32_bf16 v[84:87], v[180:183], v[112:115], v[84:87]\n\t"
    "ds_read_b128 v[108:111], %[da] offset:18432\n\t"
    "v_mfma_f32_16x16x32_bf16 v[88:91], v[176:179], v[116:119], v[88:91]\n\t"
    "v_mfma_f32_16x16x32_bf16 v[92:95], v[180:183], v[116:119], v[92:95]\n\t"
    "s_waitcnt lgkmcnt(4)\n\t"
    "v_mfma_f32_16x16x32_bf16 v[80:83], v[184:187], v[120:123], v[80:83]\n\t"
    "ds_read_b128 v[112:115], %[ea] offset:20480\n\t"
    "v_mfma_f32_16x16x32_bf16 v[84:87], v[188:191], v[120:123], v[84:87]\n\t"
    "ds_read_b128 v[116:119], %[ea] offset:22528\n\t"
    "v_mfma_f32_16x16x32_bf16 v[88:91], v[184:187], v[124:127], v[88:91]\n\t"
    "v_mfma_f32_16x16x32_bf16 v[92:95], v[188:191], v[124:127], v[92:95]\n\t"
    "s_waitcnt lgkmcnt(4)\n\t"
    "v_mfma_f32_16x16x32_bf16 v[80:83], v[192:195], v[96:99], v[80:83]\n\t"
    "ds_read_b128 v[120:123], %[da] offset:20480\n\t"
    "v_mfma_f32_16x16x32_bf16 v[84:87], v[196:199], v[96:99], v[84:87]\n\t"
    "ds_read_b128 v[124:127], %[da] offset:22528\n\t"
    "v_mfma_f32_16x16x32_bf16 v[88:91], v[192:195], v[100:103], v[88:91]\n\t"
    "v_mfma_f32_16x16x32_bf16 v[92:95], v[196:199], v[100:103], v[92:95]\n\t"
    "s_barrier\n\t"
    "s_and_b32 %[st], %[x], 3\n\t"
    "s_cbranch_scc1 3f\n\t"
    "4:\n\t"
    "s_waitcnt lgkmcnt(4)\n\t"
    "v_mfma_f32_16x16x32_bf16 v[80:83], v[200:203], v[104:107], v[80:83]\n\t"
    "ds_read_b128 v[96:99], %[ea] offset:24576\n\t"
    "v_mfma_f32_16x16x32_bf16 v[84:87], v[204:207], v[104:107], v[84:87]\n\t"
    "ds_read_b128 v[100:103], %[ea] offset:26624\n\t"
    "v_mfma_f32_16x16x32_bf16 v[88:91], v[200:203], v[108:111], v[88:91]\n\t"
    "v_mfma_f32_16x16x32_bf16 v[92:95], v[204:207], v[108:111], v[92:95]\n\t"
    "s_waitcnt lgkmcnt(4)\n\t"
    "v_mfma_f32_16x16x32_bf16 v[80:83], v[208:211], v[112:115], v[80:83]\n\t"
    "ds_read_b128 v[104:107], %[da] offset:24576\n\t"
    "v_mfma_f32_16x16x32_bf16 v[84:87], v[212:215], v[112:115], v[84:87]\n\t"
    "ds_read_b128 v[108:111], %[da] offset:26624\n\t"
    "v_mfma_f32_16x16x32_bf16 v[88:91], v[208:211], v[116:119], v[88:91]\n\t"
    "v_mfma_f32_16x16x32_bf16 v[92:95], v[212:215], v[116:119], v[92:95]\n\t"
    "s_waitcnt lgkmcnt(4)\n\t"
    "v_mfma_f32_16x16x32_bf16 v[80:83], v[216:219], v[120:123], v[80:83]\n\t"
    "ds_read_b128 v[112:115], %[ea] offset:28672\n\t"
    "v_mfma_f32_16x16x32_bf16 v[84:87], v[220:223], v[120:123], v[84:87]\n\t"
    "ds_read_b128 v[116:119], %[ea] offset:30720\n\t"
    "v_mfma_f32_16x16x32_bf16 v[88:91], v[216:219], v[124:127], v[88:91]\n\t"
    "v_mfma_f32_16x16x32_bf16 v[92:95], v[220:223], v[124:127], v[92:95]\n\t"
    "s_waitcnt lgkmcnt(4)\n\t"
    "v_mfma_f32_16x16x32_bf16 v[80:83], v[224:227], v[96:99], v[80:83]\n\t"
    "ds_read_b128 v[120:123], %[da] offset:28672\n\t"
    "v_mfma_f32_16x16x32_bf16 v[84:87], v[228:231], v[96:99], v[84:87]\n\t"
    "ds_read_b128 v[124:127], %[da] offset:30720\n\t"
    "v_mfma_f32_16x16x32_bf16 v[88:91], v[224:227], v[100:103], v[88:91]\n\t"
    "v_mfma_f32_16x16x32_bf16 v[92:95], v[228:231], v[100:103], v[92:95]\n\t"
    "s_waitcnt lgkmcnt(4)\n\t"
    "v_mfma_f32_16x16x32_bf16 v[80:83], v[232:235], v[104:107], v[80:83]\n\t"
    "v_mfma_f32_16x16x32_bf16 v[84:87], v[236:239], v[104:107], v[84:87]\n\t"
    "v_mfma_f32_16x16x32_bf16 v[88:91], v[232:235], v[108:111], v[88:91]\n\t"
    "v_mfma_f32_16x16x32_bf16 v[92:95], v[236:239], v[108:111], v[92:95]\n\t"
    "s_waitcnt lgkmcnt(2)\n\t"
    "v_mfma_f32_16x16x32_bf16 v[80:83], v[240:243], v[112:115], v[80:83]\n\t"
    "v_mfma_f32_16x16x32_bf16 v[84:87], v[244:247], v[112:115], v[84:87]\n\t"
    "v_mfma_f32_16x16x32_bf16 v[88:91], v[240:243], v[116:119], v[88:91]\n\t"
    "v_mfma_f32_16x16x32_bf16 v[92:95], v[244:247], v[116:119], v[92:95]\n\t"
    "s_waitcnt lgkmcnt(0)\n\t"
    "v_mfma_f32_16x16x32_bf16 v[80:83], v[248:251], v[120:123], v[80:83]\n\t"
    "v_mfma_f32_16x16x32_bf16 v[84:87], v[252:255], v[120:123], v[84:87]\n\t"
    "v_mfma_f32_16x16x32_bf16 v[88:91], v[248:251], v[124:127], v[88:91]\n\t"
    "v_mfma_f32_16x16x32_bf16 v[92:95], v[252:255], v[124:127], v[92:95]\n\t"
    "s_nop 6\n\t"
    "s_branch 9f\n\t"
    "3:\n\t"
    "s_bitcmp0_b32 %[x], 0\n\t"
    "s_cbranch_scc1 5f\n\t"
    "7:\n\t"
    "v_mov_b32 v76, %[cn]\n\t"
    "v_mov_b32 v77, 1\n\t"
    "v_mov_b32 v78, %[pv]\n\t"
    "v_mov_b32 v79, 0\n\t"
    "s_waitcnt vmcnt(0)\n\t"
    "s_mov_b64 exec, 1\n\t"
    "ds_add_rtn_u32 v77, v76, v77\n\t"
    "s_waitcnt lgkmcnt(0)\n\t"
    "v_readfirstlane_b32 %[st], v77\n\t"
    "s_and_b32 %[st], %[st], 3\n\t"
    "s_cmp_lg_u32 %[st], 3\n\t"
    "s_cbranch_scc1 6f\n\t"
    "global_store_dword v79, v78, %[pg] sc1\n\t"
    "6:\n\t"
    "s_mov_b64 exec, -1\n\t"
    "s_bitcmp1_b32 %[x], 2\n\t"
    "s_cbranch_scc1 8b\n\t"
    "5:\n\t"
    "s_bitcmp0_b32 %[x], 1\n\t"
    "s_cbranch_scc1 4b\n\t"
    "s_mov_b32 m0, %[sl]\n\t"
    "s_nop 0\n\t"
    "global_load_lds_dword %[dv], %[db] sc1\n\t"
    "s_branch 4b\n\t"
    "9:"
    : "=&{v[80:95]}"(pd), [st] "=&s"(st)
    : [ea] "v"(ea), [da] "v"(da), [x] "s"(x), "{v[128:159]}"(W[0]), "{v[160:191]}"(W[1]), "{v[192:223]}"(W[2]), "{v[224:255]}"(W[3]), [pv] "s"(pv), [cn] "s"(cn), [sl] "s"(sl), [dv] "v"(dv), [db] "s"(db), [pg] "s"(pg)
    : "v96", "v97", "v98", "v99", "v100", "v101", "v102", "v103", "v104", "v105", "v106", "v107", "v108", "v109", "v110", "v111", "v112", "v113", "v114", "v115", "v116", "v117", "v118", "v119", "v120", "v121", "v122", "v123", "v124", "v125", "v126", "v127", "v76", "v77", "v78", "v79", "vcc", "scc", "m0", "memory");
}
#define LEAN_QB 1
__device__ __forceinline__ void paced_ld0(const u32x32 (&W)[4], unsigned ea, unsigned da, int b, int ns, unsigned ab, unsigned rw, unsigned fa,
    const char* p0, const char* p1, unsigned lb, int& cap, int wm, unsigned rq, unsigned mu, unsigned ad, unsigned dv, const char* db, unsigned sl, unsigned sa, const unsigned (&av)[8]) {
  unsigned st;
  asm volatile("s_setprio 2\n\t"
    "v_mov_b32 v78, %[wm]\n\t"
    "s_cmp_lg_u32 %[rq], 0\n\t"
    "s_cbranch_scc1 3f\n\t"
    "2:\n\t"
    "v_cmp_ge_i32 vcc, %[b], v78\n\t"
    "s_cbranch_vccz 4f\n\t"
    "21:\n\t"
    "s_cmp_eq_u32 %[sl], 0\n\t"
    "s_cbranch_scc1 71f\n\t"
    "s_mov_b32 m0, %[sl]\n\t"
    "s_nop 0\n\t"
    "global_load_lds_dword %[dv], %[db] sc1\n\t"
    "71:\n\t"
    "s_sub_u32 %[cap], %[cap], 1\n\t"
    "s_max_i32 %[cap], %[cap], 0\n\t"
    "s_waitcnt vmcnt(0)\n\t"
    "ds_read_b32 v79, %[sa]\n\t"
    "s_waitcnt lgkmcnt(0)\n\t"
    "v_mad_u32_u24 v79, v79, %[mu], %[ad]\n\t"
    "v_max_i32 v78, v78, v79\n\t"
    "v_cmp_ge_i32 vcc, %[b], v78\n\t"
    "s_cbranch_vccz 4f\n\t"
    "s_sleep 1\n\t"
    "s_cmp_lg_u32 %[cap], 0\n\t"
    "s_cbranch_scc1 21b\n\t"
    "4:\n\t"
    "s_add_u32 m0, %[lb], 0\n\t"
    "s_nop 0\n\t"
    "global_load_lds_dwordx4 %[av0], s[36:37] sc1\n\t"
    "s_add_u32 m0, %[lb], 32768\n\t"
    "s_nop 0\n\t"
    "global_load_lds_dwordx4 %[av1], s[36:37] sc1\n\t"
    "s_add_u32 m0, %[lb], 4096\n\t"
    "s_nop 0\n\t"
    "global_load_lds_dwordx4 %[av2], s[36:37] sc1\n\t"
    "s_add_u32 m0, %[lb], 36864\n\t"
    "s_nop 0\n\t"
    "global_load_lds_dwordx4 %[av3], s[36:37] sc1\n\t"
    "s_add_u32 m0, %[lb], 8192\n\t"
    "s_nop 0\n\t"
    "global_load_lds_dwordx4 %[av4], s[36:37] sc1\n\t"
    "s_add_u32 m0, %[lb], 40960\n\t"
    "s_nop 0\n\t"
    "global_load_lds_dwordx4 %[av5], s[36:37] sc1\n\t"
    "s_add_u32 m0, %[lb], 12288\n\t"
    "s_nop 0\n\t"
    "global_load_lds_dwordx4 %[av6], s[36:37] sc1\n\t"
    "s_add_u32 m0, %[lb], 45056\n\t"
    "s_nop 0\n\t"
    "global_load_lds_dwordx4 %[av7], s[36:37] sc1\n\t"
    "s_add_u32 m0, %[lb], 16384\n\t"
    "s_nop 0\n\t"
    "global_load_lds_dwordx4 %[av0], s[38:39] sc1\n\t"
    "s_add_u32 m0, %[lb], 49152\n\t"
    "s_nop 0\n\t"
    "global_load_lds_dwordx4 %[av1], s[38:39] sc1\n\t"
    "s_add_u32 m0, %[lb], 20480\n\t"
    "s_nop 0\n\t"
    "global_load_lds_dwordx4 %[av2], s[38:39] sc1\n\t"
    "s_add_u32 m0, %[lb], 53248\n\t"
    "s_nop 0\n\t"
    "global_load_lds_dwordx4 %[av3], s[38:39] sc1\n\t"
    "s_add_u32 m0, %[lb], 24576\n\t"
    "s_nop 0\n\t"
    "global_load_lds_dwordx4 %[av4], s[38:39] sc1\n\t"
    "s_add_u32 m0, %[lb], 57344\n\t"
    "s_nop 0\n\t"
    "global_load_lds_dwordx4 %[av5], s[38:39] sc1\n\t"
    "s_add_u32 m0, %[lb], 28672\n\t"
    "s_nop 0\n\t"
    "global_load_lds_dwordx4 %[av6], s[38:39] sc1\n\t"
    "s_add_u32 m0, %[lb], 61440\n\t"
    "s_nop 0\n\t"
    "global_load_lds_dwordx4 %[av7], s[38:39] sc1\n\t"
    "s_add_u32 s36, s36, %[ab]\n\t"
    "s_addc_u32 s37, s37, 0\n\t"
    "s_add_u32 s38, s38, %[ab]\n\t"
    "s_addc_u32 s39, s39, 0\n\t"
    "s_xor_b32 %[lb], %[lb], 0x10000\n\t"
    "3:\n\t"
    "s_add_u32 %[st], %[b], 1\n\t"
    "v_min_i32 v78, %[ns], v78\n\t"
    "v_cmp_ge_i32 vcc, %[st], v78\n\t"
    "s_waitcnt vmcnt(12) lgkmcnt(0)\n\t"
    "s_barrier\n\t"
    "ds_read_b128 v[96:99], %[ea] offset:0\n\t"
    "ds_read_b128 v[100:103], %[ea] offset:2048\n\t"
    "ds_read_b128 v[104:107], %[da] offset:0\n\t"
    "ds_read_b128 v[108:111], %[da] offset:2048\n\t"
    "ds_read_b128 v[112:115], %[ea] offset:4096\n\t"
    "ds_read_b128 v[116:119], %[ea] offset:6144\n\t"
    "s_cbranch_vccz 5f\n\t"
    "s_waitcnt lgkmcnt(4)\n\t"
    "v_mfma_f32_16x16x32_bf16 v[80:83], v[128:131], v[96:99], 0\n\t"
    "ds_read_b128 v[120:123], %[da] offset:4096\n\t"
    "v_mfma_f32_16x16x32_bf16 v[84:87], v[132:135], v[96:99], 0\n\t"
    "ds_read_b128 v[124:127], %[da] offset:6144\n\t"
    "v_mfma_f32_16x16x32_bf16 v[88:91], v[128:131], v[100:103], 0\n\t"
    "v_mfma_f32_16x16x32_bf16 v[92:95], v[132:135], v[100:103], 0\n\t"
    "s_waitcnt vmcnt(8)\n\t"
    "s_barrier\n\t"
    "s_waitcnt lgkmcnt(4)\n\t"
    "v_mfma_f32_16x16x32_bf16 v[80:83], v[136:139], v[104:107], v[80:83]\n\t"
    "ds_read_b128 v[96:99], %[ea] offset:8192\n\t"
    "v_mfma_f32_16x16x32_bf16 v[84:87], v[140:143], v[104:107], v[84:87]\n\t"
    "ds_read_b128 v[100:103], %[ea] offset:10240\n\t"
    "v_mfma_f32_16x16x32_bf16 v[88:91], v[136:139], v[108:111], v[88:91]\n\t"
    "v_mfma_f32_16x16x32_bf16 v[92:95], v[140:143], v[108:111], v[92:95]\n\t"
    "s_waitcnt lgkmcnt(4)\n\t"
    "v_mfma_f32_16x16x32_bf16 v[80:83], v[144:147], v[112:115], v[80:83]\n\t"
    "ds_read_b128 v[104:107], %[da] offset:8192\n\t"
    "v_mfma_f32_16x16x32_bf16 v[84:87], v[148:151], v[112:115], v[84:87]\n\t"
    "ds_read_b128 v[108:111], %[da] offset:10240\n\t"
    "v_mfma_f32_16x16x32_bf16 v[88:91], v[144:147], v[116:119], v[88:91]\n\t"
    "v_mfma_f32_16x16x32_bf16 v[92:95], v[148:151], v[116:119], v[92:95]\n\t"
    "s_waitcnt lgkmcnt(4)\n\t"
    "v_mfma_f32_16x16x32_bf16 v[80:83], v[152:155], v[120:123], v[80:83]\n\t"
    "ds_read_b128 v[112:115], %[ea] offset:12288\n\t"
    "v_mfma_f32_16x16x32_bf16 v[84:87], v[156:159], v[120:123], v[84:87]\n\t"
    "ds_read_b128 v[116:119], %[ea] offset:14336\n\t"
    "v_mfma_f32_16x16x32_bf16 v[88:91], v[152:155], v[124:127], v[88:91]\n\t"
    "v_mfma_f32_16x16x32_bf16 v[92:95], v[156:159], v[124:127], v[92:95]\n\t"
    "s_waitcnt lgkmcnt(4)\n\t"
    "v_mfma_f32_16x16x32_bf16 v[80:83], v[160:163], v[96:99], v[80:83]\n\t"
    "ds_read_b128 v[120:123], %[da] offset:12288\n\t"
    "v_mfma_f32_16x16x32_bf16 v[84:87], v[164:167], v[96:99], v[84:87]\n\t"
    "ds_read_b128 v[124:127], %[da] offset:14336\n\t"
    "v_mfma_f32_16x16x32_bf16 v[88:91], v[160:163], v[100:103], v[88:91]\n\t"
    "v_mfma_f32_16x16x32_bf16 v[92:95], v[164:167], v[100:103], v[92:95]\n\t"
    "s_waitcnt vmcnt(4)\n\t"
    "s_barrier\n\t"
    "s_waitcnt lgkmcnt(4)\n\t"
    "v_mfma_f32_16x16x32_bf16 v[80:83], v[168:171], v[104:107], v[80:83]\n\t"
    "ds_read_b128 v[96:99], %[ea] offset:16384\n\t"
    "v_mfma_f32_16x16x32_bf16 v[84:87], v[172:175], v[104:107], v[84:87]\n\t"
    "ds_read_b128 v[100:103], %[ea] offset:18432\n\t"
    "v_mfma_f32_16x16x32_bf16 v[88:91], v[168:171], v[108:111], v[88:91]\n\t"
    "v_mfma_f32_16x16x32_bf16 v[92:95], v[172:175], v[108:111], v[92:95]\n\t"
    "s_waitcnt lgkmcnt(4)\n\t"
    "v_mfma_f32_16x16x32_bf16 v[80:83], v[176:179], v[112:115], v[80:83]\n\t"
    "ds_read_b128 v[104:107], %[da] offset:16384\n\t"
    "v_mfma_f32_16x16x32_bf16 v[84:87], v[180:183], v[112:115], v[84:87]\n\t"
    "ds_read_b128 v[108:111], %[da] offset:18432\n\t"
    "v_mfma_f32_16x16x32_bf16 v[88:91], v[176:179], v[116:119], v[88:91]\n\t"
    "v_mfma_f32_16x16x32_bf16 v[92:95], v[180:183], v[116:119], v[92:95]\n\t"
    "s_waitcnt lgkmcnt(4)\n\t"
    "v_mfma_f32_16x16x32_bf16 v[80:83], v[184:187], v[120:123], v[80:83]\n\t"
    "ds_read_b128 v[112:115], %[ea] offset:20480\n\t"
    "v_mfma_f32_16x16x32_bf16 v[84:87], v[188:191], v[120:123], v[84:87]\n\t"
    "ds_read_b128 v[116:119], %[ea] offset:22528\n\t"
    "v_mfma_f32_16x16x32_bf16 v[88:91], v[184:187], v[124:127], v[88:91]\n\t"
    "v_mfma_f32_16x16x32_bf16 v[92:95], v[188:191], v[124:127], v[92:95]\n\t"
    "s_waitcnt lgkmcnt(4)\n\t"
    "v_mfma_f32_16x16x32_bf16 v[80:83], v[192:195], v[96:99], v[80:83]\n\t"
    "ds_read_b128 v[120:123], %[da] offset:20480\n\t"
    "v_mfma_f32_16x16x32_bf16 v[84:87], v[196:199], v[96:99], v[84:87]\n\t"
    "ds_read_b128 v[124:127], %[da] offset:22528\n\t"
    "v_mfma_f32_16x16x32_bf16 v[88:91], v[192:195], v[100:103], v[88:91]\n\t"
    "v_mfma_f32_16x16x32_bf16 v[92:95], v[196:199], v[100:103], v[92:95]\n\t"
    "s_waitcnt vmcnt(0)\n\t"
    "s_barrier\n\t"
    "s_waitcnt lgkmcnt(4)\n\t"
    "v_mfma_f32_16x16x32_bf16 v[80:83], v[200:203], v[104:107], v[80:83]\n\t"
    "ds_read_b128 v[96:99], %[ea] offset:24576\n\t"
    "v_mfma_f32_16x16x32_bf16 v[84:87], v[204:207], v[104:107], v[84:87]\n\t"
    "ds_read_b128 v[100:103], %[ea] offset:26624\n\t"
    "v_mfma_f32_16x16x32_bf16 v[88:91], v[200:203], v[108:111], v[88:91]\n\t"
    "v_mfma_f32_16x16x32_bf16 v[92:95], v[204:207], v[108:111], v[92:95]\n\t"
    "s_waitcnt lgkmcnt(4)\n\t"
    "v_mfma_f32_16x16x32_bf16 v[80:83], v[208:211], v[112:115], v[80:83]\n\t"
    "ds_read_b128 v[104:107], %[da] offset:24576\n\t"
    "v_mfma_f32_16x16x32_bf16 v[84:87], v[212:215], v[112:115], v[84:87]\n\t"
    "ds_read_b128 v[108:111], %[da] offset:26624\n\t"
    "v_mfma_f32_16x16x32_bf16 v[88:91], v[208:211], v[116:119], v[88:91]\n\t"
    "v_mfma_f32_16x16x32_bf16 v[92:95], v[212:215], v[116:119], v[92:95]\n\t"
    "s_cmp_eq_u32 %[sl], 0\n\t"
    "s_cbranch_scc1 72f\n\t"
    "s_mov_b32 m0, %[sl]\n\t"
    "s_nop 0\n\t"
    "global_load_lds_dword %[dv], %[db] sc1\n\t"
    "72:\n\t"
    "s_waitcnt lgkmcnt(4)\n\t"
    "v_mfma_f32_16x16x32_bf16 v[80:83], v[216:219], v[120:123], v[80:83]\n\t"
    "ds_read_b128 v[112:115], %[ea] offset:28672\n\t"
    "v_mfma_f32_16x16x32_bf16 v[84:87], v[220:223], v[120:123], v[84:87]\n\t"
    "ds_read_b128 v[116:119], %[ea] offset:30720\n\t"
    "v_mfma_f32_16x16x32_bf16 v[88:91], v[216:219], v[124:127], v[88:91]\n\t"
    "v_mfma_f32_16x16x32_bf16 v[92:95], v[220:223], v[124:127], v[92:95]\n\t"
    "s_waitcnt lgkmcnt(4)\n\t"
    "v_mfma_f32_16x16x32_bf16 v[80:83], v[224:227], v[96:99], v[80:83]\n\t"
    "ds_read_b128 v[120:123], %[da] offset:28672\n\t"
    "v_mfma_f32_16x16x32_bf16 v[84:87], v[228:231], v[96:99], v[84:87]\n\t"
    "ds_read_b128 v[124:127], %[da] offset:30720\n\t"
    "v_mfma_f32_16x16x32_bf16 v[88:91], v[224:227], v[100:103], v[88:91]\n\t"
    "v_mfma_f32_16x16x32_bf16 v[92:95], v[228:231], v[100:103], v[92:95]\n\t"
    "s_waitcnt lgkmcnt(4)\n\t"
    "v_mfma_f32_16x16x32_bf16 v[80:83], v[232:235], v[104:107], v[80:83]\n\t"
    "v_mfma_f32_16x16x32_bf16 v[84:87], v[236:239], v[104:107], v[84:87]\n\t"
    "v_mfma_f32_16x16x32_bf16 v[88:91], v[232:235], v[108:111], v[88:91]\n\t"
    "v_xor_b32 %[ea], 0x10000, %[ea]\n\t"
    "v_mfma_f32_16x16x32_bf16 v[92:95], v[236:239], v[108:111], v[92:95]\n\t"
    "v_xor_b32 %[da], 0x10000, %[da]\n\t"
    "s_waitcnt lgkmcnt(2)\n\t"
    "v_mfma_f32_16x16x32_bf16 v[80:83], v[240:243], v[112:115], v[80:83]\n\t"
    "v_mfma_f32_16x16x32_bf16 v[84:87], v[244:247], v[112:115], v[84:87]\n\t"
    "v_mfma_f32_16x16x32_bf16 v[88:91], v[240:243], v[116:119], v[88:91]\n\t"
    "s_add_u32 %[b], %[b], 1\n\t"
    "v_mfma_f32_16x16x32_bf16 v[92:95], v[244:247], v[116:119], v[92:95]\n\t"
    "s_waitcnt lgkmcnt(0)\n\t"
    "v_mfma_f32_16x16x32_bf16 v[80:83], v[248:251], v[120:123], v[80:83]\n\t"
    "v_mfma_f32_16x16x32_bf16 v[84:87], v[252:255], v[120:123], v[84:87]\n\t"
    "v_mfma_f32_16x16x32_bf16 v[88:91], v[248:251], v[124:127], v[88:91]\n\t"
    "v_mfma_f32_16x16x32_bf16 v[92:95], v[252:255], v[124:127], v[92:95]\n\t"
    "s_nop 4\n\t"
    "ds_write_b128 %[rw], v[80:83] offset:0\n\t"
    "ds_write_b128 %[rw], v[84:87] offset:1024\n\t"
    "ds_write_b128 %[rw], v[88:91] offset:2048\n\t"
    "ds_write_b128 %[rw], v[92:95] offset:3072\n\t"
    "s_waitcnt vmcnt(0)\n\t"
    "ds_read_b32 v79, %[sa]\n\t"
    "s_waitcnt lgkmcnt(0)\n\t"
    "v_mad_u32_u24 v79, v79, %[mu], %[ad]\n\t"
    "v_max_i32 v78, v78, v79\n\t"
    "v_mov_b32 v77, %[b]\n\t"
    "v_cmp_ge_i32 vcc, %[b], v78\n\t"
    "s_cmp_ge_i32 %[b], %[ns]\n\t"
    "s_cbranch_scc1 6f\n\t"
    "s_cbranch_vccnz 6f\n\t"
    "v_or_b32 v77, 0x100, v77\n\t"
    "ds_write_b32 %[fa], v77\n\t"
    "s_branch 4b\n\t"
    "6:\n\t"
    "ds_write_b32 %[fa], v77\n\t"
    "s_cbranch_scc0 21b\n\t"
    "s_branch 9f\n\t"
    "5:\n\t"
    "s_waitcnt lgkmcnt(4)\n\t"
    "v_mfma_f32_16x16x32_bf16 v[80:83], v[128:131], v[96:99], 0\n\t"
    "ds_read_b128 v[120:123], %[da] offset:4096\n\t"
    "s_add_u32 m0, %[lb], 0\n\t"
    "v_mfma_f32_16x16x32_bf16 v[84:87], v[132:135], v[96:99], 0\n\t"
    "ds_read_b128 v[124:127], %[da] offset:6144\n\t"
    "global_load_lds_dwordx4 %[av0], s[36:37] sc1\n\t"
    "v_mfma_f32_16x16x32_bf16 v[88:91], v[128:131], v[100:103], 0\n\t"
    "s_add_u32 m0, %[lb], 32768\n\t"
    "v_mfma_f32_16x16x32_bf16 v[92:95], v[132:135], v[100:103], 0\n\t"
    "global_load_lds_dwordx4 %[av1], s[36:37] sc1\n\t"
    "s_waitcnt vmcnt(10)\n\t"
    "s_barrier\n\t"
    "s_waitcnt lgkmcnt(4)\n\t"
    "v_mfma_f32_16x16x32_bf16 v[80:83], v[136:139], v[104:107], v[80:83]\n\t"
    "ds_read_b128 v[96:99], %[ea] offset:8192\n\t"
    "s_add_u32 m0, %[lb], 4096\n\t"
    "v_mfma_f32_16x16x32_bf16 v[84:87], v[140:143], v[104:107], v[84:87]\n\t"
    "ds_read_b128 v[100:103], %[ea] offset:10240\n\t"
    "global_load_lds_dwordx4 %[av2], s[36:37] sc1\n\t"
    "v_mfma_f32_16x16x32_bf16 v[88:91], v[136:139], v[108:111], v[88:91]\n\t"
    "s_add_u32 m0, %[lb], 36864\n\t"
    "v_mfma_f32_16x16x32_bf16 v[92:95], v[140:143], v[108:111], v[92:95]\n\t"
    "global_load_lds_dwordx4 %[av3], s[36:37] sc1\n\t"
    "s_waitcnt lgkmcnt(4)\n\t"
    "v_mfma_f32_16x16x32_bf16 v[80:83], v[144:147], v[112:115], v[80:83]\n\t"
    "ds_read_b128 v[104:107], %[da] offset:8192\n\t"
    "s_add_u32 m0, %[lb], 8192\n\t"
    "v_mfma_f32_16x16x32_bf16 v[84:87], v[148:151], v[112:115], v[84:87]\n\t"
    "ds_read_b128 v[108:111], %[da] offset:10240\n\t"
    "global_load_lds_dwordx4 %[av4], s[36:37] sc1\n\t"
    "v_mfma_f32_16x16x32_bf16 v[88:91], v[144:147], v[116:119], v[88:91]\n\t"
    "s_add_u32 m0, %[lb], 40960\n\t"
    "v_mfma_f32_16x16x32_bf16 v[92:95], v[148:151], v[116:119], v[92:95]\n\t"
    "global_load_lds_dwordx4 %[av5], s[36:37] sc1\n\t"
    "s_waitcnt lgkmcnt(4)\n\t"
    "v_mfma_f32_16x16x32_bf16 v[80:83], v[152:155], v[120:123], v[80:83]\n\t"
    "ds_read_b128 v[112:115], %[ea] offset:12288\n\t"
    "s_add_u32 m0, %[lb], 12288\n\t"
    "v_mfma_f32_16x16x32_bf16 v[84:87], v[156:159], v[120:123], v[84:87]\n\t"
    "ds_read_b128 v[116:119], %[ea] offset:14336\n\t"
    "global_load_lds_dwordx4 %[av6], s[36:37] sc1\n\t"
    "v_mfma_f32_16x16x32_bf16 v[88:91], v[152:155], v[124:127], v[88:91]\n\t"
    "s_add_u32 m0, %[lb], 45056\n\t"
    "v_mfma_f32_16x16x32_bf16 v[92:95], v[156:159], v[124:127], v[92:95]\n\t"
    "global_load_lds_dwordx4 %[av7], s[36:37] sc1\n\t"
    "s_waitcnt lgkmcnt(4)\n\t"
    "v_mfma_f32_16x16x32_bf16 v[80:83], v[160:163], v[96:99], v[80:83]\n\t"
    "ds_read_b128 v[120:123], %[da] offset:12288\n\t"
    "s_add_u32 m0, %[lb], 16384\n\t"
    "v_mfma_f32_16x16x32_bf16 v[84:87], v[164:167], v[96:99], v[84:87]\n\t"
    "ds_read_b128 v[124:127], %[da] offset:14336\n\t"
    "global_load_lds_dwordx4 %[av0], s[38:39] sc1\n\t"
    "v_mfma_f32_16x16x32_bf16 v[88:91], v[160:163], v[100:103], v[88:91]\n\t"
    "s_add_u32 m0, %[lb], 49152\n\t"
    "v_mfma_f32_16x16x32_bf16 v[92:95], v[164:167], v[100:103], v[92:95]\n\t"
    "global_load_lds_dwordx4 %[av1], s[38:39] sc1\n\t"
    "s_waitcnt vmcnt(14)\n\t"
    "s_barrier\n\t"
    "s_waitcnt lgkmcnt(4)\n\t"
    "v_mfma_f32_16x16x32_bf16 v[80:83], v[168:171], v[104:107], v[80:83]\n\t"
    "ds_read_b128 v[96:99], %[ea] offset:16384\n\t"
    "s_add_u32 m0, %[lb], 20480\n\t"
    "v_mfma_f32_16x16x32_bf16 v[84:87], v[172:175], v[104:107], v[84:87]\n\t"
    "ds_read_b128 v[100:103], %[ea] offset:18432\n\t"
    "global_load_lds_dwordx4 %[av2], s[38:39] sc1\n\t"
    "v_mfma_f32_16x16x32_bf16 v[88:91], v[168:171], v[108:111], v[88:91]\n\t"
    "s_add_u32 m0, %[lb], 53248\n\t"
    "v_mfma_f32_16x16x32_bf16 v[92:95], v[172:175], v[108:111], v[92:95]\n\t"
    "global_load_lds_dwordx4 %[av3], s[38:39] sc1\n\t"
    "s_waitcnt lgkmcnt(4)\n\t"
    "v_mfma_f32_16x16x32_bf16 v[80:83], v[176:179], v[112:115], v[80:83]\n\t"
    "ds_read_b128 v[104:107], %[da] offset:16384\n\t"
    "s_add_u32 m0, %[lb], 24576\n\t"
    "v_mfma_f32_16x16x32_bf16 v[84:87], v[180:183], v[112:115], v[84:87]\n\t"
    "ds_read_b128 v[108:111], %[da] offset:18432\n\t"
    "global_load_lds_dwordx4 %[av4], s[38:39] sc1\n\t"
    "v_mfma_f32_16x16x32_bf16 v[88:91], v[176:179], v[116:119], v[88:91]\n\t"
    "s_add_u32 m0, %[lb], 57344\n\t"
    "v_mfma_f32_16x16x32_bf16 v[92:95], v[180:183], v[116:119], v[92:95]\n\t"
    "global_load_lds_dwordx4 %[av5], s[38:39] sc1\n\t"
    "s_waitcnt lgkmcnt(4)\n\t"
    "v_mfma_f32_16x16x32_bf16 v[80:83], v[184:187], v[120:123], v[80:83]\n\t"
    "ds_read_b128 v[112:115], %[ea] offset:20480\n\t"
    "s_add_u32 m0, %[lb], 28672\n\t"
    "v_mfma_f32_16x16x32_bf16 v[84:87], v[188:191], v[120:123], v[84:87]\n\t"
    "ds_read_b128 v[116:119], %[ea] offset:22528\n\t"
    "global_load_lds_dwordx4 %[av6], s[38:39] sc1\n\t"
    "v_mfma_f32_16x16x32_bf16 v[88:91], v[184:187], v[124:127], v[88:91]\n\t"
    "s_add_u32 m0, %[lb], 61440\n\t"
    "v_mfma_f32_16x16x32_bf16 v[92:95], v[188:191], v[124:127], v[92:95]\n\t"
    "global_load_lds_dwordx4 %[av7], s[38:39] sc1\n\t"
    "s_waitcnt lgkmcnt(4)\n\t"
    "v_mfma_f32_16x16x32_bf16 v[80:83], v[192:195], v[96:99], v[80:83]\n\t"
    "ds_read_b128 v[120:123], %[da] offset:20480\n\t"
    "v_mfma_f32_16x16x32_bf16 v[84:87], v[196:199], v[96:99], v[84:87]\n\t"
    "ds_read_b128 v[124:127], %[da] offset:22528\n\t"
    "v_mfma_f32_16x16x32_bf16 v[88:91], v[192:195], v[100:103], v[88:91]\n\t"
    "v_mfma_f32_16x16x32_bf16 v[92:95], v[196:199], v[100:103], v[92:95]\n\t"
    "s_add_u32 s36, s36, %[ab]\n\t"
    "s_addc_u32 s37, s37, 0\n\t"
    "s_add_u32 s38, s38, %[ab]\n\t"
    "s_addc_u32 s39, s39, 0\n\t"
    "s_xor_b32 %[lb], %[lb], 0x10000\n\t"
    "s_waitcnt vmcnt(16)\n\t"
    "s_barrier\n\t"
    "s_waitcnt lgkmcnt(4)\n\t"
    "v_mfma_f32_16x16x32_bf16 v[80:83], v[200:203], v[104:107], v[80:83]\n\t"
    "ds_read_b128 v[96:99], %[ea] offset:24576\n\t"
    "v_mfma_f32_16x16x32_bf16 v[84:87], v[204:207], v[104:107], v[84:87]\n\t"
    "ds_read_b128 v[100:103], %[ea] offset:26624\n\t"
    "v_mfma_f32_16x16x32_bf16 v[88:91], v[200:203], v[108:111], v[88:91]\n\t"
    "v_mfma_f32_16x16x32_bf16 v[92:95], v[204:207], v[108:111], v[92:95]\n\t"
    "s_waitcnt lgkmcnt(4)\n\t"
    "v_mfma_f32_16x16x32_bf16 v[80:83], v[208:211], v[112:115], v[80:83]\n\t"
    "ds_read_b128 v[104:107], %[da] offset:24576\n\t"
    "v_mfma_f32_16x16x32_bf16 v[84:87], v[212:215], v[112:115], v[84:87]\n\t"
    "ds_read_b128 v[108:111], %[da] offset:26624\n\t"
    "v_mfma_f32_16x16x32_bf16 v[88:91], v[208:211], v[116:119], v[88:91]\n\t"
    "v_mfma_f32_16x16x32_bf16 v[92:95], v[212:215], v[116:119], v[92:95]\n\t"
    "s_cmp_eq_u32 %[sl], 0\n\t"
    "s_cbranch_scc1 73f\n\t"
    "s_mov_b32 m0, %[sl]\n\t"
    "s_nop 0\n\t"
    "global_load_lds_dword %[dv], %[db] sc1\n\t"
    "73:\n\t"
    "s_waitcnt lgkmcnt(4)\n\t"
    "v_mfma_f32_16x16x32_bf16 v[80:83], v[216:219], v[120:123], v[80:83]\n\t"
    "ds_read_b128 v[112:115], %[ea] offset:28672\n\t"
    "v_mfma_f32_16x16x32_bf16 v[84:87], v[220:223], v[120:123], v[84:87]\n\t"
    "ds_read_b128 v[116:119], %[ea] offset:30720\n\t"
    "v_mfma_f32_16x16x32_bf16 v[88:91], v[216:219], v[124:127], v[88:91]\n\t"
    "v_mfma_f32_16x16x32_bf16 v[92:95], v[220:223], v[124:127], v[92:95]\n\t"
    "s_waitcnt lgkmcnt(4)\n\t"
    "v_mfma_f32_16x16x32_bf16 v[80:83], v[224:227], v[96:99], v[80:83]\n\t"
    "ds_read_b128 v[120:123], %[da] offset:28672\n\t"
    "v_mfma_f32_16x16x32_bf16 v[84:87], v[228:231], v[96:99], v[84:87]\n\t"
    "ds_read_b128 v[124:127], %[da] offset:30720\n\t"
    "v_mfma_f32_16x16x32_bf16 v[88:91], v[224:227], v[100:103], v[88:91]\n\t"
    "v_mfma_f32_16x16x32_bf16 v[92:95], v[228:231], v[100:103], v[92:95]\n\t"
    "s_waitcnt lgkmcnt(4)\n\t"
    "v_mfma_f32_16x16x32_bf16 v[80:83], v[232:235], v[104:107], v[80:83]\n\t"
    "v_mfma_f32_16x16x32_bf16 v[84:87], v[236:239], v[104:107], v[84:87]\n\t"
    "v_mfma_f32_16x16x32_bf16 v[88:91], v[232:235], v[108:111], v[88:91]\n\t"
    "v_xor_b32 %[ea], 0x10000, %[ea]\n\t"
    "v_mfma_f32_16x16x32_bf16 v[92:95], v[236:239], v[108:111], v[92:95]\n\t"
    "v_xor_b32 %[da], 0x10000, %[da]\n\t"
    "s_waitcnt lgkmcnt(2)\n\t"
    "v_mfma_f32_16x16x32_bf16 v[80:83], v[240:243], v[112:115], v[80:83]\n\t"
    "v_mfma_f32_16x16x32_bf16 v[84:87], v[244:247], v[112:115], v[84:87]\n\t"
    "v_mfma_f32_16x16x32_bf16 v[88:91], v[240:243], v[116:119], v[88:91]\n\t"
    "s_add_u32 %[b], %[b], 1\n\t"
    "v_mfma_f32_16x16x32_bf16 v[92:95], v[244:247], v[116:119], v[92:95]\n\t"
    "s_waitcnt lgkmcnt(0)\n\t"
    "v_mfma_f32_16x16x32_bf16 v[80:83], v[248:251], v[120:123], v[80:83]\n\t"
    "v_mfma_f32_16x16x32_bf16 v[84:87], v[252:255], v[120:123], v[84:87]\n\t"
    "v_mfma_f32_16x16x32_bf16 v[88:91], v[248:251], v[124:127], v[88:91]\n\t"
    "v_mfma_f32_16x16x32_bf16 v[92:95], v[252:255], v[124:127], v[92:95]\n\t"
    "v_mov_b32 v77, %[b]\n\t"
    "v_or_b32 v77, 0x100, v77\n\t"
    "s_nop 2\n\t"
    "ds_write_b128 %[rw], v[80:83] offset:0\n\t"
    "ds_write_b128 %[rw], v[84:87] offset:1024\n\t"
    "ds_write_b128 %[rw], v[88:91] offset:2048\n\t"
    "ds_write_b128 %[rw], v[92:95] offset:3072\n\t"
    "ds_write_b32 %[fa], v77\n\t"
    "s_waitcnt vmcnt(0)\n\t"
    "ds_read_b32 v79, %[sa]\n\t"
    "s_waitcnt lgkmcnt(0)\n\t"
    "v_mad_u32_u24 v79, v79, %[mu], %[ad]\n\t"
    "v_max_i32 v78, v78, v79\n\t"
    "s_branch 3b\n\t"
    "9:\n\t"
    "s_waitcnt lgkmcnt(0)"
    : [ea] "+&v"(ea), [da] "+&v"(da), [b] "+&s"(b), [st] "=&s"(st), "+{s[36:37]}"(p0), "+{s[38:39]}"(p1), [lb] "+&s"(lb), [cap] "+&s"(cap)
    : [ns] "s"(ns), [ab] "s"(ab), [rw] "v"(rw), [fa] "v"(fa), "{v[128:159]}"(W[0]), "{v[160:191]}"(W[1]), "{v[192:223]}"(W[2]), "{v[224:255]}"(W[3]), [wm] "s"(wm), [rq] "s"(rq), [mu] "s"(mu), [ad] "v"(ad), [dv] "v"(dv), [db] "s"(db), [sl] "s"(sl), [sa] "v"(sa), [av0] "v"(av[0]), [av1] "v"(av[1]), [av2] "v"(av[2]), [av3] "v"(av[3]), [av4] "v"(av[4]), [av5] "v"(av[5]), [av6] "v"(av[6]), [av7] "v"(av[7])
    : "v77", "v78", "v79", "v80", "v81", "v82", "v83", "v84", "v85", "v86", "v87", "v88", "v89", "v90", "v91", "v92", "v93", "v94", "v95", "v96", "v97", "v98", "v99", "v100", "v101", "v102", "v103", "v104", "v105", "v106", "v107", "v108", "v109", "v110", "v111", "v112", "v113", "v114", "v115", "v116", "v117", "v118", "v119", "v120", "v121", "v122", "v123", "v124", "v125", "v126", "v127", "vcc", "scc", "m0", "memory");
}
__device__ __forceinline__ void paced_ld2(const u32x32 (&W)[4], unsigned ea, unsigned da, int b, int ns, unsigned ab, unsigned rw, unsigned fa,
    const char* p0, const char* p1, unsigned lb, int& cap, int wm, unsigned rq, unsigned mu, unsigned ad, unsigned dv, const char* db, unsigned sl, unsigned sa, const unsigned (&av)[8]) {
  unsigned st;
  asm volatile("s_setprio 2\n\t"
    "v_mov_b32 v78, %[wm]\n\t"
    "s_cmp_lg_u32 %[rq], 0\n\t"
    "s_cbranch_scc1 3f\n\t"
    "2:\n\t"
    "v_cmp_ge_i32 vcc, %[b], v78\n\t"
    "s_cbranch_vccz 4f\n\t"
    "21:\n\t"
    "s_cmp_eq_u32 %[sl], 0\n\t"
    "s_cbranch_scc1 71f\n\t"
    "s_mov_b32 m0, %[sl]\n\t"
    "s_nop 0\n\t"
    "global_load_lds_dword %[dv], %[db] sc1\n\t"
    "71:\n\t"
    "s_sub_u32 %[cap], %[cap], 1\n\t"
    "s_max_i32 %[cap], %[cap], 0\n\t"
    "s_waitcnt vmcnt(0)\n\t"
    "ds_read_b32 v79, %[sa]\n\t"
    "s_waitcnt lgkmcnt(0)\n\t"
    "v_mad_u32_u24 v79, v79, %[mu], %[ad]\n\t"
    "v_max_i32 v78, v78, v79\n\t"
    "v_cmp_ge_i32 vcc, %[b], v78\n\t"
    "s_cbranch_vccz 4f\n\t"
    "s_sleep 1\n\t"
    "s_cmp_lg_u32 %[cap], 0\n\t"
    "s_cbranch_scc1 21b\n\t"
    "4:\n\t"
    "s_add_u32 m0, %[lb], 0\n\t"
    "s_nop 0\n\t"
    "global_load_lds_dwordx4 %[av0], s[36:37] sc1\n\t"
    "s_add_u32 m0, %[lb], 4096\n\t"
    "s_nop 0\n\t"
    "global_load_lds_dwordx4 %[av2], s[36:37] sc1\n\t"
    "s_add_u32 m0, %[lb], 8192\n\t"
    "s_nop 0\n\t"
    "global_load_lds_dwordx4 %[av4], s[36:37] sc1\n\t"
    "s_add_u32 m0, %[lb], 12288\n\t"
    "s_nop 0\n\t"
    "global_load_lds_dwordx4 %[av6], s[36:37] sc1\n\t"
    "s_add_u32 m0, %[lb], 16384\n\t"
    "s_nop 0\n\t"
    "global_load_lds_dwordx4 %[av0], s[38:39] sc1\n\t"
    "s_add_u32 m0, %[lb], 20480\n\t"
    "s_nop 0\n\t"
    "global_load_lds_dwordx4 %[av2], s[38:39] sc1\n\t"
    "s_add_u32 m0, %[lb], 24576\n\t"
    "s_nop 0\n\t"
    "global_load_lds_dwordx4 %[av4], s[38:39] sc1\n\t"
    "s_add_u32 m0, %[lb], 28672\n\t"
    "s_nop 0\n\t"
    "global_load_lds_dwordx4 %[av6], s[38:39] sc1\n\t"
    "s_add_u32 s36, s36, %[ab]\n\t"
    "s_addc_u32 s37, s37, 0\n\t"
    "s_add_u32 s38, s38, %[ab]\n\t"
    "s_addc_u32 s39, s39, 0\n\t"
    "s_xor_b32 %[lb], %[lb], 0x10000\n\t"
    "3:\n\t"
    "s_add_u32 %[st], %[b], 1\n\t"
    "v_min_i32 v78, %[ns], v78\n\t"
    "v_cmp_ge_i32 vcc, %[st], v78\n\t"
    "s_waitcnt vmcnt(6) lgkmcnt(0)\n\t"
    "s_barrier\n\t"
    "ds_read_b128 v[96:99], %[ea] offset:0\n\t"
    "ds_read_b128 v[100:103], %[ea] offset:2048\n\t"
    "ds_read_b128 v[104:107], %[da] offset:0\n\t"
    "ds_read_b128 v[108:111], %[da] offset:2048\n\t"
    "ds_read_b128 v[112:115], %[ea] offset:4096\n\t"
    "ds_read_b128 v[116:119], %[ea] offset:6144\n\t"
    "s_cbranch_vccz 5f\n\t"
    "s_waitcnt lgkmcnt(4)\n\t"
    "v_mfma_f32_16x16x32_bf16 v[80:83], v[128:131], v[96:99], 0\n\t"
    "ds_read_b128 v[120:123], %[da] offset:4096\n\t"
    "v_mfma_f32_16x16x32_bf16 v[84:87], v[132:135], v[96:99], 0\n\t"
    "ds_read_b128 v[124:127], %[da] offset:6144\n\t"
    "v_mfma_f32_16x16x32_bf16 v[88:91], v[128:131], v[100:103], 0\n\t"
    "v_mfma_f32_16x16x32_bf16 v[92:95], v[132:135], v[100:103], 0\n\t"
    "s_waitcnt vmcnt(4)\n\t"
    "s_barrier\n\t"
    "s_waitcnt lgkmcnt(4)\n\t"
    "v_mfma_f32_16x16x32_bf16 v[80:83], v[136:139], v[104:107], v[80:83]\n\t"
    "ds_read_b128 v[96:99], %[ea] offset:8192\n\t"
    "v_mfma_f32_16x16x32_bf16 v[84:87], v[140:143], v[104:107], v[84:87]\n\t"
    "ds_read_b128 v[100:103], %[ea] offset:10240\n\t"
    "v_mfma_f32_16x16x32_bf16 v[88:91], v[136:139], v[108:111], v[88:91]\n\t"
    "v_mfma_f32_16x16x32_bf16 v[92:95], v[140:143], v[108:111], v[92:95]\n\t"
    "s_waitcnt lgkmcnt(4)\n\t"
    "v_mfma_f32_16x16x32_bf16 v[80:83], v[144:147], v[112:115], v[80:83]\n\t"
    "ds_read_b128 v[104:107], %[da] offset:8192\n\t"
    "v_mfma_f32_16x16x32_bf16 v[84:87], v[148:151], v[112:115], v[84:87]\n\t"
    "ds_read_b128 v[108:111], %[da] offset:10240\n\t"
    "v_mfma_f32_16x16x32_bf16 v[88:91], v[144:147], v[116:119], v[88:91]\n\t"
    "v_mfma_f32_16x16x32_bf16 v[92:95], v[148:151], v[116:119], v[92:95]\n\t"
    "s_waitcnt lgkmcnt(4)\n\t"
    "v_mfma_f32_16x16x32_bf16 v[80:83], v[152:155], v[120:123], v[80:83]\n\t"
    "ds_read_b128 v[112:115], %[ea] offset:12288\n\t"
    "v_mfma_f32_16x16x32_bf16 v[84:87], v[156:159], v[120:123], v[84:87]\n\t"
    "ds_read_b128 v[116:119], %[ea] offset:14336\n\t"
    "v_mfma_f32_16x16x32_bf16 v[88:91], v[152:155], v[124:127], v[88:91]\n\t"
    "v_mfma_f32_16x16x32_bf16 v[92:95], v[156:159], v[124:127], v[92:95]\n\t"
    "s_waitcnt lgkmcnt(4)\n\t"
    "v_mfma_f32_16x16x32_bf16 v[80:83], v[160:163], v[96:99], v[80:83]\n\t"
    "ds_read_b128 v[120:123], %[da] offset:12288\n\t"
    "v_mfma_f32_16x16x32_bf16 v[84:87], v[164:167], v[96:99], v[84:87]\n\t"
    "ds_read_b128 v[124:127], %[da] offset:14336\n\t"
    "v_mfma_f32_16x16x32_bf16 v[88:91], v[160:163], v[100:103], v[88:91]\n\t"
    "v_mfma_f32_16x16x32_bf16 v[92:95], v[164:167], v[100:103], v[92:95]\n\t"
    "s_waitcnt vmcnt(2)\n\t"
    "s_barrier\n\t"
    "s_waitcnt lgkmcnt(4)\n\t"
    "v_mfma_f32_16x16x32_bf16 v[80:83], v[168:171], v[104:107], v[80:83]\n\t"
    "ds_read_b128 v[96:99], %[ea] offset:16384\n\t"
    "v_mfma_f32_16x16x32_bf16 v[84:87], v[172:175], v[104:107], v[84:87]\n\t"
    "ds_read_b128 v[100:103], %[ea] offset:18432\n\t"
    "v_mfma_f32_16x16x32_bf16 v[88:91], v[168:171], v[108:111], v[88:91]\n\t"
    "v_mfma_f32_16x16x32_bf16 v[92:95], v[172:175], v[108:111], v[92:95]\n\t"
    "s_waitcnt lgkmcnt(4)\n\t"
    "v_mfma_f32_16x16x32_bf16 v[80:83], v[176:179], v[112:115], v[80:83]\n\t"
    "ds_read_b128 v[104:107], %[da] offset:16384\n\t"
    "v_mfma_f32_16x16x32_bf16 v[84:87], v[180:183], v[112:115], v[84:87]\n\t"
    "ds_read_b128 v[108:111], %[da] offset:18432\n\t"
    "v_mfma_f32_16x16x32_bf16 v[88:91], v[176:179], v[116:119], v[88:91]\n\t"
    "v_mfma_f32_16x16x32_bf16 v[92:95], v[180:183], v[116:119], v[92:95]\n\t"
    "s_waitcnt lgkmcnt(4)\n\t"
    "v_mfma_f32_16x16x32_bf16 v[80:83], v[184:187], v[120:123], v[80:83]\n\t"
    "ds_read_b128 v[112:115], %[ea] offset:20480\n\t"
    "v_mfma_f32_16x16x32_bf16 v[84:87], v[188:191], v[120:123], v[84:87]\n\t"
    "ds_read_b128 v[116:119], %[ea] offset:22528\n\t"
    "v_mfma_f32_16x16x32_bf16 v[88:91], v[184:187], v[124:127], v[88:91]\n\t"
    "v_mfma_f32_16x16x32_bf16 v[92:95], v[188:191], v[124:127], v[92:95]\n\t"
    "s_waitcnt lgkmcnt(4)\n\t"
    "v_mfma_f32_16x16x32_bf16 v[80:83], v[192:195], v[96:99], v[80:83]\n\t"
    "ds_read_b128 v[120:123], %[da] offset:20480\n\t"
    "v_mfma_f32_16x16x32_bf16 v[84:87], v[196:199], v[96:99], v[84:87]\n\t"
    "ds_read_b128 v[124:127], %[da] offset:22528\n\t"
    "v_mfma_f32_16x16x32_bf16 v[88:91], v[192:195], v[100:103], v[88:91]\n\t"
    "v_mfma_f32_16x16x32_bf16 v[92:95], v[196:199], v[100:103], v[92:95]\n\t"
    "s_waitcnt vmcnt(0)\n\t"
    "s_barrier\n\t"
    "s_waitcnt lgkmcnt(4)\n\t"
    "v_mfma_f32_16x16x32_bf16 v[80:83], v[200:203], v[104:107], v[80:83]\n\t"
    "ds_read_b128 v[96:99], %[ea] offset:24576\n\t"
    "v_mfma_f32_16x16x32_bf16 v[84:87], v[204:207], v[104:107], v[84:87]\n\t"
    "ds_read_b128 v[100:103], %[ea] offset:26624\n\t"
    "v_mfma_f32_16x16x32_bf16 v[88:91], v[200:203], v[108:111], v[88:91]\n\t"
    "v_mfma_f32_16x16x32_bf16 v[92:95], v[204:207], v[108:111], v[92:95]\n\t"
    "s_waitcnt lgkmcnt(4)\n\t"
    "v_mfma_f32_16x16x32_bf16 v[80:83], v[208:211], v[112:115], v[80:83]\n\t"
    "ds_read_b128 v[104:107], %[da] offset:24576\n\t"
    "v_mfma_f32_16x16x32_bf16 v[84:87], v[212:215], v[112:115], v[84:87]\n\t"
    "ds_read_b128 v[108:111], %[da] offset:26624\n\t"
    "v_mfma_f32_16x16x32_bf16 v[88:91], v[208:211], v[116:119], v[88:91]\n\t"
    "v_mfma_f32_16x16x32_bf16 v[92:95], v[212:215], v[116:119], v[92:95]\n\t"
    "s_cmp_eq_u32 %[sl], 0\n\t"
    "s_cbranch_scc1 72f\n\t"
    "s_mov_b32 m0, %[sl]\n\t"
    "s_nop 0\n\t"
    "global_load_lds_dword %[dv], %[db] sc1\n\t"
    "72:\n\t"
    "s_waitcnt lgkmcnt(4)\n\t"
    "v_mfma_f32_16x16x32_bf16 v[80:83], v[216:219], v[120:123], v[80:83]\n\t"
    "ds_read_b128 v[112:115], %[ea] offset:28672\n\t"
    "v_mfma_f32_16x16x32_bf16 v[84:87], v[220:223], v[120:123], v[84:87]\n\t"
    "ds_read_b128 v[116:119], %[ea] offset:30720\n\t"
    "v_mfma_f32_16x16x32_bf16 v[88:91], v[216:219], v[124:127], v[88:91]\n\t"
    "v_mfma_f32_16x16x32_bf16 v[92:95], v[220:223], v[124:127], v[92:95]\n\t"
    "s_waitcnt lgkmcnt(4)\n\t"
    "v_mfma_f32_16x16x32_bf16 v[80:83], v[224:227], v[96:99], v[80:83]\n\t"
    "ds_read_b128 v[120:123], %[da] offset:28672\n\t"
    "v_mfma_f32_16x16x32_bf16 v[84:87], v[228:231], v[96:99], v[84:87]\n\t"
    "ds_read_b128 v[124:127], %[da] offset:30720\n\t"
    "v_mfma_f32_16x16x32_bf16 v[88:91], v[224:227], v[100:103], v[88:91]\n\t"
    "v_mfma_f32_16x16x32_bf16 v[92:95], v[228:231], v[100:103], v[92:95]\n\t"
    "s_waitcnt lgkmcnt(4)\n\t"
    "v_mfma_f32_16x16x32_bf16 v[80:83], v[232:235], v[104:107], v[80:83]\n\t"
    "v_mfma_f32_16x16x32_bf16 v[84:87], v[236:239], v[104:107], v[84:87]\n\t"
    "v_mfma_f32_16x16x32_bf16 v[88:91], v[232:235], v[108:111], v[88:91]\n\t"
    "v_xor_b32 %[ea], 0x10000, %[ea]\n\t"
    "v_mfma_f32_16x16x32_bf16 v[92:95], v[236:239], v[108:111], v[92:95]\n\t"
    "v_xor_b32 %[da], 0x10000, %[da]\n\t"
    "s_waitcnt lgkmcnt(2)\n\t"
    "v_mfma_f32_16x16x32_bf16 v[80:83], v[240:243], v[112:115], v[80:83]\n\t"
    "v_mfma_f32_16x16x32_bf16 v[84:87], v[244:247], v[112:115], v[84:87]\n\t"
    "v_mfma_f32_16x16x32_bf16 v[88:91], v[240:243], v[116:119], v[88:91]\n\t"
    "s_add_u32 %[b], %[b], 1\n\t"
    "v_mfma_f32_16x16x32_bf16 v[92:95], v[244:247], v[116:119], v[92:95]\n\t"
    "s_waitcnt lgkmcnt(0)\n\t"
    "v_mfma_f32_16x16x32_bf16 v[80:83], v[248:251], v[120:123], v[80:83]\n\t"
    "v_mfma_f32_16x16x32_bf16 v[84:87], v[252:255], v[120:123], v[84:87]\n\t"
    "v_mfma_f32_16x16x32_bf16 v[88:91], v[248:251], v[124:127], v[88:91]\n\t"
    "v_mfma_f32_16x16x32_bf16 v[92:95], v[252:255], v[124:127], v[92:95]\n\t"
    "s_nop 4\n\t"
    "ds_write_b128 %[rw], v[80:83] offset:0\n\t"
    "ds_write_b128 %[rw], v[84:87] offset:1024\n\t"
    "ds_write_b128 %[rw], v[88:91] offset:2048\n\t"
    "ds_write_b128 %[rw], v[92:95] offset:3072\n\t"
    "s_waitcnt vmcnt(0)\n\t"
    "ds_read_b32 v79, %[sa]\n\t"
    "s_waitcnt lgkmcnt(0)\n\t"
    "v_mad_u32_u24 v79, v79, %[mu], %[ad]\n\t"
    "v_max_i32 v78, v78, v79\n\t"
    "v_mov_b32 v77, %[b]\n\t"
    "v_cmp_ge_i32 vcc, %[b], v78\n\t"
    "s_cmp_ge_i32 %[b], %[ns]\n\t"
    "s_cbranch_scc1 6f\n\t"
    "s_cbranch_vccnz 6f\n\t"
    "v_or_b32 v77, 0x100, v77\n\t"
    "ds_write_b32 %[fa], v77\n\t"
    "s_branch 4b\n\t"
    "6:\n\t"
    "ds_write_b32 %[fa], v77\n\t"
    "s_cbranch_scc0 21b\n\t"
    "s_branch 9f\n\t"
    "5:\n\t"
    "s_waitcnt lgkmcnt(4)\n\t"
    "v_mfma_f32_16x16x32_bf16 v[80:83], v[128:131], v[96:99], 0\n\t"
    "ds_read_b128 v[120:123], %[da] offset:4096\n\t"
    "s_add_u32 m0, %[lb], 0\n\t"
    "v_mfma_f32_16x16x32_bf16 v[84:87], v[132:135], v[96:99], 0\n\t"
    "ds_read_b128 v[124:127], %[da] offset:6144\n\t"
    "global_load_lds_dwordx4 %[av0], s[36:37] sc1\n\t"
    "v_mfma_f32_16x16x32_bf16 v[88:91], v[128:131], v[100:103], 0\n\t"
    "v_mfma_f32_16x16x32_bf16 v[92:95], v[132:135], v[100:103], 0\n\t"
    "s_waitcnt vmcnt(5)\n\t"
    "s_barrier\n\t"
    "s_waitcnt lgkmcnt(4)\n\t"
    "v_mfma_f32_16x16x32_bf16 v[80:83], v[136:139], v[104:107], v[80:83]\n\t"
    "ds_read_b128 v[96:99], %[ea] offset:8192\n\t"
    "s_add_u32 m0, %[lb], 4096\n\t"
    "v_mfma_f32_16x16x32_bf16 v[84:87], v[140:143], v[104:107], v[84:87]\n\t"
    "ds_read_b128 v[100:103], %[ea] offset:10240\n\t"
    "global_load_lds_dwordx4 %[av2], s[36:37] sc1\n\t"
    "v_mfma_f32_16x16x32_bf16 v[88:91], v[136:139], v[108:111], v[88:91]\n\t"
    "v_mfma_f32_16x16x32_bf16 v[92:95], v[140:143], v[108:111], v[92:95]\n\t"
    "s_waitcnt lgkmcnt(4)\n\t"
    "v_mfma_f32_16x16x32_bf16 v[80:83], v[144:147], v[112:115], v[80:83]\n\t"
    "ds_read_b128 v[104:107], %[da] offset:8192\n\t"
    "s_add_u32 m0, %[lb], 8192\n\t"
    "v_mfma_f32_16x16x32_bf16 v[84:87], v[148:151], v[112:115], v[84:87]\n\t"
    "ds_read_b128 v[108:111], %[da] offset:10240\n\t"
    "global_load_lds_dwordx4 %[av4], s[36:37] sc1\n\t"
    "v_mfma_f32_16x16x32_bf16 v[88:91], v[144:147], v[116:119], v[88:91]\n\t"
    "v_mfma_f32_16x16x32_bf16 v[92:95], v[148:151], v[116:119], v[92:95]\n\t"
    "s_waitcnt lgkmcnt(4)\n\t"
    "v_mfma_f32_16x16x32_bf16 v[80:83], v[152:155], v[120:123], v[80:83]\n\t"
    "ds_read_b128 v[112:115], %[ea] offset:12288\n\t"
    "s_add_u32 m0, %[lb], 12288\n\t"
    "v_mfma_f32_16x16x32_bf16 v[84:87], v[156:159], v[120:123], v[84:87]\n\t"
    "ds_read_b128 v[116:119], %[ea] offset:14336\n\t"
    "global_load_lds_dwordx4 %[av6], s[36:37] sc1\n\t"
    "v_mfma_f32_16x16x32_bf16 v[88:91], v[152:155], v[124:127], v[88:91]\n\t"
    "v_mfma_f32_16x16x32_bf16 v[92:95], v[156:159], v[124:127], v[92:95]\n\t"
    "s_waitcnt lgkmcnt(4)\n\t"
    "v_mfma_f32_16x16x32_bf16 v[80:83], v[160:163], v[96:99], v[80:83]\n\t"
    "ds_read_b128 v[120:123], %[da] offset:12288\n\t"
    "s_add_u32 m0, %[lb], 16384\n\t"
    "v_mfma_f32_16x16x32_bf16 v[84:87], v[164:167], v[96:99], v[84:87]\n\t"
    "ds_read_b128 v[124:127], %[da] offset:14336\n\t"
    "global_load_lds_dwordx4 %[av0], s[38:39] sc1\n\t"
    "v_mfma_f32_16x16x32_bf16 v[88:91], v[160:163], v[100:103], v[88:91]\n\t"
    "v_mfma_f32_16x16x32_bf16 v[92:95], v[164:167], v[100:103], v[92:95]\n\t"
    "s_waitcnt vmcnt(7)\n\t"
    "s_barrier\n\t"
    "s_waitcnt lgkmcnt(4)\n\t"
    "v_mfma_f32_16x16x32_bf16 v[80:83], v[168:171], v[104:107], v[80:83]\n\t"
    "ds_read_b128 v[96:99], %[ea] offset:16384\n\t"
    "s_add_u32 m0, %[lb], 20480\n\t"
    "v_mfma_f32_16x16x32_bf16 v[84:87], v[172:175], v[104:107], v[84:87]\n\t"
    "ds_read_b128 v[100:103], %[ea] offset:18432\n\t"
    "global_load_lds_dwordx4 %[av2], s[38:39] sc1\n\t"
    "v_mfma_f32_16x16x32_bf16 v[88:91], v[168:171], v[108:111], v[88:91]\n\t"
    "v_mfma_f32_16x16x32_bf16 v[92:95], v[172:175], v[108:111], v[92:95]\n\t"
    "s_waitcnt lgkmcnt(4)\n\t"
    "v_mfma_f32_16x16x32_bf16 v[80:83], v[176:179], v[112:115], v[80:83]\n\t"
    "ds_read_b128 v[104:107], %[da] offset:16384\n\t"
    "s_add_u32 m0, %[lb], 24576\n\t"
    "v_mfma_f32_16x16x32_bf16 v[84:87], v[180:183], v[112:115], v[84:87]\n\t"
    "ds_read_b128 v[108:111], %[da] offset:18432\n\t"
    "global_load_lds_dwordx4 %[av4], s[38:39] sc1\n\t"
    "v_mfma_f32_16x16x32_bf16 v[88:91], v[176:179], v[116:119], v[88:91]\n\t"
    "v_mfma_f32_16x16x32_bf16 v[92:95], v[180:183], v[116:119], v[92:95]\n\t"
    "s_waitcnt lgkmcnt(4)\n\t"
    "v_mfma_f32_16x16x32_bf16 v[80:83], v[184:187], v[120:123], v[80:83]\n\t"
    "ds_read_b128 v[112:115], %[ea] offset:20480\n\t"
    "s_add_u32 m0, %[lb], 28672\n\t"
    "v_mfma_f32_16x16x32_bf16 v[84:87], v[188:191], v[120:123], v[84:87]\n\t"
    "ds_read_b128 v[116:119], %[ea] offset:22528\n\t"
    "global_load_lds_dwordx4 %[av6], s[38:39] sc1\n\t"
    "v_mfma_f32_16x16x32_bf16 v[88:91], v[184:187], v[124:127], v[88:91]\n\t"
    "v_mfma_f32_16x16x32_bf16 v[92:95], v[188:191], v[124:127], v[92:95]\n\t"
    "s_waitcnt lgkmcnt(4)\n\t"
    "v_mfma_f32_16x16x32_bf16 v[80:83], v[192:195], v[96:99], v[80:83]\n\t"
    "ds_read_b128 v[120:123], %[da] offset:20480\n\t"
    "v_mfma_f32_16x16x32_bf16 v[84:87], v[196:199], v[96:99], v[84:87]\n\t"
    "ds_read_b128 v[124:127], %[da] offset:22528\n\t"
    "v_mfma_f32_16x16x32_bf16 v[88:91], v[192:195], v[100:103], v[88:91]\n\t"
    "v_mfma_f32_16x16x32_bf16 v[92:95], v[196:199], v[100:103], v[92:95]\n\t"
    "s_add_u32 s36, s36, %[ab]\n\t"
    "s_addc_u32 s37, s37, 0\n\t"
    "s_add_u32 s38, s38, %[ab]\n\t"
    "s_addc_u32 s39, s39, 0\n\t"
    "s_xor_b32 %[lb], %[lb], 0x10000\n\t"
    "s_waitcnt vmcnt(8)\n\t"
    "s_barrier\n\t"
    "s_waitcnt lgkmcnt(4)\n\t"
    "v_mfma_f32_16x16x32_bf16 v[80:83], v[200:203], v[104:107], v[80:83]\n\t"
    "ds_read_b128 v[96:99], %[ea] offset:24576\n\t"
    "v_mfma_f32_16x16x32_bf16 v[84:87], v[204:207], v[104:107], v[84:87]\n\t"
    "ds_read_b128 v[100:103], %[ea] offset:26624\n\t"
    "v_mfma_f32_16x16x32_bf16 v[88:91], v[200:203], v[108:111], v[88:91]\n\t"
    "v_mfma_f32_16x16x32_bf16 v[92:95], v[204:207], v[108:111], v[92:95]\n\t"
    "s_waitcnt lgkmcnt(4)\n\t"
    "v_mfma_f32_16x16x32_bf16 v[80:83], v[208:211], v[112:115], v[80:83]\n\t"
    "ds_read_b128 v[104:107], %[da] offset:24576\n\t"
    "v_mfma_f32_16x16x32_bf16 v[84:87], v[212:215], v[112:115], v[84:87]\n\t"
    "ds_read_b128 v[108:111], %[da] offset:26624\n\t"
    "v_mfma_f32_16x16x32_bf16 v[88:91], v[208:211], v[116:119], v[88:91]\n\t"
    "v_mfma_f32_16x16x32_bf16 v[92:95], v[212:215], v[116:119], v[92:95]\n\t"
    "s_cmp_eq_u32 %[sl], 0\n\t"
    "s_cbranch_scc1 73f\n\t"
    "s_mov_b32 m0, %[sl]\n\t"
    "s_nop 0\n\t"
    "global_load_lds_dword %[dv], %[db] sc1\n\t"
    "73:\n\t"
    "s_waitcnt lgkmcnt(4)\n\t"
    "v_mfma_f32_16x16x32_bf16 v[80:83], v[216:219], v[120:123], v[80:83]\n\t"
    "ds_read_b128 v[112:115], %[ea] offset:28672\n\t"
    "v_mfma_f32_16x16x32_bf16 v[84:87], v[220:223], v[120:123], v[84:87]\n\t"
    "ds_read_b128 v[116:119], %[ea] offset:30720\n\t"
    "v_mfma_f32_16x16x32_bf16 v[88:91], v[216:219], v[124:127], v[88:91]\n\t"
    "v_mfma_f32_16x16x32_bf16 v[92:95], v[220:223], v[124:127], v[92:95]\n\t"
    "s_waitcnt lgkmcnt(4)\n\t"
    "v_mfma_f32_16x16x32_bf16 v[80:83], v[224:227], v[96:99], v[80:83]\n\t"
    "ds_read_b128 v[120:123], %[da] offset:28672\n\t"
    "v_mfma_f32_16x16x32_bf16 v[84:87], v[228:231], v[96:99], v[84:87]\n\t"
    "ds_read_b128 v[124:127], %[da] offset:30720\n\t"
    "v_mfma_f32_16x16x32_bf16 v[88:91], v[224:227], v[100:103], v[88:91]\n\t"
    "v_mfma_f32_16x16x32_bf16 v[92:95], v[228:231], v[100:103], v[92:95]\n\t"
    "s_waitcnt lgkmcnt(4)\n\t"
    "v_mfma_f32_16x16x32_bf16 v[80:83], v[232:235], v[104:107], v[80:83]\n\t"
    "v_mfma_f32_16x16x32_bf16 v[84:87], v[236:239], v[104:107], v[84:87]\n\t"
    "v_mfma_f32_16x16x32_bf16 v[88:91], v[232:235], v[108:111], v[88:91]\n\t"
    "v_xor_b32 %[ea], 0x10000, %[ea]\n\t"
    "v_mfma_f32_16x16x32_bf16 v[92:95], v[236:239], v[108:111], v[92:95]\n\t"
    "v_xor_b32 %[da], 0x10000, %[da]\n\t"
    "s_waitcnt lgkmcnt(2)\n\t"
    "v_mfma_f32_16x16x32_bf16 v[80:83], v[240:243], v[112:115], v[80:83]\n\t"
    "v_mfma_f32_16x16x32_bf16 v[84:87], v[244:247], v[112:115], v[84:87]\n\t"
    "v_mfma_f32_16x16x32_bf16 v[88:91], v[240:243], v[116:119], v[88:91]\n\t"
    "s_add_u32 %[b], %[b], 1\n\t"
    "v_mfma_f32_16x16x32_bf16 v[92:95], v[244:247], v[116:119], v[92:95]\n\t"
    "s_waitcnt lgkmcnt(0)\n\t"
    "v_mfma_f32_16x16x32_bf16 v[80:83], v[248:251], v[120:123], v[80:83]\n\t"
    "v_mfma_f32_16x16x32_bf16 v[84:87], v[252:255], v[120:123], v[84:87]\n\t"
    "v_mfma_f32_16x16x32_bf16 v[88:91], v[248:251], v[124:127], v[88:91]\n\t"
    "v_mfma_f32_16x16x32_bf16 v[92:95], v[252:255], v[124:127], v[92:95]\n\t"
    "v_mov_b32 v77, %[b]\n\t"
    "v_or_b32 v77, 0x100, v77\n\t"
    "s_nop 2\n\t"
    "ds_write_b128 %[rw], v[80:83] offset:0\n\t"
    "ds_write_b128 %[rw], v[84:87] offset:1024\n\t"
    "ds_write_b128 %[rw], v[88:91] offset:2048\n\t"
    "ds_write_b128 %[rw], v[92:95] offset:3072\n\t"
    "ds_write_b32 %[fa], v77\n\t"
    "s_waitcnt vmcnt(0)\n\t"
    "ds_read_b32 v79, %[sa]\n\t"
    "s_waitcnt lgkmcnt(0)\n\t"
    "v_mad_u32_u24 v79, v79, %[mu], %[ad]\n\t"
    "v_max_i32 v78, v78, v79\n\t"
    "s_branch 3b\n\t"
    "9:\n\t"
    "s_waitcnt lgkmcnt(0)"
    : [ea] "+&v"(ea), [da] "+&v"(da), [b] "+&s"(b), [st] "=&s"(st), "+{s[36:37]}"(p0), "+{s[38:39]}"(p1), [lb] "+&s"(lb), [cap] "+&s"(cap)
    : [ns] "s"(ns), [ab] "s"(ab), [rw] "v"(rw), [fa] "v"(fa), "{v[128:159]}"(W[0]), "{v[160:191]}"(W[1]), "{v[192:223]}"(W[2]), "{v[224:255]}"(W[3]), [wm] "s"(wm), [rq] "s"(rq), [mu] "s"(mu), [ad] "v"(ad), [dv] "v"(dv), [db] "s"(db), [sl] "s"(sl), [sa] "v"(sa), [av0] "v"(av[0]), [av1] "v"(av[1]), [av2] "v"(av[2]), [av3] "v"(av[3]), [av4] "v"(av[4]), [av5] "v"(av[5]), [av6] "v"(av[6]), [av7] "v"(av[7])
    : "v77", "v78", "v79", "v80", "v81", "v82", "v83", "v84", "v85", "v86", "v87", "v88", "v89", "v90", "v91", "v92", "v93", "v94", "v95", "v96", "v97", "v98", "v99", "v100", "v101", "v102", "v103", "v104", "v105", "v106", "v107", "v108", "v109", "v110", "v111", "v112", "v113", "v114", "v115", "v116", "v117", "v118", "v119", "v120", "v121", "v122", "v123", "v124", "v125", "v126", "v127", "vcc", "scc", "m0", "memory");
}
__device__ __forceinline__ void paced_st0(const u32x32 (&W)[4], unsigned ea, unsigned da, int b, int ns, unsigned ab, unsigned rw, unsigned fa,
    const char* p0, unsigned pp, int po, unsigned cn, const char* pg, unsigned co0, unsigned co1) {
  unsigned st;
  asm volatile("1:\n\t"
    "s_barrier\n\t"
    "ds_read_b128 v[96:99], %[ea] offset:0\n\t"
    "ds_read_b128 v[100:103], %[ea] offset:2048\n\t"
    "ds_read_b128 v[104:107], %[da] offset:0\n\t"
    "ds_read_b128 v[108:111], %[da] offset:2048\n\t"
    "ds_read_b128 v[112:115], %[ea] offset:4096\n\t"
    "ds_read_b128 v[116:119], %[ea] offset:6144\n\t"
    "s_waitcnt lgkmcnt(4)\n\t"
    "v_mfma_f32_16x16x32_bf16 v[80:83], v[128:131], v[96:99], 0\n\t"
    "ds_read_b128 v[120:123], %[da] offset:4096\n\t"
    "v_mfma_f32_16x16x32_bf16 v[84:87], v[132:135], v[96:99], 0\n\t"
    "ds_read_b128 v[124:127], %[da] offset:6144\n\t"
    "v_mfma_f32_16x16x32_bf16 v[88:91], v[128:131], v[100:103], 0\n\t"
    "v_mfma_f32_16x16x32_bf16 v[92:95], v[132:135], v[100:103], 0\n\t"
    "s_barrier\n\t"
    "s_waitcnt lgkmcnt(4)\n\t"
    "v_mfma_f32_16x16x32_bf16 v[80:83], v[136:139], v[104:107], v[80:83]\n\t"
    "ds_read_b128 v[96:99], %[ea] offset:8192\n\t"
    "v_mfma_f32_16x16x32_bf16 v[84:87], v[140:143], v[104:107], v[84:87]\n\t"
    "ds_read_b128 v[100:103], %[ea] offset:10240\n\t"
    "v_mfma_f32_16x16x32_bf16 v[88:91], v[136:139], v[108:111], v[88:91]\n\t"
    "v_mfma_f32_16x16x32_bf16 v[92:95], v[140:143], v[108:111], v[92:95]\n\t"
    "s_waitcnt lgkmcnt(4)\n\t"
    "v_mfma_f32_16x16x32_bf16 v[80:83], v[144:147], v[112:115], v[80:83]\n\t"
    "ds_read_b128 v[104:107], %[da] offset:8192\n\t"
    "v_mfma_f32_16x16x32_bf16 v[84:87], v[148:151], v[112:115], v[84:87]\n\t"
    "ds_read_b128 v[108:111], %[da] offset:10240\n\t"
    "v_mfma_f32_16x16x32_bf16 v[88:91], v[144:147], v[116:119], v[88:91]\n\t"
    "v_mfma_f32_16x16x32_bf16 v[92:95], v[148:151], v[116:119], v[92:95]\n\t"
    "s_waitcnt lgkmcnt(4)\n\t"
    "v_mfma_f32_16x16x32_bf16 v[80:83], v[152:155], v[120:123], v[80:83]\n\t"
    "ds_read_b128 v[112:115], %[ea] offset:12288\n\t"
    "v_mfma_f32_16x16x32_bf16 v[84:87], v[156:159], v[120:123], v[84:87]\n\t"
    "ds_read_b128 v[116:119], %[ea] offset:14336\n\t"
    "v_mfma_f32_16x16x32_bf16 v[88:91], v[152:155], v[124:127], v[88:91]\n\t"
    "v_mfma_f32_16x16x32_bf16 v[92:95], v[156:159], v[124:127], v[92:95]\n\t"
    "s_cmp_lg_u32 %[pp], 0\n\t"
    "s_cbranch_scc1 7f\n\t"
    "8:\n\t"
    "s_waitcnt lgkmcnt(4)\n\t"
    "v_mfma_f32_16x16x32_bf16 v[80:83], v[160:163], v[96:99], v[80:83]\n\t"
    "ds_read_b128 v[120:123], %[da] offset:12288\n\t"
    "v_mfma_f32_16x16x32_bf16 v[84:87], v[164:167], v[96:99], v[84:87]\n\t"
    "ds_read_b128 v[124:127], %[da] offset:14336\n\t"
    "v_mfma_f32_16x16x32_bf16 v[88:91], v[160:163], v[100:103], v[88:91]\n\t"
    "v_mfma_f32_16x16x32_bf16 v[92:95], v[164:167], v[100:103], v[92:95]\n\t"
    "s_barrier\n\t"
    "s_waitcnt lgkmcnt(4)\n\t"
    "v_mfma_f32_16x16x32_bf16 v[80:83], v[168:171], v[104:107], v[80:83]\n\t"
    "ds_read_b128 v[96:99], %[ea] offset:16384\n\t"
    "v_mfma_f32_16x16x32_bf16 v[84:87], v[172:175], v[104:107], v[84:87]\n\t"
    "ds_read_b128 v[100:103], %[ea] offset:18432\n\t"
    "v_mfma_f32_16x16x32_bf16 v[88:91], v[168:171], v[108:111], v[88:91]\n\t"
    "v_mfma_f32_16x16x32_bf16 v[92:95], v[172:175], v[108:111], v[92:95]\n\t"
    "s_waitcnt lgkmcnt(4)\n\t"
    "v_mfma_f32_16x16x32_bf16 v[80:83], v[176:179], v[112:115], v[80:83]\n\t"
    "ds_read_b128 v[104:107], %[da] offset:16384\n\t"
    "v_mfma_f32_16x16x32_bf16 v[84:87], v[180:183], v[112:115], v[84:87]\n\t"
    "ds_read_b128 v[108:111], %[da] offset:18432\n\t"
    "v_mfma_f32_16x16x32_bf16 v[88:91], v[176:179], v[116:119], v[88:91]\n\t"
    "v_mfma_f32_16x16x32_bf16 v[92:95], v[180:183], v[116:119], v[92:95]\n\t"
    "s_waitcnt lgkmcnt(4)\n\t"
    "v_mfma_f32_16x16x32_bf16 v[80:83], v[184:187], v[120:123], v[80:83]\n\t"
    "ds_read_b128 v[112:115], %[ea] offset:20480\n\t"
    "v_mfma_f32_16x16x32_bf16 v[84:87], v[188:191], v[120:123], v[84:87]\n\t"
    "ds_read_b128 v[116:119], %[ea] offset:22528\n\t"
    "v_mfma_f32_16x16x32_bf16 v[88:91], v[184:187], v[124:127], v[88:91]\n\t"
    "v_mfma_f32_16x16x32_bf16 v[92:95], v[188:191], v[124:127], v[92:95]\n\t"
    "s_waitcnt lgkmcnt(4)\n\t"
    "v_mfma_f32_16x16x32_bf16 v[80:83], v[192:195], v[96:99], v[80:83]\n\t"
    "ds_read_b128 v[120:123], %[da] offset:20480\n\t"
    "v_mfma_f32_16x16x32_bf16 v[84:87], v[196:199], v[96:99], v[84:87]\n\t"
    "ds_read_b128 v[124:127], %[da] offset:22528\n\t"
    "v_mfma_f32_16x16x32_bf16 v[88:91], v[192:195], v[100:103], v[88:91]\n\t"
    "v_mfma_f32_16x16x32_bf16 v[92:95], v[196:199], v[100:103], v[92:95]\n\t"
    "s_barrier\n\t"
    "s_waitcnt lgkmcnt(4)\n\t"
    "v_mfma_f32_16x16x32_bf16 v[80:83], v[200:203], v[104:107], v[80:83]\n\t"
    "ds_read_b128 v[96:99], %[ea] offset:24576\n\t"
    "v_mfma_f32_16x16x32_bf16 v[84:87], v[204:207], v[104:107], v[84:87]\n\t"
    "ds_read_b128 v[100:103], %[ea] offset:26624\n\t"
    "v_mfma_f32_16x16x32_bf16 v[88:91], v[200:203], v[108:111], v[88:91]\n\t"
    "v_mfma_f32_16x16x32_bf16 v[92:95], v[204:207], v[108:111], v[92:95]\n\t"
    "s_waitcnt lgkmcnt(4)\n\t"
    "v_mfma_f32_16x16x32_bf16 v[80:83], v[208:211], v[112:115], v[80:83]\n\t"
    "ds_read_b128 v[104:107], %[da] offset:24576\n\t"
    "v_mfma_f32_16x16x32_bf16 v[84:87], v[212:215], v[112:115], v[84:87]\n\t"
    "ds_read_b128 v[108:111], %[da] offset:26624\n\t"
    "v_mfma_f32_16x16x32_bf16 v[88:91], v[208:211], v[116:119], v[88:91]\n\t"
    "v_mfma_f32_16x16x32_bf16 v[92:95], v[212:215], v[116:119], v[92:95]\n\t"
    "s_waitcnt lgkmcnt(4)\n\t"
    "v_mfma_f32_16x16x32_bf16 v[80:83], v[216:219], v[120:123], v[80:83]\n\t"
    "ds_read_b128 v[112:115], %[ea] offset:28672\n\t"
    "v_mfma_f32_16x16x32_bf16 v[84:87], v[220:223], v[120:123], v[84:87]\n\t"
    "ds_read_b128 v[116:119], %[ea] offset:30720\n\t"
    "v_mfma_f32_16x16x32_bf16 v[88:91], v[216:219], v[124:127], v[88:91]\n\t"
    "v_mfma_f32_16x16x32_bf16 v[92:95], v[220:223], v[124:127], v[92:95]\n\t"
    "s_waitcnt lgkmcnt(4)\n\t"
    "v_mfma_f32_16x16x32_bf16 v[80:83], v[224:227], v[96:99], v[80:83]\n\t"
    "ds_read_b128 v[120:123], %[da] offset:28672\n\t"
    "v_mfma_f32_16x16x32_bf16 v[84:87], v[228:231], v[96:99], v[84:87]\n\t"
    "ds_read_b128 v[124:127], %[da] offset:30720\n\t"
    "v_mfma_f32_16x16x32_bf16 v[88:91], v[224:227], v[100:103], v[88:91]\n\t"
    "v_mfma_f32_16x16x32_bf16 v[92:95], v[228:231], v[100:103], v[92:95]\n\t"
    "s_waitcnt lgkmcnt(4)\n\t"
    "v_mfma_f32_16x16x32_bf16 v[80:83], v[232:235], v[104:107], v[80:83]\n\t"
    "v_mfma_f32_16x16x32_bf16 v[84:87], v[236:239], v[104:107], v[84:87]\n\t"
    "v_mfma_f32_16x16x32_bf16 v[88:91], v[232:235], v[108:111], v[88:91]\n\t"
    "v_xor_b32 %[ea], 0x10000, %[ea]\n\t"
    "v_mfma_f32_16x16x32_bf16 v[92:95], v[236:239], v[108:111], v[92:95]\n\t"
    "v_xor_b32 %[da], 0x10000, %[da]\n\t"
    "s_waitcnt lgkmcnt(2)\n\t"
    "v_mfma_f32_16x16x32_bf16 v[80:83], v[240:243], v[112:115], v[80:83]\n\t"
    "v_mfma_f32_16x16x32_bf16 v[84:87], v[244:247], v[112:115], v[84:87]\n\t"
    "v_mfma_f32_16x16x32_bf16 v[88:91], v[240:243], v[116:119], v[88:91]\n\t"
    "s_add_u32 %[b], %[b], 1\n\t"
    "v_mfma_f32_16x16x32_bf16 v[92:95], v[244:247], v[116:119], v[92:95]\n\t"
    "s_waitcnt lgkmcnt(0)\n\t"
    "v_mfma_f32_16x16x32_bf16 v[80:83], v[248:251], v[120:123], v[80:83]\n\t"
    "v_mfma_f32_16x16x32_bf16 v[84:87], v[252:255], v[120:123], v[84:87]\n\t"
    "v_mfma_f32_16x16x32_bf16 v[88:91], v[248:251], v[124:127], v[88:91]\n\t"
    "v_mfma_f32_16x16x32_bf16 v[92:95], v[252:255], v[124:127], v[92:95]\n\t"
    "2:\n\t"
    "ds_read_b32 v39, %[fa]\n\t"
    "ds_read_b128 v[48:51], %[rw] offset:0\n\t"
    "ds_read_b128 v[52:55], %[rw] offset:1024\n\t"
    "ds_read_b128 v[56:59], %[rw] offset:2048\n\t"
    "ds_read_b128 v[60:63], %[rw] offset:3072\n\t"
    "s_waitcnt lgkmcnt(2)\n\t"
    "v_readfirstlane_b32 %[pp], v39\n\t"
    "s_and_b32 %[st], %[pp], 0xff\n\t"
    "s_cmp_eq_u32 %[st], %[b]\n\t"
    "s_cbranch_scc1 3f\n\t"
    "s_waitcnt lgkmcnt(0)\n\t"
    "s_branch 2b\n\t"
    "3:\n\t"
    "v_add_f32 v48, v48, v80\n\t"
    "v_add_f32 v49, v49, v81\n\t"
    "v_add_f32 v50, v50, v82\n\t"
    "v_add_f32 v51, v51, v83\n\t"
    "v_add_f32 v52, v52, v84\n\t"
    "v_add_f32 v53, v53, v85\n\t"
    "v_add_f32 v54, v54, v86\n\t"
    "v_add_f32 v55, v55, v87\n\t"
    "v_cvt_pk_bf16_f32 v40, v48, v49\n\t"
    "v_cvt_pk_bf16_f32 v41, v50, v51\n\t"
    "v_cvt_pk_bf16_f32 v42, v52, v53\n\t"
    "v_cvt_pk_bf16_f32 v43, v54, v55\n\t"
    "global_store_dwordx4 %[co0], v[40:43], s[36:37] sc1\n\t"
    "s_waitcnt lgkmcnt(0)\n\t"
    "v_add_f32 v56, v56, v88\n\t"
    "v_add_f32 v57, v57, v89\n\t"
    "v_add_f32 v58, v58, v90\n\t"
    "v_add_f32 v59, v59, v91\n\t"
    "v_add_f32 v60, v60, v92\n\t"
    "v_add_f32 v61, v61, v93\n\t"
    "v_add_f32 v62, v62, v94\n\t"
    "v_add_f32 v63, v63, v95\n\t"
    "v_cvt_pk_bf16_f32 v44, v56, v57\n\t"
    "v_cvt_pk_bf16_f32 v45, v58, v59\n\t"
    "v_cvt_pk_bf16_f32 v46, v60, v61\n\t"
    "v_cvt_pk_bf16_f32 v47, v62, v63\n\t"
    "global_store_dwordx4 %[co1], v[44:47], s[36:37] sc1\n\t"
    "s_add_u32 s36, s36, %[ab]\n\t"
    "s_addc_u32 s37, s37, 0\n\t"
    "s_lshr_b32 %[pp], %[pp], 8\n\t"
    "s_cbranch_scc1 1b\n\t"
    "v_mov_b32 v72, %[cn]\n\t"
    "v_mov_b32 v73, 1\n\t"
    "s_add_u32 %[st], %[b], %[po]\n\t"
    "v_mov_b32 v74, %[st]\n\t"
    "v_mov_b32 v75, 0\n\t"
    "s_waitcnt vmcnt(0)\n\t"
    "s_mov_b64 exec, 1\n\t"
    "ds_add_rtn_u32 v73, v72, v73\n\t"
    "s_waitcnt lgkmcnt(0)\n\t"
    "v_readfirstlane_b32 %[st], v73\n\t"
    "s_and_b32 %[st], %[st], 3\n\t"
    "s_cmp_lg_u32 %[st], 3\n\t"
    "s_cbranch_scc1 61f\n\t"
    "global_store_dword v75, v74, %[pg] sc1\n\t"
    "61:\n\t"
    "s_mov_b64 exec, -1\n\t"
    "s_mov_b32 %[pp], 0\n\t"
    "s_cmp_lt_i32 %[b], %[ns]\n\t"
    "s_cbranch_scc1 1b\n\t"
    "s_branch 9f\n\t"
    "7:\n\t"
    "v_mov_b32 v72, %[cn]\n\t"
    "v_mov_b32 v73, 1\n\t"
    "s_add_u32 %[st], %[b], %[po]\n\t"
    "v_mov_b32 v74, %[st]\n\t"
    "v_mov_b32 v75, 0\n\t"
    "s_waitcnt vmcnt(0)\n\t"
    "s_mov_b64 exec, 1\n\t"
    "ds_add_rtn_u32 v73, v72, v73\n\t"
    "s_waitcnt lgkmcnt(0)\n\t"
    "v_readfirstlane_b32 %[st], v73\n\t"
    "s_and_b32 %[st], %[st], 3\n\t"
    "s_cmp_lg_u32 %[st], 3\n\t"
    "s_cbranch_scc1 61f\n\t"
    "global_store_dword v75, v74, %[pg] sc1\n\t"
    "61:\n\t"
    "s_mov_b64 exec, -1\n\t"
    "s_mov_b32 %[pp], 0\n\t"
    "s_branch 8b\n\t"
    "9:"
    : [ea] "+&v"(ea), [da] "+&v"(da), [b] "+&s"(b), [st] "=&s"(st), "+{s[36:37]}"(p0), [pp] "+&s"(pp)
    : [ns] "s"(ns), [ab] "s"(ab), [rw] "v"(rw), [fa] "v"(fa), "{v[128:159]}"(W[0]), "{v[160:191]}"(W[1]), "{v[192:223]}"(W[2]), "{v[224:255]}"(W[3]), [po] "s"(po), [cn] "s"(cn), [pg] "s"(pg), [co0] "v"(co0), [co1] "v"(co1)
    : "v39", "v40", "v41", "v42", "v43", "v44", "v45", "v46", "v47", "v48", "v49", "v50", "v51", "v52", "v53", "v54", "v55", "v56", "v57", "v58", "v59", "v60", "v61", "v62", "v63", "v72", "v73", "v74", "v75", "v80", "v81", "v82", "v83", "v84", "v85", "v86", "v87", "v88", "v89", "v90", "v91", "v92", "v93", "v94", "v95", "v96", "v97", "v98", "v99", "v100", "v101", "v102", "v103", "v104", "v105", "v106", "v107", "v108", "v109", "v110", "v111", "v112", "v113", "v114", "v115", "v116", "v117", "v118", "v119", "v120", "v121", "v122", "v123", "v124", "v125", "v126", "v127", "vcc", "scc", "m0", "memory");
}
__device__ __forceinline__ void paced_st1(const u32x32 (&W)[4], unsigned ea, unsigned da, int b, int ns, unsigned ab, unsigned rw, unsigned fa,
    const char* p0, unsigned pp, int po, unsigned cn, const char* pg, unsigned co0, unsigned co1) {
  unsigned st;
  asm volatile("1:\n\t"
    "s_barrier\n\t"
    "ds_read_b128 v[96:99], %[ea] offset:0\n\t"
    "ds_read_b128 v[100:103], %[ea] offset:2048\n\t"
    "ds_read_b128 v[104:107], %[da] offset:0\n\t"
    "ds_read_b128 v[108:111], %[da] offset:2048\n\t"
    "ds_read_b128 v[112:115], %[ea] offset:4096\n\t"
    "ds_read_b128 v[116:119], %[ea] offset:6144\n\t"
    "s_waitcnt lgkmcnt(4)\n\t"
    "v_mfma_f32_16x16x32_bf16 v[80:83], v[128:131], v[96:99], 0\n\t"
    "ds_read_b128 v[120:123], %[da] offset:4096\n\t"
    "v_mfma_f32_16x16x32_bf16 v[84:87], v[132:135], v[96:99], 0\n\t"
    "ds_read_b128 v[124:127], %[da] offset:6144\n\t"
    "v_mfma_f32_16x16x32_bf16 v[88:91], v[128:131], v[100:103], 0\n\t"
    "v_mfma_f32_16x16x32_bf16 v[92:95], v[132:135], v[100:103], 0\n\t"
    "s_barrier\n\t"
    "s_waitcnt lgkmcnt(4)\n\t"
    "v_mfma_f32_16x16x32_bf16 v[80:83], v[136:139], v[104:107], v[80:83]\n\t"
    "ds_read_b128 v[96:99], %[ea] offset:8192\n\t"
    "v_mfma_f32_16x16x32_bf16 v[84:87], v[140:143], v[104:107], v[84:87]\n\t"
    "ds_read_b128 v[100:103], %[ea] offset:10240\n\t"
    "v_mfma_f32_16x16x32_bf16 v[88:91], v[136:139], v[108:111], v[88:91]\n\t"
    "v_mfma_f32_16x16x32_bf16 v[92:95], v[140:143], v[108:111], v[92:95]\n\t"
    "s_waitcnt lgkmcnt(4)\n\t"
    "v_mfma_f32_16x16x32_bf16 v[80:83], v[144:147], v[112:115], v[80:83]\n\t"
    "ds_read_b128 v[104:107], %[da] offset:8192\n\t"
    "v_mfma_f32_16x16x32_bf16 v[84:87], v[148:151], v[112:115], v[84:87]\n\t"
    "ds_read_b128 v[108:111], %[da] offset:10240\n\t"
    "v_mfma_f32_16x16x32_bf16 v[88:91], v[144:147], v[116:119], v[88:91]\n\t"
    "v_mfma_f32_16x16x32_bf16 v[92:95], v[148:151], v[116:119], v[92:95]\n\t"
    "s_waitcnt lgkmcnt(4)\n\t"
    "v_mfma_f32_16x16x32_bf16 v[80:83], v[152:155], v[120:123], v[80:83]\n\t"
    "ds_read_b128 v[112:115], %[ea] offset:12288\n\t"
    "v_mfma_f32_16x16x32_bf16 v[84:87], v[156:159], v[120:123], v[84:87]\n\t"
    "ds_read_b128 v[116:119], %[ea] offset:14336\n\t"
    "v_mfma_f32_16x16x32_bf16 v[88:91], v[152:155], v[124:127], v[88:91]\n\t"
    "v_mfma_f32_16x16x32_bf16 v[92:95], v[156:159], v[124:127], v[92:95]\n\t"
    "s_cmp_lg_u32 %[pp], 0\n\t"
    "s_cbranch_scc1 7f\n\t"
    "8:\n\t"
    "s_waitcnt lgkmcnt(4)\n\t"
    "v_mfma_f32_16x16x32_bf16 v[80:83], v[160:163], v[96:99], v[80:83]\n\t"
    "ds_read_b128 v[120:123], %[da] offset:12288\n\t"
    "v_mfma_f32_16x16x32_bf16 v[84:87], v[164:167], v[96:99], v[84:87]\n\t"
    "ds_read_b128 v[124:127], %[da] offset:14336\n\t"
    "v_mfma_f32_16x16x32_bf16 v[88:91], v[160:163], v[100:103], v[88:91]\n\t"
    "v_mfma_f32_16x16x32_bf16 v[92:95], v[164:167], v[100:103], v[92:95]\n\t"
    "s_barrier\n\t"
    "s_waitcnt lgkmcnt(4)\n\t"
    "v_mfma_f32_16x16x32_bf16 v[80:83], v[168:171], v[104:107], v[80:83]\n\t"
    "ds_read_b128 v[96:99], %[ea] offset:16384\n\t"
    "v_mfma_f32_16x16x32_bf16 v[84:87], v[172:175], v[104:107], v[84:87]\n\t"
    "ds_read_b128 v[100:103], %[ea] offset:18432\n\t"
    "v_mfma_f32_16x16x32_bf16 v[88:91], v[168:171], v[108:111], v[88:91]\n\t"
    "v_mfma_f32_16x16x32_bf16 v[92:95], v[172:175], v[108:111], v[92:95]\n\t"
    "s_waitcnt lgkmcnt(4)\n\t"
    "v_mfma_f32_16x16x32_bf16 v[80:83], v[176:179], v[112:115], v[80:83]\n\t"
    "ds_read_b128 v[104:107], %[da] offset:16384\n\t"
    "v_mfma_f32_16x16x32_bf16 v[84:87], v[180:183], v[112:115], v[84:87]\n\t"
    "ds_read_b128 v[108:111], %[da] offset:18432\n\t"
    "v_mfma_f32_16x16x32_bf16 v[88:91], v[176:179], v[116:119], v[88:91]\n\t"
    "v_mfma_f32_16x16x32_bf16 v[92:95], v[180:183], v[116:119], v[92:95]\n\t"
    "s_waitcnt lgkmcnt(4)\n\t"
    "v_mfma_f32_16x16x32_bf16 v[80:83], v[184:187], v[120:123], v[80:83]\n\t"
    "ds_read_b128 v[112:115], %[ea] offset:20480\n\t"
    "v_mfma_f32_16x16x32_bf16 v[84:87], v[188:191], v[120:123], v[84:87]\n\t"
    "ds_read_b128 v[116:119], %[ea] offset:22528\n\t"
    "v_mfma_f32_16x16x32_bf16 v[88:91], v[184:187], v[124:127], v[88:91]\n\t"
    "v_mfma_f32_16x16x32_bf16 v[92:95], v[188:191], v[124:127], v[92:95]\n\t"
    "s_waitcnt lgkmcnt(4)\n\t"
    "v_mfma_f32_16x16x32_bf16 v[80:83], v[192:195], v[96:99], v[80:83]\n\t"
    "ds_read_b128 v[120:123], %[da] offset:20480\n\t"
    "v_mfma_f32_16x16x32_bf16 v[84:87], v[196:199], v[96:99], v[84:87]\n\t"
    "ds_read_b128 v[124:127], %[da] offset:22528\n\t"
    "v_mfma_f32_16x16x32_bf16 v[88:91], v[192:195], v[100:103], v[88:91]\n\t"
    "v_mfma_f32_16x16x32_bf16 v[92:95], v[196:199], v[100:103], v[92:95]\n\t"
    "s_barrier\n\t"
    "s_waitcnt lgkmcnt(4)\n\t"
    "v_mfma_f32_16x16x32_bf16 v[80:83], v[200:203], v[104:107], v[80:83]\n\t"
    "ds_read_b128 v[96:99], %[ea] offset:24576\n\t"
    "v_mfma_f32_16x16x32_bf16 v[84:87], v[204:207], v[104:107], v[84:87]\n\t"
    "ds_read_b128 v[100:103], %[ea] offset:26624\n\t"
    "v_mfma_f32_16x16x32_bf16 v[88:91], v[200:203], v[108:111], v[88:91]\n\t"
    "v_mfma_f32_16x16x32_bf16 v[92:95], v[204:207], v[108:111], v[92:95]\n\t"
    "s_waitcnt lgkmcnt(4)\n\t"
    "v_mfma_f32_16x16x32_bf16 v[80:83], v[208:211], v[112:115], v[80:83]\n\t"
    "ds_read_b128 v[104:107], %[da] offset:24576\n\t"
    "v_mfma_f32_16x16x32_bf16 v[84:87], v[212:215], v[112:115], v[84:87]\n\t"
    "ds_read_b128 v[108:111], %[da] offset:26624\n\t"
    "v_mfma_f32_16x16x32_bf16 v[88:91], v[208:211], v[116:119], v[88:91]\n\t"
    "v_mfma_f32_16x16x32_bf16 v[92:95], v[212:215], v[116:119], v[92:95]\n\t"
    "s_waitcnt lgkmcnt(4)\n\t"
    "v_mfma_f32_16x16x32_bf16 v[80:83], v[216:219], v[120:123], v[80:83]\n\t"
    "ds_read_b128 v[112:115], %[ea] offset:28672\n\t"
    "v_mfma_f32_16x16x32_bf16 v[84:87], v[220:223], v[120:123], v[84:87]\n\t"
    "ds_read_b128 v[116:119], %[ea] offset:30720\n\t"
    "v_mfma_f32_16x16x32_bf16 v[88:91], v[216:219], v[124:127], v[88:91]\n\t"
    "v_mfma_f32_16x16x32_bf16 v[92:95], v[220:223], v[124:127], v[92:95]\n\t"
    "s_waitcnt lgkmcnt(4)\n\t"
    "v_mfma_f32_16x16x32_bf16 v[80:83], v[224:227], v[96:99], v[80:83]\n\t"
    "ds_read_b128 v[120:123], %[da] offset:28672\n\t"
    "v_mfma_f32_16x16x32_bf16 v[84:87], v[228:231], v[96:99], v[84:87]\n\t"
    "ds_read_b128 v[124:127], %[da] offset:30720\n\t"
    "v_mfma_f32_16x16x32_bf16 v[88:91], v[224:227], v[100:103], v[88:91]\n\t"
    "v_mfma_f32_16x16x32_bf16 v[92:95], v[228:231], v[100:103], v[92:95]\n\t"
    "s_waitcnt lgkmcnt(4)\n\t"
    "v_mfma_f32_16x16x32_bf16 v[80:83], v[232:235], v[104:107], v[80:83]\n\t"
    "v_mfma_f32_16x16x32_bf16 v[84:87], v[236:239], v[104:107], v[84:87]\n\t"
    "v_mfma_f32_16x16x32_bf16 v[88:91], v[232:235], v[108:111], v[88:91]\n\t"
    "v_xor_b32 %[ea], 0x10000, %[ea]\n\t"
    "v_mfma_f32_16x16x32_bf16 v[92:95], v[236:239], v[108:111], v[92:95]\n\t"
    "v_xor_b32 %[da], 0x10000, %[da]\n\t"
    "s_waitcnt lgkmcnt(2)\n\t"
    "v_mfma_f32_16x16x32_bf16 v[80:83], v[240:243], v[112:115], v[80:83]\n\t"
    "v_mfma_f32_16x16x32_bf16 v[84:87], v[244:247], v[112:115], v[84:87]\n\t"
    "v_mfma_f32_16x16x32_bf16 v[88:91], v[240:243], v[116:119], v[88:91]\n\t"
    "s_add_u32 %[b], %[b], 1\n\t"
    "v_mfma_f32_16x16x32_bf16 v[92:95], v[244:247], v[116:119], v[92:95]\n\t"
    "s_waitcnt lgkmcnt(0)\n\t"
    "v_mfma_f32_16x16x32_bf16 v[80:83], v[248:251], v[120:123], v[80:83]\n\t"
    "v_mfma_f32_16x16x32_bf16 v[84:87], v[252:255], v[120:123], v[84:87]\n\t"
    "v_mfma_f32_16x16x32_bf16 v[88:91], v[248:251], v[124:127], v[88:91]\n\t"
    "v_mfma_f32_16x16x32_bf16 v[92:95], v[252:255], v[124:127], v[92:95]\n\t"
    "2:\n\t"
    "ds_read_b32 v39, %[fa]\n\t"
    "ds_read_b128 v[48:51], %[rw] offset:0\n\t"
    "ds_read_b128 v[52:55], %[rw] offset:1024\n\t"
    "ds_read_b128 v[56:59], %[rw] offset:2048\n\t"
    "ds_read_b128 v[60:63], %[rw] offset:3072\n\t"
    "s_waitcnt lgkmcnt(2)\n\t"
    "v_readfirstlane_b32 %[pp], v39\n\t"
    "s_and_b32 %[st], %[pp], 0xff\n\t"
    "s_cmp_eq_u32 %[st], %[b]\n\t"
    "s_cbranch_scc1 3f\n\t"
    "s_waitcnt lgkmcnt(0)\n\t"
    "s_branch 2b\n\t"
    "3:\n\t"
    "v_add_f32 v48, v48, v80\n\t"
    "v_add_f32 v49, v49, v81\n\t"
    "v_add_f32 v50, v50, v82\n\t"
    "v_add_f32 v51, v51, v83\n\t"
    "v_add_f32 v52, v52, v84\n\t"
    "v_add_f32 v53, v53, v85\n\t"
    "v_add_f32 v54, v54, v86\n\t"
    "v_add_f32 v55, v55, v87\n\t"
    "s_waitcnt lgkmcnt(0)\n\t"
    "v_add_f32 v56, v56, v88\n\t"
    "v_add_f32 v57, v57, v89\n\t"
    "v_add_f32 v58, v58, v90\n\t"
    "v_add_f32 v59, v59, v91\n\t"
    "v_add_f32 v60, v60, v92\n\t"
    "v_add_f32 v61, v61, v93\n\t"
    "v_add_f32 v62, v62, v94\n\t"
    "v_add_f32 v63, v63, v95\n\t"
    "v_mul_f32 v40, %[nl], v48\n\t"
    "v_mul_f32 v41, %[nl], v49\n\t"
    "v_mul_f32 v42, %[nl], v50\n\t"
    "v_mul_f32 v43, %[nl], v51\n\t"
    "v_mul_f32 v44, %[nl], v56\n\t"
    "v_mul_f32 v45, %[nl], v57\n\t"
    "v_mul_f32 v46, %[nl], v58\n\t"
    "v_mul_f32 v47, %[nl], v59\n\t"
    "v_exp_f32 v40, v40\n\t"
    "v_exp_f32 v41, v41\n\t"
    "v_exp_f32 v42, v42\n\t"
    "v_exp_f32 v43, v43\n\t"
    "v_exp_f32 v44, v44\n\t"
    "v_exp_f32 v45, v45\n\t"
    "v_exp_f32 v46, v46\n\t"
    "v_exp_f32 v47, v47\n\t"
    "v_add_f32 v40, 1.0, v40\n\t"
    "v_add_f32 v41, 1.0, v41\n\t"
    "v_add_f32 v42, 1.0, v42\n\t"
    "v_add_f32 v43, 1.0, v43\n\t"
    "v_add_f32 v44, 1.0, v44\n\t"
    "v_add_f32 v45, 1.0, v45\n\t"
    "v_add_f32 v46, 1.0, v46\n\t"
    "v_add_f32 v47, 1.0, v47\n\t"
    "v_rcp_f32 v40, v40\n\t"
    "v_rcp_f32 v41, v41\n\t"
    "v_rcp_f32 v42, v42\n\t"
    "v_rcp_f32 v43, v43\n\t"
    "v_rcp_f32 v44, v44\n\t"
    "v_rcp_f32 v45, v45\n\t"
    "v_rcp_f32 v46, v46\n\t"
    "v_rcp_f32 v47, v47\n\t"
    "v_mul_f32 v40, v48, v40\n\t"
    "v_mul_f32 v41, v49, v41\n\t"
    "v_mul_f32 v42, v50, v42\n\t"
    "v_mul_f32 v43, v51, v43\n\t"
    "v_mul_f32 v44, v56, v44\n\t"
    "v_mul_f32 v45, v57, v45\n\t"
    "v_mul_f32 v46, v58, v46\n\t"
    "v_mul_f32 v47, v59, v47\n\t"
    "v_mul_f32 v40, v40, v52\n\t"
    "v_mul_f32 v41, v41, v53\n\t"
    "v_mul_f32 v42, v42, v54\n\t"
    "v_mul_f32 v43, v43, v55\n\t"
    "v_mul_f32 v44, v44, v60\n\t"
    "v_mul_f32 v45, v45, v61\n\t"
    "v_mul_f32 v46, v46, v62\n\t"
    "v_mul_f32 v47, v47, v63\n\t"
    "v_cvt_pk_bf16_f32 v48, v40, v41\n\t"
    "v_cvt_pk_bf16_f32 v49, v42, v43\n\t"
    "v_cvt_pk_bf16_f32 v50, v44, v45\n\t"
    "v_cvt_pk_bf16_f32 v51, v46, v47\n\t"
    "s_nop 1\n\t"
    "v_permlane16_swap_b32 v48, v50\n\t"
    "v_permlane16_swap_b32 v49, v51\n\t"
    "global_store_dwordx4 %[co0], v[48:51], s[36:37] sc1\n\t"
    "s_add_u32 s36, s36, %[ab]\n\t"
    "s_addc_u32 s37, s37, 0\n\t"
    "s_lshr_b32 %[pp], %[pp], 8\n\t"
    "s_cbranch_scc1 1b\n\t"
    "v_mov_b32 v72, %[cn]\n\t"
    "v_mov_b32 v73, 1\n\t"
    "s_add_u32 %[st], %[b], %[po]\n\t"
    "v_mov_b32 v74, %[st]\n\t"
    "v_mov_b32 v75, 0\n\t"
    "s_waitcnt vmcnt(0)\n\t"
    "s_mov_b64 exec, 1\n\t"
    "ds_add_rtn_u32 v73, v72, v73\n\t"
    "s_waitcnt lgkmcnt(0)\n\t"
    "v_readfirstlane_b32 %[st], v73\n\t"
    "s_and_b32 %[st], %[st], 3\n\t"
    "s_cmp_lg_u32 %[st], 3\n\t"
    "s_cbranch_scc1 61f\n\t"
    "global_store_dword v75, v74, %[pg] sc1\n\t"
    "61:\n\t"
    "s_mov_b64 exec, -1\n\t"
    "s_mov_b32 %[pp], 0\n\t"
    "s_cmp_lt_i32 %[b], %[ns]\n\t"
    "s_cbranch_scc1 1b\n\t"
    "s_branch 9f\n\t"
    "7:\n\t"
    "v_mov_b32 v72, %[cn]\n\t"
    "v_mov_b32 v73, 1\n\t"
    "s_add_u32 %[st], %[b], %[po]\n\t"
    "v_mov_b32 v74, %[st]\n\t"
    "v_mov_b32 v75, 0\n\t"
    "s_waitcnt vmcnt(0)\n\t"
    "s_mov_b64 exec, 1\n\t"
    "ds_add_rtn_u32 v73, v72, v73\n\t"
    "s_waitcnt lgkmcnt(0)\n\t"
    "v_readfirstlane_b32 %[st], v73\n\t"
    "s_and_b32 %[st], %[st], 3\n\t"
    "s_cmp_lg_u32 %[st], 3\n\t"
    "s_cbranch_scc1 61f\n\t"
    "global_store_dword v75, v74, %[pg] sc1\n\t"
    "61:\n\t"
    "s_mov_b64 exec, -1\n\t"
    "s_mov_b32 %[pp], 0\n\t"
    "s_branch 8b\n\t"
    "9:"
    : [ea] "+&v"(ea), [da] "+&v"(da), [b] "+&s"(b), [st] "=&s"(st), "+{s[36:37]}"(p0), [pp] "+&s"(pp)
    : [ns] "s"(ns), [ab] "s"(ab), [rw] "v"(rw), [fa] "v"(fa), "{v[128:159]}"(W[0]), "{v[160:191]}"(W[1]), "{v[192:223]}"(W[2]), "{v[224:255]}"(W[3]), [po] "s"(po), [cn] "s"(cn), [pg] "s"(pg), [co0] "v"(co0), [nl] "s"(-LOG2E)
    : "v39", "v40", "v41", "v42", "v43", "v44", "v45", "v46", "v47", "v48", "v49", "v50", "v51", "v52", "v53", "v54", "v55", "v56", "v57", "v58", "v59", "v60", "v61", "v62", "v63", "v72", "v73", "v74", "v75", "v80", "v81", "v82", "v83", "v84", "v85", "v86", "v87", "v88", "v89", "v90", "v91", "v92", "v93", "v94", "v95", "v96", "v97", "v98", "v99", "v100", "v101", "v102", "v103", "v104", "v105", "v106", "v107", "v108", "v109", "v110", "v111", "v112", "v113", "v114", "v115", "v116", "v117", "v118", "v119", "v120", "v121", "v122", "v123", "v124", "v125", "v126", "v127", "vcc", "scc", "m0", "memory");
}
__device__ __forceinline__ void paced_st2(const u32x32 (&W)[4], unsigned ea, unsigned da, int b, int ns, unsigned ab, unsigned rw, unsigned fa,
    const char* p0, unsigned pp, int po, unsigned cn, const char* pg, unsigned co0, unsigned co1) {
  unsigned st;
  asm volatile("1:\n\t"
    "s_barrier\n\t"
    "ds_read_b128 v[96:99], %[ea] offset:0\n\t"
    "ds_read_b128 v[100:103], %[ea] offset:2048\n\t"
    "ds_read_b128 v[104:107], %[da] offset:0\n\t"
    "ds_read_b128 v[108:111], %[da] offset:2048\n\t"
    "ds_read_b128 v[112:115], %[ea] offset:4096\n\t"
    "ds_read_b128 v[116:119], %[ea] offset:6144\n\t"
    "s_waitcnt lgkmcnt(4)\n\t"
    "v_mfma_f32_16x16x32_bf16 v[80:83], v[128:131], v[96:99], 0\n\t"
    "ds_read_b128 v[120:123], %[da] offset:4096\n\t"
    "v_mfma_f32_16x16x32_bf16 v[84:87], v[132:135], v[96:99], 0\n\t"
    "ds_read_b128 v[124:127], %[da] offset:6144\n\t"
    "v_mfma_f32_16x16x32_bf16 v[88:91], v[128:131], v[100:103], 0\n\t"
    "v_mfma_f32_16x16x32_bf16 v[92:95], v[132:135], v[100:103], 0\n\t"
    "s_barrier\n\t"
    "s_waitcnt lgkmcnt(4)\n\t"
    "v_mfma_f32_16x16x32_bf16 v[80:83], v[136:139], v[104:107], v[80:83]\n\t"
    "ds_read_b128 v[96:99], %[ea] offset:8192\n\t"
    "v_mfma_f32_16x16x32_bf16 v[84:87], v[140:143], v[104:107], v[84:87]\n\t"
    "ds_read_b128 v[100:103], %[ea] offset:10240\n\t"
    "v_mfma_f32_16x16x32_bf16 v[88:91], v[136:139], v[108:111], v[88:91]\n\t"
    "v_mfma_f32_16x16x32_bf16 v[92:95], v[140:143], v[108:111], v[92:95]\n\t"
    "s_waitcnt lgkmcnt(4)\n\t"
    "v_mfma_f32_16x16x32_bf16 v[80:83], v[144:147], v[112:115], v[80:83]\n\t"
    "ds_read_b128 v[104:107], %[da] offset:8192\n\t"
    "v_mfma_f32_16x16x32_bf16 v[84:87], v[148:151], v[112:115], v[84:87]\n\t"
    "ds_read_b128 v[108:111], %[da] offset:10240\n\t"
    "v_mfma_f32_16x16x32_bf16 v[88:91], v[144:147], v[116:119], v[88:91]\n\t"
    "v_mfma_f32_16x16x32_bf16 v[92:95], v[148:151], v[116:119], v[92:95]\n\t"
    "s_waitcnt lgkmcnt(4)\n\t"
    "v_mfma_f32_16x16x32_bf16 v[80:83], v[152:155], v[120:123], v[80:83]\n\t"
    "ds_read_b128 v[112:115], %[ea] offset:12288\n\t"
    "v_mfma_f32_16x16x32_bf16 v[84:87], v[156:159], v[120:123], v[84:87]\n\t"
    "ds_read_b128 v[116:119], %[ea] offset:14336\n\t"
    "v_mfma_f32_16x16x32_bf16 v[88:91], v[152:155], v[124:127], v[88:91]\n\t"
    "v_mfma_f32_16x16x32_bf16 v[92:95], v[156:159], v[124:127], v[92:95]\n\t"
    "s_cmp_lg_u32 %[pp], 0\n\t"
    "s_cbranch_scc1 7f\n\t"
    "8:\n\t"
    "s_waitcnt lgkmcnt(4)\n\t"
    "v_mfma_f32_16x16x32_bf16 v[80:83], v[160:163], v[96:99], v[80:83]\n\t"
    "ds_read_b128 v[120:123], %[da] offset:12288\n\t"
    "v_mfma_f32_16x16x32_bf16 v[84:87], v[164:167], v[96:99], v[84:87]\n\t"
    "ds_read_b128 v[124:127], %[da] offset:14336\n\t"
    "v_mfma_f32_16x16x32_bf16 v[88:91], v[160:163], v[100:103], v[88:91]\n\t"
    "v_mfma_f32_16x16x32_bf16 v[92:95], v[164:167], v[100:103], v[92:95]\n\t"
    "s_barrier\n\t"
    "s_waitcnt lgkmcnt(4)\n\t"
    "v_mfma_f32_16x16x32_bf16 v[80:83], v[168:171], v[104:107], v[80:83]\n\t"
    "ds_read_b128 v[96:99], %[ea] offset:16384\n\t"
    "v_mfma_f32_16x16x32_bf16 v[84:87], v[172:175], v[104:107], v[84:87]\n\t"
    "ds_read_b128 v[100:103], %[ea] offset:18432\n\t"
    "v_mfma_f32_16x16x32_bf16 v[88:91], v[168:171], v[108:111], v[88:91]\n\t"
    "v_mfma_f32_16x16x32_bf16 v[92:95], v[172:175], v[108:111], v[92:95]\n\t"
    "s_waitcnt lgkmcnt(4)\n\t"
    "v_mfma_f32_16x16x32_bf16 v[80:83], v[176:179], v[112:115], v[80:83]\n\t"
    "ds_read_b128 v[104:107], %[da] offset:16384\n\t"
    "v_mfma_f32_16x16x32_bf16 v[84:87], v[180:183], v[112:115], v[84:87]\n\t"
    "ds_read_b128 v[108:111], %[da] offset:18432\n\t"
    "v_mfma_f32_16x16x32_bf16 v[88:91], v[176:179], v[116:119], v[88:91]\n\t"
    "v_mfma_f32_16x16x32_bf16 v[92:95], v[180:183], v[116:119], v[92:95]\n\t"
    "s_waitcnt lgkmcnt(4)\n\t"
    "v_mfma_f32_16x16x32_bf16 v[80:83], v[184:187], v[120:123], v[80:83]\n\t"
    "ds_read_b128 v[112:115], %[ea] offset:20480\n\t"
    "v_mfma_f32_16x16x32_bf16 v[84:87], v[188:191], v[120:123], v[84:87]\n\t"
    "ds_read_b128 v[116:119], %[ea] offset:22528\n\t"
    "v_mfma_f32_16x16x32_bf16 v[88:91], v[184:187], v[124:127], v[88:91]\n\t"
    "v_mfma_f32_16x16x32_bf16 v[92:95], v[188:191], v[124:127], v[92:95]\n\t"
    "s_waitcnt lgkmcnt(4)\n\t"
    "v_mfma_f32_16x16x32_bf16 v[80:83], v[192:195], v[96:99], v[80:83]\n\t"
    "ds_read_b128 v[120:123], %[da] offset:20480\n\t"
    "v_mfma_f32_16x16x32_bf16 v[84:87], v[196:199], v[96:99], v[84:87]\n\t"
    "ds_read_b128 v[124:127], %[da] offset:22528\n\t"
    "v_mfma_f32_16x16x32_bf16 v[88:91], v[192:195], v[100:103], v[88:91]\n\t"
    "v_mfma_f32_16x16x32_bf16 v[92:95], v[196:199], v[100:103], v[92:95]\n\t"
    "s_barrier\n\t"
    "s_waitcnt lgkmcnt(4)\n\t"
    "v_mfma_f32_16x16x32_bf16 v[80:83], v[200:203], v[104:107], v[80:83]\n\t"
    "ds_read_b128 v[96:99], %[ea] offset:24576\n\t"
    "v_mfma_f32_16x16x32_bf16 v[84:87], v[204:207], v[104:107], v[84:87]\n\t"
    "ds_read_b128 v[100:103], %[ea] offset:26624\n\t"
    "v_mfma_f32_16x16x32_bf16 v[88:91], v[200:203], v[108:111], v[88:91]\n\t"
    "v_mfma_f32_16x16x32_bf16 v[92:95], v[204:207], v[108:111], v[92:95]\n\t"
    "s_waitcnt lgkmcnt(4)\n\t"
    "v_mfma_f32_16x16x32_bf16 v[80:83], v[208:211], v[112:115], v[80:83]\n\t"
    "ds_read_b128 v[104:107], %[da] offset:24576\n\t"
    "v_mfma_f32_16x16x32_bf16 v[84:87], v[212:215], v[112:115], v[84:87]\n\t"
    "ds_read_b128 v[108:111], %[da] offset:26624\n\t"
    "v_mfma_f32_16x16x32_bf16 v[88:91], v[208:211], v[116:119], v[88:91]\n\t"
    "v_mfma_f32_16x16x32_bf16 v[92:95], v[212:215], v[116:119], v[92:95]\n\t"
    "s_waitcnt lgkmcnt(4)\n\t"
    "v_mfma_f32_16x16x32_bf16 v[80:83], v[216:219], v[120:123], v[80:83]\n\t"
    "ds_read_b128 v[112:115], %[ea] offset:28672\n\t"
    "v_mfma_f32_16x16x32_bf16 v[84:87], v[220:223], v[120:123], v[84:87]\n\t"
    "ds_read_b128 v[116:119], %[ea] offset:30720\n\t"
    "v_mfma_f32_16x16x32_bf16 v[88:91], v[216:219], v[124:127], v[88:91]\n\t"
    "v_mfma_f32_16x16x32_bf16 v[92:95], v[220:223], v[124:127], v[92:95]\n\t"
    "s_waitcnt lgkmcnt(4)\n\t"
    "v_mfma_f32_16x16x32_bf16 v[80:83], v[224:227], v[96:99], v[80:83]\n\t"
    "ds_read_b128 v[120:123], %[da] offset:28672\n\t"
    "v_mfma_f32_16x16x32_bf16 v[84:87], v[228:231], v[96:99], v[84:87]\n\t"
    "ds_read_b128 v[124:127], %[da] offset:30720\n\t"
    "v_mfma_f32_16x16x32_bf16 v[88:91], v[224:227], v[100:103], v[88:91]\n\t"
    "v_mfma_f32_16x16x32_bf16 v[92:95], v[228:231], v[100:103], v[92:95]\n\t"
    "s_waitcnt lgkmcnt(4)\n\t"
    "v_mfma_f32_16x16x32_bf16 v[80:83], v[232:235], v[104:107], v[80:83]\n\t"
    "v_mfma_f32_16x16x32_bf16 v[84:87], v[236:239], v[104:107], v[84:87]\n\t"
    "v_mfma_f32_16x16x32_bf16 v[88:91], v[232:235], v[108:111], v[88:91]\n\t"
    "v_xor_b32 %[ea], 0x10000, %[ea]\n\t"
    "v_mfma_f32_16x16x32_bf16 v[92:95], v[236:239], v[108:111], v[92:95]\n\t"
    "v_xor_b32 %[da], 0x10000, %[da]\n\t"
    "s_waitcnt lgkmcnt(2)\n\t"
    "v_mfma_f32_16x16x32_bf16 v[80:83], v[240:243], v[112:115], v[80:83]\n\t"
    "v_mfma_f32_16x16x32_bf16 v[84:87], v[244:247], v[112:115], v[84:87]\n\t"
    "v_mfma_f32_16x16x32_bf16 v[88:91], v[240:243], v[116:119], v[88:91]\n\t"
    "s_add_u32 %[b], %[b], 1\n\t"
    "v_mfma_f32_16x16x32_bf16 v[92:95], v[244:247], v[116:119], v[92:95]\n\t"
    "s_waitcnt lgkmcnt(0)\n\t"
    "v_mfma_f32_16x16x32_bf16 v[80:83], v[248:251], v[120:123], v[80:83]\n\t"
    "v_mfma_f32_16x16x32_bf16 v[84:87], v[252:255], v[120:123], v[84:87]\n\t"
    "v_mfma_f32_16x16x32_bf16 v[88:91], v[248:251], v[124:127], v[88:91]\n\t"
    "v_mfma_f32_16x16x32_bf16 v[92:95], v[252:255], v[124:127], v[92:95]\n\t"
    "s_nop 6\n\t"
    "v_cvt_pk_bf16_f32 v40, v80, v81\n\t"
    "v_cvt_pk_bf16_f32 v41, v82, v83\n\t"
    "v_cvt_pk_bf16_f32 v42, v84, v85\n\t"
    "v_cvt_pk_bf16_f32 v43, v86, v87\n\t"
    "global_store_dwordx4 %[co0], v[40:43], s[36:37] sc1\n\t"
    "v_cvt_pk_bf16_f32 v44, v88, v89\n\t"
    "v_cvt_pk_bf16_f32 v45, v90, v91\n\t"
    "v_cvt_pk_bf16_f32 v46, v92, v93\n\t"
    "v_cvt_pk_bf16_f32 v47, v94, v95\n\t"
    "global_store_dwordx4 %[co1], v[44:47], s[36:37] sc1\n\t"
    "2:\n\t"
    "ds_read_b32 v39, %[fa]\n\t"
    "ds_read_b128 v[48:51], %[rw] offset:0\n\t"
    "ds_read_b128 v[52:55], %[rw] offset:1024\n\t"
    "ds_read_b128 v[56:59], %[rw] offset:2048\n\t"
    "ds_read_b128 v[60:63], %[rw] offset:3072\n\t"
    "s_waitcnt lgkmcnt(2)\n\t"
    "v_readfirstlane_b32 %[pp], v39\n\t"
    "s_and_b32 %[st], %[pp], 0xff\n\t"
    "s_cmp_eq_u32 %[st], %[b]\n\t"
    "s_cbranch_scc1 3f\n\t"
    "s_waitcnt lgkmcnt(0)\n\t"
    "s_branch 2b\n\t"
    "3:\n\t"
    "v_cvt_pk_bf16_f32 v48, v48, v49\n\t"
    "v_cvt_pk_bf16_f32 v49, v50, v51\n\t"
    "v_cvt_pk_bf16_f32 v50, v52, v53\n\t"
    "v_cvt_pk_bf16_f32 v51, v54, v55\n\t"
    "global_store_dwordx4 %[co0], v[48:51], s[36:37] offset:2048 sc1\n\t"
    "s_waitcnt lgkmcnt(0)\n\t"
    "v_cvt_pk_bf16_f32 v56, v56, v57\n\t"
    "v_cvt_pk_bf16_f32 v57, v58, v59\n\t"
    "v_cvt_pk_bf16_f32 v58, v60, v61\n\t"
    "v_cvt_pk_bf16_f32 v59, v62, v63\n\t"
    "global_store_dwordx4 %[co1], v[56:59], s[36:37] offset:2048 sc1\n\t"
    "s_add_u32 s36, s36, %[ab]\n\t"
    "s_addc_u32 s37, s37, 0\n\t"
    "s_lshr_b32 %[pp], %[pp], 8\n\t"
    "s_cbranch_scc1 1b\n\t"
    "v_mov_b32 v72, %[cn]\n\t"
    "v_mov_b32 v73, 1\n\t"
    "s_add_u32 %[st], %[b], %[po]\n\t"
    "v_mov_b32 v74, %[st]\n\t"
    "v_mov_b32 v75, 0\n\t"
    "s_waitcnt vmcnt(0)\n\t"
    "s_mov_b64 exec, 1\n\t"
    "ds_add_rtn_u32 v73, v72, v73\n\t"
    "s_waitcnt lgkmcnt(0)\n\t"
    "v_readfirstlane_b32 %[st], v73\n\t"
    "s_and_b32 %[st], %[st], 3\n\t"
    "s_cmp_lg_u32 %[st], 3\n\t"
    "s_cbranch_scc1 61f\n\t"
    "global_store_dword v75, v74, %[pg] sc1\n\t"
    "61:\n\t"
    "s_mov_b64 exec, -1\n\t"
    "s_mov_b32 %[pp], 0\n\t"
    "s_cmp_lt_i32 %[b], %[ns]\n\t"
    "s_cbranch_scc1 1b\n\t"
    "s_branch 9f\n\t"
    "7:\n\t"
    "v_mov_b32 v72, %[cn]\n\t"
    "v_mov_b32 v73, 1\n\t"
    "s_add_u32 %[st], %[b], %[po]\n\t"
    "v_mov_b32 v74, %[st]\n\t"
    "v_mov_b32 v75, 0\n\t"
    "s_waitcnt vmcnt(0)\n\t"
    "s_mov_b64 exec, 1\n\t"
    "ds_add_rtn_u32 v73, v72, v73\n\t"
    "s_waitcnt lgkmcnt(0)\n\t"
    "v_readfirstlane_b32 %[st], v73\n\t"
    "s_and_b32 %[st], %[st], 3\n\t"
    "s_cmp_lg_u32 %[st], 3\n\t"
    "s_cbranch_scc1 61f\n\t"
    "global_store_dword v75, v74, %[pg] sc1\n\t"
    "61:\n\t"
    "s_mov_b64 exec, -1\n\t"
    "s_mov_b32 %[pp], 0\n\t"
    "s_branch 8b\n\t"
    "9:"
    : [ea] "+&v"(ea), [da] "+&v"(da), [b] "+&s"(b), [st] "=&s"(st), "+{s[36:37]}"(p0), [pp] "+&s"(pp)
    : [ns] "s"(ns), [ab] "s"(ab), [rw] "v"(rw), [fa] "v"(fa), "{v[128:159]}"(W[0]), "{v[160:191]}"(W[1]), "{v[192:223]}"(W[2]), "{v[224:255]}"(W[3]), [po] "s"(po), [cn] "s"(cn), [pg] "s"(pg), [co0] "v"(co0), [co1] "v"(co1)
    : "v39", "v40", "v41", "v42", "v43", "v44", "v45", "v46", "v47", "v48", "v49", "v50", "v51", "v52", "v53", "v54", "v55", "v56", "v57", "v58", "v59", "v60", "v61", "v62", "v63", "v72", "v73", "v74", "v75", "v80", "v81", "v82", "v83", "v84", "v85", "v86", "v87", "v88", "v89", "v90", "v91", "v92", "v93", "v94", "v95", "v96", "v97", "v98", "v99", "v100", "v101", "v102", "v103", "v104", "v105", "v106", "v107", "v108", "v109", "v110", "v111", "v112", "v113", "v114", "v115", "v116", "v117", "v118", "v119", "v120", "v121", "v122", "v123", "v124", "v125", "v126", "v127", "vcc", "scc", "m0", "memory");
}
__device__ __forceinline__ void fin_ld(const u32x16& pd, unsigned rw, unsigned fa, unsigned fv) {
  asm volatile("ds_write_b128 %[rw], v[80:83] offset:0\n\t"
    "ds_write_b128 %[rw], v[84:87] offset:1024\n\t"
    "ds_write_b128 %[rw], v[88:91] offset:2048\n\t"
    "ds_write_b128 %[rw], v[92:95] offset:3072\n\t"
    "ds_write_b32 %[fa], %[fv]"
    :
    : [rw] "v"(rw), "{v[80:95]}"(pd), [fa] "v"(fa), [fv] "v"(fv)
    : "memory");
}
__device__ __forceinline__ void fin_st0(const u32x16& pd, unsigned rw, unsigned fa, unsigned fv, const char* cb, unsigned co0, unsigned co1) {
  asm volatile("1:\n\t"
    "ds_read_b32 v39, %[fa]\n\t"
    "ds_read_b128 v[48:51], %[rw] offset:0\n\t"
    "ds_read_b128 v[52:55], %[rw] offset:1024\n\t"
    "ds_read_b128 v[56:59], %[rw] offset:2048\n\t"
    "ds_read_b128 v[60:63], %[rw] offset:3072\n\t"
    "s_waitcnt lgkmcnt(2)\n\t"
    "v_cmp_ne_u32 vcc, v39, %[fv]\n\t"
    "s_cbranch_vccz 2f\n\t"
    "s_waitcnt lgkmcnt(0)\n\t"
    "s_branch 1b\n\t"
    "2:\n\t"
    "v_add_f32 v48, v48, v80\n\t"
    "v_add_f32 v49, v49, v81\n\t"
    "v_add_f32 v50, v50, v82\n\t"
    "v_add_f32 v51, v51, v83\n\t"
    "v_add_f32 v52, v52, v84\n\t"
    "v_add_f32 v53, v53, v85\n\t"
    "v_add_f32 v54, v54, v86\n\t"
    "v_add_f32 v55, v55, v87\n\t"
    "v_cvt_pk_bf16_f32 v40, v48, v49\n\t"
    "v_cvt_pk_bf16_f32 v41, v50, v51\n\t"
    "v_cvt_pk_bf16_f32 v42, v52, v53\n\t"
    "v_cvt_pk_bf16_f32 v43, v54, v55\n\t"
    "global_store_dwordx4 %[co0], v[40:43], %[cb] sc1\n\t"
    "s_waitcnt lgkmcnt(0)\n\t"
    "v_add_f32 v56, v56, v88\n\t"
    "v_add_f32 v57, v57, v89\n\t"
    "v_add_f32 v58, v58, v90\n\t"
    "v_add_f32 v59, v59, v91\n\t"
    "v_add_f32 v60, v60, v92\n\t"
    "v_add_f32 v61, v61, v93\n\t"
    "v_add_f32 v62, v62, v94\n\t"
    "v_add_f32 v63, v63, v95\n\t"
    "v_cvt_pk_bf16_f32 v44, v56, v57\n\t"
    "v_cvt_pk_bf16_f32 v45, v58, v59\n\t"
    "v_cvt_pk_bf16_f32 v46, v60, v61\n\t"
    "v_cvt_pk_bf16_f32 v47, v62, v63\n\t"
    "global_store_dwordx4 %[co1], v[44:47], %[cb] sc1\n\t"
    "s_nop 1"
    :
    : [rw] "v"(rw), "{v[80:95]}"(pd), [fa] "v"(fa), [fv] "v"(fv), [cb] "s"(cb), [co0] "v"(co0), [co1] "v"(co1)
    : "v39", "v40", "v41", "v42", "v43", "v44", "v45", "v46", "v47", "v48", "v49", "v50", "v51", "v52", "v53", "v54", "v55", "v56", "v57", "v58", "v59", "v60", "v61", "v62", "v63", "vcc", "memory");
}
__device__ __forceinline__ void fin_st1(const u32x16& pd, unsigned rw, unsigned fa, unsigned fv, const char* cb, unsigned co0, unsigned co1) {
  asm volatile("1:\n\t"
    "ds_read_b32 v39, %[fa]\n\t"
    "ds_read_b128 v[48:51], %[rw] offset:0\n\t"
    "ds_read_b128 v[52:55], %[rw] offset:1024\n\t"
    "ds_read_b128 v[56:59], %[rw] offset:2048\n\t"
    "ds_read_b128 v[60:63], %[rw] offset:3072\n\t"
    "s_waitcnt lgkmcnt(2)\n\t"
    "v_cmp_ne_u32 vcc, v39, %[fv]\n\t"
    "s_cbranch_vccz 2f\n\t"
    "s_waitcnt lgkmcnt(0)\n\t"
    "s_branch 1b\n\t"
    "2:\n\t"
    "v_add_f32 v48, v48, v80\n\t"
    "v_add_f32 v49, v49, v81\n\t"
    "v_add_f32 v50, v50, v82\n\t"
    "v_add_f32 v51, v51, v83\n\t"
    "v_add_f32 v52, v52, v84\n\t"
    "v_add_f32 v53, v53, v85\n\t"
    "v_add_f32 v54, v54, v86\n\t"
    "v_add_f32 v55, v55, v87\n\t"
    "s_waitcnt lgkmcnt(0)\n\t"
    "v_add_f32 v56, v56, v88\n\t"
    "v_add_f32 v57, v57, v89\n\t"
    "v_add_f32 v58, v58, v90\n\t"
    "v_add_f32 v59, v59, v91\n\t"
    "v_add_f32 v60, v60, v92\n\t"
    "v_add_f32 v61, v61, v93\n\t"
    "v_add_f32 v62, v62, v94\n\t"
    "v_add_f32 v63, v63, v95\n\t"
    "v_mul_f32 v40, %[nl], v48\n\t"
    "v_mul_f32 v41, %[nl], v49\n\t"
    "v_mul_f32 v42, %[nl], v50\n\t"
    "v_mul_f32 v43, %[nl], v51\n\t"
    "v_mul_f32 v44, %[nl], v56\n\t"
    "v_mul_f32 v45, %[nl], v57\n\t"
    "v_mul_f32 v46, %[nl], v58\n\t"
    "v_mul_f32 v47, %[nl], v59\n\t"
    "v_exp_f32 v40, v40\n\t"
    "v_exp_f32 v41, v41\n\t"
    "v_exp_f32 v42, v42\n\t"
    "v_exp_f32 v43, v43\n\t"
    "v_exp_f32 v44, v44\n\t"
    "v_exp_f32 v45, v45\n\t"
    "v_exp_f32 v46, v46\n\t"
    "v_exp_f32 v47, v47\n\t"
    "v_add_f32 v40, 1.0, v40\n\t"
    "v_add_f32 v41, 1.0, v41\n\t"
    "v_add_f32 v42, 1.0, v42\n\t"
    "v_add_f32 v43, 1.0, v43\n\t"
    "v_add_f32 v44, 1.0, v44\n\t"
    "v_add_f32 v45, 1.0, v45\n\t"
    "v_add_f32 v46, 1.0, v46\n\t"
    "v_add_f32 v47, 1.0, v47\n\t"
    "v_rcp_f32 v40, v40\n\t"
    "v_rcp_f32 v41, v41\n\t"
    "v_rcp_f32 v42, v42\n\t"
    "v_rcp_f32 v43, v43\n\t"
    "v_rcp_f32 v44, v44\n\t"
    "v_rcp_f32 v45, v45\n\t"
    "v_rcp_f32 v46, v46\n\t"
    "v_rcp_f32 v47, v47\n\t"
    "v_mul_f32 v40, v48, v40\n\t"
    "v_mul_f32 v41, v49, v41\n\t"
    "v_mul_f32 v42, v50, v42\n\t"
    "v_mul_f32 v43, v51, v43\n\t"
    "v_mul_f32 v44, v56, v44\n\t"
    "v_mul_f32 v45, v57, v45\n\t"
    "v_mul_f32 v46, v58, v46\n\t"
    "v_mul_f32 v47, v59, v47\n\t"
    "v_mul_f32 v40, v40, v52\n\t"
    "v_mul_f32 v41, v41, v53\n\t"
    "v_mul_f32 v42, v42, v54\n\t"
    "v_mul_f32 v43, v43, v55\n\t"
    "v_mul_f32 v44, v44, v60\n\t"
    "v_mul_f32 v45, v45, v61\n\t"
    "v_mul_f32 v46, v46, v62\n\t"
    "v_mul_f32 v47, v47, v63\n\t"
    "v_cvt_pk_bf16_f32 v48, v40, v41\n\t"
    "v_cvt_pk_bf16_f32 v49, v42, v43\n\t"
    "v_cvt_pk_bf16_f32 v50, v44, v45\n\t"
    "v_cvt_pk_bf16_f32 v51, v46, v47\n\t"
    "s_nop 1\n\t"
    "v_permlane16_swap_b32 v48, v50\n\t"
    "v_permlane16_swap_b32 v49, v51\n\t"
    "global_store_dwordx4 %[co0], v[48:51], %[cb] sc1\n\t"
    "s_nop 1"
    :
    : [rw] "v"(rw), "{v[80:95]}"(pd), [fa] "v"(fa), [fv] "v"(fv), [cb] "s"(cb), [co0] "v"(co0), [nl] "s"(-LOG2E)
    : "v39", "v40", "v41", "v42", "v43", "v44", "v45", "v46", "v47", "v48", "v49", "v50", "v51", "v52", "v53", "v54", "v55", "v56", "v57", "v58", "v59", "v60", "v61", "v62", "v63", "vcc", "memory");
}
__device__ __forceinline__ void fin_st2(const u32x16& pd, unsigned rw, unsigned fa, unsigned fv, const char* cb, unsigned co0, unsigned co1) {
  asm volatile("s_nop 1\n\t"
    "v_cvt_pk_bf16_f32 v40, v80, v81\n\t"
    "v_cvt_pk_bf16_f32 v41, v82, v83\n\t"
    "v_cvt_pk_bf16_f32 v42, v84, v85\n\t"
    "v_cvt_pk_bf16_f32 v43, v86, v87\n\t"
    "global_store_dwordx4 %[co0], v[40:43], %[cb] sc1\n\t"
    "v_cvt_pk_bf16_f32 v44, v88, v89\n\t"
    "v_cvt_pk_bf16_f32 v45, v90, v91\n\t"
    "v_cvt_pk_bf16_f32 v46, v92, v93\n\t"
    "v_cvt_pk_bf16_f32 v47, v94, v95\n\t"
    "global_store_dwordx4 %[co1], v[44:47], %[cb] sc1\n\t"
    "1:\n\t"
    "ds_read_b32 v39, %[fa]\n\t"
    "ds_read_b128 v[48:51], %[rw] offset:0\n\t"
    "ds_read_b128 v[52:55], %[rw] offset:1024\n\t"
    "ds_read_b128 v[56:59], %[rw] offset:2048\n\t"
    "ds_read_b128 v[60:63], %[rw] offset:3072\n\t"
    "s_waitcnt lgkmcnt(2)\n\t"
    "v_cmp_ne_u32 vcc, v39, %[fv]\n\t"
    "s_cbranch_vccz 2f\n\t"
    "s_waitcnt lgkmcnt(0)\n\t"
    "s_branch 1b\n\t"
    "2:\n\t"
    "v_cvt_pk_bf16_f32 v48, v48, v49\n\t"
    "v_cvt_pk_bf16_f32 v49, v50, v51\n\t"
    "v_cvt_pk_bf16_f32 v50, v52, v53\n\t"
    "v_cvt_pk_bf16_f32 v51, v54, v55\n\t"
    "global_store_dwordx4 %[co0], v[48:51], %[cb] offset:2048 sc1\n\t"
    "s_waitcnt lgkmcnt(0)\n\t"
    "v_cvt_pk_bf16_f32 v56, v56, v57\n\t"
    "v_cvt_pk_bf16_f32 v57, v58, v59\n\t"
    "v_cvt_pk_bf16_f32 v58, v60, v61\n\t"
    "v_cvt_pk_bf16_f32 v59, v62, v63\n\t"
    "global_store_dwordx4 %[co1], v[56:59], %[cb] offset:2048 sc1\n\t"
    "s_nop 1"
    :
    : [rw] "v"(rw), "{v[80:95]}"(pd), [fa] "v"(fa), [fv] "v"(fv), [cb] "s"(cb), [co0] "v"(co0), [co1] "v"(co1)
    : "v39", "v40", "v41", "v42", "v43", "v44", "v45", "v46", "v47", "v48", "v49", "v50", "v51", "v52", "v53", "v54", "v55", "v56", "v57", "v58", "v59", "v60", "v61", "v62", "v63", "vcc", "memory");
}
template <int R> __device__ __forceinline__ void gw_load(u32x32& w, unsigned a0, unsigned a1);
template <> __device__ __forceinline__ void gw_load<0>(u32x32& w, unsigned a0, unsigned a1) {
  asm volatile("ds_read_b64_tr_b16 v[128:129], %1 offset:0\n\t"
    "ds_read_b64_tr_b16 v[130:131], %1 offset:512\n\t"
    "ds_read_b64_tr_b16 v[132:133], %2 offset:0\n\t"
    "ds_read_b64_tr_b16 v[134:135], %2 offset:512\n\t"
    "ds_read_b64_tr_b16 v[136:137], %1 offset:4096\n\t"
    "ds_read_b64_tr_b16 v[138:139], %1 offset:4608\n\t"
    "ds_read_b64_tr_b16 v[140:141], %2 offset:4096\n\t"
    "ds_read_b64_tr_b16 v[142:143], %2 offset:4608\n\t"
    "s_waitcnt lgkmcnt(0)\n\t"
    "ds_read_b64_tr_b16 v[144:145], %1 offset:16384\n\t"
    "ds_read_b64_tr_b16 v[146:147], %1 offset:16896\n\t"
    "ds_read_b64_tr_b16 v[148:149], %2 offset:16384\n\t"
    "ds_read_b64_tr_b16 v[150:151], %2 offset:16896\n\t"
    "ds_read_b64_tr_b16 v[152:153], %1 offset:20480\n\t"
    "ds_read_b64_tr_b16 v[154:155], %1 offset:20992\n\t"
    "ds_read_b64_tr_b16 v[156:157], %2 offset:20480\n\t"
    "ds_read_b64_tr_b16 v[158:159], %2 offset:20992\n\t"
    "s_waitcnt lgkmcnt(0)"
    : "=&{v[128:159]}"(w) : "v"(a0), "v"(a1) : "memory");
}
template <> __device__ __forceinline__ void gw_load<1>(u32x32& w, unsigned a0, unsigned a1) {
  asm volatile("ds_read_b64_tr_b16 v[160:161], %1 offset:0\n\t"
    "ds_read_b64_tr_b16 v[162:163], %1 offset:512\n\t"
    "ds_read_b64_tr_b16 v[164:165], %2 offset:0\n\t"
    "ds_read_b64_tr_b16 v[166:167], %2 offset:512\n\t"
    "ds_read_b64_tr_b16 v[168:169], %1 offset:4096\n\t"
    "ds_read_b64_tr_b16 v[170:171], %1 offset:4608\n\t"
    "ds_read_b64_tr_b16 v[172:173], %2 offset:4096\n\t"
    "ds_read_b64_tr_b16 v[174:175], %2 offset:4608\n\t"
    "s_waitcnt lgkmcnt(0)\n\t"
    "ds_read_b64_tr_b16 v[176:177], %1 offset:16384\n\t"
    "ds_read_b64_tr_b16 v[178:179], %1 offset:16896\n\t"
    "ds_read_b64_tr_b16 v[180:181], %2 offset:16384\n\t"
    "ds_read_b64_tr_b16 v[182:183], %2 offset:16896\n\t"
    "ds_read_b64_tr_b16 v[184:185], %1 offset:20480\n\t"
    "ds_read_b64_tr_b16 v[186:187], %1 offset:20992\n\t"
    "ds_read_b64_tr_b16 v[188:189], %2 offset:20480\n\t"
    "ds_read_b64_tr_b16 v[190:191], %2 offset:20992\n\t"
    "s_waitcnt lgkmcnt(0)"
    : "=&{v[160:191]}"(w) : "v"(a0), "v"(a1) : "memory");
}
template <> __device__ __forceinline__ void gw_load<2>(u32x32& w, unsigned a0, unsigned a1) {
  asm volatile("ds_read_b64_tr_b16 v[192:193], %1 offset:0\n\t"
    "ds_read_b64_tr_b16 v[194:195], %1 offset:512\n\t"
    "ds_read_b64_tr_b16 v[196:197], %2 offset:0\n\t"
    "ds_read_b64_tr_b16 v[198:199], %2 offset:512\n\t"
    "ds_read_b64_tr_b16 v[200:201], %1 offset:4096\n\t"
    "ds_read_b64_tr_b16 v[202:203], %1 offset:4608\n\t"
    "ds_read_b64_tr_b16 v[204:205], %2 offset:4096\n\t"
    "ds_read_b64_tr_b16 v[206:207], %2 offset:4608\n\t"
    "s_waitcnt lgkmcnt(0)\n\t"
    "ds_read_b64_tr_b16 v[208:209], %1 offset:16384\n\t"
    "ds_read_b64_tr_b16 v[210:211], %1 offset:16896\n\t"
    "ds_read_b64_tr_b16 v[212:213], %2 offset:16384\n\t"
    "ds_read_b64_tr_b16 v[214:215], %2 offset:16896\n\t"
    "ds_read_b64_tr_b16 v[216:217], %1 offset:20480\n\t"
    "ds_read_b64_tr_b16 v[218:219], %1 offset:20992\n\t"
    "ds_read_b64_tr_b16 v[220:221], %2 offset:20480\n\t"
    "ds_read_b64_tr_b16 v[222:223], %2 offset:20992\n\t"
    "s_waitcnt lgkmcnt(0)"
    : "=&{v[192:223]}"(w) : "v"(a0), "v"(a1) : "memory");
}
template <> __device__ __forceinline__ void gw_load<3>(u32x32& w, unsigned a0, unsigned a1) {
  asm volatile("ds_read_b64_tr_b16 v[224:225], %1 offset:0\n\t"
    "ds_read_b64_tr_b16 v[226:227], %1 offset:512\n\t"
    "ds_read_b64_tr_b16 v[228:229], %2 offset:0\n\t"
    "ds_read_b64_tr_b16 v[230:231], %2 offset:512\n\t"
    "ds_read_b64_tr_b16 v[232:233], %1 offset:4096\n\t"
    "ds_read_b64_tr_b16 v[234:235], %1 offset:4608\n\t"
    "ds_read_b64_tr_b16 v[236:237], %2 offset:4096\n\t"
    "ds_read_b64_tr_b16 v[238:239], %2 offset:4608\n\t"
    "s_waitcnt lgkmcnt(0)\n\t"
    "ds_read_b64_tr_b16 v[240:241], %1 offset:16384\n\t"
    "ds_read_b64_tr_b16 v[242:243], %1 offset:16896\n\t"
    "ds_read_b64_tr_b16 v[244:245], %2 offset:16384\n\t"
    "ds_read_b64_tr_b16 v[246:247], %2 offset:16896\n\t"
    "ds_read_b64_tr_b16 v[248:249], %1 offset:20480\n\t"
    "ds_read_b64_tr_b16 v[250:251], %1 offset:20992\n\t"
    "ds_read_b64_tr_b16 v[252:253], %2 offset:20480\n\t"
    "ds_read_b64_tr_b16 v[254:255], %2 offset:20992\n\t"
    "s_waitcnt lgkmcnt(0)"
    : "=&{v[224:255]}"(w) : "v"(a0), "v"(a1) : "memory");
}
template <int I, int E, class F> __device__ __forceinline__ void sfor(F&& f) { if constexpr (I < E) { f(std::integral_constant<int, I>{}); sfor<I + 1, E>(f); } }
#define WALL 1
template <int ROLE, int KQ, class F>
__device__ __forceinline__ void wload(char* smem, int wave_id_, const u16* W0, const u16* W1, int ldb, u32x32 (&bw)[4], int rot, F&& hook) {
  constexpr int ABUF = 65536;
  const int tid = opaque_tid(), lane = tid & 63, wave = WAVE_ID;
  const int kq = KQ == 2 ? wave >> 2 : 0, nq = KQ == 2 ? wave & 3 : wave;
  const int g = lane >> 4, r16 = lane & 15, q4 = r16 >> 2, p4 = lane & 3;
  const unsigned ldsb = __builtin_amdgcn_readfirstlane((unsigned)(size_t)(LAS char*)smem);
  {
    const int srow = 8 * wave + (lane >> 3), ch = (lane & 7) ^ bswz(srow);
    const unsigned voff = (unsigned)(srow * ldb + ch * 8) * 2;
    const char* w0 = uni((const char*)W0); const char* w1 = uni((const char*)(ROLE == R_F1 ? W1 : W0 + 64));
    const size_t kb = (size_t)64 * ldb * 2;
    auto wsrc = [&](int r, int i) {
      const int kbi = KQ == 2 ? (ROLE == R_PART2 ? 0 : (i >> 2) * 8) + ((2 * r + ((i >> 1) & 1)) ^ rot) : 2 * r + (i >> 2);
      return KQ == 2 ? ((i & 1) ? w1 : w0) + kbi * kb + (ROLE == R_PART2 ? (i >> 2) * 256 : 0) : w0 + kbi * kb + (i & 3) * 128;
    };
    auto wstage = [&](int r) {
      const unsigned ld = ldsb + (r & 1) * ABUF + wave * 1024;
#pragma unroll
      for (int i = 0; i < 8; ++i) {
        unsigned keep;
        asm volatile("s_mov_b32 %0, m0\n\ts_mov_b32 m0, %3\n\ts_nop 0\n\tglobal_load_lds_dwordx4 %1, %2" WNT_ "\n\ts_mov_b32 m0, %0"
                     : "=&s"(keep) : "v"(voff), "s"(wsrc(r, i)), "s"(ld + i * 8192) : "memory");
      }
    };
    const int brow = 8 * g + q4, bs = bswz(brow);
    unsigned la[2];
#pragma unroll
    for (int j = 0; j < 2; ++j) {
      int blk, c16, sub;
      if (ROLE == R_F1) { blk = j; c16 = nq * 2 + (p4 >> 1); sub = (p4 & 1) * 8; }
      else { blk = nq >> 1; c16 = (nq & 1) * 4 + p4; sub = j * 8; }
      int set = kq;
      if (ROLE == R_PART2 && TLP) { set = nq >> 1; blk = nq & 1; c16 = kq * 4 + p4; }
      la[j] = (unsigned)((KQ == 2 ? set * 4 + blk : blk) * 8192 + brow * 128 + ((c16 ^ bs) << 4) + sub);
    }
    wstage(0); wstage(1);
    u32x4 raw[2][8];
#pragma unroll
    for (int r = 0; r < 2; ++r)
#pragma unroll
      for (int i = 0; i < 8; ++i) asm volatile("global_load_dwordx4 %0, %1, %2" WNT_ : "=&v"(raw[r][i]) : "v"(voff), "s"(wsrc(r + 2, i)) : "memory");
    sfor<0, 4>([&](auto r_c) __attribute__((always_inline)) {
      constexpr int r = decltype(r_c)::value;
      if (r == 0) WAIT_V(24); else if (r == 1) WAIT_V(16);
      if (r == 3) BAR(); else if (r < 2) { __builtin_amdgcn_s_barrier(); asm volatile("" ::: "memory"); }
      gw_load<r>(bw[r], ldsb + (r & 1) * ABUF + la[0], ldsb + (r & 1) * ABUF + la[1]);
      if (r == 1 || r == 2) {
        u32x4 (&q)[8] = raw[(r + 1) & 1];
        if (r == 1) asm volatile("s_waitcnt vmcnt(8)" : "+v"(q[0]), "+v"(q[1]), "+v"(q[2]), "+v"(q[3]), "+v"(q[4]), "+v"(q[5]), "+v"(q[6]), "+v"(q[7]) :: "memory");
        else asm volatile("s_waitcnt vmcnt(0)" : "+v"(q[0]), "+v"(q[1]), "+v"(q[2]), "+v"(q[3]), "+v"(q[4]), "+v"(q[5]), "+v"(q[6]), "+v"(q[7]) :: "memory");
#pragma unroll
        for (int i = 0; i < 8; ++i) *(u32x4*)(smem + ((r + 1) & 1) * ABUF + wave * 1024 + i * 8192 + lane * 16) = q[i];
        if (r == 1) BAR();
      }
      if (r == 3) { hook(); BAR(); }
    });
  }
}
#define DSR2(d0, d1, a0_, a1_, off) asm volatile("ds_read_b128 %0, %2 offset:%4\n\tds_read_b128 %1, %3 offset:%4" : "=&v"(d0), "=&v"(d1) : "v"(a0_), "v"(a1_), "i"(off))
template <int N> __device__ __forceinline__ void waitl(u32x4& a, u32x4& b) { asm volatile("s_waitcnt lgkmcnt(%2)" : "+v"(a), "+v"(b) : "i"(N)); }
__device__ __forceinline__ void glds1(const char* sbase, unsigned voff, unsigned lds) {
  unsigned keep;
  asm volatile("s_mov_b32 %0, m0\n\ts_mov_b32 m0, %3\n\ts_nop 0\n\tglobal_load_lds_dwordx4 %1, %2 sc1\n\ts_mov_b32 m0, %0"
               : "=&s"(keep) : "v"(voff), "s"(sbase), "s"(lds) : "memory");
}
#define XS(i) do { } while (0)
#define XFL(b_) do { } while (0)
#define XA_ 0
#define XF_ 0
#define XE_ 0
#define XP_ 0
#define PUBK 9
#define LATE 0
#define NOWP 2
#define NEARM 15
#define PNEAR 3
#define LATEQ 0
#define DEFP 1
#define ROTM 0
#define ROTK 7
#define BPACE 40
#define W2 1
#define PDEEP 4
template <int ROLE>
__device__ __forceinline__ void gemm7(char* smem, int wave_id_, const u16* W0, const u16* W1, int ldb, const char* A_, size_t a_blk, const Dep& dep,
                                      char* C_, size_t c_blk, unsigned* myprog, unsigned* tmo, int slot, unsigned long long* mts,
                                      bool gl, int nimm = 0, int nseg = NSEG, int wm0 = 0, int pofs = 0) {
  volatile LAS unsigned* wsl = (volatile LAS unsigned*)(smem + L_SLOT + 512);
  if (opaque_tid() < 144) *(volatile LAS unsigned*)(smem + L_SLOT + opaque_tid() * 4) = 0u;
  const int tid = opaque_tid(), lane = tid & 63, wave = WAVE_ID;
  const int kq = wave >> 2, nq = wave & 3;
  const unsigned ldsb = __builtin_amdgcn_readfirstlane((unsigned)(size_t)(LAS char*)smem);
  const int rot = (ROTM >> ROLE) & 1 ? slot & ROTK : 0;
  const char* A = uni(A_) + nq * 1024;
  constexpr bool NEAR = (NEARM >> ROLE) & 1;
  constexpr int AH = NEAR ? 2 : 3, PD = NEAR ? PNEAR : PDEEP;
  constexpr bool P2C = ROLE == R_PART2;
  int pw = 0;
  int wm = wm0, nh = 0;
  auto lpoll = [&](int b, bool hs = false) {
    unsigned w;
    if (wave == 4) {
      for (int spins = 0;; ++spins) {
        unsigned v;
        if (hs) { WAIT_V(0); v = *(volatile LAS unsigned*)(smem + L_SLOT + 768 + lane_id() * 4); }
        else asm volatile("global_load_dword %0, %1, %2 sc1\n\ts_waitcnt vmcnt(0)" : "=&v"(v) : "v"(dep.voff), "s"(dep.base) : "memory");
        w = dep_eval(v, dep);
        if ((int)w > b) break;
        if (!hs) __builtin_amdgcn_s_sleep(2);
        hs = false;
        if (give_up(spins, tmo, 9)) { w = NSEG; break; }
      }
      w = w < 255u ? w : 255u;
      if (lane == 0) wsl[0] = ((unsigned)(b + 1) << 8) | w;
    } else {
      for (;;) {
        w = __builtin_amdgcn_readfirstlane(wsl[0]);
        if ((w >> 8) == (unsigned)(b + 1)) break;
        __builtin_amdgcn_s_sleep(1);
      }
      w &= 255u;
    }
    return (int)w;
  };
  auto pubw = [&](int n) {
    WAIT_V(0);
    if (lane == 0) {
      const unsigned o = __hip_atomic_fetch_add((LAS unsigned*)(smem + L_SLOT + 516), 1u, __ATOMIC_RELAXED, __HIP_MEMORY_SCOPE_WORKGROUP);
      if ((o & 3u) == 3u) {
        st4_sc1(myprog, (unsigned)(n + pofs));
      }
    }
    asm volatile("s_waitcnt lgkmcnt(0)" ::: "memory");
  };
  auto hsrc = [&](int hh) { return A + (size_t)(hh >> 1) * a_blk + ((hh ^ (rot >> 2)) & 1) * 16384; };
  auto hlds = [&](int hh) { return ldsb + (unsigned)(((hh >> 1) & 1) * 65536 + (hh & 1) * 16384 + nq * 1024); };
#define POFF(i_) (((i_) & 1) * 32768 + ((i_) >> 1) * 4096)
#define PIECE(s_, l_, i_) asm volatile("s_add_u32 m0, %2, %3\n\ts_nop 0\n\tglobal_load_lds_dwordx4 %0, %1 sc1" :: "v"(av[i_]), "s"(s_), "s"(l_), "i"(POFF(i_)) : "memory", "scc", "m0")
  u32x32 BW[4];
  KTS(1);
  wload<ROLE, 2>(smem, wave_id_, W0, W1, ldb, BW, rot, [&]() {
    if (ROLE == R_QKV && nimm > 0) {
      if (kq) {
        wm = lpoll(0);
        const unsigned l16 = lane_id() * 16;
#pragma unroll
        for (int i = 0; i < 16; ++i) glds1(hsrc(i >> 3) + (i & 1) * 32768 + ((((i & 7) >> 1) ^ rot) & 3) * 4096, l16, hlds(i >> 3) + POFF(i & 7));
      }
      nh = 2;
    }
  });
  KTS(2);
  if (ROLE == R_QKV && nimm > 0 && !kq) wm = (int)(__builtin_amdgcn_readfirstlane(wsl[0]) & 255u);
  if (ROLE == R_QKV && opaque_tid() == 0) STS((char*)tmo - OFF_TMO, 128 + slot, 0);
  auto body = [&](auto ldr_c) __attribute__((always_inline)) {
    constexpr bool LDR = decltype(ldr_c)::value;
    if (XP_ == 0 ? !LDR : (XP_ == 1 ? LDR : false)) __builtin_amdgcn_s_setprio(1);
    unsigned av[8];
#pragma unroll
    for (int i = 0; i < 8; ++i) av[i] = lane * 16 + (P2C ? 0 : (i & 1) * 32768) + (((i >> 1) ^ rot) & 3) * 4096;
    auto issue_all = [&](int hh) {
      if (LDR) {
        const char* s_ = hsrc(hh); const unsigned l_ = hlds(hh);
#pragma unroll
        for (int i = 0; i < 8; ++i) if (!(P2C && (i & 1))) PIECE(s_, l_, i);
      }
    };
    int b = 0;
    bool hs = false;
    while (b < nseg) {
      const int ln = lane_id(), g = ln >> 4, r16 = ln & 15;
      const unsigned e0 = ldsb + (unsigned)((P2C ? 0 : kq * 32768) + (b & 1) * 65536 + (gl ? (g >> 1) * 512 + r16 * 32 + (g & 1) * 16 : r16 * 128 + ((g ^ (r16 & 7)) << 4)));
      const unsigned d0 = e0 ^ (gl ? 1024u : 64u);
      const unsigned rw = ldsb + L_RED + nq * 4096 + ln * 16;
      const unsigned co0 = ROLE == R_F1 ? (unsigned)((g & 1) * 2048 + nq * 512 + r16 * 32 + (g >> 1) * 16)
                         : ((ROLE == R_QKV ? TLQ : TLP) ? (unsigned)(nq * (P2C ? 4096 : 2048) + r16 * 64 + g * 16)
                         : (ROLE == R_QKV ? (unsigned)(r16 * 256 + (((4 * nq + g) ^ r16) << 4)) : (unsigned)(r16 * PROW + (32 * nq + 8 * g) * 2)));
      const unsigned co1 = co0 + ((ROLE == R_QKV ? TLQ : TLP) ? 1024u : (ROLE == R_QKV ? 4096u : 16u * PROW));
      u32x16 pv_;
      bool smp = false, lt0 = false;
      if ((wm < nseg ? wm : nseg) >= b + PD && b >= nimm && nh <= 2 * b + AH) {
        __builtin_amdgcn_s_barrier(); asm volatile("" ::: "memory");
        for (; nh < 2 * b + AH; ++nh) issue_all(nh);
        int lf = (wm < nseg ? wm : nseg) - (AH - 1), cp = nseg - (AH - 1);
        if (!LDR) KTS(128 + b);
        const unsigned sa = ldsb + L_SLOT + ln * 4, ad = dep.add - (AH - 1);
        if constexpr (LDR) (NEAR ? (P2C ? near_ld2 : near_ld0) : (P2C ? fast_ld2 : fast_ld0))(BW, pv_, e0, d0, b, lf, cp, sa, dep.mul, ad, rw, (unsigned)a_blk, hsrc(2 * b + AH), hsrc(2 * b + AH + 1), hlds(2 * b + AH), hlds(2 * b + AH + 1), av);
        else {
          const char* c0_ = uni((const char*)tmo - OFF_TMO + OFF_DUMP); const char* c1_ = uni(C_ + (size_t)b * c_blk);
          const int sf = b + pofs - (pw > 0 ? 1 : 0);
          const char* pg = uni((const char*)myprog);
          (NEAR ? (ROLE == R_F1 ? near_st1 : (P2C ? near_st2 : near_st0)) : (ROLE == R_F1 ? fast_st1 : (P2C ? fast_st2 : fast_st0)))(BW, pv_, e0, d0, b, lf, cp, sa, dep.mul, ad, rw, (unsigned)c_blk, c0_, c1_, co0, co1, pofs, sf, (unsigned)wave,
                                                                   ldsb + L_SLOT, dep.voff, dep.base, pg);
        }
        if (!LDR) KTS(64 + b);
        wm = lf + (AH - 1) > wm ? lf + (AH - 1) : wm;
        nh = 2 * b + AH;
        --b;
        __builtin_amdgcn_s_barrier(); asm volatile("" ::: "memory");
      } else if (NEAR && b >= BPACE) {
        if constexpr (LDR) {
          int cap = wave == 4 ? SPIN_CAP : 8 * SPIN_CAP;
          if (hs) WAIT_V(0);
          (P2C ? paced_ld2 : paced_ld0)(BW, e0, d0, b, nseg, (unsigned)a_blk, rw, ldsb + L_SLOT + 528 + nq * 4, hsrc(nh), hsrc(nh + 1), hlds(nh), cap, wm, (unsigned)(nh - 2 * b),
                                        dep.mul, dep.add, dep.voff, dep.base, wave == 4 ? ldsb + L_SLOT : 0u, ldsb + L_SLOT + ln * 4, av);
          if (cap == 0 && ln == 0) st4_sc1(tmo, 15u);
        } else (ROLE == R_F1 ? paced_st1 : (P2C ? paced_st2 : paced_st0))(BW, e0, d0, b, nseg, (unsigned)c_blk, rw, ldsb + L_SLOT + 528 + nq * 4, uni(C_ + (size_t)b * c_blk),
                                                                          __builtin_amdgcn_readfirstlane(pw > 0 ? 1u : 0u), pofs, ldsb + L_SLOT + 516, uni((const char*)myprog), co0, co1);
        break;
      } else {
        const bool pl = wm <= b;
        XS(0);
        if (pl && LDR) wm = lpoll(b, hs);
        XS(1);
        const bool cold = nh < 2 * b + 2;
        { const int upto = nh == 2 * b + 3 ? 2 * b + 4 : 2 * b + 2; for (; nh < upto; ++nh) issue_all(nh); }
        smp = nh == 2 * b + 2 && b + 1 < nseg && wm0 < nseg;
        if (LDR && wave == 4 && smp)
          asm volatile("s_mov_b32 m0, %0\n\ts_nop 0\n\tglobal_load_lds_dword %1, %2 sc1" :: "s"(ldsb + L_SLOT + (b & 1) * 256), "v"(dep.voff), "s"(dep.base) : "memory", "m0");
        XS(2);
        if (LDR && LEAN_QB) { if (nh >= 2 * b + 4) { if (P2C) WAIT_V(14); else WAIT_V(28); } else { if (P2C) WAIT_V(6); else WAIT_V(12); } }
        else if (LDR) { if (nh >= 2 * b + 4) { if (P2C) WAIT_V(12); else WAIT_V(24); } else { if (P2C) WAIT_V(4); else WAIT_V(8); } }
        BAR();
        XS(3);
        if (pl && !LDR) wm = (int)(__builtin_amdgcn_readfirstlane(wsl[0]) & 255u);
        const bool dm = nh == 2 * b + 2 && wm > b + 1 && b + 1 < nseg;
        if (ROLE == R_QKV && b < 2 && opaque_tid() == 0) STS((char*)tmo - OFF_TMO, 128 + slot, 1 + b);
        lt0 = smp && !dm;
        if constexpr (LDR) {
          if (dm) (P2C ? lean_dm1 : lean_dm0)(BW, pv_, e0, d0, 0u, hlds(nh), hsrc(nh), hsrc(nh + 1), av);
          else (P2C ? lean_ld1 : lean_ld0)(BW, pv_, e0, d0, (unsigned)(nh - 2 * b - 2), hlds(nh), hsrc(nh), hsrc(nh + 1), av, ldsb + L_SLOT + (b & 1) * 256 + ln * 4, dep.mul, dep.add, LATEQ && lt0 ? b + 1 : 0x7FFFFFFF,
                                           __builtin_amdgcn_readfirstlane(lt0 && wave == 4 ? ldsb + L_SLOT + 768 : 0u), dep.voff, dep.base);
        }
        else lean_st0(BW, pv_, e0, d0, __builtin_amdgcn_readfirstlane(pw > 0 ? (cold ? 4u : 1u) : 0u), (unsigned)(pw + pofs), ldsb + L_SLOT + 516, ldsb + L_SLOT + 256,
                     dep.voff, dep.base, uni((const char*)myprog));
        if (dm) nh += 2;
        XS(5);
      }
      if constexpr (LDR) fin_ld(pv_, rw, ldsb + L_SLOT + 528 + nq * 4, (unsigned)(b + 1));
      else (ROLE == R_F1 ? fin_st1 : (P2C ? fin_st2 : fin_st0))(pv_, rw, ldsb + L_SLOT + 528 + nq * 4, (unsigned)(b + 1), uni(C_ + (size_t)b * c_blk), co0, co1);
      XS(6);
      if (smp) {
        const int w = (int)dep_eval(*(volatile LAS unsigned*)(smem + L_SLOT + (b & 1) * 256 + lane_id() * 4), dep);
        if (LATEQ && lt0 && w > b + 1) nh += 2;
        wm = w > wm ? w : wm;
      }
      bool now = (NOWP == 2 ? nh < 2 * b + 4 && wm < b + 1 + PD : wm <= b + 1) || b + 1 >= nseg || b < nimm;
      if (!LDR && now) {
        if (DEFP && b >= nimm && b + 1 < nseg && (wm > b + 1 || (__builtin_amdgcn_readfirstlane(wsl[0]) >> 8) == (unsigned)(b + 2))) now = false;
        else pubw(b + 1);
      }
      XS(7);
      XFL(b);
      pw = now ? 0 : b + 1;
      hs = W2 && lt0;
      ++b;
    }
    __builtin_amdgcn_s_setprio(0);
  };
  if (kq) body(std::true_type{}); else body(std::false_type{});
#undef PIECE
#undef POFF
  WAIT_V(0);
  BAR();
  KTS(3);
}
__device__ __forceinline__ void fin_role(char* smem, int wave_id_, int fidx, const float* mod, const char* P2, const u16* x1, u16* out, unsigned* cnt, unsigned* tmo,
                                         unsigned long long* mts) {
  const int tid = opaque_tid(), lane = tid & 63, wave = WAVE_ID;
  row_table(smem + L_GB, mod, cnt, tmo, tid);
  volatile LAS unsigned* wsl = (volatile LAS unsigned*)(smem + L_SLOT + 512);
  int wf = 0, par = 0;
  for (int nf = fidx >> 3; nf < NSEG; nf += 2) {
    if (wf <= nf) {
      if (wave == 0) {
        unsigned w;
        for (int spins = 0;; ++spins) {
          const unsigned v = rowmin16(ld4_sc1(cnt + (PF2 + ((lane & 31) < 28 ? (lane & 31) : 0)) * 32));
          w = min(rdl(v, 0), rdl(v, 16));
          if ((int)w > nf) break;
          __builtin_amdgcn_s_sleep(2);
          if (give_up(spins, tmo, 9)) { w = NSEG; break; }
        }
        if (lane == 0) wsl[par] = w;
      }
      BAR();
      wf = (int)__builtin_amdgcn_readfirstlane(wsl[par]);
      par ^= 1;
    }
    int ln = lane; asm volatile("" : "+v"(ln));
    if (wave == 0) WTS(256 + 8 + fidx, nf / 2, 0);
    fin_half(P2, x1, smem + L_GB + 12288, out, nf * 32 + 4 * (fidx & 7) + (wave >> 1), wave & 1, ln);
    if (wave == 0) WTS(256 + 8 + fidx, nf / 2, 2);
  }
}
constexpr int Q_MEM = 0;
constexpr int Q_QK  = 16384;
constexpr int Q_TM  = 22528;
constexpr int Q_PQ  = 24576;
constexpr int Q_K   = 32768;
constexpr int Q_RING = 32768;
constexpr int NCHAIN = 96;
#define NQQ 6
#define NQK 4
#define E_AG 8
#define RDYI -1
#define ROTW 2
#define DQU 6
#define DQM 18
#define DQD 1
#define ORSEL (xg >> 1)
#define D_Q 85
#define D_O 5
#define D_A 0
#define WOF 1
#define E_S -1
#define RAE 0
#define RAW 5
#define E_MIN 3
#define E_W 3
#define DOU 0
#define DOM 12
#define CL0 1
#define TSW 0
#define E_B 8
#define E_O 1900
#define KDEL 5
#define VPOS 0

typedef __bf16 bf16x16 __attribute__((ext_vector_type(16)));
typedef unsigned u32x8 __attribute__((ext_vector_type(8)));
__device__ __forceinline__ f32x4 SMF(u32x4 a, u32x8 b, f32x4 c, int idx) {
  return __builtin_amdgcn_smfmac_f32_16x16x64_bf16(__builtin_bit_cast(bf16x8, a), __builtin_bit_cast(bf16x16, b), c, idx, 0, 0);
}
__device__ __forceinline__ float fold32(float a, float b) {
  const auto p = __builtin_amdgcn_permlane32_swap(__float_as_uint(a), __float_as_uint(b), false, false);
  return __uint_as_float(p[0]) + __uint_as_float(p[1]);
}
__device__ __forceinline__ f32x2 rowsum(f32x4 x) { return f32x2{fold32(x[0], x[2]), fold32(x[1], x[3])}; }
__device__ __forceinline__ unsigned nosent1(unsigned v) { return v < 0xFFFFFFFEu ? v : 0xFFFFFFFEu; }
__device__ __forceinline__ void st4s(const char* sb, unsigned vo, unsigned v) { asm volatile("global_store_dword %0, %1, %2 sc1\n\ts_nop 0" :: "v"(vo), "v"(v), "s"(sb) : "memory"); }
__device__ __forceinline__ void st8s(const char* sb, unsigned vo, u32x2 v) { asm volatile("global_store_dwordx2 %0, %1, %2 sc1\n\ts_nop 1" :: "v"(vo), "v"(v), "s"(sb) : "memory"); }
__device__ __forceinline__ void gldsw(const char* sbase, unsigned voff, unsigned lds) {
  unsigned keep;
  asm volatile("s_mov_b32 %0, m0\n\ts_mov_b32 m0, %3\n\ts_nop 0\n\tglobal_load_lds_dwordx4 %1, %2" CNT_ "\n\ts_mov_b32 m0, %0"
               : "=&s"(keep) : "v"(voff), "s"(sbase), "s"(lds) : "memory");
}
__device__ __forceinline__ void wait_clk(unsigned t0, unsigned d) {
  while ((unsigned)__builtin_amdgcn_s_memtime() - t0 < d) __builtin_amdgcn_s_sleep(1);
}
__device__ __forceinline__ unsigned bell_time(const char* a, unsigned vo, unsigned tag, unsigned cap, unsigned v = 0) {
  for (unsigned spins = 0; !__all((v >> 24) >= tag); ++spins) {
    asm volatile("global_load_dword %0, %1, %2 sc1\n\ts_waitcnt vmcnt(0)" : "=&v"(v) : "v"(vo), "s"(a) : "memory");
    if (__all((v >> 24) >= tag) || spins >= cap) break;
  }
  const unsigned now = (unsigned)__builtin_amdgcn_s_memrealtime() + 250u;
  const unsigned age = rowmin16((now - v) & 0xFFFFFFu);
  unsigned r = __builtin_amdgcn_readfirstlane(now - min(min(rdl(age, 0), rdl(age, 16)), 500u));
  asm volatile("" : "+s"(r));
  return r;
}
__device__ __forceinline__ void wait_rt(unsigned t0, int d) {
  while ((int)((unsigned)__builtin_amdgcn_s_memrealtime() - t0) < d) { }
}
#define WBAR() do { __builtin_amdgcn_s_barrier(); asm volatile("" ::: "memory"); } while (0)
#define SB() __builtin_amdgcn_sched_barrier(0)
#define TN(k, v) do { } while (0)
template <bool HALF>
__device__ __forceinline__ void stage_proj(const char* src, unsigned lds, int tid, int wave) {
  const int row = tid >> 4, f = 2 * ((row & 1) | (((row >> 2) & 3) << 1));
#pragma unroll
  for (int j = 0; j < 4; ++j) {
    const int pc = (tid & 15) ^ f;
    gldsw(src + (size_t)j * 32 * HD * 2, (unsigned)(HALF ? (row + (pc >> 3) * 128) * HD * 2 + ((pc & 7) << 4) : row * HD * 2 + (pc << 4)), lds + (wave * 64 + 512 * j) * 16);
  }
}
__device__ __forceinline__ unsigned proj_base(int T, int lane) {
  const int g = lane >> 4, q4 = (lane & 15) >> 2, p4 = lane & 3, fs = 2 * ((q4 & 1) | ((g & 1) << 1) | ((q4 >> 1) << 2));
  return (unsigned)((16 * (g >> 1) + 4 * (g & 1) + (q4 & 1) + 8 * (q4 >> 1)) * 256 + (((2 * T) ^ fs) << 4) + p4 * 8);
}
__device__ __forceinline__ f32x4 zero4() { float z = 0.0f; asm volatile("" : "+v"(z)); return f32x4{z, z, z, z}; }
__device__ __forceinline__ unsigned lds_word(const char* p) { return __builtin_amdgcn_readfirstlane(*(volatile LAS unsigned*)p); }

#define MFMA16(a, b, c) __builtin_amdgcn_mfma_f32_16x16x16bf16_1k((a), (b), (c), 0, 0, 0)
#define UF(x) __uint_as_float(x)
__device__ __forceinline__ unsigned umax3(unsigned a, unsigned b, unsigned c) { return max(max(a, b), c); }
__device__ __forceinline__ f32x2 logit2(float a, float b) {
  return f32x2{a, b};
}
constexpr float LSC = LOG2E * SCALE;

#define FCS() do { } while (0)
struct QKW { u32x32 w0, w1, w2, w3, w4, w5; };
template <int W_> __device__ __forceinline__ void qk_step(const QKW& W, const char* ag, const char* vs, const char* v2, const char* od, unsigned pb, unsigned sn, unsigned& cap, unsigned& sp,
    unsigned long long& tc, unsigned long long* ts, unsigned& d0, unsigned& d1);
template <> __device__ __forceinline__ void qk_step<0>(const QKW& W, const char* ag, const char* vs, const char* v2, const char* od, unsigned pb, unsigned sn, unsigned& cap, unsigned& sp,
    unsigned long long& tc, unsigned long long* ts, unsigned& d0, unsigned& d1) {
  unsigned st, bg;
  asm volatile("v_mbcnt_lo_u32_b32 v60, -1, 0\n\t"
    "v_mbcnt_hi_u32_b32 v60, -1, v60\n\t"
    "v_lshlrev_b32 v5, 4, v60\n\t"
    "s_mov_b32 %[sp], 0\n\t"
    "s_setprio 2\n\t"
    "global_load_dwordx4 v[24:27], v5, %[ag] sc1\n\t"
    "global_load_dwordx4 v[28:31], v5, %[ag] offset:1024 sc1\n\t"
    "s_memtime %[tc]\n\t"
    "v_lshrrev_b32 v32, 4, v60\n\t"
    "v_and_b32 v33, 15, v60\n\t"
    "v_lshrrev_b32 v34, 2, v33\n\t"
    "v_and_b32 v35, 3, v60\n\t"
    "v_and_b32 v61, 1, v60\n\t"
    "v_lshlrev_b32 v61, 3, v61\n\t"
    "v_lshl_or_b32 v7, v34, 4, v61\n\t"
    "v_lshl_or_b32 v7, v32, 7, v7\n\t"
    "v_xor_b32 v10, v32, v33\n\t"
    "v_lshlrev_b32 v10, 4, v10\n\t"
    "v_lshl_or_b32 v10, v33, 8, v10\n\t"
    "v_or_b32 v10, %[pb], v10\n\t"
    "v_xor_b32 v11, 0x40, v10\n\t"
    "v_xor_b32 v12, 0x80, v10\n\t"
    "v_xor_b32 v13, 0xc0, v10\n\t"
    "v_lshl_or_b32 v34, v32, 2, v34\n\t"
    "v_lshrrev_b32 v35, 1, v35\n\t"
    "v_or_b32 v35, 0, v35\n\t"
    "v_xor_b32 v35, v35, v34\n\t"
    "v_lshl_or_b32 v14, v35, 4, v61\n\t"
    "v_lshl_or_b32 v14, v34, 8, v14\n\t"
    "v_or_b32 v14, %[pb], v14\n\t"
    "v_and_b32 v61, 3, v60\n\t"
    "v_lshlrev_b32 v61, 6, v61\n\t"
    "v_lshl_or_b32 v4, v32, 4, v61\n\t"
    "v_and_b32 v61, 4, v60\n\t"
    "v_lshl_or_b32 v4, v61, 10, v4\n\t"
    "v_and_b32 v61, 8, v60\n\t"
    "v_lshl_or_b32 v4, v61, 14, v4\n\t"
    "v_or_b32 v4, 0, v4\n\t"
    "v_and_b32 v61, 2, v60\n\t"
    "v_mul_u32_u24 v61, 0x5555, v61\n\t"
    "v_or_b32 v6, %[c44], v61\n\t"
    "v_lshlrev_b32 v61, 2, v32\n\t"
    "v_lshl_or_b32 v15, v33, 4, v61\n\t"
    "3:\n\t"
    "v_mov_b32 v16, 0\n\t"
    "v_mov_b32 v17, 0\n\t"
    "v_mov_b32 v18, 0\n\t"
    "v_mov_b32 v19, 0\n\t"
    "v_mov_b32 v20, 0\n\t"
    "v_mov_b32 v21, 0\n\t"
    "v_mov_b32 v22, 0\n\t"
    "v_mov_b32 v23, 0\n\t"
    "s_waitcnt vmcnt(0) lgkmcnt(0)\n\t"
    "ds_write_b128 v5, v[24:27] offset:0\n\t"
    "ds_write_b128 v5, v[28:31] offset:1024\n\t"
    "v_smfmac_f32_16x16x64_bf16 v[16:19], v[24:27], v[64:71], v6\n\t"
    "v_max3_u32 v60, v24, v25, v26\n\t"
    "v_max3_u32 v61, v27, v28, v29\n\t"
    "v_smfmac_f32_16x16x64_bf16 v[20:23], v[24:27], v[72:79], v6\n\t"
    "v_max3_u32 v60, v60, v30, v31\n\t"
    "v_max_u32 v60, v60, v61\n\t"
    "v_cmp_eq_u32 vcc, -1, v60\n\t"
    "s_cbranch_vccnz 1f\n\t"
    "2:\n\t"
    "s_waitcnt lgkmcnt(0)\n\t"
    "s_barrier\n\t"
    "ds_read_b128 v[32:35], v5 offset:2048\n\t"
    "ds_read_b128 v[36:39], v5 offset:3072\n\t"
    "v_smfmac_f32_16x16x64_bf16 v[16:19], v[28:31], v[80:87], v6\n\t"
    "ds_read_b128 v[40:43], v5 offset:4096\n\t"
    "ds_read_b128 v[44:47], v5 offset:5120\n\t"
    "v_smfmac_f32_16x16x64_bf16 v[20:23], v[28:31], v[88:95], v6\n\t"
    "s_waitcnt lgkmcnt(3)\n\t"
    "v_smfmac_f32_16x16x64_bf16 v[16:19], v[32:35], v[96:103], v6\n\t"
    "ds_read_b128 v[48:51], v5 offset:6144\n\t"
    "v_smfmac_f32_16x16x64_bf16 v[20:23], v[32:35], v[104:111], v6\n\t"
    "ds_read_b128 v[52:55], v5 offset:7168\n\t"
    "s_waitcnt lgkmcnt(4)\n\t"
    "v_smfmac_f32_16x16x64_bf16 v[16:19], v[36:39], v[112:119], v6\n\t"
    "ds_read_b128 v[24:27], v5 offset:8192\n\t"
    "v_smfmac_f32_16x16x64_bf16 v[20:23], v[36:39], v[120:127], v6\n\t"
    "ds_read_b128 v[28:31], v5 offset:9216\n\t"
    "s_waitcnt lgkmcnt(5)\n\t"
    "v_smfmac_f32_16x16x64_bf16 v[16:19], v[40:43], v[128:135], v6\n\t"
    "ds_read_b128 v[56:59], v5 offset:10240\n\t"
    "v_smfmac_f32_16x16x64_bf16 v[20:23], v[40:43], v[136:143], v6\n\t"
    "ds_read_b128 v[60:63], v5 offset:11264\n\t"
    "ds_read_b128 v[36:39], v11 offset:36864\n\t"
    "s_waitcnt lgkmcnt(7)\n\t"
    "v_smfmac_f32_16x16x64_bf16 v[16:19], v[44:47], v[144:151], v6\n\t"
    "ds_read_b128 v[40:43], v12 offset:32768\n\t"
    "v_smfmac_f32_16x16x64_bf16 v[20:23], v[44:47], v[152:159], v6\n\t"
    "ds_read_b128 v[44:47], v12 offset:36864\n\t"
    "s_waitcnt lgkmcnt(8)\n\t"
    "v_smfmac_f32_16x16x64_bf16 v[16:19], v[48:51], v[160:167], v6\n\t"
    "v_smfmac_f32_16x16x64_bf16 v[20:23], v[48:51], v[168:175], v6\n\t"
    "ds_read_b128 v[48:51], v13 offset:32768\n\t"
    "s_waitcnt lgkmcnt(8)\n\t"
    "v_smfmac_f32_16x16x64_bf16 v[16:19], v[52:55], v[176:183], v6\n\t"
    "v_smfmac_f32_16x16x64_bf16 v[20:23], v[52:55], v[184:191], v6\n\t"
    "ds_read_b128 v[52:55], v13 offset:36864\n\t"
    "s_waitcnt lgkmcnt(8)\n\t"
    "v_smfmac_f32_16x16x64_bf16 v[16:19], v[24:27], v[192:199], v6\n\t"
    "ds_read_b32 v8, v15 offset:28672\n\t"
    "ds_read_b128 v[32:35], v5 offset:24576\n\t"
    "s_setprio 3\n\t"
    "v_smfmac_f32_16x16x64_bf16 v[20:23], v[24:27], v[200:207], v6\n\t"
    "ds_read_b128 v[24:27], v10 offset:32768\n\t"
    "s_waitcnt lgkmcnt(10)\n\t"
    "v_smfmac_f32_16x16x64_bf16 v[16:19], v[28:31], v[208:215], v6\n\t"
    "s_waitcnt lgkmcnt(9)\n\t"
    "v_smfmac_f32_16x16x64_bf16 v[16:19], v[56:59], v[224:231], v6\n\t"
    "s_waitcnt lgkmcnt(8)\n\t"
    "v_smfmac_f32_16x16x64_bf16 v[16:19], v[60:63], v[240:247], v6\n\t"
    "v_smfmac_f32_16x16x64_bf16 v[20:23], v[28:31], v[216:223], v6\n\t"
    "s_waitcnt lgkmcnt(1)\n\t"
    "21:\n\t"
    "v_cmp_ne_u32 vcc, %[sn], v8\n\t"
    "s_cbranch_vccnz 22f\n\t"
    "v_mul_f32 v32, %[lsc], v32\n\t"
    "v_mul_f32 v33, %[lsc], v33\n\t"
    "v_smfmac_f32_16x16x64_bf16 v[20:23], v[56:59], v[232:239], v6\n\t"
    "v_mul_f32 v34, %[lsc], v34\n\t"
    "v_mul_f32 v35, %[lsc], v35\n\t"
    "v_add_f32 v16, v16, v18\n\t"
    "v_add_f32 v17, v17, v19\n\t"
    "v_smfmac_f32_16x16x64_bf16 v[20:23], v[60:63], v[248:255], v6\n\t"
    "v_fma_f32 v16, v16, %[lsc], v32\n\t"
    "v_fma_f32 v17, v17, %[lsc], v33\n\t"
    "v_cvt_pk_bf16_f32 v16, v16, v17\n\t"
    "ds_write_b32 v15, v16 offset:16384\n\t"
    "s_mov_b32 %[bg], %[big]\n\t"
    "s_nop 2\n\t"
    "v_add_f32 v20, v20, v22\n\t"
    "v_add_f32 v21, v21, v23\n\t"
    "v_fma_f32 v20, v20, %[lsc], v34\n\t"
    "v_fma_f32 v21, v21, %[lsc], v35\n\t"
    "v_cvt_pk_bf16_f32 v20, v20, v21\n\t"
    "ds_write_b32 v15, v20 offset:16640\n\t"
    "ds_read_b128 v[28:31], v10 offset:36864\n\t"
    "ds_read_b128 v[32:35], v11 offset:32768\n\t"
    "s_waitcnt lgkmcnt(2)\n\t"
    "s_barrier\n\t"
    "ds_read_b64_tr_b16 v[16:17], v7 offset:16384\n\t"
    "ds_read_b64_tr_b16 v[18:19], v7 offset:16448\n\t"
    "ds_read_b64_tr_b16 v[20:21], v7 offset:16896\n\t"
    "ds_read_b64_tr_b16 v[22:23], v7 offset:16960\n\t"
    "ds_read_b64_tr_b16 v[56:57], v7 offset:17408\n\t"
    "ds_read_b64_tr_b16 v[58:59], v7 offset:17472\n\t"
    "ds_read_b64_tr_b16 v[60:61], v7 offset:17920\n\t"
    "ds_read_b64_tr_b16 v[62:63], v7 offset:17984\n\t"
    "global_load_dwordx4 v[6:9], v5, %[vs] sc1\n\t"
    "global_load_dwordx4 v[10:13], v5, %[vs] offset:1024 sc1\n\t"
    "s_waitcnt lgkmcnt(6)\n\t"
    "v_mfma_f32_16x16x32_bf16 v[24:27], v[24:27], v[16:19], 0\n\t"
    "v_mfma_f32_16x16x32_bf16 v[28:31], v[28:31], v[16:19], 0\n\t"
    "s_waitcnt lgkmcnt(4)\n\t"
    "v_mfma_f32_16x16x32_bf16 v[24:27], v[32:35], v[20:23], v[24:27]\n\t"
    "v_mfma_f32_16x16x32_bf16 v[28:31], v[36:39], v[20:23], v[28:31]\n\t"
    "v_xor_b32 v39, 32, v14\n\t"
    "ds_read_b64_tr_b16 v[32:33], v14 offset:40960\n\t"
    "ds_read_b64_tr_b16 v[34:35], v14 offset:45056\n\t"
    "ds_read_b64_tr_b16 v[36:37], v39 offset:40960\n\t"
    "ds_read_b64_tr_b16 v[38:39], v39 offset:45056\n\t"
    "s_waitcnt lgkmcnt(6)\n\t"
    "v_mfma_f32_16x16x32_bf16 v[24:27], v[40:43], v[56:59], v[24:27]\n\t"
    "v_mfma_f32_16x16x32_bf16 v[28:31], v[44:47], v[56:59], v[28:31]\n\t"
    "s_waitcnt lgkmcnt(4)\n\t"
    "v_mfma_f32_16x16x32_bf16 v[24:27], v[48:51], v[60:63], v[24:27]\n\t"
    "v_mfma_f32_16x16x32_bf16 v[28:31], v[52:55], v[60:63], v[28:31]\n\t"
    "v_mov_b32 v52, %[one]\n\t"
    "v_mov_b32 v53, %[one]\n\t"
    "v_mov_b32 v54, %[one]\n\t"
    "v_mov_b32 v55, %[one]\n\t"
    "v_mov_b32 v45, 0\n\t"
    "v_mov_b32 v47, 0\n\t"
    "v_mov_b32 v48, %[one]\n\t"
    "v_max3_f32 v14, |v24|, |v25|, |v26|\n\t"
    "v_max3_f32 v40, |v27|, |v28|, |v29|\n\t"
    "v_max3_f32 v14, v14, |v30|, |v31|\n\t"
    "v_max_f32 v14, v14, v40\n\t"
    "v_cmp_nge_f32 vcc, %[big], v14\n\t"
    "v_mov_b32 v14, 0\n\t"
    "s_cbranch_vccnz 12f\n\t"
    "13:\n\t"
    "v_exp_f32 v24, v24\n\t"
    "v_exp_f32 v25, v25\n\t"
    "v_exp_f32 v26, v26\n\t"
    "v_exp_f32 v27, v27\n\t"
    "v_exp_f32 v28, v28\n\t"
    "v_exp_f32 v29, v29\n\t"
    "v_exp_f32 v30, v30\n\t"
    "v_exp_f32 v31, v31\n\t"
    "v_cvt_pk_bf16_f32 v40, v24, v25\n\t"
    "v_cvt_pk_bf16_f32 v41, v26, v27\n\t"
    "v_cvt_pk_bf16_f32 v42, v28, v29\n\t"
    "v_cvt_pk_bf16_f32 v43, v30, v31\n\t"
    "v_mov_b32 v49, 0\n\t"
    "v_mov_b32 v51, 0\n\t"
    "s_waitcnt lgkmcnt(0)\n\t"
    "v_mfma_f32_16x16x32_bf16 v[52:55], v[52:55], v[40:43], 0\n\t"
    "v_mfma_f32_16x16x32_bf16 v[24:27], v[32:35], v[40:43], 0\n\t"
    "v_mfma_f32_16x16x32_bf16 v[28:31], v[36:39], v[40:43], 0\n\t"
    "s_waitcnt vmcnt(1)\n\t"
    "5:\n\t"
    "v_mfma_f32_16x16x32_bf16 v[32:35], v[6:9], v[16:19], 0\n\t"
    "v_mfma_f32_16x16x32_bf16 v[36:39], v[6:9], v[20:23], 0\n\t"
    "s_waitcnt vmcnt(0)\n\t"
    "v_mfma_f32_16x16x32_bf16 v[32:35], v[10:13], v[56:59], v[32:35]\n\t"
    "v_mfma_f32_16x16x32_bf16 v[36:39], v[10:13], v[60:63], v[36:39]\n\t"
    "ds_read_b32 v44, v15 offset:18432\n\t"
    "ds_read_b32 v46, v15 offset:18688\n\t"
    "v_max3_u32 v40, v6, v7, v8\n\t"
    "v_max3_u32 v41, v9, v10, v11\n\t"
    "v_max3_u32 v40, v40, v12, v13\n\t"
    "v_max_u32 v40, v40, v41\n\t"
    "v_cmp_eq_u32 vcc, -1, v40\n\t"
    "s_cbranch_vccnz 4f\n\t"
    "6:\n\t"
    "s_nop 0\n\t"
    "v_add_f32 v32, v32, v38\n\t"
    "v_add_f32 v33, v33, v39\n\t"
    "v_exp_f32 v34, v32\n\t"
    "v_max_f32 v40, v32, v33\n\t"
    "v_exp_f32 v35, v33\n\t"
    "v_cmp_nge_f32 vcc, %[bg], v40\n\t"
    "s_cbranch_vccnz 10f\n\t"
    "11:\n\t"
    "v_cvt_pk_bf16_f32 v50, v34, v35\n\t"
    "17:\n\t"
    "s_waitcnt vmcnt(0) lgkmcnt(0)\n\t"
    "v_max_u32 v40, v44, v46\n\t"
    "v_cmp_eq_u32 vcc, -1, v40\n\t"
    "s_cbranch_vccnz 18f\n\t"
    "19:\n\t"
    "v_mfma_f32_16x16x16_bf16 v[52:55], v[48:49], v[50:51], v[52:55]\n\t"
    "v_mfma_f32_16x16x16_bf16 v[24:27], v[44:45], v[50:51], v[24:27]\n\t"
    "v_mfma_f32_16x16x16_bf16 v[28:31], v[46:47], v[50:51], v[28:31]\n\t"
    "s_nop 5\n\t"
    "v_rcp_f32 v42, v52\n\t"
    "s_nop 0\n\t"
    "v_pk_mul_f32 v[24:25], v[24:25], v[42:43] op_sel_hi:[1,0]\n\t"
    "v_pk_mul_f32 v[26:27], v[26:27], v[42:43] op_sel_hi:[1,0]\n\t"
    "v_pk_mul_f32 v[28:29], v[28:29], v[42:43] op_sel_hi:[1,0]\n\t"
    "v_pk_mul_f32 v[30:31], v[30:31], v[42:43] op_sel_hi:[1,0]\n\t"
    "v_cvt_pk_bf16_f32 v36, v24, v25\n\t"
    "v_cvt_pk_bf16_f32 v37, v26, v27\n\t"
    "v_cvt_pk_bf16_f32 v38, v28, v29\n\t"
    "v_cvt_pk_bf16_f32 v39, v30, v31\n\t"
    "v_min_u32 v36, -2, v36\n\t"
    "v_min_u32 v37, -2, v37\n\t"
    "v_min_u32 v38, -2, v38\n\t"
    "v_min_u32 v39, -2, v39\n\t"
    "global_store_dwordx4 v4, v[36:39], %[od] sc1\n\t"
    "ds_write_b128 v5, v[6:9] offset:20480\n\t"
    "s_setprio 1\n\t"
    "s_waitcnt lgkmcnt(0)\n\t"
    "s_barrier\n\t"
    "s_waitcnt lgkmcnt(0)\n\t"
    "s_branch 9f\n\t"
    "1:\n\t"
    "s_add_u32 %[sp], %[sp], 1\n\t"
    "s_cmp_ge_u32 %[sp], %[cap]\n\t"
    "s_cbranch_scc1 7f\n\t"
    "global_load_dwordx4 v[24:27], v5, %[ag] sc1\n\t"
    "global_load_dwordx4 v[28:31], v5, %[ag] offset:1024 sc1\n\t"
    "s_waitcnt lgkmcnt(0)\n\t"
    "s_memtime %[tc]\n\t"
    "s_nop 7\n\t"
    "s_branch 3b\n\t"
    "7:\n\t"
    "s_mov_b32 %[cap], 0\n\t"
    "s_branch 2b\n\t"
    "4:\n\t"
    "s_add_u32 %[sp], %[sp], 0x10000\n\t"
    "s_lshr_b32 %[st], %[sp], 16\n\t"
    "s_cmp_ge_u32 %[st], %[cap]\n\t"
    "s_cbranch_scc1 8f\n\t"
    "global_load_dwordx4 v[6:9], v5, %[vs] sc1\n\t"
    "global_load_dwordx4 v[10:13], v5, %[vs] offset:1024 sc1\n\t"
    "s_waitcnt vmcnt(0) lgkmcnt(0)\n\t"
    "s_branch 5b\n\t"
    "8:\n\t"
    "s_mov_b32 %[cap], 0\n\t"
    "s_branch 6b\n\t"
    "18:\n\t"
    "s_add_u32 %[sp], %[sp], 0x10000\n\t"
    "s_lshr_b32 %[st], %[sp], 16\n\t"
    "s_cmp_ge_u32 %[st], %[cap]\n\t"
    "s_cbranch_scc1 20f\n\t"
    "ds_read_b32 v44, v15 offset:18432\n\t"
    "ds_read_b32 v46, v15 offset:18688\n\t"
    "s_waitcnt vmcnt(0) lgkmcnt(0)\n\t"
    "s_branch 17b\n\t"
    "20:\n\t"
    "s_mov_b32 %[cap], 0\n\t"
    "s_branch 19b\n\t"
    "22:\n\t"
    "ds_read_b32 v8, v15 offset:28672\n\t"
    "ds_read_b128 v[32:35], v5 offset:24576\n\t"
    "s_waitcnt lgkmcnt(0)\n\t"
    "s_branch 21b\n\t"
    "10:\n\t"
    "v_max_f32 v40, v40, v14\n\t"
    "v_mov_b32 v42, v40\n\t"
    "s_nop 1\n\t"
    "v_permlane16_swap_b32 v40, v42\n\t"
    "v_max_f32 v40, v40, v42\n\t"
    "v_mov_b32 v42, v40\n\t"
    "s_nop 1\n\t"
    "v_permlane32_swap_b32 v40, v42\n\t"
    "v_max_f32 v40, v40, v42\n\t"
    "v_sub_f32 v34, v32, v40\n\t"
    "v_sub_f32 v35, v33, v40\n\t"
    "v_sub_f32 v40, v14, v40\n\t"
    "v_exp_f32 v40, v40\n\t"
    "v_exp_f32 v34, v34\n\t"
    "v_exp_f32 v35, v35\n\t"
    "v_mul_f32 v24, v24, v40\n\t"
    "v_mul_f32 v25, v25, v40\n\t"
    "v_mul_f32 v26, v26, v40\n\t"
    "v_mul_f32 v27, v27, v40\n\t"
    "v_mul_f32 v28, v28, v40\n\t"
    "v_mul_f32 v29, v29, v40\n\t"
    "v_mul_f32 v30, v30, v40\n\t"
    "v_mul_f32 v31, v31, v40\n\t"
    "v_mul_f32 v52, v52, v40\n\t"
    "v_mul_f32 v53, v53, v40\n\t"
    "v_mul_f32 v54, v54, v40\n\t"
    "v_mul_f32 v55, v55, v40\n\t"
    "s_branch 11b\n\t"
    "12:\n\t"
    "v_max3_f32 v14, v24, v25, v26\n\t"
    "v_max3_f32 v40, v27, v28, v29\n\t"
    "v_max3_f32 v14, v14, v30, v31\n\t"
    "v_max3_f32 v14, v14, v40, %[neg]\n\t"
    "v_mov_b32 v40, v14\n\t"
    "s_nop 1\n\t"
    "v_permlane16_swap_b32 v14, v40\n\t"
    "v_max_f32 v14, v14, v40\n\t"
    "v_mov_b32 v40, v14\n\t"
    "s_nop 1\n\t"
    "v_permlane32_swap_b32 v14, v40\n\t"
    "v_max_f32 v14, v14, v40\n\t"
    "v_sub_f32 v24, v24, v14\n\t"
    "v_sub_f32 v25, v25, v14\n\t"
    "v_sub_f32 v26, v26, v14\n\t"
    "v_sub_f32 v27, v27, v14\n\t"
    "v_sub_f32 v28, v28, v14\n\t"
    "v_sub_f32 v29, v29, v14\n\t"
    "v_sub_f32 v30, v30, v14\n\t"
    "v_sub_f32 v31, v31, v14\n\t"
    "s_mov_b32 %[bg], 0x7fc00000\n\t"
    "s_branch 13b\n\t"
    "9:"
    : [tc] "=&s"(tc), [sp] "=&s"(sp), [cap] "+s"(cap), [st] "=&s"(st), [bg] "=&s"(bg)
    : [ag] "s"(ag), [c44] "s"(0x4444u), [lsc] "s"(LSC), [sn] "s"(sn), [pb] "s"(pb), [vs] "s"(vs), [od] "s"(od), [neg] "s"(-1.0e38f), [one] "s"(0x3F803F80u), [big] "s"(24.0f), "{v[64:95]}"(W.w0), "{v[96:127]}"(W.w1), "{v[128:159]}"(W.w2), "{v[160:191]}"(W.w3), "{v[192:223]}"(W.w4), "{v[224:255]}"(W.w5)
    : "v4", "v5", "v6", "v7", "v8", "v9", "v10", "v11", "v12", "v13", "v14", "v15", "v16", "v17", "v18", "v19", "v20", "v21", "v22", "v23", "v24", "v25", "v26", "v27", "v28", "v29", "v30", "v31", "v32", "v33", "v34", "v35", "v36", "v37", "v38", "v39", "v40", "v41", "v42", "v43", "v44", "v45", "v46", "v47", "v48", "v49", "v50", "v51", "v52", "v53", "v54", "v55", "v56", "v57", "v58", "v59", "v60", "v61", "v62", "v63", "vcc", "scc", "memory");
}
template <> __device__ __forceinline__ void qk_step<1>(const QKW& W, const char* ag, const char* vs, const char* v2, const char* od, unsigned pb, unsigned sn, unsigned& cap, unsigned& sp,
    unsigned long long& tc, unsigned long long* ts, unsigned& d0, unsigned& d1) {
  unsigned st, bg;
  asm volatile("v_mbcnt_lo_u32_b32 v60, -1, 0\n\t"
    "v_mbcnt_hi_u32_b32 v60, -1, v60\n\t"
    "v_lshlrev_b32 v5, 4, v60\n\t"
    "s_mov_b32 %[sp], 0\n\t"
    "s_setprio 2\n\t"
    "global_load_dwordx4 v[24:27], v5, %[ag] sc1\n\t"
    "global_load_dwordx4 v[28:31], v5, %[ag] offset:1024 sc1\n\t"
    "s_memtime %[tc]\n\t"
    "v_lshrrev_b32 v32, 4, v60\n\t"
    "v_and_b32 v33, 15, v60\n\t"
    "v_lshrrev_b32 v34, 2, v33\n\t"
    "v_and_b32 v35, 3, v60\n\t"
    "v_and_b32 v61, 1, v60\n\t"
    "v_lshlrev_b32 v61, 3, v61\n\t"
    "v_lshl_or_b32 v7, v34, 4, v61\n\t"
    "v_lshl_or_b32 v7, v32, 7, v7\n\t"
    "v_xor_b32 v10, v32, v33\n\t"
    "v_lshlrev_b32 v10, 4, v10\n\t"
    "v_lshl_or_b32 v10, v33, 8, v10\n\t"
    "v_or_b32 v10, %[pb], v10\n\t"
    "v_xor_b32 v11, 0x40, v10\n\t"
    "v_xor_b32 v12, 0x80, v10\n\t"
    "v_xor_b32 v13, 0xc0, v10\n\t"
    "v_lshl_or_b32 v34, v32, 2, v34\n\t"
    "v_lshrrev_b32 v35, 1, v35\n\t"
    "v_or_b32 v35, 4, v35\n\t"
    "v_xor_b32 v35, v35, v34\n\t"
    "v_lshl_or_b32 v14, v35, 4, v61\n\t"
    "v_lshl_or_b32 v14, v34, 8, v14\n\t"
    "v_or_b32 v14, %[pb], v14\n\t"
    "v_and_b32 v61, 3, v60\n\t"
    "v_lshlrev_b32 v61, 6, v61\n\t"
    "v_lshl_or_b32 v4, v32, 4, v61\n\t"
    "v_and_b32 v61, 4, v60\n\t"
    "v_lshl_or_b32 v4, v61, 10, v4\n\t"
    "v_and_b32 v61, 8, v60\n\t"
    "v_lshl_or_b32 v4, v61, 14, v4\n\t"
    "v_or_b32 v4, 256, v4\n\t"
    "v_and_b32 v61, 2, v60\n\t"
    "v_mul_u32_u24 v61, 0x5555, v61\n\t"
    "v_or_b32 v6, %[c44], v61\n\t"
    "v_lshlrev_b32 v61, 2, v32\n\t"
    "v_lshl_or_b32 v15, v33, 4, v61\n\t"
    "3:\n\t"
    "v_mov_b32 v16, 0\n\t"
    "v_mov_b32 v17, 0\n\t"
    "v_mov_b32 v18, 0\n\t"
    "v_mov_b32 v19, 0\n\t"
    "v_mov_b32 v20, 0\n\t"
    "v_mov_b32 v21, 0\n\t"
    "v_mov_b32 v22, 0\n\t"
    "v_mov_b32 v23, 0\n\t"
    "s_waitcnt vmcnt(0) lgkmcnt(0)\n\t"
    "ds_write_b128 v5, v[24:27] offset:2048\n\t"
    "ds_write_b128 v5, v[28:31] offset:3072\n\t"
    "v_smfmac_f32_16x16x64_bf16 v[16:19], v[24:27], v[96:103], v6\n\t"
    "v_max3_u32 v60, v24, v25, v26\n\t"
    "v_max3_u32 v61, v27, v28, v29\n\t"
    "v_smfmac_f32_16x16x64_bf16 v[20:23], v[24:27], v[104:111], v6\n\t"
    "v_max3_u32 v60, v60, v30, v31\n\t"
    "v_max_u32 v60, v60, v61\n\t"
    "v_cmp_eq_u32 vcc, -1, v60\n\t"
    "s_cbranch_vccnz 1f\n\t"
    "2:\n\t"
    "s_waitcnt lgkmcnt(0)\n\t"
    "s_barrier\n\t"
    "ds_read_b128 v[32:35], v5 offset:0\n\t"
    "ds_read_b128 v[36:39], v5 offset:1024\n\t"
    "v_smfmac_f32_16x16x64_bf16 v[16:19], v[28:31], v[112:119], v6\n\t"
    "ds_read_b128 v[40:43], v5 offset:4096\n\t"
    "ds_read_b128 v[44:47], v5 offset:5120\n\t"
    "v_smfmac_f32_16x16x64_bf16 v[20:23], v[28:31], v[120:127], v6\n\t"
    "s_waitcnt lgkmcnt(3)\n\t"
    "v_smfmac_f32_16x16x64_bf16 v[16:19], v[32:35], v[64:71], v6\n\t"
    "ds_read_b128 v[48:51], v5 offset:6144\n\t"
    "v_smfmac_f32_16x16x64_bf16 v[20:23], v[32:35], v[72:79], v6\n\t"
    "ds_read_b128 v[52:55], v5 offset:7168\n\t"
    "s_waitcnt lgkmcnt(4)\n\t"
    "v_smfmac_f32_16x16x64_bf16 v[16:19], v[36:39], v[80:87], v6\n\t"
    "ds_read_b128 v[24:27], v5 offset:8192\n\t"
    "v_smfmac_f32_16x16x64_bf16 v[20:23], v[36:39], v[88:95], v6\n\t"
    "ds_read_b128 v[28:31], v5 offset:9216\n\t"
    "s_waitcnt lgkmcnt(5)\n\t"
    "v_smfmac_f32_16x16x64_bf16 v[16:19], v[40:43], v[128:135], v6\n\t"
    "ds_read_b128 v[56:59], v5 offset:10240\n\t"
    "v_smfmac_f32_16x16x64_bf16 v[20:23], v[40:43], v[136:143], v6\n\t"
    "ds_read_b128 v[60:63], v5 offset:11264\n\t"
    "ds_read_b128 v[36:39], v11 offset:36864\n\t"
    "s_waitcnt lgkmcnt(7)\n\t"
    "v_smfmac_f32_16x16x64_bf16 v[16:19], v[44:47], v[144:151], v6\n\t"
    "ds_read_b128 v[40:43], v12 offset:32768\n\t"
    "v_smfmac_f32_16x16x64_bf16 v[20:23], v[44:47], v[152:159], v6\n\t"
    "ds_read_b128 v[44:47], v12 offset:36864\n\t"
    "s_waitcnt lgkmcnt(8)\n\t"
    "v_smfmac_f32_16x16x64_bf16 v[16:19], v[48:51], v[160:167], v6\n\t"
    "v_smfmac_f32_16x16x64_bf16 v[20:23], v[48:51], v[168:175], v6\n\t"
    "ds_read_b128 v[48:51], v13 offset:32768\n\t"
    "s_waitcnt lgkmcnt(8)\n\t"
    "v_smfmac_f32_16x16x64_bf16 v[16:19], v[52:55], v[176:183], v6\n\t"
    "v_smfmac_f32_16x16x64_bf16 v[20:23], v[52:55], v[184:191], v6\n\t"
    "ds_read_b128 v[52:55], v13 offset:36864\n\t"
    "s_waitcnt lgkmcnt(8)\n\t"
    "v_smfmac_f32_16x16x64_bf16 v[16:19], v[24:27], v[192:199], v6\n\t"
    "ds_read_b32 v8, v15 offset:28928\n\t"
    "ds_read_b128 v[32:35], v5 offset:25600\n\t"
    "s_setprio 3\n\t"
    "v_smfmac_f32_16x16x64_bf16 v[20:23], v[24:27], v[200:207], v6\n\t"
    "ds_read_b128 v[24:27], v10 offset:32768\n\t"
    "s_waitcnt lgkmcnt(10)\n\t"
    "v_smfmac_f32_16x16x64_bf16 v[16:19], v[28:31], v[208:215], v6\n\t"
    "s_waitcnt lgkmcnt(9)\n\t"
    "v_smfmac_f32_16x16x64_bf16 v[16:19], v[56:59], v[224:231], v6\n\t"
    "s_waitcnt lgkmcnt(8)\n\t"
    "v_smfmac_f32_16x16x64_bf16 v[16:19], v[60:63], v[240:247], v6\n\t"
    "v_smfmac_f32_16x16x64_bf16 v[20:23], v[28:31], v[216:223], v6\n\t"
    "s_waitcnt lgkmcnt(1)\n\t"
    "21:\n\t"
    "v_cmp_ne_u32 vcc, %[sn], v8\n\t"
    "s_cbranch_vccnz 22f\n\t"
    "v_mul_f32 v32, %[lsc], v32\n\t"
    "v_mul_f32 v33, %[lsc], v33\n\t"
    "v_smfmac_f32_16x16x64_bf16 v[20:23], v[56:59], v[232:239], v6\n\t"
    "v_mul_f32 v34, %[lsc], v34\n\t"
    "v_mul_f32 v35, %[lsc], v35\n\t"
    "v_add_f32 v16, v16, v18\n\t"
    "v_add_f32 v17, v17, v19\n\t"
    "v_smfmac_f32_16x16x64_bf16 v[20:23], v[60:63], v[248:255], v6\n\t"
    "v_fma_f32 v16, v16, %[lsc], v32\n\t"
    "v_fma_f32 v17, v17, %[lsc], v33\n\t"
    "v_cvt_pk_bf16_f32 v16, v16, v17\n\t"
    "ds_write_b32 v15, v16 offset:16896\n\t"
    "s_mov_b32 %[bg], %[big]\n\t"
    "s_nop 2\n\t"
    "v_add_f32 v20, v20, v22\n\t"
    "v_add_f32 v21, v21, v23\n\t"
    "v_fma_f32 v20, v20, %[lsc], v34\n\t"
    "v_fma_f32 v21, v21, %[lsc], v35\n\t"
    "v_cvt_pk_bf16_f32 v20, v20, v21\n\t"
    "ds_write_b32 v15, v20 offset:17152\n\t"
    "ds_read_b128 v[28:31], v10 offset:36864\n\t"
    "ds_read_b128 v[32:35], v11 offset:32768\n\t"
    "s_waitcnt lgkmcnt(2)\n\t"
    "s_barrier\n\t"
    "ds_read_b64_tr_b16 v[16:17], v7 offset:16384\n\t"
    "ds_read_b64_tr_b16 v[18:19], v7 offset:16448\n\t"
    "ds_read_b64_tr_b16 v[20:21], v7 offset:16896\n\t"
    "ds_read_b64_tr_b16 v[22:23], v7 offset:16960\n\t"
    "ds_read_b64_tr_b16 v[56:57], v7 offset:17408\n\t"
    "ds_read_b64_tr_b16 v[58:59], v7 offset:17472\n\t"
    "ds_read_b64_tr_b16 v[60:61], v7 offset:17920\n\t"
    "ds_read_b64_tr_b16 v[62:63], v7 offset:17984\n\t"
    "global_load_dwordx4 v[6:9], v5, %[vs] sc1\n\t"
    "global_load_dwordx4 v[10:13], v5, %[vs] offset:1024 sc1\n\t"
    "s_waitcnt lgkmcnt(6)\n\t"
    "v_mfma_f32_16x16x32_bf16 v[24:27], v[24:27], v[16:19], 0\n\t"
    "v_mfma_f32_16x16x32_bf16 v[28:31], v[28:31], v[16:19], 0\n\t"
    "s_waitcnt lgkmcnt(4)\n\t"
    "v_mfma_f32_16x16x32_bf16 v[24:27], v[32:35], v[20:23], v[24:27]\n\t"
    "v_mfma_f32_16x16x32_bf16 v[28:31], v[36:39], v[20:23], v[28:31]\n\t"
    "v_xor_b32 v39, 32, v14\n\t"
    "ds_read_b64_tr_b16 v[32:33], v14 offset:40960\n\t"
    "ds_read_b64_tr_b16 v[34:35], v14 offset:45056\n\t"
    "ds_read_b64_tr_b16 v[36:37], v39 offset:40960\n\t"
    "ds_read_b64_tr_b16 v[38:39], v39 offset:45056\n\t"
    "s_waitcnt lgkmcnt(6)\n\t"
    "v_mfma_f32_16x16x32_bf16 v[24:27], v[40:43], v[56:59], v[24:27]\n\t"
    "v_mfma_f32_16x16x32_bf16 v[28:31], v[44:47], v[56:59], v[28:31]\n\t"
    "s_waitcnt lgkmcnt(4)\n\t"
    "v_mfma_f32_16x16x32_bf16 v[24:27], v[48:51], v[60:63], v[24:27]\n\t"
    "v_mfma_f32_16x16x32_bf16 v[28:31], v[52:55], v[60:63], v[28:31]\n\t"
    "v_mov_b32 v52, %[one]\n\t"
    "v_mov_b32 v53, %[one]\n\t"
    "v_mov_b32 v54, %[one]\n\t"
    "v_mov_b32 v55, %[one]\n\t"
    "v_mov_b32 v45, 0\n\t"
    "v_mov_b32 v47, 0\n\t"
    "v_mov_b32 v48, %[one]\n\t"
    "v_max3_f32 v14, |v24|, |v25|, |v26|\n\t"
    "v_max3_f32 v40, |v27|, |v28|, |v29|\n\t"
    "v_max3_f32 v14, v14, |v30|, |v31|\n\t"
    "v_max_f32 v14, v14, v40\n\t"
    "v_cmp_nge_f32 vcc, %[big], v14\n\t"
    "v_mov_b32 v14, 0\n\t"
    "s_cbranch_vccnz 12f\n\t"
    "13:\n\t"
    "v_exp_f32 v24, v24\n\t"
    "v_exp_f32 v25, v25\n\t"
    "v_exp_f32 v26, v26\n\t"
    "v_exp_f32 v27, v27\n\t"
    "v_exp_f32 v28, v28\n\t"
    "v_exp_f32 v29, v29\n\t"
    "v_exp_f32 v30, v30\n\t"
    "v_exp_f32 v31, v31\n\t"
    "v_cvt_pk_bf16_f32 v40, v24, v25\n\t"
    "v_cvt_pk_bf16_f32 v41, v26, v27\n\t"
    "v_cvt_pk_bf16_f32 v42, v28, v29\n\t"
    "v_cvt_pk_bf16_f32 v43, v30, v31\n\t"
    "v_mov_b32 v49, 0\n\t"
    "v_mov_b32 v51, 0\n\t"
    "s_waitcnt lgkmcnt(0)\n\t"
    "v_mfma_f32_16x16x32_bf16 v[52:55], v[52:55], v[40:43], 0\n\t"
    "v_mfma_f32_16x16x32_bf16 v[24:27], v[32:35], v[40:43], 0\n\t"
    "v_mfma_f32_16x16x32_bf16 v[28:31], v[36:39], v[40:43], 0\n\t"
    "s_waitcnt vmcnt(1)\n\t"
    "5:\n\t"
    "v_mfma_f32_16x16x32_bf16 v[32:35], v[6:9], v[16:19], 0\n\t"
    "v_mfma_f32_16x16x32_bf16 v[36:39], v[6:9], v[20:23], 0\n\t"
    "s_waitcnt vmcnt(0)\n\t"
    "v_mfma_f32_16x16x32_bf16 v[32:35], v[10:13], v[56:59], v[32:35]\n\t"
    "v_mfma_f32_16x16x32_bf16 v[36:39], v[10:13], v[60:63], v[36:39]\n\t"
    "ds_read_b32 v44, v15 offset:18944\n\t"
    "ds_read_b32 v46, v15 offset:19200\n\t"
    "v_max3_u32 v40, v6, v7, v8\n\t"
    "v_max3_u32 v41, v9, v10, v11\n\t"
    "v_max3_u32 v40, v40, v12, v13\n\t"
    "v_max_u32 v40, v40, v41\n\t"
    "v_cmp_eq_u32 vcc, -1, v40\n\t"
    "s_cbranch_vccnz 4f\n\t"
    "6:\n\t"
    "s_nop 0\n\t"
    "v_add_f32 v32, v32, v38\n\t"
    "v_add_f32 v33, v33, v39\n\t"
    "v_exp_f32 v34, v32\n\t"
    "v_max_f32 v40, v32, v33\n\t"
    "v_exp_f32 v35, v33\n\t"
    "v_cmp_nge_f32 vcc, %[bg], v40\n\t"
    "s_cbranch_vccnz 10f\n\t"
    "11:\n\t"
    "v_cvt_pk_bf16_f32 v50, v34, v35\n\t"
    "17:\n\t"
    "s_waitcnt vmcnt(0) lgkmcnt(0)\n\t"
    "v_max_u32 v40, v44, v46\n\t"
    "v_cmp_eq_u32 vcc, -1, v40\n\t"
    "s_cbranch_vccnz 18f\n\t"
    "19:\n\t"
    "v_mfma_f32_16x16x16_bf16 v[52:55], v[48:49], v[50:51], v[52:55]\n\t"
    "v_mfma_f32_16x16x16_bf16 v[24:27], v[44:45], v[50:51], v[24:27]\n\t"
    "v_mfma_f32_16x16x16_bf16 v[28:31], v[46:47], v[50:51], v[28:31]\n\t"
    "s_nop 5\n\t"
    "v_rcp_f32 v42, v52\n\t"
    "s_nop 0\n\t"
    "v_pk_mul_f32 v[24:25], v[24:25], v[42:43] op_sel_hi:[1,0]\n\t"
    "v_pk_mul_f32 v[26:27], v[26:27], v[42:43] op_sel_hi:[1,0]\n\t"
    "v_pk_mul_f32 v[28:29], v[28:29], v[42:43] op_sel_hi:[1,0]\n\t"
    "v_pk_mul_f32 v[30:31], v[30:31], v[42:43] op_sel_hi:[1,0]\n\t"
    "v_cvt_pk_bf16_f32 v36, v24, v25\n\t"
    "v_cvt_pk_bf16_f32 v37, v26, v27\n\t"
    "v_cvt_pk_bf16_f32 v38, v28, v29\n\t"
    "v_cvt_pk_bf16_f32 v39, v30, v31\n\t"
    "v_min_u32 v36, -2, v36\n\t"
    "v_min_u32 v37, -2, v37\n\t"
    "v_min_u32 v38, -2, v38\n\t"
    "v_min_u32 v39, -2, v39\n\t"
    "global_store_dwordx4 v4, v[36:39], %[od] sc1\n\t"
    "ds_write_b128 v5, v[10:13] offset:21504\n\t"
    "s_setprio 1\n\t"
    "s_waitcnt lgkmcnt(0)\n\t"
    "s_barrier\n\t"
    "s_waitcnt lgkmcnt(0)\n\t"
    "s_branch 9f\n\t"
    "1:\n\t"
    "s_add_u32 %[sp], %[sp], 1\n\t"
    "s_cmp_ge_u32 %[sp], %[cap]\n\t"
    "s_cbranch_scc1 7f\n\t"
    "global_load_dwordx4 v[24:27], v5, %[ag] sc1\n\t"
    "global_load_dwordx4 v[28:31], v5, %[ag] offset:1024 sc1\n\t"
    "s_waitcnt lgkmcnt(0)\n\t"
    "s_memtime %[tc]\n\t"
    "s_nop 7\n\t"
    "s_branch 3b\n\t"
    "7:\n\t"
    "s_mov_b32 %[cap], 0\n\t"
    "s_branch 2b\n\t"
    "4:\n\t"
    "s_add_u32 %[sp], %[sp], 0x10000\n\t"
    "s_lshr_b32 %[st], %[sp], 16\n\t"
    "s_cmp_ge_u32 %[st], %[cap]\n\t"
    "s_cbranch_scc1 8f\n\t"
    "global_load_dwordx4 v[6:9], v5, %[vs] sc1\n\t"
    "global_load_dwordx4 v[10:13], v5, %[vs] offset:1024 sc1\n\t"
    "s_waitcnt vmcnt(0) lgkmcnt(0)\n\t"
    "s_branch 5b\n\t"
    "8:\n\t"
    "s_mov_b32 %[cap], 0\n\t"
    "s_branch 6b\n\t"
    "18:\n\t"
    "s_add_u32 %[sp], %[sp], 0x10000\n\t"
    "s_lshr_b32 %[st], %[sp], 16\n\t"
    "s_cmp_ge_u32 %[st], %[cap]\n\t"
    "s_cbranch_scc1 20f\n\t"
    "ds_read_b32 v44, v15 offset:18944\n\t"
    "ds_read_b32 v46, v15 offset:19200\n\t"
    "s_waitcnt vmcnt(0) lgkmcnt(0)\n\t"
    "s_branch 17b\n\t"
    "20:\n\t"
    "s_mov_b32 %[cap], 0\n\t"
    "s_branch 19b\n\t"
    "22:\n\t"
    "ds_read_b32 v8, v15 offset:28928\n\t"
    "ds_read_b128 v[32:35], v5 offset:25600\n\t"
    "s_waitcnt lgkmcnt(0)\n\t"
    "s_branch 21b\n\t"
    "10:\n\t"
    "v_max_f32 v40, v40, v14\n\t"
    "v_mov_b32 v42, v40\n\t"
    "s_nop 1\n\t"
    "v_permlane16_swap_b32 v40, v42\n\t"
    "v_max_f32 v40, v40, v42\n\t"
    "v_mov_b32 v42, v40\n\t"
    "s_nop 1\n\t"
    "v_permlane32_swap_b32 v40, v42\n\t"
    "v_max_f32 v40, v40, v42\n\t"
    "v_sub_f32 v34, v32, v40\n\t"
    "v_sub_f32 v35, v33, v40\n\t"
    "v_sub_f32 v40, v14, v40\n\t"
    "v_exp_f32 v40, v40\n\t"
    "v_exp_f32 v34, v34\n\t"
    "v_exp_f32 v35, v35\n\t"
    "v_mul_f32 v24, v24, v40\n\t"
    "v_mul_f32 v25, v25, v40\n\t"
    "v_mul_f32 v26, v26, v40\n\t"
    "v_mul_f32 v27, v27, v40\n\t"
    "v_mul_f32 v28, v28, v40\n\t"
    "v_mul_f32 v29, v29, v40\n\t"
    "v_mul_f32 v30, v30, v40\n\t"
    "v_mul_f32 v31, v31, v40\n\t"
    "v_mul_f32 v52, v52, v40\n\t"
    "v_mul_f32 v53, v53, v40\n\t"
    "v_mul_f32 v54, v54, v40\n\t"
    "v_mul_f32 v55, v55, v40\n\t"
    "s_branch 11b\n\t"
    "12:\n\t"
    "v_max3_f32 v14, v24, v25, v26\n\t"
    "v_max3_f32 v40, v27, v28, v29\n\t"
    "v_max3_f32 v14, v14, v30, v31\n\t"
    "v_max3_f32 v14, v14, v40, %[neg]\n\t"
    "v_mov_b32 v40, v14\n\t"
    "s_nop 1\n\t"
    "v_permlane16_swap_b32 v14, v40\n\t"
    "v_max_f32 v14, v14, v40\n\t"
    "v_mov_b32 v40, v14\n\t"
    "s_nop 1\n\t"
    "v_permlane32_swap_b32 v14, v40\n\t"
    "v_max_f32 v14, v14, v40\n\t"
    "v_sub_f32 v24, v24, v14\n\t"
    "v_sub_f32 v25, v25, v14\n\t"
    "v_sub_f32 v26, v26, v14\n\t"
    "v_sub_f32 v27, v27, v14\n\t"
    "v_sub_f32 v28, v28, v14\n\t"
    "v_sub_f32 v29, v29, v14\n\t"
    "v_sub_f32 v30, v30, v14\n\t"
    "v_sub_f32 v31, v31, v14\n\t"
    "s_mov_b32 %[bg], 0x7fc00000\n\t"
    "s_branch 13b\n\t"
    "9:"
    : [tc] "=&s"(tc), [sp] "=&s"(sp), [cap] "+s"(cap), [st] "=&s"(st), [bg] "=&s"(bg)
    : [ag] "s"(ag), [c44] "s"(0x4444u), [lsc] "s"(LSC), [sn] "s"(sn), [pb] "s"(pb), [vs] "s"(vs), [od] "s"(od), [neg] "s"(-1.0e38f), [one] "s"(0x3F803F80u), [big] "s"(24.0f), "{v[64:95]}"(W.w0), "{v[96:127]}"(W.w1), "{v[128:159]}"(W.w2), "{v[160:191]}"(W.w3), "{v[192:223]}"(W.w4), "{v[224:255]}"(W.w5)
    : "v4", "v5", "v6", "v7", "v8", "v9", "v10", "v11", "v12", "v13", "v14", "v15", "v16", "v17", "v18", "v19", "v20", "v21", "v22", "v23", "v24", "v25", "v26", "v27", "v28", "v29", "v30", "v31", "v32", "v33", "v34", "v35", "v36", "v37", "v38", "v39", "v40", "v41", "v42", "v43", "v44", "v45", "v46", "v47", "v48", "v49", "v50", "v51", "v52", "v53", "v54", "v55", "v56", "v57", "v58", "v59", "v60", "v61", "v62", "v63", "vcc", "scc", "memory");
}
template <> __device__ __forceinline__ void qk_step<2>(const QKW& W, const char* ag, const char* vs, const char* v2, const char* od, unsigned pb, unsigned sn, unsigned& cap, unsigned& sp,
    unsigned long long& tc, unsigned long long* ts, unsigned& d0, unsigned& d1) {
  unsigned st, bg;
  asm volatile("v_mbcnt_lo_u32_b32 v60, -1, 0\n\t"
    "v_mbcnt_hi_u32_b32 v60, -1, v60\n\t"
    "v_lshlrev_b32 v5, 4, v60\n\t"
    "s_mov_b32 %[sp], 0\n\t"
    "s_setprio 2\n\t"
    "global_load_dwordx4 v[24:27], v5, %[ag] sc1\n\t"
    "global_load_dwordx4 v[28:31], v5, %[ag] offset:1024 sc1\n\t"
    "s_memtime %[tc]\n\t"
    "v_lshrrev_b32 v32, 4, v60\n\t"
    "v_and_b32 v33, 15, v60\n\t"
    "v_lshrrev_b32 v34, 2, v33\n\t"
    "v_and_b32 v35, 3, v60\n\t"
    "v_and_b32 v61, 1, v60\n\t"
    "v_lshlrev_b32 v61, 3, v61\n\t"
    "v_lshl_or_b32 v7, v34, 4, v61\n\t"
    "v_lshl_or_b32 v7, v32, 7, v7\n\t"
    "v_xor_b32 v10, v32, v33\n\t"
    "v_lshlrev_b32 v10, 4, v10\n\t"
    "v_lshl_or_b32 v10, v33, 8, v10\n\t"
    "v_or_b32 v10, %[pb], v10\n\t"
    "v_xor_b32 v11, 0x40, v10\n\t"
    "v_xor_b32 v12, 0x80, v10\n\t"
    "v_xor_b32 v13, 0xc0, v10\n\t"
    "v_lshl_or_b32 v34, v32, 2, v34\n\t"
    "v_lshrrev_b32 v35, 1, v35\n\t"
    "v_or_b32 v35, 8, v35\n\t"
    "v_xor_b32 v35, v35, v34\n\t"
    "v_lshl_or_b32 v14, v35, 4, v61\n\t"
    "v_lshl_or_b32 v14, v34, 8, v14\n\t"
    "v_or_b32 v14, %[pb], v14\n\t"
    "v_and_b32 v61, 3, v60\n\t"
    "v_lshlrev_b32 v61, 6, v61\n\t"
    "v_lshl_or_b32 v4, v32, 4, v61\n\t"
    "v_and_b32 v61, 4, v60\n\t"
    "v_lshl_or_b32 v4, v61, 10, v4\n\t"
    "v_and_b32 v61, 8, v60\n\t"
    "v_lshl_or_b32 v4, v61, 14, v4\n\t"
    "v_or_b32 v4, 512, v4\n\t"
    "v_and_b32 v61, 2, v60\n\t"
    "v_mul_u32_u24 v61, 0x5555, v61\n\t"
    "v_or_b32 v6, %[c44], v61\n\t"
    "v_lshlrev_b32 v61, 2, v32\n\t"
    "v_lshl_or_b32 v15, v33, 4, v61\n\t"
    "3:\n\t"
    "v_mov_b32 v16, 0\n\t"
    "v_mov_b32 v17, 0\n\t"
    "v_mov_b32 v18, 0\n\t"
    "v_mov_b32 v19, 0\n\t"
    "v_mov_b32 v20, 0\n\t"
    "v_mov_b32 v21, 0\n\t"
    "v_mov_b32 v22, 0\n\t"
    "v_mov_b32 v23, 0\n\t"
    "s_waitcnt vmcnt(0) lgkmcnt(0)\n\t"
    "ds_write_b128 v5, v[24:27] offset:4096\n\t"
    "ds_write_b128 v5, v[28:31] offset:5120\n\t"
    "v_smfmac_f32_16x16x64_bf16 v[16:19], v[24:27], v[128:135], v6\n\t"
    "v_max3_u32 v60, v24, v25, v26\n\t"
    "v_max3_u32 v61, v27, v28, v29\n\t"
    "v_smfmac_f32_16x16x64_bf16 v[20:23], v[24:27], v[136:143], v6\n\t"
    "v_max3_u32 v60, v60, v30, v31\n\t"
    "v_max_u32 v60, v60, v61\n\t"
    "v_cmp_eq_u32 vcc, -1, v60\n\t"
    "s_cbranch_vccnz 1f\n\t"
    "2:\n\t"
    "s_waitcnt lgkmcnt(0)\n\t"
    "s_barrier\n\t"
    "ds_read_b128 v[32:35], v5 offset:0\n\t"
    "ds_read_b128 v[36:39], v5 offset:1024\n\t"
    "v_smfmac_f32_16x16x64_bf16 v[16:19], v[28:31], v[144:151], v6\n\t"
    "ds_read_b128 v[40:43], v5 offset:2048\n\t"
    "ds_read_b128 v[44:47], v5 offset:3072\n\t"
    "v_smfmac_f32_16x16x64_bf16 v[20:23], v[28:31], v[152:159], v6\n\t"
    "s_waitcnt lgkmcnt(3)\n\t"
    "v_smfmac_f32_16x16x64_bf16 v[16:19], v[32:35], v[64:71], v6\n\t"
    "ds_read_b128 v[48:51], v5 offset:6144\n\t"
    "v_smfmac_f32_16x16x64_bf16 v[20:23], v[32:35], v[72:79], v6\n\t"
    "ds_read_b128 v[52:55], v5 offset:7168\n\t"
    "s_waitcnt lgkmcnt(4)\n\t"
    "v_smfmac_f32_16x16x64_bf16 v[16:19], v[36:39], v[80:87], v6\n\t"
    "ds_read_b128 v[24:27], v5 offset:8192\n\t"
    "v_smfmac_f32_16x16x64_bf16 v[20:23], v[36:39], v[88:95], v6\n\t"
    "ds_read_b128 v[28:31], v5 offset:9216\n\t"
    "s_waitcnt lgkmcnt(5)\n\t"
    "v_smfmac_f32_16x16x64_bf16 v[16:19], v[40:43], v[96:103], v6\n\t"
    "ds_read_b128 v[56:59], v5 offset:10240\n\t"
    "v_smfmac_f32_16x16x64_bf16 v[20:23], v[40:43], v[104:111], v6\n\t"
    "ds_read_b128 v[60:63], v5 offset:11264\n\t"
    "ds_read_b128 v[36:39], v11 offset:36864\n\t"
    "s_waitcnt lgkmcnt(7)\n\t"
    "v_smfmac_f32_16x16x64_bf16 v[16:19], v[44:47], v[112:119], v6\n\t"
    "ds_read_b128 v[40:43], v12 offset:32768\n\t"
    "v_smfmac_f32_16x16x64_bf16 v[20:23], v[44:47], v[120:127], v6\n\t"
    "ds_read_b128 v[44:47], v12 offset:36864\n\t"
    "s_waitcnt lgkmcnt(8)\n\t"
    "v_smfmac_f32_16x16x64_bf16 v[16:19], v[48:51], v[160:167], v6\n\t"
    "v_smfmac_f32_16x16x64_bf16 v[20:23], v[48:51], v[168:175], v6\n\t"
    "ds_read_b128 v[48:51], v13 offset:32768\n\t"
    "s_waitcnt lgkmcnt(8)\n\t"
    "v_smfmac_f32_16x16x64_bf16 v[16:19], v[52:55], v[176:183], v6\n\t"
    "v_smfmac_f32_16x16x64_bf16 v[20:23], v[52:55], v[184:191], v6\n\t"
    "ds_read_b128 v[52:55], v13 offset:36864\n\t"
    "s_waitcnt lgkmcnt(8)\n\t"
    "v_smfmac_f32_16x16x64_bf16 v[16:19], v[24:27], v[192:199], v6\n\t"
    "ds_read_b32 v8, v15 offset:29184\n\t"
    "ds_read_b128 v[32:35], v5 offset:26624\n\t"
    "s_setprio 3\n\t"
    "v_smfmac_f32_16x16x64_bf16 v[20:23], v[24:27], v[200:207], v6\n\t"
    "ds_read_b128 v[24:27], v10 offset:32768\n\t"
    "s_waitcnt lgkmcnt(10)\n\t"
    "v_smfmac_f32_16x16x64_bf16 v[16:19], v[28:31], v[208:215], v6\n\t"
    "s_waitcnt lgkmcnt(9)\n\t"
    "v_smfmac_f32_16x16x64_bf16 v[16:19], v[56:59], v[224:231], v6\n\t"
    "s_waitcnt lgkmcnt(8)\n\t"
    "v_smfmac_f32_16x16x64_bf16 v[16:19], v[60:63], v[240:247], v6\n\t"
    "v_smfmac_f32_16x16x64_bf16 v[20:23], v[28:31], v[216:223], v6\n\t"
    "s_waitcnt lgkmcnt(1)\n\t"
    "21:\n\t"
    "v_cmp_ne_u32 vcc, %[sn], v8\n\t"
    "s_cbranch_vccnz 22f\n\t"
    "v_mul_f32 v32, %[lsc], v32\n\t"
    "v_mul_f32 v33, %[lsc], v33\n\t"
    "v_smfmac_f32_16x16x64_bf16 v[20:23], v[56:59], v[232:239], v6\n\t"
    "v_mul_f32 v34, %[lsc], v34\n\t"
    "v_mul_f32 v35, %[lsc], v35\n\t"
    "v_add_f32 v16, v16, v18\n\t"
    "v_add_f32 v17, v17, v19\n\t"
    "v_smfmac_f32_16x16x64_bf16 v[20:23], v[60:63], v[248:255], v6\n\t"
    "v_fma_f32 v16, v16, %[lsc], v32\n\t"
    "v_fma_f32 v17, v17, %[lsc], v33\n\t"
    "v_cvt_pk_bf16_f32 v16, v16, v17\n\t"
    "ds_write_b32 v15, v16 offset:17408\n\t"
    "s_mov_b32 %[bg], %[big]\n\t"
    "s_nop 2\n\t"
    "v_add_f32 v20, v20, v22\n\t"
    "v_add_f32 v21, v21, v23\n\t"
    "v_fma_f32 v20, v20, %[lsc], v34\n\t"
    "v_fma_f32 v21, v21, %[lsc], v35\n\t"
    "v_cvt_pk_bf16_f32 v20, v20, v21\n\t"
    "ds_write_b32 v15, v20 offset:17664\n\t"
    "ds_read_b128 v[28:31], v10 offset:36864\n\t"
    "ds_read_b128 v[32:35], v11 offset:32768\n\t"
    "s_waitcnt lgkmcnt(2)\n\t"
    "s_barrier\n\t"
    "ds_read_b64_tr_b16 v[16:17], v7 offset:16384\n\t"
    "ds_read_b64_tr_b16 v[18:19], v7 offset:16448\n\t"
    "ds_read_b64_tr_b16 v[20:21], v7 offset:16896\n\t"
    "ds_read_b64_tr_b16 v[22:23], v7 offset:16960\n\t"
    "ds_read_b64_tr_b16 v[56:57], v7 offset:17408\n\t"
    "ds_read_b64_tr_b16 v[58:59], v7 offset:17472\n\t"
    "ds_read_b64_tr_b16 v[60:61], v7 offset:17920\n\t"
    "ds_read_b64_tr_b16 v[62:63], v7 offset:17984\n\t"
    "global_load_dwordx4 v[6:9], v5, %[vs] sc1\n\t"
    "global_load_dwordx4 v[10:13], v5, %[vs] offset:1024 sc1\n\t"
    "s_waitcnt lgkmcnt(6)\n\t"
    "v_mfma_f32_16x16x32_bf16 v[24:27], v[24:27], v[16:19], 0\n\t"
    "v_mfma_f32_16x16x32_bf16 v[28:31], v[28:31], v[16:19], 0\n\t"
    "s_waitcnt lgkmcnt(4)\n\t"
    "v_mfma_f32_16x16x32_bf16 v[24:27], v[32:35], v[20:23], v[24:27]\n\t"
    "v_mfma_f32_16x16x32_bf16 v[28:31], v[36:39], v[20:23], v[28:31]\n\t"
    "v_xor_b32 v39, 32, v14\n\t"
    "ds_read_b64_tr_b16 v[32:33], v14 offset:40960\n\t"
    "ds_read_b64_tr_b16 v[34:35], v14 offset:45056\n\t"
    "ds_read_b64_tr_b16 v[36:37], v39 offset:40960\n\t"
    "ds_read_b64_tr_b16 v[38:39], v39 offset:45056\n\t"
    "s_waitcnt lgkmcnt(6)\n\t"
    "v_mfma_f32_16x16x32_bf16 v[24:27], v[40:43], v[56:59], v[24:27]\n\t"
    "v_mfma_f32_16x16x32_bf16 v[28:31], v[44:47], v[56:59], v[28:31]\n\t"
    "s_waitcnt lgkmcnt(4)\n\t"
    "v_mfma_f32_16x16x32_bf16 v[24:27], v[48:51], v[60:63], v[24:27]\n\t"
    "v_mfma_f32_16x16x32_bf16 v[28:31], v[52:55], v[60:63], v[28:31]\n\t"
    "v_mov_b32 v52, %[one]\n\t"
    "v_mov_b32 v53, %[one]\n\t"
    "v_mov_b32 v54, %[one]\n\t"
    "v_mov_b32 v55, %[one]\n\t"
    "v_mov_b32 v45, 0\n\t"
    "v_mov_b32 v47, 0\n\t"
    "v_mov_b32 v48, %[one]\n\t"
    "v_max3_f32 v14, |v24|, |v25|, |v26|\n\t"
    "v_max3_f32 v40, |v27|, |v28|, |v29|\n\t"
    "v_max3_f32 v14, v14, |v30|, |v31|\n\t"
    "v_max_f32 v14, v14, v40\n\t"
    "v_cmp_nge_f32 vcc, %[big], v14\n\t"
    "v_mov_b32 v14, 0\n\t"
    "s_cbranch_vccnz 12f\n\t"
    "13:\n\t"
    "v_lshrrev_b32 v46, 2, v5\n\t"
    "global_load_dword v44, v46, %[v2] sc1\n\t"
    "global_load_dword v46, v46, %[v2] offset:256 sc1\n\t"
    "v_exp_f32 v24, v24\n\t"
    "v_exp_f32 v25, v25\n\t"
    "v_exp_f32 v26, v26\n\t"
    "v_exp_f32 v27, v27\n\t"
    "v_exp_f32 v28, v28\n\t"
    "v_exp_f32 v29, v29\n\t"
    "v_exp_f32 v30, v30\n\t"
    "v_exp_f32 v31, v31\n\t"
    "v_cvt_pk_bf16_f32 v40, v24, v25\n\t"
    "v_cvt_pk_bf16_f32 v41, v26, v27\n\t"
    "v_cvt_pk_bf16_f32 v42, v28, v29\n\t"
    "v_cvt_pk_bf16_f32 v43, v30, v31\n\t"
    "v_mov_b32 v49, 0\n\t"
    "v_mov_b32 v51, 0\n\t"
    "s_waitcnt lgkmcnt(0)\n\t"
    "v_mfma_f32_16x16x32_bf16 v[52:55], v[52:55], v[40:43], 0\n\t"
    "v_mfma_f32_16x16x32_bf16 v[24:27], v[32:35], v[40:43], 0\n\t"
    "v_mfma_f32_16x16x32_bf16 v[28:31], v[36:39], v[40:43], 0\n\t"
    "s_waitcnt vmcnt(3)\n\t"
    "5:\n\t"
    "v_mfma_f32_16x16x32_bf16 v[32:35], v[6:9], v[16:19], 0\n\t"
    "v_mfma_f32_16x16x32_bf16 v[36:39], v[6:9], v[20:23], 0\n\t"
    "s_waitcnt vmcnt(2)\n\t"
    "v_mfma_f32_16x16x32_bf16 v[32:35], v[10:13], v[56:59], v[32:35]\n\t"
    "v_mfma_f32_16x16x32_bf16 v[36:39], v[10:13], v[60:63], v[36:39]\n\t"
    "v_max3_u32 v40, v6, v7, v8\n\t"
    "v_max3_u32 v41, v9, v10, v11\n\t"
    "v_max3_u32 v40, v40, v12, v13\n\t"
    "v_max_u32 v40, v40, v41\n\t"
    "v_cmp_eq_u32 vcc, -1, v40\n\t"
    "s_cbranch_vccnz 4f\n\t"
    "6:\n\t"
    "s_nop 0\n\t"
    "s_nop 0\n\t"
    "v_add_f32 v32, v32, v38\n\t"
    "v_add_f32 v33, v33, v39\n\t"
    "v_exp_f32 v34, v32\n\t"
    "v_max_f32 v40, v32, v33\n\t"
    "v_exp_f32 v35, v33\n\t"
    "v_cmp_nge_f32 vcc, %[bg], v40\n\t"
    "s_cbranch_vccnz 10f\n\t"
    "11:\n\t"
    "v_cvt_pk_bf16_f32 v50, v34, v35\n\t"
    "17:\n\t"
    "s_waitcnt vmcnt(0) lgkmcnt(0)\n\t"
    "v_max_u32 v40, v44, v46\n\t"
    "v_cmp_eq_u32 vcc, -1, v40\n\t"
    "s_cbranch_vccnz 18f\n\t"
    "19:\n\t"
    "v_mfma_f32_16x16x16_bf16 v[52:55], v[48:49], v[50:51], v[52:55]\n\t"
    "v_mfma_f32_16x16x16_bf16 v[24:27], v[44:45], v[50:51], v[24:27]\n\t"
    "v_mfma_f32_16x16x16_bf16 v[28:31], v[46:47], v[50:51], v[28:31]\n\t"
    "s_nop 5\n\t"
    "v_rcp_f32 v42, v52\n\t"
    "s_nop 0\n\t"
    "v_pk_mul_f32 v[24:25], v[24:25], v[42:43] op_sel_hi:[1,0]\n\t"
    "v_pk_mul_f32 v[26:27], v[26:27], v[42:43] op_sel_hi:[1,0]\n\t"
    "v_pk_mul_f32 v[28:29], v[28:29], v[42:43] op_sel_hi:[1,0]\n\t"
    "v_pk_mul_f32 v[30:31], v[30:31], v[42:43] op_sel_hi:[1,0]\n\t"
    "v_cvt_pk_bf16_f32 v36, v24, v25\n\t"
    "v_cvt_pk_bf16_f32 v37, v26, v27\n\t"
    "v_cvt_pk_bf16_f32 v38, v28, v29\n\t"
    "v_cvt_pk_bf16_f32 v39, v30, v31\n\t"
    "v_min_u32 v36, -2, v36\n\t"
    "v_min_u32 v37, -2, v37\n\t"
    "v_min_u32 v38, -2, v38\n\t"
    "v_min_u32 v39, -2, v39\n\t"
    "global_store_dwordx4 v4, v[36:39], %[od] sc1\n\t"
    "ds_write_b32 v15, v44 offset:19456\n\t"
    "ds_write_b32 v15, v46 offset:19712\n\t"
    "s_setprio 1\n\t"
    "s_waitcnt lgkmcnt(0)\n\t"
    "s_barrier\n\t"
    "s_waitcnt lgkmcnt(0)\n\t"
    "s_branch 9f\n\t"
    "1:\n\t"
    "s_add_u32 %[sp], %[sp], 1\n\t"
    "s_cmp_ge_u32 %[sp], %[cap]\n\t"
    "s_cbranch_scc1 7f\n\t"
    "global_load_dwordx4 v[24:27], v5, %[ag] sc1\n\t"
    "global_load_dwordx4 v[28:31], v5, %[ag] offset:1024 sc1\n\t"
    "s_waitcnt lgkmcnt(0)\n\t"
    "s_memtime %[tc]\n\t"
    "s_nop 7\n\t"
    "s_branch 3b\n\t"
    "7:\n\t"
    "s_mov_b32 %[cap], 0\n\t"
    "s_branch 2b\n\t"
    "4:\n\t"
    "s_add_u32 %[sp], %[sp], 0x10000\n\t"
    "s_lshr_b32 %[st], %[sp], 16\n\t"
    "s_cmp_ge_u32 %[st], %[cap]\n\t"
    "s_cbranch_scc1 8f\n\t"
    "global_load_dwordx4 v[6:9], v5, %[vs] sc1\n\t"
    "global_load_dwordx4 v[10:13], v5, %[vs] offset:1024 sc1\n\t"
    "s_waitcnt vmcnt(0) lgkmcnt(0)\n\t"
    "s_branch 5b\n\t"
    "8:\n\t"
    "s_mov_b32 %[cap], 0\n\t"
    "s_branch 6b\n\t"
    "18:\n\t"
    "s_add_u32 %[sp], %[sp], 0x10000\n\t"
    "s_lshr_b32 %[st], %[sp], 16\n\t"
    "s_cmp_ge_u32 %[st], %[cap]\n\t"
    "s_cbranch_scc1 20f\n\t"
    "v_lshrrev_b32 v46, 2, v5\n\t"
    "global_load_dword v44, v46, %[v2] sc1\n\t"
    "global_load_dword v46, v46, %[v2] offset:256 sc1\n\t"
    "s_waitcnt vmcnt(0) lgkmcnt(0)\n\t"
    "s_branch 17b\n\t"
    "20:\n\t"
    "s_mov_b32 %[cap], 0\n\t"
    "s_branch 19b\n\t"
    "22:\n\t"
    "ds_read_b32 v8, v15 offset:29184\n\t"
    "ds_read_b128 v[32:35], v5 offset:26624\n\t"
    "s_waitcnt lgkmcnt(0)\n\t"
    "s_branch 21b\n\t"
    "10:\n\t"
    "v_max_f32 v40, v40, v14\n\t"
    "v_mov_b32 v42, v40\n\t"
    "s_nop 1\n\t"
    "v_permlane16_swap_b32 v40, v42\n\t"
    "v_max_f32 v40, v40, v42\n\t"
    "v_mov_b32 v42, v40\n\t"
    "s_nop 1\n\t"
    "v_permlane32_swap_b32 v40, v42\n\t"
    "v_max_f32 v40, v40, v42\n\t"
    "v_sub_f32 v34, v32, v40\n\t"
    "v_sub_f32 v35, v33, v40\n\t"
    "v_sub_f32 v40, v14, v40\n\t"
    "v_exp_f32 v40, v40\n\t"
    "v_exp_f32 v34, v34\n\t"
    "v_exp_f32 v35, v35\n\t"
    "v_mul_f32 v24, v24, v40\n\t"
    "v_mul_f32 v25, v25, v40\n\t"
    "v_mul_f32 v26, v26, v40\n\t"
    "v_mul_f32 v27, v27, v40\n\t"
    "v_mul_f32 v28, v28, v40\n\t"
    "v_mul_f32 v29, v29, v40\n\t"
    "v_mul_f32 v30, v30, v40\n\t"
    "v_mul_f32 v31, v31, v40\n\t"
    "v_mul_f32 v52, v52, v40\n\t"
    "v_mul_f32 v53, v53, v40\n\t"
    "v_mul_f32 v54, v54, v40\n\t"
    "v_mul_f32 v55, v55, v40\n\t"
    "s_branch 11b\n\t"
    "12:\n\t"
    "v_max3_f32 v14, v24, v25, v26\n\t"
    "v_max3_f32 v40, v27, v28, v29\n\t"
    "v_max3_f32 v14, v14, v30, v31\n\t"
    "v_max3_f32 v14, v14, v40, %[neg]\n\t"
    "v_mov_b32 v40, v14\n\t"
    "s_nop 1\n\t"
    "v_permlane16_swap_b32 v14, v40\n\t"
    "v_max_f32 v14, v14, v40\n\t"
    "v_mov_b32 v40, v14\n\t"
    "s_nop 1\n\t"
    "v_permlane32_swap_b32 v14, v40\n\t"
    "v_max_f32 v14, v14, v40\n\t"
    "v_sub_f32 v24, v24, v14\n\t"
    "v_sub_f32 v25, v25, v14\n\t"
    "v_sub_f32 v26, v26, v14\n\t"
    "v_sub_f32 v27, v27, v14\n\t"
    "v_sub_f32 v28, v28, v14\n\t"
    "v_sub_f32 v29, v29, v14\n\t"
    "v_sub_f32 v30, v30, v14\n\t"
    "v_sub_f32 v31, v31, v14\n\t"
    "s_mov_b32 %[bg], 0x7fc00000\n\t"
    "s_branch 13b\n\t"
    "9:"
    : [tc] "=&s"(tc), [sp] "=&s"(sp), [cap] "+s"(cap), [st] "=&s"(st), [bg] "=&s"(bg)
    : [ag] "s"(ag), [c44] "s"(0x4444u), [lsc] "s"(LSC), [sn] "s"(sn), [pb] "s"(pb), [vs] "s"(vs), [od] "s"(od), [neg] "s"(-1.0e38f), [one] "s"(0x3F803F80u), [big] "s"(24.0f), [v2] "s"(v2), "{v[64:95]}"(W.w0), "{v[96:127]}"(W.w1), "{v[128:159]}"(W.w2), "{v[160:191]}"(W.w3), "{v[192:223]}"(W.w4), "{v[224:255]}"(W.w5)
    : "v4", "v5", "v6", "v7", "v8", "v9", "v10", "v11", "v12", "v13", "v14", "v15", "v16", "v17", "v18", "v19", "v20", "v21", "v22", "v23", "v24", "v25", "v26", "v27", "v28", "v29", "v30", "v31", "v32", "v33", "v34", "v35", "v36", "v37", "v38", "v39", "v40", "v41", "v42", "v43", "v44", "v45", "v46", "v47", "v48", "v49", "v50", "v51", "v52", "v53", "v54", "v55", "v56", "v57", "v58", "v59", "v60", "v61", "v62", "v63", "vcc", "scc", "memory");
}
template <> __device__ __forceinline__ void qk_step<3>(const QKW& W, const char* ag, const char* vs, const char* v2, const char* od, unsigned pb, unsigned sn, unsigned& cap, unsigned& sp,
    unsigned long long& tc, unsigned long long* ts, unsigned& d0, unsigned& d1) {
  unsigned st, bg;
  asm volatile("v_mbcnt_lo_u32_b32 v60, -1, 0\n\t"
    "v_mbcnt_hi_u32_b32 v60, -1, v60\n\t"
    "v_lshlrev_b32 v5, 4, v60\n\t"
    "s_mov_b32 %[sp], 0\n\t"
    "s_setprio 2\n\t"
    "global_load_dwordx4 v[24:27], v5, %[ag] sc1\n\t"
    "global_load_dwordx4 v[28:31], v5, %[ag] offset:1024 sc1\n\t"
    "s_memtime %[tc]\n\t"
    "v_lshrrev_b32 v32, 4, v60\n\t"
    "v_and_b32 v33, 15, v60\n\t"
    "v_lshrrev_b32 v34, 2, v33\n\t"
    "v_and_b32 v35, 3, v60\n\t"
    "v_and_b32 v61, 1, v60\n\t"
    "v_lshlrev_b32 v61, 3, v61\n\t"
    "v_lshl_or_b32 v7, v34, 4, v61\n\t"
    "v_lshl_or_b32 v7, v32, 7, v7\n\t"
    "v_xor_b32 v10, v32, v33\n\t"
    "v_lshlrev_b32 v10, 4, v10\n\t"
    "v_lshl_or_b32 v10, v33, 8, v10\n\t"
    "v_or_b32 v10, %[pb], v10\n\t"
    "v_xor_b32 v11, 0x40, v10\n\t"
    "v_xor_b32 v12, 0x80, v10\n\t"
    "v_xor_b32 v13, 0xc0, v10\n\t"
    "v_lshl_or_b32 v34, v32, 2, v34\n\t"
    "v_lshrrev_b32 v35, 1, v35\n\t"
    "v_or_b32 v35, 12, v35\n\t"
    "v_xor_b32 v35, v35, v34\n\t"
    "v_lshl_or_b32 v14, v35, 4, v61\n\t"
    "v_lshl_or_b32 v14, v34, 8, v14\n\t"
    "v_or_b32 v14, %[pb], v14\n\t"
    "v_and_b32 v61, 3, v60\n\t"
    "v_lshlrev_b32 v61, 6, v61\n\t"
    "v_lshl_or_b32 v4, v32, 4, v61\n\t"
    "v_and_b32 v61, 4, v60\n\t"
    "v_lshl_or_b32 v4, v61, 10, v4\n\t"
    "v_and_b32 v61, 8, v60\n\t"
    "v_lshl_or_b32 v4, v61, 14, v4\n\t"
    "v_or_b32 v4, 768, v4\n\t"
    "v_and_b32 v61, 2, v60\n\t"
    "v_mul_u32_u24 v61, 0x5555, v61\n\t"
    "v_or_b32 v6, %[c44], v61\n\t"
    "v_lshlrev_b32 v61, 2, v32\n\t"
    "v_lshl_or_b32 v15, v33, 4, v61\n\t"
    "3:\n\t"
    "v_mov_b32 v16, 0\n\t"
    "v_mov_b32 v17, 0\n\t"
    "v_mov_b32 v18, 0\n\t"
    "v_mov_b32 v19, 0\n\t"
    "v_mov_b32 v20, 0\n\t"
    "v_mov_b32 v21, 0\n\t"
    "v_mov_b32 v22, 0\n\t"
    "v_mov_b32 v23, 0\n\t"
    "s_waitcnt vmcnt(0) lgkmcnt(0)\n\t"
    "ds_write_b128 v5, v[24:27] offset:6144\n\t"
    "ds_write_b128 v5, v[28:31] offset:7168\n\t"
    "v_smfmac_f32_16x16x64_bf16 v[16:19], v[24:27], v[160:167], v6\n\t"
    "v_max3_u32 v60, v24, v25, v26\n\t"
    "v_max3_u32 v61, v27, v28, v29\n\t"
    "v_smfmac_f32_16x16x64_bf16 v[20:23], v[24:27], v[168:175], v6\n\t"
    "v_max3_u32 v60, v60, v30, v31\n\t"
    "v_max_u32 v60, v60, v61\n\t"
    "v_cmp_eq_u32 vcc, -1, v60\n\t"
    "s_cbranch_vccnz 1f\n\t"
    "2:\n\t"
    "s_waitcnt lgkmcnt(0)\n\t"
    "s_barrier\n\t"
    "ds_read_b128 v[32:35], v5 offset:0\n\t"
    "ds_read_b128 v[36:39], v5 offset:1024\n\t"
    "v_smfmac_f32_16x16x64_bf16 v[16:19], v[28:31], v[176:183], v6\n\t"
    "ds_read_b128 v[40:43], v5 offset:2048\n\t"
    "ds_read_b128 v[44:47], v5 offset:3072\n\t"
    "v_smfmac_f32_16x16x64_bf16 v[20:23], v[28:31], v[184:191], v6\n\t"
    "s_waitcnt lgkmcnt(3)\n\t"
    "v_smfmac_f32_16x16x64_bf16 v[16:19], v[32:35], v[64:71], v6\n\t"
    "ds_read_b128 v[48:51], v5 offset:4096\n\t"
    "v_smfmac_f32_16x16x64_bf16 v[20:23], v[32:35], v[72:79], v6\n\t"
    "ds_read_b128 v[52:55], v5 offset:5120\n\t"
    "s_waitcnt lgkmcnt(4)\n\t"
    "v_smfmac_f32_16x16x64_bf16 v[16:19], v[36:39], v[80:87], v6\n\t"
    "ds_read_b128 v[24:27], v5 offset:8192\n\t"
    "v_smfmac_f32_16x16x64_bf16 v[20:23], v[36:39], v[88:95], v6\n\t"
    "ds_read_b128 v[28:31], v5 offset:9216\n\t"
    "s_waitcnt lgkmcnt(5)\n\t"
    "v_smfmac_f32_16x16x64_bf16 v[16:19], v[40:43], v[96:103], v6\n\t"
    "ds_read_b128 v[56:59], v5 offset:10240\n\t"
    "v_smfmac_f32_16x16x64_bf16 v[20:23], v[40:43], v[104:111], v6\n\t"
    "ds_read_b128 v[60:63], v5 offset:11264\n\t"
    "ds_read_b128 v[36:39], v11 offset:36864\n\t"
    "s_waitcnt lgkmcnt(7)\n\t"
    "v_smfmac_f32_16x16x64_bf16 v[16:19], v[44:47], v[112:119], v6\n\t"
    "ds_read_b128 v[40:43], v12 offset:32768\n\t"
    "v_smfmac_f32_16x16x64_bf16 v[20:23], v[44:47], v[120:127], v6\n\t"
    "ds_read_b128 v[44:47], v12 offset:36864\n\t"
    "s_waitcnt lgkmcnt(8)\n\t"
    "v_smfmac_f32_16x16x64_bf16 v[16:19], v[48:51], v[128:135], v6\n\t"
    "v_smfmac_f32_16x16x64_bf16 v[20:23], v[48:51], v[136:143], v6\n\t"
    "ds_read_b128 v[48:51], v13 offset:32768\n\t"
    "s_waitcnt lgkmcnt(8)\n\t"
    "v_smfmac_f32_16x16x64_bf16 v[16:19], v[52:55], v[144:151], v6\n\t"
    "v_smfmac_f32_16x16x64_bf16 v[20:23], v[52:55], v[152:159], v6\n\t"
    "ds_read_b128 v[52:55], v13 offset:36864\n\t"
    "s_waitcnt lgkmcnt(8)\n\t"
    "v_smfmac_f32_16x16x64_bf16 v[16:19], v[24:27], v[192:199], v6\n\t"
    "ds_read_b32 v8, v15 offset:29440\n\t"
    "ds_read_b128 v[32:35], v5 offset:27648\n\t"
    "s_setprio 3\n\t"
    "v_smfmac_f32_16x16x64_bf16 v[20:23], v[24:27], v[200:207], v6\n\t"
    "ds_read_b128 v[24:27], v10 offset:32768\n\t"
    "s_waitcnt lgkmcnt(10)\n\t"
    "v_smfmac_f32_16x16x64_bf16 v[16:19], v[28:31], v[208:215], v6\n\t"
    "s_waitcnt lgkmcnt(9)\n\t"
    "v_smfmac_f32_16x16x64_bf16 v[16:19], v[56:59], v[224:231], v6\n\t"
    "s_waitcnt lgkmcnt(8)\n\t"
    "v_smfmac_f32_16x16x64_bf16 v[16:19], v[60:63], v[240:247], v6\n\t"
    "v_smfmac_f32_16x16x64_bf16 v[20:23], v[28:31], v[216:223], v6\n\t"
    "s_waitcnt lgkmcnt(1)\n\t"
    "21:\n\t"
    "v_cmp_ne_u32 vcc, %[sn], v8\n\t"
    "s_cbranch_vccnz 22f\n\t"
    "v_mul_f32 v32, %[lsc], v32\n\t"
    "v_mul_f32 v33, %[lsc], v33\n\t"
    "v_smfmac_f32_16x16x64_bf16 v[20:23], v[56:59], v[232:239], v6\n\t"
    "v_mul_f32 v34, %[lsc], v34\n\t"
    "v_mul_f32 v35, %[lsc], v35\n\t"
    "v_add_f32 v16, v16, v18\n\t"
    "v_add_f32 v17, v17, v19\n\t"
    "v_smfmac_f32_16x16x64_bf16 v[20:23], v[60:63], v[248:255], v6\n\t"
    "v_fma_f32 v16, v16, %[lsc], v32\n\t"
    "v_fma_f32 v17, v17, %[lsc], v33\n\t"
    "v_cvt_pk_bf16_f32 v16, v16, v17\n\t"
    "ds_write_b32 v15, v16 offset:17920\n\t"
    "s_mov_b32 %[bg], %[big]\n\t"
    "s_nop 2\n\t"
    "v_add_f32 v20, v20, v22\n\t"
    "v_add_f32 v21, v21, v23\n\t"
    "v_fma_f32 v20, v20, %[lsc], v34\n\t"
    "v_fma_f32 v21, v21, %[lsc], v35\n\t"
    "v_cvt_pk_bf16_f32 v20, v20, v21\n\t"
    "ds_write_b32 v15, v20 offset:18176\n\t"
    "ds_read_b128 v[28:31], v10 offset:36864\n\t"
    "ds_read_b128 v[32:35], v11 offset:32768\n\t"
    "s_waitcnt lgkmcnt(2)\n\t"
    "s_barrier\n\t"
    "ds_read_b64_tr_b16 v[16:17], v7 offset:16384\n\t"
    "ds_read_b64_tr_b16 v[18:19], v7 offset:16448\n\t"
    "ds_read_b64_tr_b16 v[20:21], v7 offset:16896\n\t"
    "ds_read_b64_tr_b16 v[22:23], v7 offset:16960\n\t"
    "ds_read_b64_tr_b16 v[56:57], v7 offset:17408\n\t"
    "ds_read_b64_tr_b16 v[58:59], v7 offset:17472\n\t"
    "ds_read_b64_tr_b16 v[60:61], v7 offset:17920\n\t"
    "ds_read_b64_tr_b16 v[62:63], v7 offset:17984\n\t"
    "global_load_dwordx4 v[6:9], v5, %[vs] sc1\n\t"
    "global_load_dwordx4 v[10:13], v5, %[vs] offset:1024 sc1\n\t"
    "s_waitcnt lgkmcnt(6)\n\t"
    "v_mfma_f32_16x16x32_bf16 v[24:27], v[24:27], v[16:19], 0\n\t"
    "v_mfma_f32_16x16x32_bf16 v[28:31], v[28:31], v[16:19], 0\n\t"
    "s_waitcnt lgkmcnt(4)\n\t"
    "v_mfma_f32_16x16x32_bf16 v[24:27], v[32:35], v[20:23], v[24:27]\n\t"
    "v_mfma_f32_16x16x32_bf16 v[28:31], v[36:39], v[20:23], v[28:31]\n\t"
    "v_xor_b32 v39, 32, v14\n\t"
    "ds_read_b64_tr_b16 v[32:33], v14 offset:40960\n\t"
    "ds_read_b64_tr_b16 v[34:35], v14 offset:45056\n\t"
    "ds_read_b64_tr_b16 v[36:37], v39 offset:40960\n\t"
    "ds_read_b64_tr_b16 v[38:39], v39 offset:45056\n\t"
    "s_waitcnt lgkmcnt(6)\n\t"
    "v_mfma_f32_16x16x32_bf16 v[24:27], v[40:43], v[56:59], v[24:27]\n\t"
    "v_mfma_f32_16x16x32_bf16 v[28:31], v[44:47], v[56:59], v[28:31]\n\t"
    "s_waitcnt lgkmcnt(4)\n\t"
    "v_mfma_f32_16x16x32_bf16 v[24:27], v[48:51], v[60:63], v[24:27]\n\t"
    "v_mfma_f32_16x16x32_bf16 v[28:31], v[52:55], v[60:63], v[28:31]\n\t"
    "v_mov_b32 v52, %[one]\n\t"
    "v_mov_b32 v53, %[one]\n\t"
    "v_mov_b32 v54, %[one]\n\t"
    "v_mov_b32 v55, %[one]\n\t"
    "v_mov_b32 v45, 0\n\t"
    "v_mov_b32 v47, 0\n\t"
    "v_mov_b32 v48, %[one]\n\t"
    "v_max3_f32 v14, |v24|, |v25|, |v26|\n\t"
    "v_max3_f32 v40, |v27|, |v28|, |v29|\n\t"
    "v_max3_f32 v14, v14, |v30|, |v31|\n\t"
    "v_max_f32 v14, v14, v40\n\t"
    "v_cmp_nge_f32 vcc, %[big], v14\n\t"
    "v_mov_b32 v14, 0\n\t"
    "s_cbranch_vccnz 12f\n\t"
    "13:\n\t"
    "v_lshrrev_b32 v46, 2, v5\n\t"
    "global_load_dword v44, v46, %[v2] sc1\n\t"
    "global_load_dword v46, v46, %[v2] offset:256 sc1\n\t"
    "v_exp_f32 v24, v24\n\t"
    "v_exp_f32 v25, v25\n\t"
    "v_exp_f32 v26, v26\n\t"
    "v_exp_f32 v27, v27\n\t"
    "v_exp_f32 v28, v28\n\t"
    "v_exp_f32 v29, v29\n\t"
    "v_exp_f32 v30, v30\n\t"
    "v_exp_f32 v31, v31\n\t"
    "v_cvt_pk_bf16_f32 v40, v24, v25\n\t"
    "v_cvt_pk_bf16_f32 v41, v26, v27\n\t"
    "v_cvt_pk_bf16_f32 v42, v28, v29\n\t"
    "v_cvt_pk_bf16_f32 v43, v30, v31\n\t"
    "v_mov_b32 v49, 0\n\t"
    "v_mov_b32 v51, 0\n\t"
    "s_waitcnt lgkmcnt(0)\n\t"
    "v_mfma_f32_16x16x32_bf16 v[52:55], v[52:55], v[40:43], 0\n\t"
    "v_mfma_f32_16x16x32_bf16 v[24:27], v[32:35], v[40:43], 0\n\t"
    "v_mfma_f32_16x16x32_bf16 v[28:31], v[36:39], v[40:43], 0\n\t"
    "s_waitcnt vmcnt(3)\n\t"
    "5:\n\t"
    "v_mfma_f32_16x16x32_bf16 v[32:35], v[6:9], v[16:19], 0\n\t"
    "v_mfma_f32_16x16x32_bf16 v[36:39], v[6:9], v[20:23], 0\n\t"
    "s_waitcnt vmcnt(2)\n\t"
    "v_mfma_f32_16x16x32_bf16 v[32:35], v[10:13], v[56:59], v[32:35]\n\t"
    "v_mfma_f32_16x16x32_bf16 v[36:39], v[10:13], v[60:63], v[36:39]\n\t"
    "v_max3_u32 v40, v6, v7, v8\n\t"
    "v_max3_u32 v41, v9, v10, v11\n\t"
    "v_max3_u32 v40, v40, v12, v13\n\t"
    "v_max_u32 v40, v40, v41\n\t"
    "v_cmp_eq_u32 vcc, -1, v40\n\t"
    "s_cbranch_vccnz 4f\n\t"
    "6:\n\t"
    "s_nop 0\n\t"
    "s_nop 0\n\t"
    "v_add_f32 v32, v32, v38\n\t"
    "v_add_f32 v33, v33, v39\n\t"
    "v_exp_f32 v34, v32\n\t"
    "v_max_f32 v40, v32, v33\n\t"
    "v_exp_f32 v35, v33\n\t"
    "v_cmp_nge_f32 vcc, %[bg], v40\n\t"
    "s_cbranch_vccnz 10f\n\t"
    "11:\n\t"
    "v_cvt_pk_bf16_f32 v50, v34, v35\n\t"
    "17:\n\t"
    "s_waitcnt vmcnt(0) lgkmcnt(0)\n\t"
    "v_max_u32 v40, v44, v46\n\t"
    "v_cmp_eq_u32 vcc, -1, v40\n\t"
    "s_cbranch_vccnz 18f\n\t"
    "19:\n\t"
    "v_mfma_f32_16x16x16_bf16 v[52:55], v[48:49], v[50:51], v[52:55]\n\t"
    "v_mfma_f32_16x16x16_bf16 v[24:27], v[44:45], v[50:51], v[24:27]\n\t"
    "v_mfma_f32_16x16x16_bf16 v[28:31], v[46:47], v[50:51], v[28:31]\n\t"
    "s_nop 5\n\t"
    "v_rcp_f32 v42, v52\n\t"
    "s_nop 0\n\t"
    "v_pk_mul_f32 v[24:25], v[24:25], v[42:43] op_sel_hi:[1,0]\n\t"
    "v_pk_mul_f32 v[26:27], v[26:27], v[42:43] op_sel_hi:[1,0]\n\t"
    "v_pk_mul_f32 v[28:29], v[28:29], v[42:43] op_sel_hi:[1,0]\n\t"
    "v_pk_mul_f32 v[30:31], v[30:31], v[42:43] op_sel_hi:[1,0]\n\t"
    "v_cvt_pk_bf16_f32 v36, v24, v25\n\t"
    "v_cvt_pk_bf16_f32 v37, v26, v27\n\t"
    "v_cvt_pk_bf16_f32 v38, v28, v29\n\t"
    "v_cvt_pk_bf16_f32 v39, v30, v31\n\t"
    "v_min_u32 v36, -2, v36\n\t"
    "v_min_u32 v37, -2, v37\n\t"
    "v_min_u32 v38, -2, v38\n\t"
    "v_min_u32 v39, -2, v39\n\t"
    "global_store_dwordx4 v4, v[36:39], %[od] sc1\n\t"
    "ds_write_b32 v15, v44 offset:19968\n\t"
    "ds_write_b32 v15, v46 offset:20224\n\t"
    "s_setprio 1\n\t"
    "s_waitcnt lgkmcnt(0)\n\t"
    "s_barrier\n\t"
    "s_waitcnt lgkmcnt(0)\n\t"
    "s_branch 9f\n\t"
    "1:\n\t"
    "s_add_u32 %[sp], %[sp], 1\n\t"
    "s_cmp_ge_u32 %[sp], %[cap]\n\t"
    "s_cbranch_scc1 7f\n\t"
    "global_load_dwordx4 v[24:27], v5, %[ag] sc1\n\t"
    "global_load_dwordx4 v[28:31], v5, %[ag] offset:1024 sc1\n\t"
    "s_waitcnt lgkmcnt(0)\n\t"
    "s_memtime %[tc]\n\t"
    "s_nop 7\n\t"
    "s_branch 3b\n\t"
    "7:\n\t"
    "s_mov_b32 %[cap], 0\n\t"
    "s_branch 2b\n\t"
    "4:\n\t"
    "s_add_u32 %[sp], %[sp], 0x10000\n\t"
    "s_lshr_b32 %[st], %[sp], 16\n\t"
    "s_cmp_ge_u32 %[st], %[cap]\n\t"
    "s_cbranch_scc1 8f\n\t"
    "global_load_dwordx4 v[6:9], v5, %[vs] sc1\n\t"
    "global_load_dwordx4 v[10:13], v5, %[vs] offset:1024 sc1\n\t"
    "s_waitcnt vmcnt(0) lgkmcnt(0)\n\t"
    "s_branch 5b\n\t"
    "8:\n\t"
    "s_mov_b32 %[cap], 0\n\t"
    "s_branch 6b\n\t"
    "18:\n\t"
    "s_add_u32 %[sp], %[sp], 0x10000\n\t"
    "s_lshr_b32 %[st], %[sp], 16\n\t"
    "s_cmp_ge_u32 %[st], %[cap]\n\t"
    "s_cbranch_scc1 20f\n\t"
    "v_lshrrev_b32 v46, 2, v5\n\t"
    "global_load_dword v44, v46, %[v2] sc1\n\t"
    "global_load_dword v46, v46, %[v2] offset:256 sc1\n\t"
    "s_waitcnt vmcnt(0) lgkmcnt(0)\n\t"
    "s_branch 17b\n\t"
    "20:\n\t"
    "s_mov_b32 %[cap], 0\n\t"
    "s_branch 19b\n\t"
    "22:\n\t"
    "ds_read_b32 v8, v15 offset:29440\n\t"
    "ds_read_b128 v[32:35], v5 offset:27648\n\t"
    "s_waitcnt lgkmcnt(0)\n\t"
    "s_branch 21b\n\t"
    "10:\n\t"
    "v_max_f32 v40, v40, v14\n\t"
    "v_mov_b32 v42, v40\n\t"
    "s_nop 1\n\t"
    "v_permlane16_swap_b32 v40, v42\n\t"
    "v_max_f32 v40, v40, v42\n\t"
    "v_mov_b32 v42, v40\n\t"
    "s_nop 1\n\t"
    "v_permlane32_swap_b32 v40, v42\n\t"
    "v_max_f32 v40, v40, v42\n\t"
    "v_sub_f32 v34, v32, v40\n\t"
    "v_sub_f32 v35, v33, v40\n\t"
    "v_sub_f32 v40, v14, v40\n\t"
    "v_exp_f32 v40, v40\n\t"
    "v_exp_f32 v34, v34\n\t"
    "v_exp_f32 v35, v35\n\t"
    "v_mul_f32 v24, v24, v40\n\t"
    "v_mul_f32 v25, v25, v40\n\t"
    "v_mul_f32 v26, v26, v40\n\t"
    "v_mul_f32 v27, v27, v40\n\t"
    "v_mul_f32 v28, v28, v40\n\t"
    "v_mul_f32 v29, v29, v40\n\t"
    "v_mul_f32 v30, v30, v40\n\t"
    "v_mul_f32 v31, v31, v40\n\t"
    "v_mul_f32 v52, v52, v40\n\t"
    "v_mul_f32 v53, v53, v40\n\t"
    "v_mul_f32 v54, v54, v40\n\t"
    "v_mul_f32 v55, v55, v40\n\t"
    "s_branch 11b\n\t"
    "12:\n\t"
    "v_max3_f32 v14, v24, v25, v26\n\t"
    "v_max3_f32 v40, v27, v28, v29\n\t"
    "v_max3_f32 v14, v14, v30, v31\n\t"
    "v_max3_f32 v14, v14, v40, %[neg]\n\t"
    "v_mov_b32 v40, v14\n\t"
    "s_nop 1\n\t"
    "v_permlane16_swap_b32 v14, v40\n\t"
    "v_max_f32 v14, v14, v40\n\t"
    "v_mov_b32 v40, v14\n\t"
    "s_nop 1\n\t"
    "v_permlane32_swap_b32 v14, v40\n\t"
    "v_max_f32 v14, v14, v40\n\t"
    "v_sub_f32 v24, v24, v14\n\t"
    "v_sub_f32 v25, v25, v14\n\t"
    "v_sub_f32 v26, v26, v14\n\t"
    "v_sub_f32 v27, v27, v14\n\t"
    "v_sub_f32 v28, v28, v14\n\t"
    "v_sub_f32 v29, v29, v14\n\t"
    "v_sub_f32 v30, v30, v14\n\t"
    "v_sub_f32 v31, v31, v14\n\t"
    "s_mov_b32 %[bg], 0x7fc00000\n\t"
    "s_branch 13b\n\t"
    "9:"
    : [tc] "=&s"(tc), [sp] "=&s"(sp), [cap] "+s"(cap), [st] "=&s"(st), [bg] "=&s"(bg)
    : [ag] "s"(ag), [c44] "s"(0x4444u), [lsc] "s"(LSC), [sn] "s"(sn), [pb] "s"(pb), [vs] "s"(vs), [od] "s"(od), [neg] "s"(-1.0e38f), [one] "s"(0x3F803F80u), [big] "s"(24.0f), [v2] "s"(v2), "{v[64:95]}"(W.w0), "{v[96:127]}"(W.w1), "{v[128:159]}"(W.w2), "{v[160:191]}"(W.w3), "{v[192:223]}"(W.w4), "{v[224:255]}"(W.w5)
    : "v4", "v5", "v6", "v7", "v8", "v9", "v10", "v11", "v12", "v13", "v14", "v15", "v16", "v17", "v18", "v19", "v20", "v21", "v22", "v23", "v24", "v25", "v26", "v27", "v28", "v29", "v30", "v31", "v32", "v33", "v34", "v35", "v36", "v37", "v38", "v39", "v40", "v41", "v42", "v43", "v44", "v45", "v46", "v47", "v48", "v49", "v50", "v51", "v52", "v53", "v54", "v55", "v56", "v57", "v58", "v59", "v60", "v61", "v62", "v63", "vcc", "scc", "memory");
}
template <> __device__ __forceinline__ void qk_step<4>(const QKW& W, const char* ag, const char* vs, const char* v2, const char* od, unsigned pb, unsigned sn, unsigned& cap, unsigned& sp,
    unsigned long long& tc, unsigned long long* ts, unsigned& d0, unsigned& d1) {
  unsigned st, bg;
  asm volatile("v_mbcnt_lo_u32_b32 v60, -1, 0\n\t"
    "v_mbcnt_hi_u32_b32 v60, -1, v60\n\t"
    "v_lshlrev_b32 v6, 4, v60\n\t"
    "s_mov_b32 %[sp], 0\n\t"
    "s_setprio 3\n\t"
    "global_load_dwordx4 v[24:27], v6, %[ag] sc1\n\t"
    "global_load_dwordx4 v[28:31], v6, %[ag] offset:-4096 sc1\n\t"
    "s_memtime %[tc]\n\t"
    "v_lshrrev_b32 v56, 4, v60\n\t"
    "v_and_b32 v57, 15, v60\n\t"
    "v_mov_b32 v20, 0\n\t"
    "v_mov_b32 v21, 0\n\t"
    "v_mov_b32 v22, 0\n\t"
    "v_mov_b32 v23, 0\n\t"
    "v_mov_b32 v11, %[sn]\n\t"
    "v_mov_b32 v10, -1\n\t"
    "v_and_b32 v61, 2, v60\n\t"
    "v_mul_u32_u24 v61, 0x5555, v61\n\t"
    "v_or_b32 v8, %[c44], v61\n\t"
    "v_lshlrev_b32 v61, 2, v56\n\t"
    "v_lshl_or_b32 v9, v57, 4, v61\n\t"
    "3:\n\t"
    "v_mov_b32 v12, 0\n\t"
    "v_mov_b32 v13, 0\n\t"
    "v_mov_b32 v14, 0\n\t"
    "v_mov_b32 v15, 0\n\t"
    "v_mov_b32 v16, 0\n\t"
    "v_mov_b32 v17, 0\n\t"
    "v_mov_b32 v18, 0\n\t"
    "v_mov_b32 v19, 0\n\t"
    "s_waitcnt vmcnt(0) lgkmcnt(0)\n\t"
    "ds_write_b128 v6, v[24:27] offset:12288\n\t"
    "ds_write_b128 v6, v[28:31] offset:8192\n\t"
    "v_smfmac_f32_16x16x64_bf16 v[12:15], v[24:27], v[64:71], v8\n\t"
    "v_max3_u32 v60, v24, v25, v26\n\t"
    "v_max3_u32 v61, v27, v28, v29\n\t"
    "v_smfmac_f32_16x16x64_bf16 v[16:19], v[24:27], v[72:79], v8\n\t"
    "v_max3_u32 v60, v60, v30, v31\n\t"
    "v_max_u32 v60, v60, v61\n\t"
    "v_cmp_eq_u32 vcc, -1, v60\n\t"
    "s_cbranch_vccnz 1f\n\t"
    "2:\n\t"
    "s_waitcnt lgkmcnt(0)\n\t"
    "s_barrier\n\t"
    "ds_read_b128 v[32:35], v6 offset:13312\n\t"
    "ds_read_b128 v[36:39], v6 offset:14336\n\t"
    "ds_read_b128 v[40:43], v6 offset:15360\n\t"
    "ds_write_b32 v9, v10 offset:18432\n\t"
    "s_waitcnt lgkmcnt(3)\n\t"
    "v_smfmac_f32_16x16x64_bf16 v[12:15], v[32:35], v[80:87], v8\n\t"
    "v_smfmac_f32_16x16x64_bf16 v[16:19], v[32:35], v[88:95], v8\n\t"
    "s_waitcnt lgkmcnt(2)\n\t"
    "v_smfmac_f32_16x16x64_bf16 v[12:15], v[36:39], v[96:103], v8\n\t"
    "v_smfmac_f32_16x16x64_bf16 v[16:19], v[36:39], v[104:111], v8\n\t"
    "s_waitcnt lgkmcnt(1)\n\t"
    "v_smfmac_f32_16x16x64_bf16 v[12:15], v[40:43], v[112:119], v8\n\t"
    "v_smfmac_f32_16x16x64_bf16 v[16:19], v[40:43], v[120:127], v8\n\t"
    "s_nop 6\n\t"
    "v_add_f32 v12, v12, v14\n\t"
    "v_add_f32 v13, v13, v15\n\t"
    "v_add_f32 v14, v16, v18\n\t"
    "v_add_f32 v15, v17, v19\n\t"
    "ds_write_b128 v6, v[12:15] offset:24576\n\t"
    "ds_write_b32 v9, v11 offset:28672\n\t"
    "s_setprio 0\n\t"
    "s_memrealtime s[36:37]\n\t"
    "s_waitcnt lgkmcnt(0)\n\t"
    "s_mov_b32 s38, s36\n\t"
    "s_min_u32 s37, %[d0], %[d1]\n\t"
    "s_add_u32 s36, s36, s37\n\t"
    "s_and_b32 s36, s36, 0xffffff\n\t"
    "s_or_b32 s36, s36, %[sn]\n\t"
    "v_lshlrev_b32 v10, 10, v6\n\t"
    "v_mov_b32 v5, s36\n\t"
    "s_mov_b64 exec, 15\n\t"
    "global_store_dword v10, v5, %[vs] offset:-4096 sc1\n\t"
    "s_mov_b64 exec, -1\n\t"
    "ds_read_b128 v[44:47], v6 offset:0\n\t"
    "ds_read_b128 v[48:51], v6 offset:1024\n\t"
    "ds_read_b128 v[52:55], v6 offset:2048\n\t"
    "ds_read_b128 v[56:59], v6 offset:3072\n\t"
    "ds_read_b128 v[60:63], v6 offset:4096\n\t"
    "s_sleep 2\n\t"
    "v_smfmac_f32_16x16x64_bf16 v[20:23], v[28:31], v[192:199], v8\n\t"
    "ds_read_b128 v[28:31], v6 offset:5120\n\t"
    "v_smfmac_f32_16x16x64_bf16 v[20:23], v[24:27], v[224:231], v8\n\t"
    "ds_read_b128 v[24:27], v6 offset:6144\n\t"
    "v_smfmac_f32_16x16x64_bf16 v[20:23], v[32:35], v[240:247], v8\n\t"
    "ds_read_b128 v[32:35], v6 offset:7168\n\t"
    "v_smfmac_f32_16x16x64_bf16 v[20:23], v[36:39], v[232:239], v8\n\t"
    "ds_read_b128 v[36:39], v6 offset:9216\n\t"
    "v_smfmac_f32_16x16x64_bf16 v[20:23], v[40:43], v[248:255], v8\n\t"
    "ds_read_b128 v[40:43], v6 offset:10240\n\t"
    "s_waitcnt lgkmcnt(9)\n\t"
    "v_smfmac_f32_16x16x64_bf16 v[20:23], v[44:47], v[128:135], v8\n\t"
    "ds_read_b128 v[44:47], v6 offset:11264\n\t"
    "s_barrier\n\t"
    "s_setprio 0\n\t"
    "s_waitcnt lgkmcnt(9)\n\t"
    "v_smfmac_f32_16x16x64_bf16 v[20:23], v[48:51], v[144:151], v8\n\t"
    "s_waitcnt lgkmcnt(8)\n\t"
    "v_smfmac_f32_16x16x64_bf16 v[20:23], v[52:55], v[136:143], v8\n\t"
    "s_waitcnt lgkmcnt(7)\n\t"
    "v_smfmac_f32_16x16x64_bf16 v[20:23], v[56:59], v[152:159], v8\n\t"
    "s_waitcnt lgkmcnt(6)\n\t"
    "v_smfmac_f32_16x16x64_bf16 v[20:23], v[60:63], v[160:167], v8\n\t"
    "s_waitcnt lgkmcnt(5)\n\t"
    "v_smfmac_f32_16x16x64_bf16 v[20:23], v[28:31], v[176:183], v8\n\t"
    "s_waitcnt lgkmcnt(4)\n\t"
    "v_smfmac_f32_16x16x64_bf16 v[20:23], v[24:27], v[168:175], v8\n\t"
    "s_waitcnt lgkmcnt(3)\n\t"
    "v_smfmac_f32_16x16x64_bf16 v[20:23], v[32:35], v[184:191], v8\n\t"
    "s_waitcnt lgkmcnt(2)\n\t"
    "v_smfmac_f32_16x16x64_bf16 v[20:23], v[36:39], v[208:215], v8\n\t"
    "s_waitcnt lgkmcnt(1)\n\t"
    "v_smfmac_f32_16x16x64_bf16 v[20:23], v[40:43], v[200:207], v8\n\t"
    "s_waitcnt lgkmcnt(0)\n\t"
    "v_smfmac_f32_16x16x64_bf16 v[20:23], v[44:47], v[216:223], v8\n\t"
    "s_nop 7\n\t"
    "v_add_f32 v20, v20, v22\n\t"
    "v_add_f32 v21, v21, v23\n\t"
    "v_cvt_pk_bf16_f32 v20, v20, v21\n\t"
    "v_min_u32 v20, -2, v20\n\t"
    "ds_write_b32 v9, v20 offset:18432\n\t"
    "s_waitcnt lgkmcnt(0)\n\t"
    "s_setprio 3\n\t"
    "s_barrier\n\t"
    "s_memrealtime s[36:37]\n\t"
    "s_mov_b32 %[d1], %[d0]\n\t"
    "s_waitcnt lgkmcnt(0)\n\t"
    "s_sub_u32 %[d0], s36, s38\n\t"
    "s_min_u32 %[d0], %[d0], 200\n\t"
    "s_and_b32 s36, s36, 0xffffff\n\t"
    "s_or_b32 s36, s36, %[sn]\n\t"
    "v_lshlrev_b32 v10, 10, v6\n\t"
    "v_mov_b32 v5, s36\n\t"
    "s_mov_b64 exec, 15\n\t"
    "global_store_dword v10, v5, %[vs] offset:0 sc1\n\t"
    "s_mov_b64 exec, -1\n\t"
    "s_waitcnt lgkmcnt(0)\n\t"
    "s_branch 9f\n\t"
    "1:\n\t"
    "s_add_u32 %[sp], %[sp], 1\n\t"
    "s_cmp_ge_u32 %[sp], %[cap]\n\t"
    "s_cbranch_scc1 7f\n\t"
    "global_load_dwordx4 v[24:27], v6, %[ag] sc1\n\t"
    "global_load_dwordx4 v[28:31], v6, %[ag] offset:-4096 sc1\n\t"
    "s_waitcnt lgkmcnt(0)\n\t"
    "s_memtime %[tc]\n\t"
    "s_nop 7\n\t"
    "s_branch 3b\n\t"
    "7:\n\t"
    "s_mov_b32 %[cap], 0\n\t"
    "s_branch 2b\n\t"
    "9:"
    : [tc] "=&s"(tc), [sp] "=&s"(sp), [cap] "+s"(cap), [d0] "+s"(d0), [d1] "+s"(d1)
    : [ag] "s"(ag), [c44] "s"(0x4444u), [lsc] "s"(LSC), [sn] "s"(sn), [vs] "s"(vs), "{v[64:95]}"(W.w0), "{v[96:127]}"(W.w1), "{v[128:159]}"(W.w2), "{v[160:191]}"(W.w3), "{v[192:223]}"(W.w4), "{v[224:255]}"(W.w5)
    : "v5", "v6", "v7", "v8", "v9", "v10", "v11", "v12", "v13", "v14", "v15", "v16", "v17", "v18", "v19", "v20", "v21", "v22", "v23", "v24", "v25", "v26", "v27", "v28", "v29", "v30", "v31", "v32", "v33", "v34", "v35", "v36", "v37", "v38", "v39", "v40", "v41", "v42", "v43", "v44", "v45", "v46", "v47", "v48", "v49", "v50", "v51", "v52", "v53", "v54", "v55", "v56", "v57", "v58", "v59", "v60", "v61", "v62", "v63", "vcc", "scc", "memory", "s36", "s37", "s38");
}
template <> __device__ __forceinline__ void qk_step<5>(const QKW& W, const char* ag, const char* vs, const char* v2, const char* od, unsigned pb, unsigned sn, unsigned& cap, unsigned& sp,
    unsigned long long& tc, unsigned long long* ts, unsigned& d0, unsigned& d1) {
  unsigned st, bg;
  asm volatile("v_mbcnt_lo_u32_b32 v60, -1, 0\n\t"
    "v_mbcnt_hi_u32_b32 v60, -1, v60\n\t"
    "v_lshlrev_b32 v6, 4, v60\n\t"
    "s_mov_b32 %[sp], 0\n\t"
    "s_setprio 3\n\t"
    "global_load_dwordx4 v[24:27], v6, %[ag] sc1\n\t"
    "global_load_dwordx4 v[28:31], v6, %[ag] offset:-4096 sc1\n\t"
    "s_memtime %[tc]\n\t"
    "v_lshrrev_b32 v56, 4, v60\n\t"
    "v_and_b32 v57, 15, v60\n\t"
    "v_mov_b32 v20, 0\n\t"
    "v_mov_b32 v21, 0\n\t"
    "v_mov_b32 v22, 0\n\t"
    "v_mov_b32 v23, 0\n\t"
    "v_mov_b32 v11, %[sn]\n\t"
    "v_mov_b32 v10, -1\n\t"
    "v_and_b32 v61, 2, v60\n\t"
    "v_mul_u32_u24 v61, 0x5555, v61\n\t"
    "v_or_b32 v8, %[c44], v61\n\t"
    "v_lshlrev_b32 v61, 2, v56\n\t"
    "v_lshl_or_b32 v9, v57, 4, v61\n\t"
    "3:\n\t"
    "v_mov_b32 v12, 0\n\t"
    "v_mov_b32 v13, 0\n\t"
    "v_mov_b32 v14, 0\n\t"
    "v_mov_b32 v15, 0\n\t"
    "v_mov_b32 v16, 0\n\t"
    "v_mov_b32 v17, 0\n\t"
    "v_mov_b32 v18, 0\n\t"
    "v_mov_b32 v19, 0\n\t"
    "s_waitcnt vmcnt(0) lgkmcnt(0)\n\t"
    "ds_write_b128 v6, v[24:27] offset:13312\n\t"
    "ds_write_b128 v6, v[28:31] offset:9216\n\t"
    "v_smfmac_f32_16x16x64_bf16 v[12:15], v[24:27], v[80:87], v8\n\t"
    "v_max3_u32 v60, v24, v25, v26\n\t"
    "v_max3_u32 v61, v27, v28, v29\n\t"
    "v_smfmac_f32_16x16x64_bf16 v[16:19], v[24:27], v[88:95], v8\n\t"
    "v_max3_u32 v60, v60, v30, v31\n\t"
    "v_max_u32 v60, v60, v61\n\t"
    "v_cmp_eq_u32 vcc, -1, v60\n\t"
    "s_cbranch_vccnz 1f\n\t"
    "2:\n\t"
    "s_waitcnt lgkmcnt(0)\n\t"
    "s_barrier\n\t"
    "ds_read_b128 v[32:35], v6 offset:12288\n\t"
    "ds_read_b128 v[36:39], v6 offset:14336\n\t"
    "ds_read_b128 v[40:43], v6 offset:15360\n\t"
    "ds_write_b32 v9, v10 offset:18688\n\t"
    "s_waitcnt lgkmcnt(3)\n\t"
    "v_smfmac_f32_16x16x64_bf16 v[12:15], v[32:35], v[64:71], v8\n\t"
    "v_smfmac_f32_16x16x64_bf16 v[16:19], v[32:35], v[72:79], v8\n\t"
    "s_waitcnt lgkmcnt(2)\n\t"
    "v_smfmac_f32_16x16x64_bf16 v[12:15], v[36:39], v[96:103], v8\n\t"
    "v_smfmac_f32_16x16x64_bf16 v[16:19], v[36:39], v[104:111], v8\n\t"
    "s_waitcnt lgkmcnt(1)\n\t"
    "v_smfmac_f32_16x16x64_bf16 v[12:15], v[40:43], v[112:119], v8\n\t"
    "v_smfmac_f32_16x16x64_bf16 v[16:19], v[40:43], v[120:127], v8\n\t"
    "s_nop 6\n\t"
    "v_add_f32 v12, v12, v14\n\t"
    "v_add_f32 v13, v13, v15\n\t"
    "v_add_f32 v14, v16, v18\n\t"
    "v_add_f32 v15, v17, v19\n\t"
    "ds_write_b128 v6, v[12:15] offset:25600\n\t"
    "ds_write_b32 v9, v11 offset:28928\n\t"
    "s_setprio 0\n\t"
    "ds_read_b128 v[44:47], v6 offset:0\n\t"
    "ds_read_b128 v[48:51], v6 offset:1024\n\t"
    "ds_read_b128 v[52:55], v6 offset:2048\n\t"
    "ds_read_b128 v[56:59], v6 offset:3072\n\t"
    "ds_read_b128 v[60:63], v6 offset:4096\n\t"
    "s_sleep 2\n\t"
    "v_smfmac_f32_16x16x64_bf16 v[20:23], v[28:31], v[208:215], v8\n\t"
    "ds_read_b128 v[28:31], v6 offset:5120\n\t"
    "v_smfmac_f32_16x16x64_bf16 v[20:23], v[24:27], v[240:247], v8\n\t"
    "ds_read_b128 v[24:27], v6 offset:6144\n\t"
    "v_smfmac_f32_16x16x64_bf16 v[20:23], v[32:35], v[224:231], v8\n\t"
    "ds_read_b128 v[32:35], v6 offset:7168\n\t"
    "v_smfmac_f32_16x16x64_bf16 v[20:23], v[36:39], v[232:239], v8\n\t"
    "ds_read_b128 v[36:39], v6 offset:8192\n\t"
    "v_smfmac_f32_16x16x64_bf16 v[20:23], v[40:43], v[248:255], v8\n\t"
    "ds_read_b128 v[40:43], v6 offset:10240\n\t"
    "s_waitcnt lgkmcnt(9)\n\t"
    "v_smfmac_f32_16x16x64_bf16 v[20:23], v[44:47], v[128:135], v8\n\t"
    "ds_read_b128 v[44:47], v6 offset:11264\n\t"
    "s_barrier\n\t"
    "s_setprio 0\n\t"
    "s_waitcnt lgkmcnt(9)\n\t"
    "v_smfmac_f32_16x16x64_bf16 v[20:23], v[48:51], v[144:151], v8\n\t"
    "s_waitcnt lgkmcnt(8)\n\t"
    "v_smfmac_f32_16x16x64_bf16 v[20:23], v[52:55], v[136:143], v8\n\t"
    "s_waitcnt lgkmcnt(7)\n\t"
    "v_smfmac_f32_16x16x64_bf16 v[20:23], v[56:59], v[152:159], v8\n\t"
    "s_waitcnt lgkmcnt(6)\n\t"
    "v_smfmac_f32_16x16x64_bf16 v[20:23], v[60:63], v[160:167], v8\n\t"
    "s_waitcnt lgkmcnt(5)\n\t"
    "v_smfmac_f32_16x16x64_bf16 v[20:23], v[28:31], v[176:183], v8\n\t"
    "s_waitcnt lgkmcnt(4)\n\t"
    "v_smfmac_f32_16x16x64_bf16 v[20:23], v[24:27], v[168:175], v8\n\t"
    "s_waitcnt lgkmcnt(3)\n\t"
    "v_smfmac_f32_16x16x64_bf16 v[20:23], v[32:35], v[184:191], v8\n\t"
    "s_waitcnt lgkmcnt(2)\n\t"
    "v_smfmac_f32_16x16x64_bf16 v[20:23], v[36:39], v[192:199], v8\n\t"
    "s_waitcnt lgkmcnt(1)\n\t"
    "v_smfmac_f32_16x16x64_bf16 v[20:23], v[40:43], v[200:207], v8\n\t"
    "s_waitcnt lgkmcnt(0)\n\t"
    "v_smfmac_f32_16x16x64_bf16 v[20:23], v[44:47], v[216:223], v8\n\t"
    "s_nop 7\n\t"
    "v_add_f32 v20, v20, v22\n\t"
    "v_add_f32 v21, v21, v23\n\t"
    "v_cvt_pk_bf16_f32 v20, v20, v21\n\t"
    "v_min_u32 v20, -2, v20\n\t"
    "ds_write_b32 v9, v20 offset:18688\n\t"
    "s_waitcnt lgkmcnt(0)\n\t"
    "s_setprio 0\n\t"
    "s_waitcnt lgkmcnt(0)\n\t"
    "s_branch 9f\n\t"
    "1:\n\t"
    "s_add_u32 %[sp], %[sp], 1\n\t"
    "s_cmp_ge_u32 %[sp], %[cap]\n\t"
    "s_cbranch_scc1 7f\n\t"
    "global_load_dwordx4 v[24:27], v6, %[ag] sc1\n\t"
    "global_load_dwordx4 v[28:31], v6, %[ag] offset:-4096 sc1\n\t"
    "s_waitcnt lgkmcnt(0)\n\t"
    "s_memtime %[tc]\n\t"
    "s_nop 7\n\t"
    "s_branch 3b\n\t"
    "7:\n\t"
    "s_mov_b32 %[cap], 0\n\t"
    "s_branch 2b\n\t"
    "9:"
    : [tc] "=&s"(tc), [sp] "=&s"(sp), [cap] "+s"(cap)
    : [ag] "s"(ag), [c44] "s"(0x4444u), [lsc] "s"(LSC), [sn] "s"(sn), "{v[64:95]}"(W.w0), "{v[96:127]}"(W.w1), "{v[128:159]}"(W.w2), "{v[160:191]}"(W.w3), "{v[192:223]}"(W.w4), "{v[224:255]}"(W.w5)
    : "v6", "v7", "v8", "v9", "v10", "v11", "v12", "v13", "v14", "v15", "v16", "v17", "v18", "v19", "v20", "v21", "v22", "v23", "v24", "v25", "v26", "v27", "v28", "v29", "v30", "v31", "v32", "v33", "v34", "v35", "v36", "v37", "v38", "v39", "v40", "v41", "v42", "v43", "v44", "v45", "v46", "v47", "v48", "v49", "v50", "v51", "v52", "v53", "v54", "v55", "v56", "v57", "v58", "v59", "v60", "v61", "v62", "v63", "vcc", "scc", "memory");
}
template <> __device__ __forceinline__ void qk_step<6>(const QKW& W, const char* ag, const char* vs, const char* v2, const char* od, unsigned pb, unsigned sn, unsigned& cap, unsigned& sp,
    unsigned long long& tc, unsigned long long* ts, unsigned& d0, unsigned& d1) {
  unsigned st, bg;
  asm volatile("v_mbcnt_lo_u32_b32 v60, -1, 0\n\t"
    "v_mbcnt_hi_u32_b32 v60, -1, v60\n\t"
    "v_lshlrev_b32 v6, 4, v60\n\t"
    "s_mov_b32 %[sp], 0\n\t"
    "s_setprio 3\n\t"
    "global_load_dwordx4 v[24:27], v6, %[ag] sc1\n\t"
    "global_load_dwordx4 v[28:31], v6, %[ag] offset:-4096 sc1\n\t"
    "s_memtime %[tc]\n\t"
    "v_lshrrev_b32 v56, 4, v60\n\t"
    "v_and_b32 v57, 15, v60\n\t"
    "v_mov_b32 v20, 0\n\t"
    "v_mov_b32 v21, 0\n\t"
    "v_mov_b32 v22, 0\n\t"
    "v_mov_b32 v23, 0\n\t"
    "v_mov_b32 v11, %[sn]\n\t"
    "v_mov_b32 v10, -1\n\t"
    "v_and_b32 v61, 2, v60\n\t"
    "v_mul_u32_u24 v61, 0x5555, v61\n\t"
    "v_or_b32 v8, %[c44], v61\n\t"
    "v_lshlrev_b32 v61, 2, v56\n\t"
    "v_lshl_or_b32 v9, v57, 4, v61\n\t"
    "3:\n\t"
    "v_mov_b32 v12, 0\n\t"
    "v_mov_b32 v13, 0\n\t"
    "v_mov_b32 v14, 0\n\t"
    "v_mov_b32 v15, 0\n\t"
    "v_mov_b32 v16, 0\n\t"
    "v_mov_b32 v17, 0\n\t"
    "v_mov_b32 v18, 0\n\t"
    "v_mov_b32 v19, 0\n\t"
    "s_waitcnt vmcnt(0) lgkmcnt(0)\n\t"
    "ds_write_b128 v6, v[24:27] offset:14336\n\t"
    "ds_write_b128 v6, v[28:31] offset:10240\n\t"
    "v_smfmac_f32_16x16x64_bf16 v[12:15], v[24:27], v[96:103], v8\n\t"
    "v_max3_u32 v60, v24, v25, v26\n\t"
    "v_max3_u32 v61, v27, v28, v29\n\t"
    "v_smfmac_f32_16x16x64_bf16 v[16:19], v[24:27], v[104:111], v8\n\t"
    "v_max3_u32 v60, v60, v30, v31\n\t"
    "v_max_u32 v60, v60, v61\n\t"
    "v_cmp_eq_u32 vcc, -1, v60\n\t"
    "s_cbranch_vccnz 1f\n\t"
    "2:\n\t"
    "s_waitcnt lgkmcnt(0)\n\t"
    "s_barrier\n\t"
    "ds_read_b128 v[32:35], v6 offset:12288\n\t"
    "ds_read_b128 v[36:39], v6 offset:13312\n\t"
    "ds_read_b128 v[40:43], v6 offset:15360\n\t"
    "ds_write_b32 v9, v10 offset:18944\n\t"
    "s_waitcnt lgkmcnt(3)\n\t"
    "v_smfmac_f32_16x16x64_bf16 v[12:15], v[32:35], v[64:71], v8\n\t"
    "v_smfmac_f32_16x16x64_bf16 v[16:19], v[32:35], v[72:79], v8\n\t"
    "s_waitcnt lgkmcnt(2)\n\t"
    "v_smfmac_f32_16x16x64_bf16 v[12:15], v[36:39], v[80:87], v8\n\t"
    "v_smfmac_f32_16x16x64_bf16 v[16:19], v[36:39], v[88:95], v8\n\t"
    "s_waitcnt lgkmcnt(1)\n\t"
    "v_smfmac_f32_16x16x64_bf16 v[12:15], v[40:43], v[112:119], v8\n\t"
    "v_smfmac_f32_16x16x64_bf16 v[16:19], v[40:43], v[120:127], v8\n\t"
    "s_nop 6\n\t"
    "v_add_f32 v12, v12, v14\n\t"
    "v_add_f32 v13, v13, v15\n\t"
    "v_add_f32 v14, v16, v18\n\t"
    "v_add_f32 v15, v17, v19\n\t"
    "ds_write_b128 v6, v[12:15] offset:26624\n\t"
    "ds_write_b32 v9, v11 offset:29184\n\t"
    "s_setprio 0\n\t"
    "ds_read_b128 v[44:47], v6 offset:0\n\t"
    "ds_read_b128 v[48:51], v6 offset:1024\n\t"
    "ds_read_b128 v[52:55], v6 offset:2048\n\t"
    "ds_read_b128 v[56:59], v6 offset:3072\n\t"
    "ds_read_b128 v[60:63], v6 offset:4096\n\t"
    "s_sleep 2\n\t"
    "v_smfmac_f32_16x16x64_bf16 v[20:23], v[28:31], v[200:207], v8\n\t"
    "ds_read_b128 v[28:31], v6 offset:5120\n\t"
    "v_smfmac_f32_16x16x64_bf16 v[20:23], v[24:27], v[232:239], v8\n\t"
    "ds_read_b128 v[24:27], v6 offset:6144\n\t"
    "v_smfmac_f32_16x16x64_bf16 v[20:23], v[32:35], v[224:231], v8\n\t"
    "ds_read_b128 v[32:35], v6 offset:7168\n\t"
    "v_smfmac_f32_16x16x64_bf16 v[20:23], v[36:39], v[240:247], v8\n\t"
    "ds_read_b128 v[36:39], v6 offset:8192\n\t"
    "v_smfmac_f32_16x16x64_bf16 v[20:23], v[40:43], v[248:255], v8\n\t"
    "ds_read_b128 v[40:43], v6 offset:9216\n\t"
    "s_waitcnt lgkmcnt(9)\n\t"
    "v_smfmac_f32_16x16x64_bf16 v[20:23], v[44:47], v[128:135], v8\n\t"
    "ds_read_b128 v[44:47], v6 offset:11264\n\t"
    "s_barrier\n\t"
    "s_setprio 0\n\t"
    "s_waitcnt lgkmcnt(9)\n\t"
    "v_smfmac_f32_16x16x64_bf16 v[20:23], v[48:51], v[144:151], v8\n\t"
    "s_waitcnt lgkmcnt(8)\n\t"
    "v_smfmac_f32_16x16x64_bf16 v[20:23], v[52:55], v[136:143], v8\n\t"
    "s_waitcnt lgkmcnt(7)\n\t"
    "v_smfmac_f32_16x16x64_bf16 v[20:23], v[56:59], v[152:159], v8\n\t"
    "s_waitcnt lgkmcnt(6)\n\t"
    "v_smfmac_f32_16x16x64_bf16 v[20:23], v[60:63], v[160:167], v8\n\t"
    "s_waitcnt lgkmcnt(5)\n\t"
    "v_smfmac_f32_16x16x64_bf16 v[20:23], v[28:31], v[176:183], v8\n\t"
    "s_waitcnt lgkmcnt(4)\n\t"
    "v_smfmac_f32_16x16x64_bf16 v[20:23], v[24:27], v[168:175], v8\n\t"
    "s_waitcnt lgkmcnt(3)\n\t"
    "v_smfmac_f32_16x16x64_bf16 v[20:23], v[32:35], v[184:191], v8\n\t"
    "s_waitcnt lgkmcnt(2)\n\t"
    "v_smfmac_f32_16x16x64_bf16 v[20:23], v[36:39], v[192:199], v8\n\t"
    "s_waitcnt lgkmcnt(1)\n\t"
    "v_smfmac_f32_16x16x64_bf16 v[20:23], v[40:43], v[208:215], v8\n\t"
    "s_waitcnt lgkmcnt(0)\n\t"
    "v_smfmac_f32_16x16x64_bf16 v[20:23], v[44:47], v[216:223], v8\n\t"
    "s_nop 7\n\t"
    "v_add_f32 v20, v20, v22\n\t"
    "v_add_f32 v21, v21, v23\n\t"
    "v_cvt_pk_bf16_f32 v20, v20, v21\n\t"
    "v_min_u32 v20, -2, v20\n\t"
    "ds_write_b32 v9, v20 offset:18944\n\t"
    "s_waitcnt lgkmcnt(0)\n\t"
    "s_setprio 0\n\t"
    "s_waitcnt lgkmcnt(0)\n\t"
    "s_branch 9f\n\t"
    "1:\n\t"
    "s_add_u32 %[sp], %[sp], 1\n\t"
    "s_cmp_ge_u32 %[sp], %[cap]\n\t"
    "s_cbranch_scc1 7f\n\t"
    "global_load_dwordx4 v[24:27], v6, %[ag] sc1\n\t"
    "global_load_dwordx4 v[28:31], v6, %[ag] offset:-4096 sc1\n\t"
    "s_waitcnt lgkmcnt(0)\n\t"
    "s_memtime %[tc]\n\t"
    "s_nop 7\n\t"
    "s_branch 3b\n\t"
    "7:\n\t"
    "s_mov_b32 %[cap], 0\n\t"
    "s_branch 2b\n\t"
    "9:"
    : [tc] "=&s"(tc), [sp] "=&s"(sp), [cap] "+s"(cap)
    : [ag] "s"(ag), [c44] "s"(0x4444u), [lsc] "s"(LSC), [sn] "s"(sn), "{v[64:95]}"(W.w0), "{v[96:127]}"(W.w1), "{v[128:159]}"(W.w2), "{v[160:191]}"(W.w3), "{v[192:223]}"(W.w4), "{v[224:255]}"(W.w5)
    : "v6", "v7", "v8", "v9", "v10", "v11", "v12", "v13", "v14", "v15", "v16", "v17", "v18", "v19", "v20", "v21", "v22", "v23", "v24", "v25", "v26", "v27", "v28", "v29", "v30", "v31", "v32", "v33", "v34", "v35", "v36", "v37", "v38", "v39", "v40", "v41", "v42", "v43", "v44", "v45", "v46", "v47", "v48", "v49", "v50", "v51", "v52", "v53", "v54", "v55", "v56", "v57", "v58", "v59", "v60", "v61", "v62", "v63", "vcc", "scc", "memory");
}
template <> __device__ __forceinline__ void qk_step<7>(const QKW& W, const char* ag, const char* vs, const char* v2, const char* od, unsigned pb, unsigned sn, unsigned& cap, unsigned& sp,
    unsigned long long& tc, unsigned long long* ts, unsigned& d0, unsigned& d1) {
  unsigned st, bg;
  asm volatile("v_mbcnt_lo_u32_b32 v60, -1, 0\n\t"
    "v_mbcnt_hi_u32_b32 v60, -1, v60\n\t"
    "v_lshlrev_b32 v6, 4, v60\n\t"
    "s_mov_b32 %[sp], 0\n\t"
    "s_setprio 3\n\t"
    "global_load_dwordx4 v[24:27], v6, %[ag] sc1\n\t"
    "global_load_dwordx4 v[28:31], v6, %[ag] offset:-4096 sc1\n\t"
    "s_memtime %[tc]\n\t"
    "v_lshrrev_b32 v56, 4, v60\n\t"
    "v_and_b32 v57, 15, v60\n\t"
    "v_mov_b32 v20, 0\n\t"
    "v_mov_b32 v21, 0\n\t"
    "v_mov_b32 v22, 0\n\t"
    "v_mov_b32 v23, 0\n\t"
    "v_mov_b32 v11, %[sn]\n\t"
    "v_mov_b32 v10, -1\n\t"
    "v_and_b32 v61, 2, v60\n\t"
    "v_mul_u32_u24 v61, 0x5555, v61\n\t"
    "v_or_b32 v8, %[c44], v61\n\t"
    "v_lshlrev_b32 v61, 2, v56\n\t"
    "v_lshl_or_b32 v9, v57, 4, v61\n\t"
    "3:\n\t"
    "v_mov_b32 v12, 0\n\t"
    "v_mov_b32 v13, 0\n\t"
    "v_mov_b32 v14, 0\n\t"
    "v_mov_b32 v15, 0\n\t"
    "v_mov_b32 v16, 0\n\t"
    "v_mov_b32 v17, 0\n\t"
    "v_mov_b32 v18, 0\n\t"
    "v_mov_b32 v19, 0\n\t"
    "s_waitcnt vmcnt(0) lgkmcnt(0)\n\t"
    "ds_write_b128 v6, v[24:27] offset:15360\n\t"
    "ds_write_b128 v6, v[28:31] offset:11264\n\t"
    "v_smfmac_f32_16x16x64_bf16 v[12:15], v[24:27], v[112:119], v8\n\t"
    "v_max3_u32 v60, v24, v25, v26\n\t"
    "v_max3_u32 v61, v27, v28, v29\n\t"
    "v_smfmac_f32_16x16x64_bf16 v[16:19], v[24:27], v[120:127], v8\n\t"
    "v_max3_u32 v60, v60, v30, v31\n\t"
    "v_max_u32 v60, v60, v61\n\t"
    "v_cmp_eq_u32 vcc, -1, v60\n\t"
    "s_cbranch_vccnz 1f\n\t"
    "2:\n\t"
    "s_waitcnt lgkmcnt(0)\n\t"
    "s_barrier\n\t"
    "ds_read_b128 v[32:35], v6 offset:12288\n\t"
    "ds_read_b128 v[36:39], v6 offset:13312\n\t"
    "ds_read_b128 v[40:43], v6 offset:14336\n\t"
    "ds_write_b32 v9, v10 offset:19200\n\t"
    "s_waitcnt lgkmcnt(3)\n\t"
    "v_smfmac_f32_16x16x64_bf16 v[12:15], v[32:35], v[64:71], v8\n\t"
    "v_smfmac_f32_16x16x64_bf16 v[16:19], v[32:35], v[72:79], v8\n\t"
    "s_waitcnt lgkmcnt(2)\n\t"
    "v_smfmac_f32_16x16x64_bf16 v[12:15], v[36:39], v[80:87], v8\n\t"
    "v_smfmac_f32_16x16x64_bf16 v[16:19], v[36:39], v[88:95], v8\n\t"
    "s_waitcnt lgkmcnt(1)\n\t"
    "v_smfmac_f32_16x16x64_bf16 v[12:15], v[40:43], v[96:103], v8\n\t"
    "v_smfmac_f32_16x16x64_bf16 v[16:19], v[40:43], v[104:111], v8\n\t"
    "s_nop 6\n\t"
    "v_add_f32 v12, v12, v14\n\t"
    "v_add_f32 v13, v13, v15\n\t"
    "v_add_f32 v14, v16, v18\n\t"
    "v_add_f32 v15, v17, v19\n\t"
    "ds_write_b128 v6, v[12:15] offset:27648\n\t"
    "ds_write_b32 v9, v11 offset:29440\n\t"
    "s_setprio 0\n\t"
    "v_lshrrev_b32 v10, 24, v11\n\t"
    "v_add_u32 v10, -1, v10\n\t"
    "s_mov_b64 exec, 1\n\t"
    "global_store_dword v6, v10, %[vs] sc1\n\t"
    "s_mov_b64 exec, -1\n\t"
    "ds_read_b128 v[44:47], v6 offset:0\n\t"
    "ds_read_b128 v[48:51], v6 offset:1024\n\t"
    "ds_read_b128 v[52:55], v6 offset:2048\n\t"
    "ds_read_b128 v[56:59], v6 offset:3072\n\t"
    "ds_read_b128 v[60:63], v6 offset:4096\n\t"
    "s_sleep 2\n\t"
    "v_smfmac_f32_16x16x64_bf16 v[20:23], v[28:31], v[216:223], v8\n\t"
    "ds_read_b128 v[28:31], v6 offset:5120\n\t"
    "v_smfmac_f32_16x16x64_bf16 v[20:23], v[24:27], v[248:255], v8\n\t"
    "ds_read_b128 v[24:27], v6 offset:6144\n\t"
    "v_smfmac_f32_16x16x64_bf16 v[20:23], v[32:35], v[224:231], v8\n\t"
    "ds_read_b128 v[32:35], v6 offset:7168\n\t"
    "v_smfmac_f32_16x16x64_bf16 v[20:23], v[36:39], v[240:247], v8\n\t"
    "ds_read_b128 v[36:39], v6 offset:8192\n\t"
    "v_smfmac_f32_16x16x64_bf16 v[20:23], v[40:43], v[232:239], v8\n\t"
    "ds_read_b128 v[40:43], v6 offset:9216\n\t"
    "s_waitcnt lgkmcnt(9)\n\t"
    "v_smfmac_f32_16x16x64_bf16 v[20:23], v[44:47], v[128:135], v8\n\t"
    "ds_read_b128 v[44:47], v6 offset:10240\n\t"
    "s_barrier\n\t"
    "s_setprio 0\n\t"
    "s_waitcnt lgkmcnt(9)\n\t"
    "v_smfmac_f32_16x16x64_bf16 v[20:23], v[48:51], v[144:151], v8\n\t"
    "s_waitcnt lgkmcnt(8)\n\t"
    "v_smfmac_f32_16x16x64_bf16 v[20:23], v[52:55], v[136:143], v8\n\t"
    "s_waitcnt lgkmcnt(7)\n\t"
    "v_smfmac_f32_16x16x64_bf16 v[20:23], v[56:59], v[152:159], v8\n\t"
    "s_waitcnt lgkmcnt(6)\n\t"
    "v_smfmac_f32_16x16x64_bf16 v[20:23], v[60:63], v[160:167], v8\n\t"
    "s_waitcnt lgkmcnt(5)\n\t"
    "v_smfmac_f32_16x16x64_bf16 v[20:23], v[28:31], v[176:183], v8\n\t"
    "s_waitcnt lgkmcnt(4)\n\t"
    "v_smfmac_f32_16x16x64_bf16 v[20:23], v[24:27], v[168:175], v8\n\t"
    "s_waitcnt lgkmcnt(3)\n\t"
    "v_smfmac_f32_16x16x64_bf16 v[20:23], v[32:35], v[184:191], v8\n\t"
    "s_waitcnt lgkmcnt(2)\n\t"
    "v_smfmac_f32_16x16x64_bf16 v[20:23], v[36:39], v[192:199], v8\n\t"
    "s_waitcnt lgkmcnt(1)\n\t"
    "v_smfmac_f32_16x16x64_bf16 v[20:23], v[40:43], v[208:215], v8\n\t"
    "s_waitcnt lgkmcnt(0)\n\t"
    "v_smfmac_f32_16x16x64_bf16 v[20:23], v[44:47], v[200:207], v8\n\t"
    "s_nop 7\n\t"
    "v_add_f32 v20, v20, v22\n\t"
    "v_add_f32 v21, v21, v23\n\t"
    "v_cvt_pk_bf16_f32 v20, v20, v21\n\t"
    "v_min_u32 v20, -2, v20\n\t"
    "ds_write_b32 v9, v20 offset:19200\n\t"
    "s_waitcnt lgkmcnt(0)\n\t"
    "s_setprio 0\n\t"
    "s_waitcnt lgkmcnt(0)\n\t"
    "s_branch 9f\n\t"
    "1:\n\t"
    "s_add_u32 %[sp], %[sp], 1\n\t"
    "s_cmp_ge_u32 %[sp], %[cap]\n\t"
    "s_cbranch_scc1 7f\n\t"
    "global_load_dwordx4 v[24:27], v6, %[ag] sc1\n\t"
    "global_load_dwordx4 v[28:31], v6, %[ag] offset:-4096 sc1\n\t"
    "s_waitcnt lgkmcnt(0)\n\t"
    "s_memtime %[tc]\n\t"
    "s_nop 7\n\t"
    "s_branch 3b\n\t"
    "7:\n\t"
    "s_mov_b32 %[cap], 0\n\t"
    "s_branch 2b\n\t"
    "9:"
    : [tc] "=&s"(tc), [sp] "=&s"(sp), [cap] "+s"(cap)
    : [ag] "s"(ag), [c44] "s"(0x4444u), [lsc] "s"(LSC), [sn] "s"(sn), [vs] "s"(vs), "{v[64:95]}"(W.w0), "{v[96:127]}"(W.w1), "{v[128:159]}"(W.w2), "{v[160:191]}"(W.w3), "{v[192:223]}"(W.w4), "{v[224:255]}"(W.w5)
    : "v6", "v7", "v8", "v9", "v10", "v11", "v12", "v13", "v14", "v15", "v16", "v17", "v18", "v19", "v20", "v21", "v22", "v23", "v24", "v25", "v26", "v27", "v28", "v29", "v30", "v31", "v32", "v33", "v34", "v35", "v36", "v37", "v38", "v39", "v40", "v41", "v42", "v43", "v44", "v45", "v46", "v47", "v48", "v49", "v50", "v51", "v52", "v53", "v54", "v55", "v56", "v57", "v58", "v59", "v60", "v61", "v62", "v63", "vcc", "scc", "memory");
}
template <int W_> __device__ __forceinline__ void seg_attn(const QKW& W, unsigned pb, const char* od, unsigned long long* ts) { }
template <> __device__ __forceinline__ void seg_attn<0>(const QKW& W, unsigned pb, const char* od, unsigned long long* ts) {
  asm volatile("v_mbcnt_lo_u32_b32 v32, -1, 0\n\t"
    "v_mbcnt_hi_u32_b32 v32, -1, v32\n\t"
    "v_lshlrev_b32 v35, 4, v32\n\t"
    "ds_read_b128 v[24:27], v35 offset:20480\n\t"
    "ds_read_b128 v[28:31], v35 offset:21504\n\t"
    "v_lshrrev_b32 v33, 4, v32\n\t"
    "v_and_b32 v34, 15, v32\n\t"
    "v_xor_b32 v4, v33, v34\n\t"
    "v_lshl_or_b32 v4, v4, 4, %[pb]\n\t"
    "v_lshl_or_b32 v4, v34, 8, v4\n\t"
    "ds_read_b128 v[8:11], v4 offset:49152\n\t"
    "v_xor_b32 v5, 0x40, v4\n\t"
    "ds_read_b128 v[12:15], v5 offset:49152\n\t"
    "v_xor_b32 v6, 0x80, v4\n\t"
    "ds_read_b128 v[16:19], v6 offset:49152\n\t"
    "v_xor_b32 v7, 0xc0, v4\n\t"
    "ds_read_b128 v[20:23], v7 offset:49152\n\t"
    "ds_read_b128 v[40:43], v4 offset:32768\n\t"
    "ds_read_b128 v[44:47], v5 offset:32768\n\t"
    "ds_read_b128 v[48:51], v6 offset:32768\n\t"
    "ds_read_b128 v[52:55], v7 offset:32768\n\t"
    "v_lshlrev_b32 v35, 2, v33\n\t"
    "v_sub_u32 v35, v34, v35\n\t"
    "v_ashrrev_i32 v60, 31, v35\n\t"
    "v_and_b32 v60, %[neg], v60\n\t"
    "v_add_u32 v61, -1, v35\n\t"
    "v_ashrrev_i32 v61, 31, v61\n\t"
    "v_and_b32 v61, %[neg], v61\n\t"
    "v_add_u32 v62, -2, v35\n\t"
    "v_ashrrev_i32 v62, 31, v62\n\t"
    "v_and_b32 v62, %[neg], v62\n\t"
    "v_add_u32 v63, -3, v35\n\t"
    "v_ashrrev_i32 v63, 31, v63\n\t"
    "v_and_b32 v63, %[neg], v63\n\t"
    "s_waitcnt lgkmcnt(6)\n\t"
    "v_mfma_f32_16x16x32_bf16 v[32:35], v[24:27], v[8:11], 0\n\t"
    "v_mfma_f32_16x16x32_bf16 v[36:39], v[24:27], v[12:15], 0\n\t"
    "s_waitcnt lgkmcnt(4)\n\t"
    "v_mfma_f32_16x16x32_bf16 v[32:35], v[28:31], v[16:19], v[32:35]\n\t"
    "v_mfma_f32_16x16x32_bf16 v[36:39], v[28:31], v[20:23], v[36:39]\n\t"
    "s_waitcnt lgkmcnt(3)\n\t"
    "v_mfma_f32_16x16x32_bf16 v[60:63], v[40:43], v[8:11], v[60:63]\n\t"
    "s_waitcnt lgkmcnt(2)\n\t"
    "v_mfma_f32_16x16x32_bf16 v[60:63], v[44:47], v[12:15], v[60:63]\n\t"
    "v_mbcnt_lo_u32_b32 v7, -1, 0\n\t"
    "v_mbcnt_hi_u32_b32 v7, -1, v7\n\t"
    "s_waitcnt lgkmcnt(1)\n\t"
    "v_mfma_f32_16x16x32_bf16 v[60:63], v[48:51], v[16:19], v[60:63]\n\t"
    "v_and_or_b32 v4, v7, 3, 0\n\t"
    "v_lshrrev_b32 v6, 2, v7\n\t"
    "s_waitcnt lgkmcnt(0)\n\t"
    "v_mfma_f32_16x16x32_bf16 v[60:63], v[52:55], v[20:23], v[60:63]\n\t"
    "v_xor_b32 v4, v4, v6\n\t"
    "v_lshl_or_b32 v4, v4, 4, %[pb]\n\t"
    "v_lshl_or_b32 v4, v6, 8, v4\n\t"
    "v_xor_b32 v5, 64, v4\n\t"
    "ds_read_b64_tr_b16 v[48:49], v4 offset:40960\n\t"
    "ds_read_b64_tr_b16 v[52:53], v4 offset:40968\n\t"
    "v_add_f32 v32, v32, v38\n\t"
    "v_add_f32 v33, v33, v39\n\t"
    "v_and_b32 v6, 12, v7\n\t"
    "v_and_b32 v4, 3, v7\n\t"
    "v_lshl_add_u32 v4, v6, 1, v4\n\t"
    "v_lshrrev_b32 v6, 4, v7\n\t"
    "v_lshl_add_u32 v4, v4, 2, v6\n\t"
    "v_lshlrev_b32 v4, 2, v4\n\t"
    "v_add_u32 v4, 18432, v4\n\t"
    "ds_read_b64_tr_b16 v[40:41], v5 offset:40960\n\t"
    "ds_read_b64_tr_b16 v[44:45], v5 offset:40968\n\t"
    "ds_read2_b32 v[36:37], v4 offset0:0 offset1:16\n\t"
    "ds_read2_b32 v[38:39], v4 offset0:128 offset1:144\n\t"
    "v_and_b32 v4, 7, v7\n\t"
    "v_xor_b32 v6, v6, v4\n\t"
    "v_max3_f32 v34, v32, v33, %[lo]\n\t"
    "v_max3_f32 v34, v34, v60, v61\n\t"
    "v_max3_f32 v34, v34, v62, v63\n\t"
    "v_mov_b32 v35, v34\n\t"
    "v_lshlrev_b32 v6, 4, v6\n\t"
    "v_and_b32 v4, 15, v7\n\t"
    "v_permlane16_swap_b32 v34, v35\n\t"
    "v_max_f32 v34, v34, v35\n\t"
    "v_mov_b32 v35, v34\n\t"
    "v_lshl_or_b32 v6, v4, 7, v6\n\t"
    "v_xor_b32 v4, 64, v6\n\t"
    "v_permlane32_swap_b32 v34, v35\n\t"
    "v_max_f32 v34, v34, v35\n\t"
    "v_mul_f32 v34, %[nl], v34\n\t"
    "v_fma_f32 v32, v32, %[lsc], v34\n\t"
    "v_fma_f32 v33, v33, %[lsc], v34\n\t"
    "v_fma_f32 v60, v60, %[lsc], v34\n\t"
    "v_fma_f32 v61, v61, %[lsc], v34\n\t"
    "v_fma_f32 v62, v62, %[lsc], v34\n\t"
    "v_fma_f32 v63, v63, %[lsc], v34\n\t"
    "v_exp_f32 v60, v60\n\t"
    "v_exp_f32 v61, v61\n\t"
    "v_exp_f32 v62, v62\n\t"
    "v_exp_f32 v63, v63\n\t"
    "v_exp_f32 v32, v32\n\t"
    "v_exp_f32 v33, v33\n\t"
    "v_cvt_pk_bf16_f32 v24, v60, v61\n\t"
    "v_cvt_pk_bf16_f32 v25, v62, v63\n\t"
    "v_cvt_pk_bf16_f32 v28, v32, v33\n\t"
    "s_waitcnt lgkmcnt(0)\n\t"
    "v_mfma_f32_16x16x16_bf16 v[8:11], v[48:49], v[24:25], 0\n\t"
    "v_mov_b32 v56, %[one]\n\t"
    "v_mov_b32 v57, %[one]\n\t"
    "v_mfma_f32_16x16x16_bf16 v[12:15], v[52:53], v[24:25], 0\n\t"
    "v_mov_b32 v58, %[one]\n\t"
    "v_mov_b32 v59, %[one]\n\t"
    "v_mfma_f32_16x16x16_bf16 v[16:19], v[40:41], v[24:25], 0\n\t"
    "v_mov_b32 v29, 0\n\t"
    "v_mov_b32 v30, 0\n\t"
    "v_mfma_f32_16x16x16_bf16 v[60:63], v[56:57], v[24:25], 0\n\t"
    "v_mov_b32 v31, v28\n\t"
    "v_mfma_f32_16x16x16_bf16 v[20:23], v[44:45], v[24:25], 0\n\t"
    "v_mfma_f32_16x16x16_bf16 v[8:11], v[36:37], v[28:29], v[8:11]\n\t"
    "v_mfma_f32_16x16x16_bf16 v[12:15], v[36:37], v[30:31], v[12:15]\n\t"
    "s_nop 1\n\t"
    "v_mfma_f32_16x16x16_bf16 v[16:19], v[38:39], v[28:29], v[16:19]\n\t"
    "v_mfma_f32_16x16x16_bf16 v[60:63], v[56:57], v[28:29], v[60:63]\n\t"
    "s_nop 1\n\t"
    "v_mfma_f32_16x16x16_bf16 v[20:23], v[38:39], v[30:31], v[20:23]\n\t"
    "s_nop 4\n\t"
    "v_rcp_f32 v60, v60\n\t"
    "s_nop 0\n\t"
    "v_pk_mul_f32 v[8:9], v[8:9], v[60:61] op_sel_hi:[1,0]\n\t"
    "v_pk_mul_f32 v[10:11], v[10:11], v[60:61] op_sel_hi:[1,0]\n\t"
    "v_pk_mul_f32 v[12:13], v[12:13], v[60:61] op_sel_hi:[1,0]\n\t"
    "v_pk_mul_f32 v[14:15], v[14:15], v[60:61] op_sel_hi:[1,0]\n\t"
    "v_cvt_pk_bf16_f32 v24, v8, v9\n\t"
    "v_cvt_pk_bf16_f32 v25, v10, v11\n\t"
    "v_cvt_pk_bf16_f32 v26, v12, v13\n\t"
    "v_cvt_pk_bf16_f32 v27, v14, v15\n\t"
    "global_store_dwordx4 v6, v[24:27], %[od] offset:0 sc1\n\t"
    "v_pk_mul_f32 v[16:17], v[16:17], v[60:61] op_sel_hi:[1,0]\n\t"
    "v_pk_mul_f32 v[18:19], v[18:19], v[60:61] op_sel_hi:[1,0]\n\t"
    "v_pk_mul_f32 v[20:21], v[20:21], v[60:61] op_sel_hi:[1,0]\n\t"
    "v_pk_mul_f32 v[22:23], v[22:23], v[60:61] op_sel_hi:[1,0]\n\t"
    "v_cvt_pk_bf16_f32 v28, v16, v17\n\t"
    "v_cvt_pk_bf16_f32 v29, v18, v19\n\t"
    "v_cvt_pk_bf16_f32 v30, v20, v21\n\t"
    "v_cvt_pk_bf16_f32 v31, v22, v23\n\t"
    "global_store_dwordx4 v4, v[28:31], %[od] offset:0 sc1\n\t"
    "s_waitcnt lgkmcnt(0)\n\t"
    "s_nop 0"
    :
    : [pb] "s"(pb), [od] "s"(od), [neg] "s"(-3.0e38f), [lo] "s"(-1.0e38f), [one] "s"(0x3F803F80u), [lsc] "s"(LSC), [nl] "s"(-LSC), "{v[64:95]}"(W.w0), "{v[96:127]}"(W.w1), "{v[128:159]}"(W.w2), "{v[160:191]}"(W.w3), "{v[192:223]}"(W.w4), "{v[224:255]}"(W.w5)
    : "v4", "v5", "v6", "v7", "v8", "v9", "v10", "v11", "v12", "v13", "v14", "v15", "v16", "v17", "v18", "v19", "v20", "v21", "v22", "v23", "v24", "v25", "v26", "v27", "v28", "v29", "v30", "v31", "v32", "v33", "v34", "v35", "v36", "v37", "v38", "v39", "v40", "v41", "v42", "v43", "v44", "v45", "v46", "v47", "v48", "v49", "v50", "v51", "v52", "v53", "v54", "v55", "v56", "v57", "v58", "v59", "v60", "v61", "v62", "v63", "memory");
}
template <> __device__ __forceinline__ void seg_attn<1>(const QKW& W, unsigned pb, const char* od, unsigned long long* ts) {
  asm volatile("v_mbcnt_lo_u32_b32 v32, -1, 0\n\t"
    "v_mbcnt_hi_u32_b32 v32, -1, v32\n\t"
    "v_lshlrev_b32 v35, 4, v32\n\t"
    "ds_read_b128 v[24:27], v35 offset:20480\n\t"
    "ds_read_b128 v[28:31], v35 offset:21504\n\t"
    "v_lshrrev_b32 v33, 4, v32\n\t"
    "v_and_b32 v34, 15, v32\n\t"
    "v_xor_b32 v4, v33, v34\n\t"
    "v_lshl_or_b32 v4, v4, 4, %[pb]\n\t"
    "v_lshl_or_b32 v4, v34, 8, v4\n\t"
    "ds_read_b128 v[8:11], v4 offset:53248\n\t"
    "v_xor_b32 v5, 0x40, v4\n\t"
    "ds_read_b128 v[12:15], v5 offset:53248\n\t"
    "v_xor_b32 v6, 0x80, v4\n\t"
    "ds_read_b128 v[16:19], v6 offset:53248\n\t"
    "v_xor_b32 v7, 0xc0, v4\n\t"
    "ds_read_b128 v[20:23], v7 offset:53248\n\t"
    "ds_read_b128 v[40:43], v4 offset:32768\n\t"
    "ds_read_b128 v[44:47], v5 offset:32768\n\t"
    "ds_read_b128 v[48:51], v6 offset:32768\n\t"
    "ds_read_b128 v[52:55], v7 offset:32768\n\t"
    "v_lshlrev_b32 v35, 2, v33\n\t"
    "v_sub_u32 v35, v34, v35\n\t"
    "v_ashrrev_i32 v60, 31, v35\n\t"
    "v_and_b32 v60, %[neg], v60\n\t"
    "v_add_u32 v61, -1, v35\n\t"
    "v_ashrrev_i32 v61, 31, v61\n\t"
    "v_and_b32 v61, %[neg], v61\n\t"
    "v_add_u32 v62, -2, v35\n\t"
    "v_ashrrev_i32 v62, 31, v62\n\t"
    "v_and_b32 v62, %[neg], v62\n\t"
    "v_add_u32 v63, -3, v35\n\t"
    "v_ashrrev_i32 v63, 31, v63\n\t"
    "v_and_b32 v63, %[neg], v63\n\t"
    "s_waitcnt lgkmcnt(6)\n\t"
    "v_mfma_f32_16x16x32_bf16 v[32:35], v[24:27], v[8:11], 0\n\t"
    "v_mfma_f32_16x16x32_bf16 v[36:39], v[24:27], v[12:15], 0\n\t"
    "s_waitcnt lgkmcnt(4)\n\t"
    "v_mfma_f32_16x16x32_bf16 v[32:35], v[28:31], v[16:19], v[32:35]\n\t"
    "v_mfma_f32_16x16x32_bf16 v[36:39], v[28:31], v[20:23], v[36:39]\n\t"
    "ds_read_b128 v[24:27], v4 offset:36864\n\t"
    "ds_read_b128 v[28:31], v5 offset:36864\n\t"
    "s_waitcnt lgkmcnt(5)\n\t"
    "v_mfma_f32_16x16x32_bf16 v[56:59], v[40:43], v[8:11], 0\n\t"
    "s_waitcnt lgkmcnt(4)\n\t"
    "v_mfma_f32_16x16x32_bf16 v[56:59], v[44:47], v[12:15], v[56:59]\n\t"
    "ds_read_b128 v[40:43], v6 offset:36864\n\t"
    "ds_read_b128 v[44:47], v7 offset:36864\n\t"
    "v_mbcnt_lo_u32_b32 v7, -1, 0\n\t"
    "v_mbcnt_hi_u32_b32 v7, -1, v7\n\t"
    "s_waitcnt lgkmcnt(5)\n\t"
    "v_mfma_f32_16x16x32_bf16 v[56:59], v[48:51], v[16:19], v[56:59]\n\t"
    "v_and_or_b32 v4, v7, 3, 0\n\t"
    "v_lshrrev_b32 v6, 2, v7\n\t"
    "s_waitcnt lgkmcnt(4)\n\t"
    "v_mfma_f32_16x16x32_bf16 v[56:59], v[52:55], v[20:23], v[56:59]\n\t"
    "v_xor_b32 v4, v4, v6\n\t"
    "v_lshl_or_b32 v4, v4, 4, %[pb]\n\t"
    "v_lshl_or_b32 v4, v6, 8, v4\n\t"
    "v_xor_b32 v5, 64, v4\n\t"
    "ds_read_b64_tr_b16 v[48:49], v4 offset:40960\n\t"
    "ds_read_b64_tr_b16 v[50:51], v4 offset:45056\n\t"
    "ds_read_b64_tr_b16 v[52:53], v4 offset:40968\n\t"
    "ds_read_b64_tr_b16 v[54:55], v4 offset:45064\n\t"
    "v_add_f32 v32, v32, v38\n\t"
    "v_add_f32 v33, v33, v39\n\t"
    "s_waitcnt lgkmcnt(7)\n\t"
    "v_mfma_f32_16x16x32_bf16 v[60:63], v[24:27], v[8:11], v[60:63]\n\t"
    "v_and_b32 v6, 12, v7\n\t"
    "v_and_b32 v4, 3, v7\n\t"
    "s_waitcnt lgkmcnt(6)\n\t"
    "v_mfma_f32_16x16x32_bf16 v[60:63], v[28:31], v[12:15], v[60:63]\n\t"
    "v_lshl_add_u32 v4, v6, 1, v4\n\t"
    "v_lshrrev_b32 v6, 4, v7\n\t"
    "s_waitcnt lgkmcnt(5)\n\t"
    "v_mfma_f32_16x16x32_bf16 v[60:63], v[40:43], v[16:19], v[60:63]\n\t"
    "v_lshl_add_u32 v4, v4, 2, v6\n\t"
    "v_lshlrev_b32 v4, 2, v4\n\t"
    "s_waitcnt lgkmcnt(4)\n\t"
    "v_mfma_f32_16x16x32_bf16 v[60:63], v[44:47], v[20:23], v[60:63]\n\t"
    "v_add_u32 v4, 18432, v4\n\t"
    "ds_read_b64_tr_b16 v[40:41], v5 offset:40960\n\t"
    "ds_read_b64_tr_b16 v[42:43], v5 offset:45056\n\t"
    "ds_read_b64_tr_b16 v[44:45], v5 offset:40968\n\t"
    "ds_read_b64_tr_b16 v[46:47], v5 offset:45064\n\t"
    "ds_read2_b32 v[36:37], v4 offset0:0 offset1:16\n\t"
    "ds_read2_b32 v[38:39], v4 offset0:128 offset1:144\n\t"
    "v_and_b32 v4, 7, v7\n\t"
    "v_xor_b32 v6, v6, v4\n\t"
    "v_max3_f32 v34, v32, v33, %[lo]\n\t"
    "v_max3_f32 v34, v34, v56, v57\n\t"
    "v_max3_f32 v34, v34, v58, v59\n\t"
    "v_max3_f32 v34, v34, v60, v61\n\t"
    "v_max3_f32 v34, v34, v62, v63\n\t"
    "v_mov_b32 v35, v34\n\t"
    "v_lshlrev_b32 v6, 4, v6\n\t"
    "v_and_b32 v4, 15, v7\n\t"
    "v_permlane16_swap_b32 v34, v35\n\t"
    "v_max_f32 v34, v34, v35\n\t"
    "v_mov_b32 v35, v34\n\t"
    "v_lshl_or_b32 v6, v4, 7, v6\n\t"
    "v_xor_b32 v4, 64, v6\n\t"
    "v_permlane32_swap_b32 v34, v35\n\t"
    "v_max_f32 v34, v34, v35\n\t"
    "v_mul_f32 v34, %[nl], v34\n\t"
    "v_fma_f32 v32, v32, %[lsc], v34\n\t"
    "v_fma_f32 v33, v33, %[lsc], v34\n\t"
    "v_fma_f32 v56, v56, %[lsc], v34\n\t"
    "v_fma_f32 v57, v57, %[lsc], v34\n\t"
    "v_fma_f32 v58, v58, %[lsc], v34\n\t"
    "v_fma_f32 v59, v59, %[lsc], v34\n\t"
    "v_fma_f32 v60, v60, %[lsc], v34\n\t"
    "v_fma_f32 v61, v61, %[lsc], v34\n\t"
    "v_fma_f32 v62, v62, %[lsc], v34\n\t"
    "v_fma_f32 v63, v63, %[lsc], v34\n\t"
    "v_exp_f32 v56, v56\n\t"
    "v_exp_f32 v57, v57\n\t"
    "v_exp_f32 v58, v58\n\t"
    "v_exp_f32 v59, v59\n\t"
    "v_exp_f32 v60, v60\n\t"
    "v_exp_f32 v61, v61\n\t"
    "v_exp_f32 v62, v62\n\t"
    "v_exp_f32 v63, v63\n\t"
    "v_exp_f32 v32, v32\n\t"
    "v_exp_f32 v33, v33\n\t"
    "v_cvt_pk_bf16_f32 v24, v56, v57\n\t"
    "v_cvt_pk_bf16_f32 v25, v58, v59\n\t"
    "v_cvt_pk_bf16_f32 v26, v60, v61\n\t"
    "v_cvt_pk_bf16_f32 v27, v62, v63\n\t"
    "v_cvt_pk_bf16_f32 v28, v32, v33\n\t"
    "s_waitcnt lgkmcnt(0)\n\t"
    "v_mfma_f32_16x16x32_bf16 v[8:11], v[48:51], v[24:27], 0\n\t"
    "v_mov_b32 v56, %[one]\n\t"
    "v_mov_b32 v57, %[one]\n\t"
    "v_mfma_f32_16x16x32_bf16 v[12:15], v[52:55], v[24:27], 0\n\t"
    "v_mov_b32 v58, %[one]\n\t"
    "v_mov_b32 v59, %[one]\n\t"
    "v_mfma_f32_16x16x32_bf16 v[16:19], v[40:43], v[24:27], 0\n\t"
    "v_mov_b32 v29, 0\n\t"
    "v_mov_b32 v30, 0\n\t"
    "v_mfma_f32_16x16x32_bf16 v[60:63], v[56:59], v[24:27], 0\n\t"
    "v_mov_b32 v31, v28\n\t"
    "v_mfma_f32_16x16x32_bf16 v[20:23], v[44:47], v[24:27], 0\n\t"
    "v_mfma_f32_16x16x16_bf16 v[8:11], v[36:37], v[28:29], v[8:11]\n\t"
    "v_mfma_f32_16x16x16_bf16 v[12:15], v[36:37], v[30:31], v[12:15]\n\t"
    "s_nop 1\n\t"
    "v_mfma_f32_16x16x16_bf16 v[16:19], v[38:39], v[28:29], v[16:19]\n\t"
    "v_mfma_f32_16x16x16_bf16 v[60:63], v[56:57], v[28:29], v[60:63]\n\t"
    "s_nop 1\n\t"
    "v_mfma_f32_16x16x16_bf16 v[20:23], v[38:39], v[30:31], v[20:23]\n\t"
    "s_nop 4\n\t"
    "v_rcp_f32 v60, v60\n\t"
    "s_nop 0\n\t"
    "v_pk_mul_f32 v[8:9], v[8:9], v[60:61] op_sel_hi:[1,0]\n\t"
    "v_pk_mul_f32 v[10:11], v[10:11], v[60:61] op_sel_hi:[1,0]\n\t"
    "v_pk_mul_f32 v[12:13], v[12:13], v[60:61] op_sel_hi:[1,0]\n\t"
    "v_pk_mul_f32 v[14:15], v[14:15], v[60:61] op_sel_hi:[1,0]\n\t"
    "v_cvt_pk_bf16_f32 v24, v8, v9\n\t"
    "v_cvt_pk_bf16_f32 v25, v10, v11\n\t"
    "v_cvt_pk_bf16_f32 v26, v12, v13\n\t"
    "v_cvt_pk_bf16_f32 v27, v14, v15\n\t"
    "global_store_dwordx4 v6, v[24:27], %[od] offset:2048 sc1\n\t"
    "v_pk_mul_f32 v[16:17], v[16:17], v[60:61] op_sel_hi:[1,0]\n\t"
    "v_pk_mul_f32 v[18:19], v[18:19], v[60:61] op_sel_hi:[1,0]\n\t"
    "v_pk_mul_f32 v[20:21], v[20:21], v[60:61] op_sel_hi:[1,0]\n\t"
    "v_pk_mul_f32 v[22:23], v[22:23], v[60:61] op_sel_hi:[1,0]\n\t"
    "v_cvt_pk_bf16_f32 v28, v16, v17\n\t"
    "v_cvt_pk_bf16_f32 v29, v18, v19\n\t"
    "v_cvt_pk_bf16_f32 v30, v20, v21\n\t"
    "v_cvt_pk_bf16_f32 v31, v22, v23\n\t"
    "global_store_dwordx4 v4, v[28:31], %[od] offset:2048 sc1\n\t"
    "s_waitcnt lgkmcnt(0)\n\t"
    "s_nop 0"
    :
    : [pb] "s"(pb), [od] "s"(od), [neg] "s"(-3.0e38f), [lo] "s"(-1.0e38f), [one] "s"(0x3F803F80u), [lsc] "s"(LSC), [nl] "s"(-LSC), "{v[64:95]}"(W.w0), "{v[96:127]}"(W.w1), "{v[128:159]}"(W.w2), "{v[160:191]}"(W.w3), "{v[192:223]}"(W.w4), "{v[224:255]}"(W.w5)
    : "v4", "v5", "v6", "v7", "v8", "v9", "v10", "v11", "v12", "v13", "v14", "v15", "v16", "v17", "v18", "v19", "v20", "v21", "v22", "v23", "v24", "v25", "v26", "v27", "v28", "v29", "v30", "v31", "v32", "v33", "v34", "v35", "v36", "v37", "v38", "v39", "v40", "v41", "v42", "v43", "v44", "v45", "v46", "v47", "v48", "v49", "v50", "v51", "v52", "v53", "v54", "v55", "v56", "v57", "v58", "v59", "v60", "v61", "v62", "v63", "memory");
}
template <> __device__ __forceinline__ void seg_attn<2>(const QKW& W, unsigned pb, const char* od, unsigned long long* ts) {
  asm volatile("v_mbcnt_lo_u32_b32 v32, -1, 0\n\t"
    "v_mbcnt_hi_u32_b32 v32, -1, v32\n\t"
    "v_lshlrev_b32 v35, 4, v32\n\t"
    "ds_read_b128 v[24:27], v35 offset:20480\n\t"
    "ds_read_b128 v[28:31], v35 offset:21504\n\t"
    "v_lshrrev_b32 v33, 4, v32\n\t"
    "v_and_b32 v34, 15, v32\n\t"
    "v_xor_b32 v4, v33, v34\n\t"
    "v_lshl_or_b32 v4, v4, 4, %[pb]\n\t"
    "v_lshl_or_b32 v4, v34, 8, v4\n\t"
    "ds_read_b128 v[8:11], v4 offset:49152\n\t"
    "v_xor_b32 v5, 0x40, v4\n\t"
    "ds_read_b128 v[12:15], v5 offset:49152\n\t"
    "v_xor_b32 v6, 0x80, v4\n\t"
    "ds_read_b128 v[16:19], v6 offset:49152\n\t"
    "v_xor_b32 v7, 0xc0, v4\n\t"
    "ds_read_b128 v[20:23], v7 offset:49152\n\t"
    "ds_read_b128 v[40:43], v4 offset:32768\n\t"
    "ds_read_b128 v[44:47], v5 offset:32768\n\t"
    "ds_read_b128 v[48:51], v6 offset:32768\n\t"
    "ds_read_b128 v[52:55], v7 offset:32768\n\t"
    "v_lshlrev_b32 v35, 2, v33\n\t"
    "v_sub_u32 v35, v34, v35\n\t"
    "v_ashrrev_i32 v60, 31, v35\n\t"
    "v_and_b32 v60, %[neg], v60\n\t"
    "v_add_u32 v61, -1, v35\n\t"
    "v_ashrrev_i32 v61, 31, v61\n\t"
    "v_and_b32 v61, %[neg], v61\n\t"
    "v_add_u32 v62, -2, v35\n\t"
    "v_ashrrev_i32 v62, 31, v62\n\t"
    "v_and_b32 v62, %[neg], v62\n\t"
    "v_add_u32 v63, -3, v35\n\t"
    "v_ashrrev_i32 v63, 31, v63\n\t"
    "v_and_b32 v63, %[neg], v63\n\t"
    "s_waitcnt lgkmcnt(6)\n\t"
    "v_mfma_f32_16x16x32_bf16 v[32:35], v[24:27], v[8:11], 0\n\t"
    "v_mfma_f32_16x16x32_bf16 v[36:39], v[24:27], v[12:15], 0\n\t"
    "s_waitcnt lgkmcnt(4)\n\t"
    "v_mfma_f32_16x16x32_bf16 v[32:35], v[28:31], v[16:19], v[32:35]\n\t"
    "v_mfma_f32_16x16x32_bf16 v[36:39], v[28:31], v[20:23], v[36:39]\n\t"
    "s_waitcnt lgkmcnt(3)\n\t"
    "v_mfma_f32_16x16x32_bf16 v[60:63], v[40:43], v[8:11], v[60:63]\n\t"
    "s_waitcnt lgkmcnt(2)\n\t"
    "v_mfma_f32_16x16x32_bf16 v[60:63], v[44:47], v[12:15], v[60:63]\n\t"
    "v_mbcnt_lo_u32_b32 v7, -1, 0\n\t"
    "v_mbcnt_hi_u32_b32 v7, -1, v7\n\t"
    "s_waitcnt lgkmcnt(1)\n\t"
    "v_mfma_f32_16x16x32_bf16 v[60:63], v[48:51], v[16:19], v[60:63]\n\t"
    "v_and_or_b32 v4, v7, 3, 8\n\t"
    "v_lshrrev_b32 v6, 2, v7\n\t"
    "s_waitcnt lgkmcnt(0)\n\t"
    "v_mfma_f32_16x16x32_bf16 v[60:63], v[52:55], v[20:23], v[60:63]\n\t"
    "v_xor_b32 v4, v4, v6\n\t"
    "v_lshl_or_b32 v4, v4, 4, %[pb]\n\t"
    "v_lshl_or_b32 v4, v6, 8, v4\n\t"
    "v_xor_b32 v5, 64, v4\n\t"
    "ds_read_b64_tr_b16 v[48:49], v4 offset:40960\n\t"
    "ds_read_b64_tr_b16 v[52:53], v4 offset:40968\n\t"
    "v_add_f32 v32, v32, v38\n\t"
    "v_add_f32 v33, v33, v39\n\t"
    "v_and_b32 v6, 12, v7\n\t"
    "v_and_b32 v4, 3, v7\n\t"
    "v_lshl_add_u32 v4, v6, 1, v4\n\t"
    "v_lshrrev_b32 v6, 4, v7\n\t"
    "v_lshl_add_u32 v4, v4, 2, v6\n\t"
    "v_lshlrev_b32 v4, 2, v4\n\t"
    "v_add_u32 v4, 19456, v4\n\t"
    "ds_read_b64_tr_b16 v[40:41], v5 offset:40960\n\t"
    "ds_read_b64_tr_b16 v[44:45], v5 offset:40968\n\t"
    "ds_read2_b32 v[36:37], v4 offset0:0 offset1:16\n\t"
    "ds_read2_b32 v[38:39], v4 offset0:128 offset1:144\n\t"
    "v_and_b32 v4, 7, v7\n\t"
    "v_xor_b32 v6, v6, v4\n\t"
    "v_max3_f32 v34, v32, v33, %[lo]\n\t"
    "v_max3_f32 v34, v34, v60, v61\n\t"
    "v_max3_f32 v34, v34, v62, v63\n\t"
    "v_mov_b32 v35, v34\n\t"
    "v_lshlrev_b32 v6, 4, v6\n\t"
    "v_and_b32 v4, 15, v7\n\t"
    "v_permlane16_swap_b32 v34, v35\n\t"
    "v_max_f32 v34, v34, v35\n\t"
    "v_mov_b32 v35, v34\n\t"
    "v_lshl_or_b32 v6, v4, 7, v6\n\t"
    "v_xor_b32 v4, 64, v6\n\t"
    "v_permlane32_swap_b32 v34, v35\n\t"
    "v_max_f32 v34, v34, v35\n\t"
    "v_mul_f32 v34, %[nl], v34\n\t"
    "v_fma_f32 v32, v32, %[lsc], v34\n\t"
    "v_fma_f32 v33, v33, %[lsc], v34\n\t"
    "v_fma_f32 v60, v60, %[lsc], v34\n\t"
    "v_fma_f32 v61, v61, %[lsc], v34\n\t"
    "v_fma_f32 v62, v62, %[lsc], v34\n\t"
    "v_fma_f32 v63, v63, %[lsc], v34\n\t"
    "v_exp_f32 v60, v60\n\t"
    "v_exp_f32 v61, v61\n\t"
    "v_exp_f32 v62, v62\n\t"
    "v_exp_f32 v63, v63\n\t"
    "v_exp_f32 v32, v32\n\t"
    "v_exp_f32 v33, v33\n\t"
    "v_cvt_pk_bf16_f32 v24, v60, v61\n\t"
    "v_cvt_pk_bf16_f32 v25, v62, v63\n\t"
    "v_cvt_pk_bf16_f32 v28, v32, v33\n\t"
    "s_waitcnt lgkmcnt(0)\n\t"
    "v_mfma_f32_16x16x16_bf16 v[8:11], v[48:49], v[24:25], 0\n\t"
    "v_mov_b32 v56, %[one]\n\t"
    "v_mov_b32 v57, %[one]\n\t"
    "v_mfma_f32_16x16x16_bf16 v[12:15], v[52:53], v[24:25], 0\n\t"
    "v_mov_b32 v58, %[one]\n\t"
    "v_mov_b32 v59, %[one]\n\t"
    "v_mfma_f32_16x16x16_bf16 v[16:19], v[40:41], v[24:25], 0\n\t"
    "v_mov_b32 v29, 0\n\t"
    "v_mov_b32 v30, 0\n\t"
    "v_mfma_f32_16x16x16_bf16 v[60:63], v[56:57], v[24:25], 0\n\t"
    "v_mov_b32 v31, v28\n\t"
    "v_mfma_f32_16x16x16_bf16 v[20:23], v[44:45], v[24:25], 0\n\t"
    "v_mfma_f32_16x16x16_bf16 v[8:11], v[36:37], v[28:29], v[8:11]\n\t"
    "v_mfma_f32_16x16x16_bf16 v[12:15], v[36:37], v[30:31], v[12:15]\n\t"
    "s_nop 1\n\t"
    "v_mfma_f32_16x16x16_bf16 v[16:19], v[38:39], v[28:29], v[16:19]\n\t"
    "v_mfma_f32_16x16x16_bf16 v[60:63], v[56:57], v[28:29], v[60:63]\n\t"
    "s_nop 1\n\t"
    "v_mfma_f32_16x16x16_bf16 v[20:23], v[38:39], v[30:31], v[20:23]\n\t"
    "s_nop 4\n\t"
    "v_rcp_f32 v60, v60\n\t"
    "s_nop 0\n\t"
    "v_pk_mul_f32 v[8:9], v[8:9], v[60:61] op_sel_hi:[1,0]\n\t"
    "v_pk_mul_f32 v[10:11], v[10:11], v[60:61] op_sel_hi:[1,0]\n\t"
    "v_pk_mul_f32 v[12:13], v[12:13], v[60:61] op_sel_hi:[1,0]\n\t"
    "v_pk_mul_f32 v[14:15], v[14:15], v[60:61] op_sel_hi:[1,0]\n\t"
    "v_cvt_pk_bf16_f32 v24, v8, v9\n\t"
    "v_cvt_pk_bf16_f32 v25, v10, v11\n\t"
    "v_cvt_pk_bf16_f32 v26, v12, v13\n\t"
    "v_cvt_pk_bf16_f32 v27, v14, v15\n\t"
    "global_store_dwordx4 v6, v[24:27], %[od] offset:0 sc1\n\t"
    "v_pk_mul_f32 v[16:17], v[16:17], v[60:61] op_sel_hi:[1,0]\n\t"
    "v_pk_mul_f32 v[18:19], v[18:19], v[60:61] op_sel_hi:[1,0]\n\t"
    "v_pk_mul_f32 v[20:21], v[20:21], v[60:61] op_sel_hi:[1,0]\n\t"
    "v_pk_mul_f32 v[22:23], v[22:23], v[60:61] op_sel_hi:[1,0]\n\t"
    "v_cvt_pk_bf16_f32 v28, v16, v17\n\t"
    "v_cvt_pk_bf16_f32 v29, v18, v19\n\t"
    "v_cvt_pk_bf16_f32 v30, v20, v21\n\t"
    "v_cvt_pk_bf16_f32 v31, v22, v23\n\t"
    "global_store_dwordx4 v4, v[28:31], %[od] offset:0 sc1\n\t"
    "s_waitcnt lgkmcnt(0)\n\t"
    "s_nop 0"
    :
    : [pb] "s"(pb), [od] "s"(od), [neg] "s"(-3.0e38f), [lo] "s"(-1.0e38f), [one] "s"(0x3F803F80u), [lsc] "s"(LSC), [nl] "s"(-LSC), "{v[64:95]}"(W.w0), "{v[96:127]}"(W.w1), "{v[128:159]}"(W.w2), "{v[160:191]}"(W.w3), "{v[192:223]}"(W.w4), "{v[224:255]}"(W.w5)
    : "v4", "v5", "v6", "v7", "v8", "v9", "v10", "v11", "v12", "v13", "v14", "v15", "v16", "v17", "v18", "v19", "v20", "v21", "v22", "v23", "v24", "v25", "v26", "v27", "v28", "v29", "v30", "v31", "v32", "v33", "v34", "v35", "v36", "v37", "v38", "v39", "v40", "v41", "v42", "v43", "v44", "v45", "v46", "v47", "v48", "v49", "v50", "v51", "v52", "v53", "v54", "v55", "v56", "v57", "v58", "v59", "v60", "v61", "v62", "v63", "memory");
}
template <> __device__ __forceinline__ void seg_attn<3>(const QKW& W, unsigned pb, const char* od, unsigned long long* ts) {
  asm volatile("v_mbcnt_lo_u32_b32 v32, -1, 0\n\t"
    "v_mbcnt_hi_u32_b32 v32, -1, v32\n\t"
    "v_lshlrev_b32 v35, 4, v32\n\t"
    "ds_read_b128 v[24:27], v35 offset:20480\n\t"
    "ds_read_b128 v[28:31], v35 offset:21504\n\t"
    "v_lshrrev_b32 v33, 4, v32\n\t"
    "v_and_b32 v34, 15, v32\n\t"
    "v_xor_b32 v4, v33, v34\n\t"
    "v_lshl_or_b32 v4, v4, 4, %[pb]\n\t"
    "v_lshl_or_b32 v4, v34, 8, v4\n\t"
    "ds_read_b128 v[8:11], v4 offset:53248\n\t"
    "v_xor_b32 v5, 0x40, v4\n\t"
    "ds_read_b128 v[12:15], v5 offset:53248\n\t"
    "v_xor_b32 v6, 0x80, v4\n\t"
    "ds_read_b128 v[16:19], v6 offset:53248\n\t"
    "v_xor_b32 v7, 0xc0, v4\n\t"
    "ds_read_b128 v[20:23], v7 offset:53248\n\t"
    "ds_read_b128 v[40:43], v4 offset:32768\n\t"
    "ds_read_b128 v[44:47], v5 offset:32768\n\t"
    "ds_read_b128 v[48:51], v6 offset:32768\n\t"
    "ds_read_b128 v[52:55], v7 offset:32768\n\t"
    "v_lshlrev_b32 v35, 2, v33\n\t"
    "v_sub_u32 v35, v34, v35\n\t"
    "v_ashrrev_i32 v60, 31, v35\n\t"
    "v_and_b32 v60, %[neg], v60\n\t"
    "v_add_u32 v61, -1, v35\n\t"
    "v_ashrrev_i32 v61, 31, v61\n\t"
    "v_and_b32 v61, %[neg], v61\n\t"
    "v_add_u32 v62, -2, v35\n\t"
    "v_ashrrev_i32 v62, 31, v62\n\t"
    "v_and_b32 v62, %[neg], v62\n\t"
    "v_add_u32 v63, -3, v35\n\t"
    "v_ashrrev_i32 v63, 31, v63\n\t"
    "v_and_b32 v63, %[neg], v63\n\t"
    "s_waitcnt lgkmcnt(6)\n\t"
    "v_mfma_f32_16x16x32_bf16 v[32:35], v[24:27], v[8:11], 0\n\t"
    "v_mfma_f32_16x16x32_bf16 v[36:39], v[24:27], v[12:15], 0\n\t"
    "s_waitcnt lgkmcnt(4)\n\t"
    "v_mfma_f32_16x16x32_bf16 v[32:35], v[28:31], v[16:19], v[32:35]\n\t"
    "v_mfma_f32_16x16x32_bf16 v[36:39], v[28:31], v[20:23], v[36:39]\n\t"
    "ds_read_b128 v[24:27], v4 offset:36864\n\t"
    "ds_read_b128 v[28:31], v5 offset:36864\n\t"
    "s_waitcnt lgkmcnt(5)\n\t"
    "v_mfma_f32_16x16x32_bf16 v[56:59], v[40:43], v[8:11], 0\n\t"
    "s_waitcnt lgkmcnt(4)\n\t"
    "v_mfma_f32_16x16x32_bf16 v[56:59], v[44:47], v[12:15], v[56:59]\n\t"
    "ds_read_b128 v[40:43], v6 offset:36864\n\t"
    "ds_read_b128 v[44:47], v7 offset:36864\n\t"
    "v_mbcnt_lo_u32_b32 v7, -1, 0\n\t"
    "v_mbcnt_hi_u32_b32 v7, -1, v7\n\t"
    "s_waitcnt lgkmcnt(5)\n\t"
    "v_mfma_f32_16x16x32_bf16 v[56:59], v[48:51], v[16:19], v[56:59]\n\t"
    "v_and_or_b32 v4, v7, 3, 8\n\t"
    "v_lshrrev_b32 v6, 2, v7\n\t"
    "s_waitcnt lgkmcnt(4)\n\t"
    "v_mfma_f32_16x16x32_bf16 v[56:59], v[52:55], v[20:23], v[56:59]\n\t"
    "v_xor_b32 v4, v4, v6\n\t"
    "v_lshl_or_b32 v4, v4, 4, %[pb]\n\t"
    "v_lshl_or_b32 v4, v6, 8, v4\n\t"
    "v_xor_b32 v5, 64, v4\n\t"
    "ds_read_b64_tr_b16 v[48:49], v4 offset:40960\n\t"
    "ds_read_b64_tr_b16 v[50:51], v4 offset:45056\n\t"
    "ds_read_b64_tr_b16 v[52:53], v4 offset:40968\n\t"
    "ds_read_b64_tr_b16 v[54:55], v4 offset:45064\n\t"
    "v_add_f32 v32, v32, v38\n\t"
    "v_add_f32 v33, v33, v39\n\t"
    "s_waitcnt lgkmcnt(7)\n\t"
    "v_mfma_f32_16x16x32_bf16 v[60:63], v[24:27], v[8:11], v[60:63]\n\t"
    "v_and_b32 v6, 12, v7\n\t"
    "v_and_b32 v4, 3, v7\n\t"
    "s_waitcnt lgkmcnt(6)\n\t"
    "v_mfma_f32_16x16x32_bf16 v[60:63], v[28:31], v[12:15], v[60:63]\n\t"
    "v_lshl_add_u32 v4, v6, 1, v4\n\t"
    "v_lshrrev_b32 v6, 4, v7\n\t"
    "s_waitcnt lgkmcnt(5)\n\t"
    "v_mfma_f32_16x16x32_bf16 v[60:63], v[40:43], v[16:19], v[60:63]\n\t"
    "v_lshl_add_u32 v4, v4, 2, v6\n\t"
    "v_lshlrev_b32 v4, 2, v4\n\t"
    "s_waitcnt lgkmcnt(4)\n\t"
    "v_mfma_f32_16x16x32_bf16 v[60:63], v[44:47], v[20:23], v[60:63]\n\t"
    "v_add_u32 v4, 19456, v4\n\t"
    "ds_read_b64_tr_b16 v[40:41], v5 offset:40960\n\t"
    "ds_read_b64_tr_b16 v[42:43], v5 offset:45056\n\t"
    "ds_read_b64_tr_b16 v[44:45], v5 offset:40968\n\t"
    "ds_read_b64_tr_b16 v[46:47], v5 offset:45064\n\t"
    "ds_read2_b32 v[36:37], v4 offset0:0 offset1:16\n\t"
    "ds_read2_b32 v[38:39], v4 offset0:128 offset1:144\n\t"
    "v_and_b32 v4, 7, v7\n\t"
    "v_xor_b32 v6, v6, v4\n\t"
    "v_max3_f32 v34, v32, v33, %[lo]\n\t"
    "v_max3_f32 v34, v34, v56, v57\n\t"
    "v_max3_f32 v34, v34, v58, v59\n\t"
    "v_max3_f32 v34, v34, v60, v61\n\t"
    "v_max3_f32 v34, v34, v62, v63\n\t"
    "v_mov_b32 v35, v34\n\t"
    "v_lshlrev_b32 v6, 4, v6\n\t"
    "v_and_b32 v4, 15, v7\n\t"
    "v_permlane16_swap_b32 v34, v35\n\t"
    "v_max_f32 v34, v34, v35\n\t"
    "v_mov_b32 v35, v34\n\t"
    "v_lshl_or_b32 v6, v4, 7, v6\n\t"
    "v_xor_b32 v4, 64, v6\n\t"
    "v_permlane32_swap_b32 v34, v35\n\t"
    "v_max_f32 v34, v34, v35\n\t"
    "v_mul_f32 v34, %[nl], v34\n\t"
    "v_fma_f32 v32, v32, %[lsc], v34\n\t"
    "v_fma_f32 v33, v33, %[lsc], v34\n\t"
    "v_fma_f32 v56, v56, %[lsc], v34\n\t"
    "v_fma_f32 v57, v57, %[lsc], v34\n\t"
    "v_fma_f32 v58, v58, %[lsc], v34\n\t"
    "v_fma_f32 v59, v59, %[lsc], v34\n\t"
    "v_fma_f32 v60, v60, %[lsc], v34\n\t"
    "v_fma_f32 v61, v61, %[lsc], v34\n\t"
    "v_fma_f32 v62, v62, %[lsc], v34\n\t"
    "v_fma_f32 v63, v63, %[lsc], v34\n\t"
    "v_exp_f32 v56, v56\n\t"
    "v_exp_f32 v57, v57\n\t"
    "v_exp_f32 v58, v58\n\t"
    "v_exp_f32 v59, v59\n\t"
    "v_exp_f32 v60, v60\n\t"
    "v_exp_f32 v61, v61\n\t"
    "v_exp_f32 v62, v62\n\t"
    "v_exp_f32 v63, v63\n\t"
    "v_exp_f32 v32, v32\n\t"
    "v_exp_f32 v33, v33\n\t"
    "v_cvt_pk_bf16_f32 v24, v56, v57\n\t"
    "v_cvt_pk_bf16_f32 v25, v58, v59\n\t"
    "v_cvt_pk_bf16_f32 v26, v60, v61\n\t"
    "v_cvt_pk_bf16_f32 v27, v62, v63\n\t"
    "v_cvt_pk_bf16_f32 v28, v32, v33\n\t"
    "s_waitcnt lgkmcnt(0)\n\t"
    "v_mfma_f32_16x16x32_bf16 v[8:11], v[48:51], v[24:27], 0\n\t"
    "v_mov_b32 v56, %[one]\n\t"
    "v_mov_b32 v57, %[one]\n\t"
    "v_mfma_f32_16x16x32_bf16 v[12:15], v[52:55], v[24:27], 0\n\t"
    "v_mov_b32 v58, %[one]\n\t"
    "v_mov_b32 v59, %[one]\n\t"
    "v_mfma_f32_16x16x32_bf16 v[16:19], v[40:43], v[24:27], 0\n\t"
    "v_mov_b32 v29, 0\n\t"
    "v_mov_b32 v30, 0\n\t"
    "v_mfma_f32_16x16x32_bf16 v[60:63], v[56:59], v[24:27], 0\n\t"
    "v_mov_b32 v31, v28\n\t"
    "v_mfma_f32_16x16x32_bf16 v[20:23], v[44:47], v[24:27], 0\n\t"
    "v_mfma_f32_16x16x16_bf16 v[8:11], v[36:37], v[28:29], v[8:11]\n\t"
    "v_mfma_f32_16x16x16_bf16 v[12:15], v[36:37], v[30:31], v[12:15]\n\t"
    "s_nop 1\n\t"
    "v_mfma_f32_16x16x16_bf16 v[16:19], v[38:39], v[28:29], v[16:19]\n\t"
    "v_mfma_f32_16x16x16_bf16 v[60:63], v[56:57], v[28:29], v[60:63]\n\t"
    "s_nop 1\n\t"
    "v_mfma_f32_16x16x16_bf16 v[20:23], v[38:39], v[30:31], v[20:23]\n\t"
    "s_nop 4\n\t"
    "v_rcp_f32 v60, v60\n\t"
    "s_nop 0\n\t"
    "v_pk_mul_f32 v[8:9], v[8:9], v[60:61] op_sel_hi:[1,0]\n\t"
    "v_pk_mul_f32 v[10:11], v[10:11], v[60:61] op_sel_hi:[1,0]\n\t"
    "v_pk_mul_f32 v[12:13], v[12:13], v[60:61] op_sel_hi:[1,0]\n\t"
    "v_pk_mul_f32 v[14:15], v[14:15], v[60:61] op_sel_hi:[1,0]\n\t"
    "v_cvt_pk_bf16_f32 v24, v8, v9\n\t"
    "v_cvt_pk_bf16_f32 v25, v10, v11\n\t"
    "v_cvt_pk_bf16_f32 v26, v12, v13\n\t"
    "v_cvt_pk_bf16_f32 v27, v14, v15\n\t"
    "global_store_dwordx4 v6, v[24:27], %[od] offset:2048 sc1\n\t"
    "v_pk_mul_f32 v[16:17], v[16:17], v[60:61] op_sel_hi:[1,0]\n\t"
    "v_pk_mul_f32 v[18:19], v[18:19], v[60:61] op_sel_hi:[1,0]\n\t"
    "v_pk_mul_f32 v[20:21], v[20:21], v[60:61] op_sel_hi:[1,0]\n\t"
    "v_pk_mul_f32 v[22:23], v[22:23], v[60:61] op_sel_hi:[1,0]\n\t"
    "v_cvt_pk_bf16_f32 v28, v16, v17\n\t"
    "v_cvt_pk_bf16_f32 v29, v18, v19\n\t"
    "v_cvt_pk_bf16_f32 v30, v20, v21\n\t"
    "v_cvt_pk_bf16_f32 v31, v22, v23\n\t"
    "global_store_dwordx4 v4, v[28:31], %[od] offset:2048 sc1\n\t"
    "s_waitcnt lgkmcnt(0)\n\t"
    "s_nop 0"
    :
    : [pb] "s"(pb), [od] "s"(od), [neg] "s"(-3.0e38f), [lo] "s"(-1.0e38f), [one] "s"(0x3F803F80u), [lsc] "s"(LSC), [nl] "s"(-LSC), "{v[64:95]}"(W.w0), "{v[96:127]}"(W.w1), "{v[128:159]}"(W.w2), "{v[160:191]}"(W.w3), "{v[192:223]}"(W.w4), "{v[224:255]}"(W.w5)
    : "v4", "v5", "v6", "v7", "v8", "v9", "v10", "v11", "v12", "v13", "v14", "v15", "v16", "v17", "v18", "v19", "v20", "v21", "v22", "v23", "v24", "v25", "v26", "v27", "v28", "v29", "v30", "v31", "v32", "v33", "v34", "v35", "v36", "v37", "v38", "v39", "v40", "v41", "v42", "v43", "v44", "v45", "v46", "v47", "v48", "v49", "v50", "v51", "v52", "v53", "v54", "v55", "v56", "v57", "v58", "v59", "v60", "v61", "v62", "v63", "memory");
}
template <int I> __device__ __forceinline__ void qk_wload(u32x32& w, unsigned a0, unsigned a1);
template <> __device__ __forceinline__ void qk_wload<0>(u32x32& w, unsigned a0, unsigned a1) {
  asm volatile("ds_read_b64_tr_b16 v[64:65], %1 offset:0\n\t"
    "ds_read_b64_tr_b16 v[66:67], %1 offset:512\n\t"
    "ds_read_b64_tr_b16 v[68:69], %1 offset:8192\n\t"
    "ds_read_b64_tr_b16 v[70:71], %1 offset:8704\n\t"
    "ds_read_b64_tr_b16 v[72:73], %2 offset:0\n\t"
    "ds_read_b64_tr_b16 v[74:75], %2 offset:512\n\t"
    "ds_read_b64_tr_b16 v[76:77], %2 offset:8192\n\t"
    "ds_read_b64_tr_b16 v[78:79], %2 offset:8704\n\t"
    "s_waitcnt lgkmcnt(0)\n\t"
    "ds_read_b64_tr_b16 v[80:81], %1 offset:16384\n\t"
    "ds_read_b64_tr_b16 v[82:83], %1 offset:16896\n\t"
    "ds_read_b64_tr_b16 v[84:85], %1 offset:24576\n\t"
    "ds_read_b64_tr_b16 v[86:87], %1 offset:25088\n\t"
    "ds_read_b64_tr_b16 v[88:89], %2 offset:16384\n\t"
    "ds_read_b64_tr_b16 v[90:91], %2 offset:16896\n\t"
    "ds_read_b64_tr_b16 v[92:93], %2 offset:24576\n\t"
    "ds_read_b64_tr_b16 v[94:95], %2 offset:25088\n\t"
    "s_waitcnt lgkmcnt(0)"
    : "=&{v[64:95]}"(w) : "v"(a0), "v"(a1) : "memory");
}
template <> __device__ __forceinline__ void qk_wload<1>(u32x32& w, unsigned a0, unsigned a1) {
  asm volatile("ds_read_b64_tr_b16 v[96:97], %1 offset:0\n\t"
    "ds_read_b64_tr_b16 v[98:99], %1 offset:512\n\t"
    "ds_read_b64_tr_b16 v[100:101], %1 offset:8192\n\t"
    "ds_read_b64_tr_b16 v[102:103], %1 offset:8704\n\t"
    "ds_read_b64_tr_b16 v[104:105], %2 offset:0\n\t"
    "ds_read_b64_tr_b16 v[106:107], %2 offset:512\n\t"
    "ds_read_b64_tr_b16 v[108:109], %2 offset:8192\n\t"
    "ds_read_b64_tr_b16 v[110:111], %2 offset:8704\n\t"
    "s_waitcnt lgkmcnt(0)\n\t"
    "ds_read_b64_tr_b16 v[112:113], %1 offset:16384\n\t"
    "ds_read_b64_tr_b16 v[114:115], %1 offset:16896\n\t"
    "ds_read_b64_tr_b16 v[116:117], %1 offset:24576\n\t"
    "ds_read_b64_tr_b16 v[118:119], %1 offset:25088\n\t"
    "ds_read_b64_tr_b16 v[120:121], %2 offset:16384\n\t"
    "ds_read_b64_tr_b16 v[122:123], %2 offset:16896\n\t"
    "ds_read_b64_tr_b16 v[124:125], %2 offset:24576\n\t"
    "ds_read_b64_tr_b16 v[126:127], %2 offset:25088\n\t"
    "s_waitcnt lgkmcnt(0)"
    : "=&{v[96:127]}"(w) : "v"(a0), "v"(a1) : "memory");
}
template <> __device__ __forceinline__ void qk_wload<2>(u32x32& w, unsigned a0, unsigned a1) {
  asm volatile("ds_read_b64_tr_b16 v[128:129], %1 offset:0\n\t"
    "ds_read_b64_tr_b16 v[130:131], %1 offset:512\n\t"
    "ds_read_b64_tr_b16 v[132:133], %1 offset:8192\n\t"
    "ds_read_b64_tr_b16 v[134:135], %1 offset:8704\n\t"
    "ds_read_b64_tr_b16 v[136:137], %2 offset:0\n\t"
    "ds_read_b64_tr_b16 v[138:139], %2 offset:512\n\t"
    "ds_read_b64_tr_b16 v[140:141], %2 offset:8192\n\t"
    "ds_read_b64_tr_b16 v[142:143], %2 offset:8704\n\t"
    "s_waitcnt lgkmcnt(0)\n\t"
    "ds_read_b64_tr_b16 v[144:145], %1 offset:16384\n\t"
    "ds_read_b64_tr_b16 v[146:147], %1 offset:16896\n\t"
    "ds_read_b64_tr_b16 v[148:149], %1 offset:24576\n\t"
    "ds_read_b64_tr_b16 v[150:151], %1 offset:25088\n\t"
    "ds_read_b64_tr_b16 v[152:153], %2 offset:16384\n\t"
    "ds_read_b64_tr_b16 v[154:155], %2 offset:16896\n\t"
    "ds_read_b64_tr_b16 v[156:157], %2 offset:24576\n\t"
    "ds_read_b64_tr_b16 v[158:159], %2 offset:25088\n\t"
    "s_waitcnt lgkmcnt(0)"
    : "=&{v[128:159]}"(w) : "v"(a0), "v"(a1) : "memory");
}
template <> __device__ __forceinline__ void qk_wload<3>(u32x32& w, unsigned a0, unsigned a1) {
  asm volatile("ds_read_b64_tr_b16 v[160:161], %1 offset:0\n\t"
    "ds_read_b64_tr_b16 v[162:163], %1 offset:512\n\t"
    "ds_read_b64_tr_b16 v[164:165], %1 offset:8192\n\t"
    "ds_read_b64_tr_b16 v[166:167], %1 offset:8704\n\t"
    "ds_read_b64_tr_b16 v[168:169], %2 offset:0\n\t"
    "ds_read_b64_tr_b16 v[170:171], %2 offset:512\n\t"
    "ds_read_b64_tr_b16 v[172:173], %2 offset:8192\n\t"
    "ds_read_b64_tr_b16 v[174:175], %2 offset:8704\n\t"
    "s_waitcnt lgkmcnt(0)\n\t"
    "ds_read_b64_tr_b16 v[176:177], %1 offset:16384\n\t"
    "ds_read_b64_tr_b16 v[178:179], %1 offset:16896\n\t"
    "ds_read_b64_tr_b16 v[180:181], %1 offset:24576\n\t"
    "ds_read_b64_tr_b16 v[182:183], %1 offset:25088\n\t"
    "ds_read_b64_tr_b16 v[184:185], %2 offset:16384\n\t"
    "ds_read_b64_tr_b16 v[186:187], %2 offset:16896\n\t"
    "ds_read_b64_tr_b16 v[188:189], %2 offset:24576\n\t"
    "ds_read_b64_tr_b16 v[190:191], %2 offset:25088\n\t"
    "s_waitcnt lgkmcnt(0)"
    : "=&{v[160:191]}"(w) : "v"(a0), "v"(a1) : "memory");
}
template <> __device__ __forceinline__ void qk_wload<4>(u32x32& w, unsigned a0, unsigned a1) {
  asm volatile("ds_read_b64_tr_b16 v[192:193], %1 offset:0\n\t"
    "ds_read_b64_tr_b16 v[194:195], %1 offset:512\n\t"
    "ds_read_b64_tr_b16 v[196:197], %1 offset:8192\n\t"
    "ds_read_b64_tr_b16 v[198:199], %1 offset:8704\n\t"
    "ds_read_b64_tr_b16 v[200:201], %2 offset:0\n\t"
    "ds_read_b64_tr_b16 v[202:203], %2 offset:512\n\t"
    "ds_read_b64_tr_b16 v[204:205], %2 offset:8192\n\t"
    "ds_read_b64_tr_b16 v[206:207], %2 offset:8704\n\t"
    "s_waitcnt lgkmcnt(0)\n\t"
    "ds_read_b64_tr_b16 v[208:209], %1 offset:16384\n\t"
    "ds_read_b64_tr_b16 v[210:211], %1 offset:16896\n\t"
    "ds_read_b64_tr_b16 v[212:213], %1 offset:24576\n\t"
    "ds_read_b64_tr_b16 v[214:215], %1 offset:25088\n\t"
    "ds_read_b64_tr_b16 v[216:217], %2 offset:16384\n\t"
    "ds_read_b64_tr_b16 v[218:219], %2 offset:16896\n\t"
    "ds_read_b64_tr_b16 v[220:221], %2 offset:24576\n\t"
    "ds_read_b64_tr_b16 v[222:223], %2 offset:25088\n\t"
    "s_waitcnt lgkmcnt(0)"
    : "=&{v[192:223]}"(w) : "v"(a0), "v"(a1) : "memory");
}
template <> __device__ __forceinline__ void qk_wload<5>(u32x32& w, unsigned a0, unsigned a1) {
  asm volatile("ds_read_b64_tr_b16 v[224:225], %1 offset:0\n\t"
    "ds_read_b64_tr_b16 v[226:227], %1 offset:512\n\t"
    "ds_read_b64_tr_b16 v[228:229], %1 offset:8192\n\t"
    "ds_read_b64_tr_b16 v[230:231], %1 offset:8704\n\t"
    "ds_read_b64_tr_b16 v[232:233], %2 offset:0\n\t"
    "ds_read_b64_tr_b16 v[234:235], %2 offset:512\n\t"
    "ds_read_b64_tr_b16 v[236:237], %2 offset:8192\n\t"
    "ds_read_b64_tr_b16 v[238:239], %2 offset:8704\n\t"
    "s_waitcnt lgkmcnt(0)\n\t"
    "ds_read_b64_tr_b16 v[240:241], %1 offset:16384\n\t"
    "ds_read_b64_tr_b16 v[242:243], %1 offset:16896\n\t"
    "ds_read_b64_tr_b16 v[244:245], %1 offset:24576\n\t"
    "ds_read_b64_tr_b16 v[246:247], %1 offset:25088\n\t"
    "ds_read_b64_tr_b16 v[248:249], %2 offset:16384\n\t"
    "ds_read_b64_tr_b16 v[250:251], %2 offset:16896\n\t"
    "ds_read_b64_tr_b16 v[252:253], %2 offset:24576\n\t"
    "ds_read_b64_tr_b16 v[254:255], %2 offset:25088\n\t"
    "s_waitcnt lgkmcnt(0)"
    : "=&{v[224:255]}"(w) : "v"(a0), "v"(a1) : "memory");
}
#define OREP 2
#define AREP 2
constexpr int V_P1 = 131072, V_PV = 149504, V_P2 = 157696;
struct VRW { u32x32 k0, k1, o0, o1, o2, o3; };
__device__ __forceinline__ void vr_v0(const VRW& W, const char* ag, const char* vd, unsigned wl, unsigned wa, unsigned wb, unsigned ra, unsigned& cap, unsigned& sp, unsigned long long& tc, unsigned long long* ts) {
  asm volatile("s_mov_b32 %[sp], 0\n\t"
    "v_mbcnt_lo_u32_b32 v20, -1, 0\n\t"
    "v_mbcnt_hi_u32_b32 v20, -1, v20\n\t"
    "v_lshlrev_b32 v21, 4, v20\n\t"
    "global_load_dwordx4 v[24:27], v21, %[ag] sc1\n\t"
    "global_load_dwordx4 v[28:31], v21, %[ag] offset:1024 sc1\n\t"
    "s_memtime %[tc]\n\t"
    "v_lshl_add_u32 v6, v20, 4, %[wl]\n\t"
    "ds_read_b128 v[32:35], v6 offset:0\n\t"
    "ds_read_b128 v[36:39], v6 offset:1024\n\t"
    "ds_read_b128 v[40:43], v6 offset:2048\n\t"
    "ds_read_b128 v[44:47], v6 offset:3072\n\t"
    "ds_read_b128 v[48:51], v6 offset:4096\n\t"
    "ds_read_b128 v[52:55], v6 offset:5120\n\t"
    "ds_read_b128 v[56:59], v6 offset:6144\n\t"
    "ds_read_b128 v[60:63], v6 offset:7168\n\t"
    "s_setprio 2\n\t"
    "v_and_b32 v6, 2, v20\n\t"
    "v_mul_u32_u24 v6, 0x5555, v6\n\t"
    "v_or_b32 v4, %[c44], v6\n\t"
    "v_and_b32 v6, 15, v20\n\t"
    "v_lshl_add_u32 v5, v6, 2, %[wa]\n\t"
    "v_and_b32 v6, 48, v20\n\t"
    "v_mad_u32_u24 v5, v6, 36, v5\n\t"
    "v_mul_u32_u24 v7, 36, v6\n\t"
    "v_and_b32 v6, 4, v20\n\t"
    "v_mul_u32_u24 v6, 72, v6\n\t"
    "v_add_u32 v7, v7, v6\n\t"
    "v_and_b32 v6, 8, v20\n\t"
    "v_lshl_add_u32 v7, v6, 4, v7\n\t"
    "v_and_b32 v6, 3, v20\n\t"
    "v_lshl_add_u32 v7, v6, 3, v7\n\t"
    "v_add_u32 v7, %[ra], v7\n\t"
    "3:\n\t"
    "v_mov_b32 v8, 0\n\t"
    "v_mov_b32 v9, 0\n\t"
    "v_mov_b32 v10, 0\n\t"
    "v_mov_b32 v11, 0\n\t"
    "v_mov_b32 v12, 0\n\t"
    "v_mov_b32 v13, 0\n\t"
    "v_mov_b32 v14, 0\n\t"
    "v_mov_b32 v15, 0\n\t"
    "v_mov_b32 v16, 0\n\t"
    "v_mov_b32 v17, 0\n\t"
    "v_mov_b32 v18, 0\n\t"
    "v_mov_b32 v19, 0\n\t"
    "v_mov_b32 v20, 0\n\t"
    "v_mov_b32 v21, 0\n\t"
    "v_mov_b32 v22, 0\n\t"
    "v_mov_b32 v23, 0\n\t"
    "s_waitcnt vmcnt(0) lgkmcnt(0)\n\t"
    "v_smfmac_f32_16x16x64_bf16 v[8:11], v[24:27], v[64:71], v4\n\t"
    "v_smfmac_f32_16x16x64_bf16 v[8:11], v[28:31], v[72:79], v4\n\t"
    "v_max3_u32 v6, v24, v25, v26\n\t"
    "v_max3_u32 v6, v6, v27, v28\n\t"
    "v_smfmac_f32_16x16x64_bf16 v[12:15], v[24:27], v[80:87], v4\n\t"
    "v_max3_u32 v6, v6, v29, v30\n\t"
    "v_max_u32 v6, v6, v31\n\t"
    "v_smfmac_f32_16x16x64_bf16 v[12:15], v[28:31], v[88:95], v4\n\t"
    "v_cmp_eq_u32 vcc, -1, v6\n\t"
    "s_cbranch_vccnz 1f\n\t"
    "2:\n\t"
    "v_smfmac_f32_16x16x64_bf16 v[16:19], v[24:27], v[96:103], v4\n\t"
    "v_add_f32 v8, v8, v10\n\t"
    "v_add_f32 v9, v9, v11\n\t"
    "ds_write2_b32 v5, v8, v9 offset0:0 offset1:72\n\t"
    "v_smfmac_f32_16x16x64_bf16 v[16:19], v[28:31], v[104:111], v4\n\t"
    "v_mov_b32 v8, 0\n\t"
    "v_mov_b32 v9, 0\n\t"
    "v_smfmac_f32_16x16x64_bf16 v[20:23], v[24:27], v[112:119], v4\n\t"
    "v_add_f32 v12, v12, v14\n\t"
    "v_add_f32 v13, v13, v15\n\t"
    "ds_write2_b32 v5, v12, v13 offset0:16 offset1:88\n\t"
    "v_smfmac_f32_16x16x64_bf16 v[20:23], v[28:31], v[120:127], v4\n\t"
    "v_mov_b32 v10, 0\n\t"
    "v_mov_b32 v11, 0\n\t"
    "v_add_f32 v16, v16, v18\n\t"
    "v_add_f32 v17, v17, v19\n\t"
    "ds_write2_b32 v5, v16, v17 offset0:32 offset1:104\n\t"
    "v_mov_b32 v12, 0\n\t"
    "v_mov_b32 v13, 0\n\t"
    "v_mov_b32 v14, 0\n\t"
    "v_mov_b32 v15, 0\n\t"
    "v_add_f32 v20, v20, v22\n\t"
    "v_add_f32 v21, v21, v23\n\t"
    "ds_write2_b32 v5, v20, v21 offset0:48 offset1:120\n\t"
    "s_setprio 1\n\t"
    "v_smfmac_f32_16x16x64_bf16 v[8:11], v[24:27], v[32:39], v4\n\t"
    "v_smfmac_f32_16x16x64_bf16 v[8:11], v[28:31], v[40:47], v4\n\t"
    "v_smfmac_f32_16x16x64_bf16 v[12:15], v[24:27], v[48:55], v4\n\t"
    "v_smfmac_f32_16x16x64_bf16 v[12:15], v[28:31], v[56:63], v4\n\t"
    "v_mbcnt_lo_u32_b32 v17, -1, 0\n\t"
    "v_mbcnt_hi_u32_b32 v17, -1, v17\n\t"
    "v_lshl_add_u32 v16, v17, 3, %[wb]\n\t"
    "v_lshlrev_b32 v17, 2, v17\n\t"
    "s_nop 1\n\t"
    "v_add_f32 v8, v8, v10\n\t"
    "v_add_f32 v9, v9, v11\n\t"
    "v_add_f32 v12, v12, v14\n\t"
    "v_add_f32 v13, v13, v15\n\t"
    "ds_write2st64_b64 v16, v[8:9], v[12:13] offset1:1\n\t"
    "s_setprio 3\n\t"
    "s_waitcnt lgkmcnt(1)\n\t"
    "s_barrier\n\t"
    "ds_read_b64 v[32:33], v7 offset:0\n\t"
    "ds_read_b64 v[34:35], v7 offset:2304\n\t"
    "ds_read_b64 v[36:37], v7 offset:4608\n\t"
    "ds_read_b64 v[38:39], v7 offset:6912\n\t"
    "ds_read_b64 v[40:41], v7 offset:9216\n\t"
    "ds_read_b64 v[42:43], v7 offset:11520\n\t"
    "ds_read_b64 v[44:45], v7 offset:13824\n\t"
    "ds_read_b64 v[46:47], v7 offset:16128\n\t"
    "s_waitcnt lgkmcnt(6)\n\t"
    "v_pk_add_f32 v[32:33], v[32:33], v[34:35]\n\t"
    "s_waitcnt lgkmcnt(4)\n\t"
    "v_pk_add_f32 v[36:37], v[36:37], v[38:39]\n\t"
    "v_pk_add_f32 v[32:33], v[32:33], v[36:37]\n\t"
    "s_waitcnt lgkmcnt(2)\n\t"
    "v_pk_add_f32 v[40:41], v[40:41], v[42:43]\n\t"
    "s_waitcnt lgkmcnt(0)\n\t"
    "v_pk_add_f32 v[44:45], v[44:45], v[46:47]\n\t"
    "v_pk_add_f32 v[40:41], v[40:41], v[44:45]\n\t"
    "v_pk_add_f32 v[32:33], v[32:33], v[40:41]\n\t"
    "v_cvt_pk_bf16_f32 v6, v32, v33\n\t"
    "v_min_u32 v6, -2, v6\n\t"
    "global_store_dword v17, v6, %[vd] sc1\n\t"
    "s_barrier\n\t"
    "s_setprio 2\n\t"
    "s_waitcnt lgkmcnt(0)\n\t"
    "s_branch 9f\n\t"
    "1:\n\t"
    "s_add_u32 %[sp], %[sp], 1\n\t"
    "s_cmp_ge_u32 %[sp], %[cap]\n\t"
    "s_cbranch_scc1 7f\n\t"
    "v_mbcnt_lo_u32_b32 v20, -1, 0\n\t"
    "v_mbcnt_hi_u32_b32 v20, -1, v20\n\t"
    "v_lshlrev_b32 v21, 4, v20\n\t"
    "global_load_dwordx4 v[24:27], v21, %[ag] sc1\n\t"
    "global_load_dwordx4 v[28:31], v21, %[ag] offset:1024 sc1\n\t"
    "s_waitcnt lgkmcnt(0)\n\t"
    "s_memtime %[tc]\n\t"
    "s_nop 7\n\t"
    "s_branch 3b\n\t"
    "7:\n\t"
    "s_mov_b32 %[cap], 0\n\t"
    "s_branch 2b\n\t"
    "9:"
    : [tc] "=&s"(tc), [sp] "=&s"(sp), [cap] "+s"(cap)
    : [ag] "s"(ag), [vd] "s"(vd), [wl] "s"(wl), [wa] "s"(wa), [wb] "s"(wb), [ra] "s"(ra), [c44] "s"(0x4444u), "{v[64:95]}"(W.k0), "{v[96:127]}"(W.k1), "{v[128:159]}"(W.o0), "{v[160:191]}"(W.o1), "{v[192:223]}"(W.o2), "{v[224:255]}"(W.o3)
    : "v4", "v5", "v6", "v7", "v8", "v9", "v10", "v11", "v12", "v13", "v14", "v15", "v16", "v17", "v18", "v19", "v20", "v21", "v22", "v23", "v24", "v25", "v26", "v27", "v28", "v29", "v30", "v31", "v32", "v33", "v34", "v35", "v36", "v37", "v38", "v39", "v40", "v41", "v42", "v43", "v44", "v45", "v46", "v47", "v48", "v49", "v50", "v51", "v52", "v53", "v54", "v55", "v56", "v57", "v58", "v59", "v60", "v61", "v62", "v63", "vcc", "scc", "memory");
}
__device__ __forceinline__ void vr_v1(const VRW& W, const char* ag, const char* vd, unsigned wl, unsigned wa, unsigned wb, unsigned ra, unsigned& cap, unsigned& sp, unsigned long long& tc, unsigned long long* ts) {
  asm volatile("s_mov_b32 %[sp], 0\n\t"
    "v_mbcnt_lo_u32_b32 v20, -1, 0\n\t"
    "v_mbcnt_hi_u32_b32 v20, -1, v20\n\t"
    "v_lshlrev_b32 v21, 4, v20\n\t"
    "global_load_dwordx4 v[24:27], v21, %[ag] sc1\n\t"
    "global_load_dwordx4 v[28:31], v21, %[ag] offset:1024 sc1\n\t"
    "s_memtime %[tc]\n\t"
    "v_lshl_add_u32 v6, v20, 4, %[wl]\n\t"
    "ds_read_b128 v[32:35], v6 offset:0\n\t"
    "ds_read_b128 v[36:39], v6 offset:1024\n\t"
    "ds_read_b128 v[40:43], v6 offset:2048\n\t"
    "ds_read_b128 v[44:47], v6 offset:3072\n\t"
    "ds_read_b128 v[48:51], v6 offset:4096\n\t"
    "ds_read_b128 v[52:55], v6 offset:5120\n\t"
    "ds_read_b128 v[56:59], v6 offset:6144\n\t"
    "ds_read_b128 v[60:63], v6 offset:7168\n\t"
    "s_setprio 3\n\t"
    "v_and_b32 v6, 2, v20\n\t"
    "v_mul_u32_u24 v6, 0x5555, v6\n\t"
    "v_or_b32 v4, %[c44], v6\n\t"
    "v_and_b32 v6, 15, v20\n\t"
    "v_lshl_add_u32 v5, v6, 2, %[wa]\n\t"
    "v_and_b32 v6, 48, v20\n\t"
    "v_mad_u32_u24 v5, v6, 36, v5\n\t"
    "v_and_b32 v6, 32, v20\n\t"
    "v_mul_u32_u24 v7, 120, v6\n\t"
    "v_lshl_add_u32 v7, v20, 3, v7\n\t"
    "v_add_u32 v7, %[ra], v7\n\t"
    "3:\n\t"
    "v_mov_b32 v8, 0\n\t"
    "v_mov_b32 v9, 0\n\t"
    "v_mov_b32 v10, 0\n\t"
    "v_mov_b32 v11, 0\n\t"
    "v_mov_b32 v12, 0\n\t"
    "v_mov_b32 v13, 0\n\t"
    "v_mov_b32 v14, 0\n\t"
    "v_mov_b32 v15, 0\n\t"
    "v_mov_b32 v16, 0\n\t"
    "v_mov_b32 v17, 0\n\t"
    "v_mov_b32 v18, 0\n\t"
    "v_mov_b32 v19, 0\n\t"
    "v_mov_b32 v20, 0\n\t"
    "v_mov_b32 v21, 0\n\t"
    "v_mov_b32 v22, 0\n\t"
    "v_mov_b32 v23, 0\n\t"
    "s_waitcnt vmcnt(0) lgkmcnt(0)\n\t"
    "v_smfmac_f32_16x16x64_bf16 v[8:11], v[24:27], v[64:71], v4\n\t"
    "v_smfmac_f32_16x16x64_bf16 v[8:11], v[28:31], v[72:79], v4\n\t"
    "v_max3_u32 v6, v24, v25, v26\n\t"
    "v_max3_u32 v6, v6, v27, v28\n\t"
    "v_smfmac_f32_16x16x64_bf16 v[12:15], v[24:27], v[80:87], v4\n\t"
    "v_max3_u32 v6, v6, v29, v30\n\t"
    "v_max_u32 v6, v6, v31\n\t"
    "v_smfmac_f32_16x16x64_bf16 v[12:15], v[28:31], v[88:95], v4\n\t"
    "v_cmp_eq_u32 vcc, -1, v6\n\t"
    "s_cbranch_vccnz 1f\n\t"
    "2:\n\t"
    "v_smfmac_f32_16x16x64_bf16 v[16:19], v[24:27], v[96:103], v4\n\t"
    "v_add_f32 v8, v8, v10\n\t"
    "v_add_f32 v9, v9, v11\n\t"
    "ds_write2_b32 v5, v8, v9 offset0:0 offset1:72\n\t"
    "v_smfmac_f32_16x16x64_bf16 v[16:19], v[28:31], v[104:111], v4\n\t"
    "v_mov_b32 v8, 0\n\t"
    "v_mov_b32 v9, 0\n\t"
    "v_smfmac_f32_16x16x64_bf16 v[20:23], v[24:27], v[112:119], v4\n\t"
    "v_add_f32 v12, v12, v14\n\t"
    "v_add_f32 v13, v13, v15\n\t"
    "ds_write2_b32 v5, v12, v13 offset0:16 offset1:88\n\t"
    "v_smfmac_f32_16x16x64_bf16 v[20:23], v[28:31], v[120:127], v4\n\t"
    "v_mov_b32 v10, 0\n\t"
    "v_mov_b32 v11, 0\n\t"
    "v_add_f32 v16, v16, v18\n\t"
    "v_add_f32 v17, v17, v19\n\t"
    "ds_write2_b32 v5, v16, v17 offset0:32 offset1:104\n\t"
    "v_mov_b32 v12, 0\n\t"
    "v_mov_b32 v13, 0\n\t"
    "v_mov_b32 v14, 0\n\t"
    "v_mov_b32 v15, 0\n\t"
    "v_add_f32 v20, v20, v22\n\t"
    "v_add_f32 v21, v21, v23\n\t"
    "ds_write2_b32 v5, v20, v21 offset0:48 offset1:120\n\t"
    "s_waitcnt lgkmcnt(0)\n\t"
    "s_barrier\n\t"
    "v_smfmac_f32_16x16x64_bf16 v[8:11], v[24:27], v[32:39], v4\n\t"
    "v_smfmac_f32_16x16x64_bf16 v[8:11], v[28:31], v[40:47], v4\n\t"
    "v_smfmac_f32_16x16x64_bf16 v[12:15], v[24:27], v[48:55], v4\n\t"
    "v_smfmac_f32_16x16x64_bf16 v[12:15], v[28:31], v[56:63], v4\n\t"
    "v_mbcnt_lo_u32_b32 v17, -1, 0\n\t"
    "v_mbcnt_hi_u32_b32 v17, -1, v17\n\t"
    "v_lshl_add_u32 v16, v17, 3, %[wb]\n\t"
    "v_lshlrev_b32 v17, 2, v17\n\t"
    "s_nop 1\n\t"
    "v_add_f32 v8, v8, v10\n\t"
    "v_add_f32 v9, v9, v11\n\t"
    "v_add_f32 v12, v12, v14\n\t"
    "v_add_f32 v13, v13, v15\n\t"
    "ds_write2st64_b64 v16, v[8:9], v[12:13] offset1:1\n\t"
    "s_waitcnt lgkmcnt(0)\n\t"
    "s_barrier\n\t"
    "ds_read_b64 v[32:33], v7 offset:0\n\t"
    "ds_read_b64 v[34:35], v7 offset:1024\n\t"
    "ds_read_b64 v[36:37], v7 offset:2048\n\t"
    "ds_read_b64 v[38:39], v7 offset:3072\n\t"
    "s_waitcnt lgkmcnt(2)\n\t"
    "v_pk_add_f32 v[32:33], v[32:33], v[34:35]\n\t"
    "s_waitcnt lgkmcnt(0)\n\t"
    "v_pk_add_f32 v[36:37], v[36:37], v[38:39]\n\t"
    "v_pk_add_f32 v[32:33], v[32:33], v[36:37]\n\t"
    "s_nop 1\n\t"
    "v_permlane32_swap_b32 v32, v34\n\t"
    "v_permlane32_swap_b32 v33, v35\n\t"
    "s_mov_b32 exec_hi, 0\n\t"
    "v_pk_add_f32 v[32:33], v[32:33], v[34:35]\n\t"
    "v_cvt_pk_bf16_f32 v6, v32, v33\n\t"
    "v_min_u32 v6, -2, v6\n\t"
    "global_store_dword v17, v6, %[vd] sc1\n\t"
    "s_mov_b32 exec_hi, -1\n\t"
    "s_setprio 2\n\t"
    "s_waitcnt lgkmcnt(0)\n\t"
    "s_branch 9f\n\t"
    "1:\n\t"
    "s_add_u32 %[sp], %[sp], 1\n\t"
    "s_cmp_ge_u32 %[sp], %[cap]\n\t"
    "s_cbranch_scc1 7f\n\t"
    "v_mbcnt_lo_u32_b32 v20, -1, 0\n\t"
    "v_mbcnt_hi_u32_b32 v20, -1, v20\n\t"
    "v_lshlrev_b32 v21, 4, v20\n\t"
    "global_load_dwordx4 v[24:27], v21, %[ag] sc1\n\t"
    "global_load_dwordx4 v[28:31], v21, %[ag] offset:1024 sc1\n\t"
    "s_waitcnt lgkmcnt(0)\n\t"
    "s_memtime %[tc]\n\t"
    "s_nop 7\n\t"
    "s_branch 3b\n\t"
    "7:\n\t"
    "s_mov_b32 %[cap], 0\n\t"
    "s_branch 2b\n\t"
    "9:"
    : [tc] "=&s"(tc), [sp] "=&s"(sp), [cap] "+s"(cap)
    : [ag] "s"(ag), [vd] "s"(vd), [wl] "s"(wl), [wa] "s"(wa), [wb] "s"(wb), [ra] "s"(ra), [c44] "s"(0x4444u), "{v[64:95]}"(W.k0), "{v[96:127]}"(W.k1), "{v[128:159]}"(W.o0), "{v[160:191]}"(W.o1), "{v[192:223]}"(W.o2), "{v[224:255]}"(W.o3)
    : "v4", "v5", "v6", "v7", "v8", "v9", "v10", "v11", "v12", "v13", "v14", "v15", "v16", "v17", "v18", "v19", "v20", "v21", "v22", "v23", "v24", "v25", "v26", "v27", "v28", "v29", "v30", "v31", "v32", "v33", "v34", "v35", "v36", "v37", "v38", "v39", "v40", "v41", "v42", "v43", "v44", "v45", "v46", "v47", "v48", "v49", "v50", "v51", "v52", "v53", "v54", "v55", "v56", "v57", "v58", "v59", "v60", "v61", "v62", "v63", "vcc", "scc", "memory");
}
__device__ __forceinline__ void vr_r(const VRW& W, const char* os, const char* ad, unsigned wa, unsigned& cap, unsigned& sp, unsigned long long* ts) {
  asm volatile("v_mbcnt_lo_u32_b32 v4, -1, 0\n\t"
    "v_mbcnt_hi_u32_b32 v4, -1, v4\n\t"
    "v_lshlrev_b32 v5, 4, v4\n\t"
    "global_load_dwordx4 v[12:15], v5, %[os] offset:0 sc1\n\t"
    "global_load_dwordx4 v[16:19], v5, %[os] offset:1024 sc1\n\t"
    "global_load_dwordx4 v[20:23], v5, %[os] offset:2048 sc1\n\t"
    "global_load_dwordx4 v[24:27], v5, %[os] offset:3072 sc1\n\t"
    "s_mov_b32 %[sp], 0\n\t"
    "v_and_b32 v10, 1, v4\n\t"
    "v_mul_u32_u24 v10, 0xaaaa, v10\n\t"
    "v_or_b32 v6, %[c44], v10\n\t"
    "v_and_b32 v10, 15, v4\n\t"
    "v_lshl_add_u32 v7, v10, 2, %[wa]\n\t"
    "v_and_b32 v10, 48, v4\n\t"
    "v_mad_u32_u24 v7, v10, 12, v7\n\t"
    "v_and_b32 v10, 16, v4\n\t"
    "v_mul_u32_u24 v8, 24, v10\n\t"
    "v_and_b32 v10, 4, v4\n\t"
    "v_mad_u32_u24 v8, v10, 48, v8\n\t"
    "v_and_b32 v10, 32, v4\n\t"
    "v_lshl_add_u32 v8, v10, 1, v8\n\t"
    "v_and_b32 v10, 8, v4\n\t"
    "v_lshl_add_u32 v8, v10, 2, v8\n\t"
    "v_and_b32 v10, 3, v4\n\t"
    "v_lshl_add_u32 v8, v10, 3, v8\n\t"
    "v_add_u32 v8, 157696, v8\n\t"
    "v_and_b32 v10, 32, v4\n\t"
    "v_add_u32 v9, v10, v4\n\t"
    "v_lshlrev_b32 v9, 2, v9\n\t"
    "3:\n\t"
    "v_mov_b32 v28, 0\n\t"
    "v_mov_b32 v29, 0\n\t"
    "v_mov_b32 v30, 0\n\t"
    "v_mov_b32 v31, 0\n\t"
    "v_mov_b32 v32, 0\n\t"
    "v_mov_b32 v33, 0\n\t"
    "v_mov_b32 v34, 0\n\t"
    "v_mov_b32 v35, 0\n\t"
    "v_mov_b32 v36, 0\n\t"
    "v_mov_b32 v37, 0\n\t"
    "v_mov_b32 v38, 0\n\t"
    "v_mov_b32 v39, 0\n\t"
    "v_mov_b32 v40, 0\n\t"
    "v_mov_b32 v41, 0\n\t"
    "v_mov_b32 v42, 0\n\t"
    "v_mov_b32 v43, 0\n\t"
    "v_mov_b32 v10, 0\n\t"
    "s_waitcnt vmcnt(3)\n\t"
    "v_smfmac_f32_16x16x64_bf16 v[28:31], v[12:15], v[128:135], v6\n\t"
    "v_max3_u32 v10, v10, v12, v13\n\t"
    "v_smfmac_f32_16x16x64_bf16 v[32:35], v[12:15], v[136:143], v6\n\t"
    "v_max3_u32 v10, v10, v14, v15\n\t"
    "v_smfmac_f32_16x16x64_bf16 v[36:39], v[12:15], v[144:151], v6\n\t"
    "v_smfmac_f32_16x16x64_bf16 v[40:43], v[12:15], v[152:159], v6\n\t"
    "s_waitcnt vmcnt(2)\n\t"
    "v_smfmac_f32_16x16x64_bf16 v[28:31], v[16:19], v[160:167], v6\n\t"
    "v_max3_u32 v10, v10, v16, v17\n\t"
    "v_smfmac_f32_16x16x64_bf16 v[32:35], v[16:19], v[168:175], v6\n\t"
    "v_max3_u32 v10, v10, v18, v19\n\t"
    "v_smfmac_f32_16x16x64_bf16 v[36:39], v[16:19], v[176:183], v6\n\t"
    "v_smfmac_f32_16x16x64_bf16 v[40:43], v[16:19], v[184:191], v6\n\t"
    "s_waitcnt vmcnt(1)\n\t"
    "v_smfmac_f32_16x16x64_bf16 v[28:31], v[20:23], v[192:199], v6\n\t"
    "v_max3_u32 v10, v10, v20, v21\n\t"
    "v_smfmac_f32_16x16x64_bf16 v[32:35], v[20:23], v[200:207], v6\n\t"
    "v_max3_u32 v10, v10, v22, v23\n\t"
    "v_smfmac_f32_16x16x64_bf16 v[36:39], v[20:23], v[208:215], v6\n\t"
    "v_smfmac_f32_16x16x64_bf16 v[40:43], v[20:23], v[216:223], v6\n\t"
    "s_waitcnt vmcnt(0)\n\t"
    "v_smfmac_f32_16x16x64_bf16 v[28:31], v[24:27], v[224:231], v6\n\t"
    "v_max3_u32 v10, v10, v24, v25\n\t"
    "v_smfmac_f32_16x16x64_bf16 v[32:35], v[24:27], v[232:239], v6\n\t"
    "v_max3_u32 v10, v10, v26, v27\n\t"
    "v_smfmac_f32_16x16x64_bf16 v[36:39], v[24:27], v[240:247], v6\n\t"
    "v_cmp_eq_u32 vcc, -1, v10\n\t"
    "v_smfmac_f32_16x16x64_bf16 v[40:43], v[24:27], v[248:255], v6\n\t"
    "s_cbranch_vccnz 4f\n\t"
    "5:\n\t"
    "s_cmp_ge_u32 %[wa], 159232\n\t"
    "s_nop 1\n\t"
    "v_add_f32 v28, v28, v29\n\t"
    "v_add_f32 v32, v32, v33\n\t"
    "v_add_f32 v36, v38, v39\n\t"
    "v_add_f32 v28, v28, v36\n\t"
    "v_add_f32 v40, v42, v43\n\t"
    "v_add_f32 v32, v32, v40\n\t"
    "ds_write2_b32 v7, v28, v32 offset1:16\n\t"
    "s_waitcnt lgkmcnt(0)\n\t"
    "s_barrier\n\t"
    "s_cbranch_scc1 9f\n\t"
    "ds_read_b64 v[44:45], v8 offset:0\n\t"
    "ds_read_b64 v[46:47], v8 offset:768\n\t"
    "ds_read_b64 v[48:49], v8 offset:1536\n\t"
    "ds_read_b64 v[50:51], v8 offset:2304\n\t"
    "ds_read_b64 v[52:53], v8 offset:3072\n\t"
    "ds_read_b64 v[54:55], v8 offset:3840\n\t"
    "ds_read_b64 v[56:57], v8 offset:4608\n\t"
    "ds_read_b64 v[58:59], v8 offset:5376\n\t"
    "s_waitcnt lgkmcnt(6)\n\t"
    "v_pk_add_f32 v[44:45], v[44:45], v[46:47]\n\t"
    "s_waitcnt lgkmcnt(4)\n\t"
    "v_pk_add_f32 v[48:49], v[48:49], v[50:51]\n\t"
    "v_pk_add_f32 v[44:45], v[44:45], v[48:49]\n\t"
    "s_waitcnt lgkmcnt(2)\n\t"
    "v_pk_add_f32 v[52:53], v[52:53], v[54:55]\n\t"
    "s_waitcnt lgkmcnt(0)\n\t"
    "v_pk_add_f32 v[56:57], v[56:57], v[58:59]\n\t"
    "v_pk_add_f32 v[52:53], v[52:53], v[56:57]\n\t"
    "v_pk_add_f32 v[44:45], v[44:45], v[52:53]\n\t"
    "v_cvt_pk_bf16_f32 v10, v44, v45\n\t"
    "v_min_u32 v10, -2, v10\n\t"
    "global_store_dword v9, v10, %[ad] sc1\n\t"
    "s_waitcnt lgkmcnt(0)\n\t"
    "s_branch 9f\n\t"
    "4:\n\t"
    "s_add_u32 %[sp], %[sp], 1\n\t"
    "s_cmp_ge_u32 %[sp], %[cap]\n\t"
    "s_cbranch_scc1 7f\n\t"
    "global_load_dwordx4 v[12:15], v5, %[os] offset:0 sc1\n\t"
    "global_load_dwordx4 v[16:19], v5, %[os] offset:1024 sc1\n\t"
    "global_load_dwordx4 v[20:23], v5, %[os] offset:2048 sc1\n\t"
    "global_load_dwordx4 v[24:27], v5, %[os] offset:3072 sc1\n\t"
    "s_nop 3\n\t"
    "s_branch 3b\n\t"
    "7:\n\t"
    "s_mov_b32 %[cap], 0\n\t"
    "s_branch 5b\n\t"
    "9:"
    : [sp] "=&s"(sp), [cap] "+s"(cap)
    : [os] "s"(os), [ad] "s"(ad), [wa] "s"(wa), [c44] "s"(0x4444u), "{v[64:95]}"(W.k0), "{v[96:127]}"(W.k1), "{v[128:159]}"(W.o0), "{v[160:191]}"(W.o1), "{v[192:223]}"(W.o2), "{v[224:255]}"(W.o3)
    : "v4", "v5", "v6", "v7", "v8", "v9", "v10", "v11", "v12", "v13", "v14", "v15", "v16", "v17", "v18", "v19", "v20", "v21", "v22", "v23", "v24", "v25", "v26", "v27", "v28", "v29", "v30", "v31", "v32", "v33", "v34", "v35", "v36", "v37", "v38", "v39", "v40", "v41", "v42", "v43", "v44", "v45", "v46", "v47", "v48", "v49", "v50", "v51", "v52", "v53", "v54", "v55", "v56", "v57", "v58", "v59", "vcc", "scc", "memory");
}
__device__ __forceinline__ void kr_load_k0(VRW& W, unsigned a) {
  asm volatile("ds_read_b64_tr_b16 v[64:65], %[a] offset:0\n\t"
    "ds_read_b64_tr_b16 v[66:67], %[a] offset:256\n\t"
    "ds_read_b64_tr_b16 v[68:69], %[a] offset:4096\n\t"
    "ds_read_b64_tr_b16 v[70:71], %[a] offset:4352\n\t"
    "ds_read_b64_tr_b16 v[80:81], %[a] offset:32\n\t"
    "ds_read_b64_tr_b16 v[82:83], %[a] offset:288\n\t"
    "ds_read_b64_tr_b16 v[84:85], %[a] offset:4128\n\t"
    "ds_read_b64_tr_b16 v[86:87], %[a] offset:4384\n\t"
    "s_waitcnt lgkmcnt(0)\n\t"
    "ds_read_b64_tr_b16 v[96:97], %[a] offset:64\n\t"
    "ds_read_b64_tr_b16 v[98:99], %[a] offset:320\n\t"
    "ds_read_b64_tr_b16 v[100:101], %[a] offset:4160\n\t"
    "ds_read_b64_tr_b16 v[102:103], %[a] offset:4416\n\t"
    "ds_read_b64_tr_b16 v[112:113], %[a] offset:96\n\t"
    "ds_read_b64_tr_b16 v[114:115], %[a] offset:352\n\t"
    "ds_read_b64_tr_b16 v[116:117], %[a] offset:4192\n\t"
    "ds_read_b64_tr_b16 v[118:119], %[a] offset:4448\n\t"
    "s_waitcnt lgkmcnt(0)"
    : "=&{v[64:95]}"(W.k0), "=&{v[96:127]}"(W.k1) : [a] "v"(a) : "memory");
}
__device__ __forceinline__ void kr_load_k1(VRW& W, unsigned a) {
  asm volatile("ds_read_b64_tr_b16 v[72:73], %[a] offset:0\n\t"
    "ds_read_b64_tr_b16 v[74:75], %[a] offset:256\n\t"
    "ds_read_b64_tr_b16 v[76:77], %[a] offset:4096\n\t"
    "ds_read_b64_tr_b16 v[78:79], %[a] offset:4352\n\t"
    "ds_read_b64_tr_b16 v[88:89], %[a] offset:32\n\t"
    "ds_read_b64_tr_b16 v[90:91], %[a] offset:288\n\t"
    "ds_read_b64_tr_b16 v[92:93], %[a] offset:4128\n\t"
    "ds_read_b64_tr_b16 v[94:95], %[a] offset:4384\n\t"
    "s_waitcnt lgkmcnt(0)\n\t"
    "ds_read_b64_tr_b16 v[104:105], %[a] offset:64\n\t"
    "ds_read_b64_tr_b16 v[106:107], %[a] offset:320\n\t"
    "ds_read_b64_tr_b16 v[108:109], %[a] offset:4160\n\t"
    "ds_read_b64_tr_b16 v[110:111], %[a] offset:4416\n\t"
    "ds_read_b64_tr_b16 v[120:121], %[a] offset:96\n\t"
    "ds_read_b64_tr_b16 v[122:123], %[a] offset:352\n\t"
    "ds_read_b64_tr_b16 v[124:125], %[a] offset:4192\n\t"
    "ds_read_b64_tr_b16 v[126:127], %[a] offset:4448\n\t"
    "s_waitcnt lgkmcnt(0)"
    : "+&{v[64:95]}"(W.k0), "+&{v[96:127]}"(W.k1) : [a] "v"(a) : "memory");
}
__device__ __forceinline__ void kr_load_o0(VRW& W, unsigned a) {
  asm volatile("ds_read_b64_tr_b16 v[128:129], %[a] offset:0\n\t"
    "ds_read_b64_tr_b16 v[130:131], %[a] offset:128\n\t"
    "ds_read_b64_tr_b16 v[132:133], %[a] offset:1024\n\t"
    "ds_read_b64_tr_b16 v[134:135], %[a] offset:1152\n\t"
    "ds_read_b64_tr_b16 v[136:137], %[a] offset:32\n\t"
    "ds_read_b64_tr_b16 v[138:139], %[a] offset:160\n\t"
    "ds_read_b64_tr_b16 v[140:141], %[a] offset:1056\n\t"
    "ds_read_b64_tr_b16 v[142:143], %[a] offset:1184\n\t"
    "s_waitcnt lgkmcnt(0)\n\t"
    "ds_read_b64_tr_b16 v[144:145], %[a] offset:4096\n\t"
    "ds_read_b64_tr_b16 v[146:147], %[a] offset:4224\n\t"
    "ds_read_b64_tr_b16 v[148:149], %[a] offset:5120\n\t"
    "ds_read_b64_tr_b16 v[150:151], %[a] offset:5248\n\t"
    "ds_read_b64_tr_b16 v[152:153], %[a] offset:4128\n\t"
    "ds_read_b64_tr_b16 v[154:155], %[a] offset:4256\n\t"
    "ds_read_b64_tr_b16 v[156:157], %[a] offset:5152\n\t"
    "ds_read_b64_tr_b16 v[158:159], %[a] offset:5280\n\t"
    "s_waitcnt lgkmcnt(0)"
    : "=&{v[128:159]}"(W.o0) : [a] "v"(a) : "memory");
}
__device__ __forceinline__ void kr_load_o1(VRW& W, unsigned a) {
  asm volatile("ds_read_b64_tr_b16 v[160:161], %[a] offset:0\n\t"
    "ds_read_b64_tr_b16 v[162:163], %[a] offset:128\n\t"
    "ds_read_b64_tr_b16 v[164:165], %[a] offset:1024\n\t"
    "ds_read_b64_tr_b16 v[166:167], %[a] offset:1152\n\t"
    "ds_read_b64_tr_b16 v[168:169], %[a] offset:32\n\t"
    "ds_read_b64_tr_b16 v[170:171], %[a] offset:160\n\t"
    "ds_read_b64_tr_b16 v[172:173], %[a] offset:1056\n\t"
    "ds_read_b64_tr_b16 v[174:175], %[a] offset:1184\n\t"
    "s_waitcnt lgkmcnt(0)\n\t"
    "ds_read_b64_tr_b16 v[176:177], %[a] offset:4096\n\t"
    "ds_read_b64_tr_b16 v[178:179], %[a] offset:4224\n\t"
    "ds_read_b64_tr_b16 v[180:181], %[a] offset:5120\n\t"
    "ds_read_b64_tr_b16 v[182:183], %[a] offset:5248\n\t"
    "ds_read_b64_tr_b16 v[184:185], %[a] offset:4128\n\t"
    "ds_read_b64_tr_b16 v[186:187], %[a] offset:4256\n\t"
    "ds_read_b64_tr_b16 v[188:189], %[a] offset:5152\n\t"
    "ds_read_b64_tr_b16 v[190:191], %[a] offset:5280\n\t"
    "s_waitcnt lgkmcnt(0)"
    : "=&{v[160:191]}"(W.o1) : [a] "v"(a) : "memory");
}
__device__ __forceinline__ void kr_load_o2(VRW& W, unsigned a) {
  asm volatile("ds_read_b64_tr_b16 v[192:193], %[a] offset:0\n\t"
    "ds_read_b64_tr_b16 v[194:195], %[a] offset:128\n\t"
    "ds_read_b64_tr_b16 v[196:197], %[a] offset:1024\n\t"
    "ds_read_b64_tr_b16 v[198:199], %[a] offset:1152\n\t"
    "ds_read_b64_tr_b16 v[200:201], %[a] offset:32\n\t"
    "ds_read_b64_tr_b16 v[202:203], %[a] offset:160\n\t"
    "ds_read_b64_tr_b16 v[204:205], %[a] offset:1056\n\t"
    "ds_read_b64_tr_b16 v[206:207], %[a] offset:1184\n\t"
    "s_waitcnt lgkmcnt(0)\n\t"
    "ds_read_b64_tr_b16 v[208:209], %[a] offset:4096\n\t"
    "ds_read_b64_tr_b16 v[210:211], %[a] offset:4224\n\t"
    "ds_read_b64_tr_b16 v[212:213], %[a] offset:5120\n\t"
    "ds_read_b64_tr_b16 v[214:215], %[a] offset:5248\n\t"
    "ds_read_b64_tr_b16 v[216:217], %[a] offset:4128\n\t"
    "ds_read_b64_tr_b16 v[218:219], %[a] offset:4256\n\t"
    "ds_read_b64_tr_b16 v[220:221], %[a] offset:5152\n\t"
    "ds_read_b64_tr_b16 v[222:223], %[a] offset:5280\n\t"
    "s_waitcnt lgkmcnt(0)"
    : "=&{v[192:223]}"(W.o2) : [a] "v"(a) : "memory");
}
__device__ __forceinline__ void kr_load_o3(VRW& W, unsigned a) {
  asm volatile("ds_read_b64_tr_b16 v[224:225], %[a] offset:0\n\t"
    "ds_read_b64_tr_b16 v[226:227], %[a] offset:128\n\t"
    "ds_read_b64_tr_b16 v[228:229], %[a] offset:1024\n\t"
    "ds_read_b64_tr_b16 v[230:231], %[a] offset:1152\n\t"
    "ds_read_b64_tr_b16 v[232:233], %[a] offset:32\n\t"
    "ds_read_b64_tr_b16 v[234:235], %[a] offset:160\n\t"
    "ds_read_b64_tr_b16 v[236:237], %[a] offset:1056\n\t"
    "ds_read_b64_tr_b16 v[238:239], %[a] offset:1184\n\t"
    "s_waitcnt lgkmcnt(0)\n\t"
    "ds_read_b64_tr_b16 v[240:241], %[a] offset:4096\n\t"
    "ds_read_b64_tr_b16 v[242:243], %[a] offset:4224\n\t"
    "ds_read_b64_tr_b16 v[244:245], %[a] offset:5120\n\t"
    "ds_read_b64_tr_b16 v[246:247], %[a] offset:5248\n\t"
    "ds_read_b64_tr_b16 v[248:249], %[a] offset:4128\n\t"
    "ds_read_b64_tr_b16 v[250:251], %[a] offset:4256\n\t"
    "ds_read_b64_tr_b16 v[252:253], %[a] offset:5152\n\t"
    "ds_read_b64_tr_b16 v[254:255], %[a] offset:5280\n\t"
    "s_waitcnt lgkmcnt(0)"
    : "=&{v[224:255]}"(W.o3) : [a] "v"(a) : "memory");
}
__device__ __forceinline__ void qk_body(char* smem, int wave_id_, int xg, int h, int wg, const u16* __restrict__ Wq, const u16* __restrict__ Wv,
                                        char* ws, unsigned* tmo, unsigned* cnt, const char* xb, const char* ob) {
  const int wave = WAVE_ID;
  const int ty = wave >> 2, w4 = wave & 3;
  const char* oh = ob + (size_t)(h >> 2) * 16384 + (h & 3) * 1024;
  const char* wq = uni((const char*)(Wq + h * DH)); const char* wv = uni((const char*)(Wv + h * DH));
  auto stage2 = [&](int i) {
    const int tid = wave * 64 + lane_id();
    if (i < 6) {
      stage_proj<false>(wq + (size_t)i * 128 * HD * 2, Q_RING + ((2 * i) & 3) * 32768, tid, wave);
      if (i < 2) stage_proj<false>(wq + (size_t)(i + 6) * 128 * HD * 2, Q_RING + ((2 * i + 1) & 3) * 32768, tid, wave);
      else stage_proj<true>(wv + (size_t)(i - 2) * 256 * HD * 2, Q_RING + ((2 * i + 1) & 3) * 32768, tid, wave);
    }
  };
  auto rearm = [&](int ps) {
    unsigned z = SENT; asm volatile("" : "+v"(z));
    const unsigned l16 = lane_id() * 16;
#pragma unroll
    for (int r = 0; r < 2 * OREP; ++r) st16s(oh + (size_t)(ps * 4 + (r >> 1)) * 131072, (r & 1) * 4096u + l16, u32x4{z, z, z, z});
  };
  const char* dbw = xb + (size_t)48 * 16384 + 4096 + h * 128;
  QKW W;
  stage2(0); stage2(1);
#define WLD(i, w) do { if ((i) < 5) WAIT_V(8); else WAIT_V(0); \
    WBAR(); { const int l_ = lane_id(); const unsigned o_ = Q_RING + ty * 32768 + ((i) & 1) * 65536; const bool v_ = ty && (i) >= 2; \
      qk_wload<i>(w, proj_base(v_ ? w4 : 2 * w4, l_) + o_, proj_base(v_ ? w4 + 4 : 2 * w4 + 1, l_) + o_); } \
    WBAR(); stage2((i) + 2); if (wave == 0) STS(ws, wg, 1 + (i)); if ((i) - 4 == RDYI && opaque_tid() == 64) st4_sc1(cnt + (PQKV + h) * 32 + 3, 1u); } while (0)
  WLD(0, W.w0); WLD(1, W.w1); WLD(2, W.w2); WLD(3, W.w3); WLD(4, W.w4); WLD(5, W.w5);
#undef WLD
  if (wave == 4) *(volatile LAS u32x2*)(smem + Q_TM) = u32x2{0u, 0u};
  if (wave == 6) *(u32x4*)(smem + Q_PQ + 4096 + lane_id() * 16) = u32x4{0u, 0u, 0u, 0u};
  BAR();
  if (wave == 0) STS(ws, wg, 0);
  unsigned avail = 0;
  auto stage_kv = [&](int s) {
    if (avail <= (unsigned)s) {
      for (int spins = 0;; ++spins) {
        avail = qkv_avail<0>(cnt + (PQKV + h) * 32);
        if (avail > (unsigned)s) break;
        __builtin_amdgcn_s_sleep(8);
        if (give_up(spins, tmo, 5)) break;
      }
    }
    head_to_lds(uni(ws + (wave == 5 ? OFF_K : (wave == 6 ? OFF_V : OFF_Q)) + ((size_t)s * NH + h) * 8192), lane_id(), Q_K + (s & 1) * 32768 + (wave - 5) * 8192);
  };
  auto loop = [&](auto w_c) __attribute__((always_inline)) {
    constexpr int WV = decltype(w_c)::value;
    if (WV >= 5) stage_kv(0);
    unsigned t0 = 0, cap = 60000, d0 = 105, d1 = 200;
    int dq = D_Q;
    int sl = 0;
    unsigned fc = 0;
    for (int s = 0; s < NSEG; ++s) {
      TSDECL;
      const char* ag = xb + (size_t)((sl * 4 + (AREP == 4 ? xg : xg >> 1)) * 2 + (WV >> 2)) * 16384 + (WV < 4 ? WV * 2048 : WV * 1024);
      const unsigned pb = (s & 1) * 32768;
      const char* vs = WV == 4 ? dbw : WV == 7 ? (const char*)(cnt + (PROG + h) * 32) : xb + (size_t)(24 + sl * 8 + (h >> 2)) * 16384 + (h & 3) * 2048;
      const char* v2 = xb + (size_t)(52 + sl * 4 + (h >> 3)) * 16384 + (h & 7) * 1024 + (WV & 1) * 512;
      const char* od = oh + (size_t)sl * 524288;
      const unsigned sn = (unsigned)(s + 1) << 24;
      if (s > 0) wait_rt(t0, dq);
      TS(0);
      unsigned long long tc; unsigned sp;
      qk_step<WV>(W, ag, vs, v2, od, pb, sn, cap, sp, tc, nullptr, d0, d1);
      dq = (sp & 0xFFFFu) ? min(dq + DQU, D_Q + DQM) : max(dq - DQD, D_Q);
      if (WV >= 5) {
        const bool st0 = E_S >= 0 && s + 1 < NSEG && avail > (unsigned)(s + 1);
        if (st0) { delay64<(E_S > 0 ? E_S : 0)>(); stage_kv(s + 1); }
        if (RAE && WV == RAW && s > 0 && s < NSEG - 1) rearm(sl == 0 ? 2 : sl - 1);
        WBAR();
        if (!st0 && s + 1 < NSEG) stage_kv(s + 1);
      }
      if (!RAE && WV == RAW && s > 0 && s < NSEG - 1) rearm(sl == 0 ? 2 : sl - 1);
      unsigned long long* tsp = nullptr;
      seg_attn<WV>(W, pb, ws + OFF_O + ((size_t)s * 64 + 2 * h + (WV >> 1)) * 4096, tsp);
      if (WV == 1) TSFLUSH(0x8000);
      if (s < NSEG - 1) {
        if (WV == 4) {
          delay64<E_B>();
          t0 = bell_time(xb + (size_t)(48 + xg) * 16384 + 4096, (lane_id() & 31) * 128, (unsigned)(s + 1), cap);
          *(volatile LAS u32x2*)(smem + Q_TM) = u32x2{(unsigned)(s + 1), t0};
          TS(14);
          TSFLUSH(0x4000);
        } else {
          for (;;) {
            const u32x2 v = *(volatile LAS u32x2*)(smem + Q_TM);
            t0 = __builtin_amdgcn_readfirstlane(v.y);
            if (__builtin_amdgcn_readfirstlane(v.x) == (unsigned)(s + 1)) break;
            __builtin_amdgcn_s_sleep(1);
          }
        }
      }
      sl = sl == 2 ? 0 : sl + 1;
    }
    WAIT_V(0);
    WBAR();
    if (WV == 7 && lane_id() == 0) st4_sc1(cnt + (PROG + h) * 32, (unsigned)NSEG);
    if (cap == 0 && lane_id() == 0) st4_sc1(tmo, 1u);
    FCS();
  };
  switch (wave) {
    case 0: loop(std::integral_constant<int, 0>{}); break;
    case 1: loop(std::integral_constant<int, 1>{}); break;
    case 2: loop(std::integral_constant<int, 2>{}); break;
    case 3: loop(std::integral_constant<int, 3>{}); break;
    case 4: loop(std::integral_constant<int, 4>{}); break;
    case 5: loop(std::integral_constant<int, 5>{}); break;
    case 6: loop(std::integral_constant<int, 6>{}); break;
    default: loop(std::integral_constant<int, 7>{}); break;
  }
  __builtin_amdgcn_s_setprio(0);
}

__device__ __forceinline__ void vr_body(char* smem, int wave_id_, int xg, int h, int c, int wg, const u16* __restrict__ Wk, const u16* __restrict__ Wv,
                                        const u16* __restrict__ Wo, char* ws, unsigned* tmo, unsigned* cnt, const char* xb, const char* ob) {
  const int wave = WAVE_ID;
  const unsigned po = (unsigned)(h >> 4) * 16384u + ((h >> 1) & 7) * 1024 + (h & 1) * 512 + c * 128;
  const unsigned ko_ = (unsigned)(24 + (h >> 2)) * 16384u + (h & 3) * 2048 + c * 1024;
  const unsigned vo_ = (unsigned)(52 + (h >> 3)) * 16384u + (h & 7) * 1024 + c * 512;
  auto rearm = [&](int ps) {
    const int lane = lane_id();
    unsigned z = SENT; asm volatile("" : "+v"(z));
    const u32x4 sv = u32x4{z, z, z, z};
    static_assert(AREP == 2, "");
    st16s(xb, lane < 32 ? (unsigned)(ps * 8 + (lane >> 4) * 2) * 16384u + po + ((lane >> 3) & 1) * 256 + (lane & 7) * 16 : (unsigned)ps * 65536u + vo_ + (lane & 31) * 16, sv);
    st16s(xb, (unsigned)ps * 131072u + ko_ + lane * 16, sv);
  };
  const int hg = (wave + ROTW * ((2 * h + c) & 3)) & 7;
  VRW W;
  {
    const int lane = lane_id();
    const int g = lane >> 4, q4 = (lane & 15) >> 2, p4 = lane & 3, fr = 4 * g + (q4 & 1) + 32 * (q4 >> 1);
    const unsigned lw = wave * 16384;
    const char* sk = uni((const char*)(Wk + (size_t)(128 * wave) * HD + h * DH + 64 * c));
    const char* sv_ = uni((const char*)(Wv + (size_t)(128 * wave) * HD + h * DH + 64 + 32 * c));
    const char* so = uni((const char*)(Wo + (size_t)(512 * hg) * DM + 32 * h));
    const int kp = lane >> 2, sr = 32 * (kp >> 3) + 16 * ((kp >> 2) & 1) + (kp & 3);
    auto issue = [&](int p) {
#pragma unroll
      for (int i = 0; i < 8; ++i) {
        const unsigned d = lw + (p & 1) * 8192 + i * 1024;
        if (p < 2) gldsw(sk + (size_t)(8 * p + i) * 8 * HD * 2, (unsigned)((lane >> 3) * HD * 2 + (lane & 7) * 16), d);
        else if (p < 6 && (NTM & 8)) gldsw(so + (size_t)(128 * (p - 2) + 64 * (i & 1) + 4 * (i >> 1)) * DM * 2, (unsigned)(sr * DM * 2 + p4 * 16), d);
        else if (p < 6) glds16s<0>(so + (size_t)(128 * (p - 2) + 64 * (i & 1) + 4 * (i >> 1)) * DM * 2, (unsigned)(sr * DM * 2 + p4 * 16), d);
        else if (NTM & 16) gldsw(sv_ + (size_t)i * 16 * HD * 2, (unsigned)(kp * HD * 2 + p4 * 16), d);
        else glds16s<0>(sv_ + (size_t)i * 16 * HD * 2, (unsigned)(kp * HD * 2 + p4 * 16), d);
      }
    };
#define VTS(p) do { } while (0)
    const int fm = 16 * (g >> 1) + 4 * (g & 1) + (q4 & 1) + 8 * (q4 >> 1);
    const unsigned ak = lw + fm * 128 + p4 * 8, ao = lw + fr * 64 + p4 * 8;
    issue(2); issue(3);
    WAIT_V(8); VTS(1); kr_load_o0(W, ao); issue(4);
    WAIT_V(8); VTS(2); kr_load_o1(W, ao + 8192); issue(5);
    WAIT_V(8); VTS(3); kr_load_o2(W, ao); issue(0);
    WAIT_V(8); VTS(4); kr_load_o3(W, ao + 8192); issue(1);
    WAIT_V(8); VTS(5); kr_load_k0(W, ak); issue(6);
    WAIT_V(8); VTS(6); kr_load_k1(W, ak + 8192);
    WAIT_V(0);
    u32x8 x[4];
#pragma unroll
    for (int j = 0; j < 4; ++j) {
      const unsigned a = lw + fm * 64 + p4 * 8 + (j & 1) * 4096 + (j >> 1) * 32;
      const u32x2 t0 = trread(smem, a), t1 = trread(smem, a + 128), t2 = trread(smem, a + 2048), t3 = trread(smem, a + 2176);
      x[j] = u32x8{t0.x, t0.y, t1.x, t1.y, t2.x, t2.y, t3.x, t3.y};
    }
    char* d = smem + lw + 8192 + lane * 16;
#pragma unroll
    for (int j = 0; j < 4; ++j) { *(u32x4*)(d + j * 2048) = u32x4{x[j][0], x[j][1], x[j][2], x[j][3]}; *(u32x4*)(d + j * 2048 + 1024) = u32x4{x[j][4], x[j][5], x[j][6], x[j][7]}; }
    VTS(7);
  }
  BAR();
  if (wave == 0) STS(ws, wg, 0);
  __builtin_amdgcn_s_setprio(2);
  auto loop = [&](auto cl_c) __attribute__((always_inline)) {
    constexpr int CL = decltype(cl_c)::value;
    unsigned cap = 60000, tr = 0, fc = 0;
    int da = D_A, dd = D_O;
    int sl = 0;
    for (int s = 0; s < NSEG; ++s) {
      TSDECL;
      const int sn = sl == 2 ? 0 : sl + 1;
      const char* ag = xb + (size_t)((sl * 4 + (AREP == 4 ? xg : xg >> 1)) * 2 + (wave >> 2)) * 16384 + (wave & 3) * 2048;
      const char* vd = CL == 0 ? xb + (size_t)sl * 131072 + ko_ + (wave & 3) * 256 : xb + (size_t)sl * 65536 + vo_ + (wave & 3) * 128;
      const char* os = ob + (size_t)((sl * 4 + ORSEL) * 8 + hg) * 16384 + c * 4096;
      const char* ad = xb + (size_t)((sn * 4 + (wave & 3)) * 2) * 16384 + po;
      if (s > 0) { if (wave < AREP) delay64<E_AG - E_W>(); else delay64<E_AG>(); }
      TS(0);
      unsigned long long tc; unsigned sp;
      unsigned long long* tsp = nullptr;
      if (CL == 0) vr_v0(W, ag, vd, wave * 16384 + 8192, V_P1 + wave * 2304, V_PV + wave * 1024, V_P1 + (wave & 3) * 32, cap, sp, tc, tsp);
      else vr_v1(W, ag, vd, wave * 16384 + 8192, V_P1 + wave * 2304, V_PV + wave * 1024, V_PV + (wave & 3) * 256, cap, sp, tc, tsp);
      TN(12, sp + 1);
      if (s > 3 && sp) fc += 1u;
      da = sp ? min(da + DQU, D_A + DQM) : max(da - DQD, D_A);
      if (s == NSEG - 1) break;
      if (wave == 7 && s > 0) rearm(sl == 0 ? 2 : sl - 1);
      wait_clk((unsigned)tc, E_O);
      TS(3);
      const unsigned tb = bell_time(xb + (size_t)(48 + xg) * 16384 + hg * 512, (lane_id() & 3) * 128, (unsigned)(s + 1), cap);
      tr = tb;
      wait_rt(tb, dd);
      TS(4);
      vr_r(W, os, ad, V_P2 + wave * 768, cap, sp, tsp);
      if (s > 3 && sp) fc += 0x10000u;
      dd = sp ? min(dd + DOU, D_O + DOM) : max(dd - DQD, D_O);
      TN(14, (sp & 0xFFFF) + 1);
      if (wave == 1) TSFLUSH(0x6078);
      sl = sn;
    }
    if (cap == 0 && lane_id() == 0) st4_sc1(tmo, 2u);
    FCS();
  };
  if ((wave >> 2) == CL0) loop(std::integral_constant<int, 0>{}); else loop(std::integral_constant<int, 1>{});
  __builtin_amdgcn_s_setprio(0);
}
#define GEMMX gemm7
#define PF0 0
#define NIMM 3
enum { T_NONE, T_QV, T_KR, T_WO, T_F1, T_F2, T_F2C, T_FIN, T_LN, T_SA };
__device__ const unsigned ROLES[256][6] = {
  {0x1, 0x0, 0x0, 0x0, 0x0, 0x0},
  {0x11, 0x0, 0x0, 0x0, 0x0, 0x0},
  {0x21, 0x0, 0x0, 0x0, 0x0, 0x0},
  {0x31, 0x0, 0x0, 0x0, 0x0, 0x0},
  {0x41, 0x0, 0x0, 0x0, 0x0, 0x0},
  {0x51, 0x0, 0x0, 0x0, 0x0, 0x0},
  {0x61, 0x0, 0x0, 0x0, 0x0, 0x0},
  {0x71, 0x0, 0x0, 0x0, 0x0, 0x0},
  {0x81, 0x0, 0x0, 0x0, 0x0, 0x0},
  {0x91, 0x0, 0x0, 0x0, 0x0, 0x0},
  {0xa1, 0x0, 0x0, 0x0, 0x0, 0x0},
  {0xb1, 0x0, 0x0, 0x0, 0x0, 0x0},
  {0x2, 0x0, 0x0, 0x0, 0x0, 0x0},
  {0x12, 0x0, 0x0, 0x0, 0x0, 0x0},
  {0x22, 0x0, 0x0, 0x0, 0x0, 0x0},
  {0x32, 0x0, 0x0, 0x0, 0x0, 0x0},
  {0x42, 0x0, 0x0, 0x0, 0x0, 0x0},
  {0x52, 0x0, 0x0, 0x0, 0x0, 0x0},
  {0x62, 0x0, 0x0, 0x0, 0x0, 0x0},
  {0x72, 0x0, 0x0, 0x0, 0x0, 0x0},
  {0x82, 0x0, 0x0, 0x0, 0x0, 0x0},
  {0x92, 0x0, 0x0, 0x0, 0x0, 0x0},
  {0xa2, 0x0, 0x0, 0x0, 0x0, 0x0},
  {0xb2, 0x0, 0x0, 0x0, 0x0, 0x0},
  {0xc2, 0x0, 0x0, 0x0, 0x0, 0x0},
  {0xd2, 0x0, 0x0, 0x0, 0x0, 0x0},
  {0xe2, 0x0, 0x0, 0x0, 0x0, 0x0},
  {0xf2, 0x0, 0x0, 0x0, 0x0, 0x0},
  {0x102, 0x0, 0x0, 0x0, 0x0, 0x0},
  {0x112, 0x0, 0x0, 0x0, 0x0, 0x0},
  {0x122, 0x0, 0x0, 0x0, 0x0, 0x0},
  {0x132, 0x0, 0x0, 0x0, 0x0, 0x0},
  {0xc1, 0x0, 0x0, 0x0, 0x0, 0x0},
  {0xd1, 0x0, 0x0, 0x0, 0x0, 0x0},
  {0xe1, 0x0, 0x0, 0x0, 0x0, 0x0},
  {0xf1, 0x0, 0x0, 0x0, 0x0, 0x0},
  {0x101, 0x0, 0x0, 0x0, 0x0, 0x0},
  {0x111, 0x0, 0x0, 0x0, 0x0, 0x0},
  {0x121, 0x0, 0x0, 0x0, 0x0, 0x0},
  {0x131, 0x0, 0x0, 0x0, 0x0, 0x0},
  {0x142, 0x0, 0x0, 0x0, 0x0, 0x0},
  {0x152, 0x0, 0x0, 0x0, 0x0, 0x0},
  {0x162, 0x0, 0x0, 0x0, 0x0, 0x0},
  {0x172, 0x0, 0x0, 0x0, 0x0, 0x0},
  {0x182, 0x0, 0x0, 0x0, 0x0, 0x0},
  {0x192, 0x0, 0x0, 0x0, 0x0, 0x0},
  {0x1a2, 0x0, 0x0, 0x0, 0x0, 0x0},
  {0x1b2, 0x0, 0x0, 0x0, 0x0, 0x0},
  {0x1c2, 0x0, 0x0, 0x0, 0x0, 0x0},
  {0x1d2, 0x0, 0x0, 0x0, 0x0, 0x0},
  {0x1e2, 0x0, 0x0, 0x0, 0x0, 0x0},
  {0x1f2, 0x0, 0x0, 0x0, 0x0, 0x0},
  {0x202, 0x0, 0x0, 0x0, 0x0, 0x0},
  {0x212, 0x0, 0x0, 0x0, 0x0, 0x0},
  {0x222, 0x0, 0x0, 0x0, 0x0, 0x0},
  {0x232, 0x0, 0x0, 0x0, 0x0, 0x0},
  {0x242, 0x0, 0x0, 0x0, 0x0, 0x0},
  {0x252, 0x0, 0x0, 0x0, 0x0, 0x0},
  {0x262, 0x0, 0x0, 0x0, 0x0, 0x0},
  {0x272, 0x0, 0x0, 0x0, 0x0, 0x0},
  {0x282, 0x0, 0x0, 0x0, 0x0, 0x0},
  {0x292, 0x0, 0x0, 0x0, 0x0, 0x0},
  {0x2a2, 0x0, 0x0, 0x0, 0x0, 0x0},
  {0x2b2, 0x0, 0x0, 0x0, 0x0, 0x0},
  {0x141, 0x0, 0x0, 0x0, 0x0, 0x0},
  {0x151, 0x0, 0x0, 0x0, 0x0, 0x0},
  {0x161, 0x0, 0x0, 0x0, 0x0, 0x0},
  {0x171, 0x0, 0x0, 0x0, 0x0, 0x0},
  {0x181, 0x0, 0x0, 0x0, 0x0, 0x0},
  {0x191, 0x0, 0x0, 0x0, 0x0, 0x0},
  {0x1a1, 0x0, 0x0, 0x0, 0x0, 0x0},
  {0x1b1, 0x0, 0x0, 0x0, 0x0, 0x0},
  {0x1c1, 0x0, 0x0, 0x0, 0x0, 0x0},
  {0x1d1, 0x0, 0x0, 0x0, 0x0, 0x0},
  {0x1e1, 0x0, 0x0, 0x0, 0x0, 0x0},
  {0x1f1, 0x0, 0x0, 0x0, 0x0, 0x0},
  {0x2c2, 0x0, 0x0, 0x0, 0x0, 0x0},
  {0x2d2, 0x0, 0x0, 0x0, 0x0, 0x0},
  {0x2e2, 0x0, 0x0, 0x0, 0x0, 0x0},
  {0x2f2, 0x0, 0x0, 0x0, 0x0, 0x0},
  {0x302, 0x0, 0x0, 0x0, 0x0, 0x0},
  {0x312, 0x0, 0x0, 0x0, 0x0, 0x0},
  {0x322, 0x0, 0x0, 0x0, 0x0, 0x0},
  {0x332, 0x0, 0x0, 0x0, 0x0, 0x0},
  {0x342, 0x0, 0x0, 0x0, 0x0, 0x0},
  {0x352, 0x0, 0x0, 0x0, 0x0, 0x0},
  {0x362, 0x0, 0x0, 0x0, 0x0, 0x0},
  {0x372, 0x0, 0x0, 0x0, 0x0, 0x0},
  {0x382, 0x0, 0x0, 0x0, 0x0, 0x0},
  {0x392, 0x0, 0x0, 0x0, 0x0, 0x0},
  {0x3a2, 0x0, 0x0, 0x0, 0x0, 0x0},
  {0x3b2, 0x0, 0x0, 0x0, 0x0, 0x0},
  {0x3c2, 0x0, 0x0, 0x0, 0x0, 0x0},
  {0x3d2, 0x0, 0x0, 0x0, 0x0, 0x0},
  {0x3e2, 0x0, 0x0, 0x0, 0x0, 0x0},
  {0x3f2, 0x0, 0x0, 0x0, 0x0, 0x0},
  {0x100003, 0x180000, 0x0, 0x0, 0x0, 0x0},
  {0x100013, 0x180004, 0x0, 0x0, 0x0, 0x0},
  {0x100023, 0x180008, 0x0, 0x0, 0x0, 0x0},
  {0x100033, 0x18000c, 0x0, 0x0, 0x0, 0x0},
  {0x100043, 0x180010, 0x0, 0x0, 0x0, 0x0},
  {0x100053, 0x180014, 0x0, 0x0, 0x0, 0x0},
  {0x100063, 0x180018, 0x0, 0x0, 0x0, 0x0},
  {0x100073, 0x18001c, 0x0, 0x0, 0x0, 0x0},
  {0x100083, 0x180020, 0x0, 0x0, 0x0, 0x0},
  {0x100093, 0x180024, 0x0, 0x0, 0x0, 0x0},
  {0x1000a3, 0x180028, 0x0, 0x0, 0x0, 0x0},
  {0x1000b3, 0x18002c, 0x0, 0x0, 0x0, 0x0},
  {0x1000c3, 0x180030, 0x0, 0x0, 0x0, 0x0},
  {0x1000d3, 0x180034, 0x0, 0x0, 0x0, 0x0},
  {0x1000e3, 0x180038, 0x0, 0x0, 0x0, 0x0},
  {0x1000f3, 0x18003c, 0x0, 0x0, 0x0, 0x0},
  {0x100103, 0x180040, 0x0, 0x0, 0x0, 0x0},
  {0x100113, 0x180044, 0x0, 0x0, 0x0, 0x0},
  {0x100123, 0x180048, 0x0, 0x0, 0x0, 0x0},
  {0x100133, 0x18004c, 0x0, 0x0, 0x0, 0x0},
  {0x100143, 0x180050, 0x0, 0x0, 0x0, 0x0},
  {0x100153, 0x180054, 0x0, 0x0, 0x0, 0x0},
  {0x100163, 0x180058, 0x0, 0x0, 0x0, 0x0},
  {0x100173, 0x18005c, 0x0, 0x0, 0x0, 0x0},
  {0x100183, 0x180060, 0x0, 0x0, 0x0, 0x0},
  {0x100193, 0x180064, 0x0, 0x0, 0x0, 0x0},
  {0x1001a3, 0x180068, 0x0, 0x0, 0x0, 0x0},
  {0x1001b3, 0x18006c, 0x0, 0x0, 0x0, 0x0},
  {0x1001c3, 0x180070, 0x0, 0x0, 0x0, 0x0},
  {0x1001d3, 0x180074, 0x0, 0x0, 0x0, 0x0},
  {0x1001e3, 0x180078, 0x0, 0x0, 0x0, 0x0},
  {0x1001f3, 0x18007c, 0x0, 0x0, 0x0, 0x0},
  {0x100064, 0x5391800, 0x0, 0x0, 0x0, 0x0},
  {0x100074, 0x5391804, 0x0, 0x0, 0x0, 0x0},
  {0x100084, 0x5391808, 0x0, 0x0, 0x0, 0x0},
  {0x100094, 0x539180c, 0x0, 0x0, 0x0, 0x0},
  {0x1000a4, 0x5391810, 0x0, 0x0, 0x0, 0x0},
  {0x1000d4, 0x5391814, 0x0, 0x0, 0x0, 0x0},
  {0x100034, 0x5391818, 0x0, 0x0, 0x0, 0x0},
  {0x1000b4, 0x539181c, 0x0, 0x0, 0x0, 0x0},
  {0x200000, 0x400001, 0x1402e1e, 0x0, 0x0, 0x0},
  {0x200007, 0x400005, 0x2403900, 0x0, 0x0, 0x0},
  {0x200017, 0x400009, 0x2403904, 0x0, 0x0, 0x0},
  {0x200027, 0x40000d, 0x2403908, 0x0, 0x0, 0x0},
  {0x200037, 0x400011, 0x240390c, 0x0, 0x0, 0x0},
  {0x100025, 0x2e0015, 0x0, 0x0, 0x0, 0x0},
  {0x100035, 0x2e0019, 0x0, 0x0, 0x0, 0x0},
  {0x100045, 0x2e001d, 0x0, 0x0, 0x0, 0x0},
  {0x100004, 0x260002, 0x0, 0x0, 0x0, 0x0},
  {0x100014, 0x260006, 0x0, 0x0, 0x0, 0x0},
  {0x100024, 0x26000a, 0x0, 0x0, 0x0, 0x0},
  {0x1000c4, 0x26000e, 0x0, 0x0, 0x0, 0x0},
  {0x100044, 0x260012, 0x0, 0x0, 0x0, 0x0},
  {0x100054, 0x260016, 0x0, 0x0, 0x0, 0x0},
  {0x100005, 0x2e001a, 0x0, 0x0, 0x0, 0x0},
  {0x100015, 0x2e001e, 0x0, 0x0, 0x0, 0x0},
  {0x101055, 0x1402612, 0x0, 0x0, 0x0, 0x0},
  {0x102065, 0x1402616, 0x0, 0x0, 0x0, 0x0},
  {0x303000, 0x1402602, 0x1402e15, 0x2403910, 0x0, 0x0},
  {0x304000, 0x1402606, 0x1402e19, 0x2403914, 0x0, 0x0},
  {0x5008, 0x0, 0x0, 0x0, 0x0, 0x0},
  {0x6018, 0x0, 0x0, 0x0, 0x0, 0x0},
  {0x307000, 0x140260a, 0x1402e1d, 0x2403918, 0x0, 0x0},
  {0x308000, 0x140260e, 0x1402e1a, 0x240391c, 0x0, 0x0},
  {0x100144, 0x5391820, 0x0, 0x0, 0x0, 0x0},
  {0x100154, 0x5391824, 0x0, 0x0, 0x0, 0x0},
  {0x100164, 0x5391828, 0x0, 0x0, 0x0, 0x0},
  {0x100174, 0x539182c, 0x0, 0x0, 0x0, 0x0},
  {0x100184, 0x5391830, 0x0, 0x0, 0x0, 0x0},
  {0x100194, 0x5391834, 0x0, 0x0, 0x0, 0x0},
  {0x100134, 0x5391838, 0x0, 0x0, 0x0, 0x0},
  {0x1001b4, 0x539183c, 0x0, 0x0, 0x0, 0x0},
  {0x200000, 0x400021, 0x1402e3e, 0x0, 0x0, 0x0},
  {0x200047, 0x400025, 0x2403920, 0x0, 0x0, 0x0},
  {0x200057, 0x400029, 0x2403924, 0x0, 0x0, 0x0},
  {0x200067, 0x40002d, 0x2403928, 0x0, 0x0, 0x0},
  {0x200077, 0x400031, 0x240392c, 0x0, 0x0, 0x0},
  {0x1000a5, 0x2e0035, 0x0, 0x0, 0x0, 0x0},
  {0x1000b5, 0x2e0039, 0x0, 0x0, 0x0, 0x0},
  {0x1000c5, 0x2e003d, 0x0, 0x0, 0x0, 0x0},
  {0x1000e4, 0x260022, 0x0, 0x0, 0x0, 0x0},
  {0x1000f4, 0x260026, 0x0, 0x0, 0x0, 0x0},
  {0x100104, 0x26002a, 0x0, 0x0, 0x0, 0x0},
  {0x100114, 0x26002e, 0x0, 0x0, 0x0, 0x0},
  {0x100124, 0x260032, 0x0, 0x0, 0x0, 0x0},
  {0x1001a4, 0x260036, 0x0, 0x0, 0x0, 0x0},
  {0x100085, 0x2e003a, 0x0, 0x0, 0x0, 0x0},
  {0x100095, 0x2e003e, 0x0, 0x0, 0x0, 0x0},
  {0x1090d5, 0x1402632, 0x0, 0x0, 0x0, 0x0},
  {0x10a0e5, 0x1402636, 0x0, 0x0, 0x0, 0x0},
  {0x30b000, 0x1402622, 0x1402e35, 0x2403930, 0x0, 0x0},
  {0x30c000, 0x1402626, 0x1402e39, 0x2403934, 0x0, 0x0},
  {0xd028, 0x0, 0x0, 0x0, 0x0, 0x0},
  {0xe038, 0x0, 0x0, 0x0, 0x0, 0x0},
  {0x30f000, 0x140262a, 0x1402e3d, 0x2403938, 0x0, 0x0},
  {0x310000, 0x140262e, 0x1402e3a, 0x240393c, 0x0, 0x0},
  {0x100224, 0x5391840, 0x0, 0x0, 0x0, 0x0},
  {0x100284, 0x5391844, 0x0, 0x0, 0x0, 0x0},
  {0x100244, 0x5391848, 0x0, 0x0, 0x0, 0x0},
  {0x100254, 0x539184c, 0x0, 0x0, 0x0, 0x0},
  {0x100264, 0x5391850, 0x0, 0x0, 0x0, 0x0},
  {0x100274, 0x5391854, 0x0, 0x0, 0x0, 0x0},
  {0x100234, 0x5391858, 0x0, 0x0, 0x0, 0x0},
  {0x100294, 0x539185c, 0x0, 0x0, 0x0, 0x0},
  {0x200000, 0x400041, 0x1402e5e, 0x0, 0x0, 0x0},
  {0x200087, 0x400045, 0x2403940, 0x0, 0x0, 0x0},
  {0x200097, 0x400049, 0x2403944, 0x0, 0x0, 0x0},
  {0x2000a7, 0x40004d, 0x2403948, 0x0, 0x0, 0x0},
  {0x2000b7, 0x400051, 0x240394c, 0x0, 0x0, 0x0},
  {0x100125, 0x2e0055, 0x0, 0x0, 0x0, 0x0},
  {0x100135, 0x2e0059, 0x0, 0x0, 0x0, 0x0},
  {0x100145, 0x2e005d, 0x0, 0x0, 0x0, 0x0},
  {0x1001c4, 0x260042, 0x0, 0x0, 0x0, 0x0},
  {0x1001d4, 0x260046, 0x0, 0x0, 0x0, 0x0},
  {0x1001e4, 0x26004a, 0x0, 0x0, 0x0, 0x0},
  {0x1001f4, 0x26004e, 0x0, 0x0, 0x0, 0x0},
  {0x100204, 0x260052, 0x0, 0x0, 0x0, 0x0},
  {0x100214, 0x260056, 0x0, 0x0, 0x0, 0x0},
  {0x100105, 0x2e005a, 0x0, 0x0, 0x0, 0x0},
  {0x100115, 0x2e005e, 0x0, 0x0, 0x0, 0x0},
  {0x111155, 0x1402652, 0x0, 0x0, 0x0, 0x0},
  {0x112165, 0x1402656, 0x0, 0x0, 0x0, 0x0},
  {0x313000, 0x1402642, 0x1402e55, 0x2403950, 0x0, 0x0},
  {0x314000, 0x1402646, 0x1402e59, 0x2403954, 0x0, 0x0},
  {0x15048, 0x0, 0x0, 0x0, 0x0, 0x0},
  {0x16058, 0x0, 0x0, 0x0, 0x0, 0x0},
  {0x317000, 0x140264a, 0x1402e5d, 0x2403958, 0x0, 0x0},
  {0x318000, 0x140264e, 0x1402e5a, 0x240395c, 0x0, 0x0},
  {0x100304, 0x5391860, 0x0, 0x0, 0x0, 0x0},
  {0x100314, 0x5391864, 0x0, 0x0, 0x0, 0x0},
  {0x100324, 0x5391868, 0x0, 0x0, 0x0, 0x0},
  {0x100374, 0x539186c, 0x0, 0x0, 0x0, 0x0},
  {0x100344, 0x5391870, 0x0, 0x0, 0x0, 0x0},
  {0x100354, 0x5391874, 0x0, 0x0, 0x0, 0x0},
  {0x1002b4, 0x5391878, 0x0, 0x0, 0x0, 0x0},
  {0x100334, 0x539187c, 0x0, 0x0, 0x0, 0x0},
  {0x200000, 0x400061, 0x1402e7e, 0x0, 0x0, 0x0},
  {0x2000c7, 0x400065, 0x2403960, 0x0, 0x0, 0x0},
  {0x2000d7, 0x400069, 0x2403964, 0x0, 0x0, 0x0},
  {0x2000e7, 0x40006d, 0x2403968, 0x0, 0x0, 0x0},
  {0x2000f7, 0x400071, 0x240396c, 0x0, 0x0, 0x0},
  {0x100175, 0x2e0075, 0x0, 0x0, 0x0, 0x0},
  {0x100006, 0x2e0079, 0x0, 0x0, 0x0, 0x0},
  {0x100016, 0x2e007d, 0x0, 0x0, 0x0, 0x0},
  {0x1002a4, 0x260062, 0x0, 0x0, 0x0, 0x0},
  {0x100364, 0x260066, 0x0, 0x0, 0x0, 0x0},
  {0x1002c4, 0x26006a, 0x0, 0x0, 0x0, 0x0},
  {0x1002d4, 0x26006e, 0x0, 0x0, 0x0, 0x0},
  {0x1002e4, 0x260072, 0x0, 0x0, 0x0, 0x0},
  {0x1002f4, 0x260076, 0x0, 0x0, 0x0, 0x0},
  {0x100075, 0x2e007a, 0x0, 0x0, 0x0, 0x0},
  {0x1000f5, 0x2e007e, 0x0, 0x0, 0x0, 0x0},
  {0x119026, 0x1402672, 0x0, 0x0, 0x0, 0x0},
  {0x11a036, 0x1402676, 0x0, 0x0, 0x0, 0x0},
  {0x31b000, 0x1402662, 0x1402e75, 0x2403970, 0x0, 0x0},
  {0x31c000, 0x1402666, 0x1402e79, 0x2403974, 0x0, 0x0},
  {0x1d068, 0x0, 0x0, 0x0, 0x0, 0x0},
  {0x1e078, 0x0, 0x0, 0x0, 0x0, 0x0},
  {0x31f000, 0x140266a, 0x1402e7d, 0x2403978, 0x0, 0x0},
  {0x320000, 0x140266e, 0x1402e7a, 0x240397c, 0x0, 0x0},
};
constexpr int PFN = 32;
__device__ const unsigned PFT[8][PFN + 1] = {
  {0x40000f, 0x41000f, 0x42000f, 0x43000f, 0x44000f, 0x45000f, 0x46000f, 0x47000f, 0x48000f, 0x49000f, 0x4a000f, 0x4b000f, 0x4c000f, 0x4d000f, 0x4e000f, 0x4f000f, 0x40000c, 0x41000c, 0x42000c, 0x43000c, 0x40002c, 0x41002c, 0x42002c, 0x43002c, 0x40004c, 0x41004c, 0x42004c, 0x43004c, 0x40000d, 0x41000d, 0x42000d, 0x43000d, 0x0},
  {0x40006c, 0x41006c, 0x42006c, 0x43006c, 0x40008c, 0x41008c, 0x42008c, 0x43008c, 0x40006d, 0x41006d, 0x42006d, 0x43006d, 0x40006e, 0x41006e, 0x42006e, 0x43006e, 0x40008d, 0x41008d, 0x42008d, 0x43008d, 0x40008e, 0x41008e, 0x42008e, 0x43008e, 0x4000ad, 0x4100ad, 0x4200ad, 0x4300ad, 0x4000ae, 0x4100ae, 0x4200ae, 0x4300ae, 0x0},
  {0x40002f, 0x41002f, 0x42002f, 0x43002f, 0x44002f, 0x45002f, 0x46002f, 0x47002f, 0x48002f, 0x49002f, 0x4a002f, 0x4b002f, 0x4c002f, 0x4d002f, 0x4e002f, 0x4f002f, 0x4000ac, 0x4100ac, 0x4200ac, 0x4300ac, 0x4000cc, 0x4100cc, 0x4200cc, 0x4300cc, 0x4000ec, 0x4100ec, 0x4200ec, 0x4300ec, 0x4000cd, 0x4100cd, 0x4200cd, 0x4300cd, 0x0},
  {0x40000c, 0x41000c, 0x42000c, 0x43000c, 0x40002c, 0x41002c, 0x42002c, 0x43002c, 0x40004c, 0x41004c, 0x42004c, 0x43004c, 0x40006c, 0x41006c, 0x42006c, 0x43006c, 0x40008c, 0x41008c, 0x42008c, 0x43008c, 0x4000ac, 0x4100ac, 0x4200ac, 0x4300ac, 0x4000cc, 0x4100cc, 0x4200cc, 0x4300cc, 0x4000ec, 0x4100ec, 0x4200ec, 0x4300ec, 0x0},
  {0x40000d, 0x41000d, 0x42000d, 0x43000d, 0x40002d, 0x41002d, 0x42002d, 0x43002d, 0x40000e, 0x41000e, 0x42000e, 0x43000e, 0x40002e, 0x41002e, 0x42002e, 0x43002e, 0x600001, 0x600002, 0x620001, 0x620002, 0x601001, 0x601002, 0x621001, 0x621002, 0x602001, 0x602002, 0x622001, 0x622002, 0x603001, 0x603002, 0x623001, 0x623002, 0x0},
  {0x40004d, 0x41004d, 0x42004d, 0x43004d, 0x40006d, 0x41006d, 0x42006d, 0x43006d, 0x40004e, 0x41004e, 0x42004e, 0x43004e, 0x40006e, 0x41006e, 0x42006e, 0x43006e, 0x600041, 0x600042, 0x620041, 0x620042, 0x601041, 0x601042, 0x621041, 0x621042, 0x602041, 0x602042, 0x622041, 0x622042, 0x603041, 0x603042, 0x623041, 0x623042, 0x0},
  {0x40008d, 0x41008d, 0x42008d, 0x43008d, 0x4000ad, 0x4100ad, 0x4200ad, 0x4300ad, 0x40008e, 0x41008e, 0x42008e, 0x43008e, 0x4000ae, 0x4100ae, 0x4200ae, 0x4300ae, 0x600081, 0x600082, 0x620081, 0x620082, 0x601081, 0x601082, 0x621081, 0x621082, 0x602081, 0x602082, 0x622081, 0x622082, 0x603081, 0x603082, 0x623081, 0x623082, 0x0},
  {0x4000cd, 0x4100cd, 0x4200cd, 0x4300cd, 0x4000ed, 0x4100ed, 0x4200ed, 0x4300ed, 0x4000ce, 0x4100ce, 0x4200ce, 0x4300ce, 0x4000ee, 0x4100ee, 0x4200ee, 0x4300ee, 0x6000c1, 0x6000c2, 0x6200c1, 0x6200c2, 0x6010c1, 0x6010c2, 0x6210c1, 0x6210c2, 0x6020c1, 0x6020c2, 0x6220c1, 0x6220c2, 0x6030c1, 0x6030c2, 0x6230c1, 0x6230c2, 0x0},
};
__global__ void __launch_bounds__(512, 2) k_mega(const u16* __restrict__ Wq, const u16* __restrict__ Wk, const u16* __restrict__ Wv,
                                                 const u16* __restrict__ Wo, const u16* __restrict__ w1, const u16* __restrict__ w3,
                                                 const u16* __restrict__ w2, const u16* __restrict__ x, const u16* __restrict__ cvec,
                                                 const u16* __restrict__ wmod, char* ws, u16* out) {
  extern __shared__ __attribute__((aligned(16))) char smem[];
  int wave_id_ = __builtin_amdgcn_readfirstlane((int)threadIdx.x >> 6);
  asm volatile("" : "+s"(wave_id_));
  unsigned* tmo = (unsigned*)(ws + OFF_TMO); unsigned* tick = (unsigned*)(ws + OFF_CNT);
  unsigned long long* mts = nullptr;
  const float* mod = (const float*)(ws + OFF_MOD);
  volatile LAS unsigned* tslot = (volatile LAS unsigned*)(smem + L_SLOT + 768);
  if (opaque_tid() == 0) {
    unsigned xcc; asm volatile("s_getreg_b32 %0, hwreg(HW_REG_XCC_ID)" : "=s"(xcc));
    const int4* cq = (const int4*)(ws + OFF_CAL + 4 * NCALW * 4);
    const int4 ca = cq[0], cb = cq[1], cc = cq[2], cd = cq[3];
    unsigned sl = 0xFFFFu;
    for (int i = 0; i < 8; ++i) {
      const unsigned xg = (xcc + i) & 7;
      const unsigned t = __hip_atomic_fetch_add(tick + (TICK + xg) * 32, 1u, __ATOMIC_RELAXED, __HIP_MEMORY_SCOPE_AGENT);
      if (t < 32) { sl = xg * 32 + t; break; }
    }
    const int sum = ca.x + ca.y + ca.z + ca.w + cb.x + cb.y + cb.z + cb.w + cc.x + cc.y + cc.z + cc.w + cd.x + cd.y + cd.z + cd.w;
    *tslot = sl | ((unsigned)((sum > 0 ? 0 : 1) ^ XPARC) << 16);
  }
  __syncthreads();
  const unsigned tsl = __builtin_amdgcn_readfirstlane(*tslot);
  __syncthreads();
  const int slot = (int)(tsl & 0xFFFFu);
  unsigned* cnt = (unsigned*)(xch_base(ws) + ((size_t)4 << 21) + (size_t)((tsl >> 16) << 13));
  if (slot >= 256) return;
  const int xg = slot >> 5;
  const unsigned rw = ROLES[slot][0];
  const int role = rw & 15, idx = (rw >> 4) & 255;
  const char* cb = uni((const char*)cnt);
  if (role == T_QV || role == T_KR) {
    char* xch = xch_base(ws);
    const int* cal = (const int*)(ws + OFF_CAL);
    auto chunk = [&](int c) { int sum = 0; for (int w = 0; w < NCALW; ++w) sum += cal[c * NCALW + w]; return uni(xch + ((size_t)c << 21) + (XPARF(sum) << 13)); };
    const char* xb = chunk(3);
    const char* ob = chunk(0);
    const int wg = role == T_QV ? (idx >> 3) * 32 + (idx & 7) : (idx >> 4) * 32 + 8 + (idx & 15);
    if (role == T_QV) qk_body(smem, wave_id_, xg, idx, wg, Wq, Wv, ws, tmo, cnt, xb, ob);
    else vr_body(smem, wave_id_, xg, idx >> 1, idx & 1, wg, Wk, Wv, Wo, ws, tmo, cnt, xb, ob);
    return;
  }
  if ((rw >> 12) & 255) l1_role(smem, wave_id_, ((rw >> 12) & 255) - 1, x, mod, cvec, wmod, ws + OFF_H1, cnt, tmo, mts);
  if (rw >> 24) {
    const int tid = opaque_tid();
    const size_t o = (size_t)(tid >> 4) * HD + ((rw >> 24) - 1) * DH + (tid & 15) * 8;
    u32x4 acc = u32x4{0u, 0u, 0u, 0u};
#pragma unroll 1
    for (int i = PF0; i < 4; ++i) {
      u32x4 v[8];
#pragma unroll
      for (int j = 0; j < 8; ++j) v[j] = ldnt<4>(Wv + o + (size_t)(i * 256 + 32 * j) * HD);
#pragma unroll
      for (int j = 0; j < 8; ++j) acc ^= v[j];
    }
    asm volatile("" :: "v"(acc));
  }
  const int ntask = (rw >> 20) & 15;
  if (ntask == 0 && role == T_WO) { if (wave_id_ == 0) wait_k0(cnt, idx, tmo); __syncthreads(); }
  for (int it = 0; it < ntask; ++it) {
    const unsigned tk = ROLES[slot][1 + it];
    const int m = tk & 3, h = (tk >> 2) & 31, bo = (tk >> 8) & 255, ns = ((tk >> 16) & 255) - bo, part = (tk >> 24) & 3;
    const int lane = lane_id();
    const Dep d{cb, (unsigned)(PL1 + (lane & 31)) * 128, 8u, (unsigned)-bo};
    if (wave_id_ == 0) {
      if ((tk >> 26) & 1) wait_k0(cnt, h, tmo);
      if (bo) wait_l1(cnt, 8u, tmo);
    }
    __syncthreads();
    gemm7<R_QKV>(smem, wave_id_, (m == 0 ? Wq : (m == 1 ? Wk : Wv)) + h * DH, nullptr, HD, ws + OFF_H1 + (size_t)bo * 65536, 65536, d,
                    ws + (m == 0 ? OFF_Q : (m == 1 ? OFF_K : OFF_V)) + h * 8192 + (size_t)bo * 32 * 8192, 32 * 8192,
                    cnt + (PQKV + h) * 32 + 4 * part + m, tmo, part ? 319 : (h >> 3) * 32 + m * 8 + (h & 7), mts, false,
                    bo ? 0 : NIMM, ns, bo ? ns : 0, bo * 257);
  }
  __syncthreads();
  if (role == T_WO || role == T_F2) {
    const bool wo = role == T_WO;
    const int ks = idx >> 3, cn = idx & 7;
    const int lane = lane_id();
    const Dep d{cb, (unsigned)(wo ? PROG + 8 * ks + (lane & 7) : PF1 + 16 * ks + (lane & 15)) * 128, 1u, 0u};
    gemm7<R_PART>(smem, wave_id_, (wo ? Wo : w2) + (size_t)ks * 1024 * DM + cn * 128, nullptr, DM, ws + (wo ? OFF_O : OFF_G) + ks * 16 * 4096, (wo ? 64 : 56) * 4096, d,
                  ws + (wo ? OFF_P : OFF_P2) + (ks * DM + cn * 128) * (TLP ? 64 : 2), (size_t)32 * PROW, cnt + ((wo ? PWO : PF2) + idx) * 32, tmo,
                  wo ? 128 + ks * 32 + cn : 128 + (idx / 7) * 32 + 22 + idx % 7, mts, !wo);
  } else if (role == T_F2C) {
    const int lane = lane_id();
    const Dep d{cb, (unsigned)(PF1 + 48 + (lane & 7)) * 128, 1u, 0u};
    gemm7<R_PART2>(smem, wave_id_, w2 + (size_t)3072 * DM + idx * 256, nullptr, DM, ws + OFF_G + 48 * 4096, 56 * 4096, d,
                   ws + OFF_P2 + (3 * DM + idx * 256) * (TLP ? 64 : 2), (size_t)32 * PROW, cnt + (PF2 + 24 + idx) * 32, tmo, 128 + 96 + 25 + idx, mts, true);
  } else if (role == T_F1) {
    const int lane = lane_id();
    const Dep d{cb, (unsigned)((PLN + (lane >> 3)) * 128 + (lane & 7) * 4), 1u, 0u};
    gemm7<R_F1>(smem, wave_id_, w1 + 64 * idx, w3 + 64 * idx, FF, ws + OFF_H2, 65536, d, ws + OFF_G + idx * 4096, 56 * 4096, cnt + (PF1 + idx) * 32, tmo,
                128 + (idx / 14) * 32 + 8 + idx % 14, mts, false);
  } else if (role == T_FIN) {
    fin_role(smem, wave_id_, idx, mod, ws + OFF_P2, (const u16*)(ws + OFF_X1), out, cnt, tmo, mts);
  } else if (role == T_LN) {
    row_role(smem, wave_id_, 8 * (idx >> 1), idx & 1, mod, ws + OFF_P, x, (u16*)(ws + OFF_X1), ws + OFF_H2, cnt, tmo, 256 + (idx >> 1), mts);
  }
}

#define PFNT 0
constexpr int NARM = 8 + 160;
#define NCS 6
__global__ void __launch_bounds__(256) k_prep(const u16* __restrict__ c, const u16* __restrict__ wmod, const u16* __restrict__ mem0,
                                              float* __restrict__ mod, char* __restrict__ ws, unsigned* __restrict__ cnt,
                                              const u16* __restrict__ Wq, const u16* __restrict__ Wk, const u16* __restrict__ Wv, const u16* __restrict__ Wo) {
  const int t = threadIdx.x;
  int b = blockIdx.x;
  char* xch = xch_base(ws);
  if (b < NCALB) {
    if (t >= 64) return;
    const int ci = b % 3, w = b / 3, chunk = ci == 0 ? 0 : ci + 2;
    char* base = xch + ((size_t)chunk << 21) + 127 * 16384 + w * 512;
    st16_sc1(base + (t >> 5) * 8192 + (t & 31) * 16, u32x4{SENT, SENT, SENT, SENT});
    asm volatile("s_waitcnt vmcnt(0)" ::: "memory");
    unsigned best[2] = {1u << 30, 1u << 30};
    const char* sb = uni(base);
#pragma unroll
    for (int r = 1; r < NCS; ++r) {
      unsigned long long t0, t1; unsigned v;
      asm volatile("s_memtime %0\n\ts_waitcnt lgkmcnt(0)\n\ts_load_dword %2, %3, %4 glc\n\ts_waitcnt lgkmcnt(0)\n\ts_memtime %1\n\ts_waitcnt lgkmcnt(0)"
                   : "=&s"(t0), "=&s"(t1), "=&s"(v) : "s"(sb), "i"((r & 1) * 8192 + (r >> 1) * 128) : "memory");
      if (r > 1) best[r & 1] = min(best[r & 1], (unsigned)(t1 - t0));
    }
    unsigned xcc; asm volatile("s_getreg_b32 %0, hwreg(HW_REG_XCC_ID)" : "=s"(xcc));
    const int d = max(-250, min(250, (int)best[1] - (int)best[0]));
    if (t == 0) ((int*)(ws + OFF_CAL))[chunk * NCALW + w] = (xcc & 4) ? -d : d;
    return;
  }
  b -= NCALB;
  if (b < NMODB) {
    __shared__ float red[32][65];
    const int cgp = b & 31, ksl = b >> 5;
    const int cg = t & 7, r0 = t >> 3, n0 = cgp * 64 + cg * 8;
    float acc[8] = {0, 0, 0, 0, 0, 0, 0, 0};
    u32x4 w[4]; u16 cv16[4];
#pragma unroll
    for (int i = 0; i < 4; ++i) {
      const int k = ksl * 128 + r0 + 32 * i;
      w[i] = ldnt<1>(wmod + (size_t)k * (6 * DM) + n0);
      cv16[i] = c[k];
    }
#pragma unroll
    for (int i = 0; i < 4; ++i) {
      const float cv = bf2f(cv16[i]);
      const float sv = bfr(cv / (1.0f + __expf(-cv)));
      acc[0] += sv * bflo(w[i].x); acc[1] += sv * bfhi(w[i].x); acc[2] += sv * bflo(w[i].y); acc[3] += sv * bfhi(w[i].y);
      acc[4] += sv * bflo(w[i].z); acc[5] += sv * bfhi(w[i].z); acc[6] += sv * bflo(w[i].w); acc[7] += sv * bfhi(w[i].w);
    }
#pragma unroll
    for (int e = 0; e < 8; ++e) red[r0][cg * 8 + e] = acc[e];
    __syncthreads();
    if (t < 64) {
      float s = 0;
#pragma unroll
      for (int r = 0; r < 32; ++r) s += red[r][t];
      mod[ksl * (2 * DM) + cgp * 64 + t] = s;
    }
    return;
  }
  b -= NMODB;
  if (b >= NARM) {
    unsigned xcc; asm volatile("s_getreg_b32 %0, hwreg(HW_REG_XCC_ID)" : "=s"(xcc));
    const int j = (b - NARM) >> 3;
    unsigned acc = 0;
#pragma unroll
    for (int i = 0; i < PFN / 32; ++i) {
      const unsigned e = PFT[xcc & 7][i * 32 + j];
      if (e) {
        const int m = e & 3, wide = (e >> 21) & 1, row = ((e >> 8) & 8191) + (wide ? t >> 4 : t), cl = ((e >> 2) & 63) + (wide ? t & 15 : 0);
        const unsigned* p = (const unsigned*)((m == 0 ? Wq : (m == 1 ? Wk : (m == 2 ? Wv : Wo))) + (size_t)row * (m == 3 ? DM : HD) + cl * 64);
        acc ^= PFNT ? __builtin_nontemporal_load(p) : *p;
      }
    }
    asm volatile("" :: "v"(acc));
    return;
  }
  if (b >= 8) {
    b -= 8;
    char* p = b < 96 ? xch + (size_t)b * 16384 : (b < 152 ? xch + ((size_t)3 << 21) + (size_t)(b - 96 + 8) * 16384 : xch + ((size_t)4 << 21) + (size_t)(b - 152) * 16384);
    if (b >= 157) return;
    const unsigned v = b < 96 + 40 || (b >= 140 && b < 152) ? SENT : 0u;
#pragma unroll
    for (int i = 0; i < 4; ++i) *(u32x4*)(p + i * 4096 + t * 16) = u32x4{v, v, v, v};
    return;
  }
  {
    const int rep = b & 3, par = b >> 2;
    if (b == 2 && t < 64) *(u32x4*)(cnt + t * 4) = u32x4{0u, 0u, 0u, 0u};
    if (b == 1 && t == 0) *(unsigned*)(ws + OFF_TMO) = 0;
    for (int i = 0; i < 4; ++i) {
      const int u = t + 256 * i, pairidx = u >> 6, lane = u & 63, g = lane >> 4, hf = (lane >> 1) & 1, row = ((lane >> 1) & 6) + (lane & 1);
      const int k0 = 64 * pairidx + 16 * g + 8 * hf;
      u32x4 v = *(const u32x4*)(mem0 + row * DM + k0);
      v.x = nosent(v.x); v.y = nosent(v.y); v.z = nosent(v.z); v.w = nosent(v.w);
      *(u32x4*)(xch + ((size_t)3 << 21) + (size_t)(rep * 2 + (u >> 9)) * 16384 + par * 8192 + (u & 511) * 16) = v;
    }
  }
}
extern "C" void kernel_launch(void* const* d_in, const int* in_sizes, int n_in, void* d_out, int out_size, void* d_ws, size_t ws_size,
                              hipStream_t stream) {
  const u16* x    = (const u16*)d_in[0];
  const u16* mem0 = (const u16*)d_in[1];
  const u16* Wq   = (const u16*)d_in[2];
  const u16* Wk   = (const u16*)d_in[3];
  const u16* Wv   = (const u16*)d_in[4];
  const u16* Wo   = (const u16*)d_in[5];
  const u16* w1   = (const u16*)d_in[8];
  const u16* w3   = (const u16*)d_in[9];
  const u16* w2   = (const u16*)d_in[10];
  const u16* c    = (const u16*)d_in[11];
  const u16* wmod = (const u16*)d_in[12];
#define WSHIFT 0
  char* ws = (char*)d_ws + ((size_t)WSHIFT << 20);
  k_prep<<<NCALB + NMODB + NARM + (PFN > 0 ? 256 : 0), 256, 0, stream>>>(c, wmod, mem0, (float*)(ws + OFF_MOD), ws, (unsigned*)(ws + OFF_CNT), Wq, Wk, Wv, Wo);
  hipFuncSetAttribute((const void*)k_mega, hipFuncAttributeMaxDynamicSharedMemorySize, MEGA_LDS);
  k_mega<<<256, 512, MEGA_LDS, stream>>>(Wq, Wk, Wv, Wo, w1, w3, w2, x, c, wmod, ws, (u16*)d_out);
}
```

```cpp
#include <hip/hip_runtime.h>
#include <hip/hip_bf16.h>
#include <stdint.h>
#include <type_traits>
#pragma clang diagnostic ignored "-Winline-asm"
#pragma clang diagnostic ignored "-Wunused-value"

typedef unsigned short u16;
typedef short s16x4 __attribute__((ext_vector_type(4)));
typedef __bf16 bf16x8 __attribute__((ext_vector_type(8)));
typedef float f32x4 __attribute__((ext_vector_type(4)));
typedef float f32x2 __attribute__((ext_vector_type(2)));
typedef unsigned u32x4 __attribute__((ext_vector_type(4)));
typedef unsigned u32x2 __attribute__((ext_vector_type(2)));
#define LAS __attribute__((address_space(3)))

constexpr int SEQ = 2048, DM = 1024, NH = 32, DH = 128, HD = 4096, NMEM = 8, SEG = 32, NSEG = 64, FF = 3584;
constexpr unsigned SENT = 0xFFFFFFFFu;

__device__ __forceinline__ int lane_id() { int l; asm volatile("v_mbcnt_lo_u32_b32 %0, -1, 0\n\tv_mbcnt_hi_u32_b32 %0, -1, %0" : "=v"(l)); return l; }
#define WAVE_ID wave_id_
#define opaque_tid() (WAVE_ID * 64 + lane_id())
__device__ __forceinline__ float bf2f(u16 v) { return __builtin_bit_cast(float, (unsigned)v << 16); }
__device__ __forceinline__ float bflo(unsigned v) { return __builtin_bit_cast(float, v << 16); }
__device__ __forceinline__ float bfhi(unsigned v) { return __builtin_bit_cast(float, v & 0xffff0000u); }
__device__ __forceinline__ float bfr(float x) { return (float)(__bf16)x; }
__device__ __forceinline__ unsigned pack2(float a, float b) {
  typedef __bf16 bf16x2 __attribute__((ext_vector_type(2)));
  bf16x2 v; v[0] = (__bf16)a; v[1] = (__bf16)b; return __builtin_bit_cast(unsigned, v);
}
__device__ __forceinline__ unsigned nosent(unsigned v) { return v == SENT ? 0x7FC07FC0u : v; }
__device__ __forceinline__ bf16x8 asbf8(u32x4 v) { return __builtin_bit_cast(bf16x8, v); }
__device__ __forceinline__ s16x4 ass4(u32x2 v) { return __builtin_bit_cast(s16x4, v); }
__device__ __forceinline__ u32x2 trread(const char* lds_base, unsigned off) {
  s16x4 v = __builtin_amdgcn_ds_read_tr16_b64_v4i16((LAS s16x4*)(lds_base + off));
  return __builtin_bit_cast(u32x2, v);
}
__device__ __forceinline__ void glds16(const void* gsrc, unsigned lds_dst) {
  unsigned keep;
  asm volatile("s_mov_b32 %0, m0\n\ts_mov_b32 m0, %2\n\ts_nop 0\n\tglobal_load_lds_dwordx4 %1, off\n\ts_mov_b32 m0, %0"
               : "=&s"(keep) : "v"(gsrc), "s"(lds_dst) : "memory");
}
#define CNT_ " nt"
#define WNT_ " nt"
__device__ __forceinline__ void glds16_w(const void* gsrc, unsigned lds_dst) {
  unsigned keep;
  asm volatile("s_mov_b32 %0, m0\n\ts_mov_b32 m0, %2\n\ts_nop 0\n\tglobal_load_lds_dwordx4 %1, off" CNT_ "\n\ts_mov_b32 m0, %0"
               : "=&s"(keep) : "v"(gsrc), "s"(lds_dst) : "memory");
}
__device__ __forceinline__ void glds16_sc1(const void* gsrc, unsigned lds_dst) {
  unsigned keep;
  asm volatile("s_mov_b32 %0, m0\n\ts_mov_b32 m0, %2\n\ts_nop 0\n\tglobal_load_lds_dwordx4 %1, off sc1\n\ts_mov_b32 m0, %0"
               : "=&s"(keep) : "v"(gsrc), "s"(lds_dst) : "memory");
}
#define NTM 17
template <int B> __device__ __forceinline__ u32x4 ldnt(const void* p) { if (NTM & B) return __builtin_nontemporal_load((const u32x4*)p); else return *(const u32x4*)p; }
#define MFMA32(a, b, c) __builtin_amdgcn_mfma_f32_16x16x32_bf16((a), (b), (c), 0, 0, 0)

__device__ __forceinline__ void st16_sc1(void* p, u32x4 v) { asm volatile("global_store_dwordx4 %0, %1, off sc1\n\ts_nop 1" :: "v"(p), "v"(v) : "memory"); }
__device__ __forceinline__ void st8_sc1(void* p, u32x2 v) { asm volatile("global_store_dwordx2 %0, %1, off sc1\n\ts_nop 1" :: "v"(p), "v"(v) : "memory"); }
template <bool WT> __device__ __forceinline__ void st8(void* p, u32x2 v) { if (WT) st8_sc1(p, v); else *(u32x2*)p = v; }
template <bool WT> __device__ __forceinline__ void st16(void* p, u32x4 v) { if (WT) st16_sc1(p, v); else *(u32x4*)p = v; }
__device__ __forceinline__ u32x4 ld16_sc1(const void* p) {
  u32x4 r; asm volatile("global_load_dwordx4 %0, %1, off sc1\n\ts_waitcnt vmcnt(0)" : "=&v"(r) : "v"(p) : "memory"); return r;
}
__device__ __forceinline__ void ld16x4_sc1(const void* p, u32x4& a, u32x4& b, u32x4& c, u32x4& d) {
  asm volatile("global_load_dwordx4 %0, %4, off sc1\n\tglobal_load_dwordx4 %1, %4, off offset:1024 sc1\n\t"
               "global_load_dwordx4 %2, %4, off offset:2048 sc1\n\tglobal_load_dwordx4 %3, %4, off offset:3072 sc1\n\ts_waitcnt vmcnt(0)"
               : "=&v"(a), "=&v"(b), "=&v"(c), "=&v"(d) : "v"(p) : "memory");
}
typedef __amdgpu_buffer_rsrc_t rsrc_t;
__device__ __forceinline__ rsrc_t mkrs(const void* p) { return __builtin_amdgcn_make_buffer_rsrc((void*)p, 0, 0x7FFFFFFF, 0x00020000); }
__device__ __forceinline__ u32x4 bld16(rsrc_t r, unsigned off) { return __builtin_amdgcn_raw_buffer_load_b128(r, off, 0, 16); }
__device__ __forceinline__ u32x2 bld8(rsrc_t r, unsigned off) { return __builtin_amdgcn_raw_buffer_load_b64(r, off, 0, 16); }
__device__ __forceinline__ bool ok4(u32x4 v) { return v.x != SENT && v.y != SENT && v.z != SENT && v.w != SENT; }
constexpr int SPIN_CAP = 2000000;
__device__ __forceinline__ bool give_up(int spins, unsigned* tmo, unsigned code) {
  if ((spins & 4095) != 4095) return false;
  if (spins > SPIN_CAP) { __hip_atomic_store(tmo, code, __ATOMIC_RELAXED, __HIP_MEMORY_SCOPE_AGENT); return true; }
  return __hip_atomic_load(tmo, __ATOMIC_RELAXED, __HIP_MEMORY_SCOPE_AGENT) != 0u;
}
template <bool ACQ = true>
__device__ __forceinline__ void wait_cnt(unsigned* c, unsigned n, unsigned* tmo) {
  for (int spins = 0;; ++spins) {
    const unsigned v = __hip_atomic_load(c, __ATOMIC_RELAXED, __HIP_MEMORY_SCOPE_AGENT);
    if (v >= n) break;
    __builtin_amdgcn_s_sleep(8);
    if (give_up(spins, tmo, 5)) break;
  }
  if (ACQ) {
    __builtin_amdgcn_fence(__ATOMIC_ACQUIRE, "agent");
    asm volatile("s_waitcnt vmcnt(0)" ::: "memory");
  }
}

__device__ __forceinline__ const char* uni(const char* p) {
  const unsigned long long v = (unsigned long long)p;
  const unsigned lo = __builtin_amdgcn_readfirstlane((unsigned)v), hi = __builtin_amdgcn_readfirstlane((unsigned)(v >> 32));
  return (const char*)(((unsigned long long)hi << 32) | lo);
}
#define TSCLK "s_memrealtime"
#define TC(k) do { } while (0)
#define TS(k) do { } while (0)
#define TSDECL do { } while (0)
#define TSFLUSH(mask) do { } while (0)

constexpr size_t OFF_MOD  = 0;
constexpr size_t OFF_MOD2 = 65536;
constexpr size_t OFF_TMO  = 98304;
constexpr size_t OFF_CAL  = 98304 + 256;
constexpr int    NCALW    = 16;
constexpr size_t OFF_CNT  = 131072;
constexpr int    NCNT     = 128;
constexpr size_t OFF_H1   = 262144;
constexpr size_t OFF_Q    = OFF_H1 + (size_t)SEQ * DM * 2;
constexpr size_t OFF_K    = OFF_Q + (size_t)SEQ * HD * 2;
constexpr size_t OFF_V    = OFF_K + (size_t)SEQ * HD * 2;
constexpr size_t OFF_O    = OFF_V + (size_t)SEQ * HD * 2;
constexpr size_t OFF_X1   = OFF_O + (size_t)SEQ * HD * 2;
constexpr size_t OFF_H2   = OFF_X1 + (size_t)SEQ * DM * 2;
constexpr size_t OFF_G    = OFF_H2 + (size_t)SEQ * DM * 2;
constexpr size_t OFF_XCH  = (size_t)100 << 20;
constexpr size_t OFF_P    = (size_t)112 << 20;
constexpr size_t OFF_P2   = (size_t)128 << 20;
constexpr size_t OFF_DUMP = (size_t)144 << 20;
constexpr int PROW = 4 * DM * 2;
#define TLQ 1
#define TLP 1
static_assert(OFF_G + (size_t)SEQ * FF * 2 <= OFF_XCH, "layout");
__device__ __forceinline__ char* xch_base(char* ws) { return (char*)(((size_t)(ws + OFF_XCH) + ((2u << 20) - 1)) & ~(size_t)((2u << 20) - 1)); }
#define XPAR 0
#define XPARF(sum) ((size_t)(((sum) > 0 ? 0 : 1) ^ XPAR))
constexpr int TICK = 0, ARMED = 8;
constexpr int PROG = 16;
constexpr int PQKV = 64;
constexpr int PL1 = 96;
constexpr int PWO = 192;
constexpr int PLN = 224;
constexpr int PF2 = 320;
constexpr int PF1 = 448;
constexpr int L_RED = 131072;
constexpr int L_SLOT = L_RED + 16384;
constexpr int L_GB = L_SLOT + 1024;
constexpr int MEGA_LDS = 163840;
static_assert(L_GB + 12288 + 2048 <= MEGA_LDS, "lds");
constexpr int NMODB = 256;
constexpr int NCALB = 3 * NCALW;
#define XPARC 0

__device__ __forceinline__ int bswz(int row) { return (((row >> 1) & 1) | (((row >> 3) & 1) << 1)) << 1; }
template <int OFF, bool SC1 = false>
__device__ __forceinline__ void glds16s(const char* sbase, unsigned voff, unsigned lds_base) {
  unsigned keep;
  if (SC1)
    asm volatile("s_mov_b32 %0, m0\n\ts_add_u32 m0, %3, %4\n\ts_nop 0\n\tglobal_load_lds_dwordx4 %1, %2 sc1\n\ts_mov_b32 m0, %0"
                 : "=&s"(keep) : "v"(voff), "s"(sbase), "s"(lds_base), "i"(OFF) : "memory", "scc");
  else
    asm volatile("s_mov_b32 %0, m0\n\ts_add_u32 m0, %3, %4\n\ts_nop 0\n\tglobal_load_lds_dwordx4 %1, %2\n\ts_mov_b32 m0, %0"
                 : "=&s"(keep) : "v"(voff), "s"(sbase), "s"(lds_base), "i"(OFF) : "memory", "scc");
}
template <int OFF>
__device__ __forceinline__ void glds16x2s(const char* sbase, unsigned voff, unsigned lds_base) {
  unsigned keep;
  asm volatile("s_mov_b32 %0, m0\n\ts_add_u32 m0, %3, %4\n\ts_nop 0\n\tglobal_load_lds_dwordx4 %1, %2 sc1\n\tglobal_load_lds_dwordx4 %1, %2 offset:1024 sc1\n\ts_mov_b32 m0, %0"
               : "=&s"(keep) : "v"(voff), "s"(sbase), "s"(lds_base), "i"(OFF) : "memory", "scc");
}
__device__ __forceinline__ void st16s(const char* sb, unsigned vo, u32x4 v) { asm volatile("global_store_dwordx4 %0, %1, %2 sc1\n\ts_nop 1" :: "v"(vo), "v"(v), "s"(sb) : "memory"); }
__device__ __forceinline__ void head_to_lds(const char* src, int lane, unsigned lds) {
  if (TLQ) {
    const int c = (lane & 15) ^ (lane >> 4);
    const unsigned v0 = (unsigned)((c >> 2) * 2048 + (lane >> 4) * 64 + (c & 3) * 16);
#pragma unroll
    for (int i = 0; i < 8; ++i) glds16s<0, true>(src + i * 256, v0 ^ ((i & 3) * 2048), lds + i * 1024);
  } else {
#pragma unroll
    for (int i = 0; i < 4; ++i) glds16x2s<0>(src + i * 2048, (unsigned)lane * 16, lds + i * 2048);
  }
}
#define WAIT_V(n) asm volatile("s_waitcnt vmcnt(" #n ")" ::: "memory")
#define BAR() do { asm volatile("s_waitcnt lgkmcnt(0)" ::: "memory"); __builtin_amdgcn_s_barrier(); asm volatile("" ::: "memory"); } while (0)
__device__ __forceinline__ void st4_sc1(void* p, unsigned v) { asm volatile("global_store_dword %0, %1, off sc1" :: "v"(p), "v"(v) : "memory"); }
__device__ __forceinline__ unsigned ld4_sc1(const void* p) {
  unsigned r; asm volatile("global_load_dword %0, %1, off sc1\n\ts_waitcnt vmcnt(0)" : "=&v"(r) : "v"(p) : "memory"); return r;
}
template <int CTRL> __device__ __forceinline__ unsigned dppu(unsigned x) { return (unsigned)__builtin_amdgcn_update_dpp(0, (int)x, CTRL, 0xf, 0xf, false); }
__device__ __forceinline__ unsigned rowmin16(unsigned v) {
  v = min(v, dppu<0x128>(v)); v = min(v, dppu<0x124>(v)); v = min(v, dppu<0x122>(v)); v = min(v, dppu<0x121>(v));
  return v;
}
__device__ __forceinline__ unsigned rdl(unsigned v, int l) { return (unsigned)__builtin_amdgcn_readlane((int)v, l); }
constexpr float SCALE = 0.08838834764831845f;
constexpr float LOG2E = 1.4426950408889634f;
template <int N> __device__ __forceinline__ void delay64() {
  if (N >= 16) { __builtin_amdgcn_s_sleep(15); delay64<(N >= 16 ? N - 15 : 0)>(); } else if (N > 0) __builtin_amdgcn_s_sleep(N);
}
__device__ __forceinline__ void sw16(float x, float& a, float& b) {
  a = x; b = x; asm volatile("s_nop 1\n\tv_permlane16_swap_b32 %0, %1" : "+v"(a), "+v"(b));
}
__device__ __forceinline__ void sw32(float x, float& a, float& b) {
  a = x; b = x; asm volatile("s_nop 1\n\tv_permlane32_swap_b32 %0, %1" : "+v"(a), "+v"(b));
}
__device__ __forceinline__ float red16_32_max(float x) {
  float a, b; sw16(x, a, b); x = fmaxf(a, b); sw32(x, a, b); return fmaxf(a, b);
}
__device__ __forceinline__ float red16_32_add(float x) {
  float a, b; sw16(x, a, b); x = a + b; sw32(x, a, b); return a + b;
}
__device__ __forceinline__ unsigned cvtpk(float a, float b) { unsigned r; asm("v_cvt_pk_bf16_f32 %0, %1, %2" : "=v"(r) : "v"(a), "v"(b)); return r; }
__device__ __forceinline__ float max3(float a, float b, float c) { float r; asm("v_max3_f32 %0, %1, %2, %3" : "=v"(r) : "v"(a), "v"(b), "v"(c)); return r; }
__device__ __forceinline__ float max2(float a, float b) { float r; asm("v_max_f32 %0, %1, %2" : "=v"(r) : "v"(a), "v"(b)); return r; }
__device__ __forceinline__ unsigned ror8u(unsigned x) { return (unsigned)__builtin_amdgcn_update_dpp(0, (int)x, 0x128, 0xf, 0xf, false); }
__device__ __forceinline__ float ror8(float x) { return __builtin_bit_cast(float, ror8u(__builtin_bit_cast(unsigned, x))); }
__device__ __forceinline__ float add_ror8(float x) {
  return x + __builtin_bit_cast(float, __builtin_amdgcn_update_dpp(0, __builtin_bit_cast(int, x), 0x128, 0xf, 0xf, false));
}

template <int M0> __device__ __forceinline__ unsigned qkv_avail(const unsigned* line) {
  const unsigned v = ld4_sc1(line + (lane_id() & 15));
  unsigned r = NSEG;
#pragma unroll
  for (int m = M0; m < 3; ++m) {
    unsigned a = rdl(v, m);
#pragma unroll
    for (int p = 1; p < 3; ++p) { const unsigned w = rdl(v, 4 * p + m); if (a >= (w >> 8)) a = max(a, w & 255u); }
    r = min(r, a);
  }
  return r;
}
__device__ __forceinline__ void wait_l1(const unsigned* cnt, unsigned n, unsigned* tmo) {
  const int lane = lane_id();
  for (int spins = 0;; ++spins) {
    const unsigned v = ld4_sc1(cnt + (PL1 + (lane & 31)) * 32);
    if (__all(v >= n)) break;
    __builtin_amdgcn_s_sleep(8);
    if (give_up(spins, tmo, 12)) break;
  }
}
#define GKV 1
__device__ __forceinline__ void wait_k0(const unsigned* cnt, int h, unsigned* tmo, bool kv = true) {
  for (int spins = 0;; ++spins) {
    const int l = lane_id();
    const unsigned v = ld4_sc1(cnt + (PQKV + h) * 32 + (l == 0 || !kv ? 3 : 1 + (l & 1)));
    if (__all(v >= (l == 0 ? 1u : (unsigned)GKV))) break;
    __builtin_amdgcn_s_sleep(32);
    if (give_up(spins, tmo, 13)) break;
  }
}
#define KTON 0
#define WCLK(slot, b) do { } while (0)
#define WTM(slot, b, k) do { } while (0)
#define WTS(slot, b, k) do { } while (0)
#define KTS(k_) do { } while (0)
#define STS(ws_, slot_, k_) do { } while (0)
typedef __bf16 bf16x2 __attribute__((ext_vector_type(2)));
__device__ __forceinline__ f32x2 pkfma(f32x2 a, f32x2 b, f32x2 c) { return __builtin_elementwise_fma(a, b, c); }
__device__ __forceinline__ float wavesum(float v) {
  v += __builtin_bit_cast(float, dppu<0x128>(__builtin_bit_cast(unsigned, v))); v += __builtin_bit_cast(float, dppu<0x124>(__builtin_bit_cast(unsigned, v)));
  v += __builtin_bit_cast(float, dppu<0x122>(__builtin_bit_cast(unsigned, v))); v += __builtin_bit_cast(float, dppu<0x121>(__builtin_bit_cast(unsigned, v)));
  return red16_32_add(v);
}
__device__ __forceinline__ void bst16(rsrc_t r, unsigned off, unsigned soff, u32x4 v) { __builtin_amdgcn_raw_buffer_store_b128(v, r, off + soff, 0, 16); }
__device__ __forceinline__ float adds(unsigned v, unsigned sel, float c) { return __builtin_amdgcn_fdot2_f32_bf16(__builtin_bit_cast(bf16x2, v), __builtin_bit_cast(bf16x2, sel), c, false); }
__device__ __forceinline__ void sum4(const u32x4 (&p)[4], f32x2 (&a)[4]) {
  unsigned slo = 0x00003F80u, shi = 0x3F800000u;
  asm("" : "+s"(slo), "+s"(shi));
#pragma unroll
  for (int e = 0; e < 4; ++e) {
    float lo = bflo(p[0][e]), hi = bfhi(p[0][e]);
#pragma unroll
    for (int q = 1; q < 4; ++q) { lo = adds(p[q][e], slo, lo); hi = adds(p[q][e], shi, hi); }
    a[e] = f32x2{lo, hi};
  }
}
__device__ __forceinline__ u32x4 norm8(const f32x2 (&v)[4], const char* mt, float mb, float rden) {
  const f32x4 h0 = *(const f32x4*)(mt + 4096), h1 = *(const f32x4*)(mt + 4096 + 1024), c0 = *(const f32x4*)(mt + 8192), c1 = *(const f32x4*)(mt + 8192 + 1024);
  const f32x2 m2 = f32x2{mb, mb}, d2 = f32x2{rden, rden};
  const f32x2 r0 = pkfma(v[0] - m2, f32x2{c0[0], c0[1]} * d2, f32x2{h0[0], h0[1]}), r1 = pkfma(v[1] - m2, f32x2{c0[2], c0[3]} * d2, f32x2{h0[2], h0[3]});
  const f32x2 r2 = pkfma(v[2] - m2, f32x2{c1[0], c1[1]} * d2, f32x2{h1[0], h1[1]}), r3 = pkfma(v[3] - m2, f32x2{c1[2], c1[3]} * d2, f32x2{h1[2], h1[3]});
  return u32x4{pack2(r0[0], r0[1]), pack2(r1[0], r1[1]), pack2(r2[0], r2[1]), pack2(r3[0], r3[1])};
}
__device__ __forceinline__ void half_load(const char* P, const u16* res, int row, int hf, int lane, f32x2 (&a)[4], u32x4& rv) {
  const rsrc_t rp = mkrs(P), rr = mkrs(res);
  const unsigned po = TLP ? (unsigned)((row >> 5) * 32 * PROW + (hf * 16 + (lane >> 2)) * 2048 + (row & 31) * 64 + (lane & 3) * 16) : (unsigned)(row * PROW + hf * 1024 + lane * 16), ro = (unsigned)(row * DM * 2 + hf * 1024 + lane * 16);
  u32x4 p[4];
#pragma unroll
  for (int q = 0; q < 4; ++q) p[q] = bld16(rp, po + q * (TLP ? 65536 : 2048));
  if (res) rv = bld16(rr, ro);
  sum4(p, a);
}
__device__ __forceinline__ void fin_half(const char* P, const u16* res, const char* gbt, u16* __restrict__ o1, int row, int hf, int lane) {
  f32x2 a[4]; u32x4 rv;
  half_load(P, res, row, hf, lane, a, rv);
  const u32x4 gv = *(const u32x4*)(gbt + hf * 1024 + lane * 16);
  u32x4 o;
#pragma unroll
  for (int e = 0; e < 4; ++e) { const f32x2 s = pkfma(f32x2{bflo(gv[e]), bfhi(gv[e])}, a[e], f32x2{bflo(rv[e]), bfhi(rv[e])}); o[e] = pack2(s[0], s[1]); }
  st16_sc1(o1 + (size_t)row * DM + hf * 512 + lane * 8, o);
}
__device__ __forceinline__ void ln_half(const u32x4 (&p)[4], u32x4 rv, const f32x4 (&tb)[6], volatile LAS float* xs, const char* x1, unsigned xo, const char* h2, unsigned ho,
                                        int wave, int lane) {
  f32x2 a[4], v[4];
  sum4(p, a);
  u32x4 o;
#pragma unroll
  for (int e = 0; e < 4; ++e) {
    const f32x2 s = pkfma(f32x2{tb[e >> 1][2 * (e & 1)], tb[e >> 1][2 * (e & 1) + 1]}, a[e], f32x2{bflo(rv[e]), bfhi(rv[e])});
    o[e] = pack2(s[0], s[1]);
    v[e] = f32x2{bflo(o[e]), bfhi(o[e])};
  }
  const float c = __builtin_bit_cast(float, __builtin_amdgcn_readfirstlane(__builtin_bit_cast(int, v[0][0])));
  f32x2 s2 = f32x2{0, 0}, q2 = s2;
#pragma unroll
  for (int i = 0; i < 4; ++i) { const f32x2 d = v[i] - f32x2{c, c}; s2 += d; q2 = pkfma(d, d, q2); }
  float s = s2[0] + s2[1], q = q2[0] + q2[1];
#define DA(x, k) x += __builtin_bit_cast(float, dppu<k>(__builtin_bit_cast(unsigned, x)))
  DA(s, 0x128); DA(q, 0x128); DA(s, 0x124); DA(q, 0x124); DA(s, 0x122); DA(q, 0x122); DA(s, 0x121); DA(q, 0x121);
#undef DA
  {
    asm volatile("s_nop 1\n\tv_permlane16_swap_b32 %0, %1" : "+v"(s), "+v"(q));
    s += q; q = s;
    asm volatile("s_nop 1\n\tv_permlane32_swap_b32 %0, %1" : "+v"(s), "+v"(q));
    s += q;
  }
  if (lane < 48 && (lane & 15) == 0) xs[wave * 4 + (lane >> 4)] = lane == 32 ? c : s;
  st16s(x1, xo, o);
  BAR();
  const f32x4 u0 = *(const volatile LAS f32x4*)(xs + (wave & 6) * 4), u1 = *(const volatile LAS f32x4*)(xs + (wave | 1) * 4);
  const float m0 = u0[0] * (2.0f / DM), m1 = u1[0] * (2.0f / DM);
  const float dm = (u0[2] - u1[2]) + (m0 - m1);
  const float mean = ((u0[2] + m0) + (u1[2] + m1)) * 0.5f;
  const float qq = ((u0[1] - u0[0] * m0) + (u1[1] - u1[0] * m1)) + dm * dm * (DM / 4);
  const float mb = bfr(mean), vb = bfr(fmaxf(qq, 0.0f) * (1.0f / DM));
  const float rden = __builtin_amdgcn_rcpf(bfr(__builtin_amdgcn_sqrtf(bfr(vb + 1e-5f))));
  const f32x2 m2 = f32x2{mb, mb}, d2 = f32x2{rden, rden};
  u32x4 r;
#pragma unroll
  for (int e = 0; e < 4; ++e) {
    const f32x2 t = pkfma(v[e] - m2, f32x2{tb[4 + (e >> 1)][2 * (e & 1)], tb[4 + (e >> 1)][2 * (e & 1) + 1]} * d2, f32x2{tb[2 + (e >> 1)][2 * (e & 1)], tb[2 + (e >> 1)][2 * (e & 1) + 1]});
    r[e] = pack2(t[0], t[1]);
  }
  st16s(h2, ho, r);
}
__device__ __forceinline__ void row_table(char* tab, const float* mod, const unsigned* cnt, unsigned* tmo, int tid) {
  wait_l1(cnt, 9u, tmo);
  const rsrc_t rm = mkrs((const char*)mod + OFF_MOD2);
#pragma unroll
  for (int i = 0; i < 2; ++i) {
    const int u = tid + 512 * i, a = u >> 8, c = u & 255;
    f32x4 v = __builtin_bit_cast(f32x4, bld16(rm, (unsigned)u * 16));
    v = f32x4{bfr(v[0]), bfr(v[1]), bfr(v[2]), bfr(v[3])};
    if (a == 2) v = f32x4{bfr(1.0f + v[0]), bfr(1.0f + v[1]), bfr(1.0f + v[2]), bfr(1.0f + v[3])};
    if (a < 3) *(f32x4*)(tab + a * 4096 + ((c >> 7) * 2 + (c & 1)) * 1024 + ((c >> 1) & 63) * 16) = v;
    else *(u32x2*)(tab + 12288 + c * 8) = u32x2{pack2(v[0], v[1]), pack2(v[2], v[3])};
  }
  __syncthreads();
}
#define LS(i) do { } while (0)
#define LNSL 2
__device__ __forceinline__ void row_role(char* smem, int wave_id_, int grp, int set, const float* mod, const char* P, const u16* x, u16* x1, char* h2c,
                                         unsigned* cnt, unsigned* tmo, int slot, unsigned long long* mts) {
  const int tid = opaque_tid(), lane = tid & 63, wave = WAVE_ID;
  if (tid == 0) *(volatile LAS unsigned*)(smem + 16384 + 256) = 0u;
  row_table(smem, mod, cnt, tmo, tid);
  static_assert(TLP, "");
  const int hf = wave & 1, r4 = 4 * grp + (wave >> 1);
  const unsigned pw2 = (unsigned)(PWO + ((lane >> 2) & 3) * 8 + hf * 4 + (lane & 3)) * 128;
  unsigned* myp = cnt + (PLN + 8 * set + grp) * 32;
  unsigned pv = 0;
  u32x4 p[4], rv, rn;
  bool req = false;
  const char* pb = uni(P); const char* cb = uni((const char*)cnt); const char* xs_ = uni((const char*)x);
  auto publish = [&](int n) {
    if (lane == 0 && (__hip_atomic_fetch_add((LAS unsigned*)(smem + 16384 + 256), 1u, __ATOMIC_RELAXED, __HIP_MEMORY_SCOPE_WORKGROUP) & 7u) == 7u) st4_sc1(myp, (unsigned)n);
  };
  f32x4 tb[6];
#pragma unroll
  for (int i = 0; i < 6; ++i) tb[i] = *(const f32x4*)(smem + (i >> 1) * 4096 + hf * 2048 + (i & 1) * 1024 + lane * 16);
  const unsigned po = (unsigned)((hf * 16 + (lane >> 2)) * 2048 + r4 * 64 + (lane & 3) * 16), xo = (unsigned)(r4 * DM * 2 + hf * 1024 + lane * 16);
  const unsigned ho = (unsigned)(r4 * 128 + (((lane & 7) ^ (r4 & 7)) << 4) + (lane >> 3) * 4096 + hf * 32768);
  rn = *(const u32x4*)(xs_ + set * 65536 + xo);
  for (int bb = set; bb < NSEG; bb += 2) {
    if (!req) {
      for (int spins = 0; !__all(pv > (unsigned)bb); ++spins) {
        pv = ld4_sc1((const char*)cnt + pw2);
        if (__all(pv > (unsigned)bb)) break;
        __builtin_amdgcn_s_sleep(LNSL);
        if (give_up(spins, tmo, 11)) break;
      }
    }
    WTM(slot, bb, 0);
    LS(0);
    asm volatile("s_nop 4\n\tglobal_load_dwordx4 %0, %5, %9 sc1\n\tglobal_load_dwordx4 %1, %6, %9 sc1\n\tglobal_load_dwordx4 %2, %7, %9 sc1\n\tglobal_load_dwordx4 %3, %8, %9 sc1\n\t"
                 "global_load_dword %4, %10, %11 sc1\n\ts_waitcnt vmcnt(0)"
                 : "=&v"(p[0]), "=&v"(p[1]), "=&v"(p[2]), "=&v"(p[3]), "=&v"(pv)
                 : "v"(po), "v"(po + 65536), "v"(po + 131072), "v"(po + 196608), "s"(pb + (size_t)bb * 32 * PROW), "v"(pw2), "s"(cb) : "memory");
    asm volatile("v_mov_b32 %0, %4\n\tv_mov_b32 %1, %5\n\tv_mov_b32 %2, %6\n\tv_mov_b32 %3, %7" : "=&v"(rv.x), "=&v"(rv.y), "=&v"(rv.z), "=&v"(rv.w) : "v"(rn.x), "v"(rn.y), "v"(rn.z), "v"(rn.w) : "memory");
    if (bb + 2 < NSEG) rn = *(const u32x4*)(xs_ + (size_t)(bb + 2) * 65536 + xo);
    LS(1);
    if (req) publish(bb >> 1);
    ln_half(p, rv, tb, (volatile LAS float*)(smem + 16384 + ((bb >> 1) & 1) * 128), uni((const char*)x1) + (size_t)bb * 65536, xo, uni(h2c) + (size_t)bb * 65536, ho, wave, lane);
    LS(2);
    req = bb + 2 < NSEG && __all(pv > (unsigned)(bb + 2));
    if (!req) {
      if (bb + 2 < NSEG) pv = ld4_sc1((const char*)cnt + pw2);
      WAIT_V(0);
      LS(3);
      publish((bb >> 1) + 1);
    }
    WTM(slot, bb, 2);
  }
}
#define L1G 0
__device__ __forceinline__ void l1_role(char* smem, int wave_id_, int c, const u16* __restrict__ x, const float* __restrict__ mod, const u16* __restrict__ cvec,
                                        const u16* __restrict__ wmod, char* __restrict__ h1c, unsigned* cnt, unsigned* tmo, unsigned long long* mts) {
  const int tid = opaque_tid(), lane = tid & 63, wave = WAVE_ID;
  u32x4 xr[8][2];
  const u16* src = x + (size_t)(8 * c + wave) * DM + lane * 8;
  xr[0][0] = ldnt<2>(src); xr[0][1] = ldnt<2>(src + 512);
  {
    f32x4 m[8];
#pragma unroll
    for (int k = 0; k < 8; ++k) m[k] = *(const f32x4*)(mod + k * (2 * DM) + tid * 4);
    const float sva = bf2f(cvec[tid]), svb = bf2f(cvec[tid + 512]);
    f32x4 v = ((m[0] + m[1]) + (m[2] + m[3])) + ((m[4] + m[5]) + (m[6] + m[7]));
    v = f32x4{bfr(v[0]), bfr(v[1]), bfr(v[2]), bfr(v[3])};
    const int a = tid >> 8, cc = tid & 255;
    if (a) v = f32x4{bfr(1.0f + v[0]), bfr(1.0f + v[1]), bfr(1.0f + v[2]), bfr(1.0f + v[3])};
    *(f32x4*)(smem + 4096 + a * 4096 + ((cc >> 7) * 2 + (cc & 1)) * 1024 + ((cc >> 1) & 63) * 16) = v;
    float* sv = (float*)(smem + 12288);
    sv[tid] = bfr(sva / (1.0f + __expf(-sva))); sv[tid + 512] = bfr(svb / (1.0f + __expf(-svb)));
  }
  __syncthreads();
#pragma unroll
  for (int p = 0; p < 8; ++p) {
    f32x2 v[2][4];
#pragma unroll
    for (int i = 0; i < 2; ++i)
#pragma unroll
      for (int e = 0; e < 4; ++e) v[i][e] = f32x2{bflo(xr[p][i][e]), bfhi(xr[p][i][e])};
    const f32x2 s2 = ((v[0][0] + v[0][1]) + (v[0][2] + v[0][3])) + ((v[1][0] + v[1][1]) + (v[1][2] + v[1][3]));
    const float mean = wavesum(s2[0] + s2[1]) * (1.0f / DM);
    f32x2 q2 = f32x2{0, 0};
#pragma unroll
    for (int i = 0; i < 8; ++i) { const f32x2 d = v[i >> 2][i & 3] - f32x2{mean, mean}; q2 = pkfma(d, d, q2); }
    const float q = wavesum(q2[0] + q2[1]);
    if (p == 0 && wave == 0) STS((char*)cnt - OFF_CNT, 128 + (c >> 3) * 32 + 24 + (c & 7), 0);
    const float mb = bfr(mean), vb = bfr(q * (1.0f / DM));
    const float rden = 1.0f / bfr(sqrtf(bfr(vb + 1e-5f)));
    const int row = 256 * p + 8 * c + wave, rb = row & 31;
    char* dst = h1c + (size_t)(row >> 5) * 65536 + (lane >> 3) * 4096 + rb * 128 + (((lane & 7) ^ (rb & 7)) << 4);
    const char* ml = smem + lane * 16;
#pragma unroll
    for (int i = 0; i < 2; ++i) st16_sc1(dst + i * 32768, norm8(v[i], ml + i * 2048, mb, rden));
    WAIT_V(0);
    __builtin_amdgcn_s_barrier();
    if (tid == 0) st4_sc1(cnt + (PL1 + c) * 32, (unsigned)(p + 1));
    if (p == 0) {
      if (L1G) wait_k0(cnt, c, tmo, L1G == 1);
#pragma unroll
      for (int pp = 1; pp < 8; ++pp) { xr[pp][0] = ldnt<2>(src + (size_t)pp * 256 * DM); xr[pp][1] = ldnt<2>(src + (size_t)pp * 256 * DM + 512); }
    }
    if (wave == 0) WTS(c, p, 0);
    if (p == 0 && wave == 0) STS((char*)cnt - OFF_CNT, 128 + (c >> 3) * 32 + 24 + (c & 7), 1);
  }
  wait_k0(cnt, c, tmo);
  {
    const int cg = tid & 15, r0 = tid >> 4;
    const u16* wp = wmod + (size_t)r0 * (6 * DM) + 2 * DM + 128 * c + cg * 8;
    const float* sv = (const float*)(smem + 12288) + r0;
    f32x2 acc[4] = {f32x2{0, 0}, f32x2{0, 0}, f32x2{0, 0}, f32x2{0, 0}};
#pragma unroll 1
    for (int o = 0; o < 4; ++o) {
      u32x4 w[8];
#pragma unroll
      for (int i = 0; i < 8; ++i) w[i] = ldnt<1>(wp + (size_t)(o * 8 + i) * 32 * (6 * DM));
#pragma unroll
      for (int i = 0; i < 8; ++i) {
        const float s = sv[(o * 8 + i) * 32];
#pragma unroll
        for (int e = 0; e < 4; ++e) acc[e] = pkfma(f32x2{s, s}, f32x2{bflo(w[i][e]), bfhi(w[i][e])}, acc[e]);
      }
    }
    float* red = (float*)(smem + 16384);
    *(f32x4*)(red + r0 * 128 + cg * 8) = f32x4{acc[0][0], acc[0][1], acc[1][0], acc[1][1]};
    *(f32x4*)(red + r0 * 128 + cg * 8 + 4) = f32x4{acc[2][0], acc[2][1], acc[3][0], acc[3][1]};
    __syncthreads();
    if (tid < 128) {
      float s = 0;
#pragma unroll
      for (int r = 0; r < 32; ++r) s += red[r * 128 + tid];
      st4_sc1((char*)mod + OFF_MOD2 + (128 * c + tid) * 4, __builtin_bit_cast(unsigned, s));
    }
    WAIT_V(0);
    __builtin_amdgcn_s_barrier();
    if (tid == 0) st4_sc1(cnt + (PL1 + c) * 32, 9u);
  }
}
enum { R_QKV = 0, R_PART = 1, R_F1 = 2, R_PART2 = 3 };
struct Dep {
  const char* base; unsigned voff; unsigned mul, add;
};
__device__ __forceinline__ unsigned dep_eval(unsigned v, const Dep& d) {
  v = rowmin16(v * d.mul + d.add);
  return min(min(rdl(v, 0), rdl(v, 16)), min(rdl(v, 32), rdl(v, 48)));
}
typedef unsigned u32x32 __attribute__((ext_vector_type(32)));
typedef unsigned u32x16 __attribute__((ext_vector_type(16)));
__device__ __forceinline__ void fast_ld0(const u32x32 (&W)[4], u32x16& pd, unsigned ea, unsigned da, int& b, int& lf, int cp, unsigned sa, unsigned mu, unsigned ad,
    unsigned rw, unsigned ab, const char* p1, const char* p0, unsigned l1, unsigned l0, const unsigned (&av)[8]) {
  asm volatile("s_setprio 0\n\t"
    "s_waitcnt vmcnt(12)\n\t"
    "s_barrier\n\t"
    "ds_read_b128 v[96:99], %[ea] offset:0\n\t"
    "ds_read_b128 v[100:103], %[ea] offset:2048\n\t"
    "ds_read_b128 v[104:107], %[da] offset:0\n\t"
    "ds_read_b128 v[108:111], %[da] offset:2048\n\t"
    "ds_read_b128 v[112:115], %[ea] offset:4096\n\t"
    "ds_read_b128 v[116:119], %[ea] offset:6144\n\t"
    "s_branch 2f\n\t"
    "1:\n\t"
    "s_waitcnt vmcnt(12)\n\t"
    "s_nop 1\n\t"
    "s_barrier\n\t"
    "2:\n\t"
    "s_waitcnt lgkmcnt(4)\n\t"
    "v_mfma_f32_16x16x32_bf16 v[64:67], v[128:131], v[96:99], 0\n\t"
    "ds_read_b128 v[120:123], %[da] offset:4096\n\t"
    "s_add_u32 m0, %[l1], 0\n\t"
    "ds_write_b128 %[rw], v[80:83] offset:0\n\t"
    "v_mfma_f32_16x16x32_bf16 v[68:71], v[132:135], v[96:99], 0\n\t"
    "ds_read_b128 v[124:127], %[da] offset:6144\n\t"
    "global_load_lds_dwordx4 %[av0], s[36:37] sc1\n\t"
    "ds_write_b128 %[rw], v[84:87] offset:1024\n\t"
    "v_mfma_f32_16x16x32_bf16 v[72:75], v[128:131], v[100:103], 0\n\t"
    "s_add_u32 m0, %[l1], 32768\n\t"
    "ds_write_b128 %[rw], v[88:91] offset:2048\n\t"
    "v_mfma_f32_16x16x32_bf16 v[76:79], v[132:135], v[100:103], 0\n\t"
    "global_load_lds_dwordx4 %[av1], s[36:37] sc1\n\t"
    "ds_write_b128 %[rw], v[92:95] offset:3072\n\t"
    "s_waitcnt lgkmcnt(8)\n\t"
    "v_mfma_f32_16x16x32_bf16 v[64:67], v[136:139], v[104:107], v[64:67]\n\t"
    "ds_read_b128 v[96:99], %[ea] offset:8192\n\t"
    "s_add_u32 m0, %[l1], 4096\n\t"
    "v_mfma_f32_16x16x32_bf16 v[68:71], v[140:143], v[104:107], v[68:71]\n\t"
    "ds_read_b128 v[100:103], %[ea] offset:10240\n\t"
    "global_load_lds_dwordx4 %[av2], s[36:37] sc1\n\t"
    "v_mfma_f32_16x16x32_bf16 v[72:75], v[136:139], v[108:111], v[72:75]\n\t"
    "s_add_u32 m0, %[l1], 36864\n\t"
    "v_mfma_f32_16x16x32_bf16 v[76:79], v[140:143], v[108:111], v[76:79]\n\t"
    "global_load_lds_dwordx4 %[av3], s[36:37] sc1\n\t"
    "s_waitcnt lgkmcnt(8)\n\t"
    "v_mfma_f32_16x16x32_bf16 v[64:67], v[144:147], v[112:115], v[64:67]\n\t"
    "ds_read_b128 v[104:107], %[da] offset:8192\n\t"
    "s_add_u32 m0, %[l1], 8192\n\t"
    "v_mfma_f32_16x16x32_bf16 v[68:71], v[148:151], v[112:115], v[68:71]\n\t"
    "ds_read_b128 v[108:111], %[da] offset:10240\n\t"
    "global_load_lds_dwordx4 %[av4], s[36:37] sc1\n\t"
    "v_mfma_f32_16x16x32_bf16 v[72:75], v[144:147], v[116:119], v[72:75]\n\t"
    "s_add_u32 m0, %[l1], 40960\n\t"
    "v_mfma_f32_16x16x32_bf16 v[76:79], v[148:151], v[116:119], v[76:79]\n\t"
    "global_load_lds_dwordx4 %[av5], s[36:37] sc1\n\t"
    "s_waitcnt lgkmcnt(7)\n\t"
    "v_mfma_f32_16x16x32_bf16 v[64:67], v[152:155], v[120:123], v[64:67]\n\t"
    "ds_read_b128 v[112:115], %[ea] offset:12288\n\t"
    "s_add_u32 m0, %[l1], 12288\n\t"
    "v_mfma_f32_16x16x32_bf16 v[68:71], v[156:159], v[120:123], v[68:71]\n\t"
    "ds_read_b128 v[116:119], %[ea] offset:14336\n\t"
    "global_load_lds_dwordx4 %[av6], s[36:37] sc1\n\t"
    "v_mfma_f32_16x16x32_bf16 v[72:75], v[152:155], v[124:127], v[72:75]\n\t"
    "s_add_u32 m0, %[l1], 45056\n\t"
    "v_mfma_f32_16x16x32_bf16 v[76:79], v[156:159], v[124:127], v[76:79]\n\t"
    "global_load_lds_dwordx4 %[av7], s[36:37] sc1\n\t"
    "s_waitcnt lgkmcnt(4)\n\t"
    "v_mfma_f32_16x16x32_bf16 v[64:67], v[160:163], v[96:99], v[64:67]\n\t"
    "ds_read_b128 v[120:123], %[da] offset:12288\n\t"
    "v_mfma_f32_16x16x32_bf16 v[68:71], v[164:167], v[96:99], v[68:71]\n\t"
    "ds_read_b128 v[124:127], %[da] offset:14336\n\t"
    "v_mfma_f32_16x16x32_bf16 v[72:75], v[160:163], v[100:103], v[72:75]\n\t"
    "v_mfma_f32_16x16x32_bf16 v[76:79], v[164:167], v[100:103], v[76:79]\n\t"
    "s_waitcnt lgkmcnt(4)\n\t"
    "v_mfma_f32_16x16x32_bf16 v[64:67], v[168:171], v[104:107], v[64:67]\n\t"
    "ds_read_b128 v[96:99], %[ea] offset:16384\n\t"
    "v_mfma_f32_16x16x32_bf16 v[68:71], v[172:175], v[104:107], v[68:71]\n\t"
    "ds_read_b128 v[100:103], %[ea] offset:18432\n\t"
    "v_mfma_f32_16x16x32_bf16 v[72:75], v[168:171], v[108:111], v[72:75]\n\t"
    "v_mfma_f32_16x16x32_bf16 v[76:79], v[172:175], v[108:111], v[76:79]\n\t"
    "s_waitcnt lgkmcnt(4)\n\t"
    "v_mfma_f32_16x16x32_bf16 v[64:67], v[176:179], v[112:115], v[64:67]\n\t"
    "ds_read_b128 v[104:107], %[da] offset:16384\n\t"
    "v_mfma_f32_16x16x32_bf16 v[68:71], v[180:183], v[112:115], v[68:71]\n\t"
    "ds_read_b128 v[108:111], %[da] offset:18432\n\t"
    "v_mfma_f32_16x16x32_bf16 v[72:75], v[176:179], v[116:119], v[72:75]\n\t"
    "v_mfma_f32_16x16x32_bf16 v[76:79], v[180:183], v[116:119], v[76:79]\n\t"
    "s_waitcnt lgkmcnt(4)\n\t"
    "v_mfma_f32_16x16x32_bf16 v[64:67], v[184:187], v[120:123], v[64:67]\n\t"
    "ds_read_b128 v[112:115], %[ea] offset:20480\n\t"
    "v_mfma_f32_16x16x32_bf16 v[68:71], v[188:191], v[120:123], v[68:71]\n\t"
    "ds_read_b128 v[116:119], %[ea] offset:22528\n\t"
    "v_mfma_f32_16x16x32_bf16 v[72:75], v[184:187], v[124:127], v[72:75]\n\t"
    "v_mfma_f32_16x16x32_bf16 v[76:79], v[188:191], v[124:127], v[76:79]\n\t"
    "s_waitcnt vmcnt(12)\n\t"
    "s_barrier\n\t"
    "s_waitcnt lgkmcnt(4)\n\t"
    "v_mfma_f32_16x16x32_bf16 v[64:67], v[192:195], v[96:99], v[64:67]\n\t"
    "ds_read_b128 v[120:123], %[da] offset:20480\n\t"
    "s_add_u32 m0, %[l0], 0\n\t"
    "v_mfma_f32_16x16x32_bf16 v[68:71], v[196:199], v[96:99], v[68:71]\n\t"
    "ds_read_b128 v[124:127], %[da] offset:22528\n\t"
    "global_load_lds_dwordx4 %[av0], s[38:39] sc1\n\t"
    "v_mfma_f32_16x16x32_bf16 v[72:75], v[192:195], v[100:103], v[72:75]\n\t"
    "s_add_u32 m0, %[l0], 32768\n\t"
    "v_mfma_f32_16x16x32_bf16 v[76:79], v[196:199], v[100:103], v[76:79]\n\t"
    "global_load_lds_dwordx4 %[av1], s[38:39] sc1\n\t"
    "s_waitcnt lgkmcnt(4)\n\t"
    "v_mfma_f32_16x16x32_bf16 v[64:67], v[200:203], v[104:107], v[64:67]\n\t"
    "ds_read_b128 v[96:99], %[ea] offset:24576\n\t"
    "s_add_u32 m0, %[l0], 4096\n\t"
    "v_mfma_f32_16x16x32_bf16 v[68:71], v[204:207], v[104:107], v[68:71]\n\t"
    "ds_read_b128 v[100:103], %[ea] offset:26624\n\t"
    "global_load_lds_dwordx4 %[av2], s[38:39] sc1\n\t"
    "v_mfma_f32_16x16x32_bf16 v[72:75], v[200:203], v[108:111], v[72:75]\n\t"
    "s_add_u32 m0, %[l0], 36864\n\t"
    "v_mfma_f32_16x16x32_bf16 v[76:79], v[204:207], v[108:111], v[76:79]\n\t"
    "global_load_lds_dwordx4 %[av3], s[38:39] sc1\n\t"
    "s_add_u32 s36, s36, %[ab]\n\t"
    "s_addc_u32 s37, s37, 0\n\t"
    "s_waitcnt lgkmcnt(4)\n\t"
    "v_mfma_f32_16x16x32_bf16 v[64:67], v[208:211], v[112:115], v[64:67]\n\t"
    "ds_read_b128 v[104:107], %[da] offset:24576\n\t"
    "s_add_u32 m0, %[l0], 8192\n\t"
    "v_mfma_f32_16x16x32_bf16 v[68:71], v[212:215], v[112:115], v[68:71]\n\t"
    "ds_read_b128 v[108:111], %[da] offset:26624\n\t"
    "global_load_lds_dwordx4 %[av4], s[38:39] sc1\n\t"
    "v_mfma_f32_16x16x32_bf16 v[72:75], v[208:211], v[116:119], v[72:75]\n\t"
    "ds_read_b32 v39, %[sa]\n\t"
    "s_add_u32 m0, %[l0], 40960\n\t"
    "v_mfma_f32_16x16x32_bf16 v[76:79], v[212:215], v[116:119], v[76:79]\n\t"
    "v_xor_b32 %[sa], 0x100, %[sa]\n\t"
    "global_load_lds_dwordx4 %[av5], s[38:39] sc1\n\t"
    "s_xor_b32 %[l1], %[l1], 0x10000\n\t"
    "s_waitcnt lgkmcnt(5)\n\t"
    "v_mfma_f32_16x16x32_bf16 v[64:67], v[216:219], v[120:123], v[64:67]\n\t"
    "ds_read_b128 v[112:115], %[ea] offset:28672\n\t"
    "s_add_u32 m0, %[l0], 12288\n\t"
    "v_mfma_f32_16x16x32_bf16 v[68:71], v[220:223], v[120:123], v[68:71]\n\t"
    "ds_read_b128 v[116:119], %[ea] offset:30720\n\t"
    "global_load_lds_dwordx4 %[av6], s[38:39] sc1\n\t"
    "v_mfma_f32_16x16x32_bf16 v[72:75], v[216:219], v[124:127], v[72:75]\n\t"
    "v_xor_b32 %[ea], 0x10000, %[ea]\n\t"
    "s_add_u32 m0, %[l0], 45056\n\t"
    "v_mfma_f32_16x16x32_bf16 v[76:79], v[220:223], v[124:127], v[76:79]\n\t"
    "global_load_lds_dwordx4 %[av7], s[38:39] sc1\n\t"
    "s_waitcnt lgkmcnt(5)\n\t"
    "v_mfma_f32_16x16x32_bf16 v[64:67], v[224:227], v[96:99], v[64:67]\n\t"
    "ds_read_b128 v[120:123], %[da] offset:28672\n\t"
    "v_mfma_f32_16x16x32_bf16 v[68:71], v[228:231], v[96:99], v[68:71]\n\t"
    "ds_read_b128 v[124:127], %[da] offset:30720\n\t"
    "v_mfma_f32_16x16x32_bf16 v[72:75], v[224:227], v[100:103], v[72:75]\n\t"
    "v_xor_b32 %[da], 0x10000, %[da]\n\t"
    "v_mfma_f32_16x16x32_bf16 v[76:79], v[228:231], v[100:103], v[76:79]\n\t"
    "s_waitcnt lgkmcnt(5)\n\t"
    "v_mfma_f32_16x16x32_bf16 v[64:67], v[232:235], v[104:107], v[64:67]\n\t"
    "ds_read_b128 v[96:99], %[ea] offset:0\n\t"
    "v_mfma_f32_16x16x32_bf16 v[68:71], v[236:239], v[104:107], v[68:71]\n\t"
    "ds_read_b128 v[100:103], %[ea] offset:2048\n\t"
    "v_mfma_f32_16x16x32_bf16 v[72:75], v[232:235], v[108:111], v[72:75]\n\t"
    "v_mfma_f32_16x16x32_bf16 v[76:79], v[236:239], v[108:111], v[76:79]\n\t"
    "s_waitcnt lgkmcnt(4)\n\t"
    "v_mfma_f32_16x16x32_bf16 v[64:67], v[240:243], v[112:115], v[64:67]\n\t"
    "ds_read_b128 v[104:107], %[da] offset:0\n\t"
    "v_mad_u32_u24 v39, v39, %[mu], %[ad]\n\t"
    "v_mfma_f32_16x16x32_bf16 v[68:71], v[244:247], v[112:115], v[68:71]\n\t"
    "ds_read_b128 v[108:111], %[da] offset:2048\n\t"
    "v_cmp_ge_i32 vcc, %[lf], v39\n\t"
    "s_cmp_eq_u64 vcc, 0\n\t"
    "s_addc_u32 %[lf], %[lf], 0\n\t"
    "v_mfma_f32_16x16x32_bf16 v[72:75], v[240:243], v[116:119], v[72:75]\n\t"
    "v_mfma_f32_16x16x32_bf16 v[76:79], v[244:247], v[116:119], v[76:79]\n\t"
    "v_cmp_ge_i32 vcc, %[lf], v39\n\t"
    "s_cmp_eq_u64 vcc, 0\n\t"
    "s_addc_u32 %[lf], %[lf], 0\n\t"
    "s_waitcnt lgkmcnt(4)\n\t"
    "v_mfma_f32_16x16x32_bf16 v[80:83], v[248:251], v[120:123], v[64:67]\n\t"
    "ds_read_b128 v[112:115], %[ea] offset:4096\n\t"
    "s_min_i32 %[lf], %[lf], %[cp]\n\t"
    "s_add_u32 %[b], %[b], 1\n\t"
    "v_mfma_f32_16x16x32_bf16 v[84:87], v[252:255], v[120:123], v[68:71]\n\t"
    "ds_read_b128 v[116:119], %[ea] offset:6144\n\t"
    "v_mfma_f32_16x16x32_bf16 v[88:91], v[248:251], v[124:127], v[72:75]\n\t"
    "v_mfma_f32_16x16x32_bf16 v[92:95], v[252:255], v[124:127], v[76:79]\n\t"
    "s_add_u32 s38, s38, %[ab]\n\t"
    "s_addc_u32 s39, s39, 0\n\t"
    "s_xor_b32 %[l0], %[l0], 0x10000\n\t"
    "s_cmp_lt_i32 %[b], %[lf]\n\t"
    "s_cbranch_scc1 1b\n\t"
    "s_waitcnt lgkmcnt(0)\n\t"
    "s_nop 4"
    : "=&{v[80:95]}"(pd), [ea] "+&v"(ea), [da] "+&v"(da), [b] "+&s"(b), [lf] "+&s"(lf), "+{s[36:37]}"(p1), "+{s[38:39]}"(p0), [sa] "+&v"(sa), [l1] "+&s"(l1), [l0] "+&s"(l0)
    : [rw] "v"(rw), [ab] "s"(ab), [cp] "s"(cp), [mu] "s"(mu), [ad] "v"(ad), "{v[128:159]}"(W[0]), "{v[160:191]}"(W[1]), "{v[192:223]}"(W[2]), "{v[224:255]}"(W[3]), [av0] "v"(av[0]), [av1] "v"(av[1]), [av2] "v"(av[2]), [av3] "v"(av[3]), [av4] "v"(av[4]), [av5] "v"(av[5]), [av6] "v"(av[6]), [av7] "v"(av[7])
    : "v39", "v64", "v65", "v66", "v67", "v68", "v69", "v70", "v71", "v72", "v73", "v74", "v75", "v76", "v77", "v78", "v79", "v96", "v97", "v98", "v99", "v100", "v101", "v102", "v103", "v104", "v105", "v106", "v107", "v108", "v109", "v110", "v111", "v112", "v113", "v114", "v115", "v116", "v117", "v118", "v119", "v120", "v121", "v122", "v123", "v124", "v125", "v126", "v127", "vcc", "scc", "m0", "memory");
}
__device__ __forceinline__ void fast_ld2(const u32x32 (&W)[4], u32x16& pd, unsigned ea, unsigned da, int& b, int& lf, int cp, unsigned sa, unsigned mu, unsigned ad,
    unsigned rw, unsigned ab, const char* p1, const char* p0, unsigned l1, unsigned l0, const unsigned (&av)[8]) {
  asm volatile("s_setprio 0\n\t"
    "s_waitcnt vmcnt(6)\n\t"
    "s_barrier\n\t"
    "ds_read_b128 v[96:99], %[ea] offset:0\n\t"
    "ds_read_b128 v[100:103], %[ea] offset:2048\n\t"
    "ds_read_b128 v[104:107], %[da] offset:0\n\t"
    "ds_read_b128 v[108:111], %[da] offset:2048\n\t"
    "ds_read_b128 v[112:115], %[ea] offset:4096\n\t"
    "ds_read_b128 v[116:119], %[ea] offset:6144\n\t"
    "s_branch 2f\n\t"
    "1:\n\t"
    "s_waitcnt vmcnt(6)\n\t"
    "s_nop 1\n\t"
    "s_barrier\n\t"
    "2:\n\t"
    "s_waitcnt lgkmcnt(4)\n\t"
    "v_mfma_f32_16x16x32_bf16 v[64:67], v[128:131], v[96:99], 0\n\t"
    "ds_read_b128 v[120:123], %[da] offset:4096\n\t"
    "s_add_u32 m0, %[l1], 0\n\t"
    "ds_write_b128 %[rw], v[80:83] offset:0\n\t"
    "v_mfma_f32_16x16x32_bf16 v[68:71], v[132:135], v[96:99], 0\n\t"
    "ds_read_b128 v[124:127], %[da] offset:6144\n\t"
    "global_load_lds_dwordx4 %[av0], s[36:37] sc1\n\t"
    "ds_write_b128 %[rw], v[84:87] offset:1024\n\t"
    "v_mfma_f32_16x16x32_bf16 v[72:75], v[128:131], v[100:103], 0\n\t"
    "ds_write_b128 %[rw], v[88:91] offset:2048\n\t"
    "v_mfma_f32_16x16x32_bf16 v[76:79], v[132:135], v[100:103], 0\n\t"
    "ds_write_b128 %[rw], v[92:95] offset:3072\n\t"
    "s_waitcnt lgkmcnt(8)\n\t"
    "v_mfma_f32_16x16x32_bf16 v[64:67], v[136:139], v[104:107], v[64:67]\n\t"
    "ds_read_b128 v[96:99], %[ea] offset:8192\n\t"
    "s_add_u32 m0, %[l1], 4096\n\t"
    "v_mfma_f32_16x16x32_bf16 v[68:71], v[140:143], v[104:107], v[68:71]\n\t"
    "ds_read_b128 v[100:103], %[ea] offset:10240\n\t"
    "global_load_lds_dwordx4 %[av2], s[36:37] sc1\n\t"
    "v_mfma_f32_16x16x32_bf16 v[72:75], v[136:139], v[108:111], v[72:75]\n\t"
    "v_mfma_f32_16x16x32_bf16 v[76:79], v[140:143], v[108:111], v[76:79]\n\t"
    "s_waitcnt lgkmcnt(8)\n\t"
    "v_mfma_f32_16x16x32_bf16 v[64:67], v[144:147], v[112:115], v[64:67]\n\t"
    "ds_read_b128 v[104:107], %[da] offset:8192\n\t"
    "s_add_u32 m0, %[l1], 8192\n\t"
    "v_mfma_f32_16x16x32_bf16 v[68:71], v[148:151], v[112:115], v[68:71]\n\t"
    "ds_read_b128 v[108:111], %[da] offset:10240\n\t"
    "global_load_lds_dwordx4 %[av4], s[36:37] sc1\n\t"
    "v_mfma_f32_16x16x32_bf16 v[72:75], v[144:147], v[116:119], v[72:75]\n\t"
    "v_mfma_f32_16x16x32_bf16 v[76:79], v[148:151], v[116:119], v[76:79]\n\t"
    "s_waitcnt lgkmcnt(7)\n\t"
    "v_mfma_f32_16x16x32_bf16 v[64:67], v[152:155], v[120:123], v[64:67]\n\t"
    "ds_read_b128 v[112:115], %[ea] offset:12288\n\t"
    "s_add_u32 m0, %[l1], 12288\n\t"
    "v_mfma_f32_16x16x32_bf16 v[68:71], v[156:159], v[120:123], v[68:71]\n\t"
    "ds_read_b128 v[116:119], %[ea] offset:14336\n\t"
    "global_load_lds_dwordx4 %[av6], s[36:37] sc1\n\t"
    "v_mfma_f32_16x16x32_bf16 v[72:75], v[152:155], v[124:127], v[72:75]\n\t"
    "v_mfma_f32_16x16x32_bf16 v[76:79], v[156:159], v[124:127], v[76:79]\n\t"
    "s_waitcnt lgkmcnt(4)\n\t"
    "v_mfma_f32_16x16x32_bf16 v[64:67], v[160:163], v[96:99], v[64:67]\n\t"
    "ds_read_b128 v[120:123], %[da] offset:12288\n\t"
    "v_mfma_f32_16x16x32_bf16 v[68:71], v[164:167], v[96:99], v[68:71]\n\t"
    "ds_read_b128 v[124:127], %[da] offset:14336\n\t"
    "v_mfma_f32_16x16x32_bf16 v[72:75], v[160:163], v[100:103], v[72:75]\n\t"
    "v_mfma_f32_16x16x32_bf16 v[76:79], v[164:167], v[100:103], v[76:79]\n\t"
    "s_waitcnt lgkmcnt(4)\n\t"
    "v_mfma_f32_16x16x32_bf16 v[64:67], v[168:171], v[104:107], v[64:67]\n\t"
    "ds_read_b128 v[96:99], %[ea] offset:16384\n\t"
    "v_mfma_f32_16x16x32_bf16 v[68:71], v[172:175], v[104:107], v[68:71]\n\t"
    "ds_read_b128 v[100:103], %[ea] offset:18432\n\t"
    "v_mfma_f32_16x16x32_bf16 v[72:75], v[168:171], v[108:111], v[72:75]\n\t"
    "v_mfma_f32_16x16x32_bf16 v[76:79], v[172:175], v[108:111], v[76:79]\n\t"
    "s_waitcnt lgkmcnt(4)\n\t"
    "v_mfma_f32_16x16x32_bf16 v[64:67], v[176:179], v[112:115], v[64:67]\n\t"
    "ds_read_b128 v[104:107], %[da] offset:16384\n\t"
    "v_mfma_f32_16x16x32_bf16 v[68:71], v[180:183], v[112:115], v[68:71]\n\t"
    "ds_read_b128 v[108:111], %[da] offset:18432\n\t"
    "v_mfma_f32_16x16x32_bf16 v[72:75], v[176:179], v[116:119], v[72:75]\n\t"
    "v_mfma_f32_16x16x32_bf16 v[76:79], v[180:183], v[116:119], v[76:79]\n\t"
    "s_waitcnt lgkmcnt(4)\n\t"
    "v_mfma_f32_16x16x32_bf16 v[64:67], v[184:187], v[120:123], v[64:67]\n\t"
    "ds_read_b128 v[112:115], %[ea] offset:20480\n\t"
    "v_mfma_f32_16x16x32_bf16 v[68:71], v[188:191], v[120:123], v[68:71]\n\t"
    "ds_read_b128 v[116:119], %[ea] offset:22528\n\t"
    "v_mfma_f32_16x16x32_bf16 v[72:75], v[184:187], v[124:127], v[72:75]\n\t"
    "v_mfma_f32_16x16x32_bf16 v[76:79], v[188:191], v[124:127], v[76:79]\n\t"
    "s_waitcnt vmcnt(6)\n\t"
    "s_barrier\n\t"
    "s_waitcnt lgkmcnt(4)\n\t"
    "v_mfma_f32_16x16x32_bf16 v[64:67], v[192:195], v[96:99], v[64:67]\n\t"
    "ds_read_b128 v[120:123], %[da] offset:20480\n\t"
    "s_add_u32 m0, %[l0], 0\n\t"
    "v_mfma_f32_16x16x32_bf16 v[68:71], v[196:199], v[96:99], v[68:71]\n\t"
    "ds_read_b128 v[124:127], %[da] offset:22528\n\t"
    "global_load_lds_dwordx4 %[av0], s[38:39] sc1\n\t"
    "v_mfma_f32_16x16x32_bf16 v[72:75], v[192:195], v[100:103], v[72:75]\n\t"
    "v_mfma_f32_16x16x32_bf16 v[76:79], v[196:199], v[100:103], v[76:79]\n\t"
    "s_waitcnt lgkmcnt(4)\n\t"
    "v_mfma_f32_16x16x32_bf16 v[64:67], v[200:203], v[104:107], v[64:67]\n\t"
    "ds_read_b128 v[96:99], %[ea] offset:24576\n\t"
    "s_add_u32 m0, %[l0], 4096\n\t"
    "v_mfma_f32_16x16x32_bf16 v[68:71], v[204:207], v[104:107], v[68:71]\n\t"
    "ds_read_b128 v[100:103], %[ea] offset:26624\n\t"
    "global_load_lds_dwordx4 %[av2], s[38:39] sc1\n\t"
    "v_mfma_f32_16x16x32_bf16 v[72:75], v[200:203], v[108:111], v[72:75]\n\t"
    "v_mfma_f32_16x16x32_bf16 v[76:79], v[204:207], v[108:111], v[76:79]\n\t"
    "s_add_u32 s36, s36, %[ab]\n\t"
    "s_addc_u32 s37, s37, 0\n\t"
    "s_waitcnt lgkmcnt(4)\n\t"
    "v_mfma_f32_16x16x32_bf16 v[64:67], v[208:211], v[112:115], v[64:67]\n\t"
    "ds_read_b128 v[104:107], %[da] offset:24576\n\t"
    "s_add_u32 m0, %[l0], 8192\n\t"
    "v_mfma_f32_16x16x32_bf16 v[68:71], v[212:215], v[112:115], v[68:71]\n\t"
    "ds_read_b128 v[108:111], %[da] offset:26624\n\t"
    "global_load_lds_dwordx4 %[av4], s[38:39] sc1\n\t"
    "v_mfma_f32_16x16x32_bf16 v[72:75], v[208:211], v[116:119], v[72:75]\n\t"
    "ds_read_b32 v39, %[sa]\n\t"
    "v_mfma_f32_16x16x32_bf16 v[76:79], v[212:215], v[116:119], v[76:79]\n\t"
    "v_xor_b32 %[sa], 0x100, %[sa]\n\t"
    "s_xor_b32 %[l1], %[l1], 0x10000\n\t"
    "s_waitcnt lgkmcnt(5)\n\t"
    "v_mfma_f32_16x16x32_bf16 v[64:67], v[216:219], v[120:123], v[64:67]\n\t"
    "ds_read_b128 v[112:115], %[ea] offset:28672\n\t"
    "s_add_u32 m0, %[l0], 12288\n\t"
    "v_mfma_f32_16x16x32_bf16 v[68:71], v[220:223], v[120:123], v[68:71]\n\t"
    "ds_read_b128 v[116:119], %[ea] offset:30720\n\t"
    "global_load_lds_dwordx4 %[av6], s[38:39] sc1\n\t"
    "v_mfma_f32_16x16x32_bf16 v[72:75], v[216:219], v[124:127], v[72:75]\n\t"
    "v_xor_b32 %[ea], 0x10000, %[ea]\n\t"
    "v_mfma_f32_16x16x32_bf16 v[76:79], v[220:223], v[124:127], v[76:79]\n\t"
    "s_waitcnt lgkmcnt(5)\n\t"
    "v_mfma_f32_16x16x32_bf16 v[64:67], v[224:227], v[96:99], v[64:67]\n\t"
    "ds_read_b128 v[120:123], %[da] offset:28672\n\t"
    "v_mfma_f32_16x16x32_bf16 v[68:71], v[228:231], v[96:99], v[68:71]\n\t"
    "ds_read_b128 v[124:127], %[da] offset:30720\n\t"
    "v_mfma_f32_16x16x32_bf16 v[72:75], v[224:227], v[100:103], v[72:75]\n\t"
    "v_xor_b32 %[da], 0x10000, %[da]\n\t"
    "v_mfma_f32_16x16x32_bf16 v[76:79], v[228:231], v[100:103], v[76:79]\n\t"
    "s_waitcnt lgkmcnt(5)\n\t"
    "v_mfma_f32_16x16x32_bf16 v[64:67], v[232:235], v[104:107], v[64:67]\n\t"
    "ds_read_b128 v[96:99], %[ea] offset:0\n\t"
    "v_mfma_f32_16x16x32_bf16 v[68:71], v[236:239], v[104:107], v[68:71]\n\t"
    "ds_read_b128 v[100:103], %[ea] offset:2048\n\t"
    "v_mfma_f32_16x16x32_bf16 v[72:75], v[232:235], v[108:111], v[72:75]\n\t"
    "v_mfma_f32_16x16x32_bf16 v[76:79], v[236:239], v[108:111], v[76:79]\n\t"
    "s_waitcnt lgkmcnt(4)\n\t"
    "v_mfma_f32_16x16x32_bf16 v[64:67], v[240:243], v[112:115], v[64:67]\n\t"
    "ds_read_b128 v[104:107], %[da] offset:0\n\t"
    "v_mad_u32_u24 v39, v39, %[mu], %[ad]\n\t"
    "v_mfma_f32_16x16x32_bf16 v[68:71], v[244:247], v[112:115], v[68:71]\n\t"
    "ds_read_b128 v[108:111], %[da] offset:2048\n\t"
    "v_cmp_ge_i32 vcc, %[lf], v39\n\t"
    "s_cmp_eq_u64 vcc, 0\n\t"
    "s_addc_u32 %[lf], %[lf], 0\n\t"
    "v_mfma_f32_16x16x32_bf16 v[72:75], v[240:243], v[116:119], v[72:75]\n\t"
    "v_mfma_f32_16x16x32_bf16 v[76:79], v[244:247], v[116:119], v[76:79]\n\t"
    "v_cmp_ge_i32 vcc, %[lf], v39\n\t"
    "s_cmp_eq_u64 vcc, 0\n\t"
    "s_addc_u32 %[lf], %[lf], 0\n\t"
    "s_waitcnt lgkmcnt(4)\n\t"
    "v_mfma_f32_16x16x32_bf16 v[80:83], v[248:251], v[120:123], v[64:67]\n\t"
    "ds_read_b128 v[112:115], %[ea] offset:4096\n\t"
    "s_min_i32 %[lf], %[lf], %[cp]\n\t"
    "s_add_u32 %[b], %[b], 1\n\t"
    "v_mfma_f32_16x16x32_bf16 v[84:87], v[252:255], v[120:123], v[68:71]\n\t"
    "ds_read_b128 v[116:119], %[ea] offset:6144\n\t"
    "v_mfma_f32_16x16x32_bf16 v[88:91], v[248:251], v[124:127], v[72:75]\n\t"
    "v_mfma_f32_16x16x32_bf16 v[92:95], v[252:255], v[124:127], v[76:79]\n\t"
    "s_add_u32 s38, s38, %[ab]\n\t"
    "s_addc_u32 s39, s39, 0\n\t"
    "s_xor_b32 %[l0], %[l0], 0x10000\n\t"
    "s_cmp_lt_i32 %[b], %[lf]\n\t"
    "s_cbranch_scc1 1b\n\t"
    "s_waitcnt lgkmcnt(0)\n\t"
    "s_nop 4"
    : "=&{v[80:95]}"(pd), [ea] "+&v"(ea), [da] "+&v"(da), [b] "+&s"(b), [lf] "+&s"(lf), "+{s[36:37]}"(p1), "+{s[38:39]}"(p0), [sa] "+&v"(sa), [l1] "+&s"(l1), [l0] "+&s"(l0)
    : [rw] "v"(rw), [ab] "s"(ab), [cp] "s"(cp), [mu] "s"(mu), [ad] "v"(ad), "{v[128:159]}"(W[0]), "{v[160:191]}"(W[1]), "{v[192:223]}"(W[2]), "{v[224:255]}"(W[3]), [av0] "v"(av[0]), [av1] "v"(av[1]), [av2] "v"(av[2]), [av3] "v"(av[3]), [av4] "v"(av[4]), [av5] "v"(av[5]), [av6] "v"(av[6]), [av7] "v"(av[7])
    : "v39", "v64", "v65", "v66", "v67", "v68", "v69", "v70", "v71", "v72", "v73", "v74", "v75", "v76", "v77", "v78", "v79", "v96", "v97", "v98", "v99", "v100", "v101", "v102", "v103", "v104", "v105", "v106", "v107", "v108", "v109", "v110", "v111", "v112", "v113", "v114", "v115", "v116", "v117", "v118", "v119", "v120", "v121", "v122", "v123", "v124", "v125", "v126", "v127", "vcc", "scc", "m0", "memory");
}
__device__ __forceinline__ void fast_st0(const u32x32 (&W)[4], u32x16& pd, unsigned ea, unsigned da, int& b, int& lf, int cp, unsigned sa, unsigned mu, unsigned ad,
    unsigned rw, unsigned ab, const char* p1, const char* p0, unsigned co0, unsigned co1, int po, int pf, unsigned w0, unsigned sl, unsigned dv, const char* db, const char* pg) {
  unsigned st;
  asm volatile("s_barrier\n\t"
    "ds_read_b128 v[96:99], %[ea] offset:0\n\t"
    "ds_read_b128 v[100:103], %[ea] offset:2048\n\t"
    "ds_read_b128 v[104:107], %[da] offset:0\n\t"
    "ds_read_b128 v[108:111], %[da] offset:2048\n\t"
    "ds_read_b128 v[112:115], %[ea] offset:4096\n\t"
    "ds_read_b128 v[116:119], %[ea] offset:6144\n\t"
    "s_branch 2f\n\t"
    "1:\n\t"
    "s_barrier\n\t"
    "2:\n\t"
    "s_waitcnt lgkmcnt(4)\n\t"
    "v_mfma_f32_16x16x32_bf16 v[64:67], v[128:131], v[96:99], 0\n\t"
    "ds_read_b128 v[120:123], %[da] offset:4096\n\t"
    "v_mfma_f32_16x16x32_bf16 v[68:71], v[132:135], v[96:99], 0\n\t"
    "ds_read_b128 v[124:127], %[da] offset:6144\n\t"
    "v_mfma_f32_16x16x32_bf16 v[72:75], v[128:131], v[100:103], 0\n\t"
    "v_mfma_f32_16x16x32_bf16 v[76:79], v[132:135], v[100:103], 0\n\t"
    "s_waitcnt lgkmcnt(4)\n\t"
    "v_mfma_f32_16x16x32_bf16 v[64:67], v[136:139], v[104:107], v[64:67]\n\t"
    "ds_read_b128 v[96:99], %[ea] offset:8192\n\t"
    "v_mfma_f32_16x16x32_bf16 v[68:71], v[140:143], v[104:107], v[68:71]\n\t"
    "ds_read_b128 v[100:103], %[ea] offset:10240\n\t"
    "v_mfma_f32_16x16x32_bf16 v[72:75], v[136:139], v[108:111], v[72:75]\n\t"
    "v_mfma_f32_16x16x32_bf16 v[76:79], v[140:143], v[108:111], v[76:79]\n\t"
    "s_waitcnt lgkmcnt(4)\n\t"
    "v_mfma_f32_16x16x32_bf16 v[64:67], v[144:147], v[112:115], v[64:67]\n\t"
    "ds_read_b128 v[104:107], %[da] offset:8192\n\t"
    "v_mfma_f32_16x16x32_bf16 v[68:71], v[148:151], v[112:115], v[68:71]\n\t"
    "ds_read_b128 v[108:111], %[da] offset:10240\n\t"
    "v_mfma_f32_16x16x32_bf16 v[72:75], v[144:147], v[116:119], v[72:75]\n\t"
    "v_mfma_f32_16x16x32_bf16 v[76:79], v[148:151], v[116:119], v[76:79]\n\t"
    "s_waitcnt lgkmcnt(4)\n\t"
    "v_mfma_f32_16x16x32_bf16 v[64:67], v[152:155], v[120:123], v[64:67]\n\t"
    "ds_read_b128 v[112:115], %[ea] offset:12288\n\t"
    "v_mfma_f32_16x16x32_bf16 v[68:71], v[156:159], v[120:123], v[68:71]\n\t"
    "ds_read_b128 v[116:119], %[ea] offset:14336\n\t"
    "v_mfma_f32_16x16x32_bf16 v[72:75], v[152:155], v[124:127], v[72:75]\n\t"
    "v_mfma_f32_16x16x32_bf16 v[76:79], v[156:159], v[124:127], v[76:79]\n\t"
    "s_waitcnt lgkmcnt(4)\n\t"
    "v_mfma_f32_16x16x32_bf16 v[64:67], v[160:163], v[96:99], v[64:67]\n\t"
    "ds_read_b128 v[120:123], %[da] offset:12288\n\t"
    "v_mfma_f32_16x16x32_bf16 v[68:71], v[164:167], v[96:99], v[68:71]\n\t"
    "ds_read_b128 v[124:127], %[da] offset:14336\n\t"
    "v_mfma_f32_16x16x32_bf16 v[72:75], v[160:163], v[100:103], v[72:75]\n\t"
    "v_mfma_f32_16x16x32_bf16 v[76:79], v[164:167], v[100:103], v[76:79]\n\t"
    "s_waitcnt lgkmcnt(4)\n\t"
    "v_mfma_f32_16x16x32_bf16 v[64:67], v[168:171], v[104:107], v[64:67]\n\t"
    "ds_read_b128 v[96:99], %[ea] offset:16384\n\t"
    "v_mfma_f32_16x16x32_bf16 v[68:71], v[172:175], v[104:107], v[68:71]\n\t"
    "ds_read_b128 v[100:103], %[ea] offset:18432\n\t"
    "v_mfma_f32_16x16x32_bf16 v[72:75], v[168:171], v[108:111], v[72:75]\n\t"
    "v_mfma_f32_16x16x32_bf16 v[76:79], v[172:175], v[108:111], v[76:79]\n\t"
    "s_waitcnt lgkmcnt(4)\n\t"
    "v_mfma_f32_16x16x32_bf16 v[64:67], v[176:179], v[112:115], v[64:67]\n\t"
    "ds_read_b128 v[104:107], %[da] offset:16384\n\t"
    "v_mfma_f32_16x16x32_bf16 v[68:71], v[180:183], v[112:115], v[68:71]\n\t"
    "ds_read_b128 v[108:111], %[da] offset:18432\n\t"
    "v_mfma_f32_16x16x32_bf16 v[72:75], v[176:179], v[116:119], v[72:75]\n\t"
    "v_mfma_f32_16x16x32_bf16 v[76:79], v[180:183], v[116:119], v[76:79]\n\t"
    "s_waitcnt lgkmcnt(4)\n\t"
    "v_mfma_f32_16x16x32_bf16 v[64:67], v[184:187], v[120:123], v[64:67]\n\t"
    "ds_read_b128 v[112:115], %[ea] offset:20480\n\t"
    "v_mfma_f32_16x16x32_bf16 v[68:71], v[188:191], v[120:123], v[68:71]\n\t"
    "ds_read_b128 v[116:119], %[ea] offset:22528\n\t"
    "v_mfma_f32_16x16x32_bf16 v[72:75], v[184:187], v[124:127], v[72:75]\n\t"
    "v_mfma_f32_16x16x32_bf16 v[76:79], v[188:191], v[124:127], v[76:79]\n\t"
    "s_waitcnt vmcnt(0)\n\t"
    "s_barrier\n\t"
    "s_waitcnt lgkmcnt(4)\n\t"
    "v_mfma_f32_16x16x32_bf16 v[64:67], v[192:195], v[96:99], v[64:67]\n\t"
    "ds_read_b128 v[120:123], %[da] offset:20480\n\t"
    "v_mfma_f32_16x16x32_bf16 v[68:71], v[196:199], v[96:99], v[68:71]\n\t"
    "ds_read_b128 v[124:127], %[da] offset:22528\n\t"
    "v_mfma_f32_16x16x32_bf16 v[72:75], v[192:195], v[100:103], v[72:75]\n\t"
    "ds_read_b128 v[48:51], %[rw] offset:0\n\t"
    "v_mfma_f32_16x16x32_bf16 v[76:79], v[196:199], v[100:103], v[76:79]\n\t"
    "ds_read_b128 v[52:55], %[rw] offset:1024\n\t"
    "s_cmp_eq_u32 %[w0], 0\n\t"
    "s_cbranch_scc1 5f\n\t"
    "6:\n\t"
    "s_waitcnt lgkmcnt(6)\n\t"
    "v_mfma_f32_16x16x32_bf16 v[64:67], v[200:203], v[104:107], v[64:67]\n\t"
    "ds_read_b128 v[96:99], %[ea] offset:24576\n\t"
    "v_mfma_f32_16x16x32_bf16 v[68:71], v[204:207], v[104:107], v[68:71]\n\t"
    "ds_read_b128 v[100:103], %[ea] offset:26624\n\t"
    "v_mfma_f32_16x16x32_bf16 v[72:75], v[200:203], v[108:111], v[72:75]\n\t"
    "ds_read_b128 v[56:59], %[rw] offset:2048\n\t"
    "v_mfma_f32_16x16x32_bf16 v[76:79], v[204:207], v[108:111], v[76:79]\n\t"
    "ds_read_b128 v[60:63], %[rw] offset:3072\n\t"
    "s_waitcnt lgkmcnt(0)\n\t"
    "v_mfma_f32_16x16x32_bf16 v[64:67], v[208:211], v[112:115], v[64:67]\n\t"
    "ds_read_b128 v[104:107], %[da] offset:24576\n\t"
    "v_add_f32 v48, v48, v80\n\t"
    "v_add_f32 v49, v49, v81\n\t"
    "v_mfma_f32_16x16x32_bf16 v[68:71], v[212:215], v[112:115], v[68:71]\n\t"
    "ds_read_b128 v[108:111], %[da] offset:26624\n\t"
    "v_add_f32 v50, v50, v82\n\t"
    "v_mfma_f32_16x16x32_bf16 v[72:75], v[208:211], v[116:119], v[72:75]\n\t"
    "ds_read_b32 v39, %[sa]\n\t"
    "v_add_f32 v51, v51, v83\n\t"
    "v_mfma_f32_16x16x32_bf16 v[76:79], v[212:215], v[116:119], v[76:79]\n\t"
    "v_xor_b32 %[sa], 0x100, %[sa]\n\t"
    "v_add_f32 v52, v52, v84\n\t"
    "v_add_f32 v53, v53, v85\n\t"
    "v_mfma_f32_16x16x32_bf16 v[64:67], v[216:219], v[120:123], v[64:67]\n\t"
    "ds_read_b128 v[112:115], %[ea] offset:28672\n\t"
    "v_add_f32 v54, v54, v86\n\t"
    "v_mfma_f32_16x16x32_bf16 v[68:71], v[220:223], v[120:123], v[68:71]\n\t"
    "ds_read_b128 v[116:119], %[ea] offset:30720\n\t"
    "v_add_f32 v55, v55, v87\n\t"
    "v_mfma_f32_16x16x32_bf16 v[72:75], v[216:219], v[124:127], v[72:75]\n\t"
    "v_xor_b32 %[ea], 0x10000, %[ea]\n\t"
    "v_add_f32 v56, v56, v88\n\t"
    "v_add_f32 v57, v57, v89\n\t"
    "v_mfma_f32_16x16x32_bf16 v[76:79], v[220:223], v[124:127], v[76:79]\n\t"
    "v_add_f32 v58, v58, v90\n\t"
    "v_mfma_f32_16x16x32_bf16 v[64:67], v[224:227], v[96:99], v[64:67]\n\t"
    "ds_read_b128 v[120:123], %[da] offset:28672\n\t"
    "v_add_f32 v59, v59, v91\n\t"
    "v_mfma_f32_16x16x32_bf16 v[68:71], v[228:231], v[96:99], v[68:71]\n\t"
    "ds_read_b128 v[124:127], %[da] offset:30720\n\t"
    "v_add_f32 v60, v60, v92\n\t"
    "v_mfma_f32_16x16x32_bf16 v[72:75], v[224:227], v[100:103], v[72:75]\n\t"
    "v_xor_b32 %[da], 0x10000, %[da]\n\t"
    "v_add_f32 v61, v61, v93\n\t"
    "v_add_f32 v62, v62, v94\n\t"
    "v_mfma_f32_16x16x32_bf16 v[76:79], v[228:231], v[100:103], v[76:79]\n\t"
    "v_add_f32 v63, v63, v95\n\t"
    "s_waitcnt lgkmcnt(5)\n\t"
    "v_mfma_f32_16x16x32_bf16 v[64:67], v[232:235], v[104:107], v[64:67]\n\t"
    "ds_read_b128 v[96:99], %[ea] offset:0\n\t"
    "v_cvt_pk_bf16_f32 v40, v48, v49\n\t"
    "v_mfma_f32_16x16x32_bf16 v[68:71], v[236:239], v[104:107], v[68:71]\n\t"
    "ds_read_b128 v[100:103], %[ea] offset:2048\n\t"
    "v_cvt_pk_bf16_f32 v41, v50, v51\n\t"
    "v_cvt_pk_bf16_f32 v42, v52, v53\n\t"
    "v_mfma_f32_16x16x32_bf16 v[72:75], v[232:235], v[108:111], v[72:75]\n\t"
    "v_cvt_pk_bf16_f32 v43, v54, v55\n\t"
    "v_mfma_f32_16x16x32_bf16 v[76:79], v[236:239], v[108:111], v[76:79]\n\t"
    "global_store_dwordx4 %[co0], v[40:43], s[36:37] sc1\n\t"
    "s_waitcnt lgkmcnt(4)\n\t"
    "v_mfma_f32_16x16x32_bf16 v[64:67], v[240:243], v[112:115], v[64:67]\n\t"
    "ds_read_b128 v[104:107], %[da] offset:0\n\t"
    "v_mad_u32_u24 v39, v39, %[mu], %[ad]\n\t"
    "v_cvt_pk_bf16_f32 v44, v56, v57\n\t"
    "v_cvt_pk_bf16_f32 v45, v58, v59\n\t"
    "v_mfma_f32_16x16x32_bf16 v[68:71], v[244:247], v[112:115], v[68:71]\n\t"
    "ds_read_b128 v[108:111], %[da] offset:2048\n\t"
    "v_cmp_ge_i32 vcc, %[lf], v39\n\t"
    "s_cmp_eq_u64 vcc, 0\n\t"
    "s_addc_u32 %[lf], %[lf], 0\n\t"
    "v_cvt_pk_bf16_f32 v46, v60, v61\n\t"
    "v_mfma_f32_16x16x32_bf16 v[72:75], v[240:243], v[116:119], v[72:75]\n\t"
    "v_cvt_pk_bf16_f32 v47, v62, v63\n\t"
    "v_mfma_f32_16x16x32_bf16 v[76:79], v[244:247], v[116:119], v[76:79]\n\t"
    "v_cmp_ge_i32 vcc, %[lf], v39\n\t"
    "s_cmp_eq_u64 vcc, 0\n\t"
    "s_addc_u32 %[lf], %[lf], 0\n\t"
    "global_store_dwordx4 %[co1], v[44:47], s[36:37] sc1\n\t"
    "s_waitcnt lgkmcnt(4)\n\t"
    "v_mfma_f32_16x16x32_bf16 v[80:83], v[248:251], v[120:123], v[64:67]\n\t"
    "ds_read_b128 v[112:115], %[ea] offset:4096\n\t"
    "s_min_i32 %[lf], %[lf], %[cp]\n\t"
    "s_add_u32 %[b], %[b], 1\n\t"
    "v_mfma_f32_16x16x32_bf16 v[84:87], v[252:255], v[120:123], v[68:71]\n\t"
    "ds_read_b128 v[116:119], %[ea] offset:6144\n\t"
    "v_mfma_f32_16x16x32_bf16 v[88:91], v[248:251], v[124:127], v[72:75]\n\t"
    "v_mfma_f32_16x16x32_bf16 v[92:95], v[252:255], v[124:127], v[76:79]\n\t"
    "s_mov_b64 s[36:37], s[38:39]\n\t"
    "s_add_u32 s38, s38, %[ab]\n\t"
    "s_addc_u32 s39, s39, 0\n\t"
    "s_cmp_lt_i32 %[b], %[lf]\n\t"
    "s_cbranch_scc1 1b\n\t"
    "s_waitcnt lgkmcnt(0)\n\t"
    "s_nop 4\n\t"
    "s_branch 9f\n\t"
    "5:\n\t"
    "s_add_u32 %[st], %[b], %[po]\n\t"
    "s_max_i32 %[st], %[st], %[p0]\n\t"
    "v_mov_b32 v39, %[st]\n\t"
    "v_mov_b32 v38, 0\n\t"
    "s_mov_b64 exec, 1\n\t"
    "global_store_dword v38, v39, %[pg] sc1\n\t"
    "s_mov_b64 exec, -1\n\t"
    "s_mov_b32 %[p0], %[p1]\n\t"
    "s_xor_b32 %[sl], %[sl], 0x100\n\t"
    "s_cmp_ge_i32 %[lf], %[cp]\n\t"
    "s_cbranch_scc1 6b\n\t"
    "s_mov_b32 m0, %[sl]\n\t"
    "s_nop 0\n\t"
    "global_load_lds_dword %[dv], %[db] sc1\n\t"
    "s_branch 6b\n\t"
    "9:"
    : "=&{v[80:95]}"(pd), [ea] "+&v"(ea), [da] "+&v"(da), [b] "+&s"(b), [lf] "+&s"(lf), "+{s[36:37]}"(p1), "+{s[38:39]}"(p0), [sa] "+&v"(sa), [st] "=&s"(st), [sl] "+&s"(sl), [p0] "+&s"(pf)
    : [rw] "v"(rw), [ab] "s"(ab), [cp] "s"(cp), [mu] "s"(mu), [ad] "v"(ad), "{v[128:159]}"(W[0]), "{v[160:191]}"(W[1]), "{v[192:223]}"(W[2]), "{v[224:255]}"(W[3]), [co0] "v"(co0), [w0] "s"(w0), [dv] "v"(dv), [db] "s"(db), [pg] "s"(pg), [po] "s"(po - 1), [p1] "s"(b + po), [co1] "v"(co1)
    : "v38", "v39", "v40", "v41", "v42", "v43", "v44", "v45", "v46", "v47", "v48", "v49", "v50", "v51", "v52", "v53", "v54", "v55", "v56", "v57", "v58", "v59", "v60", "v61", "v62", "v63", "v64", "v65", "v66", "v67", "v68", "v69", "v70", "v71", "v72", "v73", "v74", "v75", "v76", "v77", "v78", "v79", "v96", "v97", "v98", "v99", "v100", "v101", "v102", "v103", "v104", "v105", "v106", "v107", "v108", "v109", "v110", "v111", "v112", "v113", "v114", "v115", "v116", "v117", "v118", "v119", "v120", "v121", "v122", "v123", "v124", "v125", "v126", "v127", "vcc", "scc", "m0", "memory");
}
__device__ __forceinline__ void fast_st1(const u32x32 (&W)[4], u32x16& pd, unsigned ea, unsigned da, int& b, int& lf, int cp, unsigned sa, unsigned mu, unsigned ad,
    unsigned rw, unsigned ab, const char* p1, const char* p0, unsigned co0, unsigned co1, int po, int pf, unsigned w0, unsigned sl, unsigned dv, const char* db, const char* pg) {
  unsigned st;
  asm volatile("s_barrier\n\t"
    "ds_read_b128 v[96:99], %[ea] offset:0\n\t"
    "ds_read_b128 v[100:103], %[ea] offset:2048\n\t"
    "ds_read_b128 v[104:107], %[da] offset:0\n\t"
    "ds_read_b128 v[108:111], %[da] offset:2048\n\t"
    "ds_read_b128 v[112:115], %[ea] offset:4096\n\t"
    "ds_read_b128 v[116:119], %[ea] offset:6144\n\t"
    "s_branch 2f\n\t"
    "1:\n\t"
    "s_barrier\n\t"
    "2:\n\t"
    "s_waitcnt lgkmcnt(4)\n\t"
    "v_mfma_f32_16x16x32_bf16 v[64:67], v[128:131], v[96:99], 0\n\t"
    "ds_read_b128 v[120:123], %[da] offset:4096\n\t"
    "v_mfma_f32_16x16x32_bf16 v[68:71], v[132:135], v[96:99], 0\n\t"
    "ds_read_b128 v[124:127], %[da] offset:6144\n\t"
    "v_mfma_f32_16x16x32_bf16 v[72:75], v[128:131], v[100:103], 0\n\t"
    "v_mfma_f32_16x16x32_bf16 v[76:79], v[132:135], v[100:103], 0\n\t"
    "s_waitcnt lgkmcnt(4)\n\t"
    "v_mfma_f32_16x16x32_bf16 v[64:67], v[136:139], v[104:107], v[64:67]\n\t"
    "ds_read_b128 v[96:99], %[ea] offset:8192\n\t"
    "v_mfma_f32_16x16x32_bf16 v[68:71], v[140:143], v[104:107], v[68:71]\n\t"
    "ds_read_b128 v[100:103], %[ea] offset:10240\n\t"
    "v_mfma_f32_16x16x32_bf16 v[72:75], v[136:139], v[108:111], v[72:75]\n\t"
    "v_mfma_f32_16x16x32_bf16 v[76:79], v[140:143], v[108:111], v[76:79]\n\t"
    "s_waitcnt lgkmcnt(4)\n\t"
    "v_mfma_f32_16x16x32_bf16 v[64:67], v[144:147], v[112:115], v[64:67]\n\t"
    "ds_read_b128 v[104:107], %[da] offset:8192\n\t"
    "v_mfma_f32_16x16x32_bf16 v[68:71], v[148:151], v[112:115], v[68:71]\n\t"
    "ds_read_b128 v[108:111], %[da] offset:10240\n\t"
    "v_mfma_f32_16x16x32_bf16 v[72:75], v[144:147], v[116:119], v[72:75]\n\t"
    "v_mfma_f32_16x16x32_bf16 v[76:79], v[148:151], v[116:119], v[76:79]\n\t"
    "s_waitcnt lgkmcnt(4)\n\t"
    "v_mfma_f32_16x16x32_bf16 v[64:67], v[152:155], v[120:123], v[64:67]\n\t"
    "ds_read_b128 v[112:115], %[ea] offset:12288\n\t"
    "v_mfma_f32_16x16x32_bf16 v[68:71], v[156:159], v[120:123], v[68:71]\n\t"
    "ds_read_b128 v[116:119], %[ea] offset:14336\n\t"
    "v_mfma_f32_16x16x32_bf16 v[72:75], v[152:155], v[124:127], v[72:75]\n\t"
    "v_mfma_f32_16x16x32_bf16 v[76:79], v[156:159], v[124:127], v[76:79]\n\t"
    "s_waitcnt lgkmcnt(4)\n\t"
    "v_mfma_f32_16x16x32_bf16 v[64:67], v[160:163], v[96:99], v[64:67]\n\t"
    "ds_read_b128 v[120:123], %[da] offset:12288\n\t"
    "v_mfma_f32_16x16x32_bf16 v[68:71], v[164:167], v[96:99], v[68:71]\n\t"
    "ds_read_b128 v[124:127], %[da] offset:14336\n\t"
    "v_mfma_f32_16x16x32_bf16 v[72:75], v[160:163], v[100:103], v[72:75]\n\t"
    "v_mfma_f32_16x16x32_bf16 v[76:79], v[164:167], v[100:103], v[76:79]\n\t"
    "s_waitcnt lgkmcnt(4)\n\t"
    "v_mfma_f32_16x16x32_bf16 v[64:67], v[168:171], v[104:107], v[64:67]\n\t"
    "ds_read_b128 v[96:99], %[ea] offset:16384\n\t"
    "v_mfma_f32_16x16x32_bf16 v[68:71], v[172:175], v[104:107], v[68:71]\n\t"
    "ds_read_b128 v[100:103], %[ea] offset:18432\n\t"
    "v_mfma_f32_16x16x32_bf16 v[72:75], v[168:171], v[108:111], v[72:75]\n\t"
    "v_mfma_f32_16x16x32_bf16 v[76:79], v[172:175], v[108:111], v[76:79]\n\t"
    "s_waitcnt lgkmcnt(4)\n\t"
    "v_mfma_f32_16x16x32_bf16 v[64:67], v[176:179], v[112:115], v[64:67]\n\t"
    "ds_read_b128 v[104:107], %[da] offset:16384\n\t"
    "v_mfma_f32_16x16x32_bf16 v[68:71], v[180:183], v[112:115], v[68:71]\n\t"
    "ds_read_b128 v[108:111], %[da] offset:18432\n\t"
    "v_mfma_f32_16x16x32_bf16 v[72:75], v[176:179], v[116:119], v[72:75]\n\t"
    "v_mfma_f32_16x16x32_bf16 v[76:79], v[180:183], v[116:119], v[76:79]\n\t"
    "s_waitcnt lgkmcnt(4)\n\t"
    "v_mfma_f32_16x16x32_bf16 v[64:67], v[184:187], v[120:123], v[64:67]\n\t"
    "ds_read_b128 v[112:115], %[ea] offset:20480\n\t"
    "v_mfma_f32_16x16x32_bf16 v[68:71], v[188:191], v[120:123], v[68:71]\n\t"
    "ds_read_b128 v[116:119], %[ea] offset:22528\n\t"
    "v_mfma_f32_16x16x32_bf16 v[72:75], v[184:187], v[124:127], v[72:75]\n\t"
    "v_mfma_f32_16x16x32_bf16 v[76:79], v[188:191], v[124:127], v[76:79]\n\t"
    "s_waitcnt vmcnt(0)\n\t"
    "s_barrier\n\t"
    "s_waitcnt lgkmcnt(4)\n\t"
    "v_mfma_f32_16x16x32_bf16 v[64:67], v[192:195], v[96:99], v[64:67]\n\t"
    "ds_read_b128 v[120:123], %[da] offset:20480\n\t"
    "v_mfma_f32_16x16x32_bf16 v[68:71], v[196:199], v[96:99], v[68:71]\n\t"
    "ds_read_b128 v[124:127], %[da] offset:22528\n\t"
    "v_mfma_f32_16x16x32_bf16 v[72:75], v[192:195], v[100:103], v[72:75]\n\t"
    "ds_read_b128 v[48:51], %[rw] offset:0\n\t"
    "v_mfma_f32_16x16x32_bf16 v[76:79], v[196:199], v[100:103], v[76:79]\n\t"
    "ds_read_b128 v[52:55], %[rw] offset:1024\n\t"
    "s_cmp_eq_u32 %[w0], 0\n\t"
    "s_cbranch_scc1 5f\n\t"
    "6:\n\t"
    "s_waitcnt lgkmcnt(6)\n\t"
    "v_mfma_f32_16x16x32_bf16 v[64:67], v[200:203], v[104:107], v[64:67]\n\t"
    "ds_read_b128 v[96:99], %[ea] offset:24576\n\t"
    "v_mfma_f32_16x16x32_bf16 v[68:71], v[204:207], v[104:107], v[68:71]\n\t"
    "ds_read_b128 v[100:103], %[ea] offset:26624\n\t"
    "v_mfma_f32_16x16x32_bf16 v[72:75], v[200:203], v[108:111], v[72:75]\n\t"
    "ds_read_b128 v[56:59], %[rw] offset:2048\n\t"
    "v_mfma_f32_16x16x32_bf16 v[76:79], v[204:207], v[108:111], v[76:79]\n\t"
    "ds_read_b128 v[60:63], %[rw] offset:3072\n\t"
    "s_waitcnt lgkmcnt(0)\n\t"
    "v_mfma_f32_16x16x32_bf16 v[64:67], v[208:211], v[112:115], v[64:67]\n\t"
    "ds_read_b128 v[104:107], %[da] offset:24576\n\t"
    "v_add_f32 v48, v48, v80\n\t"
    "v_add_f32 v49, v49, v81\n\t"
    "v_add_f32 v50, v50, v82\n\t"
    "v_mfma_f32_16x16x32_bf16 v[68:71], v[212:215], v[112:115], v[68:71]\n\t"
    "ds_read_b128 v[108:111], %[da] offset:26624\n\t"
    "v_add_f32 v51, v51, v83\n\t"
    "v_add_f32 v52, v52, v84\n\t"
    "v_add_f32 v53, v53, v85\n\t"
    "v_mfma_f32_16x16x32_bf16 v[72:75], v[208:211], v[116:119], v[72:75]\n\t"
    "ds_read_b32 v39, %[sa]\n\t"
    "v_add_f32 v54, v54, v86\n\t"
    "v_add_f32 v55, v55, v87\n\t"
    "v_add_f32 v56, v56, v88\n\t"
    "v_mfma_f32_16x16x32_bf16 v[76:79], v[212:215], v[116:119], v[76:79]\n\t"
    "v_xor_b32 %[sa], 0x100, %[sa]\n\t"
    "v_add_f32 v57, v57, v89\n\t"
    "v_add_f32 v58, v58, v90\n\t"
    "v_add_f32 v59, v59, v91\n\t"
    "v_mfma_f32_16x16x32_bf16 v[64:67], v[216:219], v[120:123], v[64:67]\n\t"
    "ds_read_b128 v[112:115], %[ea] offset:28672\n\t"
    "v_add_f32 v60, v60, v92\n\t"
    "v_add_f32 v61, v61, v93\n\t"
    "v_add_f32 v62, v62, v94\n\t"
    "v_mfma_f32_16x16x32_bf16 v[68:71], v[220:223], v[120:123], v[68:71]\n\t"
    "ds_read_b128 v[116:119], %[ea] offset:30720\n\t"
    "v_add_f32 v63, v63, v95\n\t"
    "v_mul_f32 v40, %[nl], v48\n\t"
    "v_mul_f32 v41, %[nl], v49\n\t"
    "v_mfma_f32_16x16x32_bf16 v[72:75], v[216:219], v[124:127], v[72:75]\n\t"
    "v_xor_b32 %[ea], 0x10000, %[ea]\n\t"
    "v_mul_f32 v42, %[nl], v50\n\t"
    "v_mul_f32 v43, %[nl], v51\n\t"
    "v_mul_f32 v44, %[nl], v56\n\t"
    "v_mfma_f32_16x16x32_bf16 v[76:79], v[220:223], v[124:127], v[76:79]\n\t"
    "v_mul_f32 v45, %[nl], v57\n\t"
    "v_mul_f32 v46, %[nl], v58\n\t"
    "v_mul_f32 v47, %[nl], v59\n\t"
    "v_mfma_f32_16x16x32_bf16 v[64:67], v[224:227], v[96:99], v[64:67]\n\t"
    "ds_read_b128 v[120:123], %[da] offset:28672\n\t"
    "v_exp_f32 v40, v40\n\t"
    "v_exp_f32 v41, v41\n\t"
    "v_exp_f32 v42, v42\n\t"
    "v_mfma_f32_16x16x32_bf16 v[68:71], v[228:231], v[96:99], v[68:71]\n\t"
    "ds_read_b128 v[124:127], %[da] offset:30720\n\t"
    "v_exp_f32 v43, v43\n\t"
    "v_exp_f32 v44, v44\n\t"
    "v_exp_f32 v45, v45\n\t"
    "v_mfma_f32_16x16x32_bf16 v[72:75], v[224:227], v[100:103], v[72:75]\n\t"
    "v_xor_b32 %[da], 0x10000, %[da]\n\t"
    "v_exp_f32 v46, v46\n\t"
    "v_exp_f32 v47, v47\n\t"
    "v_add_f32 v40, 1.0, v40\n\t"
    "v_mfma_f32_16x16x32_bf16 v[76:79], v[228:231], v[100:103], v[76:79]\n\t"
    "v_add_f32 v41, 1.0, v41\n\t"
    "v_add_f32 v42, 1.0, v42\n\t"
    "v_add_f32 v43, 1.0, v43\n\t"
    "s_waitcnt lgkmcnt(5)\n\t"
    "v_mfma_f32_16x16x32_bf16 v[64:67], v[232:235], v[104:107], v[64:67]\n\t"
    "ds_read_b128 v[96:99], %[ea] offset:0\n\t"
    "v_add_f32 v44, 1.0, v44\n\t"
    "v_add_f32 v45, 1.0, v45\n\t"
    "v_add_f32 v46, 1.0, v46\n\t"
    "v_mfma_f32_16x16x32_bf16 v[68:71], v[236:239], v[104:107], v[68:71]\n\t"
    "ds_read_b128 v[100:103], %[ea] offset:2048\n\t"
    "v_add_f32 v47, 1.0, v47\n\t"
    "v_rcp_f32 v40, v40\n\t"
    "v_rcp_f32 v41, v41\n\t"
    "v_mfma_f32_16x16x32_bf16 v[72:75], v[232:235], v[108:111], v[72:75]\n\t"
    "v_rcp_f32 v42, v42\n\t"
    "v_rcp_f32 v43, v43\n\t"
    "v_rcp_f32 v44, v44\n\t"
    "v_mfma_f32_16x16x32_bf16 v[76:79], v[236:239], v[108:111], v[76:79]\n\t"
    "v_rcp_f32 v45, v45\n\t"
    "v_rcp_f32 v46, v46\n\t"
    "v_rcp_f32 v47, v47\n\t"
    "s_waitcnt lgkmcnt(4)\n\t"
    "v_mfma_f32_16x16x32_bf16 v[64:67], v[240:243], v[112:115], v[64:67]\n\t"
    "ds_read_b128 v[104:107], %[da] offset:0\n\t"
    "v_mad_u32_u24 v39, v39, %[mu], %[ad]\n\t"
    "v_mul_f32 v40, v48, v40\n\t"
    "v_mul_f32 v41, v49, v41\n\t"
    "v_mul_f32 v42, v50, v42\n\t"
    "v_mfma_f32_16x16x32_bf16 v[68:71], v[244:247], v[112:115], v[68:71]\n\t"
    "ds_read_b128 v[108:111], %[da] offset:2048\n\t"
    "v_cmp_ge_i32 vcc, %[lf], v39\n\t"
    "s_cmp_eq_u64 vcc, 0\n\t"
    "s_addc_u32 %[lf], %[lf], 0\n\t"
    "v_mul_f32 v43, v51, v43\n\t"
    "v_mul_f32 v44, v56, v44\n\t"
    "v_mul_f32 v45, v57, v45\n\t"
    "v_mfma_f32_16x16x32_bf16 v[72:75], v[240:243], v[116:119], v[72:75]\n\t"
    "v_mul_f32 v46, v58, v46\n\t"
    "v_mul_f32 v47, v59, v47\n\t"
    "v_mul_f32 v40, v40, v52\n\t"
    "v_mfma_f32_16x16x32_bf16 v[76:79], v[244:247], v[116:119], v[76:79]\n\t"
    "v_cmp_ge_i32 vcc, %[lf], v39\n\t"
    "s_cmp_eq_u64 vcc, 0\n\t"
    "s_addc_u32 %[lf], %[lf], 0\n\t"
    "v_mul_f32 v41, v41, v53\n\t"
    "v_mul_f32 v42, v42, v54\n\t"
    "v_mul_f32 v43, v43, v55\n\t"
    "s_waitcnt lgkmcnt(4)\n\t"
    "v_mfma_f32_16x16x32_bf16 v[80:83], v[248:251], v[120:123], v[64:67]\n\t"
    "ds_read_b128 v[112:115], %[ea] offset:4096\n\t"
    "s_min_i32 %[lf], %[lf], %[cp]\n\t"
    "s_add_u32 %[b], %[b], 1\n\t"
    "v_mul_f32 v44, v44, v60\n\t"
    "v_mul_f32 v45, v45, v61\n\t"
    "v_mul_f32 v46, v46, v62\n\t"
    "v_mfma_f32_16x16x32_bf16 v[84:87], v[252:255], v[120:123], v[68:71]\n\t"
    "ds_read_b128 v[116:119], %[ea] offset:6144\n\t"
    "v_mul_f32 v47, v47, v63\n\t"
    "v_cvt_pk_bf16_f32 v48, v40, v41\n\t"
    "v_cvt_pk_bf16_f32 v49, v42, v43\n\t"
    "v_mfma_f32_16x16x32_bf16 v[88:91], v[248:251], v[124:127], v[72:75]\n\t"
    "v_cvt_pk_bf16_f32 v50, v44, v45\n\t"
    "v_cvt_pk_bf16_f32 v51, v46, v47\n\t"
    "s_nop 1\n\t"
    "v_mfma_f32_16x16x32_bf16 v[92:95], v[252:255], v[124:127], v[76:79]\n\t"
    "v_permlane16_swap_b32 v48, v50\n\t"
    "v_permlane16_swap_b32 v49, v51\n\t"
    "global_store_dwordx4 %[co0], v[48:51], s[36:37] sc1\n\t"
    "s_mov_b64 s[36:37], s[38:39]\n\t"
    "s_add_u32 s38, s38, %[ab]\n\t"
    "s_addc_u32 s39, s39, 0\n\t"
    "s_cmp_lt_i32 %[b], %[lf]\n\t"
    "s_cbranch_scc1 1b\n\t"
    "s_waitcnt lgkmcnt(0)\n\t"
    "s_nop 4\n\t"
    "s_branch 9f\n\t"
    "5:\n\t"
    "s_add_u32 %[st], %[b], %[po]\n\t"
    "s_max_i32 %[st], %[st], %[p0]\n\t"
    "v_mov_b32 v39, %[st]\n\t"
    "v_mov_b32 v38, 0\n\t"
    "s_mov_b64 exec, 1\n\t"
    "global_store_dword v38, v39, %[pg] sc1\n\t"
    "s_mov_b64 exec, -1\n\t"
    "s_mov_b32 %[p0], %[p1]\n\t"
    "s_xor_b32 %[sl], %[sl], 0x100\n\t"
    "s_cmp_ge_i32 %[lf], %[cp]\n\t"
    "s_cbranch_scc1 6b\n\t"
    "s_mov_b32 m0, %[sl]\n\t"
    "s_nop 0\n\t"
    "global_load_lds_dword %[dv], %[db] sc1\n\t"
    "s_branch 6b\n\t"
    "9:"
    : "=&{v[80:95]}"(pd), [ea] "+&v"(ea), [da] "+&v"(da), [b] "+&s"(b), [lf] "+&s"(lf), "+{s[36:37]}"(p1), "+{s[38:39]}"(p0), [sa] "+&v"(sa), [st] "=&s"(st), [sl] "+&s"(sl), [p0] "+&s"(pf)
    : [rw] "v"(rw), [ab] "s"(ab), [cp] "s"(cp), [mu] "s"(mu), [ad] "v"(ad), "{v[128:159]}"(W[0]), "{v[160:191]}"(W[1]), "{v[192:223]}"(W[2]), "{v[224:255]}"(W[3]), [co0] "v"(co0), [w0] "s"(w0), [dv] "v"(dv), [db] "s"(db), [pg] "s"(pg), [po] "s"(po - 1), [p1] "s"(b + po), [nl] "s"(-LOG2E)
    : "v38", "v39", "v40", "v41", "v42", "v43", "v44", "v45", "v46", "v47", "v48", "v49", "v50", "v51", "v52", "v53", "v54", "v55", "v56", "v57", "v58", "v59", "v60", "v61", "v62", "v63", "v64", "v65", "v66", "v67", "v68", "v69", "v70", "v71", "v72", "v73", "v74", "v75", "v76", "v77", "v78", "v79", "v96", "v97", "v98", "v99", "v100", "v101", "v102", "v103", "v104", "v105", "v106", "v107", "v108", "v109", "v110", "v111", "v112", "v113", "v114", "v115", "v116", "v117", "v118", "v119", "v120", "v121", "v122", "v123", "v124", "v125", "v126", "v127", "vcc", "scc", "m0", "memory");
}
__device__ __forceinline__ void fast_st2(const u32x32 (&W)[4], u32x16& pd, unsigned ea, unsigned da, int& b, int& lf, int cp, unsigned sa, unsigned mu, unsigned ad,
    unsigned rw, unsigned ab, const char* p1, const char* p0, unsigned co0, unsigned co1, int po, int pf, unsigned w0, unsigned sl, unsigned dv, const char* db, const char* pg) {
  unsigned st;
  asm volatile("s_barrier\n\t"
    "ds_read_b128 v[96:99], %[ea] offset:0\n\t"
    "ds_read_b128 v[100:103], %[ea] offset:2048\n\t"
    "ds_read_b128 v[104:107], %[da] offset:0\n\t"
    "ds_read_b128 v[108:111], %[da] offset:2048\n\t"
    "ds_read_b128 v[112:115], %[ea] offset:4096\n\t"
    "ds_read_b128 v[116:119], %[ea] offset:6144\n\t"
    "s_branch 2f\n\t"
    "1:\n\t"
    "s_barrier\n\t"
    "2:\n\t"
    "s_waitcnt lgkmcnt(4)\n\t"
    "v_mfma_f32_16x16x32_bf16 v[64:67], v[128:131], v[96:99], 0\n\t"
    "ds_read_b128 v[120:123], %[da] offset:4096\n\t"
    "v_mfma_f32_16x16x32_bf16 v[68:71], v[132:135], v[96:99], 0\n\t"
    "ds_read_b128 v[124:127], %[da] offset:6144\n\t"
    "v_mfma_f32_16x16x32_bf16 v[72:75], v[128:131], v[100:103], 0\n\t"
    "v_mfma_f32_16x16x32_bf16 v[76:79], v[132:135], v[100:103], 0\n\t"
    "s_waitcnt lgkmcnt(4)\n\t"
    "v_mfma_f32_16x16x32_bf16 v[64:67], v[136:139], v[104:107], v[64:67]\n\t"
    "ds_read_b128 v[96:99], %[ea] offset:8192\n\t"
    "v_mfma_f32_16x16x32_bf16 v[68:71], v[140:143], v[104:107], v[68:71]\n\t"
    "ds_read_b128 v[100:103], %[ea] offset:10240\n\t"
    "v_mfma_f32_16x16x32_bf16 v[72:75], v[136:139], v[108:111], v[72:75]\n\t"
    "v_mfma_f32_16x16x32_bf16 v[76:79], v[140:143], v[108:111], v[76:79]\n\t"
    "s_waitcnt lgkmcnt(4)\n\t"
    "v_mfma_f32_16x16x32_bf16 v[64:67], v[144:147], v[112:115], v[64:67]\n\t"
    "ds_read_b128 v[104:107], %[da] offset:8192\n\t"
    "v_mfma_f32_16x16x32_bf16 v[68:71], v[148:151], v[112:115], v[68:71]\n\t"
    "ds_read_b128 v[108:111], %[da] offset:10240\n\t"
    "v_mfma_f32_16x16x32_bf16 v[72:75], v[144:147], v[116:119], v[72:75]\n\t"
    "v_mfma_f32_16x16x32_bf16 v[76:79], v[148:151], v[116:119], v[76:79]\n\t"
    "s_waitcnt lgkmcnt(4)\n\t"
    "v_mfma_f32_16x16x32_bf16 v[64:67], v[152:155], v[120:123], v[64:67]\n\t"
    "ds_read_b128 v[112:115], %[ea] offset:12288\n\t"
    "v_mfma_f32_16x16x32_bf16 v[68:71], v[156:159], v[120:123], v[68:71]\n\t"
    "ds_read_b128 v[116:119], %[ea] offset:14336\n\t"
    "v_mfma_f32_16x16x32_bf16 v[72:75], v[152:155], v[124:127], v[72:75]\n\t"
    "v_mfma_f32_16x16x32_bf16 v[76:79], v[156:159], v[124:127], v[76:79]\n\t"
    "s_waitcnt lgkmcnt(4)\n\t"
    "v_mfma_f32_16x16x32_bf16 v[64:67], v[160:163], v[96:99], v[64:67]\n\t"
    "ds_read_b128 v[120:123], %[da] offset:12288\n\t"
    "v_mfma_f32_16x16x32_bf16 v[68:71], v[164:167], v[96:99], v[68:71]\n\t"
    "ds_read_b128 v[124:127], %[da] offset:14336\n\t"
    "v_mfma_f32_16x16x32_bf16 v[72:75], v[160:163], v[100:103], v[72:75]\n\t"
    "v_mfma_f32_16x16x32_bf16 v[76:79], v[164:167], v[100:103], v[76:79]\n\t"
    "s_waitcnt lgkmcnt(4)\n\t"
    "v_mfma_f32_16x16x32_bf16 v[64:67], v[168:171], v[104:107], v[64:67]\n\t"
    "ds_read_b128 v[96:99], %[ea] offset:16384\n\t"
    "v_mfma_f32_16x16x32_bf16 v[68:71], v[172:175], v[104:107], v[68:71]\n\t"
    "ds_read_b128 v[100:103], %[ea] offset:18432\n\t"
    "v_mfma_f32_16x16x32_bf16 v[72:75], v[168:171], v[108:111], v[72:75]\n\t"
    "v_mfma_f32_16x16x32_bf16 v[76:79], v[172:175], v[108:111], v[76:79]\n\t"
    "s_waitcnt lgkmcnt(4)\n\t"
    "v_mfma_f32_16x16x32_bf16 v[64:67], v[176:179], v[112:115], v[64:67]\n\t"
    "ds_read_b128 v[104:107], %[da] offset:16384\n\t"
    "v_mfma_f32_16x16x32_bf16 v[68:71], v[180:183], v[112:115], v[68:71]\n\t"
    "ds_read_b128 v[108:111], %[da] offset:18432\n\t"
    "v_mfma_f32_16x16x32_bf16 v[72:75], v[176:179], v[116:119], v[72:75]\n\t"
    "v_mfma_f32_16x16x32_bf16 v[76:79], v[180:183], v[116:119], v[76:79]\n\t"
    "s_waitcnt lgkmcnt(4)\n\t"
    "v_mfma_f32_16x16x32_bf16 v[64:67], v[184:187], v[120:123], v[64:67]\n\t"
    "ds_read_b128 v[112:115], %[ea] offset:20480\n\t"
    "v_mfma_f32_16x16x32_bf16 v[68:71], v[188:191], v[120:123], v[68:71]\n\t"
    "ds_read_b128 v[116:119], %[ea] offset:22528\n\t"
    "v_mfma_f32_16x16x32_bf16 v[72:75], v[184:187], v[124:127], v[72:75]\n\t"
    "v_mfma_f32_16x16x32_bf16 v[76:79], v[188:191], v[124:127], v[76:79]\n\t"
    "s_waitcnt vmcnt(0)\n\t"
    "s_barrier\n\t"
    "s_waitcnt lgkmcnt(4)\n\t"
    "v_mfma_f32_16x16x32_bf16 v[64:67], v[192:195], v[96:99], v[64:67]\n\t"
    "ds_read_b128 v[120:123], %[da] offset:20480\n\t"
    "v_mfma_f32_16x16x32_bf16 v[68:71], v[196:199], v[96:99], v[68:71]\n\t"
    "ds_read_b128 v[124:127], %[da] offset:22528\n\t"
    "v_mfma_f32_16x16x32_bf16 v[72:75], v[192:195], v[100:103], v[72:75]\n\t"
    "ds_read_b128 v[48:51], %[rw] offset:0\n\t"
    "v_mfma_f32_16x16x32_bf16 v[76:79], v[196:199], v[100:103], v[76:79]\n\t"
    "ds_read_b128 v[52:55], %[rw] offset:1024\n\t"
    "s_cmp_eq_u32 %[w0], 0\n\t"
    "s_cbranch_scc1 5f\n\t"
    "6:\n\t"
    "s_waitcnt lgkmcnt(6)\n\t"
    "v_mfma_f32_16x16x32_bf16 v[64:67], v[200:203], v[104:107], v[64:67]\n\t"
    "ds_read_b128 v[96:99], %[ea] offset:24576\n\t"
    "v_mfma_f32_16x16x32_bf16 v[68:71], v[204:207], v[104:107], v[68:71]\n\t"
    "ds_read_b128 v[100:103], %[ea] offset:26624\n\t"
    "v_mfma_f32_16x16x32_bf16 v[72:75], v[200:203], v[108:111], v[72:75]\n\t"
    "ds_read_b128 v[56:59], %[rw] offset:2048\n\t"
    "v_mfma_f32_16x16x32_bf16 v[76:79], v[204:207], v[108:111], v[76:79]\n\t"
    "ds_read_b128 v[60:63], %[rw] offset:3072\n\t"
    "s_waitcnt lgkmcnt(0)\n\t"
    "v_mfma_f32_16x16x32_bf16 v[64:67], v[208:211], v[112:115], v[64:67]\n\t"
    "ds_read_b128 v[104:107], %[da] offset:24576\n\t"
    "v_cvt_pk_bf16_f32 v40, v80, v81\n\t"
    "v_mfma_f32_16x16x32_bf16 v[68:71], v[212:215], v[112:115], v[68:71]\n\t"
    "ds_read_b128 v[108:111], %[da] offset:26624\n\t"
    "v_cvt_pk_bf16_f32 v41, v82, v83\n\t"
    "v_mfma_f32_16x16x32_bf16 v[72:75], v[208:211], v[116:119], v[72:75]\n\t"
    "ds_read_b32 v39, %[sa]\n\t"
    "v_cvt_pk_bf16_f32 v42, v84, v85\n\t"
    "v_mfma_f32_16x16x32_bf16 v[76:79], v[212:215], v[116:119], v[76:79]\n\t"
    "v_xor_b32 %[sa], 0x100, %[sa]\n\t"
    "v_cvt_pk_bf16_f32 v43, v86, v87\n\t"
    "v_mfma_f32_16x16x32_bf16 v[64:67], v[216:219], v[120:123], v[64:67]\n\t"
    "ds_read_b128 v[112:115], %[ea] offset:28672\n\t"
    "global_store_dwordx4 %[co0], v[40:43], s[36:37] sc1\n\t"
    "v_mfma_f32_16x16x32_bf16 v[68:71], v[220:223], v[120:123], v[68:71]\n\t"
    "ds_read_b128 v[116:119], %[ea] offset:30720\n\t"
    "v_cvt_pk_bf16_f32 v44, v88, v89\n\t"
    "v_mfma_f32_16x16x32_bf16 v[72:75], v[216:219], v[124:127], v[72:75]\n\t"
    "v_xor_b32 %[ea], 0x10000, %[ea]\n\t"
    "v_cvt_pk_bf16_f32 v45, v90, v91\n\t"
    "v_mfma_f32_16x16x32_bf16 v[76:79], v[220:223], v[124:127], v[76:79]\n\t"
    "v_cvt_pk_bf16_f32 v46, v92, v93\n\t"
    "v_mfma_f32_16x16x32_bf16 v[64:67], v[224:227], v[96:99], v[64:67]\n\t"
    "ds_read_b128 v[120:123], %[da] offset:28672\n\t"
    "v_cvt_pk_bf16_f32 v47, v94, v95\n\t"
    "v_mfma_f32_16x16x32_bf16 v[68:71], v[228:231], v[96:99], v[68:71]\n\t"
    "ds_read_b128 v[124:127], %[da] offset:30720\n\t"
    "global_store_dwordx4 %[co1], v[44:47], s[36:37] sc1\n\t"
    "v_mfma_f32_16x16x32_bf16 v[72:75], v[224:227], v[100:103], v[72:75]\n\t"
    "v_xor_b32 %[da], 0x10000, %[da]\n\t"
    "v_cvt_pk_bf16_f32 v48, v48, v49\n\t"
    "v_mfma_f32_16x16x32_bf16 v[76:79], v[228:231], v[100:103], v[76:79]\n\t"
    "v_cvt_pk_bf16_f32 v49, v50, v51\n\t"
    "s_waitcnt lgkmcnt(5)\n\t"
    "v_mfma_f32_16x16x32_bf16 v[64:67], v[232:235], v[104:107], v[64:67]\n\t"
    "ds_read_b128 v[96:99], %[ea] offset:0\n\t"
    "v_cvt_pk_bf16_f32 v50, v52, v53\n\t"
    "v_mfma_f32_16x16x32_bf16 v[68:71], v[236:239], v[104:107], v[68:71]\n\t"
    "ds_read_b128 v[100:103], %[ea] offset:2048\n\t"
    "v_cvt_pk_bf16_f32 v51, v54, v55\n\t"
    "v_mfma_f32_16x16x32_bf16 v[72:75], v[232:235], v[108:111], v[72:75]\n\t"
    "global_store_dwordx4 %[co0], v[48:51], s[36:37] offset:2048 sc1\n\t"
    "v_mfma_f32_16x16x32_bf16 v[76:79], v[236:239], v[108:111], v[76:79]\n\t"
    "v_cvt_pk_bf16_f32 v56, v56, v57\n\t"
    "s_waitcnt lgkmcnt(4)\n\t"
    "v_mfma_f32_16x16x32_bf16 v[64:67], v[240:243], v[112:115], v[64:67]\n\t"
    "ds_read_b128 v[104:107], %[da] offset:0\n\t"
    "v_mad_u32_u24 v39, v39, %[mu], %[ad]\n\t"
    "v_cvt_pk_bf16_f32 v57, v58, v59\n\t"
    "v_mfma_f32_16x16x32_bf16 v[68:71], v[244:247], v[112:115], v[68:71]\n\t"
    "ds_read_b128 v[108:111], %[da] offset:2048\n\t"
    "v_cmp_ge_i32 vcc, %[lf], v39\n\t"
    "s_cmp_eq_u64 vcc, 0\n\t"
    "s_addc_u32 %[lf], %[lf], 0\n\t"
    "v_cvt_pk_bf16_f32 v58, v60, v61\n\t"
    "v_mfma_f32_16x16x32_bf16 v[72:75], v[240:243], v[116:119], v[72:75]\n\t"
    "v_cvt_pk_bf16_f32 v59, v62, v63\n\t"
    "v_mfma_f32_16x16x32_bf16 v[76:79], v[244:247], v[116:119], v[76:79]\n\t"
    "v_cmp_ge_i32 vcc, %[lf], v39\n\t"
    "s_cmp_eq_u64 vcc, 0\n\t"
    "s_addc_u32 %[lf], %[lf], 0\n\t"
    "global_store_dwordx4 %[co1], v[56:59], s[36:37] offset:2048 sc1\n\t"
    "s_waitcnt lgkmcnt(4)\n\t"
    "v_mfma_f32_16x16x32_bf16 v[80:83], v[248:251], v[120:123], v[64:67]\n\t"
    "ds_read_b128 v[112:115], %[ea] offset:4096\n\t"
    "s_min_i32 %[lf], %[lf], %[cp]\n\t"
    "s_add_u32 %[b], %[b], 1\n\t"
    "v_mfma_f32_16x16x32_bf16 v[84:87], v[252:255], v[120:123], v[68:71]\n\t"
    "ds_read_b128 v[116:119], %[ea] offset:6144\n\t"
    "v_mfma_f32_16x16x32_bf16 v[88:91], v[248:251], v[124:127], v[72:75]\n\t"
    "v_mfma_f32_16x16x32_bf16 v[92:95], v[252:255], v[124:127], v[76:79]\n\t"
    "s_mov_b64 s[36:37], s[38:39]\n\t"
    "s_add_u32 s38, s38, %[ab]\n\t"
    "s_addc_u32 s39, s39, 0\n\t"
    "s_cmp_lt_i32 %[b], %[lf]\n\t"
    "s_cbranch_scc1 1b\n\t"
    "s_waitcnt lgkmcnt(0)\n\t"
    "s_nop 4\n\t"
    "s_branch 9f\n\t"
    "5:\n\t"
    "s_add_u32 %[st], %[b], %[po]\n\t"
    "s_max_i32 %[st], %[st], %[p0]\n\t"
    "v_mov_b32 v39, %[st]\n\t"
    "v_mov_b32 v38, 0\n\t"
    "s_mov_b64 exec, 1\n\t"
    "global_store_dword v38, v39, %[pg] sc1\n\t"
    "s_mov_b64 exec, -1\n\t"
    "s_mov_b32 %[p0], %[p1]\n\t"
    "s_xor_b32 %[sl], %[sl], 0x100\n\t"
    "s_cmp_ge_i32 %[lf], %[cp]\n\t"
    "s_cbranch_scc1 6b\n\t"
    "s_mov_b32 m0, %[sl]\n\t"
    "s_nop 0\n\t"
    "global_load_lds_dword %[dv], %[db] sc1\n\t"
    "s_branch 6b\n\t"
    "9:"
    : "=&{v[80:95]}"(pd), [ea] "+&v"(ea), [da] "+&v"(da), [b] "+&s"(b), [lf] "+&s"(lf), "+{s[36:37]}"(p1), "+{s[38:39]}"(p0), [sa] "+&v"(sa), [st] "=&s"(st), [sl] "+&s"(sl), [p0] "+&s"(pf)
    : [rw] "v"(rw), [ab] "s"(ab), [cp] "s"(cp), [mu] "s"(mu), [ad] "v"(ad), "{v[128:159]}"(W[0]), "{v[160:191]}"(W[1]), "{v[192:223]}"(W[2]), "{v[224:255]}"(W[3]), [co0] "v"(co0), [w0] "s"(w0), [dv] "v"(dv), [db] "s"(db), [pg] "s"(pg), [po] "s"(po - 1), [p1] "s"(b + po), [co1] "v"(co1)
    : "v38", "v39", "v40", "v41", "v42", "v43", "v44", "v45", "v46", "v47", "v48", "v49", "v50", "v51", "v52", "v53", "v54", "v55", "v56", "v57", "v58", "v59", "v60", "v61", "v62", "v63", "v64", "v65", "v66", "v67", "v68", "v69", "v70", "v71", "v72", "v73", "v74", "v75", "v76", "v77", "v78", "v79", "v96", "v97", "v98", "v99", "v100", "v101", "v102", "v103", "v104", "v105", "v106", "v107", "v108", "v109", "v110", "v111", "v112", "v113", "v114", "v115", "v116", "v117", "v118", "v119", "v120", "v121", "v122", "v123", "v124", "v125", "v126", "v127", "vcc", "scc", "m0", "memory");
}
__device__ __forceinline__ void near_ld0(const u32x32 (&W)[4], u32x16& pd, unsigned ea, unsigned da, int& b, int& lf, int cp, unsigned sa, unsigned mu, unsigned ad,
    unsigned rw, unsigned ab, const char* p1, const char* p0, unsigned l1, unsigned l0, const unsigned (&av)[8]) {
  asm volatile("s_setprio 0\n\t"
    "s_waitcnt vmcnt(4)\n\t"
    "s_barrier\n\t"
    "ds_read_b128 v[96:99], %[ea] offset:0\n\t"
    "ds_read_b128 v[100:103], %[ea] offset:2048\n\t"
    "ds_read_b128 v[104:107], %[da] offset:0\n\t"
    "ds_read_b128 v[108:111], %[da] offset:2048\n\t"
    "ds_read_b128 v[112:115], %[ea] offset:4096\n\t"
    "ds_read_b128 v[116:119], %[ea] offset:6144\n\t"
    "s_branch 2f\n\t"
    "1:\n\t"
    "s_waitcnt vmcnt(4)\n\t"
    "s_nop 1\n\t"
    "s_barrier\n\t"
    "2:\n\t"
    "s_waitcnt lgkmcnt(4)\n\t"
    "v_mfma_f32_16x16x32_bf16 v[64:67], v[128:131], v[96:99], 0\n\t"
    "ds_read_b128 v[120:123], %[da] offset:4096\n\t"
    "s_add_u32 m0, %[l1], 0\n\t"
    "ds_write_b128 %[rw], v[80:83] offset:0\n\t"
    "v_mfma_f32_16x16x32_bf16 v[68:71], v[132:135], v[96:99], 0\n\t"
    "ds_read_b128 v[124:127], %[da] offset:6144\n\t"
    "global_load_lds_dwordx4 %[av0], s[36:37] sc1\n\t"
    "ds_write_b128 %[rw], v[84:87] offset:1024\n\t"
    "v_mfma_f32_16x16x32_bf16 v[72:75], v[128:131], v[100:103], 0\n\t"
    "s_add_u32 m0, %[l1], 32768\n\t"
    "ds_write_b128 %[rw], v[88:91] offset:2048\n\t"
    "v_mfma_f32_16x16x32_bf16 v[76:79], v[132:135], v[100:103], 0\n\t"
    "global_load_lds_dwordx4 %[av1], s[36:37] sc1\n\t"
    "ds_write_b128 %[rw], v[92:95] offset:3072\n\t"
    "s_waitcnt lgkmcnt(8)\n\t"
    "v_mfma_f32_16x16x32_bf16 v[64:67], v[136:139], v[104:107], v[64:67]\n\t"
    "ds_read_b128 v[96:99], %[ea] offset:8192\n\t"
    "s_add_u32 m0, %[l1], 4096\n\t"
    "v_mfma_f32_16x16x32_bf16 v[68:71], v[140:143], v[104:107], v[68:71]\n\t"
    "ds_read_b128 v[100:103], %[ea] offset:10240\n\t"
    "global_load_lds_dwordx4 %[av2], s[36:37] sc1\n\t"
    "v_mfma_f32_16x16x32_bf16 v[72:75], v[136:139], v[108:111], v[72:75]\n\t"
    "s_add_u32 m0, %[l1], 36864\n\t"
    "v_mfma_f32_16x16x32_bf16 v[76:79], v[140:143], v[108:111], v[76:79]\n\t"
    "global_load_lds_dwordx4 %[av3], s[36:37] sc1\n\t"
    "s_waitcnt lgkmcnt(2)\n\t"
    "v_mfma_f32_16x16x32_bf16 v[64:67], v[144:147], v[112:115], v[64:67]\n\t"
    "ds_read_b128 v[104:107], %[da] offset:8192\n\t"
    "s_add_u32 m0, %[l1], 8192\n\t"
    "v_mfma_f32_16x16x32_bf16 v[68:71], v[148:151], v[112:115], v[68:71]\n\t"
    "ds_read_b128 v[108:111], %[da] offset:10240\n\t"
    "global_load_lds_dwordx4 %[av4], s[36:37] sc1\n\t"
    "v_mfma_f32_16x16x32_bf16 v[72:75], v[144:147], v[116:119], v[72:75]\n\t"
    "s_add_u32 m0, %[l1], 40960\n\t"
    "v_mfma_f32_16x16x32_bf16 v[76:79], v[148:151], v[116:119], v[76:79]\n\t"
    "global_load_lds_dwordx4 %[av5], s[36:37] sc1\n\t"
    "s_waitcnt vmcnt(6)\n\t"
    "s_barrier\n\t"
    "v_mfma_f32_16x16x32_bf16 v[64:67], v[152:155], v[120:123], v[64:67]\n\t"
    "ds_read_b128 v[112:115], %[ea] offset:12288\n\t"
    "s_add_u32 m0, %[l1], 12288\n\t"
    "v_mfma_f32_16x16x32_bf16 v[68:71], v[156:159], v[120:123], v[68:71]\n\t"
    "ds_read_b128 v[116:119], %[ea] offset:14336\n\t"
    "global_load_lds_dwordx4 %[av6], s[36:37] sc1\n\t"
    "v_mfma_f32_16x16x32_bf16 v[72:75], v[152:155], v[124:127], v[72:75]\n\t"
    "s_add_u32 m0, %[l1], 45056\n\t"
    "v_mfma_f32_16x16x32_bf16 v[76:79], v[156:159], v[124:127], v[76:79]\n\t"
    "global_load_lds_dwordx4 %[av7], s[36:37] sc1\n\t"
    "s_waitcnt lgkmcnt(4)\n\t"
    "v_mfma_f32_16x16x32_bf16 v[64:67], v[160:163], v[96:99], v[64:67]\n\t"
    "ds_read_b128 v[120:123], %[da] offset:12288\n\t"
    "s_add_u32 m0, %[l0], 0\n\t"
    "v_mfma_f32_16x16x32_bf16 v[68:71], v[164:167], v[96:99], v[68:71]\n\t"
    "ds_read_b128 v[124:127], %[da] offset:14336\n\t"
    "global_load_lds_dwordx4 %[av0], s[38:39] sc1\n\t"
    "v_mfma_f32_16x16x32_bf16 v[72:75], v[160:163], v[100:103], v[72:75]\n\t"
    "s_add_u32 m0, %[l0], 32768\n\t"
    "v_mfma_f32_16x16x32_bf16 v[76:79], v[164:167], v[100:103], v[76:79]\n\t"
    "global_load_lds_dwordx4 %[av1], s[38:39] sc1\n\t"
    "s_waitcnt lgkmcnt(4)\n\t"
    "v_mfma_f32_16x16x32_bf16 v[64:67], v[168:171], v[104:107], v[64:67]\n\t"
    "ds_read_b128 v[96:99], %[ea] offset:16384\n\t"
    "s_add_u32 m0, %[l0], 4096\n\t"
    "v_mfma_f32_16x16x32_bf16 v[68:71], v[172:175], v[104:107], v[68:71]\n\t"
    "ds_read_b128 v[100:103], %[ea] offset:18432\n\t"
    "global_load_lds_dwordx4 %[av2], s[38:39] sc1\n\t"
    "v_mfma_f32_16x16x32_bf16 v[72:75], v[168:171], v[108:111], v[72:75]\n\t"
    "s_add_u32 m0, %[l0], 36864\n\t"
    "v_mfma_f32_16x16x32_bf16 v[76:79], v[172:175], v[108:111], v[76:79]\n\t"
    "global_load_lds_dwordx4 %[av3], s[38:39] sc1\n\t"
    "s_waitcnt lgkmcnt(4)\n\t"
    "v_mfma_f32_16x16x32_bf16 v[64:67], v[176:179], v[112:115], v[64:67]\n\t"
    "ds_read_b128 v[104:107], %[da] offset:16384\n\t"
    "s_add_u32 m0, %[l0], 8192\n\t"
    "v_mfma_f32_16x16x32_bf16 v[68:71], v[180:183], v[112:115], v[68:71]\n\t"
    "ds_read_b128 v[108:111], %[da] offset:18432\n\t"
    "global_load_lds_dwordx4 %[av4], s[38:39] sc1\n\t"
    "v_mfma_f32_16x16x32_bf16 v[72:75], v[176:179], v[116:119], v[72:75]\n\t"
    "s_add_u32 m0, %[l0], 40960\n\t"
    "v_mfma_f32_16x16x32_bf16 v[76:79], v[180:183], v[116:119], v[76:79]\n\t"
    "global_load_lds_dwordx4 %[av5], s[38:39] sc1\n\t"
    "s_waitcnt lgkmcnt(4)\n\t"
    "v_mfma_f32_16x16x32_bf16 v[64:67], v[184:187], v[120:123], v[64:67]\n\t"
    "ds_read_b128 v[112:115], %[ea] offset:20480\n\t"
    "s_add_u32 m0, %[l0], 12288\n\t"
    "v_mfma_f32_16x16x32_bf16 v[68:71], v[188:191], v[120:123], v[68:71]\n\t"
    "ds_read_b128 v[116:119], %[ea] offset:22528\n\t"
    "global_load_lds_dwordx4 %[av6], s[38:39] sc1\n\t"
    "v_mfma_f32_16x16x32_bf16 v[72:75], v[184:187], v[124:127], v[72:75]\n\t"
    "s_add_u32 m0, %[l0], 45056\n\t"
    "v_mfma_f32_16x16x32_bf16 v[76:79], v[188:191], v[124:127], v[76:79]\n\t"
    "global_load_lds_dwordx4 %[av7], s[38:39] sc1\n\t"
    "s_waitcnt lgkmcnt(4)\n\t"
    "v_mfma_f32_16x16x32_bf16 v[64:67], v[192:195], v[96:99], v[64:67]\n\t"
    "ds_read_b128 v[120:123], %[da] offset:20480\n\t"
    "v_mfma_f32_16x16x32_bf16 v[68:71], v[196:199], v[96:99], v[68:71]\n\t"
    "ds_read_b128 v[124:127], %[da] offset:22528\n\t"
    "v_mfma_f32_16x16x32_bf16 v[72:75], v[192:195], v[100:103], v[72:75]\n\t"
    "v_mfma_f32_16x16x32_bf16 v[76:79], v[196:199], v[100:103], v[76:79]\n\t"
    "s_waitcnt lgkmcnt(4)\n\t"
    "v_mfma_f32_16x16x32_bf16 v[64:67], v[200:203], v[104:107], v[64:67]\n\t"
    "ds_read_b128 v[96:99], %[ea] offset:24576\n\t"
    "v_mfma_f32_16x16x32_bf16 v[68:71], v[204:207], v[104:107], v[68:71]\n\t"
    "ds_read_b128 v[100:103], %[ea] offset:26624\n\t"
    "v_mfma_f32_16x16x32_bf16 v[72:75], v[200:203], v[108:111], v[72:75]\n\t"
    "v_mfma_f32_16x16x32_bf16 v[76:79], v[204:207], v[108:111], v[76:79]\n\t"
    "s_add_u32 s36, s36, %[ab]\n\t"
    "s_addc_u32 s37, s37, 0\n\t"
    "s_waitcnt lgkmcnt(4)\n\t"
    "v_mfma_f32_16x16x32_bf16 v[64:67], v[208:211], v[112:115], v[64:67]\n\t"
    "ds_read_b128 v[104:107], %[da] offset:24576\n\t"
    "v_mfma_f32_16x16x32_bf16 v[68:71], v[212:215], v[112:115], v[68:71]\n\t"
    "ds_read_b128 v[108:111], %[da] offset:26624\n\t"
    "v_mfma_f32_16x16x32_bf16 v[72:75], v[208:211], v[116:119], v[72:75]\n\t"
    "v_mfma_f32_16x16x32_bf16 v[76:79], v[212:215], v[116:119], v[76:79]\n\t"
    "s_xor_b32 %[l1], %[l1], 0x10000\n\t"
    "s_waitcnt lgkmcnt(4)\n\t"
    "v_mfma_f32_16x16x32_bf16 v[64:67], v[216:219], v[120:123], v[64:67]\n\t"
    "ds_read_b128 v[112:115], %[ea] offset:28672\n\t"
    "v_mfma_f32_16x16x32_bf16 v[68:71], v[220:223], v[120:123], v[68:71]\n\t"
    "ds_read_b128 v[116:119], %[ea] offset:30720\n\t"
    "v_mfma_f32_16x16x32_bf16 v[72:75], v[216:219], v[124:127], v[72:75]\n\t"
    "v_xor_b32 %[ea], 0x10000, %[ea]\n\t"
    "v_mfma_f32_16x16x32_bf16 v[76:79], v[220:223], v[124:127], v[76:79]\n\t"
    "s_waitcnt vmcnt(12)\n\t"
    "s_barrier\n\t"
    "s_waitcnt lgkmcnt(4)\n\t"
    "v_mfma_f32_16x16x32_bf16 v[64:67], v[224:227], v[96:99], v[64:67]\n\t"
    "ds_read_b32 v39, %[sa]\n\t"
    "ds_read_b128 v[120:123], %[da] offset:28672\n\t"
    "v_mfma_f32_16x16x32_bf16 v[68:71], v[228:231], v[96:99], v[68:71]\n\t"
    "ds_read_b128 v[124:127], %[da] offset:30720\n\t"
    "v_mfma_f32_16x16x32_bf16 v[72:75], v[224:227], v[100:103], v[72:75]\n\t"
    "v_xor_b32 %[da], 0x10000, %[da]\n\t"
    "v_mfma_f32_16x16x32_bf16 v[76:79], v[228:231], v[100:103], v[76:79]\n\t"
    "s_waitcnt lgkmcnt(5)\n\t"
    "v_mfma_f32_16x16x32_bf16 v[64:67], v[232:235], v[104:107], v[64:67]\n\t"
    "ds_read_b128 v[96:99], %[ea] offset:0\n\t"
    "v_mfma_f32_16x16x32_bf16 v[68:71], v[236:239], v[104:107], v[68:71]\n\t"
    "ds_read_b128 v[100:103], %[ea] offset:2048\n\t"
    "v_mfma_f32_16x16x32_bf16 v[72:75], v[232:235], v[108:111], v[72:75]\n\t"
    "v_mfma_f32_16x16x32_bf16 v[76:79], v[236:239], v[108:111], v[76:79]\n\t"
    "s_waitcnt lgkmcnt(4)\n\t"
    "v_mfma_f32_16x16x32_bf16 v[64:67], v[240:243], v[112:115], v[64:67]\n\t"
    "ds_read_b128 v[104:107], %[da] offset:0\n\t"
    "v_mad_u32_u24 v39, v39, %[mu], %[ad]\n\t"
    "v_mfma_f32_16x16x32_bf16 v[68:71], v[244:247], v[112:115], v[68:71]\n\t"
    "ds_read_b128 v[108:111], %[da] offset:2048\n\t"
    "v_cmp_ge_i32 vcc, %[lf], v39\n\t"
    "s_cmp_eq_u64 vcc, 0\n\t"
    "s_addc_u32 %[lf], %[lf], 0\n\t"
    "v_mfma_f32_16x16x32_bf16 v[72:75], v[240:243], v[116:119], v[72:75]\n\t"
    "v_mfma_f32_16x16x32_bf16 v[76:79], v[244:247], v[116:119], v[76:79]\n\t"
    "v_cmp_ge_i32 vcc, %[lf], v39\n\t"
    "s_cmp_eq_u64 vcc, 0\n\t"
    "s_addc_u32 %[lf], %[lf], 0\n\t"
    "s_waitcnt lgkmcnt(4)\n\t"
    "v_mfma_f32_16x16x32_bf16 v[80:83], v[248:251], v[120:123], v[64:67]\n\t"
    "ds_read_b128 v[112:115], %[ea] offset:4096\n\t"
    "s_min_i32 %[lf], %[lf], %[cp]\n\t"
    "s_add_u32 %[b], %[b], 1\n\t"
    "v_mfma_f32_16x16x32_bf16 v[84:87], v[252:255], v[120:123], v[68:71]\n\t"
    "ds_read_b128 v[116:119], %[ea] offset:6144\n\t"
    "v_mfma_f32_16x16x32_bf16 v[88:91], v[248:251], v[124:127], v[72:75]\n\t"
    "v_mfma_f32_16x16x32_bf16 v[92:95], v[252:255], v[124:127], v[76:79]\n\t"
    "s_add_u32 s38, s38, %[ab]\n\t"
    "s_addc_u32 s39, s39, 0\n\t"
    "s_xor_b32 %[l0], %[l0], 0x10000\n\t"
    "s_cmp_lt_i32 %[b], %[lf]\n\t"
    "s_cbranch_scc1 1b\n\t"
    "s_waitcnt lgkmcnt(0)\n\t"
    "s_nop 4"
    : "=&{v[80:95]}"(pd), [ea] "+&v"(ea), [da] "+&v"(da), [b] "+&s"(b), [lf] "+&s"(lf), "+{s[36:37]}"(p1), "+{s[38:39]}"(p0), [sa] "+&v"(sa), [l1] "+&s"(l1), [l0] "+&s"(l0)
    : [rw] "v"(rw), [ab] "s"(ab), [cp] "s"(cp), [mu] "s"(mu), [ad] "v"(ad), "{v[128:159]}"(W[0]), "{v[160:191]}"(W[1]), "{v[192:223]}"(W[2]), "{v[224:255]}"(W[3]), [av0] "v"(av[0]), [av1] "v"(av[1]), [av2] "v"(av[2]), [av3] "v"(av[3]), [av4] "v"(av[4]), [av5] "v"(av[5]), [av6] "v"(av[6]), [av7] "v"(av[7])
    : "v39", "v64", "v65", "v66", "v67", "v68", "v69", "v70", "v71", "v72", "v73", "v74", "v75", "v76", "v77", "v78", "v79", "v96", "v97", "v98", "v99", "v100", "v101", "v102", "v103", "v104", "v105", "v106", "v107", "v108", "v109", "v110", "v111", "v112", "v113", "v114", "v115", "v116", "v117", "v118", "v119", "v120", "v121", "v122", "v123", "v124", "v125", "v126", "v127", "vcc", "scc", "m0", "memory");
}
__device__ __forceinline__ void near_ld2(const u32x32 (&W)[4], u32x16& pd, unsigned ea, unsigned da, int& b, int& lf, int cp, unsigned sa, unsigned mu, unsigned ad,
    unsigned rw, unsigned ab, const char* p1, const char* p0, unsigned l1, unsigned l0, const unsigned (&av)[8]) {
  asm volatile("s_setprio 0\n\t"
    "s_waitcnt vmcnt(2)\n\t"
    "s_barrier\n\t"
    "ds_read_b128 v[96:99], %[ea] offset:0\n\t"
    "ds_read_b128 v[100:103], %[ea] offset:2048\n\t"
    "ds_read_b128 v[104:107], %[da] offset:0\n\t"
    "ds_read_b128 v[108:111], %[da] offset:2048\n\t"
    "ds_read_b128 v[112:115], %[ea] offset:4096\n\t"
    "ds_read_b128 v[116:119], %[ea] offset:6144\n\t"
    "s_branch 2f\n\t"
    "1:\n\t"
    "s_waitcnt vmcnt(2)\n\t"
    "s_nop 1\n\t"
    "s_barrier\n\t"
    "2:\n\t"
    "s_waitcnt lgkmcnt(4)\n\t"
    "v_mfma_f32_16x16x32_bf16 v[64:67], v[128:131], v[96:99], 0\n\t"
    "ds_read_b128 v[120:123], %[da] offset:4096\n\t"
    "s_add_u32 m0, %[l1], 0\n\t"
    "ds_write_b128 %[rw], v[80:83] offset:0\n\t"
    "v_mfma_f32_16x16x32_bf16 v[68:71], v[132:135], v[96:99], 0\n\t"
    "ds_read_b128 v[124:127], %[da] offset:6144\n\t"
    "global_load_lds_dwordx4 %[av0], s[36:37] sc1\n\t"
    "ds_write_b128 %[rw], v[84:87] offset:1024\n\t"
    "v_mfma_f32_16x16x32_bf16 v[72:75], v[128:131], v[100:103], 0\n\t"
    "ds_write_b128 %[rw], v[88:91] offset:2048\n\t"
    "v_mfma_f32_16x16x32_bf16 v[76:79], v[132:135], v[100:103], 0\n\t"
    "ds_write_b128 %[rw], v[92:95] offset:3072\n\t"
    "s_waitcnt lgkmcnt(8)\n\t"
    "v_mfma_f32_16x16x32_bf16 v[64:67], v[136:139], v[104:107], v[64:67]\n\t"
    "ds_read_b128 v[96:99], %[ea] offset:8192\n\t"
    "s_add_u32 m0, %[l1], 4096\n\t"
    "v_mfma_f32_16x16x32_bf16 v[68:71], v[140:143], v[104:107], v[68:71]\n\t"
    "ds_read_b128 v[100:103], %[ea] offset:10240\n\t"
    "global_load_lds_dwordx4 %[av2], s[36:37] sc1\n\t"
    "v_mfma_f32_16x16x32_bf16 v[72:75], v[136:139], v[108:111], v[72:75]\n\t"
    "v_mfma_f32_16x16x32_bf16 v[76:79], v[140:143], v[108:111], v[76:79]\n\t"
    "s_waitcnt lgkmcnt(2)\n\t"
    "v_mfma_f32_16x16x32_bf16 v[64:67], v[144:147], v[112:115], v[64:67]\n\t"
    "ds_read_b128 v[104:107], %[da] offset:8192\n\t"
    "s_add_u32 m0, %[l1], 8192\n\t"
    "v_mfma_f32_16x16x32_bf16 v[68:71], v[148:151], v[112:115], v[68:71]\n\t"
    "ds_read_b128 v[108:111], %[da] offset:10240\n\t"
    "global_load_lds_dwordx4 %[av4], s[36:37] sc1\n\t"
    "v_mfma_f32_16x16x32_bf16 v[72:75], v[144:147], v[116:119], v[72:75]\n\t"
    "v_mfma_f32_16x16x32_bf16 v[76:79], v[148:151], v[116:119], v[76:79]\n\t"
    "s_waitcnt vmcnt(3)\n\t"
    "s_barrier\n\t"
    "v_mfma_f32_16x16x32_bf16 v[64:67], v[152:155], v[120:123], v[64:67]\n\t"
    "ds_read_b128 v[112:115], %[ea] offset:12288\n\t"
    "s_add_u32 m0, %[l1], 12288\n\t"
    "v_mfma_f32_16x16x32_bf16 v[68:71], v[156:159], v[120:123], v[68:71]\n\t"
    "ds_read_b128 v[116:119], %[ea] offset:14336\n\t"
    "global_load_lds_dwordx4 %[av6], s[36:37] sc1\n\t"
    "v_mfma_f32_16x16x32_bf16 v[72:75], v[152:155], v[124:127], v[72:75]\n\t"
    "v_mfma_f32_16x16x32_bf16 v[76:79], v[156:159], v[124:127], v[76:79]\n\t"
    "s_waitcnt lgkmcnt(4)\n\t"
    "v_mfma_f32_16x16x32_bf16 v[64:67], v[160:163], v[96:99], v[64:67]\n\t"
    "ds_read_b128 v[120:123], %[da] offset:12288\n\t"
    "s_add_u32 m0, %[l0], 0\n\t"
    "v_mfma_f32_16x16x32_bf16 v[68:71], v[164:167], v[96:99], v[68:71]\n\t"
    "ds_read_b128 v[124:127], %[da] offset:14336\n\t"
    "global_load_lds_dwordx4 %[av0], s[38:39] sc1\n\t"
    "v_mfma_f32_16x16x32_bf16 v[72:75], v[160:163], v[100:103], v[72:75]\n\t"
    "v_mfma_f32_16x16x32_bf16 v[76:79], v[164:167], v[100:103], v[76:79]\n\t"
    "s_waitcnt lgkmcnt(4)\n\t"
    "v_mfma_f32_16x16x32_bf16 v[64:67], v[168:171], v[104:107], v[64:67]\n\t"
    "ds_read_b128 v[96:99], %[ea] offset:16384\n\t"
    "s_add_u32 m0, %[l0], 4096\n\t"
    "v_mfma_f32_16x16x32_bf16 v[68:71], v[172:175], v[104:107], v[68:71]\n\t"
    "ds_read_b128 v[100:103], %[ea] offset:18432\n\t"
    "global_load_lds_dwordx4 %[av2], s[38:39] sc1\n\t"
    "v_mfma_f32_16x16x32_bf16 v[72:75], v[168:171], v[108:111], v[72:75]\n\t"
    "v_mfma_f32_16x16x32_bf16 v[76:79], v[172:175], v[108:111], v[76:79]\n\t"
    "s_waitcnt lgkmcnt(4)\n\t"
    "v_mfma_f32_16x16x32_bf16 v[64:67], v[176:179], v[112:115], v[64:67]\n\t"
    "ds_read_b128 v[104:107], %[da] offset:16384\n\t"
    "s_add_u32 m0, %[l0], 8192\n\t"
    "v_mfma_f32_16x16x32_bf16 v[68:71], v[180:183], v[112:115], v[68:71]\n\t"
    "ds_read_b128 v[108:111], %[da] offset:18432\n\t"
    "global_load_lds_dwordx4 %[av4], s[38:39] sc1\n\t"
    "v_mfma_f32_16x16x32_bf16 v[72:75], v[176:179], v[116:119], v[72:75]\n\t"
    "v_mfma_f32_16x16x32_bf16 v[76:79], v[180:183], v[116:119], v[76:79]\n\t"
    "s_waitcnt lgkmcnt(4)\n\t"
    "v_mfma_f32_16x16x32_bf16 v[64:67], v[184:187], v[120:123], v[64:67]\n\t"
    "ds_read_b128 v[112:115], %[ea] offset:20480\n\t"
    "s_add_u32 m0, %[l0], 12288\n\t"
    "v_mfma_f32_16x16x32_bf16 v[68:71], v[188:191], v[120:123], v[68:71]\n\t"
    "ds_read_b128 v[116:119], %[ea] offset:22528\n\t"
    "global_load_lds_dwordx4 %[av6], s[38:39] sc1\n\t"
    "v_mfma_f32_16x16x32_bf16 v[72:75], v[184:187], v[124:127], v[72:75]\n\t"
    "v_mfma_f32_16x16x32_bf16 v[76:79], v[188:191], v[124:127], v[76:79]\n\t"
    "s_waitcnt lgkmcnt(4)\n\t"
    "v_mfma_f32_16x16x32_bf16 v[64:67], v[192:195], v[96:99], v[64:67]\n\t"
    "ds_read_b128 v[120:123], %[da] offset:20480\n\t"
    "v_mfma_f32_16x16x32_bf16 v[68:71], v[196:199], v[96:99], v[68:71]\n\t"
    "ds_read_b128 v[124:127], %[da] offset:22528\n\t"
    "v_mfma_f32_16x16x32_bf16 v[72:75], v[192:195], v[100:103], v[72:75]\n\t"
    "v_mfma_f32_16x16x32_bf16 v[76:79], v[196:199], v[100:103], v[76:79]\n\t"
    "s_waitcnt lgkmcnt(4)\n\t"
    "v_mfma_f32_16x16x32_bf16 v[64:67], v[200:203], v[104:107], v[64:67]\n\t"
    "ds_read_b128 v[96:99], %[ea] offset:24576\n\t"
    "v_mfma_f32_16x16x32_bf16 v[68:71], v[204:207], v[104:107], v[68:71]\n\t"
    "ds_read_b128 v[100:103], %[ea] offset:26624\n\t"
    "v_mfma_f32_16x16x32_bf16 v[72:75], v[200:203], v[108:111], v[72:75]\n\t"
    "v_mfma_f32_16x16x32_bf16 v[76:79], v[204:207], v[108:111], v[76:79]\n\t"
    "s_add_u32 s36, s36, %[ab]\n\t"
    "s_addc_u32 s37, s37, 0\n\t"
    "s_waitcnt lgkmcnt(4)\n\t"
    "v_mfma_f32_16x16x32_bf16 v[64:67], v[208:211], v[112:115], v[64:67]\n\t"
    "ds_read_b128 v[104:107], %[da] offset:24576\n\t"
    "v_mfma_f32_16x16x32_bf16 v[68:71], v[212:215], v[112:115], v[68:71]\n\t"
    "ds_read_b128 v[108:111], %[da] offset:26624\n\t"
    "v_mfma_f32_16x16x32_bf16 v[72:75], v[208:211], v[116:119], v[72:75]\n\t"
    "v_mfma_f32_16x16x32_bf16 v[76:79], v[212:215], v[116:119], v[76:79]\n\t"
    "s_xor_b32 %[l1], %[l1], 0x10000\n\t"
    "s_waitcnt lgkmcnt(4)\n\t"
    "v_mfma_f32_16x16x32_bf16 v[64:67], v[216:219], v[120:123], v[64:67]\n\t"
    "ds_read_b128 v[112:115], %[ea] offset:28672\n\t"
    "v_mfma_f32_16x16x32_bf16 v[68:71], v[220:223], v[120:123], v[68:71]\n\t"
    "ds_read_b128 v[116:119], %[ea] offset:30720\n\t"
    "v_mfma_f32_16x16x32_bf16 v[72:75], v[216:219], v[124:127], v[72:75]\n\t"
    "v_xor_b32 %[ea], 0x10000, %[ea]\n\t"
    "v_mfma_f32_16x16x32_bf16 v[76:79], v[220:223], v[124:127], v[76:79]\n\t"
    "s_waitcnt vmcnt(6)\n\t"
    "s_barrier\n\t"
    "s_waitcnt lgkmcnt(4)\n\t"
    "v_mfma_f32_16x16x32_bf16 v[64:67], v[224:227], v[96:99], v[64:67]\n\t"
    "ds_read_b32 v39, %[sa]\n\t"
    "ds_read_b128 v[120:123], %[da] offset:28672\n\t"
    "v_mfma_f32_16x16x32_bf16 v[68:71], v[228:231], v[96:99], v[68:71]\n\t"
    "ds_read_b128 v[124:127], %[da] offset:30720\n\t"
    "v_mfma_f32_16x16x32_bf16 v[72:75], v[224:227], v[100:103], v[72:75]\n\t"
    "v_xor_b32 %[da], 0x10000, %[da]\n\t"
    "v_mfma_f32_16x16x32_bf16 v[76:79], v[228:231], v[100:103], v[76:79]\n\t"
    "s_waitcnt lgkmcnt(5)\n\t"
    "v_mfma_f32_16x16x32_bf16 v[64:67], v[232:235], v[104:107], v[64:67]\n\t"
    "ds_read_b128 v[96:99], %[ea] offset:0\n\t"
    "v_mfma_f32_16x16x32_bf16 v[68:71], v[236:239], v[104:107], v[68:71]\n\t"
    "ds_read_b128 v[100:103], %[ea] offset:2048\n\t"
    "v_mfma_f32_16x16x32_bf16 v[72:75], v[232:235], v[108:111], v[72:75]\n\t"
    "v_mfma_f32_16x16x32_bf16 v[76:79], v[236:239], v[108:111], v[76:79]\n\t"
    "s_waitcnt lgkmcnt(4)\n\t"
    "v_mfma_f32_16x16x32_bf16 v[64:67], v[240:243], v[112:115], v[64:67]\n\t"
    "ds_read_b128 v[104:107], %[da] offset:0\n\t"
    "v_mad_u32_u24 v39, v39, %[mu], %[ad]\n\t"
    "v_mfma_f32_16x16x32_bf16 v[68:71], v[244:247], v[112:115], v[68:71]\n\t"
    "ds_read_b128 v[108:111], %[da] offset:2048\n\t"
    "v_cmp_ge_i32 vcc, %[lf], v39\n\t"
    "s_cmp_eq_u64 vcc, 0\n\t"
    "s_addc_u32 %[lf], %[lf], 0\n\t"
    "v_mfma_f32_16x16x32_bf16 v[72:75], v[240:243], v[116:119], v[72:75]\n\t"
    "v_mfma_f32_16x16x32_bf16 v[76:79], v[244:247], v[116:119], v[76:79]\n\t"
    "v_cmp_ge_i32 vcc, %[lf], v39\n\t"
    "s_cmp_eq_u64 vcc, 0\n\t"
    "s_addc_u32 %[lf], %[lf], 0\n\t"
    "s_waitcnt lgkmcnt(4)\n\t"
    "v_mfma_f32_16x16x32_bf16 v[80:83], v[248:251], v[120:123], v[64:67]\n\t"
    "ds_read_b128 v[112:115], %[ea] offset:4096\n\t"
    "s_min_i32 %[lf], %[lf], %[cp]\n\t"
    "s_add_u32 %[b], %[b], 1\n\t"
    "v_mfma_f32_16x16x32_bf16 v[84:87], v[252:255], v[120:123], v[68:71]\n\t"
    "ds_read_b128 v[116:119], %[ea] offset:6144\n\t"
    "v_mfma_f32_16x16x32_bf16 v[88:91], v[248:251], v[124:127], v[72:75]\n\t"
    "v_mfma_f32_16x16x32_bf16 v[92:95], v[252:255], v[124:127], v[76:79]\n\t"
    "s_add_u32 s38, s38, %[ab]\n\t"
    "s_addc_u32 s39, s39, 0\n\t"
    "s_xor_b32 %[l0], %[l0], 0x10000\n\t"
    "s_cmp_lt_i32 %[b], %[lf]\n\t"
    "s_cbranch_scc1 1b\n\t"
    "s_waitcnt lgkmcnt(0)\n\t"
    "s_nop 4"
    : "=&{v[80:95]}"(pd), [ea] "+&v"(ea), [da] "+&v"(da), [b] "+&s"(b), [lf] "+&s"(lf), "+{s[36:37]}"(p1), "+{s[38:39]}"(p0), [sa] "+&v"(sa), [l1] "+&s"(l1), [l0] "+&s"(l0)
    : [rw] "v"(rw), [ab] "s"(ab), [cp] "s"(cp), [mu] "s"(mu), [ad] "v"(ad), "{v[128:159]}"(W[0]), "{v[160:191]}"(W[1]), "{v[192:223]}"(W[2]), "{v[224:255]}"(W[3]), [av0] "v"(av[0]), [av1] "v"(av[1]), [av2] "v"(av[2]), [av3] "v"(av[3]), [av4] "v"(av[4]), [av5] "v"(av[5]), [av6] "v"(av[6]), [av7] "v"(av[7])
    : "v39", "v64", "v65", "v66", "v67", "v68", "v69", "v70", "v71", "v72", "v73", "v74", "v75", "v76", "v77", "v78", "v79", "v96", "v97", "v98", "v99", "v100", "v101", "v102", "v103", "v104", "v105", "v106", "v107", "v108", "v109", "v110", "v111", "v112", "v113", "v114", "v115", "v116", "v117", "v118", "v119", "v120", "v121", "v122", "v123", "v124", "v125", "v126", "v127", "vcc", "scc", "m0", "memory");
}
__device__ __forceinline__ void near_st0(const u32x32 (&W)[4], u32x16& pd, unsigned ea, unsigned da, int& b, int& lf, int cp, unsigned sa, unsigned mu, unsigned ad,
    unsigned rw, unsigned ab, const char* p1, const char* p0, unsigned co0, unsigned co1, int po, int pf, unsigned w0, unsigned sl, unsigned dv, const char* db, const char* pg) {
  unsigned st;
  asm volatile("s_waitcnt vmcnt(0)\n\t"
    "s_barrier\n\t"
    "ds_read_b128 v[96:99], %[ea] offset:0\n\t"
    "ds_read_b128 v[100:103], %[ea] offset:2048\n\t"
    "ds_read_b128 v[104:107], %[da] offset:0\n\t"
    "ds_read_b128 v[108:111], %[da] offset:2048\n\t"
    "ds_read_b128 v[112:115], %[ea] offset:4096\n\t"
    "ds_read_b128 v[116:119], %[ea] offset:6144\n\t"
    "s_branch 2f\n\t"
    "1:\n\t"
    "s_waitcnt vmcnt(0)\n\t"
    "s_barrier\n\t"
    "2:\n\t"
    "s_waitcnt lgkmcnt(4)\n\t"
    "v_mfma_f32_16x16x32_bf16 v[64:67], v[128:131], v[96:99], 0\n\t"
    "ds_read_b128 v[120:123], %[da] offset:4096\n\t"
    "v_mfma_f32_16x16x32_bf16 v[68:71], v[132:135], v[96:99], 0\n\t"
    "ds_read_b128 v[124:127], %[da] offset:6144\n\t"
    "v_mfma_f32_16x16x32_bf16 v[72:75], v[128:131], v[100:103], 0\n\t"
    "v_mfma_f32_16x16x32_bf16 v[76:79], v[132:135], v[100:103], 0\n\t"
    "s_cmp_eq_u32 %[w0], 0\n\t"
    "s_cbranch_scc1 5f\n\t"
    "6:\n\t"
    "s_waitcnt lgkmcnt(4)\n\t"
    "v_mfma_f32_16x16x32_bf16 v[64:67], v[136:139], v[104:107], v[64:67]\n\t"
    "ds_read_b128 v[96:99], %[ea] offset:8192\n\t"
    "v_mfma_f32_16x16x32_bf16 v[68:71], v[140:143], v[104:107], v[68:71]\n\t"
    "ds_read_b128 v[100:103], %[ea] offset:10240\n\t"
    "v_mfma_f32_16x16x32_bf16 v[72:75], v[136:139], v[108:111], v[72:75]\n\t"
    "v_mfma_f32_16x16x32_bf16 v[76:79], v[140:143], v[108:111], v[76:79]\n\t"
    "s_waitcnt lgkmcnt(4)\n\t"
    "v_mfma_f32_16x16x32_bf16 v[64:67], v[144:147], v[112:115], v[64:67]\n\t"
    "ds_read_b128 v[104:107], %[da] offset:8192\n\t"
    "v_mfma_f32_16x16x32_bf16 v[68:71], v[148:151], v[112:115], v[68:71]\n\t"
    "ds_read_b128 v[108:111], %[da] offset:10240\n\t"
    "v_mfma_f32_16x16x32_bf16 v[72:75], v[144:147], v[116:119], v[72:75]\n\t"
    "v_mfma_f32_16x16x32_bf16 v[76:79], v[148:151], v[116:119], v[76:79]\n\t"
    "s_barrier\n\t"
    "s_waitcnt lgkmcnt(4)\n\t"
    "v_mfma_f32_16x16x32_bf16 v[64:67], v[152:155], v[120:123], v[64:67]\n\t"
    "ds_read_b128 v[112:115], %[ea] offset:12288\n\t"
    "v_mfma_f32_16x16x32_bf16 v[68:71], v[156:159], v[120:123], v[68:71]\n\t"
    "ds_read_b128 v[116:119], %[ea] offset:14336\n\t"
    "v_mfma_f32_16x16x32_bf16 v[72:75], v[152:155], v[124:127], v[72:75]\n\t"
    "ds_read_b128 v[48:51], %[rw] offset:0\n\t"
    "v_mfma_f32_16x16x32_bf16 v[76:79], v[156:159], v[124:127], v[76:79]\n\t"
    "ds_read_b128 v[52:55], %[rw] offset:1024\n\t"
    "s_waitcnt lgkmcnt(6)\n\t"
    "v_mfma_f32_16x16x32_bf16 v[64:67], v[160:163], v[96:99], v[64:67]\n\t"
    "ds_read_b128 v[120:123], %[da] offset:12288\n\t"
    "v_mfma_f32_16x16x32_bf16 v[68:71], v[164:167], v[96:99], v[68:71]\n\t"
    "ds_read_b128 v[124:127], %[da] offset:14336\n\t"
    "v_mfma_f32_16x16x32_bf16 v[72:75], v[160:163], v[100:103], v[72:75]\n\t"
    "ds_read_b128 v[56:59], %[rw] offset:2048\n\t"
    "v_mfma_f32_16x16x32_bf16 v[76:79], v[164:167], v[100:103], v[76:79]\n\t"
    "ds_read_b128 v[60:63], %[rw] offset:3072\n\t"
    "s_waitcnt lgkmcnt(0)\n\t"
    "v_mfma_f32_16x16x32_bf16 v[64:67], v[168:171], v[104:107], v[64:67]\n\t"
    "ds_read_b128 v[96:99], %[ea] offset:16384\n\t"
    "v_add_f32 v48, v48, v80\n\t"
    "v_add_f32 v49, v49, v81\n\t"
    "v_mfma_f32_16x16x32_bf16 v[68:71], v[172:175], v[104:107], v[68:71]\n\t"
    "ds_read_b128 v[100:103], %[ea] offset:18432\n\t"
    "v_add_f32 v50, v50, v82\n\t"
    "v_mfma_f32_16x16x32_bf16 v[72:75], v[168:171], v[108:111], v[72:75]\n\t"
    "v_add_f32 v51, v51, v83\n\t"
    "v_mfma_f32_16x16x32_bf16 v[76:79], v[172:175], v[108:111], v[76:79]\n\t"
    "v_add_f32 v52, v52, v84\n\t"
    "v_add_f32 v53, v53, v85\n\t"
    "v_mfma_f32_16x16x32_bf16 v[64:67], v[176:179], v[112:115], v[64:67]\n\t"
    "ds_read_b128 v[104:107], %[da] offset:16384\n\t"
    "v_add_f32 v54, v54, v86\n\t"
    "v_mfma_f32_16x16x32_bf16 v[68:71], v[180:183], v[112:115], v[68:71]\n\t"
    "ds_read_b128 v[108:111], %[da] offset:18432\n\t"
    "v_add_f32 v55, v55, v87\n\t"
    "v_mfma_f32_16x16x32_bf16 v[72:75], v[176:179], v[116:119], v[72:75]\n\t"
    "v_add_f32 v56, v56, v88\n\t"
    "v_add_f32 v57, v57, v89\n\t"
    "v_mfma_f32_16x16x32_bf16 v[76:79], v[180:183], v[116:119], v[76:79]\n\t"
    "v_add_f32 v58, v58, v90\n\t"
    "v_mfma_f32_16x16x32_bf16 v[64:67], v[184:187], v[120:123], v[64:67]\n\t"
    "ds_read_b128 v[112:115], %[ea] offset:20480\n\t"
    "v_add_f32 v59, v59, v91\n\t"
    "v_mfma_f32_16x16x32_bf16 v[68:71], v[188:191], v[120:123], v[68:71]\n\t"
    "ds_read_b128 v[116:119], %[ea] offset:22528\n\t"
    "v_add_f32 v60, v60, v92\n\t"
    "v_mfma_f32_16x16x32_bf16 v[72:75], v[184:187], v[124:127], v[72:75]\n\t"
    "v_add_f32 v61, v61, v93\n\t"
    "v_add_f32 v62, v62, v94\n\t"
    "v_mfma_f32_16x16x32_bf16 v[76:79], v[188:191], v[124:127], v[76:79]\n\t"
    "v_add_f32 v63, v63, v95\n\t"
    "s_waitcnt lgkmcnt(4)\n\t"
    "v_mfma_f32_16x16x32_bf16 v[64:67], v[192:195], v[96:99], v[64:67]\n\t"
    "ds_read_b128 v[120:123], %[da] offset:20480\n\t"
    "v_cvt_pk_bf16_f32 v40, v48, v49\n\t"
    "v_mfma_f32_16x16x32_bf16 v[68:71], v[196:199], v[96:99], v[68:71]\n\t"
    "ds_read_b128 v[124:127], %[da] offset:22528\n\t"
    "v_cvt_pk_bf16_f32 v41, v50, v51\n\t"
    "v_cvt_pk_bf16_f32 v42, v52, v53\n\t"
    "v_mfma_f32_16x16x32_bf16 v[72:75], v[192:195], v[100:103], v[72:75]\n\t"
    "v_cvt_pk_bf16_f32 v43, v54, v55\n\t"
    "v_mfma_f32_16x16x32_bf16 v[76:79], v[196:199], v[100:103], v[76:79]\n\t"
    "global_store_dwordx4 %[co0], v[40:43], s[36:37] sc1\n\t"
    "s_waitcnt lgkmcnt(4)\n\t"
    "v_mfma_f32_16x16x32_bf16 v[64:67], v[200:203], v[104:107], v[64:67]\n\t"
    "ds_read_b128 v[96:99], %[ea] offset:24576\n\t"
    "v_cvt_pk_bf16_f32 v44, v56, v57\n\t"
    "v_cvt_pk_bf16_f32 v45, v58, v59\n\t"
    "v_mfma_f32_16x16x32_bf16 v[68:71], v[204:207], v[104:107], v[68:71]\n\t"
    "ds_read_b128 v[100:103], %[ea] offset:26624\n\t"
    "v_cvt_pk_bf16_f32 v46, v60, v61\n\t"
    "v_mfma_f32_16x16x32_bf16 v[72:75], v[200:203], v[108:111], v[72:75]\n\t"
    "v_cvt_pk_bf16_f32 v47, v62, v63\n\t"
    "v_mfma_f32_16x16x32_bf16 v[76:79], v[204:207], v[108:111], v[76:79]\n\t"
    "global_store_dwordx4 %[co1], v[44:47], s[36:37] sc1\n\t"
    "s_waitcnt lgkmcnt(4)\n\t"
    "v_mfma_f32_16x16x32_bf16 v[64:67], v[208:211], v[112:115], v[64:67]\n\t"
    "ds_read_b128 v[104:107], %[da] offset:24576\n\t"
    "v_mfma_f32_16x16x32_bf16 v[68:71], v[212:215], v[112:115], v[68:71]\n\t"
    "ds_read_b128 v[108:111], %[da] offset:26624\n\t"
    "v_mfma_f32_16x16x32_bf16 v[72:75], v[208:211], v[116:119], v[72:75]\n\t"
    "v_mfma_f32_16x16x32_bf16 v[76:79], v[212:215], v[116:119], v[76:79]\n\t"
    "s_waitcnt lgkmcnt(4)\n\t"
    "v_mfma_f32_16x16x32_bf16 v[64:67], v[216:219], v[120:123], v[64:67]\n\t"
    "ds_read_b128 v[112:115], %[ea] offset:28672\n\t"
    "v_mfma_f32_16x16x32_bf16 v[68:71], v[220:223], v[120:123], v[68:71]\n\t"
    "ds_read_b128 v[116:119], %[ea] offset:30720\n\t"
    "v_mfma_f32_16x16x32_bf16 v[72:75], v[216:219], v[124:127], v[72:75]\n\t"
    "v_xor_b32 %[ea], 0x10000, %[ea]\n\t"
    "v_mfma_f32_16x16x32_bf16 v[76:79], v[220:223], v[124:127], v[76:79]\n\t"
    "s_waitcnt vmcnt(2)\n\t"
    "s_barrier\n\t"
    "s_waitcnt lgkmcnt(4)\n\t"
    "v_mfma_f32_16x16x32_bf16 v[64:67], v[224:227], v[96:99], v[64:67]\n\t"
    "ds_read_b32 v39, %[sa]\n\t"
    "ds_read_b128 v[120:123], %[da] offset:28672\n\t"
    "v_mfma_f32_16x16x32_bf16 v[68:71], v[228:231], v[96:99], v[68:71]\n\t"
    "ds_read_b128 v[124:127], %[da] offset:30720\n\t"
    "v_mfma_f32_16x16x32_bf16 v[72:75], v[224:227], v[100:103], v[72:75]\n\t"
    "v_xor_b32 %[da], 0x10000, %[da]\n\t"
    "v_mfma_f32_16x16x32_bf16 v[76:79], v[228:231], v[100:103], v[76:79]\n\t"
    "s_waitcnt lgkmcnt(5)\n\t"
    "v_mfma_f32_16x16x32_bf16 v[64:67], v[232:235], v[104:107], v[64:67]\n\t"
    "ds_read_b128 v[96:99], %[ea] offset:0\n\t"
    "v_mfma_f32_16x16x32_bf16 v[68:71], v[236:239], v[104:107], v[68:71]\n\t"
    "ds_read_b128 v[100:103], %[ea] offset:2048\n\t"
    "v_mfma_f32_16x16x32_bf16 v[72:75], v[232:235], v[108:111], v[72:75]\n\t"
    "v_mfma_f32_16x16x32_bf16 v[76:79], v[236:239], v[108:111], v[76:79]\n\t"
    "s_waitcnt lgkmcnt(4)\n\t"
    "v_mfma_f32_16x16x32_bf16 v[64:67], v[240:243], v[112:115], v[64:67]\n\t"
    "ds_read_b128 v[104:107], %[da] offset:0\n\t"
    "v_mad_u32_u24 v39, v39, %[mu], %[ad]\n\t"
    "v_mfma_f32_16x16x32_bf16 v[68:71], v[244:247], v[112:115], v[68:71]\n\t"
    "ds_read_b128 v[108:111], %[da] offset:2048\n\t"
    "v_cmp_ge_i32 vcc, %[lf], v39\n\t"
    "s_cmp_eq_u64 vcc, 0\n\t"
    "s_addc_u32 %[lf], %[lf], 0\n\t"
    "v_mfma_f32_16x16x32_bf16 v[72:75], v[240:243], v[116:119], v[72:75]\n\t"
    "v_mfma_f32_16x16x32_bf16 v[76:79], v[244:247], v[116:119], v[76:79]\n\t"
    "v_cmp_ge_i32 vcc, %[lf], v39\n\t"
    "s_cmp_eq_u64 vcc, 0\n\t"
    "s_addc_u32 %[lf], %[lf], 0\n\t"
    "s_waitcnt lgkmcnt(4)\n\t"
    "v_mfma_f32_16x16x32_bf16 v[80:83], v[248:251], v[120:123], v[64:67]\n\t"
    "ds_read_b128 v[112:115], %[ea] offset:4096\n\t"
    "s_min_i32 %[lf], %[lf], %[cp]\n\t"
    "s_add_u32 %[b], %[b], 1\n\t"
    "v_mfma_f32_16x16x32_bf16 v[84:87], v[252:255], v[120:123], v[68:71]\n\t"
    "ds_read_b128 v[116:119], %[ea] offset:6144\n\t"
    "v_mfma_f32_16x16x32_bf16 v[88:91], v[248:251], v[124:127], v[72:75]\n\t"
    "v_mfma_f32_16x16x32_bf16 v[92:95], v[252:255], v[124:127], v[76:79]\n\t"
    "s_mov_b64 s[36:37], s[38:39]\n\t"
    "s_add_u32 s38, s38, %[ab]\n\t"
    "s_addc_u32 s39, s39, 0\n\t"
    "s_cmp_lt_i32 %[b], %[lf]\n\t"
    "s_cbranch_scc1 1b\n\t"
    "s_waitcnt lgkmcnt(0)\n\t"
    "s_nop 4\n\t"
    "s_branch 9f\n\t"
    "5:\n\t"
    "s_add_u32 %[st], %[b], %[po]\n\t"
    "s_max_i32 %[st], %[st], %[p0]\n\t"
    "v_mov_b32 v39, %[st]\n\t"
    "v_mov_b32 v38, 0\n\t"
    "s_mov_b64 exec, 1\n\t"
    "global_store_dword v38, v39, %[pg] sc1\n\t"
    "s_mov_b64 exec, -1\n\t"
    "s_mov_b32 %[p0], %[p1]\n\t"
    "s_cmp_ge_i32 %[lf], %[cp]\n\t"
    "s_cbranch_scc1 6b\n\t"
    "s_mov_b32 m0, %[sl]\n\t"
    "s_nop 0\n\t"
    "global_load_lds_dword %[dv], %[db] sc1\n\t"
    "s_branch 6b\n\t"
    "9:"
    : "=&{v[80:95]}"(pd), [ea] "+&v"(ea), [da] "+&v"(da), [b] "+&s"(b), [lf] "+&s"(lf), "+{s[36:37]}"(p1), "+{s[38:39]}"(p0), [sa] "+&v"(sa), [st] "=&s"(st), [sl] "+&s"(sl), [p0] "+&s"(pf)
    : [rw] "v"(rw), [ab] "s"(ab), [cp] "s"(cp), [mu] "s"(mu), [ad] "v"(ad), "{v[128:159]}"(W[0]), "{v[160:191]}"(W[1]), "{v[192:223]}"(W[2]), "{v[224:255]}"(W[3]), [co0] "v"(co0), [w0] "s"(w0), [dv] "v"(dv), [db] "s"(db), [pg] "s"(pg), [po] "s"(po - 1), [p1] "s"(b + po), [co1] "v"(co1)
    : "v38", "v39", "v40", "v41", "v42", "v43", "v44", "v45", "v46", "v47", "v48", "v49", "v50", "v51", "v52", "v53", "v54", "v55", "v56", "v57", "v58", "v59", "v60", "v61", "v62", "v63", "v64", "v65", "v66", "v67", "v68", "v69", "v70", "v71", "v72", "v73", "v74", "v75", "v76", "v77", "v78", "v79", "v96", "v97", "v98", "v99", "v100", "v101", "v102", "v103", "v104", "v105", "v106", "v107", "v108", "v109", "v110", "v111", "v112", "v113", "v114", "v115", "v116", "v117", "v118", "v119", "v120", "v121", "v122", "v123", "v124", "v125", "v126", "v127", "vcc", "scc", "m0", "memory");
}
__device__ __forceinline__ void near_st1(const u32x32 (&W)[4], u32x16& pd, unsigned ea, unsigned da, int& b, int& lf, int cp, unsigned sa, unsigned mu, unsigned ad,
    unsigned rw, unsigned ab, const char* p1, const char* p0, unsigned co0, unsigned co1, int po, int pf, unsigned w0, unsigned sl, unsigned dv, const char* db, const char* pg) {
  unsigned st;
  asm volatile("s_waitcnt vmcnt(0)\n\t"
    "s_barrier\n\t"
    "ds_read_b128 v[96:99], %[ea] offset:0\n\t"
    "ds_read_b128 v[100:103], %[ea] offset:2048\n\t"
    "ds_read_b128 v[104:107], %[da] offset:0\n\t"
    "ds_read_b128 v[108:111], %[da] offset:2048\n\t"
    "ds_read_b128 v[112:115], %[ea] offset:4096\n\t"
    "ds_read_b128 v[116:119], %[ea] offset:6144\n\t"
    "s_branch 2f\n\t"
    "1:\n\t"
    "s_waitcnt vmcnt(0)\n\t"
    "s_barrier\n\t"
    "2:\n\t"
    "s_waitcnt lgkmcnt(4)\n\t"
    "v_mfma_f32_16x16x32_bf16 v[64:67], v[128:131], v[96:99], 0\n\t"
    "ds_read_b128 v[120:123], %[da] offset:4096\n\t"
    "v_mfma_f32_16x16x32_bf16 v[68:71], v[132:135], v[96:99], 0\n\t"
    "ds_read_b128 v[124:127], %[da] offset:6144\n\t"
    "v_mfma_f32_16x16x32_bf16 v[72:75], v[128:131], v[100:103], 0\n\t"
    "v_mfma_f32_16x16x32_bf16 v[76:79], v[132:135], v[100:103], 0\n\t"
    "s_cmp_eq_u32 %[w0], 0\n\t"
    "s_cbranch_scc1 5f\n\t"
    "6:\n\t"
    "s_waitcnt lgkmcnt(4)\n\t"
    "v_mfma_f32_16x16x32_bf16 v[64:67], v[136:139], v[104:107], v[64:67]\n\t"
    "ds_read_b128 v[96:99], %[ea] offset:8192\n\t"
    "v_mfma_f32_16x16x32_bf16 v[68:71], v[140:143], v[104:107], v[68:71]\n\t"
    "ds_read_b128 v[100:103], %[ea] offset:10240\n\t"
    "v_mfma_f32_16x16x32_bf16 v[72:75], v[136:139], v[108:111], v[72:75]\n\t"
    "v_mfma_f32_16x16x32_bf16 v[76:79], v[140:143], v[108:111], v[76:79]\n\t"
    "s_waitcnt lgkmcnt(4)\n\t"
    "v_mfma_f32_16x16x32_bf16 v[64:67], v[144:147], v[112:115], v[64:67]\n\t"
    "ds_read_b128 v[104:107], %[da] offset:8192\n\t"
    "v_mfma_f32_16x16x32_bf16 v[68:71], v[148:151], v[112:115], v[68:71]\n\t"
    "ds_read_b128 v[108:111], %[da] offset:10240\n\t"
    "v_mfma_f32_16x16x32_bf16 v[72:75], v[144:147], v[116:119], v[72:75]\n\t"
    "v_mfma_f32_16x16x32_bf16 v[76:79], v[148:151], v[116:119], v[76:79]\n\t"
    "s_barrier\n\t"
    "s_waitcnt lgkmcnt(4)\n\t"
    "v_mfma_f32_16x16x32_bf16 v[64:67], v[152:155], v[120:123], v[64:67]\n\t"
    "ds_read_b128 v[112:115], %[ea] offset:12288\n\t"
    "v_mfma_f32_16x16x32_bf16 v[68:71], v[156:159], v[120:123], v[68:71]\n\t"
    "ds_read_b128 v[116:119], %[ea] offset:14336\n\t"
    "v_mfma_f32_16x16x32_bf16 v[72:75], v[152:155], v[124:127], v[72:75]\n\t"
    "ds_read_b128 v[48:51], %[rw] offset:0\n\t"
    "v_mfma_f32_16x16x32_bf16 v[76:79], v[156:159], v[124:127], v[76:79]\n\t"
    "ds_read_b128 v[52:55], %[rw] offset:1024\n\t"
    "s_waitcnt lgkmcnt(6)\n\t"
    "v_mfma_f32_16x16x32_bf16 v[64:67], v[160:163], v[96:99], v[64:67]\n\t"
    "ds_read_b128 v[120:123], %[da] offset:12288\n\t"
    "v_mfma_f32_16x16x32_bf16 v[68:71], v[164:167], v[96:99], v[68:71]\n\t"
    "ds_read_b128 v[124:127], %[da] offset:14336\n\t"
    "v_mfma_f32_16x16x32_bf16 v[72:75], v[160:163], v[100:103], v[72:75]\n\t"
    "ds_read_b128 v[56:59], %[rw] offset:2048\n\t"
    "v_mfma_f32_16x16x32_bf16 v[76:79], v[164:167], v[100:103], v[76:79]\n\t"
    "ds_read_b128 v[60:63], %[rw] offset:3072\n\t"
    "s_waitcnt lgkmcnt(0)\n\t"
    "v_mfma_f32_16x16x32_bf16 v[64:67], v[168:171], v[104:107], v[64:67]\n\t"
    "ds_read_b128 v[96:99], %[ea] offset:16384\n\t"
    "v_add_f32 v48, v48, v80\n\t"
    "v_add_f32 v49, v49, v81\n\t"
    "v_add_f32 v50, v50, v82\n\t"
    "v_mfma_f32_16x16x32_bf16 v[68:71], v[172:175], v[104:107], v[68:71]\n\t"
    "ds_read_b128 v[100:103], %[ea] offset:18432\n\t"
    "v_add_f32 v51, v51, v83\n\t"
    "v_add_f32 v52, v52, v84\n\t"
    "v_add_f32 v53, v53, v85\n\t"
    "v_mfma_f32_16x16x32_bf16 v[72:75], v[168:171], v[108:111], v[72:75]\n\t"
    "v_add_f32 v54, v54, v86\n\t"
    "v_add_f32 v55, v55, v87\n\t"
    "v_add_f32 v56, v56, v88\n\t"
    "v_mfma_f32_16x16x32_bf16 v[76:79], v[172:175], v[108:111], v[76:79]\n\t"
    "v_add_f32 v57, v57, v89\n\t"
    "v_add_f32 v58, v58, v90\n\t"
    "v_add_f32 v59, v59, v91\n\t"
    "v_mfma_f32_16x16x32_bf16 v[64:67], v[176:179], v[112:115], v[64:67]\n\t"
    "ds_read_b128 v[104:107], %[da] offset:16384\n\t"
    "v_add_f32 v60, v60, v92\n\t"
    "v_add_f32 v61, v61, v93\n\t"
    "v_add_f32 v62, v62, v94\n\t"
    "v_mfma_f32_16x16x32_bf16 v[68:71], v[180:183], v[112:115], v[68:71]\n\t"
    "ds_read_b128 v[108:111], %[da] offset:18432\n\t"
    "v_add_f32 v63, v63, v95\n\t"
    "v_mul_f32 v40, %[nl], v48\n\t"
    "v_mul_f32 v41, %[nl], v49\n\t"
    "v_mfma_f32_16x16x32_bf16 v[72:75], v[176:179], v[116:119], v[72:75]\n\t"
    "v_mul_f32 v42, %[nl], v50\n\t"
    "v_mul_f32 v43, %[nl], v51\n\t"
    "v_mul_f32 v44, %[nl], v56\n\t"
    "v_mfma_f32_16x16x32_bf16 v[76:79], v[180:183], v[116:119], v[76:79]\n\t"
    "v_mul_f32 v45, %[nl], v57\n\t"
    "v_mul_f32 v46, %[nl], v58\n\t"
    "v_mul_f32 v47, %[nl], v59\n\t"
    "v_mfma_f32_16x16x32_bf16 v[64:67], v[184:187], v[120:123], v[64:67]\n\t"
    "ds_read_b128 v[112:115], %[ea] offset:20480\n\t"
    "v_exp_f32 v40, v40\n\t"
    "v_exp_f32 v41, v41\n\t"
    "v_exp_f32 v42, v42\n\t"
    "v_mfma_f32_16x16x32_bf16 v[68:71], v[188:191], v[120:123], v[68:71]\n\t"
    "ds_read_b128 v[116:119], %[ea] offset:22528\n\t"
    "v_exp_f32 v43, v43\n\t"
    "v_exp_f32 v44, v44\n\t"
    "v_exp_f32 v45, v45\n\t"
    "v_mfma_f32_16x16x32_bf16 v[72:75], v[184:187], v[124:127], v[72:75]\n\t"
    "v_exp_f32 v46, v46\n\t"
    "v_exp_f32 v47, v47\n\t"
    "v_add_f32 v40, 1.0, v40\n\t"
    "v_mfma_f32_16x16x32_bf16 v[76:79], v[188:191], v[124:127], v[76:79]\n\t"
    "v_add_f32 v41, 1.0, v41\n\t"
    "v_add_f32 v42, 1.0, v42\n\t"
    "v_add_f32 v43, 1.0, v43\n\t"
    "s_waitcnt lgkmcnt(4)\n\t"
    "v_mfma_f32_16x16x32_bf16 v[64:67], v[192:195], v[96:99], v[64:67]\n\t"
    "ds_read_b128 v[120:123], %[da] offset:20480\n\t"
    "v_add_f32 v44, 1.0, v44\n\t"
    "v_add_f32 v45, 1.0, v45\n\t"
    "v_add_f32 v46, 1.0, v46\n\t"
    "v_mfma_f32_16x16x32_bf16 v[68:71], v[196:199], v[96:99], v[68:71]\n\t"
    "ds_read_b128 v[124:127], %[da] offset:22528\n\t"
    "v_add_f32 v47, 1.0, v47\n\t"
    "v_rcp_f32 v40, v40\n\t"
    "v_rcp_f32 v41, v41\n\t"
    "v_mfma_f32_16x16x32_bf16 v[72:75], v[192:195], v[100:103], v[72:75]\n\t"
    "v_rcp_f32 v42, v42\n\t"
    "v_rcp_f32 v43, v43\n\t"
    "v_rcp_f32 v44, v44\n\t"
    "v_mfma_f32_16x16x32_bf16 v[76:79], v[196:199], v[100:103], v[76:79]\n\t"
    "v_rcp_f32 v45, v45\n\t"
    "v_rcp_f32 v46, v46\n\t"
    "v_rcp_f32 v47, v47\n\t"
    "s_waitcnt lgkmcnt(4)\n\t"
    "v_mfma_f32_16x16x32_bf16 v[64:67], v[200:203], v[104:107], v[64:67]\n\t"
    "ds_read_b128 v[96:99], %[ea] offset:24576\n\t"
    "v_mul_f32 v40, v48, v40\n\t"
    "v_mul_f32 v41, v49, v41\n\t"
    "v_mul_f32 v42, v50, v42\n\t"
    "v_mfma_f32_16x16x32_bf16 v[68:71], v[204:207], v[104:107], v[68:71]\n\t"
    "ds_read_b128 v[100:103], %[ea] offset:26624\n\t"
    "v_mul_f32 v43, v51, v43\n\t"
    "v_mul_f32 v44, v56, v44\n\t"
    "v_mul_f32 v45, v57, v45\n\t"
    "v_mfma_f32_16x16x32_bf16 v[72:75], v[200:203], v[108:111], v[72:75]\n\t"
    "v_mul_f32 v46, v58, v46\n\t"
    "v_mul_f32 v47, v59, v47\n\t"
    "v_mul_f32 v40, v40, v52\n\t"
    "v_mfma_f32_16x16x32_bf16 v[76:79], v[204:207], v[108:111], v[76:79]\n\t"
    "v_mul_f32 v41, v41, v53\n\t"
    "v_mul_f32 v42, v42, v54\n\t"
    "v_mul_f32 v43, v43, v55\n\t"
    "s_waitcnt lgkmcnt(4)\n\t"
    "v_mfma_f32_16x16x32_bf16 v[64:67], v[208:211], v[112:115], v[64:67]\n\t"
    "ds_read_b128 v[104:107], %[da] offset:24576\n\t"
    "v_mul_f32 v44, v44, v60\n\t"
    "v_mul_f32 v45, v45, v61\n\t"
    "v_mul_f32 v46, v46, v62\n\t"
    "v_mfma_f32_16x16x32_bf16 v[68:71], v[212:215], v[112:115], v[68:71]\n\t"
    "ds_read_b128 v[108:111], %[da] offset:26624\n\t"
    "v_mul_f32 v47, v47, v63\n\t"
    "v_cvt_pk_bf16_f32 v48, v40, v41\n\t"
    "v_cvt_pk_bf16_f32 v49, v42, v43\n\t"
    "v_mfma_f32_16x16x32_bf16 v[72:75], v[208:211], v[116:119], v[72:75]\n\t"
    "v_cvt_pk_bf16_f32 v50, v44, v45\n\t"
    "v_cvt_pk_bf16_f32 v51, v46, v47\n\t"
    "s_nop 1\n\t"
    "v_mfma_f32_16x16x32_bf16 v[76:79], v[212:215], v[116:119], v[76:79]\n\t"
    "v_permlane16_swap_b32 v48, v50\n\t"
    "v_permlane16_swap_b32 v49, v51\n\t"
    "global_store_dwordx4 %[co0], v[48:51], s[36:37] sc1\n\t"
    "s_waitcnt lgkmcnt(4)\n\t"
    "v_mfma_f32_16x16x32_bf16 v[64:67], v[216:219], v[120:123], v[64:67]\n\t"
    "ds_read_b128 v[112:115], %[ea] offset:28672\n\t"
    "v_mfma_f32_16x16x32_bf16 v[68:71], v[220:223], v[120:123], v[68:71]\n\t"
    "ds_read_b128 v[116:119], %[ea] offset:30720\n\t"
    "v_mfma_f32_16x16x32_bf16 v[72:75], v[216:219], v[124:127], v[72:75]\n\t"
    "v_xor_b32 %[ea], 0x10000, %[ea]\n\t"
    "v_mfma_f32_16x16x32_bf16 v[76:79], v[220:223], v[124:127], v[76:79]\n\t"
    "s_waitcnt vmcnt(1)\n\t"
    "s_barrier\n\t"
    "s_waitcnt lgkmcnt(4)\n\t"
    "v_mfma_f32_16x16x32_bf16 v[64:67], v[224:227], v[96:99], v[64:67]\n\t"
    "ds_read_b32 v39, %[sa]\n\t"
    "ds_read_b128 v[120:123], %[da] offset:28672\n\t"
    "v_mfma_f32_16x16x32_bf16 v[68:71], v[228:231], v[96:99], v[68:71]\n\t"
    "ds_read_b128 v[124:127], %[da] offset:30720\n\t"
    "v_mfma_f32_16x16x32_bf16 v[72:75], v[224:227], v[100:103], v[72:75]\n\t"
    "v_xor_b32 %[da], 0x10000, %[da]\n\t"
    "v_mfma_f32_16x16x32_bf16 v[76:79], v[228:231], v[100:103], v[76:79]\n\t"
    "s_waitcnt lgkmcnt(5)\n\t"
    "v_mfma_f32_16x16x32_bf16 v[64:67], v[232:235], v[104:107], v[64:67]\n\t"
    "ds_read_b128 v[96:99], %[ea] offset:0\n\t"
    "v_mfma_f32_16x16x32_bf16 v[68:71], v[236:239], v[104:107], v[68:71]\n\t"
    "ds_read_b128 v[100:103], %[ea] offset:2048\n\t"
    "v_mfma_f32_16x16x32_bf16 v[72:75], v[232:235], v[108:111], v[72:75]\n\t"
    "v_mfma_f32_16x16x32_bf16 v[76:79], v[236:239], v[108:111], v[76:79]\n\t"
    "s_waitcnt lgkmcnt(4)\n\t"
    "v_mfma_f32_16x16x32_bf16 v[64:67], v[240:243], v[112:115], v[64:67]\n\t"
    "ds_read_b128 v[104:107], %[da] offset:0\n\t"
    "v_mad_u32_u24 v39, v39, %[mu], %[ad]\n\t"
    "v_mfma_f32_16x16x32_bf16 v[68:71], v[244:247], v[112:115], v[68:71]\n\t"
    "ds_read_b128 v[108:111], %[da] offset:2048\n\t"
    "v_cmp_ge_i32 vcc, %[lf], v39\n\t"
    "s_cmp_eq_u64 vcc, 0\n\t"
    "s_addc_u32 %[lf], %[lf], 0\n\t"
    "v_mfma_f32_16x16x32_bf16 v[72:75], v[240:243], v[116:119], v[72:75]\n\t"
    "v_mfma_f32_16x16x32_bf16 v[76:79], v[244:247], v[116:119], v[76:79]\n\t"
    "v_cmp_ge_i32 vcc, %[lf], v39\n\t"
    "s_cmp_eq_u64 vcc, 0\n\t"
    "s_addc_u32 %[lf], %[lf], 0\n\t"
    "s_waitcnt lgkmcnt(4)\n\t"
    "v_mfma_f32_16x16x32_bf16 v[80:83], v[248:251], v[120:123], v[64:67]\n\t"
    "ds_read_b128 v[112:115], %[ea] offset:4096\n\t"
    "s_min_i32 %[lf], %[lf], %[cp]\n\t"
    "s_add_u32 %[b], %[b], 1\n\t"
    "v_mfma_f32_16x16x32_bf16 v[84:87], v[252:255], v[120:123], v[68:71]\n\t"
    "ds_read_b128 v[116:119], %[ea] offset:6144\n\t"
    "v_mfma_f32_16x16x32_bf16 v[88:91], v[248:251], v[124:127], v[72:75]\n\t"
    "v_mfma_f32_16x16x32_bf16 v[92:95], v[252:255], v[124:127], v[76:79]\n\t"
    "s_mov_b64 s[36:37], s[38:39]\n\t"
    "s_add_u32 s38, s38, %[ab]\n\t"
    "s_addc_u32 s39, s39, 0\n\t"
    "s_cmp_lt_i32 %[b], %[lf]\n\t"
    "s_cbranch_scc1 1b\n\t"
    "s_waitcnt lgkmcnt(0)\n\t"
    "s_nop 4\n\t"
    "s_branch 9f\n\t"
    "5:\n\t"
    "s_add_u32 %[st], %[b], %[po]\n\t"
    "s_max_i32 %[st], %[st], %[p0]\n\t"
    "v_mov_b32 v39, %[st]\n\t"
    "v_mov_b32 v38, 0\n\t"
    "s_mov_b64 exec, 1\n\t"
    "global_store_dword v38, v39, %[pg] sc1\n\t"
    "s_mov_b64 exec, -1\n\t"
    "s_mov_b32 %[p0], %[p1]\n\t"
    "s_cmp_ge_i32 %[lf], %[cp]\n\t"
    "s_cbranch_scc1 6b\n\t"
    "s_mov_b32 m0, %[sl]\n\t"
    "s_nop 0\n\t"
    "global_load_lds_dword %[dv], %[db] sc1\n\t"
    "s_branch 6b\n\t"
    "9:"
    : "=&{v[80:95]}"(pd), [ea] "+&v"(ea), [da] "+&v"(da), [b] "+&s"(b), [lf] "+&s"(lf), "+{s[36:37]}"(p1), "+{s[38:39]}"(p0), [sa] "+&v"(sa), [st] "=&s"(st), [sl] "+&s"(sl), [p0] "+&s"(pf)
    : [rw] "v"(rw), [ab] "s"(ab), [cp] "s"(cp), [mu] "s"(mu), [ad] "v"(ad), "{v[128:159]}"(W[0]), "{v[160:191]}"(W[1]), "{v[192:223]}"(W[2]), "{v[224:255]}"(W[3]), [co0] "v"(co0), [w0] "s"(w0), [dv] "v"(dv), [db] "s"(db), [pg] "s"(pg), [po] "s"(po - 1), [p1] "s"(b + po), [nl] "s"(-LOG2E)
    : "v38", "v39", "v40", "v41", "v42", "v43", "v44", "v45", "v46", "v47", "v48", "v49", "v50", "v51", "v52", "v53", "v54", "v55", "v56", "v57", "v58", "v59", "v60", "v61", "v62", "v63", "v64", "v65", "v66", "v67", "v68", "v69", "v70", "v71", "v72", "v73", "v74", "v75", "v76", "v77", "v78", "v79", "v96", "v97", "v98", "v99", "v100", "v101", "v102", "v103", "v104", "v105", "v106", "v107", "v108", "v109", "v110", "v111", "v112", "v113", "v114", "v115", "v116", "v117", "v118", "v119", "v120", "v121", "v122", "v123", "v124", "v125", "v126", "v127", "vcc", "scc", "m0", "memory");
}
__device__ __forceinline__ void near_st2(const u32x32 (&W)[4], u32x16& pd, unsigned ea, unsigned da, int& b, int& lf, int cp, unsigned sa, unsigned mu, unsigned ad,
    unsigned rw, unsigned ab, const char* p1, const char* p0, unsigned co0, unsigned co1, int po, int pf, unsigned w0, unsigned sl, unsigned dv, const char* db, const char* pg) {
  unsigned st;
  asm volatile("s_waitcnt vmcnt(0)\n\t"
    "s_barrier\n\t"
    "ds_read_b128 v[96:99], %[ea] offset:0\n\t"
    "ds_read_b128 v[100:103], %[ea] offset:2048\n\t"
    "ds_read_b128 v[104:107], %[da] offset:0\n\t"
    "ds_read_b128 v[108:111], %[da] offset:2048\n\t"
    "ds_read_b128 v[112:115], %[ea] offset:4096\n\t"
    "ds_read_b128 v[116:119], %[ea] offset:6144\n\t"
    "s_branch 2f\n\t"
    "1:\n\t"
    "s_waitcnt vmcnt(0)\n\t"
    "s_barrier\n\t"
    "2:\n\t"
    "s_waitcnt lgkmcnt(4)\n\t"
    "v_mfma_f32_16x16x32_bf16 v[64:67], v[128:131], v[96:99], 0\n\t"
    "ds_read_b128 v[120:123], %[da] offset:4096\n\t"
    "v_mfma_f32_16x16x32_bf16 v[68:71], v[132:135], v[96:99], 0\n\t"
    "ds_read_b128 v[124:127], %[da] offset:6144\n\t"
    "v_mfma_f32_16x16x32_bf16 v[72:75], v[128:131], v[100:103], 0\n\t"
    "v_mfma_f32_16x16x32_bf16 v[76:79], v[132:135], v[100:103], 0\n\t"
    "s_cmp_eq_u32 %[w0], 0\n\t"
    "s_cbranch_scc1 5f\n\t"
    "6:\n\t"
    "s_waitcnt lgkmcnt(4)\n\t"
    "v_mfma_f32_16x16x32_bf16 v[64:67], v[136:139], v[104:107], v[64:67]\n\t"
    "ds_read_b128 v[96:99], %[ea] offset:8192\n\t"
    "v_mfma_f32_16x16x32_bf16 v[68:71], v[140:143], v[104:107], v[68:71]\n\t"
    "ds_read_b128 v[100:103], %[ea] offset:10240\n\t"
    "v_mfma_f32_16x16x32_bf16 v[72:75], v[136:139], v[108:111], v[72:75]\n\t"
    "v_mfma_f32_16x16x32_bf16 v[76:79], v[140:143], v[108:111], v[76:79]\n\t"
    "s_waitcnt lgkmcnt(4)\n\t"
    "v_mfma_f32_16x16x32_bf16 v[64:67], v[144:147], v[112:115], v[64:67]\n\t"
    "ds_read_b128 v[104:107], %[da] offset:8192\n\t"
    "v_mfma_f32_16x16x32_bf16 v[68:71], v[148:151], v[112:115], v[68:71]\n\t"
    "ds_read_b128 v[108:111], %[da] offset:10240\n\t"
    "v_mfma_f32_16x16x32_bf16 v[72:75], v[144:147], v[116:119], v[72:75]\n\t"
    "v_mfma_f32_16x16x32_bf16 v[76:79], v[148:151], v[116:119], v[76:79]\n\t"
    "s_barrier\n\t"
    "s_waitcnt lgkmcnt(4)\n\t"
    "v_mfma_f32_16x16x32_bf16 v[64:67], v[152:155], v[120:123], v[64:67]\n\t"
    "ds_read_b128 v[112:115], %[ea] offset:12288\n\t"
    "v_mfma_f32_16x16x32_bf16 v[68:71], v[156:159], v[120:123], v[68:71]\n\t"
    "ds_read_b128 v[116:119], %[ea] offset:14336\n\t"
    "v_mfma_f32_16x16x32_bf16 v[72:75], v[152:155], v[124:127], v[72:75]\n\t"
    "ds_read_b128 v[48:51], %[rw] offset:0\n\t"
    "v_mfma_f32_16x16x32_bf16 v[76:79], v[156:159], v[124:127], v[76:79]\n\t"
    "ds_read_b128 v[52:55], %[rw] offset:1024\n\t"
    "s_waitcnt lgkmcnt(6)\n\t"
    "v_mfma_f32_16x16x32_bf16 v[64:67], v[160:163], v[96:99], v[64:67]\n\t"
    "ds_read_b128 v[120:123], %[da] offset:12288\n\t"
    "v_mfma_f32_16x16x32_bf16 v[68:71], v[164:167], v[96:99], v[68:71]\n\t"
    "ds_read_b128 v[124:127], %[da] offset:14336\n\t"
    "v_mfma_f32_16x16x32_bf16 v[72:75], v[160:163], v[100:103], v[72:75]\n\t"
    "ds_read_b128 v[56:59], %[rw] offset:2048\n\t"
    "v_mfma_f32_16x16x32_bf16 v[76:79], v[164:167], v[100:103], v[76:79]\n\t"
    "ds_read_b128 v[60:63], %[rw] offset:3072\n\t"
    "s_waitcnt lgkmcnt(0)\n\t"
    "v_mfma_f32_16x16x32_bf16 v[64:67], v[168:171], v[104:107], v[64:67]\n\t"
    "ds_read_b128 v[96:99], %[ea] offset:16384\n\t"
    "v_cvt_pk_bf16_f32 v40, v80, v81\n\t"
    "v_mfma_f32_16x16x32_bf16 v[68:71], v[172:175], v[104:107], v[68:71]\n\t"
    "ds_read_b128 v[100:103], %[ea] offset:18432\n\t"
    "v_cvt_pk_bf16_f32 v41, v82, v83\n\t"
    "v_mfma_f32_16x16x32_bf16 v[72:75], v[168:171], v[108:111], v[72:75]\n\t"
    "v_cvt_pk_bf16_f32 v42, v84, v85\n\t"
    "v_mfma_f32_16x16x32_bf16 v[76:79], v[172:175], v[108:111], v[76:79]\n\t"
    "v_cvt_pk_bf16_f32 v43, v86, v87\n\t"
    "v_mfma_f32_16x16x32_bf16 v[64:67], v[176:179], v[112:115], v[64:67]\n\t"
    "ds_read_b128 v[104:107], %[da] offset:16384\n\t"
    "global_store_dwordx4 %[co0], v[40:43], s[36:37] sc1\n\t"
    "v_mfma_f32_16x16x32_bf16 v[68:71], v[180:183], v[112:115], v[68:71]\n\t"
    "ds_read_b128 v[108:111], %[da] offset:18432\n\t"
    "v_cvt_pk_bf16_f32 v44, v88, v89\n\t"
    "v_mfma_f32_16x16x32_bf16 v[72:75], v[176:179], v[116:119], v[72:75]\n\t"
    "v_cvt_pk_bf16_f32 v45, v90, v91\n\t"
    "v_mfma_f32_16x16x32_bf16 v[76:79], v[180:183], v[116:119], v[76:79]\n\t"
    "v_cvt_pk_bf16_f32 v46, v92, v93\n\t"
    "v_mfma_f32_16x16x32_bf16 v[64:67], v[184:187], v[120:123], v[64:67]\n\t"
    "ds_read_b128 v[112:115], %[ea] offset:20480\n\t"
    "v_cvt_pk_bf16_f32 v47, v94, v95\n\t"
    "v_mfma_f32_16x16x32_bf16 v[68:71], v[188:191], v[120:123], v[68:71]\n\t"
    "ds_read_b128 v[116:119], %[ea] offset:22528\n\t"
    "global_store_dwordx4 %[co1], v[44:47], s[36:37] sc1\n\t"
    "v_mfma_f32_16x16x32_bf16 v[72:75], v[184:187], v[124:127], v[72:75]\n\t"
    "v_cvt_pk_bf16_f32 v48, v48, v49\n\t"
    "v_mfma_f32_16x16x32_bf16 v[76:79], v[188:191], v[124:127], v[76:79]\n\t"
    "v_cvt_pk_bf16_f32 v49, v50, v51\n\t"
    "s_waitcnt lgkmcnt(4)\n\t"
    "v_mfma_f32_16x16x32_bf16 v[64:67], v[192:195], v[96:99], v[64:67]\n\t"
    "ds_read_b128 v[120:123], %[da] offset:20480\n\t"
    "v_cvt_pk_bf16_f32 v50, v52, v53\n\t"
    "v_mfma_f32_16x16x32_bf16 v[68:71], v[196:199], v[96:99], v[68:71]\n\t"
    "ds_read_b128 v[124:127], %[da] offset:22528\n\t"
    "v_cvt_pk_bf16_f32 v51, v54, v55\n\t"
    "v_mfma_f32_16x16x32_bf16 v[72:75], v[192:195], v[100:103], v[72:75]\n\t"
    "global_store_dwordx4 %[co0], v[48:51], s[36:37] offset:2048 sc1\n\t"
    "v_mfma_f32_16x16x32_bf16 v[76:79], v[196:199], v[100:103], v[76:79]\n\t"
    "v_cvt_pk_bf16_f32 v56, v56, v57\n\t"
    "s_waitcnt lgkmcnt(4)\n\t"
    "v_mfma_f32_16x16x32_bf16 v[64:67], v[200:203], v[104:107], v[64:67]\n\t"
    "ds_read_b128 v[96:99], %[ea] offset:24576\n\t"
    "v_cvt_pk_bf16_f32 v57, v58, v59\n\t"
    "v_mfma_f32_16x16x32_bf16 v[68:71], v[204:207], v[104:107], v[68:71]\n\t"
    "ds_read_b128 v[100:103], %[ea] offset:26624\n\t"
    "v_cvt_pk_bf16_f32 v58, v60, v61\n\t"
    "v_mfma_f32_16x16x32_bf16 v[72:75], v[200:203], v[108:111], v[72:75]\n\t"
    "v_cvt_pk_bf16_f32 v59, v62, v63\n\t"
    "v_mfma_f32_16x16x32_bf16 v[76:79], v[204:207], v[108:111], v[76:79]\n\t"
    "global_store_dwordx4 %[co1], v[56:59], s[36:37] offset:2048 sc1\n\t"
    "s_waitcnt lgkmcnt(4)\n\t"
    "v_mfma_f32_16x16x32_bf16 v[64:67], v[208:211], v[112:115], v[64:67]\n\t"
    "ds_read_b128 v[104:107], %[da] offset:24576\n\t"
    "v_mfma_f32_16x16x32_bf16 v[68:71], v[212:215], v[112:115], v[68:71]\n\t"
    "ds_read_b128 v[108:111], %[da] offset:26624\n\t"
    "v_mfma_f32_16x16x32_bf16 v[72:75], v[208:211], v[116:119], v[72:75]\n\t"
    "v_mfma_f32_16x16x32_bf16 v[76:79], v[212:215], v[116:119], v[76:79]\n\t"
    "s_waitcnt lgkmcnt(4)\n\t"
    "v_mfma_f32_16x16x32_bf16 v[64:67], v[216:219], v[120:123], v[64:67]\n\t"
    "ds_read_b128 v[112:115], %[ea] offset:28672\n\t"
    "v_mfma_f32_16x16x32_bf16 v[68:71], v[220:223], v[120:123], v[68:71]\n\t"
    "ds_read_b128 v[116:119], %[ea] offset:30720\n\t"
    "v_mfma_f32_16x16x32_bf16 v[72:75], v[216:219], v[124:127], v[72:75]\n\t"
    "v_xor_b32 %[ea], 0x10000, %[ea]\n\t"
    "v_mfma_f32_16x16x32_bf16 v[76:79], v[220:223], v[124:127], v[76:79]\n\t"
    "s_waitcnt vmcnt(4)\n\t"
    "s_barrier\n\t"
    "s_waitcnt lgkmcnt(4)\n\t"
    "v_mfma_f32_16x16x32_bf16 v[64:67], v[224:227], v[96:99], v[64:67]\n\t"
    "ds_read_b32 v39, %[sa]\n\t"
    "ds_read_b128 v[120:123], %[da] offset:28672\n\t"
    "v_mfma_f32_16x16x32_bf16 v[68:71], v[228:231], v[96:99], v[68:71]\n\t"
    "ds_read_b128 v[124:127], %[da] offset:30720\n\t"
    "v_mfma_f32_16x16x32_bf16 v[72:75], v[224:227], v[100:103], v[72:75]\n\t"
    "v_xor_b32 %[da], 0x10000, %[da]\n\t"
    "v_mfma_f32_16x16x32_bf16 v[76:79], v[228:231], v[100:103], v[76:79]\n\t"
    "s_waitcnt lgkmcnt(5)\n\t"
    "v_mfma_f32_16x16x32_bf16 v[64:67], v[232:235], v[104:107], v[64:67]\n\t"
    "ds_read_b128 v[96:99], %[ea] offset:0\n\t"
    "v_mfma_f32_16x16x32_bf16 v[68:71], v[236:239], v[104:107], v[68:71]\n\t"
    "ds_read_b128 v[100:103], %[ea] offset:2048\n\t"
    "v_mfma_f32_16x16x32_bf16 v[72:75], v[232:235], v[108:111], v[72:75]\n\t"
    "v_mfma_f32_16x16x32_bf16 v[76:79], v[236:239], v[108:111], v[76:79]\n\t"
    "s_waitcnt lgkmcnt(4)\n\t"
    "v_mfma_f32_16x16x32_bf16 v[64:67], v[240:243], v[112:115], v[64:67]\n\t"
    "ds_read_b128 v[104:107], %[da] offset:0\n\t"
    "v_mad_u32_u24 v39, v39, %[mu], %[ad]\n\t"
    "v_mfma_f32_16x16x32_bf16 v[68:71], v[244:247], v[112:115], v[68:71]\n\t"
    "ds_read_b128 v[108:111], %[da] offset:2048\n\t"
    "v_cmp_ge_i32 vcc, %[lf], v39\n\t"
    "s_cmp_eq_u64 vcc, 0\n\t"
    "s_addc_u32 %[lf], %[lf], 0\n\t"
    "v_mfma_f32_16x16x32_bf16 v[72:75], v[240:243], v[116:119], v[72:75]\n\t"
    "v_mfma_f32_16x16x32_bf16 v[76:79], v[244:247], v[116:119], v[76:79]\n\t"
    "v_cmp_ge_i32 vcc, %[lf], v39\n\t"
    "s_cmp_eq_u64 vcc, 0\n\t"
    "s_addc_u32 %[lf], %[lf], 0\n\t"
    "s_waitcnt lgkmcnt(4)\n\t"
    "v_mfma_f32_16x16x32_bf16 v[80:83], v[248:251], v[120:123], v[64:67]\n\t"
    "ds_read_b128 v[112:115], %[ea] offset:4096\n\t"
    "s_min_i32 %[lf], %[lf], %[cp]\n\t"
    "s_add_u32 %[b], %[b], 1\n\t"
    "v_mfma_f32_16x16x32_bf16 v[84:87], v[252:255], v[120:123], v[68:71]\n\t"
    "ds_read_b128 v[116:119], %[ea] offset:6144\n\t"
    "v_mfma_f32_16x16x32_bf16 v[88:91], v[248:251], v[124:127], v[72:75]\n\t"
    "v_mfma_f32_16x16x32_bf16 v[92:95], v[252:255], v[124:127], v[76:79]\n\t"
    "s_mov_b64 s[36:37], s[38:39]\n\t"
    "s_add_u32 s38, s38, %[ab]\n\t"
    "s_addc_u32 s39, s39, 0\n\t"
    "s_cmp_lt_i32 %[b], %[lf]\n\t"
    "s_cbranch_scc1 1b\n\t"
    "s_waitcnt lgkmcnt(0)\n\t"
    "s_nop 4\n\t"
    "s_branch 9f\n\t"
    "5:\n\t"
    "s_add_u32 %[st], %[b], %[po]\n\t"
    "s_max_i32 %[st], %[st], %[p0]\n\t"
    "v_mov_b32 v39, %[st]\n\t"
    "v_mov_b32 v38, 0\n\t"
    "s_mov_b64 exec, 1\n\t"
    "global_store_dword v38, v39, %[pg] sc1\n\t"
    "s_mov_b64 exec, -1\n\t"
    "s_mov_b32 %[p0], %[p1]\n\t"
    "s_cmp_ge_i32 %[lf], %[cp]\n\t"
    "s_cbranch_scc1 6b\n\t"
    "s_mov_b32 m0, %[sl]\n\t"
    "s_nop 0\n\t"
    "global_load_lds_dword %[dv], %[db] sc1\n\t"
    "s_branch 6b\n\t"
    "9:"
    : "=&{v[80:95]}"(pd), [ea] "+&v"(ea), [da] "+&v"(da), [b] "+&s"(b), [lf] "+&s"(lf), "+{s[36:37]}"(p1), "+{s[38:39]}"(p0), [sa] "+&v"(sa), [st] "=&s"(st), [sl] "+&s"(sl), [p0] "+&s"(pf)
    : [rw] "v"(rw), [ab] "s"(ab), [cp] "s"(cp), [mu] "s"(mu), [ad] "v"(ad), "{v[128:159]}"(W[0]), "{v[160:191]}"(W[1]), "{v[192:223]}"(W[2]), "{v[224:255]}"(W[3]), [co0] "v"(co0), [w0] "s"(w0), [dv] "v"(dv), [db] "s"(db), [pg] "s"(pg), [po] "s"(po - 1), [p1] "s"(b + po), [co1] "v"(co1)
    : "v38", "v39", "v40", "v41", "v42", "v43", "v44", "v45", "v46", "v47", "v48", "v49", "v50", "v51", "v52", "v53", "v54", "v55", "v56", "v57", "v58", "v59", "v60", "v61", "v62", "v63", "v64", "v65", "v66", "v67", "v68", "v69", "v70", "v71", "v72", "v73", "v74", "v75", "v76", "v77", "v78", "v79", "v96", "v97", "v98", "v99", "v100", "v101", "v102", "v103", "v104", "v105", "v106", "v107", "v108", "v109", "v110", "v111", "v112", "v113", "v114", "v115", "v116", "v117", "v118", "v119", "v120", "v121", "v122", "v123", "v124", "v125", "v126", "v127", "vcc", "scc", "m0", "memory");
}
__device__ __forceinline__ void lean_ld0(const u32x32 (&W)[4], u32x16& pd, unsigned ea, unsigned da, unsigned x, unsigned lb, const char* s0, const char* s1, const unsigned (&av)[8], unsigned sa, unsigned mu, unsigned ad, int th, unsigned s2, unsigned dv, const char* db) {
  asm volatile("s_setprio 2\n\t"
    "ds_read_b128 v[96:99], %[ea] offset:0\n\t"
    "ds_read_b128 v[100:103], %[ea] offset:2048\n\t"
    "ds_read_b128 v[104:107], %[da] offset:0\n\t"
    "ds_read_b128 v[108:111], %[da] offset:2048\n\t"
    "ds_read_b128 v[112:115], %[ea] offset:4096\n\t"
    "ds_read_b128 v[116:119], %[ea] offset:6144\n\t"
    "s_waitcnt lgkmcnt(4)\n\t"
    "v_mfma_f32_16x16x32_bf16 v[80:83], v[128:131], v[96:99], 0\n\t"
    "ds_read_b128 v[120:123], %[da] offset:4096\n\t"
    "v_mfma_f32_16x16x32_bf16 v[84:87], v[132:135], v[96:99], 0\n\t"
    "ds_read_b128 v[124:127], %[da] offset:6144\n\t"
    "v_mfma_f32_16x16x32_bf16 v[88:91], v[128:131], v[100:103], 0\n\t"
    "v_mfma_f32_16x16x32_bf16 v[92:95], v[132:135], v[100:103], 0\n\t"
    "s_waitcnt vmcnt(24)\n\t"
    "s_cmp_lt_u32 %[x], 2\n\t"
    "s_cbranch_scc0 41f\n\t"
    "s_waitcnt vmcnt(8)\n\t"
    "41:\n\t"
    "s_barrier\n\t"
    "s_waitcnt lgkmcnt(4)\n\t"
    "v_mfma_f32_16x16x32_bf16 v[80:83], v[136:139], v[104:107], v[80:83]\n\t"
    "ds_read_b128 v[96:99], %[ea] offset:8192\n\t"
    "v_mfma_f32_16x16x32_bf16 v[84:87], v[140:143], v[104:107], v[84:87]\n\t"
    "ds_read_b128 v[100:103], %[ea] offset:10240\n\t"
    "v_mfma_f32_16x16x32_bf16 v[88:91], v[136:139], v[108:111], v[88:91]\n\t"
    "v_mfma_f32_16x16x32_bf16 v[92:95], v[140:143], v[108:111], v[92:95]\n\t"
    "s_waitcnt lgkmcnt(4)\n\t"
    "v_mfma_f32_16x16x32_bf16 v[80:83], v[144:147], v[112:115], v[80:83]\n\t"
    "ds_read_b128 v[104:107], %[da] offset:8192\n\t"
    "v_mfma_f32_16x16x32_bf16 v[84:87], v[148:151], v[112:115], v[84:87]\n\t"
    "ds_read_b128 v[108:111], %[da] offset:10240\n\t"
    "v_mfma_f32_16x16x32_bf16 v[88:91], v[144:147], v[116:119], v[88:91]\n\t"
    "v_mfma_f32_16x16x32_bf16 v[92:95], v[148:151], v[116:119], v[92:95]\n\t"
    "s_waitcnt lgkmcnt(4)\n\t"
    "v_mfma_f32_16x16x32_bf16 v[80:83], v[152:155], v[120:123], v[80:83]\n\t"
    "ds_read_b128 v[112:115], %[ea] offset:12288\n\t"
    "v_mfma_f32_16x16x32_bf16 v[84:87], v[156:159], v[120:123], v[84:87]\n\t"
    "ds_read_b128 v[116:119], %[ea] offset:14336\n\t"
    "v_mfma_f32_16x16x32_bf16 v[88:91], v[152:155], v[124:127], v[88:91]\n\t"
    "v_mfma_f32_16x16x32_bf16 v[92:95], v[156:159], v[124:127], v[92:95]\n\t"
    "s_waitcnt lgkmcnt(4)\n\t"
    "v_mfma_f32_16x16x32_bf16 v[80:83], v[160:163], v[96:99], v[80:83]\n\t"
    "ds_read_b128 v[120:123], %[da] offset:12288\n\t"
    "v_mfma_f32_16x16x32_bf16 v[84:87], v[164:167], v[96:99], v[84:87]\n\t"
    "ds_read_b128 v[124:127], %[da] offset:14336\n\t"
    "v_mfma_f32_16x16x32_bf16 v[88:91], v[160:163], v[100:103], v[88:91]\n\t"
    "v_mfma_f32_16x16x32_bf16 v[92:95], v[164:167], v[100:103], v[92:95]\n\t"
    "s_waitcnt vmcnt(20)\n\t"
    "s_cmp_lt_u32 %[x], 2\n\t"
    "s_cbranch_scc0 45f\n\t"
    "s_waitcnt vmcnt(4)\n\t"
    "45:\n\t"
    "s_barrier\n\t"
    "s_waitcnt lgkmcnt(4)\n\t"
    "v_mfma_f32_16x16x32_bf16 v[80:83], v[168:171], v[104:107], v[80:83]\n\t"
    "ds_read_b128 v[96:99], %[ea] offset:16384\n\t"
    "v_mfma_f32_16x16x32_bf16 v[84:87], v[172:175], v[104:107], v[84:87]\n\t"
    "ds_read_b128 v[100:103], %[ea] offset:18432\n\t"
    "v_mfma_f32_16x16x32_bf16 v[88:91], v[168:171], v[108:111], v[88:91]\n\t"
    "v_mfma_f32_16x16x32_bf16 v[92:95], v[172:175], v[108:111], v[92:95]\n\t"
    "s_waitcnt lgkmcnt(4)\n\t"
    "v_mfma_f32_16x16x32_bf16 v[80:83], v[176:179], v[112:115], v[80:83]\n\t"
    "ds_read_b128 v[104:107], %[da] offset:16384\n\t"
    "v_mfma_f32_16x16x32_bf16 v[84:87], v[180:183], v[112:115], v[84:87]\n\t"
    "ds_read_b128 v[108:111], %[da] offset:18432\n\t"
    "v_mfma_f32_16x16x32_bf16 v[88:91], v[176:179], v[116:119], v[88:91]\n\t"
    "v_mfma_f32_16x16x32_bf16 v[92:95], v[180:183], v[116:119], v[92:95]\n\t"
    "s_waitcnt lgkmcnt(4)\n\t"
    "v_mfma_f32_16x16x32_bf16 v[80:83], v[184:187], v[120:123], v[80:83]\n\t"
    "ds_read_b128 v[112:115], %[ea] offset:20480\n\t"
    "v_mfma_f32_16x16x32_bf16 v[84:87], v[188:191], v[120:123], v[84:87]\n\t"
    "ds_read_b128 v[116:119], %[ea] offset:22528\n\t"
    "v_mfma_f32_16x16x32_bf16 v[88:91], v[184:187], v[124:127], v[88:91]\n\t"
    "v_mfma_f32_16x16x32_bf16 v[92:95], v[188:191], v[124:127], v[92:95]\n\t"
    "s_waitcnt lgkmcnt(4)\n\t"
    "v_mfma_f32_16x16x32_bf16 v[80:83], v[192:195], v[96:99], v[80:83]\n\t"
    "ds_read_b128 v[120:123], %[da] offset:20480\n\t"
    "v_mfma_f32_16x16x32_bf16 v[84:87], v[196:199], v[96:99], v[84:87]\n\t"
    "ds_read_b128 v[124:127], %[da] offset:22528\n\t"
    "v_mfma_f32_16x16x32_bf16 v[88:91], v[192:195], v[100:103], v[88:91]\n\t"
    "v_mfma_f32_16x16x32_bf16 v[92:95], v[196:199], v[100:103], v[92:95]\n\t"
    "s_waitcnt vmcnt(16)\n\t"
    "s_cmp_lt_u32 %[x], 2\n\t"
    "s_cbranch_scc0 49f\n\t"
    "s_waitcnt vmcnt(0)\n\t"
    "49:\n\t"
    "s_barrier\n\t"
    "ds_read_b32 v79, %[sa]\n\t"
    "s_waitcnt lgkmcnt(5)\n\t"
    "v_mfma_f32_16x16x32_bf16 v[80:83], v[200:203], v[104:107], v[80:83]\n\t"
    "ds_read_b128 v[96:99], %[ea] offset:24576\n\t"
    "v_mfma_f32_16x16x32_bf16 v[84:87], v[204:207], v[104:107], v[84:87]\n\t"
    "ds_read_b128 v[100:103], %[ea] offset:26624\n\t"
    "v_mfma_f32_16x16x32_bf16 v[88:91], v[200:203], v[108:111], v[88:91]\n\t"
    "v_mfma_f32_16x16x32_bf16 v[92:95], v[204:207], v[108:111], v[92:95]\n\t"
    "s_waitcnt lgkmcnt(5)\n\t"
    "v_mfma_f32_16x16x32_bf16 v[80:83], v[208:211], v[112:115], v[80:83]\n\t"
    "ds_read_b128 v[104:107], %[da] offset:24576\n\t"
    "v_mfma_f32_16x16x32_bf16 v[84:87], v[212:215], v[112:115], v[84:87]\n\t"
    "ds_read_b128 v[108:111], %[da] offset:26624\n\t"
    "v_mfma_f32_16x16x32_bf16 v[88:91], v[208:211], v[116:119], v[88:91]\n\t"
    "v_mfma_f32_16x16x32_bf16 v[92:95], v[212:215], v[116:119], v[92:95]\n\t"
    "s_cmp_eq_u32 %[s2], 0\n\t"
    "s_cbranch_scc1 7f\n\t"
    "s_mov_b32 m0, %[s2]\n\t"
    "s_nop 0\n\t"
    "global_load_lds_dword %[dv], %[db] sc1\n\t"
    "7:\n\t"
    "s_waitcnt lgkmcnt(4)\n\t"
    "v_mfma_f32_16x16x32_bf16 v[80:83], v[216:219], v[120:123], v[80:83]\n\t"
    "ds_read_b128 v[112:115], %[ea] offset:28672\n\t"
    "v_mfma_f32_16x16x32_bf16 v[84:87], v[220:223], v[120:123], v[84:87]\n\t"
    "ds_read_b128 v[116:119], %[ea] offset:30720\n\t"
    "v_mfma_f32_16x16x32_bf16 v[88:91], v[216:219], v[124:127], v[88:91]\n\t"
    "v_mad_u32_u24 v79, v79, %[mu], %[ad]\n\t"
    "v_cmp_ge_i32 vcc, %[th], v79\n\t"
    "v_mfma_f32_16x16x32_bf16 v[92:95], v[220:223], v[124:127], v[92:95]\n\t"
    "s_cbranch_vccz 5f\n\t"
    "s_waitcnt lgkmcnt(4)\n\t"
    "v_mfma_f32_16x16x32_bf16 v[80:83], v[224:227], v[96:99], v[80:83]\n\t"
    "ds_read_b128 v[120:123], %[da] offset:28672\n\t"
    "v_mfma_f32_16x16x32_bf16 v[84:87], v[228:231], v[96:99], v[84:87]\n\t"
    "ds_read_b128 v[124:127], %[da] offset:30720\n\t"
    "v_mfma_f32_16x16x32_bf16 v[88:91], v[224:227], v[100:103], v[88:91]\n\t"
    "v_mfma_f32_16x16x32_bf16 v[92:95], v[228:231], v[100:103], v[92:95]\n\t"
    "s_waitcnt lgkmcnt(4)\n\t"
    "v_mfma_f32_16x16x32_bf16 v[80:83], v[232:235], v[104:107], v[80:83]\n\t"
    "v_mfma_f32_16x16x32_bf16 v[84:87], v[236:239], v[104:107], v[84:87]\n\t"
    "v_mfma_f32_16x16x32_bf16 v[88:91], v[232:235], v[108:111], v[88:91]\n\t"
    "v_mfma_f32_16x16x32_bf16 v[92:95], v[236:239], v[108:111], v[92:95]\n\t"
    "s_waitcnt lgkmcnt(2)\n\t"
    "v_mfma_f32_16x16x32_bf16 v[80:83], v[240:243], v[112:115], v[80:83]\n\t"
    "v_mfma_f32_16x16x32_bf16 v[84:87], v[244:247], v[112:115], v[84:87]\n\t"
    "v_mfma_f32_16x16x32_bf16 v[88:91], v[240:243], v[116:119], v[88:91]\n\t"
    "v_mfma_f32_16x16x32_bf16 v[92:95], v[244:247], v[116:119], v[92:95]\n\t"
    "s_waitcnt lgkmcnt(0)\n\t"
    "v_mfma_f32_16x16x32_bf16 v[80:83], v[248:251], v[120:123], v[80:83]\n\t"
    "v_mfma_f32_16x16x32_bf16 v[84:87], v[252:255], v[120:123], v[84:87]\n\t"
    "v_mfma_f32_16x16x32_bf16 v[88:91], v[248:251], v[124:127], v[88:91]\n\t"
    "v_mfma_f32_16x16x32_bf16 v[92:95], v[252:255], v[124:127], v[92:95]\n\t"
    "s_branch 6f\n\t"
    "5:\n\t"
    "s_waitcnt lgkmcnt(4)\n\t"
    "v_mfma_f32_16x16x32_bf16 v[80:83], v[224:227], v[96:99], v[80:83]\n\t"
    "ds_read_b128 v[120:123], %[da] offset:28672\n\t"
    "s_add_u32 m0, %[lb], 0\n\t"
    "s_nop 0\n\t"
    "global_load_lds_dwordx4 %[av0], %[s0] sc1\n\t"
    "v_mfma_f32_16x16x32_bf16 v[84:87], v[228:231], v[96:99], v[84:87]\n\t"
    "ds_read_b128 v[124:127], %[da] offset:30720\n\t"
    "s_add_u32 m0, %[lb], 32768\n\t"
    "s_nop 0\n\t"
    "global_load_lds_dwordx4 %[av1], %[s0] sc1\n\t"
    "v_mfma_f32_16x16x32_bf16 v[88:91], v[224:227], v[100:103], v[88:91]\n\t"
    "s_add_u32 m0, %[lb], 4096\n\t"
    "s_nop 0\n\t"
    "global_load_lds_dwordx4 %[av2], %[s0] sc1\n\t"
    "v_mfma_f32_16x16x32_bf16 v[92:95], v[228:231], v[100:103], v[92:95]\n\t"
    "s_add_u32 m0, %[lb], 36864\n\t"
    "s_nop 0\n\t"
    "global_load_lds_dwordx4 %[av3], %[s0] sc1\n\t"
    "s_waitcnt lgkmcnt(4)\n\t"
    "v_mfma_f32_16x16x32_bf16 v[80:83], v[232:235], v[104:107], v[80:83]\n\t"
    "s_add_u32 m0, %[lb], 8192\n\t"
    "s_nop 0\n\t"
    "global_load_lds_dwordx4 %[av4], %[s0] sc1\n\t"
    "v_mfma_f32_16x16x32_bf16 v[84:87], v[236:239], v[104:107], v[84:87]\n\t"
    "s_add_u32 m0, %[lb], 40960\n\t"
    "s_nop 0\n\t"
    "global_load_lds_dwordx4 %[av5], %[s0] sc1\n\t"
    "v_mfma_f32_16x16x32_bf16 v[88:91], v[232:235], v[108:111], v[88:91]\n\t"
    "s_add_u32 m0, %[lb], 12288\n\t"
    "s_nop 0\n\t"
    "global_load_lds_dwordx4 %[av6], %[s0] sc1\n\t"
    "v_mfma_f32_16x16x32_bf16 v[92:95], v[236:239], v[108:111], v[92:95]\n\t"
    "s_add_u32 m0, %[lb], 45056\n\t"
    "s_nop 0\n\t"
    "global_load_lds_dwordx4 %[av7], %[s0] sc1\n\t"
    "s_waitcnt lgkmcnt(2)\n\t"
    "v_mfma_f32_16x16x32_bf16 v[80:83], v[240:243], v[112:115], v[80:83]\n\t"
    "s_add_u32 m0, %[lb], 16384\n\t"
    "s_nop 0\n\t"
    "global_load_lds_dwordx4 %[av0], %[s1] sc1\n\t"
    "v_mfma_f32_16x16x32_bf16 v[84:87], v[244:247], v[112:115], v[84:87]\n\t"
    "s_add_u32 m0, %[lb], 49152\n\t"
    "s_nop 0\n\t"
    "global_load_lds_dwordx4 %[av1], %[s1] sc1\n\t"
    "v_mfma_f32_16x16x32_bf16 v[88:91], v[240:243], v[116:119], v[88:91]\n\t"
    "s_add_u32 m0, %[lb], 20480\n\t"
    "s_nop 0\n\t"
    "global_load_lds_dwordx4 %[av2], %[s1] sc1\n\t"
    "v_mfma_f32_16x16x32_bf16 v[92:95], v[244:247], v[116:119], v[92:95]\n\t"
    "s_add_u32 m0, %[lb], 53248\n\t"
    "s_nop 0\n\t"
    "global_load_lds_dwordx4 %[av3], %[s1] sc1\n\t"
    "s_waitcnt lgkmcnt(0)\n\t"
    "v_mfma_f32_16x16x32_bf16 v[80:83], v[248:251], v[120:123], v[80:83]\n\t"
    "s_add_u32 m0, %[lb], 24576\n\t"
    "s_nop 0\n\t"
    "global_load_lds_dwordx4 %[av4], %[s1] sc1\n\t"
    "v_mfma_f32_16x16x32_bf16 v[84:87], v[252:255], v[120:123], v[84:87]\n\t"
    "s_add_u32 m0, %[lb], 57344\n\t"
    "s_nop 0\n\t"
    "global_load_lds_dwordx4 %[av5], %[s1] sc1\n\t"
    "v_mfma_f32_16x16x32_bf16 v[88:91], v[248:251], v[124:127], v[88:91]\n\t"
    "s_add_u32 m0, %[lb], 28672\n\t"
    "s_nop 0\n\t"
    "global_load_lds_dwordx4 %[av6], %[s1] sc1\n\t"
    "v_mfma_f32_16x16x32_bf16 v[92:95], v[252:255], v[124:127], v[92:95]\n\t"
    "s_add_u32 m0, %[lb], 61440\n\t"
    "s_nop 0\n\t"
    "global_load_lds_dwordx4 %[av7], %[s1] sc1\n\t"
    "6:\n\t"
    "s_nop 6\n\t"
    "s_branch 9f\n\t"
    "3:\n\t"
    "9:"
    : "=&{v[80:95]}"(pd)
    : [ea] "v"(ea), [da] "v"(da), [x] "s"(x), "{v[128:159]}"(W[0]), "{v[160:191]}"(W[1]), "{v[192:223]}"(W[2]), "{v[224:255]}"(W[3]), [lb] "s"(lb), [s0] "s"(s0), [s1] "s"(s1), [av0] "v"(av[0]), [av1] "v"(av[1]), [av2] "v"(av[2]), [av3] "v"(av[3]), [av4] "v"(av[4]), [av5] "v"(av[5]), [av6] "v"(av[6]), [av7] "v"(av[7]), [sa] "v"(sa), [mu] "s"(mu), [ad] "v"(ad), [th] "s"(th), [s2] "s"(s2), [dv] "v"(dv), [db] "s"(db)
    : "v96", "v97", "v98", "v99", "v100", "v101", "v102", "v103", "v104", "v105", "v106", "v107", "v108", "v109", "v110", "v111", "v112", "v113", "v114", "v115", "v116", "v117", "v118", "v119", "v120", "v121", "v122", "v123", "v124", "v125", "v126", "v127", "v79", "vcc", "scc", "m0", "memory");
}
__device__ __forceinline__ void lean_dm0(const u32x32 (&W)[4], u32x16& pd, unsigned ea, unsigned da, unsigned x, unsigned lb, const char* s0, const char* s1, const unsigned (&av)[8]) {
  asm volatile("s_setprio 2\n\t"
    "ds_read_b128 v[96:99], %[ea] offset:0\n\t"
    "ds_read_b128 v[100:103], %[ea] offset:2048\n\t"
    "ds_read_b128 v[104:107], %[da] offset:0\n\t"
    "ds_read_b128 v[108:111], %[da] offset:2048\n\t"
    "ds_read_b128 v[112:115], %[ea] offset:4096\n\t"
    "ds_read_b128 v[116:119], %[ea] offset:6144\n\t"
    "s_waitcnt lgkmcnt(4)\n\t"
    "v_mfma_f32_16x16x32_bf16 v[80:83], v[128:131], v[96:99], 0\n\t"
    "ds_read_b128 v[120:123], %[da] offset:4096\n\t"
    "s_add_u32 m0, %[lb], 0\n\t"
    "v_mfma_f32_16x16x32_bf16 v[84:87], v[132:135], v[96:99], 0\n\t"
    "ds_read_b128 v[124:127], %[da] offset:6144\n\t"
    "global_load_lds_dwordx4 %[av0], %[s0] sc1\n\t"
    "v_mfma_f32_16x16x32_bf16 v[88:91], v[128:131], v[100:103], 0\n\t"
    "s_add_u32 m0, %[lb], 32768\n\t"
    "v_mfma_f32_16x16x32_bf16 v[92:95], v[132:135], v[100:103], 0\n\t"
    "global_load_lds_dwordx4 %[av1], %[s0] sc1\n\t"
    "s_waitcnt vmcnt(10)\n\t"
    "s_barrier\n\t"
    "s_waitcnt lgkmcnt(4)\n\t"
    "v_mfma_f32_16x16x32_bf16 v[80:83], v[136:139], v[104:107], v[80:83]\n\t"
    "ds_read_b128 v[96:99], %[ea] offset:8192\n\t"
    "s_add_u32 m0, %[lb], 4096\n\t"
    "v_mfma_f32_16x16x32_bf16 v[84:87], v[140:143], v[104:107], v[84:87]\n\t"
    "ds_read_b128 v[100:103], %[ea] offset:10240\n\t"
    "global_load_lds_dwordx4 %[av2], %[s0] sc1\n\t"
    "v_mfma_f32_16x16x32_bf16 v[88:91], v[136:139], v[108:111], v[88:91]\n\t"
    "s_add_u32 m0, %[lb], 36864\n\t"
    "v_mfma_f32_16x16x32_bf16 v[92:95], v[140:143], v[108:111], v[92:95]\n\t"
    "global_load_lds_dwordx4 %[av3], %[s0] sc1\n\t"
    "s_waitcnt lgkmcnt(4)\n\t"
    "v_mfma_f32_16x16x32_bf16 v[80:83], v[144:147], v[112:115], v[80:83]\n\t"
    "ds_read_b128 v[104:107], %[da] offset:8192\n\t"
    "s_add_u32 m0, %[lb], 8192\n\t"
    "v_mfma_f32_16x16x32_bf16 v[84:87], v[148:151], v[112:115], v[84:87]\n\t"
    "ds_read_b128 v[108:111], %[da] offset:10240\n\t"
    "global_load_lds_dwordx4 %[av4], %[s0] sc1\n\t"
    "v_mfma_f32_16x16x32_bf16 v[88:91], v[144:147], v[116:119], v[88:91]\n\t"
    "s_add_u32 m0, %[lb], 40960\n\t"
    "v_mfma_f32_16x16x32_bf16 v[92:95], v[148:151], v[116:119], v[92:95]\n\t"
    "global_load_lds_dwordx4 %[av5], %[s0] sc1\n\t"
    "s_waitcnt lgkmcnt(4)\n\t"
    "v_mfma_f32_16x16x32_bf16 v[80:83], v[152:155], v[120:123], v[80:83]\n\t"
    "ds_read_b128 v[112:115], %[ea] offset:12288\n\t"
    "s_add_u32 m0, %[lb], 12288\n\t"
    "v_mfma_f32_16x16x32_bf16 v[84:87], v[156:159], v[120:123], v[84:87]\n\t"
    "ds_read_b128 v[116:119], %[ea] offset:14336\n\t"
    "global_load_lds_dwordx4 %[av6], %[s0] sc1\n\t"
    "v_mfma_f32_16x16x32_bf16 v[88:91], v[152:155], v[124:127], v[88:91]\n\t"
    "s_add_u32 m0, %[lb], 45056\n\t"
    "v_mfma_f32_16x16x32_bf16 v[92:95], v[156:159], v[124:127], v[92:95]\n\t"
    "global_load_lds_dwordx4 %[av7], %[s0] sc1\n\t"
    "s_waitcnt lgkmcnt(4)\n\t"
    "v_mfma_f32_16x16x32_bf16 v[80:83], v[160:163], v[96:99], v[80:83]\n\t"
    "ds_read_b128 v[120:123], %[da] offset:12288\n\t"
    "s_add_u32 m0, %[lb], 16384\n\t"
    "v_mfma_f32_16x16x32_bf16 v[84:87], v[164:167], v[96:99], v[84:87]\n\t"
    "ds_read_b128 v[124:127], %[da] offset:14336\n\t"
    "global_load_lds_dwordx4 %[av0], %[s1] sc1\n\t"
    "v_mfma_f32_16x16x32_bf16 v[88:91], v[160:163], v[100:103], v[88:91]\n\t"
    "s_add_u32 m0, %[lb], 49152\n\t"
    "v_mfma_f32_16x16x32_bf16 v[92:95], v[164:167], v[100:103], v[92:95]\n\t"
    "global_load_lds_dwordx4 %[av1], %[s1] sc1\n\t"
    "s_waitcnt vmcnt(14)\n\t"
    "s_barrier\n\t"
    "s_waitcnt lgkmcnt(4)\n\t"
    "v_mfma_f32_16x16x32_bf16 v[80:83], v[168:171], v[104:107], v[80:83]\n\t"
    "ds_read_b128 v[96:99], %[ea] offset:16384\n\t"
    "s_add_u32 m0, %[lb], 20480\n\t"
    "v_mfma_f32_16x16x32_bf16 v[84:87], v[172:175], v[104:107], v[84:87]\n\t"
    "ds_read_b128 v[100:103], %[ea] offset:18432\n\t"
    "global_load_lds_dwordx4 %[av2], %[s1] sc1\n\t"
    "v_mfma_f32_16x16x32_bf16 v[88:91], v[168:171], v[108:111], v[88:91]\n\t"
    "s_add_u32 m0, %[lb], 53248\n\t"
    "v_mfma_f32_16x16x32_bf16 v[92:95], v[172:175], v[108:111], v[92:95]\n\t"
    "global_load_lds_dwordx4 %[av3], %[s1] sc1\n\t"
    "s_waitcnt lgkmcnt(4)\n\t"
    "v_mfma_f32_16x16x32_bf16 v[80:83], v[176:179], v[112:115], v[80:83]\n\t"
    "ds_read_b128 v[104:107], %[da] offset:16384\n\t"
    "s_add_u32 m0, %[lb], 24576\n\t"
    "v_mfma_f32_16x16x32_bf16 v[84:87], v[180:183], v[112:115], v[84:87]\n\t"
    "ds_read_b128 v[108:111], %[da] offset:18432\n\t"
    "global_load_lds_dwordx4 %[av4], %[s1] sc1\n\t"
    "v_mfma_f32_16x16x32_bf16 v[88:91], v[176:179], v[116:119], v[88:91]\n\t"
    "s_add_u32 m0, %[lb], 57344\n\t"
    "v_mfma_f32_16x16x32_bf16 v[92:95], v[180:183], v[116:119], v[92:95]\n\t"
    "global_load_lds_dwordx4 %[av5], %[s1] sc1\n\t"
    "s_waitcnt lgkmcnt(4)\n\t"
    "v_mfma_f32_16x16x32_bf16 v[80:83], v[184:187], v[120:123], v[80:83]\n\t"
    "ds_read_b128 v[112:115], %[ea] offset:20480\n\t"
    "s_add_u32 m0, %[lb], 28672\n\t"
    "v_mfma_f32_16x16x32_bf16 v[84:87], v[188:191], v[120:123], v[84:87]\n\t"
    "ds_read_b128 v[116:119], %[ea] offset:22528\n\t"
    "global_load_lds_dwordx4 %[av6], %[s1] sc1\n\t"
    "v_mfma_f32_16x16x32_bf16 v[88:91], v[184:187], v[124:127], v[88:91]\n\t"
    "s_add_u32 m0, %[lb], 61440\n\t"
    "v_mfma_f32_16x16x32_bf16 v[92:95], v[188:191], v[124:127], v[92:95]\n\t"
    "global_load_lds_dwordx4 %[av7], %[s1] sc1\n\t"
    "s_waitcnt lgkmcnt(4)\n\t"
    "v_mfma_f32_16x16x32_bf16 v[80:83], v[192:195], v[96:99], v[80:83]\n\t"
    "ds_read_b128 v[120:123], %[da] offset:20480\n\t"
    "v_mfma_f32_16x16x32_bf16 v[84:87], v[196:199], v[96:99], v[84:87]\n\t"
    "ds_read_b128 v[124:127], %[da] offset:22528\n\t"
    "v_mfma_f32_16x16x32_bf16 v[88:91], v[192:195], v[100:103], v[88:91]\n\t"
    "v_mfma_f32_16x16x32_bf16 v[92:95], v[196:199], v[100:103], v[92:95]\n\t"
    "s_waitcnt vmcnt(16)\n\t"
    "s_barrier\n\t"
    "s_waitcnt lgkmcnt(4)\n\t"
    "v_mfma_f32_16x16x32_bf16 v[80:83], v[200:203], v[104:107], v[80:83]\n\t"
    "ds_read_b128 v[96:99], %[ea] offset:24576\n\t"
    "v_mfma_f32_16x16x32_bf16 v[84:87], v[204:207], v[104:107], v[84:87]\n\t"
    "ds_read_b128 v[100:103], %[ea] offset:26624\n\t"
    "v_mfma_f32_16x16x32_bf16 v[88:91], v[200:203], v[108:111], v[88:91]\n\t"
    "v_mfma_f32_16x16x32_bf16 v[92:95], v[204:207], v[108:111], v[92:95]\n\t"
    "s_waitcnt lgkmcnt(4)\n\t"
    "v_mfma_f32_16x16x32_bf16 v[80:83], v[208:211], v[112:115], v[80:83]\n\t"
    "ds_read_b128 v[104:107], %[da] offset:24576\n\t"
    "v_mfma_f32_16x16x32_bf16 v[84:87], v[212:215], v[112:115], v[84:87]\n\t"
    "ds_read_b128 v[108:111], %[da] offset:26624\n\t"
    "v_mfma_f32_16x16x32_bf16 v[88:91], v[208:211], v[116:119], v[88:91]\n\t"
    "v_mfma_f32_16x16x32_bf16 v[92:95], v[212:215], v[116:119], v[92:95]\n\t"
    "s_waitcnt lgkmcnt(4)\n\t"
    "v_mfma_f32_16x16x32_bf16 v[80:83], v[216:219], v[120:123], v[80:83]\n\t"
    "ds_read_b128 v[112:115], %[ea] offset:28672\n\t"
    "v_mfma_f32_16x16x32_bf16 v[84:87], v[220:223], v[120:123], v[84:87]\n\t"
    "ds_read_b128 v[116:119], %[ea] offset:30720\n\t"
    "v_mfma_f32_16x16x32_bf16 v[88:91], v[216:219], v[124:127], v[88:91]\n\t"
    "v_mfma_f32_16x16x32_bf16 v[92:95], v[220:223], v[124:127], v[92:95]\n\t"
    "s_waitcnt lgkmcnt(4)\n\t"
    "v_mfma_f32_16x16x32_bf16 v[80:83], v[224:227], v[96:99], v[80:83]\n\t"
    "ds_read_b128 v[120:123], %[da] offset:28672\n\t"
    "v_mfma_f32_16x16x32_bf16 v[84:87], v[228:231], v[96:99], v[84:87]\n\t"
    "ds_read_b128 v[124:127], %[da] offset:30720\n\t"
    "v_mfma_f32_16x16x32_bf16 v[88:91], v[224:227], v[100:103], v[88:91]\n\t"
    "v_mfma_f32_16x16x32_bf16 v[92:95], v[228:231], v[100:103], v[92:95]\n\t"
    "s_waitcnt lgkmcnt(4)\n\t"
    "v_mfma_f32_16x16x32_bf16 v[80:83], v[232:235], v[104:107], v[80:83]\n\t"
    "v_mfma_f32_16x16x32_bf16 v[84:87], v[236:239], v[104:107], v[84:87]\n\t"
    "v_mfma_f32_16x16x32_bf16 v[88:91], v[232:235], v[108:111], v[88:91]\n\t"
    "v_mfma_f32_16x16x32_bf16 v[92:95], v[236:239], v[108:111], v[92:95]\n\t"
    "s_waitcnt lgkmcnt(2)\n\t"
    "v_mfma_f32_16x16x32_bf16 v[80:83], v[240:243], v[112:115], v[80:83]\n\t"
    "v_mfma_f32_16x16x32_bf16 v[84:87], v[244:247], v[112:115], v[84:87]\n\t"
    "v_mfma_f32_16x16x32_bf16 v[88:91], v[240:243], v[116:119], v[88:91]\n\t"
    "v_mfma_f32_16x16x32_bf16 v[92:95], v[244:247], v[116:119], v[92:95]\n\t"
    "s_waitcnt lgkmcnt(0)\n\t"
    "v_mfma_f32_16x16x32_bf16 v[80:83], v[248:251], v[120:123], v[80:83]\n\t"
    "v_mfma_f32_16x16x32_bf16 v[84:87], v[252:255], v[120:123], v[84:87]\n\t"
    "v_mfma_f32_16x16x32_bf16 v[88:91], v[248:251], v[124:127], v[88:91]\n\t"
    "v_mfma_f32_16x16x32_bf16 v[92:95], v[252:255], v[124:127], v[92:95]\n\t"
    "s_nop 6\n\t"
    "s_branch 9f\n\t"
    "3:\n\t"
    "9:"
    : "=&{v[80:95]}"(pd)
    : [ea] "v"(ea), [da] "v"(da), [x] "s"(x), "{v[128:159]}"(W[0]), "{v[160:191]}"(W[1]), "{v[192:223]}"(W[2]), "{v[224:255]}"(W[3]), [lb] "s"(lb), [s0] "s"(s0), [s1] "s"(s1), [av0] "v"(av[0]), [av1] "v"(av[1]), [av2] "v"(av[2]), [av3] "v"(av[3]), [av4] "v"(av[4]), [av5] "v"(av[5]), [av6] "v"(av[6]), [av7] "v"(av[7])
    : "v96", "v97", "v98", "v99", "v100", "v101", "v102", "v103", "v104", "v105", "v106", "v107", "v108", "v109", "v110", "v111", "v112", "v113", "v114", "v115", "v116", "v117", "v118", "v119", "v120", "v121", "v122", "v123", "v124", "v125", "v126", "v127", "vcc", "scc", "m0", "memory");
}
__device__ __forceinline__ void lean_ld1(const u32x32 (&W)[4], u32x16& pd, unsigned ea, unsigned da, unsigned x, unsigned lb, const char* s0, const char* s1, const unsigned (&av)[8], unsigned sa, unsigned mu, unsigned ad, int th, unsigned s2, unsigned dv, const char* db) {
  asm volatile("s_setprio 2\n\t"
    "ds_read_b128 v[96:99], %[ea] offset:0\n\t"
    "ds_read_b128 v[100:103], %[ea] offset:2048\n\t"
    "ds_read_b128 v[104:107], %[da] offset:0\n\t"
    "ds_read_b128 v[108:111], %[da] offset:2048\n\t"
    "ds_read_b128 v[112:115], %[ea] offset:4096\n\t"
    "ds_read_b128 v[116:119], %[ea] offset:6144\n\t"
    "s_waitcnt lgkmcnt(4)\n\t"
    "v_mfma_f32_16x16x32_bf16 v[80:83], v[128:131], v[96:99], 0\n\t"
    "ds_read_b128 v[120:123], %[da] offset:4096\n\t"
    "v_mfma_f32_16x16x32_bf16 v[84:87], v[132:135], v[96:99], 0\n\t"
    "ds_read_b128 v[124:127], %[da] offset:6144\n\t"
    "v_mfma_f32_16x16x32_bf16 v[88:91], v[128:131], v[100:103], 0\n\t"
    "v_mfma_f32_16x16x32_bf16 v[92:95], v[132:135], v[100:103], 0\n\t"
    "s_waitcnt vmcnt(12)\n\t"
    "s_cmp_lt_u32 %[x], 2\n\t"
    "s_cbranch_scc0 41f\n\t"
    "s_waitcnt vmcnt(4)\n\t"
    "41:\n\t"
    "s_barrier\n\t"
    "s_waitcnt lgkmcnt(4)\n\t"
    "v_mfma_f32_16x16x32_bf16 v[80:83], v[136:139], v[104:107], v[80:83]\n\t"
    "ds_read_b128 v[96:99], %[ea] offset:8192\n\t"
    "v_mfma_f32_16x16x32_bf16 v[84:87], v[140:143], v[104:107], v[84:87]\n\t"
    "ds_read_b128 v[100:103], %[ea] offset:10240\n\t"
    "v_mfma_f32_16x16x32_bf16 v[88:91], v[136:139], v[108:111], v[88:91]\n\t"
    "v_mfma_f32_16x16x32_bf16 v[92:95], v[140:143], v[108:111], v[92:95]\n\t"
    "s_waitcnt lgkmcnt(4)\n\t"
    "v_mfma_f32_16x16x32_bf16 v[80:83], v[144:147], v[112:115], v[80:83]\n\t"
    "ds_read_b128 v[104:107], %[da] offset:8192\n\t"
    "v_mfma_f32_16x16x32_bf16 v[84:87], v[148:151], v[112:115], v[84:87]\n\t"
    "ds_read_b128 v[108:111], %[da] offset:10240\n\t"
    "v_mfma_f32_16x16x32_bf16 v[88:91], v[144:147], v[116:119], v[88:91]\n\t"
    "v_mfma_f32_16x16x32_bf16 v[92:95], v[148:151], v[116:119], v[92:95]\n\t"
    "s_waitcnt lgkmcnt(4)\n\t"
    "v_mfma_f32_16x16x32_bf16 v[80:83], v[152:155], v[120:123], v[80:83]\n\t"
    "ds_read_b128 v[112:115], %[ea] offset:12288\n\t"
    "v_mfma_f32_16x16x32_bf16 v[84:87], v[156:159], v[120:123], v[84:87]\n\t"
    "ds_read_b128 v[116:119], %[ea] offset:14336\n\t"
    "v_mfma_f32_16x16x32_bf16 v[88:91], v[152:155], v[124:127], v[88:91]\n\t"
    "v_mfma_f32_16x16x32_bf16 v[92:95], v[156:159], v[124:127], v[92:95]\n\t"
    "s_waitcnt lgkmcnt(4)\n\t"
    "v_mfma_f32_16x16x32_bf16 v[80:83], v[160:163], v[96:99], v[80:83]\n\t"
    "ds_read_b128 v[120:123], %[da] offset:12288\n\t"
    "v_mfma_f32_16x16x32_bf16 v[84:87], v[164:167], v[96:99], v[84:87]\n\t"
    "ds_read_b128 v[124:127], %[da] offset:14336\n\t"
    "v_mfma_f32_16x16x32_bf16 v[88:91], v[160:163], v[100:103], v[88:91]\n\t"
    "v_mfma_f32_16x16x32_bf16 v[92:95], v[164:167], v[100:103], v[92:95]\n\t"
    "s_waitcnt vmcnt(10)\n\t"
    "s_cmp_lt_u32 %[x], 2\n\t"
    "s_cbranch_scc0 45f\n\t"
    "s_waitcnt vmcnt(2)\n\t"
    "45:\n\t"
    "s_barrier\n\t"
    "s_waitcnt lgkmcnt(4)\n\t"
    "v_mfma_f32_16x16x32_bf16 v[80:83], v[168:171], v[104:107], v[80:83]\n\t"
    "ds_read_b128 v[96:99], %[ea] offset:16384\n\t"
    "v_mfma_f32_16x16x32_bf16 v[84:87], v[172:175], v[104:107], v[84:87]\n\t"
    "ds_read_b128 v[100:103], %[ea] offset:18432\n\t"
    "v_mfma_f32_16x16x32_bf16 v[88:91], v[168:171], v[108:111], v[88:91]\n\t"
    "v_mfma_f32_16x16x32_bf16 v[92:95], v[172:175], v[108:111], v[92:95]\n\t"
    "s_waitcnt lgkmcnt(4)\n\t"
    "v_mfma_f32_16x16x32_bf16 v[80:83], v[176:179], v[112:115], v[80:83]\n\t"
    "ds_read_b128 v[104:107], %[da] offset:16384\n\t"
    "v_mfma_f32_16x16x32_bf16 v[84:87], v[180:183], v[112:115], v[84:87]\n\t"
    "ds_read_b128 v[108:111], %[da] offset:18432\n\t"
    "v_mfma_f32_16x16x32_bf16 v[88:91], v[176:179], v[116:119], v[88:91]\n\t"
    "v_mfma_f32_16x16x32_bf16 v[92:95], v[180:183], v[116:119], v[92:95]\n\t"
    "s_waitcnt lgkmcnt(4)\n\t"
    "v_mfma_f32_16x16x32_bf16 v[80:83], v[184:187], v[120:123], v[80:83]\n\t"
    "ds_read_b128 v[112:115], %[ea] offset:20480\n\t"
    "v_mfma_f32_16x16x32_bf16 v[84:87], v[188:191], v[120:123], v[84:87]\n\t"
    "ds_read_b128 v[116:119], %[ea] offset:22528\n\t"
    "v_mfma_f32_16x16x32_bf16 v[88:91], v[184:187], v[124:127], v[88:91]\n\t"
    "v_mfma_f32_16x16x32_bf16 v[92:95], v[188:191], v[124:127], v[92:95]\n\t"
    "s_waitcnt lgkmcnt(4)\n\t"
    "v_mfma_f32_16x16x32_bf16 v[80:83], v[192:195], v[96:99], v[80:83]\n\t"
    "ds_read_b128 v[120:123], %[da] offset:20480\n\t"
    "v_mfma_f32_16x16x32_bf16 v[84:87], v[196:199], v[96:99], v[84:87]\n\t"
    "ds_read_b128 v[124:127], %[da] offset:22528\n\t"
    "v_mfma_f32_16x16x32_bf16 v[88:91], v[192:195], v[100:103], v[88:91]\n\t"
    "v_mfma_f32_16x16x32_bf16 v[92:95], v[196:199], v[100:103], v[92:95]\n\t"
    "s_waitcnt vmcnt(8)\n\t"
    "s_cmp_lt_u32 %[x], 2\n\t"
    "s_cbranch_scc0 49f\n\t"
    "s_waitcnt vmcnt(0)\n\t"
    "49:\n\t"
    "s_barrier\n\t"
    "ds_read_b32 v79, %[sa]\n\t"
    "s_waitcnt lgkmcnt(5)\n\t"
    "v_mfma_f32_16x16x32_bf16 v[80:83], v[200:203], v[104:107], v[80:83]\n\t"
    "ds_read_b128 v[96:99], %[ea] offset:24576\n\t"
    "v_mfma_f32_16x16x32_bf16 v[84:87], v[204:207], v[104:107], v[84:87]\n\t"
    "ds_read_b128 v[100:103], %[ea] offset:26624\n\t"
    "v_mfma_f32_16x16x32_bf16 v[88:91], v[200:203], v[108:111], v[88:91]\n\t"
    "v_mfma_f32_16x16x32_bf16 v[92:95], v[204:207], v[108:111], v[92:95]\n\t"
    "s_waitcnt lgkmcnt(5)\n\t"
    "v_mfma_f32_16x16x32_bf16 v[80:83], v[208:211], v[112:115], v[80:83]\n\t"
    "ds_read_b128 v[104:107], %[da] offset:24576\n\t"
    "v_mfma_f32_16x16x32_bf16 v[84:87], v[212:215], v[112:115], v[84:87]\n\t"
    "ds_read_b128 v[108:111], %[da] offset:26624\n\t"
    "v_mfma_f32_16x16x32_bf16 v[88:91], v[208:211], v[116:119], v[88:91]\n\t"
    "v_mfma_f32_16x16x32_bf16 v[92:95], v[212:215], v[116:119], v[92:95]\n\t"
    "s_cmp_eq_u32 %[s2], 0\n\t"
    "s_cbranch_scc1 7f\n\t"
    "s_mov_b32 m0, %[s2]\n\t"
    "s_nop 0\n\t"
    "global_load_lds_dword %[dv], %[db] sc1\n\t"
    "7:\n\t"
    "s_waitcnt lgkmcnt(4)\n\t"
    "v_mfma_f32_16x16x32_bf16 v[80:83], v[216:219], v[120:123], v[80:83]\n\t"
    "ds_read_b128 v[112:115], %[ea] offset:28672\n\t"
    "v_mfma_f32_16x16x32_bf16 v[84:87], v[220:223], v[120:123], v[84:87]\n\t"
    "ds_read_b128 v[116:119], %[ea] offset:30720\n\t"
    "v_mfma_f32_16x16x32_bf16 v[88:91], v[216:219], v[124:127], v[88:91]\n\t"
    "v_mad_u32_u24 v79, v79, %[mu], %[ad]\n\t"
    "v_cmp_ge_i32 vcc, %[th], v79\n\t"
    "v_mfma_f32_16x16x32_bf16 v[92:95], v[220:223], v[124:127], v[92:95]\n\t"
    "s_cbranch_vccz 5f\n\t"
    "s_waitcnt lgkmcnt(4)\n\t"
    "v_mfma_f32_16x16x32_bf16 v[80:83], v[224:227], v[96:99], v[80:83]\n\t"
    "ds_read_b128 v[120:123], %[da] offset:28672\n\t"
    "v_mfma_f32_16x16x32_bf16 v[84:87], v[228:231], v[96:99], v[84:87]\n\t"
    "ds_read_b128 v[124:127], %[da] offset:30720\n\t"
    "v_mfma_f32_16x16x32_bf16 v[88:91], v[224:227], v[100:103], v[88:91]\n\t"
    "v_mfma_f32_16x16x32_bf16 v[92:95], v[228:231], v[100:103], v[92:95]\n\t"
    "s_waitcnt lgkmcnt(4)\n\t"
    "v_mfma_f32_16x16x32_bf16 v[80:83], v[232:235], v[104:107], v[80:83]\n\t"
    "v_mfma_f32_16x16x32_bf16 v[84:87], v[236:239], v[104:107], v[84:87]\n\t"
    "v_mfma_f32_16x16x32_bf16 v[88:91], v[232:235], v[108:111], v[88:91]\n\t"
    "v_mfma_f32_16x16x32_bf16 v[92:95], v[236:239], v[108:111], v[92:95]\n\t"
    "s_waitcnt lgkmcnt(2)\n\t"
    "v_mfma_f32_16x16x32_bf16 v[80:83], v[240:243], v[112:115], v[80:83]\n\t"
    "v_mfma_f32_16x16x32_bf16 v[84:87], v[244:247], v[112:115], v[84:87]\n\t"
    "v_mfma_f32_16x16x32_bf16 v[88:91], v[240:243], v[116:119], v[88:91]\n\t"
    "v_mfma_f32_16x16x32_bf16 v[92:95], v[244:247], v[116:119], v[92:95]\n\t"
    "s_waitcnt lgkmcnt(0)\n\t"
    "v_mfma_f32_16x16x32_bf16 v[80:83], v[248:251], v[120:123], v[80:83]\n\t"
    "v_mfma_f32_16x16x32_bf16 v[84:87], v[252:255], v[120:123], v[84:87]\n\t"
    "v_mfma_f32_16x16x32_bf16 v[88:91], v[248:251], v[124:127], v[88:91]\n\t"
    "v_mfma_f32_16x16x32_bf16 v[92:95], v[252:255], v[124:127], v[92:95]\n\t"
    "s_branch 6f\n\t"
    "5:\n\t"
    "s_waitcnt lgkmcnt(4)\n\t"
    "v_mfma_f32_16x16x32_bf16 v[80:83], v[224:227], v[96:99], v[80:83]\n\t"
    "ds_read_b128 v[120:123], %[da] offset:28672\n\t"
    "s_add_u32 m0, %[lb], 0\n\t"
    "s_nop 0\n\t"
    "global_load_lds_dwordx4 %[av0], %[s0] sc1\n\t"
    "v_mfma_f32_16x16x32_bf16 v[84:87], v[228:231], v[96:99], v[84:87]\n\t"
    "ds_read_b128 v[124:127], %[da] offset:30720\n\t"
    "v_mfma_f32_16x16x32_bf16 v[88:91], v[224:227], v[100:103], v[88:91]\n\t"
    "s_add_u32 m0, %[lb], 4096\n\t"
    "s_nop 0\n\t"
    "global_load_lds_dwordx4 %[av2], %[s0] sc1\n\t"
    "v_mfma_f32_16x16x32_bf16 v[92:95], v[228:231], v[100:103], v[92:95]\n\t"
    "s_waitcnt lgkmcnt(4)\n\t"
    "v_mfma_f32_16x16x32_bf16 v[80:83], v[232:235], v[104:107], v[80:83]\n\t"
    "s_add_u32 m0, %[lb], 8192\n\t"
    "s_nop 0\n\t"
    "global_load_lds_dwordx4 %[av4], %[s0] sc1\n\t"
    "v_mfma_f32_16x16x32_bf16 v[84:87], v[236:239], v[104:107], v[84:87]\n\t"
    "v_mfma_f32_16x16x32_bf16 v[88:91], v[232:235], v[108:111], v[88:91]\n\t"
    "s_add_u32 m0, %[lb], 12288\n\t"
    "s_nop 0\n\t"
    "global_load_lds_dwordx4 %[av6], %[s0] sc1\n\t"
    "v_mfma_f32_16x16x32_bf16 v[92:95], v[236:239], v[108:111], v[92:95]\n\t"
    "s_waitcnt lgkmcnt(2)\n\t"
    "v_mfma_f32_16x16x32_bf16 v[80:83], v[240:243], v[112:115], v[80:83]\n\t"
    "s_add_u32 m0, %[lb], 16384\n\t"
    "s_nop 0\n\t"
    "global_load_lds_dwordx4 %[av0], %[s1] sc1\n\t"
    "v_mfma_f32_16x16x32_bf16 v[84:87], v[244:247], v[112:115], v[84:87]\n\t"
    "v_mfma_f32_16x16x32_bf16 v[88:91], v[240:243], v[116:119], v[88:91]\n\t"
    "s_add_u32 m0, %[lb], 20480\n\t"
    "s_nop 0\n\t"
    "global_load_lds_dwordx4 %[av2], %[s1] sc1\n\t"
    "v_mfma_f32_16x16x32_bf16 v[92:95], v[244:247], v[116:119], v[92:95]\n\t"
    "s_waitcnt lgkmcnt(0)\n\t"
    "v_mfma_f32_16x16x32_bf16 v[80:83], v[248:251], v[120:123], v[80:83]\n\t"
    "s_add_u32 m0, %[lb], 24576\n\t"
    "s_nop 0\n\t"
    "global_load_lds_dwordx4 %[av4], %[s1] sc1\n\t"
    "v_mfma_f32_16x16x32_bf16 v[84:87], v[252:255], v[120:123], v[84:87]\n\t"
    "v_mfma_f32_16x16x32_bf16 v[88:91], v[248:251], v[124:127], v[88:91]\n\t"
    "s_add_u32 m0, %[lb], 28672\n\t"
    "s_nop 0\n\t"
    "global_load_lds_dwordx4 %[av6], %[s1] sc1\n\t"
    "v_mfma_f32_16x16x32_bf16 v[92:95], v[252:255], v[124:127], v[92:95]\n\t"
    "6:\n\t"
    "s_nop 6\n\t"
    "s_branch 9f\n\t"
    "3:\n\t"
    "9:"
    : "=&{v[80:95]}"(pd)
    : [ea] "v"(ea), [da] "v"(da), [x] "s"(x), "{v[128:159]}"(W[0]), "{v[160:191]}"(W[1]), "{v[192:223]}"(W[2]), "{v[224:255]}"(W[3]), [lb] "s"(lb), [s0] "s"(s0), [s1] "s"(s1), [av0] "v"(av[0]), [av1] "v"(av[1]), [av2] "v"(av[2]), [av3] "v"(av[3]), [av4] "v"(av[4]), [av5] "v"(av[5]), [av6] "v"(av[6]), [av7] "v"(av[7]), [sa] "v"(sa), [mu] "s"(mu), [ad] "v"(ad), [th] "s"(th), [s2] "s"(s2), [dv] "v"(dv), [db] "s"(db)
    : "v96", "v97", "v98", "v99", "v100", "v101", "v102", "v103", "v104", "v105", "v106", "v107", "v108", "v109", "v110", "v111", "v112", "v113", "v114", "v115", "v116", "v117", "v118", "v119", "v120", "v121", "v122", "v123", "v124", "v125", "v126", "v127", "v79", "vcc", "scc", "m0", "memory");
}
__device__ __forceinline__ void lean_dm1(const u32x32 (&W)[4], u32x16& pd, unsigned ea, unsigned da, unsigned x, unsigned lb, const char* s0, const char* s1, const unsigned (&av)[8]) {
  asm volatile("s_setprio 2\n\t"
    "ds_read_b128 v[96:99], %[ea] offset:0\n\t"
    "ds_read_b128 v[100:103], %[ea] offset:2048\n\t"
    "ds_read_b128 v[104:107], %[da] offset:0\n\t"
    "ds_read_b128 v[108:111], %[da] offset:2048\n\t"
    "ds_read_b128 v[112:115], %[ea] offset:4096\n\t"
    "ds_read_b128 v[116:119], %[ea] offset:6144\n\t"
    "s_waitcnt lgkmcnt(4)\n\t"
    "v_mfma_f32_16x16x32_bf16 v[80:83], v[128:131], v[96:99], 0\n\t"
    "ds_read_b128 v[120:123], %[da] offset:4096\n\t"
    "s_add_u32 m0, %[lb], 0\n\t"
    "v_mfma_f32_16x16x32_bf16 v[84:87], v[132:135], v[96:99], 0\n\t"
    "ds_read_b128 v[124:127], %[da] offset:6144\n\t"
    "global_load_lds_dwordx4 %[av0], %[s0] sc1\n\t"
    "v_mfma_f32_16x16x32_bf16 v[88:91], v[128:131], v[100:103], 0\n\t"
    "v_mfma_f32_16x16x32_bf16 v[92:95], v[132:135], v[100:103], 0\n\t"
    "s_waitcnt vmcnt(5)\n\t"
    "s_barrier\n\t"
    "s_waitcnt lgkmcnt(4)\n\t"
    "v_mfma_f32_16x16x32_bf16 v[80:83], v[136:139], v[104:107], v[80:83]\n\t"
    "ds_read_b128 v[96:99], %[ea] offset:8192\n\t"
    "s_add_u32 m0, %[lb], 4096\n\t"
    "v_mfma_f32_16x16x32_bf16 v[84:87], v[140:143], v[104:107], v[84:87]\n\t"
    "ds_read_b128 v[100:103], %[ea] offset:10240\n\t"
    "global_load_lds_dwordx4 %[av2], %[s0] sc1\n\t"
    "v_mfma_f32_16x16x32_bf16 v[88:91], v[136:139], v[108:111], v[88:91]\n\t"
    "v_mfma_f32_16x16x32_bf16 v[92:95], v[140:143], v[108:111], v[92:95]\n\t"
    "s_waitcnt lgkmcnt(4)\n\t"
    "v_mfma_f32_16x16x32_bf16 v[80:83], v[144:147], v[112:115], v[80:83]\n\t"
    "ds_read_b128 v[104:107], %[da] offset:8192\n\t"
    "s_add_u32 m0, %[lb], 8192\n\t"
    "v_mfma_f32_16x16x32_bf16 v[84:87], v[148:151], v[112:115], v[84:87]\n\t"
    "ds_read_b128 v[108:111], %[da] offset:10240\n\t"
    "global_load_lds_dwordx4 %[av4], %[s0] sc1\n\t"
    "v_mfma_f32_16x16x32_bf16 v[88:91], v[144:147], v[116:119], v[88:91]\n\t"
    "v_mfma_f32_16x16x32_bf16 v[92:95], v[148:151], v[116:119], v[92:95]\n\t"
    "s_waitcnt lgkmcnt(4)\n\t"
    "v_mfma_f32_16x16x32_bf16 v[80:83], v[152:155], v[120:123], v[80:83]\n\t"
    "ds_read_b128 v[112:115], %[ea] offset:12288\n\t"
    "s_add_u32 m0, %[lb], 12288\n\t"
    "v_mfma_f32_16x16x32_bf16 v[84:87], v[156:159], v[120:123], v[84:87]\n\t"
    "ds_read_b128 v[116:119], %[ea] offset:14336\n\t"
    "global_load_lds_dwordx4 %[av6], %[s0] sc1\n\t"
    "v_mfma_f32_16x16x32_bf16 v[88:91], v[152:155], v[124:127], v[88:91]\n\t"
    "v_mfma_f32_16x16x32_bf16 v[92:95], v[156:159], v[124:127], v[92:95]\n\t"
    "s_waitcnt lgkmcnt(4)\n\t"
    "v_mfma_f32_16x16x32_bf16 v[80:83], v[160:163], v[96:99], v[80:83]\n\t"
    "ds_read_b128 v[120:123], %[da] offset:12288\n\t"
    "s_add_u32 m0, %[lb], 16384\n\t"
    "v_mfma_f32_16x16x32_bf16 v[84:87], v[164:167], v[96:99], v[84:87]\n\t"
    "ds_read_b128 v[124:127], %[da] offset:14336\n\t"
    "global_load_lds_dwordx4 %[av0], %[s1] sc1\n\t"
    "v_mfma_f32_16x16x32_bf16 v[88:91], v[160:163], v[100:103], v[88:91]\n\t"
    "v_mfma_f32_16x16x32_bf16 v[92:95], v[164:167], v[100:103], v[92:95]\n\t"
    "s_waitcnt vmcnt(7)\n\t"
    "s_barrier\n\t"
    "s_waitcnt lgkmcnt(4)\n\t"
    "v_mfma_f32_16x16x32_bf16 v[80:83], v[168:171], v[104:107], v[80:83]\n\t"
    "ds_read_b128 v[96:99], %[ea] offset:16384\n\t"
    "s_add_u32 m0, %[lb], 20480\n\t"
    "v_mfma_f32_16x16x32_bf16 v[84:87], v[172:175], v[104:107], v[84:87]\n\t"
    "ds_read_b128 v[100:103], %[ea] offset:18432\n\t"
    "global_load_lds_dwordx4 %[av2], %[s1] sc1\n\t"
    "v_mfma_f32_16x16x32_bf16 v[88:91], v[168:171], v[108:111], v[88:91]\n\t"
    "v_mfma_f32_16x16x32_bf16 v[92:95], v[172:175], v[108:111], v[92:95]\n\t"
    "s_waitcnt lgkmcnt(4)\n\t"
    "v_mfma_f32_16x16x32_bf16 v[80:83], v[176:179], v[112:115], v[80:83]\n\t"
    "ds_read_b128 v[104:107], %[da] offset:16384\n\t"
    "s_add_u32 m0, %[lb], 24576\n\t"
    "v_mfma_f32_16x16x32_bf16 v[84:87], v[180:183], v[112:115], v[84:87]\n\t"
    "ds_read_b128 v[108:111], %[da] offset:18432\n\t"
    "global_load_lds_dwordx4 %[av4], %[s1] sc1\n\t"
    "v_mfma_f32_16x16x32_bf16 v[88:91], v[176:179], v[116:119], v[88:91]\n\t"
    "v_mfma_f32_16x16x32_bf16 v[92:95], v[180:183], v[116:119], v[92:95]\n\t"
    "s_waitcnt lgkmcnt(4)\n\t"
    "v_mfma_f32_16x16x32_bf16 v[80:83], v[184:187], v[120:123], v[80:83]\n\t"
    "ds_read_b128 v[112:115], %[ea] offset:20480\n\t"
    "s_add_u32 m0, %[lb], 28672\n\t"
    "v_mfma_f32_16x16x32_bf16 v[84:87], v[188:191], v[120:123], v[84:87]\n\t"
    "ds_read_b128 v[116:119], %[ea] offset:22528\n\t"
    "global_load_lds_dwordx4 %[av6], %[s1] sc1\n\t"
    "v_mfma_f32_16x16x32_bf16 v[88:91], v[184:187], v[124:127], v[88:91]\n\t"
    "v_mfma_f32_16x16x32_bf16 v[92:95], v[188:191], v[124:127], v[92:95]\n\t"
    "s_waitcnt lgkmcnt(4)\n\t"
    "v_mfma_f32_16x16x32_bf16 v[80:83], v[192:195], v[96:99], v[80:83]\n\t"
    "ds_read_b128 v[120:123], %[da] offset:20480\n\t"
    "v_mfma_f32_16x16x32_bf16 v[84:87], v[196:199], v[96:99], v[84:87]\n\t"
    "ds_read_b128 v[124:127], %[da] offset:22528\n\t"
    "v_mfma_f32_16x16x32_bf16 v[88:91], v[192:195], v[100:103], v[88:91]\n\t"
    "v_mfma_f32_16x16x32_bf16 v[92:95], v[196:199], v[100:103], v[92:95]\n\t"
    "s_waitcnt vmcnt(8)\n\t"
    "s_barrier\n\t"
    "s_waitcnt lgkmcnt(4)\n\t"
    "v_mfma_f32_16x16x32_bf16 v[80:83], v[200:203], v[104:107], v[80:83]\n\t"
    "ds_read_b128 v[96:99], %[ea] offset:24576\n\t"
    "v_mfma_f32_16x16x32_bf16 v[84:87], v[204:207], v[104:107], v[84:87]\n\t"
    "ds_read_b128 v[100:103], %[ea] offset:26624\n\t"
    "v_mfma_f32_16x16x32_bf16 v[88:91], v[200:203], v[108:111], v[88:91]\n\t"
    "v_mfma_f32_16x16x32_bf16 v[92:95], v[204:207], v[108:111], v[92:95]\n\t"
    "s_waitcnt lgkmcnt(4)\n\t"
    "v_mfma_f32_16x16x32_bf16 v[80:83], v[208:211], v[112:115], v[80:83]\n\t"
    "ds_read_b128 v[104:107], %[da] offset:24576\n\t"
    "v_mfma_f32_16x16x32_bf16 v[84:87], v[212:215], v[112:115], v[84:87]\n\t"
    "ds_read_b128 v[108:111], %[da] offset:26624\n\t"
    "v_mfma_f32_16x16x32_bf16 v[88:91], v[208:211], v[116:119], v[88:91]\n\t"
    "v_mfma_f32_16x16x32_bf16 v[92:95], v[212:215], v[116:119], v[92:95]\n\t"
    "s_waitcnt lgkmcnt(4)\n\t"
    "v_mfma_f32_16x16x32_bf16 v[80:83], v[216:219], v[120:123], v[80:83]\n\t"
    "ds_read_b128 v[112:115], %[ea] offset:28672\n\t"
    "v_mfma_f32_16x16x32_bf16 v[84:87], v[220:223], v[120:123], v[84:87]\n\t"
    "ds_read_b128 v[116:119], %[ea] offset:30720\n\t"
    "v_mfma_f32_16x16x32_bf16 v[88:91], v[216:219], v[124:127], v[88:91]\n\t"
    "v_mfma_f32_16x16x32_bf16 v[92:95], v[220:223], v[124:127], v[92:95]\n\t"
    "s_waitcnt lgkmcnt(4)\n\t"
    "v_mfma_f32_16x16x32_bf16 v[80:83], v[224:227], v[96:99], v[80:83]\n\t"
    "ds_read_b128 v[120:123], %[da] offset:28672\n\t"
    "v_mfma_f32_16x16x32_bf16 v[84:87], v[228:231], v[96:99], v[84:87]\n\t"
    "ds_read_b128 v[124:127], %[da] offset:30720\n\t"
    "v_mfma_f32_16x16x32_bf16 v[88:91], v[224:227], v[100:103], v[88:91]\n\t"
    "v_mfma_f32_16x16x32_bf16 v[92:95], v[228:231], v[100:103], v[92:95]\n\t"
    "s_waitcnt lgkmcnt(4)\n\t"
    "v_mfma_f32_16x16x32_bf16 v[80:83], v[232:235], v[104:107], v[80:83]\n\t"
    "v_mfma_f32_16x16x32_bf16 v[84:87], v[236:239], v[104:107], v[84:87]\n\t"
    "v_mfma_f32_16x16x32_bf16 v[88:91], v[232:235], v[108:111], v[88:91]\n\t"
    "v_mfma_f32_16x16x32_bf16 v[92:95], v[236:239], v[108:111], v[92:95]\n\t"
    "s_waitcnt lgkmcnt(2)\n\t"
    "v_mfma_f32_16x16x32_bf16 v[80:83], v[240:243], v[112:115], v[80:83]\n\t"
    "v_mfma_f32_16x16x32_bf16 v[84:87], v[244:247], v[112:115], v[84:87]\n\t"
    "v_mfma_f32_16x16x32_bf16 v[88:91], v[240:243], v[116:119], v[88:91]\n\t"
    "v_mfma_f32_16x16x32_bf16 v[92:95], v[244:247], v[116:119], v[92:95]\n\t"
    "s_waitcnt lgkmcnt(0)\n\t"
    "v_mfma_f32_16x16x32_bf16 v[80:83], v[248:251], v[120:123], v[80:83]\n\t"
    "v_mfma_f32_16x16x32_bf16 v[84:87], v[252:255], v[120:123], v[84:87]\n\t"
    "v_mfma_f32_16x16x32_bf16 v[88:91], v[248:251], v[124:127], v[88:91]\n\t"
    "v_mfma_f32_16x16x32_bf16 v[92:95], v[252:255], v[124:127], v[92:95]\n\t"
    "s_nop 6\n\t"
    "s_branch 9f\n\t"
    "3:\n\t"
    "9:"
    : "=&{v[80:95]}"(pd)
    : [ea] "v"(ea), [da] "v"(da), [x] "s"(x), "{v[128:159]}"(W[0]), "{v[160:191]}"(W[1]), "{v[192:223]}"(W[2]), "{v[224:255]}"(W[3]), [lb] "s"(lb), [s0] "s"(s0), [s1] "s"(s1), [av0] "v"(av[0]), [av1] "v"(av[1]), [av2] "v"(av[2]), [av3] "v"(av[3]), [av4] "v"(av[4]), [av5] "v"(av[5]), [av6] "v"(av[6]), [av7] "v"(av[7])
    : "v96", "v97", "v98", "v99", "v100", "v101", "v102", "v103", "v104", "v105", "v106", "v107", "v108", "v109", "v110", "v111", "v112", "v113", "v114", "v115", "v116", "v117", "v118", "v119", "v120", "v121", "v122", "v123", "v124", "v125", "v126", "v127", "vcc", "scc", "m0", "memory");
}
__device__ __forceinline__ void lean_st0(const u32x32 (&W)[4], u32x16& pd, unsigned ea, unsigned da, unsigned x, unsigned pv, unsigned cn, unsigned sl, unsigned dv, const char* db, const char* pg) {
  unsigned st;
  asm volatile("ds_read_b128 v[96:99], %[ea] offset:0\n\t"
    "ds_read_b128 v[100:103], %[ea] offset:2048\n\t"
    "ds_read_b128 v[104:107], %[da] offset:0\n\t"
    "ds_read_b128 v[108:111], %[da] offset:2048\n\t"
    "ds_read_b128 v[112:115], %[ea] offset:4096\n\t"
    "ds_read_b128 v[116:119], %[ea] offset:6144\n\t"
    "s_waitcnt lgkmcnt(4)\n\t"
    "v_mfma_f32_16x16x32_bf16 v[80:83], v[128:131], v[96:99], 0\n\t"
    "ds_read_b128 v[120:123], %[da] offset:4096\n\t"
    "v_mfma_f32_16x16x32_bf16 v[84:87], v[132:135], v[96:99], 0\n\t"
    "ds_read_b128 v[124:127], %[da] offset:6144\n\t"
    "v_mfma_f32_16x16x32_bf16 v[88:91], v[128:131], v[100:103], 0\n\t"
    "v_mfma_f32_16x16x32_bf16 v[92:95], v[132:135], v[100:103], 0\n\t"
    "s_barrier\n\t"
    "s_waitcnt lgkmcnt(4)\n\t"
    "v_mfma_f32_16x16x32_bf16 v[80:83], v[136:139], v[104:107], v[80:83]\n\t"
    "ds_read_b128 v[96:99], %[ea] offset:8192\n\t"
    "v_mfma_f32_16x16x32_bf16 v[84:87], v[140:143], v[104:107], v[84:87]\n\t"
    "ds_read_b128 v[100:103], %[ea] offset:10240\n\t"
    "v_mfma_f32_16x16x32_bf16 v[88:91], v[136:139], v[108:111], v[88:91]\n\t"
    "v_mfma_f32_16x16x32_bf16 v[92:95], v[140:143], v[108:111], v[92:95]\n\t"
    "s_waitcnt lgkmcnt(4)\n\t"
    "v_mfma_f32_16x16x32_bf16 v[80:83], v[144:147], v[112:115], v[80:83]\n\t"
    "ds_read_b128 v[104:107], %[da] offset:8192\n\t"
    "v_mfma_f32_16x16x32_bf16 v[84:87], v[148:151], v[112:115], v[84:87]\n\t"
    "ds_read_b128 v[108:111], %[da] offset:10240\n\t"
    "v_mfma_f32_16x16x32_bf16 v[88:91], v[144:147], v[116:119], v[88:91]\n\t"
    "v_mfma_f32_16x16x32_bf16 v[92:95], v[148:151], v[116:119], v[92:95]\n\t"
    "s_waitcnt lgkmcnt(4)\n\t"
    "v_mfma_f32_16x16x32_bf16 v[80:83], v[152:155], v[120:123], v[80:83]\n\t"
    "ds_read_b128 v[112:115], %[ea] offset:12288\n\t"
    "v_mfma_f32_16x16x32_bf16 v[84:87], v[156:159], v[120:123], v[84:87]\n\t"
    "ds_read_b128 v[116:119], %[ea] offset:14336\n\t"
    "v_mfma_f32_16x16x32_bf16 v[88:91], v[152:155], v[124:127], v[88:91]\n\t"
    "v_mfma_f32_16x16x32_bf16 v[92:95], v[156:159], v[124:127], v[92:95]\n\t"
    "s_bitcmp1_b32 %[x], 2\n\t"
    "s_cbranch_scc1 7f\n\t"
    "8:\n\t"
    "s_waitcnt lgkmcnt(4)\n\t"
    "v_mfma_f32_16x16x32_bf16 v[80:83], v[160:163], v[96:99], v[80:83]\n\t"
    "ds_read_b128 v[120:123], %[da] offset:12288\n\t"
    "v_mfma_f32_16x16x32_bf16 v[84:87], v[164:167], v[96:99], v[84:87]\n\t"
    "ds_read_b128 v[124:127], %[da] offset:14336\n\t"
    "v_mfma_f32_16x16x32_bf16 v[88:91], v[160:163], v[100:103], v[88:91]\n\t"
    "v_mfma_f32_16x16x32_bf16 v[92:95], v[164:167], v[100:103], v[92:95]\n\t"
    "s_barrier\n\t"
    "s_waitcnt lgkmcnt(4)\n\t"
    "v_mfma_f32_16x16x32_bf16 v[80:83], v[168:171], v[104:107], v[80:83]\n\t"
    "ds_read_b128 v[96:99], %[ea] offset:16384\n\t"
    "v_mfma_f32_16x16x32_bf16 v[84:87], v[172:175], v[104:107], v[84:87]\n\t"
    "ds_read_b128 v[100:103], %[ea] offset:18432\n\t"
    "v_mfma_f32_16x16x32_bf16 v[88:91], v[168:171], v[108:111], v[88:91]\n\t"
    "v_mfma_f32_16x16x32_bf16 v[92:95], v[172:175], v[108:111], v[92:95]\n\t"
    "s_waitcnt lgkmcnt(4)\n\t"
    "v_mfma_f32_16x16x32_bf16 v[80:83], v[176:179], v[112:115], v[80:83]\n\t"
    "ds_read_b128 v[104:107], %[da] offset:16384\n\t"
    "v_mfma_f32_16x16x32_bf16 v[84:87], v[180:183], v[112:115], v[84:87]\n\t"
    "ds_read_b128 v[108:111], %[da] offset:18432\n\t"
    "v_mfma_f32_16x16x32_bf16 v[88:91], v[176:179], v[116:119], v[88:91]\n\t"
    "v_mfma_f32_16x16x32_bf16 v[92:95], v[180:183], v[116:119], v[92:95]\n\t"
    "s_waitcnt lgkmcnt(4)\n\t"
    "v_mfma_f32_16x16x32_bf16 v[80:83], v[184:187], v[120:123], v[80:83]\n\t"
    "ds_read_b128 v[112:115], %[ea] offset:20480\n\t"
    "v_mfma_f32_16x16x32_bf16 v[84:87], v[188:191], v[120:123], v[84:87]\n\t"
    "ds_read_b128 v[116:119], %[ea] offset:22528\n\t"
    "v_mfma_f32_16x16x32_bf16 v[88:91], v[184:187], v[124:127], v[88:91]\n\t"
    "v_mfma_f32_16x16x32_bf16 v[92:95], v[188:191], v[124:127], v[92:95]\n\t"
    "s_waitcnt lgkmcnt(4)\n\t"
    "v_mfma_f32_16x16x32_bf16 v[80:83], v[192:195], v[96:99], v[80:83]\n\t"
    "ds_read_b128 v[120:123], %[da] offset:20480\n\t"
    "v_mfma_f32_16x16x32_bf16 v[84:87], v[196:199], v[96:99], v[84:87]\n\t"
    "ds_read_b128 v[124:127], %[da] offset:22528\n\t"
    "v_mfma_f32_16x16x32_bf16 v[88:91], v[192:195], v[100:103], v[88:91]\n\t"
    "v_mfma_f32_16x16x32_bf16 v[92:95], v[196:199], v[100:103], v[92:95]\n\t"
    "s_barrier\n\t"
    "s_and_b32 %[st], %[x], 3\n\t"
    "s_cbranch_scc1 3f\n\t"
    "4:\n\t"
    "s_waitcnt lgkmcnt(4)\n\t"
    "v_mfma_f32_16x16x32_bf16 v[80:83], v[200:203], v[104:107], v[80:83]\n\t"
    "ds_read_b128 v[96:99], %[ea] offset:24576\n\t"
    "v_mfma_f32_16x16x32_bf16 v[84:87], v[204:207], v[104:107], v[84:87]\n\t"
    "ds_read_b128 v[100:103], %[ea] offset:26624\n\t"
    "v_mfma_f32_16x16x32_bf16 v[88:91], v[200:203], v[108:111], v[88:91]\n\t"
    "v_mfma_f32_16x16x32_bf16 v[92:95], v[204:207], v[108:111], v[92:95]\n\t"
    "s_waitcnt lgkmcnt(4)\n\t"
    "v_mfma_f32_16x16x32_bf16 v[80:83], v[208:211], v[112:115], v[80:83]\n\t"
    "ds_read_b128 v[104:107], %[da] offset:24576\n\t"
    "v_mfma_f32_16x16x32_bf16 v[84:87], v[212:215], v[112:115], v[84:87]\n\t"
    "ds_read_b128 v[108:111], %[da] offset:26624\n\t"
    "v_mfma_f32_16x16x32_bf16 v[88:91], v[208:211], v[116:119], v[88:91]\n\t"
    "v_mfma_f32_16x16x32_bf16 v[92:95], v[212:215], v[116:119], v[92:95]\n\t"
    "s_waitcnt lgkmcnt(4)\n\t"
    "v_mfma_f32_16x16x32_bf16 v[80:83], v[216:219], v[120:123], v[80:83]\n\t"
    "ds_read_b128 v[112:115], %[ea] offset:28672\n\t"
    "v_mfma_f32_16x16x32_bf16 v[84:87], v[220:223], v[120:123], v[84:87]\n\t"
    "ds_read_b128 v[116:119], %[ea] offset:30720\n\t"
    "v_mfma_f32_16x16x32_bf16 v[88:91], v[216:219], v[124:127], v[88:91]\n\t"
    "v_mfma_f32_16x16x32_bf16 v[92:95], v[220:223], v[124:127], v[92:95]\n\t"
    "s_waitcnt lgkmcnt(4)\n\t"
    "v_mfma_f32_16x16x32_bf16 v[80:83], v[224:227], v[96:99], v[80:83]\n\t"
    "ds_read_b128 v[120:123], %[da] offset:28672\n\t"
    "v_mfma_f32_16x16x32_bf16 v[84:87], v[228:231], v[96:99], v[84:87]\n\t"
    "ds_read_b128 v[124:127], %[da] offset:30720\n\t"
    "v_mfma_f32_16x16x32_bf16 v[88:91], v[224:227], v[100:103], v[88:91]\n\t"
    "v_mfma_f32_16x16x32_bf16 v[92:95], v[228:231], v[100:103], v[92:95]\n\t"
    "s_waitcnt lgkmcnt(4)\n\t"
    "v_mfma_f32_16x16x32_bf16 v[80:83], v[232:235], v[104:107], v[80:83]\n\t"
    "v_mfma_f32_16x16x32_bf16 v[84:87], v[236:239], v[104:107], v[84:87]\n\t"
    "v_mfma_f32_16x16x32_bf16 v[88:91], v[232:235], v[108:111], v[88:91]\n\t"
    "v_mfma_f32_16x16x32_bf16 v[92:95], v[236:239], v[108:111], v[92:95]\n\t"
    "s_waitcnt lgkmcnt(2)\n\t"
    "v_mfma_f32_16x16x32_bf16 v[80:83], v[240:243], v[112:115], v[80:83]\n\t"
    "v_mfma_f32_16x16x32_bf16 v[84:87], v[244:247], v[112:115], v[84:87]\n\t"
    "v_mfma_f32_16x16x32_bf16 v[88:91], v[240:243], v[116:119], v[88:91]\n\t"
    "v_mfma_f32_16x16x32_bf16 v[92:95], v[244:247], v[116:119], v[92:95]\n\t"
    "s_waitcnt lgkmcnt(0)\n\t"
    "v_mfma_f32_16x16x32_bf16 v[80:83], v[248:251], v[120:123], v[80:83]\n\t"
    "v_mfma_f32_16x16x32_bf16 v[84:87], v[252:255], v[120:123], v[84:87]\n\t"
    "v_mfma_f32_16x16x32_bf16 v[88:91], v[248:251], v[124:127], v[88:91]\n\t"
    "v_mfma_f32_16x16x32_bf16 v[92:95], v[252:255], v[124:127], v[92:95]\n\t"
    "s_nop 6\n\t"
    "s_branch 9f\n\t"
    "3:\n\t"
    "s_bitcmp0_b32 %[x], 0\n\t"
    "s_cbranch_scc1 5f\n\t"
    "7:\n\t"
    "v_mov_b32 v76, %[cn]\n\t"
    "v_mov_b32 v77, 1\n\t"
    "v_mov_b32 v78, %[pv]\n\t"
    "v_mov_b32 v79, 0\n\t"
    "s_waitcnt vmcnt(0)\n\t"
    "s_mov_b64 exec, 1\n\t"
    "ds_add_rtn_u32 v77, v76, v77\n\t"
    "s_waitcnt lgkmcnt(0)\n\t"
    "v_readfirstlane_b32 %[st], v77\n\t"
    "s_and_b32 %[st], %[st], 3\n\t"
    "s_cmp_lg_u32 %[st], 3\n\t"
    "s_cbranch_scc1 6f\n\t"
    "global_store_dword v79, v78, %[pg] sc1\n\t"
    "6:\n\t"
    "s_mov_b64 exec, -1\n\t"
    "s_bitcmp1_b32 %[x], 2\n\t"
    "s_cbranch_scc1 8b\n\t"
    "5:\n\t"
    "s_bitcmp0_b32 %[x], 1\n\t"
    "s_cbranch_scc1 4b\n\t"
    "s_mov_b32 m0, %[sl]\n\t"
    "s_nop 0\n\t"
    "global_load_lds_dword %[dv], %[db] sc1\n\t"
    "s_branch 4b\n\t"
    "9:"
    : "=&{v[80:95]}"(pd), [st] "=&s"(st)
    : [ea] "v"(ea), [da] "v"(da), [x] "s"(x), "{v[128:159]}"(W[0]), "{v[160:191]}"(W[1]), "{v[192:223]}"(W[2]), "{v[224:255]}"(W[3]), [pv] "s"(pv), [cn] "s"(cn), [sl] "s"(sl), [dv] "v"(dv), [db] "s"(db), [pg] "s"(pg)
    : "v96", "v97", "v98", "v99", "v100", "v101", "v102", "v103", "v104", "v105", "v106", "v107", "v108", "v109", "v110", "v111", "v112", "v113", "v114", "v115", "v116", "v117", "v118", "v119", "v120", "v121", "v122", "v123", "v124", "v125", "v126", "v127", "v76", "v77", "v78", "v79", "vcc", "scc", "m0", "memory");
}
#define LEAN_QB 1
__device__ __forceinline__ void paced_ld0(const u32x32 (&W)[4], unsigned ea, unsigned da, int b, int ns, unsigned ab, unsigned rw, unsigned fa,
    const char* p0, const char* p1, unsigned lb, int& cap, int wm, unsigned rq, unsigned mu, unsigned ad, unsigned dv, const char* db, unsigned sl, unsigned sa, const unsigned (&av)[8]) {
  unsigned st;
  asm volatile("s_setprio 2\n\t"
    "v_mov_b32 v78, %[wm]\n\t"
    "s_cmp_lg_u32 %[rq], 0\n\t"
    "s_cbranch_scc1 3f\n\t"
    "2:\n\t"
    "v_cmp_ge_i32 vcc, %[b], v78\n\t"
    "s_cbranch_vccz 4f\n\t"
    "21:\n\t"
    "s_cmp_eq_u32 %[sl], 0\n\t"
    "s_cbranch_scc1 71f\n\t"
    "s_mov_b32 m0, %[sl]\n\t"
    "s_nop 0\n\t"
    "global_load_lds_dword %[dv], %[db] sc1\n\t"
    "71:\n\t"
    "s_sub_u32 %[cap], %[cap], 1\n\t"
    "s_max_i32 %[cap], %[cap], 0\n\t"
    "s_waitcnt vmcnt(0)\n\t"
    "ds_read_b32 v79, %[sa]\n\t"
    "s_waitcnt lgkmcnt(0)\n\t"
    "v_mad_u32_u24 v79, v79, %[mu], %[ad]\n\t"
    "v_max_i32 v78, v78, v79\n\t"
    "v_cmp_ge_i32 vcc, %[b], v78\n\t"
    "s_cbranch_vccz 4f\n\t"
    "s_sleep 1\n\t"
    "s_cmp_lg_u32 %[cap], 0\n\t"
    "s_cbranch_scc1 21b\n\t"
    "4:\n\t"
    "s_add_u32 m0, %[lb], 0\n\t"
    "s_nop 0\n\t"
    "global_load_lds_dwordx4 %[av0], s[36:37] sc1\n\t"
    "s_add_u32 m0, %[lb], 32768\n\t"
    "s_nop 0\n\t"
    "global_load_lds_dwordx4 %[av1], s[36:37] sc1\n\t"
    "s_add_u32 m0, %[lb], 4096\n\t"
    "s_nop 0\n\t"
    "global_load_lds_dwordx4 %[av2], s[36:37] sc1\n\t"
    "s_add_u32 m0, %[lb], 36864\n\t"
    "s_nop 0\n\t"
    "global_load_lds_dwordx4 %[av3], s[36:37] sc1\n\t"
    "s_add_u32 m0, %[lb], 8192\n\t"
    "s_nop 0\n\t"
    "global_load_lds_dwordx4 %[av4], s[36:37] sc1\n\t"
    "s_add_u32 m0, %[lb], 40960\n\t"
    "s_nop 0\n\t"
    "global_load_lds_dwordx4 %[av5], s[36:37] sc1\n\t"
    "s_add_u32 m0, %[lb], 12288\n\t"
    "s_nop 0\n\t"
    "global_load_lds_dwordx4 %[av6], s[36:37] sc1\n\t"
    "s_add_u32 m0, %[lb], 45056\n\t"
    "s_nop 0\n\t"
    "global_load_lds_dwordx4 %[av7], s[36:37] sc1\n\t"
    "s_add_u32 m0, %[lb], 16384\n\t"
    "s_nop 0\n\t"
    "global_load_lds_dwordx4 %[av0], s[38:39] sc1\n\t"
    "s_add_u32 m0, %[lb], 49152\n\t"
    "s_nop 0\n\t"
    "global_load_lds_dwordx4 %[av1], s[38:39] sc1\n\t"
    "s_add_u32 m0, %[lb], 20480\n\t"
    "s_nop 0\n\t"
    "global_load_lds_dwordx4 %[av2], s[38:39] sc1\n\t"
    "s_add_u32 m0, %[lb], 53248\n\t"
    "s_nop 0\n\t"
    "global_load_lds_dwordx4 %[av3], s[38:39] sc1\n\t"
    "s_add_u32 m0, %[lb], 24576\n\t"
    "s_nop 0\n\t"
    "global_load_lds_dwordx4 %[av4], s[38:39] sc1\n\t"
    "s_add_u32 m0, %[lb], 57344\n\t"
    "s_nop 0\n\t"
    "global_load_lds_dwordx4 %[av5], s[38:39] sc1\n\t"
    "s_add_u32 m0, %[lb], 28672\n\t"
    "s_nop 0\n\t"
    "global_load_lds_dwordx4 %[av6], s[38:39] sc1\n\t"
    "s_add_u32 m0, %[lb], 61440\n\t"
    "s_nop 0\n\t"
    "global_load_lds_dwordx4 %[av7], s[38:39] sc1\n\t"
    "s_add_u32 s36, s36, %[ab]\n\t"
    "s_addc_u32 s37, s37, 0\n\t"
    "s_add_u32 s38, s38, %[ab]\n\t"
    "s_addc_u32 s39, s39, 0\n\t"
    "s_xor_b32 %[lb], %[lb], 0x10000\n\t"
    "3:\n\t"
    "s_add_u32 %[st], %[b], 1\n\t"
    "v_min_i32 v78, %[ns], v78\n\t"
    "v_cmp_ge_i32 vcc, %[st], v78\n\t"
    "s_waitcnt vmcnt(12) lgkmcnt(0)\n\t"
    "s_barrier\n\t"
    "ds_read_b128 v[96:99], %[ea] offset:0\n\t"
    "ds_read_b128 v[100:103], %[ea] offset:2048\n\t"
    "ds_read_b128 v[104:107], %[da] offset:0\n\t"
    "ds_read_b128 v[108:111], %[da] offset:2048\n\t"
    "ds_read_b128 v[112:115], %[ea] offset:4096\n\t"
    "ds_read_b128 v[116:119], %[ea] offset:6144\n\t"
    "s_cbranch_vccz 5f\n\t"
    "s_waitcnt lgkmcnt(4)\n\t"
    "v_mfma_f32_16x16x32_bf16 v[80:83], v[128:131], v[96:99], 0\n\t"
    "ds_read_b128 v[120:123], %[da] offset:4096\n\t"
    "v_mfma_f32_16x16x32_bf16 v[84:87], v[132:135], v[96:99], 0\n\t"
    "ds_read_b128 v[124:127], %[da] offset:6144\n\t"
    "v_mfma_f32_16x16x32_bf16 v[88:91], v[128:131], v[100:103], 0\n\t"
    "v_mfma_f32_16x16x32_bf16 v[92:95], v[132:135], v[100:103], 0\n\t"
    "s_waitcnt vmcnt(8)\n\t"
    "s_barrier\n\t"
    "s_waitcnt lgkmcnt(4)\n\t"
    "v_mfma_f32_16x16x32_bf16 v[80:83], v[136:139], v[104:107], v[80:83]\n\t"
    "ds_read_b128 v[96:99], %[ea] offset:8192\n\t"
    "v_mfma_f32_16x16x32_bf16 v[84:87], v[140:143], v[104:107], v[84:87]\n\t"
    "ds_read_b128 v[100:103], %[ea] offset:10240\n\t"
    "v_mfma_f32_16x16x32_bf16 v[88:91], v[136:139], v[108:111], v[88:91]\n\t"
    "v_mfma_f32_16x16x32_bf16 v[92:95], v[140:143], v[108:111], v[92:95]\n\t"
    "s_waitcnt lgkmcnt(4)\n\t"
    "v_mfma_f32_16x16x32_bf16 v[80:83], v[144:147], v[112:115], v[80:83]\n\t"
    "ds_read_b128 v[104:107], %[da] offset:8192\n\t"
    "v_mfma_f32_16x16x32_bf16 v[84:87], v[148:151], v[112:115], v[84:87]\n\t"
    "ds_read_b128 v[108:111], %[da] offset:10240\n\t"
    "v_mfma_f32_16x16x32_bf16 v[88:91], v[144:147], v[116:119], v[88:91]\n\t"
    "v_mfma_f32_16x16x32_bf16 v[92:95], v[148:151], v[116:119], v[92:95]\n\t"
    "s_waitcnt lgkmcnt(4)\n\t"
    "v_mfma_f32_16x16x32_bf16 v[80:83], v[152:155], v[120:123], v[80:83]\n\t"
    "ds_read_b128 v[112:115], %[ea] offset:12288\n\t"
    "v_mfma_f32_16x16x32_bf16 v[84:87], v[156:159], v[120:123], v[84:87]\n\t"
    "ds_read_b128 v[116:119], %[ea] offset:14336\n\t"
    "v_mfma_f32_16x16x32_bf16 v[88:91], v[152:155], v[124:127], v[88:91]\n\t"
    "v_mfma_f32_16x16x32_bf16 v[92:95], v[156:159], v[124:127], v[92:95]\n\t"
    "s_waitcnt lgkmcnt(4)\n\t"
    "v_mfma_f32_16x16x32_bf16 v[80:83], v[160:163], v[96:99], v[80:83]\n\t"
    "ds_read_b128 v[120:123], %[da] offset:12288\n\t"
    "v_mfma_f32_16x16x32_bf16 v[84:87], v[164:167], v[96:99], v[84:87]\n\t"
    "ds_read_b128 v[124:127], %[da] offset:14336\n\t"
    "v_mfma_f32_16x16x32_bf16 v[88:91], v[160:163], v[100:103], v[88:91]\n\t"
    "v_mfma_f32_16x16x32_bf16 v[92:95], v[164:167], v[100:103], v[92:95]\n\t"
    "s_waitcnt vmcnt(4)\n\t"
    "s_barrier\n\t"
    "s_waitcnt lgkmcnt(4)\n\t"
    "v_mfma_f32_16x16x32_bf16 v[80:83], v[168:171], v[104:107], v[80:83]\n\t"
    "ds_read_b128 v[96:99], %[ea] offset:16384\n\t"
    "v_mfma_f32_16x16x32_bf16 v[84:87], v[172:175], v[104:107], v[84:87]\n\t"
    "ds_read_b128 v[100:103], %[ea] offset:18432\n\t"
    "v_mfma_f32_16x16x32_bf16 v[88:91], v[168:171], v[108:111], v[88:91]\n\t"
    "v_mfma_f32_16x16x32_bf16 v[92:95], v[172:175], v[108:111], v[92:95]\n\t"
    "s_waitcnt lgkmcnt(4)\n\t"
    "v_mfma_f32_16x16x32_bf16 v[80:83], v[176:179], v[112:115], v[80:83]\n\t"
    "ds_read_b128 v[104:107], %[da] offset:16384\n\t"
    "v_mfma_f32_16x16x32_bf16 v[84:87], v[180:183], v[112:115], v[84:87]\n\t"
    "ds_read_b128 v[108:111], %[da] offset:18432\n\t"
    "v_mfma_f32_16x16x32_bf16 v[88:91], v[176:179], v[116:119], v[88:91]\n\t"
    "v_mfma_f32_16x16x32_bf16 v[92:95], v[180:183], v[116:119], v[92:95]\n\t"
    "s_waitcnt lgkmcnt(4)\n\t"
    "v_mfma_f32_16x16x32_bf16 v[80:83], v[184:187], v[120:123], v[80:83]\n\t"
    "ds_read_b128 v[112:115], %[ea] offset:20480\n\t"
    "v_mfma_f32_16x16x32_bf16 v[84:87], v[188:191], v[120:123], v[84:87]\n\t"
    "ds_read_b128 v[116:119], %[ea] offset:22528\n\t"
    "v_mfma_f32_16x16x32_bf16 v[88:91], v[184:187], v[124:127], v[88:91]\n\t"
    "v_mfma_f32_16x16x32_bf16 v[92:95], v[188:191], v[124:127], v[92:95]\n\t"
    "s_waitcnt lgkmcnt(4)\n\t"
    "v_mfma_f32_16x16x32_bf16 v[80:83], v[192:195], v[96:99], v[80:83]\n\t"
    "ds_read_b128 v[120:123], %[da] offset:20480\n\t"
    "v_mfma_f32_16x16x32_bf16 v[84:87], v[196:199], v[96:99], v[84:87]\n\t"
    "ds_read_b128 v[124:127], %[da] offset:22528\n\t"
    "v_mfma_f32_16x16x32_bf16 v[88:91], v[192:195], v[100:103], v[88:91]\n\t"
    "v_mfma_f32_16x16x32_bf16 v[92:95], v[196:199], v[100:103], v[92:95]\n\t"
    "s_waitcnt vmcnt(0)\n\t"
    "s_barrier\n\t"
    "s_waitcnt lgkmcnt(4)\n\t"
    "v_mfma_f32_16x16x32_bf16 v[80:83], v[200:203], v[104:107], v[80:83]\n\t"
    "ds_read_b128 v[96:99], %[ea] offset:24576\n\t"
    "v_mfma_f32_16x16x32_bf16 v[84:87], v[204:207], v[104:107], v[84:87]\n\t"
    "ds_read_b128 v[100:103], %[ea] offset:26624\n\t"
    "v_mfma_f32_16x16x32_bf16 v[88:91], v[200:203], v[108:111], v[88:91]\n\t"
    "v_mfma_f32_16x16x32_bf16 v[92:95], v[204:207], v[108:111], v[92:95]\n\t"
    "s_waitcnt lgkmcnt(4)\n\t"
    "v_mfma_f32_16x16x32_bf16 v[80:83], v[208:211], v[112:115], v[80:83]\n\t"
    "ds_read_b128 v[104:107], %[da] offset:24576\n\t"
    "v_mfma_f32_16x16x32_bf16 v[84:87], v[212:215], v[112:115], v[84:87]\n\t"
    "ds_read_b128 v[108:111], %[da] offset:26624\n\t"
    "v_mfma_f32_16x16x32_bf16 v[88:91], v[208:211], v[116:119], v[88:91]\n\t"
    "v_mfma_f32_16x16x32_bf16 v[92:95], v[212:215], v[116:119], v[92:95]\n\t"
    "s_cmp_eq_u32 %[sl], 0\n\t"
    "s_cbranch_scc1 72f\n\t"
    "s_mov_b32 m0, %[sl]\n\t"
    "s_nop 0\n\t"
    "global_load_lds_dword %[dv], %[db] sc1\n\t"
    "72:\n\t"
    "s_waitcnt lgkmcnt(4)\n\t"
    "v_mfma_f32_16x16x32_bf16 v[80:83], v[216:219], v[120:123], v[80:83]\n\t"
    "ds_read_b128 v[112:115], %[ea] offset:28672\n\t"
    "v_mfma_f32_16x16x32_bf16 v[84:87], v[220:223], v[120:123], v[84:87]\n\t"
    "ds_read_b128 v[116:119], %[ea] offset:30720\n\t"
    "v_mfma_f32_16x16x32_bf16 v[88:91], v[216:219], v[124:127], v[88:91]\n\t"
    "v_mfma_f32_16x16x32_bf16 v[92:95], v[220:223], v[124:127], v[92:95]\n\t"
    "s_waitcnt lgkmcnt(4)\n\t"
    "v_mfma_f32_16x16x32_bf16 v[80:83], v[224:227], v[96:99], v[80:83]\n\t"
    "ds_read_b128 v[120:123], %[da] offset:28672\n\t"
    "v_mfma_f32_16x16x32_bf16 v[84:87], v[228:231], v[96:99], v[84:87]\n\t"
    "ds_read_b128 v[124:127], %[da] offset:30720\n\t"
    "v_mfma_f32_16x16x32_bf16 v[88:91], v[224:227], v[100:103], v[88:91]\n\t"
    "v_mfma_f32_16x16x32_bf16 v[92:95], v[228:231], v[100:103], v[92:95]\n\t"
    "s_waitcnt lgkmcnt(4)\n\t"
    "v_mfma_f32_16x16x32_bf16 v[80:83], v[232:235], v[104:107], v[80:83]\n\t"
    "v_mfma_f32_16x16x32_bf16 v[84:87], v[236:239], v[104:107], v[84:87]\n\t"
    "v_mfma_f32_16x16x32_bf16 v[88:91], v[232:235], v[108:111], v[88:91]\n\t"
    "v_xor_b32 %[ea], 0x10000, %[ea]\n\t"
    "v_mfma_f32_16x16x32_bf16 v[92:95], v[236:239], v[108:111], v[92:95]\n\t"
    "v_xor_b32 %[da], 0x10000, %[da]\n\t"
    "s_waitcnt lgkmcnt(2)\n\t"
    "v_mfma_f32_16x16x32_bf16 v[80:83], v[240:243], v[112:115], v[80:83]\n\t"
    "v_mfma_f32_16x16x32_bf16 v[84:87], v[244:247], v[112:115], v[84:87]\n\t"
    "v_mfma_f32_16x16x32_bf16 v[88:91], v[240:243], v[116:119], v[88:91]\n\t"
    "s_add_u32 %[b], %[b], 1\n\t"
    "v_mfma_f32_16x16x32_bf16 v[92:95], v[244:247], v[116:119], v[92:95]\n\t"
    "s_waitcnt lgkmcnt(0)\n\t"
    "v_mfma_f32_16x16x32_bf16 v[80:83], v[248:251], v[120:123], v[80:83]\n\t"
    "v_mfma_f32_16x16x32_bf16 v[84:87], v[252:255], v[120:123], v[84:87]\n\t"
    "v_mfma_f32_16x16x32_bf16 v[88:91], v[248:251], v[124:127], v[88:91]\n\t"
    "v_mfma_f32_16x16x32_bf16 v[92:95], v[252:255], v[124:127], v[92:95]\n\t"
    "s_nop 4\n\t"
    "ds_write_b128 %[rw], v[80:83] offset:0\n\t"
    "ds_write_b128 %[rw], v[84:87] offset:1024\n\t"
    "ds_write_b128 %[rw], v[88:91] offset:2048\n\t"
    "ds_write_b128 %[rw], v[92:95] offset:3072\n\t"
    "s_waitcnt vmcnt(0)\n\t"
    "ds_read_b32 v79, %[sa]\n\t"
    "s_waitcnt lgkmcnt(0)\n\t"
    "v_mad_u32_u24 v79, v79, %[mu], %[ad]\n\t"
    "v_max_i32 v78, v78, v79\n\t"
    "v_mov_b32 v77, %[b]\n\t"
    "v_cmp_ge_i32 vcc, %[b], v78\n\t"
    "s_cmp_ge_i32 %[b], %[ns]\n\t"
    "s_cbranch_scc1 6f\n\t"
    "s_cbranch_vccnz 6f\n\t"
    "v_or_b32 v77, 0x100, v77\n\t"
    "ds_write_b32 %[fa], v77\n\t"
    "s_branch 4b\n\t"
    "6:\n\t"
    "ds_write_b32 %[fa], v77\n\t"
    "s_cbranch_scc0 21b\n\t"
    "s_branch 9f\n\t"
    "5:\n\t"
    "s_waitcnt lgkmcnt(4)\n\t"
    "v_mfma_f32_16x16x32_bf16 v[80:83], v[128:131], v[96:99], 0\n\t"
    "ds_read_b128 v[120:123], %[da] offset:4096\n\t"
    "s_add_u32 m0, %[lb], 0\n\t"
    "v_mfma_f32_16x16x32_bf16 v[84:87], v[132:135], v[96:99], 0\n\t"
    "ds_read_b128 v[124:127], %[da] offset:6144\n\t"
    "global_load_lds_dwordx4 %[av0], s[36:37] sc1\n\t"
    "v_mfma_f32_16x16x32_bf16 v[88:91], v[128:131], v[100:103], 0\n\t"
    "s_add_u32 m0, %[lb], 32768\n\t"
    "v_mfma_f32_16x16x32_bf16 v[92:95], v[132:135], v[100:103], 0\n\t"
    "global_load_lds_dwordx4 %[av1], s[36:37] sc1\n\t"
    "s_waitcnt vmcnt(10)\n\t"
    "s_barrier\n\t"
    "s_waitcnt lgkmcnt(4)\n\t"
    "v_mfma_f32_16x16x32_bf16 v[80:83], v[136:139], v[104:107], v[80:83]\n\t"
    "ds_read_b128 v[96:99], %[ea] offset:8192\n\t"
    "s_add_u32 m0, %[lb], 4096\n\t"
    "v_mfma_f32_16x16x32_bf16 v[84:87], v[140:143], v[104:107], v[84:87]\n\t"
    "ds_read_b128 v[100:103], %[ea] offset:10240\n\t"
    "global_load_lds_dwordx4 %[av2], s[36:37] sc1\n\t"
    "v_mfma_f32_16x16x32_bf16 v[88:91], v[136:139], v[108:111], v[88:91]\n\t"
    "s_add_u32 m0, %[lb], 36864\n\t"
    "v_mfma_f32_16x16x32_bf16 v[92:95], v[140:143], v[108:111], v[92:95]\n\t"
    "global_load_lds_dwordx4 %[av3], s[36:37] sc1\n\t"
    "s_waitcnt lgkmcnt(4)\n\t"
    "v_mfma_f32_16x16x32_bf16 v[80:83], v[144:147], v[112:115], v[80:83]\n\t"
    "ds_read_b128 v[104:107], %[da] offset:8192\n\t"
    "s_add_u32 m0, %[lb], 8192\n\t"
    "v_mfma_f32_16x16x32_bf16 v[84:87], v[148:151], v[112:115], v[84:87]\n\t"
    "ds_read_b128 v[108:111], %[da] offset:10240\n\t"
    "global_load_lds_dwordx4 %[av4], s[36:37] sc1\n\t"
    "v_mfma_f32_16x16x32_bf16 v[88:91], v[144:147], v[116:119], v[88:91]\n\t"
    "s_add_u32 m0, %[lb], 40960\n\t"
    "v_mfma_f32_16x16x32_bf16 v[92:95], v[148:151], v[116:119], v[92:95]\n\t"
    "global_load_lds_dwordx4 %[av5], s[36:37] sc1\n\t"
    "s_waitcnt lgkmcnt(4)\n\t"
    "v_mfma_f32_16x16x32_bf16 v[80:83], v[152:155], v[120:123], v[80:83]\n\t"
    "ds_read_b128 v[112:115], %[ea] offset:12288\n\t"
    "s_add_u32 m0, %[lb], 12288\n\t"
    "v_mfma_f32_16x16x32_bf16 v[84:87], v[156:159], v[120:123], v[84:87]\n\t"
    "ds_read_b128 v[116:119], %[ea] offset:14336\n\t"
    "global_load_lds_dwordx4 %[av6], s[36:37] sc1\n\t"
    "v_mfma_f32_16x16x32_bf16 v[88:91], v[152:155], v[124:127], v[88:91]\n\t"
    "s_add_u32 m0, %[lb], 45056\n\t"
    "v_mfma_f32_16x16x32_bf16 v[92:95], v[156:159], v[124:127], v[92:95]\n\t"
    "global_load_lds_dwordx4 %[av7], s[36:37] sc1\n\t"
    "s_waitcnt lgkmcnt(4)\n\t"
    "v_mfma_f32_16x16x32_bf16 v[80:83], v[160:163], v[96:99], v[80:83]\n\t"
    "ds_read_b128 v[120:123], %[da] offset:12288\n\t"
    "s_add_u32 m0, %[lb], 16384\n\t"
    "v_mfma_f32_16x16x32_bf16 v[84:87], v[164:167], v[96:99], v[84:87]\n\t"
    "ds_read_b128 v[124:127], %[da] offset:14336\n\t"
    "global_load_lds_dwordx4 %[av0], s[38:39] sc1\n\t"
    "v_mfma_f32_16x16x32_bf16 v[88:91], v[160:163], v[100:103], v[88:91]\n\t"
    "s_add_u32 m0, %[lb], 49152\n\t"
    "v_mfma_f32_16x16x32_bf16 v[92:95], v[164:167], v[100:103], v[92:95]\n\t"
    "global_load_lds_dwordx4 %[av1], s[38:39] sc1\n\t"
    "s_waitcnt vmcnt(14)\n\t"
    "s_barrier\n\t"
    "s_waitcnt lgkmcnt(4)\n\t"
    "v_mfma_f32_16x16x32_bf16 v[80:83], v[168:171], v[104:107], v[80:83]\n\t"
    "ds_read_b128 v[96:99], %[ea] offset:16384\n\t"
    "s_add_u32 m0, %[lb], 20480\n\t"
    "v_mfma_f32_16x16x32_bf16 v[84:87], v[172:175], v[104:107], v[84:87]\n\t"
    "ds_read_b128 v[100:103], %[ea] offset:18432\n\t"
    "global_load_lds_dwordx4 %[av2], s[38:39] sc1\n\t"
    "v_mfma_f32_16x16x32_bf16 v[88:91], v[168:171], v[108:111], v[88:91]\n\t"
    "s_add_u32 m0, %[lb], 53248\n\t"
    "v_mfma_f32_16x16x32_bf16 v[92:95], v[172:175], v[108:111], v[92:95]\n\t"
    "global_load_lds_dwordx4 %[av3], s[38:39] sc1\n\t"
    "s_waitcnt lgkmcnt(4)\n\t"
    "v_mfma_f32_16x16x32_bf16 v[80:83], v[176:179], v[112:115], v[80:83]\n\t"
    "ds_read_b128 v[104:107], %[da] offset:16384\n\t"
    "s_add_u32 m0, %[lb], 24576\n\t"
    "v_mfma_f32_16x16x32_bf16 v[84:87], v[180:183], v[112:115], v[84:87]\n\t"
    "ds_read_b128 v[108:111], %[da] offset:18432\n\t"
    "global_load_lds_dwordx4 %[av4], s[38:39] sc1\n\t"
    "v_mfma_f32_16x16x32_bf16 v[88:91], v[176:179], v[116:119], v[88:91]\n\t"
    "s_add_u32 m0, %[lb], 57344\n\t"
    "v_mfma_f32_16x16x32_bf16 v[92:95], v[180:183], v[116:119], v[92:95]\n\t"
    "global_load_lds_dwordx4 %[av5], s[38:39] sc1\n\t"
    "s_waitcnt lgkmcnt(4)\n\t"
    "v_mfma_f32_16x16x32_bf16 v[80:83], v[184:187], v[120:123], v[80:83]\n\t"
    "ds_read_b128 v[112:115], %[ea] offset:20480\n\t"
    "s_add_u32 m0, %[lb], 28672\n\t"
    "v_mfma_f32_16x16x32_bf16 v[84:87], v[188:191], v[120:123], v[84:87]\n\t"
    "ds_read_b128 v[116:119], %[ea] offset:22528\n\t"
    "global_load_lds_dwordx4 %[av6], s[38:39] sc1\n\t"
    "v_mfma_f32_16x16x32_bf16 v[88:91], v[184:187], v[124:127], v[88:91]\n\t"
    "s_add_u32 m0, %[lb], 61440\n\t"
    "v_mfma_f32_16x16x32_bf16 v[92:95], v[188:191], v[124:127], v[92:95]\n\t"
    "global_load_lds_dwordx4 %[av7], s[38:39] sc1\n\t"
    "s_waitcnt lgkmcnt(4)\n\t"
    "v_mfma_f32_16x16x32_bf16 v[80:83], v[192:195], v[96:99], v[80:83]\n\t"
    "ds_read_b128 v[120:123], %[da] offset:20480\n\t"
    "v_mfma_f32_16x16x32_bf16 v[84:87], v[196:199], v[96:99], v[84:87]\n\t"
    "ds_read_b128 v[124:127], %[da] offset:22528\n\t"
    "v_mfma_f32_16x16x32_bf16 v[88:91], v[192:195], v[100:103], v[88:91]\n\t"
    "v_mfma_f32_16x16x32_bf16 v[92:95], v[196:199], v[100:103], v[92:95]\n\t"
    "s_add_u32 s36, s36, %[ab]\n\t"
    "s_addc_u32 s37, s37, 0\n\t"
    "s_add_u32 s38, s38, %[ab]\n\t"
    "s_addc_u32 s39, s39, 0\n\t"
    "s_xor_b32 %[lb], %[lb], 0x10000\n\t"
    "s_waitcnt vmcnt(16)\n\t"
    "s_barrier\n\t"
    "s_waitcnt lgkmcnt(4)\n\t"
    "v_mfma_f32_16x16x32_bf16 v[80:83], v[200:203], v[104:107], v[80:83]\n\t"
    "ds_read_b128 v[96:99], %[ea] offset:24576\n\t"
    "v_mfma_f32_16x16x32_bf16 v[84:87], v[204:207], v[104:107], v[84:87]\n\t"
    "ds_read_b128 v[100:103], %[ea] offset:26624\n\t"
    "v_mfma_f32_16x16x32_bf16 v[88:91], v[200:203], v[108:111], v[88:91]\n\t"
    "v_mfma_f32_16x16x32_bf16 v[92:95], v[204:207], v[108:111], v[92:95]\n\t"
    "s_waitcnt lgkmcnt(4)\n\t"
    "v_mfma_f32_16x16x32_bf16 v[80:83], v[208:211], v[112:115], v[80:83]\n\t"
    "ds_read_b128 v[104:107], %[da] offset:24576\n\t"
    "v_mfma_f32_16x16x32_bf16 v[84:87], v[212:215], v[112:115], v[84:87]\n\t"
    "ds_read_b128 v[108:111], %[da] offset:26624\n\t"
    "v_mfma_f32_16x16x32_bf16 v[88:91], v[208:211], v[116:119], v[88:91]\n\t"
    "v_mfma_f32_16x16x32_bf16 v[92:95], v[212:215], v[116:119], v[92:95]\n\t"
    "s_cmp_eq_u32 %[sl], 0\n\t"
    "s_cbranch_scc1 73f\n\t"
    "s_mov_b32 m0, %[sl]\n\t"
    "s_nop 0\n\t"
    "global_load_lds_dword %[dv], %[db] sc1\n\t"
    "73:\n\t"
    "s_waitcnt lgkmcnt(4)\n\t"
    "v_mfma_f32_16x16x32_bf16 v[80:83], v[216:219], v[120:123], v[80:83]\n\t"
    "ds_read_b128 v[112:115], %[ea] offset:28672\n\t"
    "v_mfma_f32_16x16x32_bf16 v[84:87], v[220:223], v[120:123], v[84:87]\n\t"
    "ds_read_b128 v[116:119], %[ea] offset:30720\n\t"
    "v_mfma_f32_16x16x32_bf16 v[88:91], v[216:219], v[124:127], v[88:91]\n\t"
    "v_mfma_f32_16x16x32_bf16 v[92:95], v[220:223], v[124:127], v[92:95]\n\t"
    "s_waitcnt lgkmcnt(4)\n\t"
    "v_mfma_f32_16x16x32_bf16 v[80:83], v[224:227], v[96:99], v[80:83]\n\t"
    "ds_read_b128 v[120:123], %[da] offset:28672\n\t"
    "v_mfma_f32_16x16x32_bf16 v[84:87], v[228:231], v[96:99], v[84:87]\n\t"
    "ds_read_b128 v[124:127], %[da] offset:30720\n\t"
    "v_mfma_f32_16x16x32_bf16 v[88:91], v[224:227], v[100:103], v[88:91]\n\t"
    "v_mfma_f32_16x16x32_bf16 v[92:95], v[228:231], v[100:103], v[92:95]\n\t"
    "s_waitcnt lgkmcnt(4)\n\t"
    "v_mfma_f32_16x16x32_bf16 v[80:83], v[232:235], v[104:107], v[80:83]\n\t"
    "v_mfma_f32_16x16x32_bf16 v[84:87], v[236:239], v[104:107], v[84:87]\n\t"
    "v_mfma_f32_16x16x32_bf16 v[88:91], v[232:235], v[108:111], v[88:91]\n\t"
    "v_xor_b32 %[ea], 0x10000, %[ea]\n\t"
    "v_mfma_f32_16x16x32_bf16 v[92:95], v[236:239], v[108:111], v[92:95]\n\t"
    "v_xor_b32 %[da], 0x10000, %[da]\n\t"
    "s_waitcnt lgkmcnt(2)\n\t"
    "v_mfma_f32_16x16x32_bf16 v[80:83], v[240:243], v[112:115], v[80:83]\n\t"
    "v_mfma_f32_16x16x32_bf16 v[84:87], v[244:247], v[112:115], v[84:87]\n\t"
    "v_mfma_f32_16x16x32_bf16 v[88:91], v[240:243], v[116:119], v[88:91]\n\t"
    "s_add_u32 %[b], %[b], 1\n\t"
    "v_mfma_f32_16x16x32_bf16 v[92:95], v[244:247], v[116:119], v[92:95]\n\t"
    "s_waitcnt lgkmcnt(0)\n\t"
    "v_mfma_f32_16x16x32_bf16 v[80:83], v[248:251], v[120:123], v[80:83]\n\t"
    "v_mfma_f32_16x16x32_bf16 v[84:87], v[252:255], v[120:123], v[84:87]\n\t"
    "v_mfma_f32_16x16x32_bf16 v[88:91], v[248:251], v[124:127], v[88:91]\n\t"
    "v_mfma_f32_16x16x32_bf16 v[92:95], v[252:255], v[124:127], v[92:95]\n\t"
    "v_mov_b32 v77, %[b]\n\t"
    "v_or_b32 v77, 0x100, v77\n\t"
    "s_nop 2\n\t"
    "ds_write_b128 %[rw], v[80:83] offset:0\n\t"
    "ds_write_b128 %[rw], v[84:87] offset:1024\n\t"
    "ds_write_b128 %[rw], v[88:91] offset:2048\n\t"
    "ds_write_b128 %[rw], v[92:95] offset:3072\n\t"
    "ds_write_b32 %[fa], v77\n\t"
    "s_waitcnt vmcnt(0)\n\t"
    "ds_read_b32 v79, %[sa]\n\t"
    "s_waitcnt lgkmcnt(0)\n\t"
    "v_mad_u32_u24 v79, v79, %[mu], %[ad]\n\t"
    "v_max_i32 v78, v78, v79\n\t"
    "s_branch 3b\n\t"
    "9:\n\t"
    "s_waitcnt lgkmcnt(0)"
    : [ea] "+&v"(ea), [da] "+&v"(da), [b] "+&s"(b), [st] "=&s"(st), "+{s[36:37]}"(p0), "+{s[38:39]}"(p1), [lb] "+&s"(lb), [cap] "+&s"(cap)
    : [ns] "s"(ns), [ab] "s"(ab), [rw] "v"(rw), [fa] "v"(fa), "{v[128:159]}"(W[0]), "{v[160:191]}"(W[1]), "{v[192:223]}"(W[2]), "{v[224:255]}"(W[3]), [wm] "s"(wm), [rq] "s"(rq), [mu] "s"(mu), [ad] "v"(ad), [dv] "v"(dv), [db] "s"(db), [sl] "s"(sl), [sa] "v"(sa), [av0] "v"(av[0]), [av1] "v"(av[1]), [av2] "v"(av[2]), [av3] "v"(av[3]), [av4] "v"(av[4]), [av5] "v"(av[5]), [av6] "v"(av[6]), [av7] "v"(av[7])
    : "v77", "v78", "v79", "v80", "v81", "v82", "v83", "v84", "v85", "v86", "v87", "v88", "v89", "v90", "v91", "v92", "v93", "v94", "v95", "v96", "v97", "v98", "v99", "v100", "v101", "v102", "v103", "v104", "v105", "v106", "v107", "v108", "v109", "v110", "v111", "v112", "v113", "v114", "v115", "v116", "v117", "v118", "v119", "v120", "v121", "v122", "v123", "v124", "v125", "v126", "v127", "vcc", "scc", "m0", "memory");
}
__device__ __forceinline__ void paced_ld2(const u32x32 (&W)[4], unsigned ea, unsigned da, int b, int ns, unsigned ab, unsigned rw, unsigned fa,
    const char* p0, const char* p1, unsigned lb, int& cap, int wm, unsigned rq, unsigned mu, unsigned ad, unsigned dv, const char* db, unsigned sl, unsigned sa, const unsigned (&av)[8]) {
  unsigned st;
  asm volatile("s_setprio 2\n\t"
    "v_mov_b32 v78, %[wm]\n\t"
    "s_cmp_lg_u32 %[rq], 0\n\t"
    "s_cbranch_scc1 3f\n\t"
    "2:\n\t"
    "v_cmp_ge_i32 vcc, %[b], v78\n\t"
    "s_cbranch_vccz 4f\n\t"
    "21:\n\t"
    "s_cmp_eq_u32 %[sl], 0\n\t"
    "s_cbranch_scc1 71f\n\t"
    "s_mov_b32 m0, %[sl]\n\t"
    "s_nop 0\n\t"
    "global_load_lds_dword %[dv], %[db] sc1\n\t"
    "71:\n\t"
    "s_sub_u32 %[cap], %[cap], 1\n\t"
    "s_max_i32 %[cap], %[cap], 0\n\t"
    "s_waitcnt vmcnt(0)\n\t"
    "ds_read_b32 v79, %[sa]\n\t"
    "s_waitcnt lgkmcnt(0)\n\t"
    "v_mad_u32_u24 v79, v79, %[mu], %[ad]\n\t"
    "v_max_i32 v78, v78, v79\n\t"
    "v_cmp_ge_i32 vcc, %[b], v78\n\t"
    "s_cbranch_vccz 4f\n\t"
    "s_sleep 1\n\t"
    "s_cmp_lg_u32 %[cap], 0\n\t"
    "s_cbranch_scc1 21b\n\t"
    "4:\n\t"
    "s_add_u32 m0, %[lb], 0\n\t"
    "s_nop 0\n\t"
    "global_load_lds_dwordx4 %[av0], s[36:37] sc1\n\t"
    "s_add_u32 m0, %[lb], 4096\n\t"
    "s_nop 0\n\t"
    "global_load_lds_dwordx4 %[av2], s[36:37] sc1\n\t"
    "s_add_u32 m0, %[lb], 8192\n\t"
    "s_nop 0\n\t"
    "global_load_lds_dwordx4 %[av4], s[36:37] sc1\n\t"
    "s_add_u32 m0, %[lb], 12288\n\t"
    "s_nop 0\n\t"
    "global_load_lds_dwordx4 %[av6], s[36:37] sc1\n\t"
    "s_add_u32 m0, %[lb], 16384\n\t"
    "s_nop 0\n\t"
    "global_load_lds_dwordx4 %[av0], s[38:39] sc1\n\t"
    "s_add_u32 m0, %[lb], 20480\n\t"
    "s_nop 0\n\t"
    "global_load_lds_dwordx4 %[av2], s[38:39] sc1\n\t"
    "s_add_u32 m0, %[lb], 24576\n\t"
    "s_nop 0\n\t"
    "global_load_lds_dwordx4 %[av4], s[38:39] sc1\n\t"
    "s_add_u32 m0, %[lb], 28672\n\t"
    "s_nop 0\n\t"
    "global_load_lds_dwordx4 %[av6], s[38:39] sc1\n\t"
    "s_add_u32 s36, s36, %[ab]\n\t"
    "s_addc_u32 s37, s37, 0\n\t"
    "s_add_u32 s38, s38, %[ab]\n\t"
    "s_addc_u32 s39, s39, 0\n\t"
    "s_xor_b32 %[lb], %[lb], 0x10000\n\t"
    "3:\n\t"
    "s_add_u32 %[st], %[b], 1\n\t"
    "v_min_i32 v78, %[ns], v78\n\t"
    "v_cmp_ge_i32 vcc, %[st], v78\n\t"
    "s_waitcnt vmcnt(6) lgkmcnt(0)\n\t"
    "s_barrier\n\t"
    "ds_read_b128 v[96:99], %[ea] offset:0\n\t"
    "ds_read_b128 v[100:103], %[ea] offset:2048\n\t"
    "ds_read_b128 v[104:107], %[da] offset:0\n\t"
    "ds_read_b128 v[108:111], %[da] offset:2048\n\t"
    "ds_read_b128 v[112:115], %[ea] offset:4096\n\t"
    "ds_read_b128 v[116:119], %[ea] offset:6144\n\t"
    "s_cbranch_vccz 5f\n\t"
    "s_waitcnt lgkmcnt(4)\n\t"
    "v_mfma_f32_16x16x32_bf16 v[80:83], v[128:131], v[96:99], 0\n\t"
    "ds_read_b128 v[120:123], %[da] offset:4096\n\t"
    "v_mfma_f32_16x16x32_bf16 v[84:87], v[132:135], v[96:99], 0\n\t"
    "ds_read_b128 v[124:127], %[da] offset:6144\n\t"
    "v_mfma_f32_16x16x32_bf16 v[88:91], v[128:131], v[100:103], 0\n\t"
    "v_mfma_f32_16x16x32_bf16 v[92:95], v[132:135], v[100:103], 0\n\t"
    "s_waitcnt vmcnt(4)\n\t"
    "s_barrier\n\t"
    "s_waitcnt lgkmcnt(4)\n\t"
    "v_mfma_f32_16x16x32_bf16 v[80:83], v[136:139], v[104:107], v[80:83]\n\t"
    "ds_read_b128 v[96:99], %[ea] offset:8192\n\t"
    "v_mfma_f32_16x16x32_bf16 v[84:87], v[140:143], v[104:107], v[84:87]\n\t"
    "ds_read_b128 v[100:103], %[ea] offset:10240\n\t"
    "v_mfma_f32_16x16x32_bf16 v[88:91], v[136:139], v[108:111], v[88:91]\n\t"
    "v_mfma_f32_16x16x32_bf16 v[92:95], v[140:143], v[108:111], v[92:95]\n\t"
    "s_waitcnt lgkmcnt(4)\n\t"
    "v_mfma_f32_16x16x32_bf16 v[80:83], v[144:147], v[112:115], v[80:83]\n\t"
    "ds_read_b128 v[104:107], %[da] offset:8192\n\t"
    "v_mfma_f32_16x16x32_bf16 v[84:87], v[148:151], v[112:115], v[84:87]\n\t"
    "ds_read_b128 v[108:111], %[da] offset:10240\n\t"
    "v_mfma_f32_16x16x32_bf16 v[88:91], v[144:147], v[116:119], v[88:91]\n\t"
    "v_mfma_f32_16x16x32_bf16 v[92:95], v[148:151], v[116:119], v[92:95]\n\t"
    "s_waitcnt lgkmcnt(4)\n\t"
    "v_mfma_f32_16x16x32_bf16 v[80:83], v[152:155], v[120:123], v[80:83]\n\t"
    "ds_read_b128 v[112:115], %[ea] offset:12288\n\t"
    "v_mfma_f32_16x16x32_bf16 v[84:87], v[156:159], v[120:123], v[84:87]\n\t"
    "ds_read_b128 v[116:119], %[ea] offset:14336\n\t"
    "v_mfma_f32_16x16x32_bf16 v[88:91], v[152:155], v[124:127], v[88:91]\n\t"
    "v_mfma_f32_16x16x32_bf16 v[92:95], v[156:159], v[124:127], v[92:95]\n\t"
    "s_waitcnt lgkmcnt(4)\n\t"
    "v_mfma_f32_16x16x32_bf16 v[80:83], v[160:163], v[96:99], v[80:83]\n\t"
    "ds_read_b128 v[120:123], %[da] offset:12288\n\t"
    "v_mfma_f32_16x16x32_bf16 v[84:87], v[164:167], v[96:99], v[84:87]\n\t"
    "ds_read_b128 v[124:127], %[da] offset:14336\n\t"
    "v_mfma_f32_16x16x32_bf16 v[88:91], v[160:163], v[100:103], v[88:91]\n\t"
    "v_mfma_f32_16x16x32_bf16 v[92:95], v[164:167], v[100:103], v[92:95]\n\t"
    "s_waitcnt vmcnt(2)\n\t"
    "s_barrier\n\t"
    "s_waitcnt lgkmcnt(4)\n\t"
    "v_mfma_f32_16x16x32_bf16 v[80:83], v[168:171], v[104:107], v[80:83]\n\t"
    "ds_read_b128 v[96:99], %[ea] offset:16384\n\t"
    "v_mfma_f32_16x16x32_bf16 v[84:87], v[172:175], v[104:107], v[84:87]\n\t"
    "ds_read_b128 v[100:103], %[ea] offset:18432\n\t"
    "v_mfma_f32_16x16x32_bf16 v[88:91], v[168:171], v[108:111], v[88:91]\n\t"
    "v_mfma_f32_16x16x32_bf16 v[92:95], v[172:175], v[108:111], v[92:95]\n\t"
    "s_waitcnt lgkmcnt(4)\n\t"
    "v_mfma_f32_16x16x32_bf16 v[80:83], v[176:179], v[112:115], v[80:83]\n\t"
    "ds_read_b128 v[104:107], %[da] offset:16384\n\t"
    "v_mfma_f32_16x16x32_bf16 v[84:87], v[180:183], v[112:115], v[84:87]\n\t"
    "ds_read_b128 v[108:111], %[da] offset:18432\n\t"
    "v_mfma_f32_16x16x32_bf16 v[88:91], v[176:179], v[116:119], v[88:91]\n\t"
    "v_mfma_f32_16x16x32_bf16 v[92:95], v[180:183], v[116:119], v[92:95]\n\t"
    "s_waitcnt lgkmcnt(4)\n\t"
    "v_mfma_f32_16x16x32_bf16 v[80:83], v[184:187], v[120:123], v[80:83]\n\t"
    "ds_read_b128 v[112:115], %[ea] offset:20480\n\t"
    "v_mfma_f32_16x16x32_bf16 v[84:87], v[188:191], v[120:123], v[84:87]\n\t"
    "ds_read_b128 v[116:119], %[ea] offset:22528\n\t"
    "v_mfma_f32_16x16x32_bf16 v[88:91], v[184:187], v[124:127], v[88:91]\n\t"
    "v_mfma_f32_16x16x32_bf16 v[92:95], v[188:191], v[124:127], v[92:95]\n\t"
    "s_waitcnt lgkmcnt(4)\n\t"
    "v_mfma_f32_16x16x32_bf16 v[80:83], v[192:195], v[96:99], v[80:83]\n\t"
    "ds_read_b128 v[120:123], %[da] offset:20480\n\t"
    "v_mfma_f32_16x16x32_bf16 v[84:87], v[196:199], v[96:99], v[84:87]\n\t"
    "ds_read_b128 v[124:127], %[da] offset:22528\n\t"
    "v_mfma_f32_16x16x32_bf16 v[88:91], v[192:195], v[100:103], v[88:91]\n\t"
    "v_mfma_f32_16x16x32_bf16 v[92:95], v[196:199], v[100:103], v[92:95]\n\t"
    "s_waitcnt vmcnt(0)\n\t"
    "s_barrier\n\t"
    "s_waitcnt lgkmcnt(4)\n\t"
    "v_mfma_f32_16x16x32_bf16 v[80:83], v[200:203], v[104:107], v[80:83]\n\t"
    "ds_read_b128 v[96:99], %[ea] offset:24576\n\t"
    "v_mfma_f32_16x16x32_bf16 v[84:87], v[204:207], v[104:107], v[84:87]\n\t"
    "ds_read_b128 v[100:103], %[ea] offset:26624\n\t"
    "v_mfma_f32_16x16x32_bf16 v[88:91], v[200:203], v[108:111], v[88:91]\n\t"
    "v_mfma_f32_16x16x32_bf16 v[92:95], v[204:207], v[108:111], v[92:95]\n\t"
    "s_waitcnt lgkmcnt(4)\n\t"
    "v_mfma_f32_16x16x32_bf16 v[80:83], v[208:211], v[112:115], v[80:83]\n\t"
    "ds_read_b128 v[104:107], %[da] offset:24576\n\t"
    "v_mfma_f32_16x16x32_bf16 v[84:87], v[212:215], v[112:115], v[84:87]\n\t"
    "ds_read_b128 v[108:111], %[da] offset:26624\n\t"
    "v_mfma_f32_16x16x32_bf16 v[88:91], v[208:211], v[116:119], v[88:91]\n\t"
    "v_mfma_f32_16x16x32_bf16 v[92:95], v[212:215], v[116:119], v[92:95]\n\t"
    "s_cmp_eq_u32 %[sl], 0\n\t"
    "s_cbranch_scc1 72f\n\t"
    "s_mov_b32 m0, %[sl]\n\t"
    "s_nop 0\n\t"
    "global_load_lds_dword %[dv], %[db] sc1\n\t"
    "72:\n\t"
    "s_waitcnt lgkmcnt(4)\n\t"
    "v_mfma_f32_16x16x32_bf16 v[80:83], v[216:219], v[120:123], v[80:83]\n\t"
    "ds_read_b128 v[112:115], %[ea] offset:28672\n\t"
    "v_mfma_f32_16x16x32_bf16 v[84:87], v[220:223], v[120:123], v[84:87]\n\t"
    "ds_read_b128 v[116:119], %[ea] offset:30720\n\t"
    "v_mfma_f32_16x16x32_bf16 v[88:91], v[216:219], v[124:127], v[88:91]\n\t"
    "v_mfma_f32_16x16x32_bf16 v[92:95], v[220:223], v[124:127], v[92:95]\n\t"
    "s_waitcnt lgkmcnt(4)\n\t"
    "v_mfma_f32_16x16x32_bf16 v[80:83], v[224:227], v[96:99], v[80:83]\n\t"
    "ds_read_b128 v[120:123], %[da] offset:28672\n\t"
    "v_mfma_f32_16x16x32_bf16 v[84:87], v[228:231], v[96:99], v[84:87]\n\t"
    "ds_read_b128 v[124:127], %[da] offset:30720\n\t"
    "v_mfma_f32_16x16x32_bf16 v[88:91], v[224:227], v[100:103], v[88:91]\n\t"
    "v_mfma_f32_16x16x32_bf16 v[92:95], v[228:231], v[100:103], v[92:95]\n\t"
    "s_waitcnt lgkmcnt(4)\n\t"
    "v_mfma_f32_16x16x32_bf16 v[80:83], v[232:235], v[104:107], v[80:83]\n\t"
    "v_mfma_f32_16x16x32_bf16 v[84:87], v[236:239], v[104:107], v[84:87]\n\t"
    "v_mfma_f32_16x16x32_bf16 v[88:91], v[232:235], v[108:111], v[88:91]\n\t"
    "v_xor_b32 %[ea], 0x10000, %[ea]\n\t"
    "v_mfma_f32_16x16x32_bf16 v[92:95], v[236:239], v[108:111], v[92:95]\n\t"
    "v_xor_b32 %[da], 0x10000, %[da]\n\t"
    "s_waitcnt lgkmcnt(2)\n\t"
    "v_mfma_f32_16x16x32_bf16 v[80:83], v[240:243], v[112:115], v[80:83]\n\t"
    "v_mfma_f32_16x16x32_bf16 v[84:87], v[244:247], v[112:115], v[84:87]\n\t"
    "v_mfma_f32_16x16x32_bf16 v[88:91], v[240:243], v[116:119], v[88:91]\n\t"
    "s_add_u32 %[b], %[b], 1\n\t"
    "v_mfma_f32_16x16x32_bf16 v[92:95], v[244:247], v[116:119], v[92:95]\n\t"
    "s_waitcnt lgkmcnt(0)\n\t"
    "v_mfma_f32_16x16x32_bf16 v[80:83], v[248:251], v[120:123], v[80:83]\n\t"
    "v_mfma_f32_16x16x32_bf16 v[84:87], v[252:255], v[120:123], v[84:87]\n\t"
    "v_mfma_f32_16x16x32_bf16 v[88:91], v[248:251], v[124:127], v[88:91]\n\t"
    "v_mfma_f32_16x16x32_bf16 v[92:95], v[252:255], v[124:127], v[92:95]\n\t"
    "s_nop 4\n\t"
    "ds_write_b128 %[rw], v[80:83] offset:0\n\t"
    "ds_write_b128 %[rw], v[84:87] offset:1024\n\t"
    "ds_write_b128 %[rw], v[88:91] offset:2048\n\t"
    "ds_write_b128 %[rw], v[92:95] offset:3072\n\t"
    "s_waitcnt vmcnt(0)\n\t"
    "ds_read_b32 v79, %[sa]\n\t"
    "s_waitcnt lgkmcnt(0)\n\t"
    "v_mad_u32_u24 v79, v79, %[mu], %[ad]\n\t"
    "v_max_i32 v78, v78, v79\n\t"
    "v_mov_b32 v77, %[b]\n\t"
    "v_cmp_ge_i32 vcc, %[b], v78\n\t"
    "s_cmp_ge_i32 %[b], %[ns]\n\t"
    "s_cbranch_scc1 6f\n\t"
    "s_cbranch_vccnz 6f\n\t"
    "v_or_b32 v77, 0x100, v77\n\t"
    "ds_write_b32 %[fa], v77\n\t"
    "s_branch 4b\n\t"
    "6:\n\t"
    "ds_write_b32 %[fa], v77\n\t"
    "s_cbranch_scc0 21b\n\t"
    "s_branch 9f\n\t"
    "5:\n\t"
    "s_waitcnt lgkmcnt(4)\n\t"
    "v_mfma_f32_16x16x32_bf16 v[80:83], v[128:131], v[96:99], 0\n\t"
    "ds_read_b128 v[120:123], %[da] offset:4096\n\t"
    "s_add_u32 m0, %[lb], 0\n\t"
    "v_mfma_f32_16x16x32_bf16 v[84:87], v[132:135], v[96:99], 0\n\t"
    "ds_read_b128 v[124:127], %[da] offset:6144\n\t"
    "global_load_lds_dwordx4 %[av0], s[36:37] sc1\n\t"
    "v_mfma_f32_16x16x32_bf16 v[88:91], v[128:131], v[100:103], 0\n\t"
    "v_mfma_f32_16x16x32_bf16 v[92:95], v[132:135], v[100:103], 0\n\t"
    "s_waitcnt vmcnt(5)\n\t"
    "s_barrier\n\t"
    "s_waitcnt lgkmcnt(4)\n\t"
    "v_mfma_f32_16x16x32_bf16 v[80:83], v[136:139], v[104:107], v[80:83]\n\t"
    "ds_read_b128 v[96:99], %[ea] offset:8192\n\t"
    "s_add_u32 m0, %[lb], 4096\n\t"
    "v_mfma_f32_16x16x32_bf16 v[84:87], v[140:143], v[104:107], v[84:87]\n\t"
    "ds_read_b128 v[100:103], %[ea] offset:10240\n\t"
    "global_load_lds_dwordx4 %[av2], s[36:37] sc1\n\t"
    "v_mfma_f32_16x16x32_bf16 v[88:91], v[136:139], v[108:111], v[88:91]\n\t"
    "v_mfma_f32_16x16x32_bf16 v[92:95], v[140:143], v[108:111], v[92:95]\n\t"
    "s_waitcnt lgkmcnt(4)\n\t"
    "v_mfma_f32_16x16x32_bf16 v[80:83], v[144:147], v[112:115], v[80:83]\n\t"
    "ds_read_b128 v[104:107], %[da] offset:8192\n\t"
    "s_add_u32 m0, %[lb], 8192\n\t"
    "v_mfma_f32_16x16x32_bf16 v[84:87], v[148:151], v[112:115], v[84:87]\n\t"
    "ds_read_b128 v[108:111], %[da] offset:10240\n\t"
    "global_load_lds_dwordx4 %[av4], s[36:37] sc1\n\t"
    "v_mfma_f32_16x16x32_bf16 v[88:91], v[144:147], v[116:119], v[88:91]\n\t"
    "v_mfma_f32_16x16x32_bf16 v[92:95], v[148:151], v[116:119], v[92:95]\n\t"
    "s_waitcnt lgkmcnt(4)\n\t"
    "v_mfma_f32_16x16x32_bf16 v[80:83], v[152:155], v[120:123], v[80:83]\n\t"
    "ds_read_b128 v[112:115], %[ea] offset:12288\n\t"
    "s_add_u32 m0, %[lb], 12288\n\t"
    "v_mfma_f32_16x16x32_bf16 v[84:87], v[156:159], v[120:123], v[84:87]\n\t"
    "ds_read_b128 v[116:119], %[ea] offset:14336\n\t"
    "global_load_lds_dwordx4 %[av6], s[36:37] sc1\n\t"
    "v_mfma_f32_16x16x32_bf16 v[88:91], v[152:155], v[124:127], v[88:91]\n\t"
    "v_mfma_f32_16x16x32_bf16 v[92:95], v[156:159], v[124:127], v[92:95]\n\t"
    "s_waitcnt lgkmcnt(4)\n\t"
    "v_mfma_f32_16x16x32_bf16 v[80:83], v[160:163], v[96:99], v[80:83]\n\t"
    "ds_read_b128 v[120:123], %[da] offset:12288\n\t"
    "s_add_u32 m0, %[lb], 16384\n\t"
    "v_mfma_f32_16x16x32_bf16 v[84:87], v[164:167], v[96:99], v[84:87]\n\t"
    "ds_read_b128 v[124:127], %[da] offset:14336\n\t"
    "global_load_lds_dwordx4 %[av0], s[38:39] sc1\n\t"
    "v_mfma_f32_16x16x32_bf16 v[88:91], v[160:163], v[100:103], v[88:91]\n\t"
    "v_mfma_f32_16x16x32_bf16 v[92:95], v[164:167], v[100:103], v[92:95]\n\t"
    "s_waitcnt vmcnt(7)\n\t"
    "s_barrier\n\t"
    "s_waitcnt lgkmcnt(4)\n\t"
    "v_mfma_f32_16x16x32_bf16 v[80:83], v[168:171], v[104:107], v[80:83]\n\t"
    "ds_read_b128 v[96:99], %[ea] offset:16384\n\t"
    "s_add_u32 m0, %[lb], 20480\n\t"
    "v_mfma_f32_16x16x32_bf16 v[84:87], v[172:175], v[104:107], v[84:87]\n\t"
    "ds_read_b128 v[100:103], %[ea] offset:18432\n\t"
    "global_load_lds_dwordx4 %[av2], s[38:39] sc1\n\t"
    "v_mfma_f32_16x16x32_bf16 v[88:91], v[168:171], v[108:111], v[88:91]\n\t"
    "v_mfma_f32_16x16x32_bf16 v[92:95], v[172:175], v[108:111], v[92:95]\n\t"
    "s_waitcnt lgkmcnt(4)\n\t"
    "v_mfma_f32_16x16x32_bf16 v[80:83], v[176:179], v[112:115], v[80:83]\n\t"
    "ds_read_b128 v[104:107], %[da] offset:16384\n\t"
    "s_add_u32 m0, %[lb], 24576\n\t"
    "v_mfma_f32_16x16x32_bf16 v[84:87], v[180:183], v[112:115], v[84:87]\n\t"
    "ds_read_b128 v[108:111], %[da] offset:18432\n\t"
    "global_load_lds_dwordx4 %[av4], s[38:39] sc1\n\t"
    "v_mfma_f32_16x16x32_bf16 v[88:91], v[176:179], v[116:119], v[88:91]\n\t"
    "v_mfma_f32_16x16x32_bf16 v[92:95], v[180:183], v[116:119], v[92:95]\n\t"
    "s_waitcnt lgkmcnt(4)\n\t"
    "v_mfma_f32_16x16x32_bf16 v[80:83], v[184:187], v[120:123], v[80:83]\n\t"
    "ds_read_b128 v[112:115], %[ea] offset:20480\n\t"
    "s_add_u32 m0, %[lb], 28672\n\t"
    "v_mfma_f32_16x16x32_bf16 v[84:87], v[188:191], v[120:123], v[84:87]\n\t"
    "ds_read_b128 v[116:119], %[ea] offset:22528\n\t"
    "global_load_lds_dwordx4 %[av6], s[38:39] sc1\n\t"
    "v_mfma_f32_16x16x32_bf16 v[88:91], v[184:187], v[124:127], v[88:91]\n\t"
    "v_mfma_f32_16x16x32_bf16 v[92:95], v[188:191], v[124:127], v[92:95]\n\t"
    "s_waitcnt lgkmcnt(4)\n\t"
    "v_mfma_f32_16x16x32_bf16 v[80:83], v[192:195], v[96:99], v[80:83]\n\t"
    "ds_read_b128 v[120:123], %[da] offset:20480\n\t"
    "v_mfma_f32_16x16x32_bf16 v[84:87], v[196:199], v[96:99], v[84:87]\n\t"
    "ds_read_b128 v[124:127], %[da] offset:22528\n\t"
    "v_mfma_f32_16x16x32_bf16 v[88:91], v[192:195], v[100:103], v[88:91]\n\t"
    "v_mfma_f32_16x16x32_bf16 v[92:95], v[196:199], v[100:103], v[92:95]\n\t"
    "s_add_u32 s36, s36, %[ab]\n\t"
    "s_addc_u32 s37, s37, 0\n\t"
    "s_add_u32 s38, s38, %[ab]\n\t"
    "s_addc_u32 s39, s39, 0\n\t"
    "s_xor_b32 %[lb], %[lb], 0x10000\n\t"
    "s_waitcnt vmcnt(8)\n\t"
    "s_barrier\n\t"
    "s_waitcnt lgkmcnt(4)\n\t"
    "v_mfma_f32_16x16x32_bf16 v[80:83], v[200:203], v[104:107], v[80:83]\n\t"
    "ds_read_b128 v[96:99], %[ea] offset:24576\n\t"
    "v_mfma_f32_16x16x32_bf16 v[84:87], v[204:207], v[104:107], v[84:87]\n\t"
    "ds_read_b128 v[100:103], %[ea] offset:26624\n\t"
    "v_mfma_f32_16x16x32_bf16 v[88:91], v[200:203], v[108:111], v[88:91]\n\t"
    "v_mfma_f32_16x16x32_bf16 v[92:95], v[204:207], v[108:111], v[92:95]\n\t"
    "s_waitcnt lgkmcnt(4)\n\t"
    "v_mfma_f32_16x16x32_bf16 v[80:83], v[208:211], v[112:115], v[80:83]\n\t"
    "ds_read_b128 v[104:107], %[da] offset:24576\n\t"
    "v_mfma_f32_16x16x32_bf16 v[84:87], v[212:215], v[112:115], v[84:87]\n\t"
    "ds_read_b128 v[108:111], %[da] offset:26624\n\t"
    "v_mfma_f32_16x16x32_bf16 v[88:91], v[208:211], v[116:119], v[88:91]\n\t"
    "v_mfma_f32_16x16x32_bf16 v[92:95], v[212:215], v[116:119], v[92:95]\n\t"
    "s_cmp_eq_u32 %[sl], 0\n\t"
    "s_cbranch_scc1 73f\n\t"
    "s_mov_b32 m0, %[sl]\n\t"
    "s_nop 0\n\t"
    "global_load_lds_dword %[dv], %[db] sc1\n\t"
    "73:\n\t"
    "s_waitcnt lgkmcnt(4)\n\t"
    "v_mfma_f32_16x16x32_bf16 v[80:83], v[216:219], v[120:123], v[80:83]\n\t"
    "ds_read_b128 v[112:115], %[ea] offset:28672\n\t"
    "v_mfma_f32_16x16x32_bf16 v[84:87], v[220:223], v[120:123], v[84:87]\n\t"
    "ds_read_b128 v[116:119], %[ea] offset:30720\n\t"
    "v_mfma_f32_16x16x32_bf16 v[88:91], v[216:219], v[124:127], v[88:91]\n\t"
    "v_mfma_f32_16x16x32_bf16 v[92:95], v[220:223], v[124:127], v[92:95]\n\t"
    "s_waitcnt lgkmcnt(4)\n\t"
    "v_mfma_f32_16x16x32_bf16 v[80:83], v[224:227], v[96:99], v[80:83]\n\t"
    "ds_read_b128 v[120:123], %[da] offset:28672\n\t"
    "v_mfma_f32_16x16x32_bf16 v[84:87], v[228:231], v[96:99], v[84:87]\n\t"
    "ds_read_b128 v[124:127], %[da] offset:30720\n\t"
    "v_mfma_f32_16x16x32_bf16 v[88:91], v[224:227], v[100:103], v[88:91]\n\t"
    "v_mfma_f32_16x16x32_bf16 v[92:95], v[228:231], v[100:103], v[92:95]\n\t"
    "s_waitcnt lgkmcnt(4)\n\t"
    "v_mfma_f32_16x16x32_bf16 v[80:83], v[232:235], v[104:107], v[80:83]\n\t"
    "v_mfma_f32_16x16x32_bf16 v[84:87], v[236:239], v[104:107], v[84:87]\n\t"
    "v_mfma_f32_16x16x32_bf16 v[88:91], v[232:235], v[108:111], v[88:91]\n\t"
    "v_xor_b32 %[ea], 0x10000, %[ea]\n\t"
    "v_mfma_f32_16x16x32_bf16 v[92:95], v[236:239], v[108:111], v[92:95]\n\t"
    "v_xor_b32 %[da], 0x10000, %[da]\n\t"
    "s_waitcnt lgkmcnt(2)\n\t"
    "v_mfma_f32_16x16x32_bf16 v[80:83], v[240:243], v[112:115], v[80:83]\n\t"
    "v_mfma_f32_16x16x32_bf16 v[84:87], v[244:247], v[112:115], v[84:87]\n\t"
    "v_mfma_f32_16x16x32_bf16 v[88:91], v[240:243], v[116:119], v[88:91]\n\t"
    "s_add_u32 %[b], %[b], 1\n\t"
    "v_mfma_f32_16x16x32_bf16 v[92:95], v[244:247], v[116:119], v[92:95]\n\t"
    "s_waitcnt lgkmcnt(0)\n\t"
    "v_mfma_f32_16x16x32_bf16 v[80:83], v[248:251], v[120:123], v[80:83]\n\t"
    "v_mfma_f32_16x16x32_bf16 v[84:87], v[252:255], v[120:123], v[84:87]\n\t"
    "v_mfma_f32_16x16x32_bf16 v[88:91], v[248:251], v[124:127], v[88:91]\n\t"
    "v_mfma_f32_16x16x32_bf16 v[92:95], v[252:255], v[124:127], v[92:95]\n\t"
    "v_mov_b32 v77, %[b]\n\t"
    "v_or_b32 v77, 0x100, v77\n\t"
    "s_nop 2\n\t"
    "ds_write_b128 %[rw], v[80:83] offset:0\n\t"
    "ds_write_b128 %[rw], v[84:87] offset:1024\n\t"
    "ds_write_b128 %[rw], v[88:91] offset:2048\n\t"
    "ds_write_b128 %[rw], v[92:95] offset:3072\n\t"
    "ds_write_b32 %[fa], v77\n\t"
    "s_waitcnt vmcnt(0)\n\t"
    "ds_read_b32 v79, %[sa]\n\t"
    "s_waitcnt lgkmcnt(0)\n\t"
    "v_mad_u32_u24 v79, v79, %[mu], %[ad]\n\t"
    "v_max_i32 v78, v78, v79\n\t"
    "s_branch 3b\n\t"
    "9:\n\t"
    "s_waitcnt lgkmcnt(0)"
    : [ea] "+&v"(ea), [da] "+&v"(da), [b] "+&s"(b), [st] "=&s"(st), "+{s[36:37]}"(p0), "+{s[38:39]}"(p1), [lb] "+&s"(lb), [cap] "+&s"(cap)
    : [ns] "s"(ns), [ab] "s"(ab), [rw] "v"(rw), [fa] "v"(fa), "{v[128:159]}"(W[0]), "{v[160:191]}"(W[1]), "{v[192:223]}"(W[2]), "{v[224:255]}"(W[3]), [wm] "s"(wm), [rq] "s"(rq), [mu] "s"(mu), [ad] "v"(ad), [dv] "v"(dv), [db] "s"(db), [sl] "s"(sl), [sa] "v"(sa), [av0] "v"(av[0]), [av1] "v"(av[1]), [av2] "v"(av[2]), [av3] "v"(av[3]), [av4] "v"(av[4]), [av5] "v"(av[5]), [av6] "v"(av[6]), [av7] "v"(av[7])
    : "v77", "v78", "v79", "v80", "v81", "v82", "v83", "v84", "v85", "v86", "v87", "v88", "v89", "v90", "v91", "v92", "v93", "v94", "v95", "v96", "v97", "v98", "v99", "v100", "v101", "v102", "v103", "v104", "v105", "v106", "v107", "v108", "v109", "v110", "v111", "v112", "v113", "v114", "v115", "v116", "v117", "v118", "v119", "v120", "v121", "v122", "v123", "v124", "v125", "v126", "v127", "vcc", "scc", "m0", "memory");
}
__device__ __forceinline__ void paced_st0(const u32x32 (&W)[4], unsigned ea, unsigned da, int b, int ns, unsigned ab, unsigned rw, unsigned fa,
    const char* p0, unsigned pp, int po, unsigned cn, const char* pg, unsigned co0, unsigned co1) {
  unsigned st;
  asm volatile("1:\n\t"
    "s_barrier\n\t"
    "ds_read_b128 v[96:99], %[ea] offset:0\n\t"
    "ds_read_b128 v[100:103], %[ea] offset:2048\n\t"
    "ds_read_b128 v[104:107], %[da] offset:0\n\t"
    "ds_read_b128 v[108:111], %[da] offset:2048\n\t"
    "ds_read_b128 v[112:115], %[ea] offset:4096\n\t"
    "ds_read_b128 v[116:119], %[ea] offset:6144\n\t"
    "s_waitcnt lgkmcnt(4)\n\t"
    "v_mfma_f32_16x16x32_bf16 v[80:83], v[128:131], v[96:99], 0\n\t"
    "ds_read_b128 v[120:123], %[da] offset:4096\n\t"
    "v_mfma_f32_16x16x32_bf16 v[84:87], v[132:135], v[96:99], 0\n\t"
    "ds_read_b128 v[124:127], %[da] offset:6144\n\t"
    "v_mfma_f32_16x16x32_bf16 v[88:91], v[128:131], v[100:103], 0\n\t"
    "v_mfma_f32_16x16x32_bf16 v[92:95], v[132:135], v[100:103], 0\n\t"
    "s_barrier\n\t"
    "s_waitcnt lgkmcnt(4)\n\t"
    "v_mfma_f32_16x16x32_bf16 v[80:83], v[136:139], v[104:107], v[80:83]\n\t"
    "ds_read_b128 v[96:99], %[ea] offset:8192\n\t"
    "v_mfma_f32_16x16x32_bf16 v[84:87], v[140:143], v[104:107], v[84:87]\n\t"
    "ds_read_b128 v[100:103], %[ea] offset:10240\n\t"
    "v_mfma_f32_16x16x32_bf16 v[88:91], v[136:139], v[108:111], v[88:91]\n\t"
    "v_mfma_f32_16x16x32_bf16 v[92:95], v[140:143], v[108:111], v[92:95]\n\t"
    "s_waitcnt lgkmcnt(4)\n\t"
    "v_mfma_f32_16x16x32_bf16 v[80:83], v[144:147], v[112:115], v[80:83]\n\t"
    "ds_read_b128 v[104:107], %[da] offset:8192\n\t"
    "v_mfma_f32_16x16x32_bf16 v[84:87], v[148:151], v[112:115], v[84:87]\n\t"
    "ds_read_b128 v[108:111], %[da] offset:10240\n\t"
    "v_mfma_f32_16x16x32_bf16 v[88:91], v[144:147], v[116:119], v[88:91]\n\t"
    "v_mfma_f32_16x16x32_bf16 v[92:95], v[148:151], v[116:119], v[92:95]\n\t"
    "s_waitcnt lgkmcnt(4)\n\t"
    "v_mfma_f32_16x16x32_bf16 v[80:83], v[152:155], v[120:123], v[80:83]\n\t"
    "ds_read_b128 v[112:115], %[ea] offset:12288\n\t"
    "v_mfma_f32_16x16x32_bf16 v[84:87], v[156:159], v[120:123], v[84:87]\n\t"
    "ds_read_b128 v[116:119], %[ea] offset:14336\n\t"
    "v_mfma_f32_16x16x32_bf16 v[88:91], v[152:155], v[124:127], v[88:91]\n\t"
    "v_mfma_f32_16x16x32_bf16 v[92:95], v[156:159], v[124:127], v[92:95]\n\t"
    "s_cmp_lg_u32 %[pp], 0\n\t"
    "s_cbranch_scc1 7f\n\t"
    "8:\n\t"
    "s_waitcnt lgkmcnt(4)\n\t"
    "v_mfma_f32_16x16x32_bf16 v[80:83], v[160:163], v[96:99], v[80:83]\n\t"
    "ds_read_b128 v[120:123], %[da] offset:12288\n\t"
    "v_mfma_f32_16x16x32_bf16 v[84:87], v[164:167], v[96:99], v[84:87]\n\t"
    "ds_read_b128 v[124:127], %[da] offset:14336\n\t"
    "v_mfma_f32_16x16x32_bf16 v[88:91], v[160:163], v[100:103], v[88:91]\n\t"
    "v_mfma_f32_16x16x32_bf16 v[92:95], v[164:167], v[100:103], v[92:95]\n\t"
    "s_barrier\n\t"
    "s_waitcnt lgkmcnt(4)\n\t"
    "v_mfma_f32_16x16x32_bf16 v[80:83], v[168:171], v[104:107], v[80:83]\n\t"
    "ds_read_b128 v[96:99], %[ea] offset:16384\n\t"
    "v_mfma_f32_16x16x32_bf16 v[84:87], v[172:175], v[104:107], v[84:87]\n\t"
    "ds_read_b128 v[100:103], %[ea] offset:18432\n\t"
    "v_mfma_f32_16x16x32_bf16 v[88:91], v[168:171], v[108:111], v[88:91]\n\t"
    "v_mfma_f32_16x16x32_bf16 v[92:95], v[172:175], v[108:111], v[92:95]\n\t"
    "s_waitcnt lgkmcnt(4)\n\t"
    "v_mfma_f32_16x16x32_bf16 v[80:83], v[176:179], v[112:115], v[80:83]\n\t"
    "ds_read_b128 v[104:107], %[da] offset:16384\n\t"
    "v_mfma_f32_16x16x32_bf16 v[84:87], v[180:183], v[112:115], v[84:87]\n\t"
    "ds_read_b128 v[108:111], %[da] offset:18432\n\t"
    "v_mfma_f32_16x16x32_bf16 v[88:91], v[176:179], v[116:119], v[88:91]\n\t"
    "v_mfma_f32_16x16x32_bf16 v[92:95], v[180:183], v[116:119], v[92:95]\n\t"
    "s_waitcnt lgkmcnt(4)\n\t"
    "v_mfma_f32_16x16x32_bf16 v[80:83], v[184:187], v[120:123], v[80:83]\n\t"
    "ds_read_b128 v[112:115], %[ea] offset:20480\n\t"
    "v_mfma_f32_16x16x32_bf16 v[84:87], v[188:191], v[120:123], v[84:87]\n\t"
    "ds_read_b128 v[116:119], %[ea] offset:22528\n\t"
    "v_mfma_f32_16x16x32_bf16 v[88:91], v[184:187], v[124:127], v[88:91]\n\t"
    "v_mfma_f32_16x16x32_bf16 v[92:95], v[188:191], v[124:127], v[92:95]\n\t"
    "s_waitcnt lgkmcnt(4)\n\t"
    "v_mfma_f32_16x16x32_bf16 v[80:83], v[192:195], v[96:99], v[80:83]\n\t"
    "ds_read_b128 v[120:123], %[da] offset:20480\n\t"
    "v_mfma_f32_16x16x32_bf16 v[84:87], v[196:199], v[96:99], v[84:87]\n\t"
    "ds_read_b128 v[124:127], %[da] offset:22528\n\t"
    "v_mfma_f32_16x16x32_bf16 v[88:91], v[192:195], v[100:103], v[88:91]\n\t"
    "v_mfma_f32_16x16x32_bf16 v[92:95], v[196:199], v[100:103], v[92:95]\n\t"
    "s_barrier\n\t"
    "s_waitcnt lgkmcnt(4)\n\t"
    "v_mfma_f32_16x16x32_bf16 v[80:83], v[200:203], v[104:107], v[80:83]\n\t"
    "ds_read_b128 v[96:99], %[ea] offset:24576\n\t"
    "v_mfma_f32_16x16x32_bf16 v[84:87], v[204:207], v[104:107], v[84:87]\n\t"
    "ds_read_b128 v[100:103], %[ea] offset:26624\n\t"
    "v_mfma_f32_16x16x32_bf16 v[88:91], v[200:203], v[108:111], v[88:91]\n\t"
    "v_mfma_f32_16x16x32_bf16 v[92:95], v[204:207], v[108:111], v[92:95]\n\t"
    "s_waitcnt lgkmcnt(4)\n\t"
    "v_mfma_f32_16x16x32_bf16 v[80:83], v[208:211], v[112:115], v[80:83]\n\t"
    "ds_read_b128 v[104:107], %[da] offset:24576\n\t"
    "v_mfma_f32_16x16x32_bf16 v[84:87], v[212:215], v[112:115], v[84:87]\n\t"
    "ds_read_b128 v[108:111], %[da] offset:26624\n\t"
    "v_mfma_f32_16x16x32_bf16 v[88:91], v[208:211], v[116:119], v[88:91]\n\t"
    "v_mfma_f32_16x16x32_bf16 v[92:95], v[212:215], v[116:119], v[92:95]\n\t"
    "s_waitcnt lgkmcnt(4)\n\t"
    "v_mfma_f32_16x16x32_bf16 v[80:83], v[216:219], v[120:123], v[80:83]\n\t"
    "ds_read_b128 v[112:115], %[ea] offset:28672\n\t"
    "v_mfma_f32_16x16x32_bf16 v[84:87], v[220:223], v[120:123], v[84:87]\n\t"
    "ds_read_b128 v[116:119], %[ea] offset:30720\n\t"
    "v_mfma_f32_16x16x32_bf16 v[88:91], v[216:219], v[124:127], v[88:91]\n\t"
    "v_mfma_f32_16x16x32_bf16 v[92:95], v[220:223], v[124:127], v[92:95]\n\t"
    "s_waitcnt lgkmcnt(4)\n\t"
    "v_mfma_f32_16x16x32_bf16 v[80:83], v[224:227], v[96:99], v[80:83]\n\t"
    "ds_read_b128 v[120:123], %[da] offset:28672\n\t"
    "v_mfma_f32_16x16x32_bf16 v[84:87], v[228:231], v[96:99], v[84:87]\n\t"
    "ds_read_b128 v[124:127], %[da] offset:30720\n\t"
    "v_mfma_f32_16x16x32_bf16 v[88:91], v[224:227], v[100:103], v[88:91]\n\t"
    "v_mfma_f32_16x16x32_bf16 v[92:95], v[228:231], v[100:103], v[92:95]\n\t"
    "s_waitcnt lgkmcnt(4)\n\t"
    "v_mfma_f32_16x16x32_bf16 v[80:83], v[232:235], v[104:107], v[80:83]\n\t"
    "v_mfma_f32_16x16x32_bf16 v[84:87], v[236:239], v[104:107], v[84:87]\n\t"
    "v_mfma_f32_16x16x32_bf16 v[88:91], v[232:235], v[108:111], v[88:91]\n\t"
    "v_xor_b32 %[ea], 0x10000, %[ea]\n\t"
    "v_mfma_f32_16x16x32_bf16 v[92:95], v[236:239], v[108:111], v[92:95]\n\t"
    "v_xor_b32 %[da], 0x10000, %[da]\n\t"
    "s_waitcnt lgkmcnt(2)\n\t"
    "v_mfma_f32_16x16x32_bf16 v[80:83], v[240:243], v[112:115], v[80:83]\n\t"
    "v_mfma_f32_16x16x32_bf16 v[84:87], v[244:247], v[112:115], v[84:87]\n\t"
    "v_mfma_f32_16x16x32_bf16 v[88:91], v[240:243], v[116:119], v[88:91]\n\t"
    "s_add_u32 %[b], %[b], 1\n\t"
    "v_mfma_f32_16x16x32_bf16 v[92:95], v[244:247], v[116:119], v[92:95]\n\t"
    "s_waitcnt lgkmcnt(0)\n\t"
    "v_mfma_f32_16x16x32_bf16 v[80:83], v[248:251], v[120:123], v[80:83]\n\t"
    "v_mfma_f32_16x16x32_bf16 v[84:87], v[252:255], v[120:123], v[84:87]\n\t"
    "v_mfma_f32_16x16x32_bf16 v[88:91], v[248:251], v[124:127], v[88:91]\n\t"
    "v_mfma_f32_16x16x32_bf16 v[92:95], v[252:255], v[124:127], v[92:95]\n\t"
    "2:\n\t"
    "ds_read_b32 v39, %[fa]\n\t"
    "ds_read_b128 v[48:51], %[rw] offset:0\n\t"
    "ds_read_b128 v[52:55], %[rw] offset:1024\n\t"
    "ds_read_b128 v[56:59], %[rw] offset:2048\n\t"
    "ds_read_b128 v[60:63], %[rw] offset:3072\n\t"
    "s_waitcnt lgkmcnt(2)\n\t"
    "v_readfirstlane_b32 %[pp], v39\n\t"
    "s_and_b32 %[st], %[pp], 0xff\n\t"
    "s_cmp_eq_u32 %[st], %[b]\n\t"
    "s_cbranch_scc1 3f\n\t"
    "s_waitcnt lgkmcnt(0)\n\t"
    "s_branch 2b\n\t"
    "3:\n\t"
    "v_add_f32 v48, v48, v80\n\t"
    "v_add_f32 v49, v49, v81\n\t"
    "v_add_f32 v50, v50, v82\n\t"
    "v_add_f32 v51, v51, v83\n\t"
    "v_add_f32 v52, v52, v84\n\t"
    "v_add_f32 v53, v53, v85\n\t"
    "v_add_f32 v54, v54, v86\n\t"
    "v_add_f32 v55, v55, v87\n\t"
    "v_cvt_pk_bf16_f32 v40, v48, v49\n\t"
    "v_cvt_pk_bf16_f32 v41, v50, v51\n\t"
    "v_cvt_pk_bf16_f32 v42, v52, v53\n\t"
    "v_cvt_pk_bf16_f32 v43, v54, v55\n\t"
    "global_store_dwordx4 %[co0], v[40:43], s[36:37] sc1\n\t"
    "s_waitcnt lgkmcnt(0)\n\t"
    "v_add_f32 v56, v56, v88\n\t"
    "v_add_f32 v57, v57, v89\n\t"
    "v_add_f32 v58, v58, v90\n\t"
    "v_add_f32 v59, v59, v91\n\t"
    "v_add_f32 v60, v60, v92\n\t"
    "v_add_f32 v61, v61, v93\n\t"
    "v_add_f32 v62, v62, v94\n\t"
    "v_add_f32 v63, v63, v95\n\t"
    "v_cvt_pk_bf16_f32 v44, v56, v57\n\t"
    "v_cvt_pk_bf16_f32 v45, v58, v59\n\t"
    "v_cvt_pk_bf16_f32 v46, v60, v61\n\t"
    "v_cvt_pk_bf16_f32 v47, v62, v63\n\t"
    "global_store_dwordx4 %[co1], v[44:47], s[36:37] sc1\n\t"
    "s_add_u32 s36, s36, %[ab]\n\t"
    "s_addc_u32 s37, s37, 0\n\t"
    "s_lshr_b32 %[pp], %[pp], 8\n\t"
    "s_cbranch_scc1 1b\n\t"
    "v_mov_b32 v72, %[cn]\n\t"
    "v_mov_b32 v73, 1\n\t"
    "s_add_u32 %[st], %[b], %[po]\n\t"
    "v_mov_b32 v74, %[st]\n\t"
    "v_mov_b32 v75, 0\n\t"
    "s_waitcnt vmcnt(0)\n\t"
    "s_mov_b64 exec, 1\n\t"
    "ds_add_rtn_u32 v73, v72, v73\n\t"
    "s_waitcnt lgkmcnt(0)\n\t"
    "v_readfirstlane_b32 %[st], v73\n\t"
    "s_and_b32 %[st], %[st], 3\n\t"
    "s_cmp_lg_u32 %[st], 3\n\t"
    "s_cbranch_scc1 61f\n\t"
    "global_store_dword v75, v74, %[pg] sc1\n\t"
    "61:\n\t"
    "s_mov_b64 exec, -1\n\t"
    "s_mov_b32 %[pp], 0\n\t"
    "s_cmp_lt_i32 %[b], %[ns]\n\t"
    "s_cbranch_scc1 1b\n\t"
    "s_branch 9f\n\t"
    "7:\n\t"
    "v_mov_b32 v72, %[cn]\n\t"
    "v_mov_b32 v73, 1\n\t"
    "s_add_u32 %[st], %[b], %[po]\n\t"
    "v_mov_b32 v74, %[st]\n\t"
    "v_mov_b32 v75, 0\n\t"
    "s_waitcnt vmcnt(0)\n\t"
    "s_mov_b64 exec, 1\n\t"
    "ds_add_rtn_u32 v73, v72, v73\n\t"
    "s_waitcnt lgkmcnt(0)\n\t"
    "v_readfirstlane_b32 %[st], v73\n\t"
    "s_and_b32 %[st], %[st], 3\n\t"
    "s_cmp_lg_u32 %[st], 3\n\t"
    "s_cbranch_scc1 61f\n\t"
    "global_store_dword v75, v74, %[pg] sc1\n\t"
    "61:\n\t"
    "s_mov_b64 exec, -1\n\t"
    "s_mov_b32 %[pp], 0\n\t"
    "s_branch 8b\n\t"
    "9:"
    : [ea] "+&v"(ea), [da] "+&v"(da), [b] "+&s"(b), [st] "=&s"(st), "+{s[36:37]}"(p0), [pp] "+&s"(pp)
    : [ns] "s"(ns), [ab] "s"(ab), [rw] "v"(rw), [fa] "v"(fa), "{v[128:159]}"(W[0]), "{v[160:191]}"(W[1]), "{v[192:223]}"(W[2]), "{v[224:255]}"(W[3]), [po] "s"(po), [cn] "s"(cn), [pg] "s"(pg), [co0] "v"(co0), [co1] "v"(co1)
    : "v39", "v40", "v41", "v42", "v43", "v44", "v45", "v46", "v47", "v48", "v49", "v50", "v51", "v52", "v53", "v54", "v55", "v56", "v57", "v58", "v59", "v60", "v61", "v62", "v63", "v72", "v73", "v74", "v75", "v80", "v81", "v82", "v83", "v84", "v85", "v86", "v87", "v88", "v89", "v90", "v91", "v92", "v93", "v94", "v95", "v96", "v97", "v98", "v99", "v100", "v101", "v102", "v103", "v104", "v105", "v106", "v107", "v108", "v109", "v110", "v111", "v112", "v113", "v114", "v115", "v116", "v117", "v118", "v119", "v120", "v121", "v122", "v123", "v124", "v125", "v126", "v127", "vcc", "scc", "m0", "memory");
}
__device__ __forceinline__ void paced_st1(const u32x32 (&W)[4], unsigned ea, unsigned da, int b, int ns, unsigned ab, unsigned rw, unsigned fa,
    const char* p0, unsigned pp, int po, unsigned cn, const char* pg, unsigned co0, unsigned co1) {
  unsigned st;
  asm volatile("1:\n\t"
    "s_barrier\n\t"
    "ds_read_b128 v[96:99], %[ea] offset:0\n\t"
    "ds_read_b128 v[100:103], %[ea] offset:2048\n\t"
    "ds_read_b128 v[104:107], %[da] offset:0\n\t"
    "ds_read_b128 v[108:111], %[da] offset:2048\n\t"
    "ds_read_b128 v[112:115], %[ea] offset:4096\n\t"
    "ds_read_b128 v[116:119], %[ea] offset:6144\n\t"
    "s_waitcnt lgkmcnt(4)\n\t"
    "v_mfma_f32_16x16x32_bf16 v[80:83], v[128:131], v[96:99], 0\n\t"
    "ds_read_b128 v[120:123], %[da] offset:4096\n\t"
    "v_mfma_f32_16x16x32_bf16 v[84:87], v[132:135], v[96:99], 0\n\t"
    "ds_read_b128 v[124:127], %[da] offset:6144\n\t"
    "v_mfma_f32_16x16x32_bf16 v[88:91], v[128:131], v[100:103], 0\n\t"
    "v_mfma_f32_16x16x32_bf16 v[92:95], v[132:135], v[100:103], 0\n\t"
    "s_barrier\n\t"
    "s_waitcnt lgkmcnt(4)\n\t"
    "v_mfma_f32_16x16x32_bf16 v[80:83], v[136:139], v[104:107], v[80:83]\n\t"
    "ds_read_b128 v[96:99], %[ea] offset:8192\n\t"
    "v_mfma_f32_16x16x32_bf16 v[84:87], v[140:143], v[104:107], v[84:87]\n\t"
    "ds_read_b128 v[100:103], %[ea] offset:10240\n\t"
    "v_mfma_f32_16x16x32_bf16 v[88:91], v[136:139], v[108:111], v[88:91]\n\t"
    "v_mfma_f32_16x16x32_bf16 v[92:95], v[140:143], v[108:111], v[92:95]\n\t"
    "s_waitcnt lgkmcnt(4)\n\t"
    "v_mfma_f32_16x16x32_bf16 v[80:83], v[144:147], v[112:115], v[80:83]\n\t"
    "ds_read_b128 v[104:107], %[da] offset:8192\n\t"
    "v_mfma_f32_16x16x32_bf16 v[84:87], v[148:151], v[112:115], v[84:87]\n\t"
    "ds_read_b128 v[108:111], %[da] offset:10240\n\t"
    "v_mfma_f32_16x16x32_bf16 v[88:91], v[144:147], v[116:119], v[88:91]\n\t"
    "v_mfma_f32_16x16x32_bf16 v[92:95], v[148:151], v[116:119], v[92:95]\n\t"
    "s_waitcnt lgkmcnt(4)\n\t"
    "v_mfma_f32_16x16x32_bf16 v[80:83], v[152:155], v[120:123], v[80:83]\n\t"
    "ds_read_b128 v[112:115], %[ea] offset:12288\n\t"
    "v_mfma_f32_16x16x32_bf16 v[84:87], v[156:159], v[120:123], v[84:87]\n\t"
    "ds_read_b128 v[116:119], %[ea] offset:14336\n\t"
    "v_mfma_f32_16x16x32_bf16 v[88:91], v[152:155], v[124:127], v[88:91]\n\t"
    "v_mfma_f32_16x16x32_bf16 v[92:95], v[156:159], v[124:127], v[92:95]\n\t"
    "s_cmp_lg_u32 %[pp], 0\n\t"
    "s_cbranch_scc1 7f\n\t"
    "8:\n\t"
    "s_waitcnt lgkmcnt(4)\n\t"
    "v_mfma_f32_16x16x32_bf16 v[80:83], v[160:163], v[96:99], v[80:83]\n\t"
    "ds_read_b128 v[120:123], %[da] offset:12288\n\t"
    "v_mfma_f32_16x16x32_bf16 v[84:87], v[164:167], v[96:99], v[84:87]\n\t"
    "ds_read_b128 v[124:127], %[da] offset:14336\n\t"
    "v_mfma_f32_16x16x32_bf16 v[88:91], v[160:163], v[100:103], v[88:91]\n\t"
    "v_mfma_f32_16x16x32_bf16 v[92:95], v[164:167], v[100:103], v[92:95]\n\t"
    "s_barrier\n\t"
    "s_waitcnt lgkmcnt(4)\n\t"
    "v_mfma_f32_16x16x32_bf16 v[80:83], v[168:171], v[104:107], v[80:83]\n\t"
    "ds_read_b128 v[96:99], %[ea] offset:16384\n\t"
    "v_mfma_f32_16x16x32_bf16 v[84:87], v[172:175], v[104:107], v[84:87]\n\t"
    "ds_read_b128 v[100:103], %[ea] offset:18432\n\t"
    "v_mfma_f32_16x16x32_bf16 v[88:91], v[168:171], v[108:111], v[88:91]\n\t"
    "v_mfma_f32_16x16x32_bf16 v[92:95], v[172:175], v[108:111], v[92:95]\n\t"
    "s_waitcnt lgkmcnt(4)\n\t"
    "v_mfma_f32_16x16x32_bf16 v[80:83], v[176:179], v[112:115], v[80:83]\n\t"
    "ds_read_b128 v[104:107], %[da] offset:16384\n\t"
    "v_mfma_f32_16x16x32_bf16 v[84:87], v[180:183], v[112:115], v[84:87]\n\t"
    "ds_read_b128 v[108:111], %[da] offset:18432\n\t"
    "v_mfma_f32_16x16x32_bf16 v[88:91], v[176:179], v[116:119], v[88:91]\n\t"
    "v_mfma_f32_16x16x32_bf16 v[92:95], v[180:183], v[116:119], v[92:95]\n\t"
    "s_waitcnt lgkmcnt(4)\n\t"
    "v_mfma_f32_16x16x32_bf16 v[80:83], v[184:187], v[120:123], v[80:83]\n\t"
    "ds_read_b128 v[112:115], %[ea] offset:20480\n\t"
    "v_mfma_f32_16x16x32_bf16 v[84:87], v[188:191], v[120:123], v[84:87]\n\t"
    "ds_read_b128 v[116:119], %[ea] offset:22528\n\t"
    "v_mfma_f32_16x16x32_bf16 v[88:91], v[184:187], v[124:127], v[88:91]\n\t"
    "v_mfma_f32_16x16x32_bf16 v[92:95], v[188:191], v[124:127], v[92:95]\n\t"
    "s_waitcnt lgkmcnt(4)\n\t"
    "v_mfma_f32_16x16x32_bf16 v[80:83], v[192:195], v[96:99], v[80:83]\n\t"
    "ds_read_b128 v[120:123], %[da] offset:20480\n\t"
    "v_mfma_f32_16x16x32_bf16 v[84:87], v[196:199], v[96:99], v[84:87]\n\t"
    "ds_read_b128 v[124:127], %[da] offset:22528\n\t"
    "v_mfma_f32_16x16x32_bf16 v[88:91], v[192:195], v[100:103], v[88:91]\n\t"
    "v_mfma_f32_16x16x32_bf16 v[92:95], v[196:199], v[100:103], v[92:95]\n\t"
    "s_barrier\n\t"
    "s_waitcnt lgkmcnt(4)\n\t"
    "v_mfma_f32_16x16x32_bf16 v[80:83], v[200:203], v[104:107], v[80:83]\n\t"
    "ds_read_b128 v[96:99], %[ea] offset:24576\n\t"
    "v_mfma_f32_16x16x32_bf16 v[84:87], v[204:207], v[104:107], v[84:87]\n\t"
    "ds_read_b128 v[100:103], %[ea] offset:26624\n\t"
    "v_mfma_f32_16x16x32_bf16 v[88:91], v[200:203], v[108:111], v[88:91]\n\t"
    "v_mfma_f32_16x16x32_bf16 v[92:95], v[204:207], v[108:111], v[92:95]\n\t"
    "s_waitcnt lgkmcnt(4)\n\t"
    "v_mfma_f32_16x16x32_bf16 v[80:83], v[208:211], v[112:115], v[80:83]\n\t"
    "ds_read_b128 v[104:107], %[da] offset:24576\n\t"
    "v_mfma_f32_16x16x32_bf16 v[84:87], v[212:215], v[112:115], v[84:87]\n\t"
    "ds_read_b128 v[108:111], %[da] offset:26624\n\t"
    "v_mfma_f32_16x16x32_bf16 v[88:91], v[208:211], v[116:119], v[88:91]\n\t"
    "v_mfma_f32_16x16x32_bf16 v[92:95], v[212:215], v[116:119], v[92:95]\n\t"
    "s_waitcnt lgkmcnt(4)\n\t"
    "v_mfma_f32_16x16x32_bf16 v[80:83], v[216:219], v[120:123], v[80:83]\n\t"
    "ds_read_b128 v[112:115], %[ea] offset:28672\n\t"
    "v_mfma_f32_16x16x32_bf16 v[84:87], v[220:223], v[120:123], v[84:87]\n\t"
    "ds_read_b128 v[116:119], %[ea] offset:30720\n\t"
    "v_mfma_f32_16x16x32_bf16 v[88:91], v[216:219], v[124:127], v[88:91]\n\t"
    "v_mfma_f32_16x16x32_bf16 v[92:95], v[220:223], v[124:127], v[92:95]\n\t"
    "s_waitcnt lgkmcnt(4)\n\t"
    "v_mfma_f32_16x16x32_bf16 v[80:83], v[224:227], v[96:99], v[80:83]\n\t"
    "ds_read_b128 v[120:123], %[da] offset:28672\n\t"
    "v_mfma_f32_16x16x32_bf16 v[84:87], v[228:231], v[96:99], v[84:87]\n\t"
    "ds_read_b128 v[124:127], %[da] offset:30720\n\t"
    "v_mfma_f32_16x16x32_bf16 v[88:91], v[224:227], v[100:103], v[88:91]\n\t"
    "v_mfma_f32_16x16x32_bf16 v[92:95], v[228:231], v[100:103], v[92:95]\n\t"
    "s_waitcnt lgkmcnt(4)\n\t"
    "v_mfma_f32_16x16x32_bf16 v[80:83], v[232:235], v[104:107], v[80:83]\n\t"
    "v_mfma_f32_16x16x32_bf16 v[84:87], v[236:239], v[104:107], v[84:87]\n\t"
    "v_mfma_f32_16x16x32_bf16 v[88:91], v[232:235], v[108:111], v[88:91]\n\t"
    "v_xor_b32 %[ea], 0x10000, %[ea]\n\t"
    "v_mfma_f32_16x16x32_bf16 v[92:95], v[236:239], v[108:111], v[92:95]\n\t"
    "v_xor_b32 %[da], 0x10000, %[da]\n\t"
    "s_waitcnt lgkmcnt(2)\n\t"
    "v_mfma_f32_16x16x32_bf16 v[80:83], v[240:243], v[112:115], v[80:83]\n\t"
    "v_mfma_f32_16x16x32_bf16 v[84:87], v[244:247], v[112:115], v[84:87]\n\t"
    "v_mfma_f32_16x16x32_bf16 v[88:91], v[240:243], v[116:119], v[88:91]\n\t"
    "s_add_u32 %[b], %[b], 1\n\t"
    "v_mfma_f32_16x16x32_bf16 v[92:95], v[244:247], v[116:119], v[92:95]\n\t"
    "s_waitcnt lgkmcnt(0)\n\t"
    "v_mfma_f32_16x16x32_bf16 v[80:83], v[248:251], v[120:123], v[80:83]\n\t"
    "v_mfma_f32_16x16x32_bf16 v[84:87], v[252:255], v[120:123], v[84:87]\n\t"
    "v_mfma_f32_16x16x32_bf16 v[88:91], v[248:251], v[124:127], v[88:91]\n\t"
    "v_mfma_f32_16x16x32_bf16 v[92:95], v[252:255], v[124:127], v[92:95]\n\t"
    "2:\n\t"
    "ds_read_b32 v39, %[fa]\n\t"
    "ds_read_b128 v[48:51], %[rw] offset:0\n\t"
    "ds_read_b128 v[52:55], %[rw] offset:1024\n\t"
    "ds_read_b128 v[56:59], %[rw] offset:2048\n\t"
    "ds_read_b128 v[60:63], %[rw] offset:3072\n\t"
    "s_waitcnt lgkmcnt(2)\n\t"
    "v_readfirstlane_b32 %[pp], v39\n\t"
    "s_and_b32 %[st], %[pp], 0xff\n\t"
    "s_cmp_eq_u32 %[st], %[b]\n\t"
    "s_cbranch_scc1 3f\n\t"
    "s_waitcnt lgkmcnt(0)\n\t"
    "s_branch 2b\n\t"
    "3:\n\t"
    "v_add_f32 v48, v48, v80\n\t"
    "v_add_f32 v49, v49, v81\n\t"
    "v_add_f32 v50, v50, v82\n\t"
    "v_add_f32 v51, v51, v83\n\t"
    "v_add_f32 v52, v52, v84\n\t"
    "v_add_f32 v53, v53, v85\n\t"
    "v_add_f32 v54, v54, v86\n\t"
    "v_add_f32 v55, v55, v87\n\t"
    "s_waitcnt lgkmcnt(0)\n\t"
    "v_add_f32 v56, v56, v88\n\t"
    "v_add_f32 v57, v57, v89\n\t"
    "v_add_f32 v58, v58, v90\n\t"
    "v_add_f32 v59, v59, v91\n\t"
    "v_add_f32 v60, v60, v92\n\t"
    "v_add_f32 v61, v61, v93\n\t"
    "v_add_f32 v62, v62, v94\n\t"
    "v_add_f32 v63, v63, v95\n\t"
    "v_mul_f32 v40, %[nl], v48\n\t"
    "v_mul_f32 v41, %[nl], v49\n\t"
    "v_mul_f32 v42, %[nl], v50\n\t"
    "v_mul_f32 v43, %[nl], v51\n\t"
    "v_mul_f32 v44, %[nl], v56\n\t"
    "v_mul_f32 v45, %[nl], v57\n\t"
    "v_mul_f32 v46, %[nl], v58\n\t"
    "v_mul_f32 v47, %[nl], v59\n\t"
    "v_exp_f32 v40, v40\n\t"
    "v_exp_f32 v41, v41\n\t"
    "v_exp_f32 v42, v42\n\t"
    "v_exp_f32 v43, v43\n\t"
    "v_exp_f32 v44, v44\n\t"
    "v_exp_f32 v45, v45\n\t"
    "v_exp_f32 v46, v46\n\t"
    "v_exp_f32 v47, v47\n\t"
    "v_add_f32 v40, 1.0, v40\n\t"
    "v_add_f32 v41, 1.0, v41\n\t"
    "v_add_f32 v42, 1.0, v42\n\t"
    "v_add_f32 v43, 1.0, v43\n\t"
    "v_add_f32 v44, 1.0, v44\n\t"
    "v_add_f32 v45, 1.0, v45\n\t"
    "v_add_f32 v46, 1.0, v46\n\t"
    "v_add_f32 v47, 1.0, v47\n\t"
    "v_rcp_f32 v40, v40\n\t"
    "v_rcp_f32 v41, v41\n\t"
    "v_rcp_f32 v42, v42\n\t"
    "v_rcp_f32 v43, v43\n\t"
    "v_rcp_f32 v44, v44\n\t"
    "v_rcp_f32 v45, v45\n\t"
    "v_rcp_f32 v46, v46\n\t"
    "v_rcp_f32 v47, v47\n\t"
    "v_mul_f32 v40, v48, v40\n\t"
    "v_mul_f32 v41, v49, v41\n\t"
    "v_mul_f32 v42, v50, v42\n\t"
    "v_mul_f32 v43, v51, v43\n\t"
    "v_mul_f32 v44, v56, v44\n\t"
    "v_mul_f32 v45, v57, v45\n\t"
    "v_mul_f32 v46, v58, v46\n\t"
    "v_mul_f32 v47, v59, v47\n\t"
    "v_mul_f32 v40, v40, v52\n\t"
    "v_mul_f32 v41, v41, v53\n\t"
    "v_mul_f32 v42, v42, v54\n\t"
    "v_mul_f32 v43, v43, v55\n\t"
    "v_mul_f32 v44, v44, v60\n\t"
    "v_mul_f32 v45, v45, v61\n\t"
    "v_mul_f32 v46, v46, v62\n\t"
    "v_mul_f32 v47, v47, v63\n\t"
    "v_cvt_pk_bf16_f32 v48, v40, v41\n\t"
    "v_cvt_pk_bf16_f32 v49, v42, v43\n\t"
    "v_cvt_pk_bf16_f32 v50, v44, v45\n\t"
    "v_cvt_pk_bf16_f32 v51, v46, v47\n\t"
    "s_nop 1\n\t"
    "v_permlane16_swap_b32 v48, v50\n\t"
    "v_permlane16_swap_b32 v49, v51\n\t"
    "global_store_dwordx4 %[co0], v[48:51], s[36:37] sc1\n\t"
    "s_add_u32 s36, s36, %[ab]\n\t"
    "s_addc_u32 s37, s37, 0\n\t"
    "s_lshr_b32 %[pp], %[pp], 8\n\t"
    "s_cbranch_scc1 1b\n\t"
    "v_mov_b32 v72, %[cn]\n\t"
    "v_mov_b32 v73, 1\n\t"
    "s_add_u32 %[st], %[b], %[po]\n\t"
    "v_mov_b32 v74, %[st]\n\t"
    "v_mov_b32 v75, 0\n\t"
    "s_waitcnt vmcnt(0)\n\t"
    "s_mov_b64 exec, 1\n\t"
    "ds_add_rtn_u32 v73, v72, v73\n\t"
    "s_waitcnt lgkmcnt(0)\n\t"
    "v_readfirstlane_b32 %[st], v73\n\t"
    "s_and_b32 %[st], %[st], 3\n\t"
    "s_cmp_lg_u32 %[st], 3\n\t"
    "s_cbranch_scc1 61f\n\t"
    "global_store_dword v75, v74, %[pg] sc1\n\t"
    "61:\n\t"
    "s_mov_b64 exec, -1\n\t"
    "s_mov_b32 %[pp], 0\n\t"
    "s_cmp_lt_i32 %[b], %[ns]\n\t"
    "s_cbranch_scc1 1b\n\t"
    "s_branch 9f\n\t"
    "7:\n\t"
    "v_mov_b32 v72, %[cn]\n\t"
    "v_mov_b32 v73, 1\n\t"
    "s_add_u32 %[st], %[b], %[po]\n\t"
    "v_mov_b32 v74, %[st]\n\t"
    "v_mov_b32 v75, 0\n\t"
    "s_waitcnt vmcnt(0)\n\t"
    "s_mov_b64 exec, 1\n\t"
    "ds_add_rtn_u32 v73, v72, v73\n\t"
    "s_waitcnt lgkmcnt(0)\n\t"
    "v_readfirstlane_b32 %[st], v73\n\t"
    "s_and_b32 %[st], %[st], 3\n\t"
    "s_cmp_lg_u32 %[st], 3\n\t"
    "s_cbranch_scc1 61f\n\t"
    "global_store_dword v75, v74, %[pg] sc1\n\t"
    "61:\n\t"
    "s_mov_b64 exec, -1\n\t"
    "s_mov_b32 %[pp], 0\n\t"
    "s_branch 8b\n\t"
    "9:"
    : [ea] "+&v"(ea), [da] "+&v"(da), [b] "+&s"(b), [st] "=&s"(st), "+{s[36:37]}"(p0), [pp] "+&s"(pp)
    : [ns] "s"(ns), [ab] "s"(ab), [rw] "v"(rw), [fa] "v"(fa), "{v[128:159]}"(W[0]), "{v[160:191]}"(W[1]), "{v[192:223]}"(W[2]), "{v[224:255]}"(W[3]), [po] "s"(po), [cn] "s"(cn), [pg] "s"(pg), [co0] "v"(co0), [nl] "s"(-LOG2E)
    : "v39", "v40", "v41", "v42", "v43", "v44", "v45", "v46", "v47", "v48", "v49", "v50", "v51", "v52", "v53", "v54", "v55", "v56", "v57", "v58", "v59", "v60", "v61", "v62", "v63", "v72", "v73", "v74", "v75", "v80", "v81", "v82", "v83", "v84", "v85", "v86", "v87", "v88", "v89", "v90", "v91", "v92", "v93", "v94", "v95", "v96", "v97", "v98", "v99", "v100", "v101", "v102", "v103", "v104", "v105", "v106", "v107", "v108", "v109", "v110", "v111", "v112", "v113", "v114", "v115", "v116", "v117", "v118", "v119", "v120", "v121", "v122", "v123", "v124", "v125", "v126", "v127", "vcc", "scc", "m0", "memory");
}
__device__ __forceinline__ void paced_st2(const u32x32 (&W)[4], unsigned ea, unsigned da, int b, int ns, unsigned ab, unsigned rw, unsigned fa,
    const char* p0, unsigned pp, int po, unsigned cn, const char* pg, unsigned co0, unsigned co1) {
  unsigned st;
  asm volatile("1:\n\t"
    "s_barrier\n\t"
    "ds_read_b128 v[96:99], %[ea] offset:0\n\t"
    "ds_read_b128 v[100:103], %[ea] offset:2048\n\t"
    "ds_read_b128 v[104:107], %[da] offset:0\n\t"
    "ds_read_b128 v[108:111], %[da] offset:2048\n\t"
    "ds_read_b128 v[112:115], %[ea] offset:4096\n\t"
    "ds_read_b128 v[116:119], %[ea] offset:6144\n\t"
    "s_waitcnt lgkmcnt(4)\n\t"
    "v_mfma_f32_16x16x32_bf16 v[80:83], v[128:131], v[96:99], 0\n\t"
    "ds_read_b128 v[120:123], %[da] offset:4096\n\t"
    "v_mfma_f32_16x16x32_bf16 v[84:87], v[132:135], v[96:99], 0\n\t"
    "ds_read_b128 v[124:127], %[da] offset:6144\n\t"
    "v_mfma_f32_16x16x32_bf16 v[88:91], v[128:131], v[100:103], 0\n\t"
    "v_mfma_f32_16x16x32_bf16 v[92:95], v[132:135], v[100:103], 0\n\t"
    "s_barrier\n\t"
    "s_waitcnt lgkmcnt(4)\n\t"
    "v_mfma_f32_16x16x32_bf16 v[80:83], v[136:139], v[104:107], v[80:83]\n\t"
    "ds_read_b128 v[96:99], %[ea] offset:8192\n\t"
    "v_mfma_f32_16x16x32_bf16 v[84:87], v[140:143], v[104:107], v[84:87]\n\t"
    "ds_read_b128 v[100:103], %[ea] offset:10240\n\t"
    "v_mfma_f32_16x16x32_bf16 v[88:91], v[136:139], v[108:111], v[88:91]\n\t"
    "v_mfma_f32_16x16x32_bf16 v[92:95], v[140:143], v[108:111], v[92:95]\n\t"
    "s_waitcnt lgkmcnt(4)\n\t"
    "v_mfma_f32_16x16x32_bf16 v[80:83], v[144:147], v[112:115], v[80:83]\n\t"
    "ds_read_b128 v[104:107], %[da] offset:8192\n\t"
    "v_mfma_f32_16x16x32_bf16 v[84:87], v[148:151], v[112:115], v[84:87]\n\t"
    "ds_read_b128 v[108:111], %[da] offset:10240\n\t"
    "v_mfma_f32_16x16x32_bf16 v[88:91], v[144:147], v[116:119], v[88:91]\n\t"
    "v_mfma_f32_16x16x32_bf16 v[92:95], v[148:151], v[116:119], v[92:95]\n\t"
    "s_waitcnt lgkmcnt(4)\n\t"
    "v_mfma_f32_16x16x32_bf16 v[80:83], v[152:155], v[120:123], v[80:83]\n\t"
    "ds_read_b128 v[112:115], %[ea] offset:12288\n\t"
    "v_mfma_f32_16x16x32_bf16 v[84:87], v[156:159], v[120:123], v[84:87]\n\t"
    "ds_read_b128 v[116:119], %[ea] offset:14336\n\t"
    "v_mfma_f32_16x16x32_bf16 v[88:91], v[152:155], v[124:127], v[88:91]\n\t"
    "v_mfma_f32_16x16x32_bf16 v[92:95], v[156:159], v[124:127], v[92:95]\n\t"
    "s_cmp_lg_u32 %[pp], 0\n\t"
    "s_cbranch_scc1 7f\n\t"
    "8:\n\t"
    "s_waitcnt lgkmcnt(4)\n\t"
    "v_mfma_f32_16x16x32_bf16 v[80:83], v[160:163], v[96:99], v[80:83]\n\t"
    "ds_read_b128 v[120:123], %[da] offset:12288\n\t"
    "v_mfma_f32_16x16x32_bf16 v[84:87], v[164:167], v[96:99], v[84:87]\n\t"
    "ds_read_b128 v[124:127], %[da] offset:14336\n\t"
    "v_mfma_f32_16x16x32_bf16 v[88:91], v[160:163], v[100:103], v[88:91]\n\t"
    "v_mfma_f32_16x16x32_bf16 v[92:95], v[164:167], v[100:103], v[92:95]\n\t"
    "s_barrier\n\t"
    "s_waitcnt lgkmcnt(4)\n\t"
    "v_mfma_f32_16x16x32_bf16 v[80:83], v[168:171], v[104:107], v[80:83]\n\t"
    "ds_read_b128 v[96:99], %[ea] offset:16384\n\t"
    "v_mfma_f32_16x16x32_bf16 v[84:87], v[172:175], v[104:107], v[84:87]\n\t"
    "ds_read_b128 v[100:103], %[ea] offset:18432\n\t"
    "v_mfma_f32_16x16x32_bf16 v[88:91], v[168:171], v[108:111], v[88:91]\n\t"
    "v_mfma_f32_16x16x32_bf16 v[92:95], v[172:175], v[108:111], v[92:95]\n\t"
    "s_waitcnt lgkmcnt(4)\n\t"
    "v_mfma_f32_16x16x32_bf16 v[80:83], v[176:179], v[112:115], v[80:83]\n\t"
    "ds_read_b128 v[104:107], %[da] offset:16384\n\t"
    "v_mfma_f32_16x16x32_bf16 v[84:87], v[180:183], v[112:115], v[84:87]\n\t"
    "ds_read_b128 v[108:111], %[da] offset:18432\n\t"
    "v_mfma_f32_16x16x32_bf16 v[88:91], v[176:179], v[116:119], v[88:91]\n\t"
    "v_mfma_f32_16x16x32_bf16 v[92:95], v[180:183], v[116:119], v[92:95]\n\t"
    "s_waitcnt lgkmcnt(4)\n\t"
    "v_mfma_f32_16x16x32_bf16 v[80:83], v[184:187], v[120:123], v[80:83]\n\t"
    "ds_read_b128 v[112:115], %[ea] offset:20480\n\t"
    "v_mfma_f32_16x16x32_bf16 v[84:87], v[188:191], v[120:123], v[84:87]\n\t"
    "ds_read_b128 v[116:119], %[ea] offset:22528\n\t"
    "v_mfma_f32_16x16x32_bf16 v[88:91], v[184:187], v[124:127], v[88:91]\n\t"
    "v_mfma_f32_16x16x32_bf16 v[92:95], v[188:191], v[124:127], v[92:95]\n\t"
    "s_waitcnt lgkmcnt(4)\n\t"
    "v_mfma_f32_16x16x32_bf16 v[80:83], v[192:195], v[96:99], v[80:83]\n\t"
    "ds_read_b128 v[120:123], %[da] offset:20480\n\t"
    "v_mfma_f32_16x16x32_bf16 v[84:87], v[196:199], v[96:99], v[84:87]\n\t"
    "ds_read_b128 v[124:127], %[da] offset:22528\n\t"
    "v_mfma_f32_16x16x32_bf16 v[88:91], v[192:195], v[100:103], v[88:91]\n\t"
    "v_mfma_f32_16x16x32_bf16 v[92:95], v[196:199], v[100:103], v[92:95]\n\t"
    "s_barrier\n\t"
    "s_waitcnt lgkmcnt(4)\n\t"
    "v_mfma_f32_16x16x32_bf16 v[80:83], v[200:203], v[104:107], v[80:83]\n\t"
    "ds_read_b128 v[96:99], %[ea] offset:24576\n\t"
    "v_mfma_f32_16x16x32_bf16 v[84:87], v[204:207], v[104:107], v[84:87]\n\t"
    "ds_read_b128 v[100:103], %[ea] offset:26624\n\t"
    "v_mfma_f32_16x16x32_bf16 v[88:91], v[200:203], v[108:111], v[88:91]\n\t"
    "v_mfma_f32_16x16x32_bf16 v[92:95], v[204:207], v[108:111], v[92:95]\n\t"
    "s_waitcnt lgkmcnt(4)\n\t"
    "v_mfma_f32_16x16x32_bf16 v[80:83], v[208:211], v[112:115], v[80:83]\n\t"
    "ds_read_b128 v[104:107], %[da] offset:24576\n\t"
    "v_mfma_f32_16x16x32_bf16 v[84:87], v[212:215], v[112:115], v[84:87]\n\t"
    "ds_read_b128 v[108:111], %[da] offset:26624\n\t"
    "v_mfma_f32_16x16x32_bf16 v[88:91], v[208:211], v[116:119], v[88:91]\n\t"
    "v_mfma_f32_16x16x32_bf16 v[92:95], v[212:215], v[116:119], v[92:95]\n\t"
    "s_waitcnt lgkmcnt(4)\n\t"
    "v_mfma_f32_16x16x32_bf16 v[80:83], v[216:219], v[120:123], v[80:83]\n\t"
    "ds_read_b128 v[112:115], %[ea] offset:28672\n\t"
    "v_mfma_f32_16x16x32_bf16 v[84:87], v[220:223], v[120:123], v[84:87]\n\t"
    "ds_read_b128 v[116:119], %[ea] offset:30720\n\t"
    "v_mfma_f32_16x16x32_bf16 v[88:91], v[216:219], v[124:127], v[88:91]\n\t"
    "v_mfma_f32_16x16x32_bf16 v[92:95], v[220:223], v[124:127], v[92:95]\n\t"
    "s_waitcnt lgkmcnt(4)\n\t"
    "v_mfma_f32_16x16x32_bf16 v[80:83], v[224:227], v[96:99], v[80:83]\n\t"
    "ds_read_b128 v[120:123], %[da] offset:28672\n\t"
    "v_mfma_f32_16x16x32_bf16 v[84:87], v[228:231], v[96:99], v[84:87]\n\t"
    "ds_read_b128 v[124:127], %[da] offset:30720\n\t"
    "v_mfma_f32_16x16x32_bf16 v[88:91], v[224:227], v[100:103], v[88:91]\n\t"
    "v_mfma_f32_16x16x32_bf16 v[92:95], v[228:231], v[100:103], v[92:95]\n\t"
    "s_waitcnt lgkmcnt(4)\n\t"
    "v_mfma_f32_16x16x32_bf16 v[80:83], v[232:235], v[104:107], v[80:83]\n\t"
    "v_mfma_f32_16x16x32_bf16 v[84:87], v[236:239], v[104:107], v[84:87]\n\t"
    "v_mfma_f32_16x16x32_bf16 v[88:91], v[232:235], v[108:111], v[88:91]\n\t"
    "v_xor_b32 %[ea], 0x10000, %[ea]\n\t"
    "v_mfma_f32_16x16x32_bf16 v[92:95], v[236:239], v[108:111], v[92:95]\n\t"
    "v_xor_b32 %[da], 0x10000, %[da]\n\t"
    "s_waitcnt lgkmcnt(2)\n\t"
    "v_mfma_f32_16x16x32_bf16 v[80:83], v[240:243], v[112:115], v[80:83]\n\t"
    "v_mfma_f32_16x16x32_bf16 v[84:87], v[244:247], v[112:115], v[84:87]\n\t"
    "v_mfma_f32_16x16x32_bf16 v[88:91], v[240:243], v[116:119], v[88:91]\n\t"
    "s_add_u32 %[b], %[b], 1\n\t"
    "v_mfma_f32_16x16x32_bf16 v[92:95], v[244:247], v[116:119], v[92:95]\n\t"
    "s_waitcnt lgkmcnt(0)\n\t"
    "v_mfma_f32_16x16x32_bf16 v[80:83], v[248:251], v[120:123], v[80:83]\n\t"
    "v_mfma_f32_16x16x32_bf16 v[84:87], v[252:255], v[120:123], v[84:87]\n\t"
    "v_mfma_f32_16x16x32_bf16 v[88:91], v[248:251], v[124:127], v[88:91]\n\t"
    "v_mfma_f32_16x16x32_bf16 v[92:95], v[252:255], v[124:127], v[92:95]\n\t"
    "s_nop 6\n\t"
    "v_cvt_pk_bf16_f32 v40, v80, v81\n\t"
    "v_cvt_pk_bf16_f32 v41, v82, v83\n\t"
    "v_cvt_pk_bf16_f32 v42, v84, v85\n\t"
    "v_cvt_pk_bf16_f32 v43, v86, v87\n\t"
    "global_store_dwordx4 %[co0], v[40:43], s[36:37] sc1\n\t"
    "v_cvt_pk_bf16_f32 v44, v88, v89\n\t"
    "v_cvt_pk_bf16_f32 v45, v90, v91\n\t"
    "v_cvt_pk_bf16_f32 v46, v92, v93\n\t"
    "v_cvt_pk_bf16_f32 v47, v94, v95\n\t"
    "global_store_dwordx4 %[co1], v[44:47], s[36:37] sc1\n\t"
    "2:\n\t"
    "ds_read_b32 v39, %[fa]\n\t"
    "ds_read_b128 v[48:51], %[rw] offset:0\n\t"
    "ds_read_b128 v[52:55], %[rw] offset:1024\n\t"
    "ds_read_b128 v[56:59], %[rw] offset:2048\n\t"
    "ds_read_b128 v[60:63], %[rw] offset:3072\n\t"
    "s_waitcnt lgkmcnt(2)\n\t"
    "v_readfirstlane_b32 %[pp], v39\n\t"
    "s_and_b32 %[st], %[pp], 0xff\n\t"
    "s_cmp_eq_u32 %[st], %[b]\n\t"
    "s_cbranch_scc1 3f\n\t"
    "s_waitcnt lgkmcnt(0)\n\t"
    "s_branch 2b\n\t"
    "3:\n\t"
    "v_cvt_pk_bf16_f32 v48, v48, v49\n\t"
    "v_cvt_pk_bf16_f32 v49, v50, v51\n\t"
    "v_cvt_pk_bf16_f32 v50, v52, v53\n\t"
    "v_cvt_pk_bf16_f32 v51, v54, v55\n\t"
    "global_store_dwordx4 %[co0], v[48:51], s[36:37] offset:2048 sc1\n\t"
    "s_waitcnt lgkmcnt(0)\n\t"
    "v_cvt_pk_bf16_f32 v56, v56, v57\n\t"
    "v_cvt_pk_bf16_f32 v57, v58, v59\n\t"
    "v_cvt_pk_bf16_f32 v58, v60, v61\n\t"
    "v_cvt_pk_bf16_f32 v59, v62, v63\n\t"
    "global_store_dwordx4 %[co1], v[56:59], s[36:37] offset:2048 sc1\n\t"
    "s_add_u32 s36, s36, %[ab]\n\t"
    "s_addc_u32 s37, s37, 0\n\t"
    "s_lshr_b32 %[pp], %[pp], 8\n\t"
    "s_cbranch_scc1 1b\n\t"
    "v_mov_b32 v72, %[cn]\n\t"
    "v_mov_b32 v73, 1\n\t"
    "s_add_u32 %[st], %[b], %[po]\n\t"
    "v_mov_b32 v74, %[st]\n\t"
    "v_mov_b32 v75, 0\n\t"
    "s_waitcnt vmcnt(0)\n\t"
    "s_mov_b64 exec, 1\n\t"
    "ds_add_rtn_u32 v73, v72, v73\n\t"
    "s_waitcnt lgkmcnt(0)\n\t"
    "v_readfirstlane_b32 %[st], v73\n\t"
    "s_and_b32 %[st], %[st], 3\n\t"
    "s_cmp_lg_u32 %[st], 3\n\t"
    "s_cbranch_scc1 61f\n\t"
    "global_store_dword v75, v74, %[pg] sc1\n\t"
    "61:\n\t"
    "s_mov_b64 exec, -1\n\t"
    "s_mov_b32 %[pp], 0\n\t"
    "s_cmp_lt_i32 %[b], %[ns]\n\t"
    "s_cbranch_scc1 1b\n\t"
    "s_branch 9f\n\t"
    "7:\n\t"
    "v_mov_b32 v72, %[cn]\n\t"
    "v_mov_b32 v73, 1\n\t"
    "s_add_u32 %[st], %[b], %[po]\n\t"
    "v_mov_b32 v74, %[st]\n\t"
    "v_mov_b32 v75, 0\n\t"
    "s_waitcnt vmcnt(0)\n\t"
    "s_mov_b64 exec, 1\n\t"
    "ds_add_rtn_u32 v73, v72, v73\n\t"
    "s_waitcnt lgkmcnt(0)\n\t"
    "v_readfirstlane_b32 %[st], v73\n\t"
    "s_and_b32 %[st], %[st], 3\n\t"
    "s_cmp_lg_u32 %[st], 3\n\t"
    "s_cbranch_scc1 61f\n\t"
    "global_store_dword v75, v74, %[pg] sc1\n\t"
    "61:\n\t"
    "s_mov_b64 exec, -1\n\t"
    "s_mov_b32 %[pp], 0\n\t"
    "s_branch 8b\n\t"
    "9:"
    : [ea] "+&v"(ea), [da] "+&v"(da), [b] "+&s"(b), [st] "=&s"(st), "+{s[36:37]}"(p0), [pp] "+&s"(pp)
    : [ns] "s"(ns), [ab] "s"(ab), [rw] "v"(rw), [fa] "v"(fa), "{v[128:159]}"(W[0]), "{v[160:191]}"(W[1]), "{v[192:223]}"(W[2]), "{v[224:255]}"(W[3]), [po] "s"(po), [cn] "s"(cn), [pg] "s"(pg), [co0] "v"(co0), [co1] "v"(co1)
    : "v39", "v40", "v41", "v42", "v43", "v44", "v45", "v46", "v47", "v48", "v49", "v50", "v51", "v52", "v53", "v54", "v55", "v56", "v57", "v58", "v59", "v60", "v61", "v62", "v63", "v72", "v73", "v74", "v75", "v80", "v81", "v82", "v83", "v84", "v85", "v86", "v87", "v88", "v89", "v90", "v91", "v92", "v93", "v94", "v95", "v96", "v97", "v98", "v99", "v100", "v101", "v102", "v103", "v104", "v105", "v106", "v107", "v108", "v109", "v110", "v111", "v112", "v113", "v114", "v115", "v116", "v117", "v118", "v119", "v120", "v121", "v122", "v123", "v124", "v125", "v126", "v127", "vcc", "scc", "m0", "memory");
}
__device__ __forceinline__ void fin_ld(const u32x16& pd, unsigned rw, unsigned fa, unsigned fv) {
  asm volatile("ds_write_b128 %[rw], v[80:83] offset:0\n\t"
    "ds_write_b128 %[rw], v[84:87] offset:1024\n\t"
    "ds_write_b128 %[rw], v[88:91] offset:2048\n\t"
    "ds_write_b128 %[rw], v[92:95] offset:3072\n\t"
    "ds_write_b32 %[fa], %[fv]"
    :
    : [rw] "v"(rw), "{v[80:95]}"(pd), [fa] "v"(fa), [fv] "v"(fv)
    : "memory");
}
__device__ __forceinline__ void fin_st0(const u32x16& pd, unsigned rw, unsigned fa, unsigned fv, const char* cb, unsigned co0, unsigned co1) {
  asm volatile("1:\n\t"
    "ds_read_b32 v39, %[fa]\n\t"
    "ds_read_b128 v[48:51], %[rw] offset:0\n\t"
    "ds_read_b128 v[52:55], %[rw] offset:1024\n\t"
    "ds_read_b128 v[56:59], %[rw] offset:2048\n\t"
    "ds_read_b128 v[60:63], %[rw] offset:3072\n\t"
    "s_waitcnt lgkmcnt(2)\n\t"
    "v_cmp_ne_u32 vcc, v39, %[fv]\n\t"
    "s_cbranch_vccz 2f\n\t"
    "s_waitcnt lgkmcnt(0)\n\t"
    "s_branch 1b\n\t"
    "2:\n\t"
    "v_add_f32 v48, v48, v80\n\t"
    "v_add_f32 v49, v49, v81\n\t"
    "v_add_f32 v50, v50, v82\n\t"
    "v_add_f32 v51, v51, v83\n\t"
    "v_add_f32 v52, v52, v84\n\t"
    "v_add_f32 v53, v53, v85\n\t"
    "v_add_f32 v54, v54, v86\n\t"
    "v_add_f32 v55, v55, v87\n\t"
    "v_cvt_pk_bf16_f32 v40, v48, v49\n\t"
    "v_cvt_pk_bf16_f32 v41, v50, v51\n\t"
    "v_cvt_pk_bf16_f32 v42, v52, v53\n\t"
    "v_cvt_pk_bf16_f32 v43, v54, v55\n\t"
    "global_store_dwordx4 %[co0], v[40:43], %[cb] sc1\n\t"
    "s_waitcnt lgkmcnt(0)\n\t"
    "v_add_f32 v56, v56, v88\n\t"
    "v_add_f32 v57, v57, v89\n\t"
    "v_add_f32 v58, v58, v90\n\t"
    "v_add_f32 v59, v59, v91\n\t"
    "v_add_f32 v60, v60, v92\n\t"
    "v_add_f32 v61, v61, v93\n\t"
    "v_add_f32 v62, v62, v94\n\t"
    "v_add_f32 v63, v63, v95\n\t"
    "v_cvt_pk_bf16_f32 v44, v56, v57\n\t"
    "v_cvt_pk_bf16_f32 v45, v58, v59\n\t"
    "v_cvt_pk_bf16_f32 v46, v60, v61\n\t"
    "v_cvt_pk_bf16_f32 v47, v62, v63\n\t"
    "global_store_dwordx4 %[co1], v[44:47], %[cb] sc1\n\t"
    "s_nop 1"
    :
    : [rw] "v"(rw), "{v[80:95]}"(pd), [fa] "v"(fa), [fv] "v"(fv), [cb] "s"(cb), [co0] "v"(co0), [co1] "v"(co1)
    : "v39", "v40", "v41", "v42", "v43", "v44", "v45", "v46", "v47", "v48", "v49", "v50", "v51", "v52", "v53", "v54", "v55", "v56", "v57", "v58", "v59", "v60", "v61", "v62", "v63", "vcc", "memory");
}
__device__ __forceinline__ void fin_st1(const u32x16& pd, unsigned rw, unsigned fa, unsigned fv, const char* cb, unsigned co0, unsigned co1) {
  asm volatile("1:\n\t"
    "ds_read_b32 v39, %[fa]\n\t"
    "ds_read_b128 v[48:51], %[rw] offset:0\n\t"
    "ds_read_b128 v[52:55], %[rw] offset:1024\n\t"
    "ds_read_b128 v[56:59], %[rw] offset:2048\n\t"
    "ds_read_b128 v[60:63], %[rw] offset:3072\n\t"
    "s_waitcnt lgkmcnt(2)\n\t"
    "v_cmp_ne_u32 vcc, v39, %[fv]\n\t"
    "s_cbranch_vccz 2f\n\t"
    "s_waitcnt lgkmcnt(0)\n\t"
    "s_branch 1b\n\t"
    "2:\n\t"
    "v_add_f32 v48, v48, v80\n\t"
    "v_add_f32 v49, v49, v81\n\t"
    "v_add_f32 v50, v50, v82\n\t"
    "v_add_f32 v51, v51, v83\n\t"
    "v_add_f32 v52, v52, v84\n\t"
    "v_add_f32 v53, v53, v85\n\t"
    "v_add_f32 v54, v54, v86\n\t"
    "v_add_f32 v55, v55, v87\n\t"
    "s_waitcnt lgkmcnt(0)\n\t"
    "v_add_f32 v56, v56, v88\n\t"
    "v_add_f32 v57, v57, v89\n\t"
    "v_add_f32 v58, v58, v90\n\t"
    "v_add_f32 v59, v59, v91\n\t"
    "v_add_f32 v60, v60, v92\n\t"
    "v_add_f32 v61, v61, v93\n\t"
    "v_add_f32 v62, v62, v94\n\t"
    "v_add_f32 v63, v63, v95\n\t"
    "v_mul_f32 v40, %[nl], v48\n\t"
    "v_mul_f32 v41, %[nl], v49\n\t"
    "v_mul_f32 v42, %[nl], v50\n\t"
    "v_mul_f32 v43, %[nl], v51\n\t"
    "v_mul_f32 v44, %[nl], v56\n\t"
    "v_mul_f32 v45, %[nl], v57\n\t"
    "v_mul_f32 v46, %[nl], v58\n\t"
    "v_mul_f32 v47, %[nl], v59\n\t"
    "v_exp_f32 v40, v40\n\t"
    "v_exp_f32 v41, v41\n\t"
    "v_exp_f32 v42, v42\n\t"
    "v_exp_f32 v43, v43\n\t"
    "v_exp_f32 v44, v44\n\t"
    "v_exp_f32 v45, v45\n\t"
    "v_exp_f32 v46, v46\n\t"
    "v_exp_f32 v47, v47\n\t"
    "v_add_f32 v40, 1.0, v40\n\t"
    "v_add_f32 v41, 1.0, v41\n\t"
    "v_add_f32 v42, 1.0, v42\n\t"
    "v_add_f32 v43, 1.0, v43\n\t"
    "v_add_f32 v44, 1.0, v44\n\t"
    "v_add_f32 v45, 1.0, v45\n\t"
    "v_add_f32 v46, 1.0, v46\n\t"
    "v_add_f32 v47, 1.0, v47\n\t"
    "v_rcp_f32 v40, v40\n\t"
    "v_rcp_f32 v41, v41\n\t"
    "v_rcp_f32 v42, v42\n\t"
    "v_rcp_f32 v43, v43\n\t"
    "v_rcp_f32 v44, v44\n\t"
    "v_rcp_f32 v45, v45\n\t"
    "v_rcp_f32 v46, v46\n\t"
    "v_rcp_f32 v47, v47\n\t"
    "v_mul_f32 v40, v48, v40\n\t"
    "v_mul_f32 v41, v49, v41\n\t"
    "v_mul_f32 v42, v50, v42\n\t"
    "v_mul_f32 v43, v51, v43\n\t"
    "v_mul_f32 v44, v56, v44\n\t"
    "v_mul_f32 v45, v57, v45\n\t"
    "v_mul_f32 v46, v58, v46\n\t"
    "v_mul_f32 v47, v59, v47\n\t"
    "v_mul_f32 v40, v40, v52\n\t"
    "v_mul_f32 v41, v41, v53\n\t"
    "v_mul_f32 v42, v42, v54\n\t"
    "v_mul_f32 v43, v43, v55\n\t"
    "v_mul_f32 v44, v44, v60\n\t"
    "v_mul_f32 v45, v45, v61\n\t"
    "v_mul_f32 v46, v46, v62\n\t"
    "v_mul_f32 v47, v47, v63\n\t"
    "v_cvt_pk_bf16_f32 v48, v40, v41\n\t"
    "v_cvt_pk_bf16_f32 v49, v42, v43\n\t"
    "v_cvt_pk_bf16_f32 v50, v44, v45\n\t"
    "v_cvt_pk_bf16_f32 v51, v46, v47\n\t"
    "s_nop 1\n\t"
    "v_permlane16_swap_b32 v48, v50\n\t"
    "v_permlane16_swap_b32 v49, v51\n\t"
    "global_store_dwordx4 %[co0], v[48:51], %[cb] sc1\n\t"
    "s_nop 1"
    :
    : [rw] "v"(rw), "{v[80:95]}"(pd), [fa] "v"(fa), [fv] "v"(fv), [cb] "s"(cb), [co0] "v"(co0), [nl] "s"(-LOG2E)
    : "v39", "v40", "v41", "v42", "v43", "v44", "v45", "v46", "v47", "v48", "v49", "v50", "v51", "v52", "v53", "v54", "v55", "v56", "v57", "v58", "v59", "v60", "v61", "v62", "v63", "vcc", "memory");
}
__device__ __forceinline__ void fin_st2(const u32x16& pd, unsigned rw, unsigned fa, unsigned fv, const char* cb, unsigned co0, unsigned co1) {
  asm volatile("s_nop 1\n\t"
    "v_cvt_pk_bf16_f32 v40, v80, v81\n\t"
    "v_cvt_pk_bf16_f32 v41, v82, v83\n\t"
    "v_cvt_pk_bf16_f32 v42, v84, v85\n\t"
    "v_cvt_pk_bf16_f32 v43, v86, v87\n\t"
    "global_store_dwordx4 %[co0], v[40:43], %[cb] sc1\n\t"
    "v_cvt_pk_bf16_f32 v44, v88, v89\n\t"
    "v_cvt_pk_bf16_f32 v45, v90, v91\n\t"
    "v_cvt_pk_bf16_f32 v46, v92, v93\n\t"
    "v_cvt_pk_bf16_f32 v47, v94, v95\n\t"
    "global_store_dwordx4 %[co1], v[44:47], %[cb] sc1\n\t"
    "1:\n\t"
    "ds_read_b32 v39, %[fa]\n\t"
    "ds_read_b128 v[48:51], %[rw] offset:0\n\t"
    "ds_read_b128 v[52:55], %[rw] offset:1024\n\t"
    "ds_read_b128 v[56:59], %[rw] offset:2048\n\t"
    "ds_read_b128 v[60:63], %[rw] offset:3072\n\t"
    "s_waitcnt lgkmcnt(2)\n\t"
    "v_cmp_ne_u32 vcc, v39, %[fv]\n\t"
    "s_cbranch_vccz 2f\n\t"
    "s_waitcnt lgkmcnt(0)\n\t"
    "s_branch 1b\n\t"
    "2:\n\t"
    "v_cvt_pk_bf16_f32 v48, v48, v49\n\t"
    "v_cvt_pk_bf16_f32 v49, v50, v51\n\t"
    "v_cvt_pk_bf16_f32 v50, v52, v53\n\t"
    "v_cvt_pk_bf16_f32 v51, v54, v55\n\t"
    "global_store_dwordx4 %[co0], v[48:51], %[cb] offset:2048 sc1\n\t"
    "s_waitcnt lgkmcnt(0)\n\t"
    "v_cvt_pk_bf16_f32 v56, v56, v57\n\t"
    "v_cvt_pk_bf16_f32 v57, v58, v59\n\t"
    "v_cvt_pk_bf16_f32 v58, v60, v61\n\t"
    "v_cvt_pk_bf16_f32 v59, v62, v63\n\t"
    "global_store_dwordx4 %[co1], v[56:59], %[cb] offset:2048 sc1\n\t"
    "s_nop 1"
    :
    : [rw] "v"(rw), "{v[80:95]}"(pd), [fa] "v"(fa), [fv] "v"(fv), [cb] "s"(cb), [co0] "v"(co0), [co1] "v"(co1)
    : "v39", "v40", "v41", "v42", "v43", "v44", "v45", "v46", "v47", "v48", "v49", "v50", "v51", "v52", "v53", "v54", "v55", "v56", "v57", "v58", "v59", "v60", "v61", "v62", "v63", "vcc", "memory");
}
template <int R> __device__ __forceinline__ void gw_load(u32x32& w, unsigned a0, unsigned a1);
template <> __device__ __forceinline__ void gw_load<0>(u32x32& w, unsigned a0, unsigned a1) {
  asm volatile("ds_read_b64_tr_b16 v[128:129], %1 offset:0\n\t"
    "ds_read_b64_tr_b16 v[130:131], %1 offset:512\n\t"
    "ds_read_b64_tr_b16 v[132:133], %2 offset:0\n\t"
    "ds_read_b64_tr_b16 v[134:135], %2 offset:512\n\t"
    "ds_read_b64_tr_b16 v[136:137], %1 offset:4096\n\t"
    "ds_read_b64_tr_b16 v[138:139], %1 offset:4608\n\t"
    "ds_read_b64_tr_b16 v[140:141], %2 offset:4096\n\t"
    "ds_read_b64_tr_b16 v[142:143], %2 offset:4608\n\t"
    "s_waitcnt lgkmcnt(0)\n\t"
    "ds_read_b64_tr_b16 v[144:145], %1 offset:16384\n\t"
    "ds_read_b64_tr_b16 v[146:147], %1 offset:16896\n\t"
    "ds_read_b64_tr_b16 v[148:149], %2 offset:16384\n\t"
    "ds_read_b64_tr_b16 v[150:151], %2 offset:16896\n\t"
    "ds_read_b64_tr_b16 v[152:153], %1 offset:20480\n\t"
    "ds_read_b64_tr_b16 v[154:155], %1 offset:20992\n\t"
    "ds_read_b64_tr_b16 v[156:157], %2 offset:20480\n\t"
    "ds_read_b64_tr_b16 v[158:159], %2 offset:20992\n\t"
    "s_waitcnt lgkmcnt(0)"
    : "=&{v[128:159]}"(w) : "v"(a0), "v"(a1) : "memory");
}
template <> __device__ __forceinline__ void gw_load<1>(u32x32& w, unsigned a0, unsigned a1) {
  asm volatile("ds_read_b64_tr_b16 v[160:161], %1 offset:0\n\t"
    "ds_read_b64_tr_b16 v[162:163], %1 offset:512\n\t"
    "ds_read_b64_tr_b16 v[164:165], %2 offset:0\n\t"
    "ds_read_b64_tr_b16 v[166:167], %2 offset:512\n\t"
    "ds_read_b64_tr_b16 v[168:169], %1 offset:4096\n\t"
    "ds_read_b64_tr_b16 v[170:171], %1 offset:4608\n\t"
    "ds_read_b64_tr_b16 v[172:173], %2 offset:4096\n\t"
    "ds_read_b64_tr_b16 v[174:175], %2 offset:4608\n\t"
    "s_waitcnt lgkmcnt(0)\n\t"
    "ds_read_b64_tr_b16 v[176:177], %1 offset:16384\n\t"
    "ds_read_b64_tr_b16 v[178:179], %1 offset:16896\n\t"
    "ds_read_b64_tr_b16 v[180:181], %2 offset:16384\n\t"
    "ds_read_b64_tr_b16 v[182:183], %2 offset:16896\n\t"
    "ds_read_b64_tr_b16 v[184:185], %1 offset:20480\n\t"
    "ds_read_b64_tr_b16 v[186:187], %1 offset:20992\n\t"
    "ds_read_b64_tr_b16 v[188:189], %2 offset:20480\n\t"
    "ds_read_b64_tr_b16 v[190:191], %2 offset:20992\n\t"
    "s_waitcnt lgkmcnt(0)"
    : "=&{v[160:191]}"(w) : "v"(a0), "v"(a1) : "memory");
}
template <> __device__ __forceinline__ void gw_load<2>(u32x32& w, unsigned a0, unsigned a1) {
  asm volatile("ds_read_b64_tr_b16 v[192:193], %1 offset:0\n\t"
    "ds_read_b64_tr_b16 v[194:195], %1 offset:512\n\t"
    "ds_read_b64_tr_b16 v[196:197], %2 offset:0\n\t"
    "ds_read_b64_tr_b16 v[198:199], %2 offset:512\n\t"
    "ds_read_b64_tr_b16 v[200:201], %1 offset:4096\n\t"
    "ds_read_b64_tr_b16 v[202:203], %1 offset:4608\n\t"
    "ds_read_b64_tr_b16 v[204:205], %2 offset:4096\n\t"
    "ds_read_b64_tr_b16 v[206:207], %2 offset:4608\n\t"
    "s_waitcnt lgkmcnt(0)\n\t"
    "ds_read_b64_tr_b16 v[208:209], %1 offset:16384\n\t"
    "ds_read_b64_tr_b16 v[210:211], %1 offset:16896\n\t"
    "ds_read_b64_tr_b16 v[212:213], %2 offset:16384\n\t"
    "ds_read_b64_tr_b16 v[214:215], %2 offset:16896\n\t"
    "ds_read_b64_tr_b16 v[216:217], %1 offset:20480\n\t"
    "ds_read_b64_tr_b16 v[218:219], %1 offset:20992\n\t"
    "ds_read_b64_tr_b16 v[220:221], %2 offset:20480\n\t"
    "ds_read_b64_tr_b16 v[222:223], %2 offset:20992\n\t"
    "s_waitcnt lgkmcnt(0)"
    : "=&{v[192:223]}"(w) : "v"(a0), "v"(a1) : "memory");
}
template <> __device__ __forceinline__ void gw_load<3>(u32x32& w, unsigned a0, unsigned a1) {
  asm volatile("ds_read_b64_tr_b16 v[224:225], %1 offset:0\n\t"
    "ds_read_b64_tr_b16 v[226:227], %1 offset:512\n\t"
    "ds_read_b64_tr_b16 v[228:229], %2 offset:0\n\t"
    "ds_read_b64_tr_b16 v[230:231], %2 offset:512\n\t"
    "ds_read_b64_tr_b16 v[232:233], %1 offset:4096\n\t"
    "ds_read_b64_tr_b16 v[234:235], %1 offset:4608\n\t"
    "ds_read_b64_tr_b16 v[236:237], %2 offset:4096\n\t"
    "ds_read_b64_tr_b16 v[238:239], %2 offset:4608\n\t"
    "s_waitcnt lgkmcnt(0)\n\t"
    "ds_read_b64_tr_b16 v[240:241], %1 offset:16384\n\t"
    "ds_read_b64_tr_b16 v[242:243], %1 offset:16896\n\t"
    "ds_read_b64_tr_b16 v[244:245], %2 offset:16384\n\t"
    "ds_read_b64_tr_b16 v[246:247], %2 offset:16896\n\t"
    "ds_read_b64_tr_b16 v[248:249], %1 offset:20480\n\t"
    "ds_read_b64_tr_b16 v[250:251], %1 offset:20992\n\t"
    "ds_read_b64_tr_b16 v[252:253], %2 offset:20480\n\t"
    "ds_read_b64_tr_b16 v[254:255], %2 offset:20992\n\t"
    "s_waitcnt lgkmcnt(0)"
    : "=&{v[224:255]}"(w) : "v"(a0), "v"(a1) : "memory");
}
template <int I, int E, class F> __device__ __forceinline__ void sfor(F&& f) { if constexpr (I < E) { f(std::integral_constant<int, I>{}); sfor<I + 1, E>(f); } }
#define WALL 1
template <int ROLE, int KQ, class F>
__device__ __forceinline__ void wload(char* smem, int wave_id_, const u16* W0, const u16* W1, int ldb, u32x32 (&bw)[4], int rot, F&& hook) {
  constexpr int ABUF = 65536;
  const int tid = opaque_tid(), lane = tid & 63, wave = WAVE_ID;
  const int kq = KQ == 2 ? wave >> 2 : 0, nq = KQ == 2 ? wave & 3 : wave;
  const int g = lane >> 4, r16 = lane & 15, q4 = r16 >> 2, p4 = lane & 3;
  const unsigned ldsb = __builtin_amdgcn_readfirstlane((unsigned)(size_t)(LAS char*)smem);
  {
    const int srow = 8 * wave + (lane >> 3), ch = (lane & 7) ^ bswz(srow);
    const unsigned voff = (unsigned)(srow * ldb + ch * 8) * 2;
    const char* w0 = uni((const char*)W0); const char* w1 = uni((const char*)(ROLE == R_F1 ? W1 : W0 + 64));
    const size_t kb = (size_t)64 * ldb * 2;
    auto wsrc = [&](int r, int i) {
      const int kbi = KQ == 2 ? (ROLE == R_PART2 ? 0 : (i >> 2) * 8) + ((2 * r + ((i >> 1) & 1)) ^ rot) : 2 * r + (i >> 2);
      return KQ == 2 ? ((i & 1) ? w1 : w0) + kbi * kb + (ROLE == R_PART2 ? (i >> 2) * 256 : 0) : w0 + kbi * kb + (i & 3) * 128;
    };
    auto wstage = [&](int r) {
      const unsigned ld = ldsb + (r & 1) * ABUF + wave * 1024;
#pragma unroll
      for (int i = 0; i < 8; ++i) {
        unsigned keep;
        asm volatile("s_mov_b32 %0, m0\n\ts_mov_b32 m0, %3\n\ts_nop 0\n\tglobal_load_lds_dwordx4 %1, %2" WNT_ "\n\ts_mov_b32 m0, %0"
                     : "=&s"(keep) : "v"(voff), "s"(wsrc(r, i)), "s"(ld + i * 8192) : "memory");
      }
    };
    const int brow = 8 * g + q4, bs = bswz(brow);
    unsigned la[2];
#pragma unroll
    for (int j = 0; j < 2; ++j) {
      int blk, c16, sub;
      if (ROLE == R_F1) { blk = j; c16 = nq * 2 + (p4 >> 1); sub = (p4 & 1) * 8; }
      else { blk = nq >> 1; c16 = (nq & 1) * 4 + p4; sub = j * 8; }
      int set = kq;
      if (ROLE == R_PART2 && TLP) { set = nq >> 1; blk = nq & 1; c16 = kq * 4 + p4; }
      la[j] = (unsigned)((KQ == 2 ? set * 4 + blk : blk) * 8192 + brow * 128 + ((c16 ^ bs) << 4) + sub);
    }
    wstage(0); wstage(1);
    u32x4 raw[2][8];
#pragma unroll
    for (int r = 0; r < 2; ++r)
#pragma unroll
      for (int i = 0; i < 8; ++i) asm volatile("global_load_dwordx4 %0, %1, %2" WNT_ : "=&v"(raw[r][i]) : "v"(voff), "s"(wsrc(r + 2, i)) : "memory");
    sfor<0, 4>([&](auto r_c) __attribute__((always_inline)) {
      constexpr int r = decltype(r_c)::value;
      if (r == 0) WAIT_V(24); else if (r == 1) WAIT_V(16);
      if (r == 3) BAR(); else if (r < 2) { __builtin_amdgcn_s_barrier(); asm volatile("" ::: "memory"); }
      gw_load<r>(bw[r], ldsb + (r & 1) * ABUF + la[0], ldsb + (r & 1) * ABUF + la[1]);
      if (r == 1 || r == 2) {
        u32x4 (&q)[8] = raw[(r + 1) & 1];
        if (r == 1) asm volatile("s_waitcnt vmcnt(8)" : "+v"(q[0]), "+v"(q[1]), "+v"(q[2]), "+v"(q[3]), "+v"(q[4]), "+v"(q[5]), "+v"(q[6]), "+v"(q[7]) :: "memory");
        else asm volatile("s_waitcnt vmcnt(0)" : "+v"(q[0]), "+v"(q[1]), "+v"(q[2]), "+v"(q[3]), "+v"(q[4]), "+v"(q[5]), "+v"(q[6]), "+v"(q[7]) :: "memory");
#pragma unroll
        for (int i = 0; i < 8; ++i) *(u32x4*)(smem + ((r + 1) & 1) * ABUF + wave * 1024 + i * 8192 + lane * 16) = q[i];
        if (r == 1) BAR();
      }
      if (r == 3) { hook(); BAR(); }
    });
  }
}
#define DSR2(d0, d1, a0_, a1_, off) asm volatile("ds_read_b128 %0, %2 offset:%4\n\tds_read_b128 %1, %3 offset:%4" : "=&v"(d0), "=&v"(d1) : "v"(a0_), "v"(a1_), "i"(off))
template <int N> __device__ __forceinline__ void waitl(u32x4& a, u32x4& b) { asm volatile("s_waitcnt lgkmcnt(%2)" : "+v"(a), "+v"(b) : "i"(N)); }
__device__ __forceinline__ void glds1(const char* sbase, unsigned voff, unsigned lds) {
  unsigned keep;
  asm volatile("s_mov_b32 %0, m0\n\ts_mov_b32 m0, %3\n\ts_nop 0\n\tglobal_load_lds_dwordx4 %1, %2 sc1\n\ts_mov_b32 m0, %0"
               : "=&s"(keep) : "v"(voff), "s"(sbase), "s"(lds) : "memory");
}
#define XS(i) do { } while (0)
#define XFL(b_) do { } while (0)
#define XA_ 0
#define XF_ 0
#define XE_ 0
#define XP_ 0
#define PUBK 9
#define LATE 0
#define NOWP 2
#define NEARM 15
#define PNEAR 2
#define LATEQ 0
#define DEFP 1
#define ROTM 0
#define ROTK 7
#define BPACE 40
#define W2 1
#define PDEEP 4
template <int ROLE>
__device__ __forceinline__ void gemm7(char* smem, int wave_id_, const u16* W0, const u16* W1, int ldb, const char* A_, size_t a_blk, const Dep& dep,
                                      char* C_, size_t c_blk, unsigned* myprog, unsigned* tmo, int slot, unsigned long long* mts,
                                      bool gl, int nimm = 0, int nseg = NSEG, int wm0 = 0, int pofs = 0) {
  volatile LAS unsigned* wsl = (volatile LAS unsigned*)(smem + L_SLOT + 512);
  if (opaque_tid() < 144) *(volatile LAS unsigned*)(smem + L_SLOT + opaque_tid() * 4) = 0u;
  const int tid = opaque_tid(), lane = tid & 63, wave = WAVE_ID;
  const int kq = wave >> 2, nq = wave & 3;
  const unsigned ldsb = __builtin_amdgcn_readfirstlane((unsigned)(size_t)(LAS char*)smem);
  const int rot = (ROTM >> ROLE) & 1 ? slot & ROTK : 0;
  const char* A = uni(A_) + nq * 1024;
  constexpr bool NEAR = (NEARM >> ROLE) & 1;
  constexpr int AH = NEAR ? 2 : 3, PD = NEAR ? PNEAR : PDEEP;
  constexpr bool P2C = ROLE == R_PART2;
  int pw = 0;
  int wm = wm0, nh = 0;
  auto lpoll = [&](int b, bool hs = false) {
    unsigned w;
    if (wave == 4) {
      for (int spins = 0;; ++spins) {
        unsigned v;
        if (hs) { WAIT_V(0); v = *(volatile LAS unsigned*)(smem + L_SLOT + 768 + lane_id() * 4); }
        else asm volatile("s_nop 4\n\tglobal_load_dword %0, %1, %2 sc1\n\ts_waitcnt vmcnt(0)" : "=&v"(v) : "v"(dep.voff), "s"(dep.base) : "memory");
        w = dep_eval(v, dep);
        if ((int)w > b) break;
        if (!hs) __builtin_amdgcn_s_sleep(2);
        hs = false;
        if (give_up(spins, tmo, 9)) { w = NSEG; break; }
      }
      w = w < 255u ? w : 255u;
      if (lane == 0) wsl[0] = ((unsigned)(b + 1) << 8) | w;
    } else {
      for (;;) {
        w = __builtin_amdgcn_readfirstlane(wsl[0]);
        if ((w >> 8) == (unsigned)(b + 1)) break;
        __builtin_amdgcn_s_sleep(1);
      }
      w &= 255u;
    }
    return (int)w;
  };
  auto pubw = [&](int n) {
    WAIT_V(0);
    if (lane == 0) {
      const unsigned o = __hip_atomic_fetch_add((LAS unsigned*)(smem + L_SLOT + 516), 1u, __ATOMIC_RELAXED, __HIP_MEMORY_SCOPE_WORKGROUP);
      if ((o & 3u) == 3u) {
        st4_sc1(myprog, (unsigned)(n + pofs));
      }
    }
    asm volatile("s_waitcnt lgkmcnt(0)" ::: "memory");
  };
  auto hsrc = [&](int hh) { return A + (size_t)(hh >> 1) * a_blk + ((hh ^ (rot >> 2)) & 1) * 16384; };
  auto hlds = [&](int hh) { return ldsb + (unsigned)(((hh >> 1) & 1) * 65536 + (hh & 1) * 16384 + nq * 1024); };
#define POFF(i_) (((i_) & 1) * 32768 + ((i_) >> 1) * 4096)
#define PIECE(s_, l_, i_) asm volatile("s_add_u32 m0, %2, %3\n\ts_nop 0\n\tglobal_load_lds_dwordx4 %0, %1 sc1" :: "v"(av[i_]), "s"(s_), "s"(l_), "i"(POFF(i_)) : "memory", "scc", "m0")
  u32x32 BW[4];
  KTS(1);
  wload<ROLE, 2>(smem, wave_id_, W0, W1, ldb, BW, rot, [&]() {
    if (ROLE == R_QKV && nimm > 0) {
      if (kq) {
        wm = lpoll(0);
        const unsigned l16 = lane_id() * 16;
#pragma unroll
        for (int i = 0; i < 16; ++i) glds1(hsrc(i >> 3) + (i & 1) * 32768 + ((((i & 7) >> 1) ^ rot) & 3) * 4096, l16, hlds(i >> 3) + POFF(i & 7));
      }
      nh = 2;
    }
  });
  KTS(2);
  if (ROLE == R_QKV && nimm > 0 && !kq) wm = (int)(__builtin_amdgcn_readfirstlane(wsl[0]) & 255u);
  if (ROLE == R_QKV && opaque_tid() == 0) STS((char*)tmo - OFF_TMO, 128 + slot, 0);
  auto body = [&](auto ldr_c) __attribute__((always_inline)) {
    constexpr bool LDR = decltype(ldr_c)::value;
    if (XP_ == 0 ? !LDR : (XP_ == 1 ? LDR : false)) __builtin_amdgcn_s_setprio(1);
    unsigned av[8];
#pragma unroll
    for (int i = 0; i < 8; ++i) av[i] = lane * 16 + (P2C ? 0 : (i & 1) * 32768) + (((i >> 1) ^ rot) & 3) * 4096;
    auto issue_all = [&](int hh) {
      if (LDR) {
        const char* s_ = hsrc(hh); const unsigned l_ = hlds(hh);
#pragma unroll
        for (int i = 0; i < 8; ++i) if (!(P2C && (i & 1))) PIECE(s_, l_, i);
      }
    };
    int b = 0;
    bool hs = false;
    while (b < nseg) {
      const int ln = lane_id(), g = ln >> 4, r16 = ln & 15;
      const unsigned e0 = ldsb + (unsigned)((P2C ? 0 : kq * 32768) + (b & 1) * 65536 + (gl ? (g >> 1) * 512 + r16 * 32 + (g & 1) * 16 : r16 * 128 + ((g ^ (r16 & 7)) << 4)));
      const unsigned d0 = e0 ^ (gl ? 1024u : 64u);
      const unsigned rw = ldsb + L_RED + nq * 4096 + ln * 16;
      const unsigned co0 = ROLE == R_F1 ? (unsigned)((g & 1) * 2048 + nq * 512 + r16 * 32 + (g >> 1) * 16)
                         : ((ROLE == R_QKV ? TLQ : TLP) ? (unsigned)(nq * (P2C ? 4096 : 2048) + r16 * 64 + g * 16)
                         : (ROLE == R_QKV ? (unsigned)(r16 * 256 + (((4 * nq + g) ^ r16) << 4)) : (unsigned)(r16 * PROW + (32 * nq + 8 * g) * 2)));
      const unsigned co1 = co0 + ((ROLE == R_QKV ? TLQ : TLP) ? 1024u : (ROLE == R_QKV ? 4096u : 16u * PROW));
      u32x16 pv_;
      bool smp = false, lt0 = false;
      if ((wm < nseg ? wm : nseg) >= b + PD && b >= nimm && nh <= 2 * b + AH) {
        __builtin_amdgcn_s_barrier(); asm volatile("" ::: "memory");
        for (; nh < 2 * b + AH; ++nh) issue_all(nh);
        int lf = (wm < nseg ? wm : nseg) - (AH - 1), cp = nseg - (AH - 1);
        if (!LDR) KTS(128 + b);
        const unsigned sa = ldsb + L_SLOT + ln * 4, ad = dep.add - (AH - 1);
        if constexpr (LDR) (NEAR ? (P2C ? near_ld2 : near_ld0) : (P2C ? fast_ld2 : fast_ld0))(BW, pv_, e0, d0, b, lf, cp, sa, dep.mul, ad, rw, (unsigned)a_blk, hsrc(2 * b + AH), hsrc(2 * b + AH + 1), hlds(2 * b + AH), hlds(2 * b + AH + 1), av);
        else {
          const char* c0_ = uni((const char*)tmo - OFF_TMO + OFF_DUMP); const char* c1_ = uni(C_ + (size_t)b * c_blk);
          const int sf = b + pofs - (pw > 0 ? 1 : 0);
          const char* pg = uni((const char*)myprog);
          (NEAR ? (ROLE == R_F1 ? near_st1 : (P2C ? near_st2 : near_st0)) : (ROLE == R_F1 ? fast_st1 : (P2C ? fast_st2 : fast_st0)))(BW, pv_, e0, d0, b, lf, cp, sa, dep.mul, ad, rw, (unsigned)c_blk, c0_, c1_, co0, co1, pofs, sf, (unsigned)wave,
                                                                   ldsb + L_SLOT, dep.voff, dep.base, pg);
        }
        if (!LDR) KTS(64 + b);
        wm = lf + (AH - 1) > wm ? lf + (AH - 1) : wm;
        nh = 2 * b + AH;
        --b;
        __builtin_amdgcn_s_barrier(); asm volatile("" ::: "memory");
      } else if (NEAR && b >= BPACE) {
        if constexpr (LDR) {
          int cap = wave == 4 ? SPIN_CAP : 8 * SPIN_CAP;
          if (hs) WAIT_V(0);
          (P2C ? paced_ld2 : paced_ld0)(BW, e0, d0, b, nseg, (unsigned)a_blk, rw, ldsb + L_SLOT + 528 + nq * 4, hsrc(nh), hsrc(nh + 1), hlds(nh), cap, wm, (unsigned)(nh - 2 * b),
                                        dep.mul, dep.add, dep.voff, dep.base, wave == 4 ? ldsb + L_SLOT : 0u, ldsb + L_SLOT + ln * 4, av);
          if (cap == 0 && ln == 0) st4_sc1(tmo, 15u);
        } else (ROLE == R_F1 ? paced_st1 : (P2C ? paced_st2 : paced_st0))(BW, e0, d0, b, nseg, (unsigned)c_blk, rw, ldsb + L_SLOT + 528 + nq * 4, uni(C_ + (size_t)b * c_blk),
                                                                          __builtin_amdgcn_readfirstlane(pw > 0 ? 1u : 0u), pofs, ldsb + L_SLOT + 516, uni((const char*)myprog), co0, co1);
        break;
      } else {
        const bool pl = wm <= b;
        XS(0);
        if (pl && LDR) wm = lpoll(b, hs);
        XS(1);
        const bool cold = nh < 2 * b + 2;
        { const int upto = nh == 2 * b + 3 ? 2 * b + 4 : 2 * b + 2; for (; nh < upto; ++nh) issue_all(nh); }
        smp = nh == 2 * b + 2 && b + 1 < nseg && wm0 < nseg;
        if (LDR && wave == 4 && smp)
          asm volatile("s_mov_b32 m0, %0\n\ts_nop 3\n\tglobal_load_lds_dword %1, %2 sc1" :: "s"(ldsb + L_SLOT + (b & 1) * 256), "v"(dep.voff), "s"(dep.base) : "memory", "m0");
        XS(2);
        if (LDR && LEAN_QB) { if (nh >= 2 * b + 4) { if (P2C) WAIT_V(14); else WAIT_V(28); } else { if (P2C) WAIT_V(6); else WAIT_V(12); } }
        else if (LDR) { if (nh >= 2 * b + 4) { if (P2C) WAIT_V(12); else WAIT_V(24); } else { if (P2C) WAIT_V(4); else WAIT_V(8); } }
        BAR();
        XS(3);
        if (pl && !LDR) wm = (int)(__builtin_amdgcn_readfirstlane(wsl[0]) & 255u);
        const bool dm = nh == 2 * b + 2 && wm > b + 1 && b + 1 < nseg;
        if (ROLE == R_QKV && b < 2 && opaque_tid() == 0) STS((char*)tmo - OFF_TMO, 128 + slot, 1 + b);
        lt0 = smp && !dm;
        if constexpr (LDR) {
          if (dm) (P2C ? lean_dm1 : lean_dm0)(BW, pv_, e0, d0, 0u, hlds(nh), hsrc(nh), hsrc(nh + 1), av);
          else (P2C ? lean_ld1 : lean_ld0)(BW, pv_, e0, d0, (unsigned)(nh - 2 * b - 2), hlds(nh), hsrc(nh), hsrc(nh + 1), av, ldsb + L_SLOT + (b & 1) * 256 + ln * 4, dep.mul, dep.add, LATEQ && lt0 ? b + 1 : 0x7FFFFFFF,
                                           __builtin_amdgcn_readfirstlane(lt0 && wave == 4 ? ldsb + L_SLOT + 768 : 0u), dep.voff, dep.base);
        }
        else lean_st0(BW, pv_, e0, d0, __builtin_amdgcn_readfirstlane(pw > 0 ? (cold ? 4u : 1u) : 0u), (unsigned)(pw + pofs), ldsb + L_SLOT + 516, ldsb + L_SLOT + 256,
                     dep.voff, dep.base, uni((const char*)myprog));
        if (dm) nh += 2;
        XS(5);
      }
      if constexpr (LDR) fin_ld(pv_, rw, ldsb + L_SLOT + 528 + nq * 4, (unsigned)(b + 1));
      else (ROLE == R_F1 ? fin_st1 : (P2C ? fin_st2 : fin_st0))(pv_, rw, ldsb + L_SLOT + 528 + nq * 4, (unsigned)(b + 1), uni(C_ + (size_t)b * c_blk), co0, co1);
      XS(6);
      if (smp) {
        const int w = (int)dep_eval(*(volatile LAS unsigned*)(smem + L_SLOT + (b & 1) * 256 + lane_id() * 4), dep);
        if (LATEQ && lt0 && w > b + 1) nh += 2;
        wm = w > wm ? w : wm;
      }
      bool now = (NOWP == 2 ? nh < 2 * b + 4 && wm < b + 1 + PD : wm <= b + 1) || b + 1 >= nseg || b < nimm;
      if (!LDR && now) {
        if (DEFP && b >= nimm && b + 1 < nseg && (wm > b + 1 || (__builtin_amdgcn_readfirstlane(wsl[0]) >> 8) == (unsigned)(b + 2))) now = false;
        else pubw(b + 1);
      }
      XS(7);
      XFL(b);
      pw = now ? 0 : b + 1;
      hs = W2 && lt0;
      ++b;
    }
    __builtin_amdgcn_s_setprio(0);
  };
  if (kq) body(std::true_type{}); else body(std::false_type{});
#undef PIECE
#undef POFF
  WAIT_V(0);
  BAR();
  KTS(3);
}
__device__ __forceinline__ void fin_role(char* smem, int wave_id_, int fidx, const float* mod, const char* P2, const u16* x1, u16* out, unsigned* cnt, unsigned* tmo,
                                         unsigned long long* mts) {
  const int tid = opaque_tid(), lane = tid & 63, wave = WAVE_ID;
  row_table(smem + L_GB, mod, cnt, tmo, tid);
  volatile LAS unsigned* wsl = (volatile LAS unsigned*)(smem + L_SLOT + 512);
  int wf = 0, par = 0;
  for (int nf = fidx >> 3; nf < NSEG; nf += 2) {
    if (wf <= nf) {
      if (wave == 0) {
        unsigned w;
        for (int spins = 0;; ++spins) {
          const unsigned v = rowmin16(ld4_sc1(cnt + (PF2 + ((lane & 31) < 28 ? (lane & 31) : 0)) * 32));
          w = min(rdl(v, 0), rdl(v, 16));
          if ((int)w > nf) break;
          __builtin_amdgcn_s_sleep(2);
          if (give_up(spins, tmo, 9)) { w = NSEG; break; }
        }
        if (lane == 0) wsl[par] = w;
      }
      BAR();
      wf = (int)__builtin_amdgcn_readfirstlane(wsl[par]);
      par ^= 1;
    }
    int ln = lane; asm volatile("" : "+v"(ln));
    if (wave == 0) WTS(256 + 8 + fidx, nf / 2, 0);
    fin_half(P2, x1, smem + L_GB + 12288, out, nf * 32 + 4 * (fidx & 7) + (wave >> 1), wave & 1, ln);
    if (wave == 0) WTS(256 + 8 + fidx, nf / 2, 2);
  }
}
constexpr int Q_MEM = 0;
constexpr int Q_QK  = 16384;
constexpr int Q_TM  = 22528;
constexpr int Q_PQ  = 24576;
constexpr int Q_K   = 32768;
constexpr int Q_RING = 32768;
constexpr int NCHAIN = 96;
#define NQQ 6
#define NQK 4
#define E_AG 8
#define RDYI -1
#define ROTW 2
#define DQU 6
#define DQM 18
#define DQD 1
#define ORSEL (xg >> 1)
#define D_Q 85
#define D_O 5
#define D_A 0
#define WOF 1
#define E_S -1
#define RAE 0
#define SPUB 60
#define RAW 5
#define E_MIN 3
#define E_W 3
#define DOU 0
#define DOM 12
#define CL0 1
#define TSW 0
#define E_B 8
#define E_O 1900
#define KDEL 5
#define VPOS 0

typedef __bf16 bf16x16 __attribute__((ext_vector_type(16)));
typedef unsigned u32x8 __attribute__((ext_vector_type(8)));
__device__ __forceinline__ f32x4 SMF(u32x4 a, u32x8 b, f32x4 c, int idx) {
  return __builtin_amdgcn_smfmac_f32_16x16x64_bf16(__builtin_bit_cast(bf16x8, a), __builtin_bit_cast(bf16x16, b), c, idx, 0, 0);
}
__device__ __forceinline__ float fold32(float a, float b) {
  const auto p = __builtin_amdgcn_permlane32_swap(__float_as_uint(a), __float_as_uint(b), false, false);
  return __uint_as_float(p[0]) + __uint_as_float(p[1]);
}
__device__ __forceinline__ f32x2 rowsum(f32x4 x) { return f32x2{fold32(x[0], x[2]), fold32(x[1], x[3])}; }
__device__ __forceinline__ unsigned nosent1(unsigned v) { return v < 0xFFFFFFFEu ? v : 0xFFFFFFFEu; }
__device__ __forceinline__ void st4s(const char* sb, unsigned vo, unsigned v) { asm volatile("global_store_dword %0, %1, %2 sc1\n\ts_nop 0" :: "v"(vo), "v"(v), "s"(sb) : "memory"); }
__device__ __forceinline__ void st8s(const char* sb, unsigned vo, u32x2 v) { asm volatile("global_store_dwordx2 %0, %1, %2 sc1\n\ts_nop 1" :: "v"(vo), "v"(v), "s"(sb) : "memory"); }
__device__ __forceinline__ void gldsw(const char* sbase, unsigned voff, unsigned lds) {
  unsigned keep;
  asm volatile("s_mov_b32 %0, m0\n\ts_mov_b32 m0, %3\n\ts_nop 0\n\tglobal_load_lds_dwordx4 %1, %2" CNT_ "\n\ts_mov_b32 m0, %0"
               : "=&s"(keep) : "v"(voff), "s"(sbase), "s"(lds) : "memory");
}
__device__ __forceinline__ void wait_clk(unsigned t0, unsigned d) {
  while ((unsigned)__builtin_amdgcn_s_memtime() - t0 < d) __builtin_amdgcn_s_sleep(1);
}
__device__ __forceinline__ unsigned bell_time(const char* a, unsigned vo, unsigned tag, unsigned cap, unsigned v = 0) {
  for (unsigned spins = 0; !__all((v >> 24) >= tag); ++spins) {
    asm volatile("s_nop 4\n\tglobal_load_dword %0, %1, %2 sc1\n\ts_waitcnt vmcnt(0)" : "=&v"(v) : "v"(vo), "s"(a) : "memory");
    if (__all((v >> 24) >= tag) || spins >= cap) break;
  }
  const unsigned now = (unsigned)__builtin_amdgcn_s_memrealtime() + 250u;
  const unsigned age = rowmin16((now - v) & 0xFFFFFFu);
  unsigned r = __builtin_amdgcn_readfirstlane(now - min(min(rdl(age, 0), rdl(age, 16)), 500u));
  asm volatile("" : "+s"(r));
  return r;
}
__device__ __forceinline__ void wait_rt(unsigned t0, int d) {
  while ((int)((unsigned)__builtin_amdgcn_s_memrealtime() - t0) < d) { }
}
#define WBAR() do { __builtin_amdgcn_s_barrier(); asm volatile("" ::: "memory"); } while (0)
#define SB() __builtin_amdgcn_sched_barrier(0)
#define TN(k, v) do { } while (0)
template <bool HALF>
__device__ __forceinline__ void stage_proj(const char* src, unsigned lds, int tid, int wave) {
  const int row = tid >> 4, f = 2 * ((row & 1) | (((row >> 2) & 3) << 1));
#pragma unroll
  for (int j = 0; j < 4; ++j) {
    const int pc = (tid & 15) ^ f;
    gldsw(src + (size_t)j * 32 * HD * 2, (unsigned)(HALF ? (row + (pc >> 3) * 128) * HD * 2 + ((pc & 7) << 4) : row * HD * 2 + (pc << 4)), lds + (wave * 64 + 512 * j) * 16);
  }
}
__device__ __forceinline__ unsigned proj_base(int T, int lane) {
  const int g = lane >> 4, q4 = (lane & 15) >> 2, p4 = lane & 3, fs = 2 * ((q4 & 1) | ((g & 1) << 1) | ((q4 >> 1) << 2));
  return (unsigned)((16 * (g >> 1) + 4 * (g & 1) + (q4 & 1) + 8 * (q4 >> 1)) * 256 + (((2 * T) ^ fs) << 4) + p4 * 8);
}
__device__ __forceinline__ f32x4 zero4() { float z = 0.0f; asm volatile("" : "+v"(z)); return f32x4{z, z, z, z}; }
__device__ __forceinline__ unsigned lds_word(const char* p) { return __builtin_amdgcn_readfirstlane(*(volatile LAS unsigned*)p); }

#define MFMA16(a, b, c) __builtin_amdgcn_mfma_f32_16x16x16bf16_1k((a), (b), (c), 0, 0, 0)
#define UF(x) __uint_as_float(x)
__device__ __forceinline__ unsigned umax3(unsigned a, unsigned b, unsigned c) { return max(max(a, b), c); }
__device__ __forceinline__ f32x2 logit2(float a, float b) {
  return f32x2{a, b};
}
constexpr float LSC = LOG2E * SCALE;

#define FCS() do { } while (0)
struct QKW { u32x32 w0, w1, w2, w3, w4, w5; };
template <int W_> __device__ __forceinline__ void qk_step(const QKW& W, const char* ag, const char* vs, const char* v2, const char* od, unsigned pb, unsigned sn, unsigned& cap, unsigned& sp,
    unsigned long long& tc, unsigned long long* ts, unsigned& d0, unsigned& d1);
template <> __device__ __forceinline__ void qk_step<0>(const QKW& W, const char* ag, const char* vs, const char* v2, const char* od, unsigned pb, unsigned sn, unsigned& cap, unsigned& sp,
    unsigned long long& tc, unsigned long long* ts, unsigned& d0, unsigned& d1) {
  unsigned st, bg;
  asm volatile("v_mbcnt_lo_u32_b32 v60, -1, 0\n\t"
    "v_mbcnt_hi_u32_b32 v60, -1, v60\n\t"
    "v_lshlrev_b32 v5, 4, v60\n\t"
    "s_mov_b32 %[sp], 0\n\t"
    "s_setprio 2\n\t"
    "global_load_dwordx4 v[24:27], v5, %[ag] sc1\n\t"
    "global_load_dwordx4 v[28:31], v5, %[ag] offset:1024 sc1\n\t"
    "s_memtime %[tc]\n\t"
    "v_lshrrev_b32 v32, 4, v60\n\t"
    "v_and_b32 v33, 15, v60\n\t"
    "v_lshrrev_b32 v34, 2, v33\n\t"
    "v_and_b32 v35, 3, v60\n\t"
    "v_and_b32 v61, 1, v60\n\t"
    "v_lshlrev_b32 v61, 3, v61\n\t"
    "v_lshl_or_b32 v7, v34, 4, v61\n\t"
    "v_lshl_or_b32 v7, v32, 7, v7\n\t"
    "v_xor_b32 v10, v32, v33\n\t"
    "v_lshlrev_b32 v10, 4, v10\n\t"
    "v_lshl_or_b32 v10, v33, 8, v10\n\t"
    "v_or_b32 v10, %[pb], v10\n\t"
    "v_xor_b32 v11, 0x40, v10\n\t"
    "v_xor_b32 v12, 0x80, v10\n\t"
    "v_xor_b32 v13, 0xc0, v10\n\t"
    "v_lshl_or_b32 v34, v32, 2, v34\n\t"
    "v_lshrrev_b32 v35, 1, v35\n\t"
    "v_or_b32 v35, 0, v35\n\t"
    "v_xor_b32 v35, v35, v34\n\t"
    "v_lshl_or_b32 v14, v35, 4, v61\n\t"
    "v_lshl_or_b32 v14, v34, 8, v14\n\t"
    "v_or_b32 v14, %[pb], v14\n\t"
    "v_and_b32 v61, 3, v60\n\t"
    "v_lshlrev_b32 v61, 6, v61\n\t"
    "v_lshl_or_b32 v4, v32, 4, v61\n\t"
    "v_and_b32 v61, 4, v60\n\t"
    "v_lshl_or_b32 v4, v61, 10, v4\n\t"
    "v_and_b32 v61, 8, v60\n\t"
    "v_lshl_or_b32 v4, v61, 14, v4\n\t"
    "v_or_b32 v4, 0, v4\n\t"
    "v_and_b32 v61, 2, v60\n\t"
    "v_mul_u32_u24 v61, 0x5555, v61\n\t"
    "v_or_b32 v6, %[c44], v61\n\t"
    "v_lshlrev_b32 v61, 2, v32\n\t"
    "v_lshl_or_b32 v15, v33, 4, v61\n\t"
    "3:\n\t"
    "v_mov_b32 v16, 0\n\t"
    "v_mov_b32 v17, 0\n\t"
    "v_mov_b32 v18, 0\n\t"
    "v_mov_b32 v19, 0\n\t"
    "v_mov_b32 v20, 0\n\t"
    "v_mov_b32 v21, 0\n\t"
    "v_mov_b32 v22, 0\n\t"
    "v_mov_b32 v23, 0\n\t"
    "s_waitcnt vmcnt(0) lgkmcnt(0)\n\t"
    "ds_write_b128 v5, v[24:27] offset:0\n\t"
    "ds_write_b128 v5, v[28:31] offset:1024\n\t"
    "v_smfmac_f32_16x16x64_bf16 v[16:19], v[24:27], v[64:71], v6\n\t"
    "v_max3_u32 v60, v24, v25, v26\n\t"
    "v_max3_u32 v61, v27, v28, v29\n\t"
    "v_smfmac_f32_16x16x64_bf16 v[20:23], v[24:27], v[72:79], v6\n\t"
    "v_max3_u32 v60, v60, v30, v31\n\t"
    "v_max_u32 v60, v60, v61\n\t"
    "v_cmp_eq_u32 vcc, -1, v60\n\t"
    "s_cbranch_vccnz 1f\n\t"
    "2:\n\t"
    "s_waitcnt lgkmcnt(0)\n\t"
    "s_barrier\n\t"
    "ds_read_b128 v[32:35], v5 offset:2048\n\t"
    "ds_read_b128 v[36:39], v5 offset:3072\n\t"
    "v_smfmac_f32_16x16x64_bf16 v[16:19], v[28:31], v[80:87], v6\n\t"
    "ds_read_b128 v[40:43], v5 offset:4096\n\t"
    "ds_read_b128 v[44:47], v5 offset:5120\n\t"
    "v_smfmac_f32_16x16x64_bf16 v[20:23], v[28:31], v[88:95], v6\n\t"
    "s_waitcnt lgkmcnt(3)\n\t"
    "v_smfmac_f32_16x16x64_bf16 v[16:19], v[32:35], v[96:103], v6\n\t"
    "ds_read_b128 v[48:51], v5 offset:6144\n\t"
    "v_smfmac_f32_16x16x64_bf16 v[20:23], v[32:35], v[104:111], v6\n\t"
    "ds_read_b128 v[52:55], v5 offset:7168\n\t"
    "s_waitcnt lgkmcnt(4)\n\t"
    "v_smfmac_f32_16x16x64_bf16 v[16:19], v[36:39], v[112:119], v6\n\t"
    "ds_read_b128 v[24:27], v5 offset:8192\n\t"
    "v_smfmac_f32_16x16x64_bf16 v[20:23], v[36:39], v[120:127], v6\n\t"
    "ds_read_b128 v[28:31], v5 offset:9216\n\t"
    "s_waitcnt lgkmcnt(5)\n\t"
    "v_smfmac_f32_16x16x64_bf16 v[16:19], v[40:43], v[128:135], v6\n\t"
    "ds_read_b128 v[56:59], v5 offset:10240\n\t"
    "v_smfmac_f32_16x16x64_bf16 v[20:23], v[40:43], v[136:143], v6\n\t"
    "ds_read_b128 v[60:63], v5 offset:11264\n\t"
    "ds_read_b128 v[36:39], v11 offset:36864\n\t"
    "s_waitcnt lgkmcnt(7)\n\t"
    "v_smfmac_f32_16x16x64_bf16 v[16:19], v[44:47], v[144:151], v6\n\t"
    "ds_read_b128 v[40:43], v12 offset:32768\n\t"
    "v_smfmac_f32_16x16x64_bf16 v[20:23], v[44:47], v[152:159], v6\n\t"
    "ds_read_b128 v[44:47], v12 offset:36864\n\t"
    "s_waitcnt lgkmcnt(8)\n\t"
    "v_smfmac_f32_16x16x64_bf16 v[16:19], v[48:51], v[160:167], v6\n\t"
    "v_smfmac_f32_16x16x64_bf16 v[20:23], v[48:51], v[168:175], v6\n\t"
    "ds_read_b128 v[48:51], v13 offset:32768\n\t"
    "s_waitcnt lgkmcnt(8)\n\t"
    "v_smfmac_f32_16x16x64_bf16 v[16:19], v[52:55], v[176:183], v6\n\t"
    "v_smfmac_f32_16x16x64_bf16 v[20:23], v[52:55], v[184:191], v6\n\t"
    "ds_read_b128 v[52:55], v13 offset:36864\n\t"
    "s_waitcnt lgkmcnt(8)\n\t"
    "v_smfmac_f32_16x16x64_bf16 v[16:19], v[24:27], v[192:199], v6\n\t"
    "ds_read_b32 v8, v15 offset:28672\n\t"
    "ds_read_b128 v[32:35], v5 offset:24576\n\t"
    "s_setprio 3\n\t"
    "v_smfmac_f32_16x16x64_bf16 v[20:23], v[24:27], v[200:207], v6\n\t"
    "ds_read_b128 v[24:27], v10 offset:32768\n\t"
    "s_waitcnt lgkmcnt(10)\n\t"
    "v_smfmac_f32_16x16x64_bf16 v[16:19], v[28:31], v[208:215], v6\n\t"
    "s_waitcnt lgkmcnt(9)\n\t"
    "v_smfmac_f32_16x16x64_bf16 v[16:19], v[56:59], v[224:231], v6\n\t"
    "s_waitcnt lgkmcnt(8)\n\t"
    "v_smfmac_f32_16x16x64_bf16 v[16:19], v[60:63], v[240:247], v6\n\t"
    "v_smfmac_f32_16x16x64_bf16 v[20:23], v[28:31], v[216:223], v6\n\t"
    "s_waitcnt lgkmcnt(1)\n\t"
    "21:\n\t"
    "v_cmp_ne_u32 vcc, %[sn], v8\n\t"
    "s_cbranch_vccnz 22f\n\t"
    "v_mul_f32 v32, %[lsc], v32\n\t"
    "v_mul_f32 v33, %[lsc], v33\n\t"
    "v_smfmac_f32_16x16x64_bf16 v[20:23], v[56:59], v[232:239], v6\n\t"
    "v_mul_f32 v34, %[lsc], v34\n\t"
    "v_mul_f32 v35, %[lsc], v35\n\t"
    "v_add_f32 v16, v16, v18\n\t"
    "v_add_f32 v17, v17, v19\n\t"
    "v_smfmac_f32_16x16x64_bf16 v[20:23], v[60:63], v[248:255], v6\n\t"
    "v_fma_f32 v16, v16, %[lsc], v32\n\t"
    "v_fma_f32 v17, v17, %[lsc], v33\n\t"
    "v_cvt_pk_bf16_f32 v16, v16, v17\n\t"
    "ds_write_b32 v15, v16 offset:16384\n\t"
    "s_mov_b32 %[bg], %[big]\n\t"
    "s_nop 2\n\t"
    "v_add_f32 v20, v20, v22\n\t"
    "v_add_f32 v21, v21, v23\n\t"
    "v_fma_f32 v20, v20, %[lsc], v34\n\t"
    "v_fma_f32 v21, v21, %[lsc], v35\n\t"
    "v_cvt_pk_bf16_f32 v20, v20, v21\n\t"
    "ds_write_b32 v15, v20 offset:16640\n\t"
    "ds_read_b128 v[28:31], v10 offset:36864\n\t"
    "ds_read_b128 v[32:35], v11 offset:32768\n\t"
    "s_waitcnt lgkmcnt(2)\n\t"
    "s_barrier\n\t"
    "ds_read_b64_tr_b16 v[16:17], v7 offset:16384\n\t"
    "ds_read_b64_tr_b16 v[18:19], v7 offset:16448\n\t"
    "ds_read_b64_tr_b16 v[20:21], v7 offset:16896\n\t"
    "ds_read_b64_tr_b16 v[22:23], v7 offset:16960\n\t"
    "ds_read_b64_tr_b16 v[56:57], v7 offset:17408\n\t"
    "ds_read_b64_tr_b16 v[58:59], v7 offset:17472\n\t"
    "ds_read_b64_tr_b16 v[60:61], v7 offset:17920\n\t"
    "ds_read_b64_tr_b16 v[62:63], v7 offset:17984\n\t"
    "global_load_dwordx4 v[6:9], v5, %[vs] sc1\n\t"
    "global_load_dwordx4 v[10:13], v5, %[vs] offset:1024 sc1\n\t"
    "s_waitcnt lgkmcnt(6)\n\t"
    "v_mfma_f32_16x16x32_bf16 v[24:27], v[24:27], v[16:19], 0\n\t"
    "v_mfma_f32_16x16x32_bf16 v[28:31], v[28:31], v[16:19], 0\n\t"
    "s_waitcnt lgkmcnt(4)\n\t"
    "v_mfma_f32_16x16x32_bf16 v[24:27], v[32:35], v[20:23], v[24:27]\n\t"
    "v_mfma_f32_16x16x32_bf16 v[28:31], v[36:39], v[20:23], v[28:31]\n\t"
    "v_xor_b32 v39, 32, v14\n\t"
    "ds_read_b64_tr_b16 v[32:33], v14 offset:40960\n\t"
    "ds_read_b64_tr_b16 v[34:35], v14 offset:45056\n\t"
    "ds_read_b64_tr_b16 v[36:37], v39 offset:40960\n\t"
    "ds_read_b64_tr_b16 v[38:39], v39 offset:45056\n\t"
    "s_waitcnt lgkmcnt(6)\n\t"
    "v_mfma_f32_16x16x32_bf16 v[24:27], v[40:43], v[56:59], v[24:27]\n\t"
    "v_mfma_f32_16x16x32_bf16 v[28:31], v[44:47], v[56:59], v[28:31]\n\t"
    "s_waitcnt lgkmcnt(4)\n\t"
    "v_mfma_f32_16x16x32_bf16 v[24:27], v[48:51], v[60:63], v[24:27]\n\t"
    "v_mfma_f32_16x16x32_bf16 v[28:31], v[52:55], v[60:63], v[28:31]\n\t"
    "v_mov_b32 v52, %[one]\n\t"
    "v_mov_b32 v53, %[one]\n\t"
    "v_mov_b32 v54, %[one]\n\t"
    "v_mov_b32 v55, %[one]\n\t"
    "v_mov_b32 v45, 0\n\t"
    "v_mov_b32 v47, 0\n\t"
    "v_mov_b32 v48, %[one]\n\t"
    "v_max3_f32 v14, |v24|, |v25|, |v26|\n\t"
    "v_max3_f32 v40, |v27|, |v28|, |v29|\n\t"
    "v_max3_f32 v14, v14, |v30|, |v31|\n\t"
    "v_max_f32 v14, v14, v40\n\t"
    "v_cmp_nge_f32 vcc, %[big], v14\n\t"
    "v_mov_b32 v14, 0\n\t"
    "s_cbranch_vccnz 12f\n\t"
    "13:\n\t"
    "v_exp_f32 v24, v24\n\t"
    "v_exp_f32 v25, v25\n\t"
    "v_exp_f32 v26, v26\n\t"
    "v_exp_f32 v27, v27\n\t"
    "v_exp_f32 v28, v28\n\t"
    "v_exp_f32 v29, v29\n\t"
    "v_exp_f32 v30, v30\n\t"
    "v_exp_f32 v31, v31\n\t"
    "v_cvt_pk_bf16_f32 v40, v24, v25\n\t"
    "v_cvt_pk_bf16_f32 v41, v26, v27\n\t"
    "v_cvt_pk_bf16_f32 v42, v28, v29\n\t"
    "v_cvt_pk_bf16_f32 v43, v30, v31\n\t"
    "v_mov_b32 v49, 0\n\t"
    "v_mov_b32 v51, 0\n\t"
    "s_waitcnt lgkmcnt(0)\n\t"
    "v_mfma_f32_16x16x32_bf16 v[52:55], v[52:55], v[40:43], 0\n\t"
    "v_mfma_f32_16x16x32_bf16 v[24:27], v[32:35], v[40:43], 0\n\t"
    "v_mfma_f32_16x16x32_bf16 v[28:31], v[36:39], v[40:43], 0\n\t"
    "s_waitcnt vmcnt(1)\n\t"
    "5:\n\t"
    "v_mfma_f32_16x16x32_bf16 v[32:35], v[6:9], v[16:19], 0\n\t"
    "v_mfma_f32_16x16x32_bf16 v[36:39], v[6:9], v[20:23], 0\n\t"
    "s_waitcnt vmcnt(0)\n\t"
    "v_mfma_f32_16x16x32_bf16 v[32:35], v[10:13], v[56:59], v[32:35]\n\t"
    "v_mfma_f32_16x16x32_bf16 v[36:39], v[10:13], v[60:63], v[36:39]\n\t"
    "ds_read_b32 v44, v15 offset:18432\n\t"
    "ds_read_b32 v46, v15 offset:18688\n\t"
    "v_max3_u32 v40, v6, v7, v8\n\t"
    "v_max3_u32 v41, v9, v10, v11\n\t"
    "v_max3_u32 v40, v40, v12, v13\n\t"
    "v_max_u32 v40, v40, v41\n\t"
    "v_cmp_eq_u32 vcc, -1, v40\n\t"
    "s_cbranch_vccnz 4f\n\t"
    "6:\n\t"
    "s_nop 0\n\t"
    "v_add_f32 v32, v32, v38\n\t"
    "v_add_f32 v33, v33, v39\n\t"
    "v_exp_f32 v34, v32\n\t"
    "v_max_f32 v40, v32, v33\n\t"
    "v_exp_f32 v35, v33\n\t"
    "v_cmp_nge_f32 vcc, %[bg], v40\n\t"
    "s_cbranch_vccnz 10f\n\t"
    "11:\n\t"
    "v_cvt_pk_bf16_f32 v50, v34, v35\n\t"
    "17:\n\t"
    "s_waitcnt vmcnt(0) lgkmcnt(0)\n\t"
    "v_max_u32 v40, v44, v46\n\t"
    "v_cmp_eq_u32 vcc, -1, v40\n\t"
    "s_cbranch_vccnz 18f\n\t"
    "19:\n\t"
    "v_mfma_f32_16x16x16_bf16 v[52:55], v[48:49], v[50:51], v[52:55]\n\t"
    "v_mfma_f32_16x16x16_bf16 v[24:27], v[44:45], v[50:51], v[24:27]\n\t"
    "v_mfma_f32_16x16x16_bf16 v[28:31], v[46:47], v[50:51], v[28:31]\n\t"
    "s_nop 5\n\t"
    "v_rcp_f32 v42, v52\n\t"
    "s_nop 0\n\t"
    "v_pk_mul_f32 v[24:25], v[24:25], v[42:43] op_sel_hi:[1,0]\n\t"
    "v_pk_mul_f32 v[26:27], v[26:27], v[42:43] op_sel_hi:[1,0]\n\t"
    "v_pk_mul_f32 v[28:29], v[28:29], v[42:43] op_sel_hi:[1,0]\n\t"
    "v_pk_mul_f32 v[30:31], v[30:31], v[42:43] op_sel_hi:[1,0]\n\t"
    "v_cvt_pk_bf16_f32 v36, v24, v25\n\t"
    "v_cvt_pk_bf16_f32 v37, v26, v27\n\t"
    "v_cvt_pk_bf16_f32 v38, v28, v29\n\t"
    "v_cvt_pk_bf16_f32 v39, v30, v31\n\t"
    "v_min_u32 v36, -2, v36\n\t"
    "v_min_u32 v37, -2, v37\n\t"
    "v_min_u32 v38, -2, v38\n\t"
    "v_min_u32 v39, -2, v39\n\t"
    "global_store_dwordx4 v4, v[36:39], %[od] sc1\n\t"
    "ds_write_b128 v5, v[6:9] offset:20480\n\t"
    "s_setprio 1\n\t"
    "s_waitcnt lgkmcnt(0)\n\t"
    "s_barrier\n\t"
    "s_waitcnt lgkmcnt(0)\n\t"
    "s_branch 9f\n\t"
    "1:\n\t"
    "s_add_u32 %[sp], %[sp], 1\n\t"
    "s_cmp_ge_u32 %[sp], %[cap]\n\t"
    "s_cbranch_scc1 7f\n\t"
    "global_load_dwordx4 v[24:27], v5, %[ag] sc1\n\t"
    "global_load_dwordx4 v[28:31], v5, %[ag] offset:1024 sc1\n\t"
    "s_waitcnt lgkmcnt(0)\n\t"
    "s_memtime %[tc]\n\t"
    "s_nop 7\n\t"
    "s_branch 3b\n\t"
    "7:\n\t"
    "s_mov_b32 %[cap], 0\n\t"
    "s_branch 2b\n\t"
    "4:\n\t"
    "s_add_u32 %[sp], %[sp], 0x10000\n\t"
    "s_lshr_b32 %[st], %[sp], 16\n\t"
    "s_cmp_ge_u32 %[st], %[cap]\n\t"
    "s_cbranch_scc1 8f\n\t"
    "global_load_dwordx4 v[6:9], v5, %[vs] sc1\n\t"
    "global_load_dwordx4 v[10:13], v5, %[vs] offset:1024 sc1\n\t"
    "s_waitcnt vmcnt(0) lgkmcnt(0)\n\t"
    "s_branch 5b\n\t"
    "8:\n\t"
    "s_mov_b32 %[cap], 0\n\t"
    "s_branch 6b\n\t"
    "18:\n\t"
    "s_add_u32 %[sp], %[sp], 0x10000\n\t"
    "s_lshr_b32 %[st], %[sp], 16\n\t"
    "s_cmp_ge_u32 %[st], %[cap]\n\t"
    "s_cbranch_scc1 20f\n\t"
    "ds_read_b32 v44, v15 offset:18432\n\t"
    "ds_read_b32 v46, v15 offset:18688\n\t"
    "s_waitcnt vmcnt(0) lgkmcnt(0)\n\t"
    "s_branch 17b\n\t"
    "20:\n\t"
    "s_mov_b32 %[cap], 0\n\t"
    "s_branch 19b\n\t"
    "22:\n\t"
    "ds_read_b32 v8, v15 offset:28672\n\t"
    "ds_read_b128 v[32:35], v5 offset:24576\n\t"
    "s_waitcnt lgkmcnt(0)\n\t"
    "s_branch 21b\n\t"
    "10:\n\t"
    "v_max_f32 v40, v40, v14\n\t"
    "v_mov_b32 v42, v40\n\t"
    "s_nop 1\n\t"
    "v_permlane16_swap_b32 v40, v42\n\t"
    "v_max_f32 v40, v40, v42\n\t"
    "v_mov_b32 v42, v40\n\t"
    "s_nop 1\n\t"
    "v_permlane32_swap_b32 v40, v42\n\t"
    "v_max_f32 v40, v40, v42\n\t"
    "v_sub_f32 v34, v32, v40\n\t"
    "v_sub_f32 v35, v33, v40\n\t"
    "v_sub_f32 v40, v14, v40\n\t"
    "v_exp_f32 v40, v40\n\t"
    "v_exp_f32 v34, v34\n\t"
    "v_exp_f32 v35, v35\n\t"
    "v_mul_f32 v24, v24, v40\n\t"
    "v_mul_f32 v25, v25, v40\n\t"
    "v_mul_f32 v26, v26, v40\n\t"
    "v_mul_f32 v27, v27, v40\n\t"
    "v_mul_f32 v28, v28, v40\n\t"
    "v_mul_f32 v29, v29, v40\n\t"
    "v_mul_f32 v30, v30, v40\n\t"
    "v_mul_f32 v31, v31, v40\n\t"
    "v_mul_f32 v52, v52, v40\n\t"
    "v_mul_f32 v53, v53, v40\n\t"
    "v_mul_f32 v54, v54, v40\n\t"
    "v_mul_f32 v55, v55, v40\n\t"
    "s_branch 11b\n\t"
    "12:\n\t"
    "v_max3_f32 v14, v24, v25, v26\n\t"
    "v_max3_f32 v40, v27, v28, v29\n\t"
    "v_max3_f32 v14, v14, v30, v31\n\t"
    "v_max3_f32 v14, v14, v40, %[neg]\n\t"
    "v_mov_b32 v40, v14\n\t"
    "s_nop 1\n\t"
    "v_permlane16_swap_b32 v14, v40\n\t"
    "v_max_f32 v14, v14, v40\n\t"
    "v_mov_b32 v40, v14\n\t"
    "s_nop 1\n\t"
    "v_permlane32_swap_b32 v14, v40\n\t"
    "v_max_f32 v14, v14, v40\n\t"
    "v_sub_f32 v24, v24, v14\n\t"
    "v_sub_f32 v25, v25, v14\n\t"
    "v_sub_f32 v26, v26, v14\n\t"
    "v_sub_f32 v27, v27, v14\n\t"
    "v_sub_f32 v28, v28, v14\n\t"
    "v_sub_f32 v29, v29, v14\n\t"
    "v_sub_f32 v30, v30, v14\n\t"
    "v_sub_f32 v31, v31, v14\n\t"
    "s_mov_b32 %[bg], 0x7fc00000\n\t"
    "s_branch 13b\n\t"
    "9:"
    : [tc] "=&s"(tc), [sp] "=&s"(sp), [cap] "+s"(cap), [st] "=&s"(st), [bg] "=&s"(bg)
    : [ag] "s"(ag), [c44] "s"(0x4444u), [lsc] "s"(LSC), [sn] "s"(sn), [pb] "s"(pb), [vs] "s"(vs), [od] "s"(od), [neg] "s"(-1.0e38f), [one] "s"(0x3F803F80u), [big] "s"(24.0f), "{v[64:95]}"(W.w0), "{v[96:127]}"(W.w1), "{v[128:159]}"(W.w2), "{v[160:191]}"(W.w3), "{v[192:223]}"(W.w4), "{v[224:255]}"(W.w5)
    : "v4", "v5", "v6", "v7", "v8", "v9", "v10", "v11", "v12", "v13", "v14", "v15", "v16", "v17", "v18", "v19", "v20", "v21", "v22", "v23", "v24", "v25", "v26", "v27", "v28", "v29", "v30", "v31", "v32", "v33", "v34", "v35", "v36", "v37", "v38", "v39", "v40", "v41", "v42", "v43", "v44", "v45", "v46", "v47", "v48", "v49", "v50", "v51", "v52", "v53", "v54", "v55", "v56", "v57", "v58", "v59", "v60", "v61", "v62", "v63", "vcc", "scc", "memory");
}
template <> __device__ __forceinline__ void qk_step<1>(const QKW& W, const char* ag, const char* vs, const char* v2, const char* od, unsigned pb, unsigned sn, unsigned& cap, unsigned& sp,
    unsigned long long& tc, unsigned long long* ts, unsigned& d0, unsigned& d1) {
  unsigned st, bg;
  asm volatile("v_mbcnt_lo_u32_b32 v60, -1, 0\n\t"
    "v_mbcnt_hi_u32_b32 v60, -1, v60\n\t"
    "v_lshlrev_b32 v5, 4, v60\n\t"
    "s_mov_b32 %[sp], 0\n\t"
    "s_setprio 2\n\t"
    "global_load_dwordx4 v[24:27], v5, %[ag] sc1\n\t"
    "global_load_dwordx4 v[28:31], v5, %[ag] offset:1024 sc1\n\t"
    "s_memtime %[tc]\n\t"
    "v_lshrrev_b32 v32, 4, v60\n\t"
    "v_and_b32 v33, 15, v60\n\t"
    "v_lshrrev_b32 v34, 2, v33\n\t"
    "v_and_b32 v35, 3, v60\n\t"
    "v_and_b32 v61, 1, v60\n\t"
    "v_lshlrev_b32 v61, 3, v61\n\t"
    "v_lshl_or_b32 v7, v34, 4, v61\n\t"
    "v_lshl_or_b32 v7, v32, 7, v7\n\t"
    "v_xor_b32 v10, v32, v33\n\t"
    "v_lshlrev_b32 v10, 4, v10\n\t"
    "v_lshl_or_b32 v10, v33, 8, v10\n\t"
    "v_or_b32 v10, %[pb], v10\n\t"
    "v_xor_b32 v11, 0x40, v10\n\t"
    "v_xor_b32 v12, 0x80, v10\n\t"
    "v_xor_b32 v13, 0xc0, v10\n\t"
    "v_lshl_or_b32 v34, v32, 2, v34\n\t"
    "v_lshrrev_b32 v35, 1, v35\n\t"
    "v_or_b32 v35, 4, v35\n\t"
    "v_xor_b32 v35, v35, v34\n\t"
    "v_lshl_or_b32 v14, v35, 4, v61\n\t"
    "v_lshl_or_b32 v14, v34, 8, v14\n\t"
    "v_or_b32 v14, %[pb], v14\n\t"
    "v_and_b32 v61, 3, v60\n\t"
    "v_lshlrev_b32 v61, 6, v61\n\t"
    "v_lshl_or_b32 v4, v32, 4, v61\n\t"
    "v_and_b32 v61, 4, v60\n\t"
    "v_lshl_or_b32 v4, v61, 10, v4\n\t"
    "v_and_b32 v61, 8, v60\n\t"
    "v_lshl_or_b32 v4, v61, 14, v4\n\t"
    "v_or_b32 v4, 256, v4\n\t"
    "v_and_b32 v61, 2, v60\n\t"
    "v_mul_u32_u24 v61, 0x5555, v61\n\t"
    "v_or_b32 v6, %[c44], v61\n\t"
    "v_lshlrev_b32 v61, 2, v32\n\t"
    "v_lshl_or_b32 v15, v33, 4, v61\n\t"
    "3:\n\t"
    "v_mov_b32 v16, 0\n\t"
    "v_mov_b32 v17, 0\n\t"
    "v_mov_b32 v18, 0\n\t"
    "v_mov_b32 v19, 0\n\t"
    "v_mov_b32 v20, 0\n\t"
    "v_mov_b32 v21, 0\n\t"
    "v_mov_b32 v22, 0\n\t"
    "v_mov_b32 v23, 0\n\t"
    "s_waitcnt vmcnt(0) lgkmcnt(0)\n\t"
    "ds_write_b128 v5, v[24:27] offset:2048\n\t"
    "ds_write_b128 v5, v[28:31] offset:3072\n\t"
    "v_smfmac_f32_16x16x64_bf16 v[16:19], v[24:27], v[96:103], v6\n\t"
    "v_max3_u32 v60, v24, v25, v26\n\t"
    "v_max3_u32 v61, v27, v28, v29\n\t"
    "v_smfmac_f32_16x16x64_bf16 v[20:23], v[24:27], v[104:111], v6\n\t"
    "v_max3_u32 v60, v60, v30, v31\n\t"
    "v_max_u32 v60, v60, v61\n\t"
    "v_cmp_eq_u32 vcc, -1, v60\n\t"
    "s_cbranch_vccnz 1f\n\t"
    "2:\n\t"
    "s_waitcnt lgkmcnt(0)\n\t"
    "s_barrier\n\t"
    "ds_read_b128 v[32:35], v5 offset:0\n\t"
    "ds_read_b128 v[36:39], v5 offset:1024\n\t"
    "v_smfmac_f32_16x16x64_bf16 v[16:19], v[28:31], v[112:119], v6\n\t"
    "ds_read_b128 v[40:43], v5 offset:4096\n\t"
    "ds_read_b128 v[44:47], v5 offset:5120\n\t"
    "v_smfmac_f32_16x16x64_bf16 v[20:23], v[28:31], v[120:127], v6\n\t"
    "s_waitcnt lgkmcnt(3)\n\t"
    "v_smfmac_f32_16x16x64_bf16 v[16:19], v[32:35], v[64:71], v6\n\t"
    "ds_read_b128 v[48:51], v5 offset:6144\n\t"
    "v_smfmac_f32_16x16x64_bf16 v[20:23], v[32:35], v[72:79], v6\n\t"
    "ds_read_b128 v[52:55], v5 offset:7168\n\t"
    "s_waitcnt lgkmcnt(4)\n\t"
    "v_smfmac_f32_16x16x64_bf16 v[16:19], v[36:39], v[80:87], v6\n\t"
    "ds_read_b128 v[24:27], v5 offset:8192\n\t"
    "v_smfmac_f32_16x16x64_bf16 v[20:23], v[36:39], v[88:95], v6\n\t"
    "ds_read_b128 v[28:31], v5 offset:9216\n\t"
    "s_waitcnt lgkmcnt(5)\n\t"
    "v_smfmac_f32_16x16x64_bf16 v[16:19], v[40:43], v[128:135], v6\n\t"
    "ds_read_b128 v[56:59], v5 offset:10240\n\t"
    "v_smfmac_f32_16x16x64_bf16 v[20:23], v[40:43], v[136:143], v6\n\t"
    "ds_read_b128 v[60:63], v5 offset:11264\n\t"
    "ds_read_b128 v[36:39], v11 offset:36864\n\t"
    "s_waitcnt lgkmcnt(7)\n\t"
    "v_smfmac_f32_16x16x64_bf16 v[16:19], v[44:47], v[144:151], v6\n\t"
    "ds_read_b128 v[40:43], v12 offset:32768\n\t"
    "v_smfmac_f32_16x16x64_bf16 v[20:23], v[44:47], v[152:159], v6\n\t"
    "ds_read_b128 v[44:47], v12 offset:36864\n\t"
    "s_waitcnt lgkmcnt(8)\n\t"
    "v_smfmac_f32_16x16x64_bf16 v[16:19], v[48:51], v[160:167], v6\n\t"
    "v_smfmac_f32_16x16x64_bf16 v[20:23], v[48:51], v[168:175], v6\n\t"
    "ds_read_b128 v[48:51], v13 offset:32768\n\t"
    "s_waitcnt lgkmcnt(8)\n\t"
    "v_smfmac_f32_16x16x64_bf16 v[16:19], v[52:55], v[176:183], v6\n\t"
    "v_smfmac_f32_16x16x64_bf16 v[20:23], v[52:55], v[184:191], v6\n\t"
    "ds_read_b128 v[52:55], v13 offset:36864\n\t"
    "s_waitcnt lgkmcnt(8)\n\t"
    "v_smfmac_f32_16x16x64_bf16 v[16:19], v[24:27], v[192:199], v6\n\t"
    "ds_read_b32 v8, v15 offset:28928\n\t"
    "ds_read_b128 v[32:35], v5 offset:25600\n\t"
    "s_setprio 3\n\t"
    "v_smfmac_f32_16x16x64_bf16 v[20:23], v[24:27], v[200:207], v6\n\t"
    "ds_read_b128 v[24:27], v10 offset:32768\n\t"
    "s_waitcnt lgkmcnt(10)\n\t"
    "v_smfmac_f32_16x16x64_bf16 v[16:19], v[28:31], v[208:215], v6\n\t"
    "s_waitcnt lgkmcnt(9)\n\t"
    "v_smfmac_f32_16x16x64_bf16 v[16:19], v[56:59], v[224:231], v6\n\t"
    "s_waitcnt lgkmcnt(8)\n\t"
    "v_smfmac_f32_16x16x64_bf16 v[16:19], v[60:63], v[240:247], v6\n\t"
    "v_smfmac_f32_16x16x64_bf16 v[20:23], v[28:31], v[216:223], v6\n\t"
    "s_waitcnt lgkmcnt(1)\n\t"
    "21:\n\t"
    "v_cmp_ne_u32 vcc, %[sn], v8\n\t"
    "s_cbranch_vccnz 22f\n\t"
    "v_mul_f32 v32, %[lsc], v32\n\t"
    "v_mul_f32 v33, %[lsc], v33\n\t"
    "v_smfmac_f32_16x16x64_bf16 v[20:23], v[56:59], v[232:239], v6\n\t"
    "v_mul_f32 v34, %[lsc], v34\n\t"
    "v_mul_f32 v35, %[lsc], v35\n\t"
    "v_add_f32 v16, v16, v18\n\t"
    "v_add_f32 v17, v17, v19\n\t"
    "v_smfmac_f32_16x16x64_bf16 v[20:23], v[60:63], v[248:255], v6\n\t"
    "v_fma_f32 v16, v16, %[lsc], v32\n\t"
    "v_fma_f32 v17, v17, %[lsc], v33\n\t"
    "v_cvt_pk_bf16_f32 v16, v16, v17\n\t"
    "ds_write_b32 v15, v16 offset:16896\n\t"
    "s_mov_b32 %[bg], %[big]\n\t"
    "s_nop 2\n\t"
    "v_add_f32 v20, v20, v22\n\t"
    "v_add_f32 v21, v21, v23\n\t"
    "v_fma_f32 v20, v20, %[lsc], v34\n\t"
    "v_fma_f32 v21, v21, %[lsc], v35\n\t"
    "v_cvt_pk_bf16_f32 v20, v20, v21\n\t"
    "ds_write_b32 v15, v20 offset:17152\n\t"
    "ds_read_b128 v[28:31], v10 offset:36864\n\t"
    "ds_read_b128 v[32:35], v11 offset:32768\n\t"
    "s_waitcnt lgkmcnt(2)\n\t"
    "s_barrier\n\t"
    "ds_read_b64_tr_b16 v[16:17], v7 offset:16384\n\t"
    "ds_read_b64_tr_b16 v[18:19], v7 offset:16448\n\t"
    "ds_read_b64_tr_b16 v[20:21], v7 offset:16896\n\t"
    "ds_read_b64_tr_b16 v[22:23], v7 offset:16960\n\t"
    "ds_read_b64_tr_b16 v[56:57], v7 offset:17408\n\t"
    "ds_read_b64_tr_b16 v[58:59], v7 offset:17472\n\t"
    "ds_read_b64_tr_b16 v[60:61], v7 offset:17920\n\t"
    "ds_read_b64_tr_b16 v[62:63], v7 offset:17984\n\t"
    "global_load_dwordx4 v[6:9], v5, %[vs] sc1\n\t"
    "global_load_dwordx4 v[10:13], v5, %[vs] offset:1024 sc1\n\t"
    "s_waitcnt lgkmcnt(6)\n\t"
    "v_mfma_f32_16x16x32_bf16 v[24:27], v[24:27], v[16:19], 0\n\t"
    "v_mfma_f32_16x16x32_bf16 v[28:31], v[28:31], v[16:19], 0\n\t"
    "s_waitcnt lgkmcnt(4)\n\t"
    "v_mfma_f32_16x16x32_bf16 v[24:27], v[32:35], v[20:23], v[24:27]\n\t"
    "v_mfma_f32_16x16x32_bf16 v[28:31], v[36:39], v[20:23], v[28:31]\n\t"
    "v_xor_b32 v39, 32, v14\n\t"
    "ds_read_b64_tr_b16 v[32:33], v14 offset:40960\n\t"
    "ds_read_b64_tr_b16 v[34:35], v14 offset:45056\n\t"
    "ds_read_b64_tr_b16 v[36:37], v39 offset:40960\n\t"
    "ds_read_b64_tr_b16 v[38:39], v39 offset:45056\n\t"
    "s_waitcnt lgkmcnt(6)\n\t"
    "v_mfma_f32_16x16x32_bf16 v[24:27], v[40:43], v[56:59], v[24:27]\n\t"
    "v_mfma_f32_16x16x32_bf16 v[28:31], v[44:47], v[56:59], v[28:31]\n\t"
    "s_waitcnt lgkmcnt(4)\n\t"
    "v_mfma_f32_16x16x32_bf16 v[24:27], v[48:51], v[60:63], v[24:27]\n\t"
    "v_mfma_f32_16x16x32_bf16 v[28:31], v[52:55], v[60:63], v[28:31]\n\t"
    "v_mov_b32 v52, %[one]\n\t"
    "v_mov_b32 v53, %[one]\n\t"
    "v_mov_b32 v54, %[one]\n\t"
    "v_mov_b32 v55, %[one]\n\t"
    "v_mov_b32 v45, 0\n\t"
    "v_mov_b32 v47, 0\n\t"
    "v_mov_b32 v48, %[one]\n\t"
    "v_max3_f32 v14, |v24|, |v25|, |v26|\n\t"
    "v_max3_f32 v40, |v27|, |v28|, |v29|\n\t"
    "v_max3_f32 v14, v14, |v30|, |v31|\n\t"
    "v_max_f32 v14, v14, v40\n\t"
    "v_cmp_nge_f32 vcc, %[big], v14\n\t"
    "v_mov_b32 v14, 0\n\t"
    "s_cbranch_vccnz 12f\n\t"
    "13:\n\t"
    "v_exp_f32 v24, v24\n\t"
    "v_exp_f32 v25, v25\n\t"
    "v_exp_f32 v26, v26\n\t"
    "v_exp_f32 v27, v27\n\t"
    "v_exp_f32 v28, v28\n\t"
    "v_exp_f32 v29, v29\n\t"
    "v_exp_f32 v30, v30\n\t"
    "v_exp_f32 v31, v31\n\t"
    "v_cvt_pk_bf16_f32 v40, v24, v25\n\t"
    "v_cvt_pk_bf16_f32 v41, v26, v27\n\t"
    "v_cvt_pk_bf16_f32 v42, v28, v29\n\t"
    "v_cvt_pk_bf16_f32 v43, v30, v31\n\t"
    "v_mov_b32 v49, 0\n\t"
    "v_mov_b32 v51, 0\n\t"
    "s_waitcnt lgkmcnt(0)\n\t"
    "v_mfma_f32_16x16x32_bf16 v[52:55], v[52:55], v[40:43], 0\n\t"
    "v_mfma_f32_16x16x32_bf16 v[24:27], v[32:35], v[40:43], 0\n\t"
    "v_mfma_f32_16x16x32_bf16 v[28:31], v[36:39], v[40:43], 0\n\t"
    "s_waitcnt vmcnt(1)\n\t"
    "5:\n\t"
    "v_mfma_f32_16x16x32_bf16 v[32:35], v[6:9], v[16:19], 0\n\t"
    "v_mfma_f32_16x16x32_bf16 v[36:39], v[6:9], v[20:23], 0\n\t"
    "s_waitcnt vmcnt(0)\n\t"
    "v_mfma_f32_16x16x32_bf16 v[32:35], v[10:13], v[56:59], v[32:35]\n\t"
    "v_mfma_f32_16x16x32_bf16 v[36:39], v[10:13], v[60:63], v[36:39]\n\t"
    "ds_read_b32 v44, v15 offset:18944\n\t"
    "ds_read_b32 v46, v15 offset:19200\n\t"
    "v_max3_u32 v40, v6, v7, v8\n\t"
    "v_max3_u32 v41, v9, v10, v11\n\t"
    "v_max3_u32 v40, v40, v12, v13\n\t"
    "v_max_u32 v40, v40, v41\n\t"
    "v_cmp_eq_u32 vcc, -1, v40\n\t"
    "s_cbranch_vccnz 4f\n\t"
    "6:\n\t"
    "s_nop 0\n\t"
    "v_add_f32 v32, v32, v38\n\t"
    "v_add_f32 v33, v33, v39\n\t"
    "v_exp_f32 v34, v32\n\t"
    "v_max_f32 v40, v32, v33\n\t"
    "v_exp_f32 v35, v33\n\t"
    "v_cmp_nge_f32 vcc, %[bg], v40\n\t"
    "s_cbranch_vccnz 10f\n\t"
    "11:\n\t"
    "v_cvt_pk_bf16_f32 v50, v34, v35\n\t"
    "17:\n\t"
    "s_waitcnt vmcnt(0) lgkmcnt(0)\n\t"
    "v_max_u32 v40, v44, v46\n\t"
    "v_cmp_eq_u32 vcc, -1, v40\n\t"
    "s_cbranch_vccnz 18f\n\t"
    "19:\n\t"
    "v_mfma_f32_16x16x16_bf16 v[52:55], v[48:49], v[50:51], v[52:55]\n\t"
    "v_mfma_f32_16x16x16_bf16 v[24:27], v[44:45], v[50:51], v[24:27]\n\t"
    "v_mfma_f32_16x16x16_bf16 v[28:31], v[46:47], v[50:51], v[28:31]\n\t"
    "s_nop 5\n\t"
    "v_rcp_f32 v42, v52\n\t"
    "s_nop 0\n\t"
    "v_pk_mul_f32 v[24:25], v[24:25], v[42:43] op_sel_hi:[1,0]\n\t"
    "v_pk_mul_f32 v[26:27], v[26:27], v[42:43] op_sel_hi:[1,0]\n\t"
    "v_pk_mul_f32 v[28:29], v[28:29], v[42:43] op_sel_hi:[1,0]\n\t"
    "v_pk_mul_f32 v[30:31], v[30:31], v[42:43] op_sel_hi:[1,0]\n\t"
    "v_cvt_pk_bf16_f32 v36, v24, v25\n\t"
    "v_cvt_pk_bf16_f32 v37, v26, v27\n\t"
    "v_cvt_pk_bf16_f32 v38, v28, v29\n\t"
    "v_cvt_pk_bf16_f32 v39, v30, v31\n\t"
    "v_min_u32 v36, -2, v36\n\t"
    "v_min_u32 v37, -2, v37\n\t"
    "v_min_u32 v38, -2, v38\n\t"
    "v_min_u32 v39, -2, v39\n\t"
    "global_store_dwordx4 v4, v[36:39], %[od] sc1\n\t"
    "ds_write_b128 v5, v[10:13] offset:21504\n\t"
    "s_setprio 1\n\t"
    "s_waitcnt lgkmcnt(0)\n\t"
    "s_barrier\n\t"
    "s_waitcnt lgkmcnt(0)\n\t"
    "s_branch 9f\n\t"
    "1:\n\t"
    "s_add_u32 %[sp], %[sp], 1\n\t"
    "s_cmp_ge_u32 %[sp], %[cap]\n\t"
    "s_cbranch_scc1 7f\n\t"
    "global_load_dwordx4 v[24:27], v5, %[ag] sc1\n\t"
    "global_load_dwordx4 v[28:31], v5, %[ag] offset:1024 sc1\n\t"
    "s_waitcnt lgkmcnt(0)\n\t"
    "s_memtime %[tc]\n\t"
    "s_nop 7\n\t"
    "s_branch 3b\n\t"
    "7:\n\t"
    "s_mov_b32 %[cap], 0\n\t"
    "s_branch 2b\n\t"
    "4:\n\t"
    "s_add_u32 %[sp], %[sp], 0x10000\n\t"
    "s_lshr_b32 %[st], %[sp], 16\n\t"
    "s_cmp_ge_u32 %[st], %[cap]\n\t"
    "s_cbranch_scc1 8f\n\t"
    "global_load_dwordx4 v[6:9], v5, %[vs] sc1\n\t"
    "global_load_dwordx4 v[10:13], v5, %[vs] offset:1024 sc1\n\t"
    "s_waitcnt vmcnt(0) lgkmcnt(0)\n\t"
    "s_branch 5b\n\t"
    "8:\n\t"
    "s_mov_b32 %[cap], 0\n\t"
    "s_branch 6b\n\t"
    "18:\n\t"
    "s_add_u32 %[sp], %[sp], 0x10000\n\t"
    "s_lshr_b32 %[st], %[sp], 16\n\t"
    "s_cmp_ge_u32 %[st], %[cap]\n\t"
    "s_cbranch_scc1 20f\n\t"
    "ds_read_b32 v44, v15 offset:18944\n\t"
    "ds_read_b32 v46, v15 offset:19200\n\t"
    "s_waitcnt vmcnt(0) lgkmcnt(0)\n\t"
    "s_branch 17b\n\t"
    "20:\n\t"
    "s_mov_b32 %[cap], 0\n\t"
    "s_branch 19b\n\t"
    "22:\n\t"
    "ds_read_b32 v8, v15 offset:28928\n\t"
    "ds_read_b128 v[32:35], v5 offset:25600\n\t"
    "s_waitcnt lgkmcnt(0)\n\t"
    "s_branch 21b\n\t"
    "10:\n\t"
    "v_max_f32 v40, v40, v14\n\t"
    "v_mov_b32 v42, v40\n\t"
    "s_nop 1\n\t"
    "v_permlane16_swap_b32 v40, v42\n\t"
    "v_max_f32 v40, v40, v42\n\t"
    "v_mov_b32 v42, v40\n\t"
    "s_nop 1\n\t"
    "v_permlane32_swap_b32 v40, v42\n\t"
    "v_max_f32 v40, v40, v42\n\t"
    "v_sub_f32 v34, v32, v40\n\t"
    "v_sub_f32 v35, v33, v40\n\t"
    "v_sub_f32 v40, v14, v40\n\t"
    "v_exp_f32 v40, v40\n\t"
    "v_exp_f32 v34, v34\n\t"
    "v_exp_f32 v35, v35\n\t"
    "v_mul_f32 v24, v24, v40\n\t"
    "v_mul_f32 v25, v25, v40\n\t"
    "v_mul_f32 v26, v26, v40\n\t"
    "v_mul_f32 v27, v27, v40\n\t"
    "v_mul_f32 v28, v28, v40\n\t"
    "v_mul_f32 v29, v29, v40\n\t"
    "v_mul_f32 v30, v30, v40\n\t"
    "v_mul_f32 v31, v31, v40\n\t"
    "v_mul_f32 v52, v52, v40\n\t"
    "v_mul_f32 v53, v53, v40\n\t"
    "v_mul_f32 v54, v54, v40\n\t"
    "v_mul_f32 v55, v55, v40\n\t"
    "s_branch 11b\n\t"
    "12:\n\t"
    "v_max3_f32 v14, v24, v25, v26\n\t"
    "v_max3_f32 v40, v27, v28, v29\n\t"
    "v_max3_f32 v14, v14, v30, v31\n\t"
    "v_max3_f32 v14, v14, v40, %[neg]\n\t"
    "v_mov_b32 v40, v14\n\t"
    "s_nop 1\n\t"
    "v_permlane16_swap_b32 v14, v40\n\t"
    "v_max_f32 v14, v14, v40\n\t"
    "v_mov_b32 v40, v14\n\t"
    "s_nop 1\n\t"
    "v_permlane32_swap_b32 v14, v40\n\t"
    "v_max_f32 v14, v14, v40\n\t"
    "v_sub_f32 v24, v24, v14\n\t"
    "v_sub_f32 v25, v25, v14\n\t"
    "v_sub_f32 v26, v26, v14\n\t"
    "v_sub_f32 v27, v27, v14\n\t"
    "v_sub_f32 v28, v28, v14\n\t"
    "v_sub_f32 v29, v29, v14\n\t"
    "v_sub_f32 v30, v30, v14\n\t"
    "v_sub_f32 v31, v31, v14\n\t"
    "s_mov_b32 %[bg], 0x7fc00000\n\t"
    "s_branch 13b\n\t"
    "9:"
    : [tc] "=&s"(tc), [sp] "=&s"(sp), [cap] "+s"(cap), [st] "=&s"(st), [bg] "=&s"(bg)
    : [ag] "s"(ag), [c44] "s"(0x4444u), [lsc] "s"(LSC), [sn] "s"(sn), [pb] "s"(pb), [vs] "s"(vs), [od] "s"(od), [neg] "s"(-1.0e38f), [one] "s"(0x3F803F80u), [big] "s"(24.0f), "{v[64:95]}"(W.w0), "{v[96:127]}"(W.w1), "{v[128:159]}"(W.w2), "{v[160:191]}"(W.w3), "{v[192:223]}"(W.w4), "{v[224:255]}"(W.w5)
    : "v4", "v5", "v6", "v7", "v8", "v9", "v10", "v11", "v12", "v13", "v14", "v15", "v16", "v17", "v18", "v19", "v20", "v21", "v22", "v23", "v24", "v25", "v26", "v27", "v28", "v29", "v30", "v31", "v32", "v33", "v34", "v35", "v36", "v37", "v38", "v39", "v40", "v41", "v42", "v43", "v44", "v45", "v46", "v47", "v48", "v49", "v50", "v51", "v52", "v53", "v54", "v55", "v56", "v57", "v58", "v59", "v60", "v61", "v62", "v63", "vcc", "scc", "memory");
}
template <> __device__ __forceinline__ void qk_step<2>(const QKW& W, const char* ag, const char* vs, const char* v2, const char* od, unsigned pb, unsigned sn, unsigned& cap, unsigned& sp,
    unsigned long long& tc, unsigned long long* ts, unsigned& d0, unsigned& d1) {
  unsigned st, bg;
  asm volatile("v_mbcnt_lo_u32_b32 v60, -1, 0\n\t"
    "v_mbcnt_hi_u32_b32 v60, -1, v60\n\t"
    "v_lshlrev_b32 v5, 4, v60\n\t"
    "s_mov_b32 %[sp], 0\n\t"
    "s_setprio 2\n\t"
    "global_load_dwordx4 v[24:27], v5, %[ag] sc1\n\t"
    "global_load_dwordx4 v[28:31], v5, %[ag] offset:1024 sc1\n\t"
    "s_memtime %[tc]\n\t"
    "v_lshrrev_b32 v32, 4, v60\n\t"
    "v_and_b32 v33, 15, v60\n\t"
    "v_lshrrev_b32 v34, 2, v33\n\t"
    "v_and_b32 v35, 3, v60\n\t"
    "v_and_b32 v61, 1, v60\n\t"
    "v_lshlrev_b32 v61, 3, v61\n\t"
    "v_lshl_or_b32 v7, v34, 4, v61\n\t"
    "v_lshl_or_b32 v7, v32, 7, v7\n\t"
    "v_xor_b32 v10, v32, v33\n\t"
    "v_lshlrev_b32 v10, 4, v10\n\t"
    "v_lshl_or_b32 v10, v33, 8, v10\n\t"
    "v_or_b32 v10, %[pb], v10\n\t"
    "v_xor_b32 v11, 0x40, v10\n\t"
    "v_xor_b32 v12, 0x80, v10\n\t"
    "v_xor_b32 v13, 0xc0, v10\n\t"
    "v_lshl_or_b32 v34, v32, 2, v34\n\t"
    "v_lshrrev_b32 v35, 1, v35\n\t"
    "v_or_b32 v35, 8, v35\n\t"
    "v_xor_b32 v35, v35, v34\n\t"
    "v_lshl_or_b32 v14, v35, 4, v61\n\t"
    "v_lshl_or_b32 v14, v34, 8, v14\n\t"
    "v_or_b32 v14, %[pb], v14\n\t"
    "v_and_b32 v61, 3, v60\n\t"
    "v_lshlrev_b32 v61, 6, v61\n\t"
    "v_lshl_or_b32 v4, v32, 4, v61\n\t"
    "v_and_b32 v61, 4, v60\n\t"
    "v_lshl_or_b32 v4, v61, 10, v4\n\t"
    "v_and_b32 v61, 8, v60\n\t"
    "v_lshl_or_b32 v4, v61, 14, v4\n\t"
    "v_or_b32 v4, 512, v4\n\t"
    "v_and_b32 v61, 2, v60\n\t"
    "v_mul_u32_u24 v61, 0x5555, v61\n\t"
    "v_or_b32 v6, %[c44], v61\n\t"
    "v_lshlrev_b32 v61, 2, v32\n\t"
    "v_lshl_or_b32 v15, v33, 4, v61\n\t"
    "3:\n\t"
    "v_mov_b32 v16, 0\n\t"
    "v_mov_b32 v17, 0\n\t"
    "v_mov_b32 v18, 0\n\t"
    "v_mov_b32 v19, 0\n\t"
    "v_mov_b32 v20, 0\n\t"
    "v_mov_b32 v21, 0\n\t"
    "v_mov_b32 v22, 0\n\t"
    "v_mov_b32 v23, 0\n\t"
    "s_waitcnt vmcnt(0) lgkmcnt(0)\n\t"
    "ds_write_b128 v5, v[24:27] offset:4096\n\t"
    "ds_write_b128 v5, v[28:31] offset:5120\n\t"
    "v_smfmac_f32_16x16x64_bf16 v[16:19], v[24:27], v[128:135], v6\n\t"
    "v_max3_u32 v60, v24, v25, v26\n\t"
    "v_max3_u32 v61, v27, v28, v29\n\t"
    "v_smfmac_f32_16x16x64_bf16 v[20:23], v[24:27], v[136:143], v6\n\t"
    "v_max3_u32 v60, v60, v30, v31\n\t"
    "v_max_u32 v60, v60, v61\n\t"
    "v_cmp_eq_u32 vcc, -1, v60\n\t"
    "s_cbranch_vccnz 1f\n\t"
    "2:\n\t"
    "s_waitcnt lgkmcnt(0)\n\t"
    "s_barrier\n\t"
    "ds_read_b128 v[32:35], v5 offset:0\n\t"
    "ds_read_b128 v[36:39], v5 offset:1024\n\t"
    "v_smfmac_f32_16x16x64_bf16 v[16:19], v[28:31], v[144:151], v6\n\t"
    "ds_read_b128 v[40:43], v5 offset:2048\n\t"
    "ds_read_b128 v[44:47], v5 offset:3072\n\t"
    "v_smfmac_f32_16x16x64_bf16 v[20:23], v[28:31], v[152:159], v6\n\t"
    "s_waitcnt lgkmcnt(3)\n\t"
    "v_smfmac_f32_16x16x64_bf16 v[16:19], v[32:35], v[64:71], v6\n\t"
    "ds_read_b128 v[48:51], v5 offset:6144\n\t"
    "v_smfmac_f32_16x16x64_bf16 v[20:23], v[32:35], v[72:79], v6\n\t"
    "ds_read_b128 v[52:55], v5 offset:7168\n\t"
    "s_waitcnt lgkmcnt(4)\n\t"
    "v_smfmac_f32_16x16x64_bf16 v[16:19], v[36:39], v[80:87], v6\n\t"
    "ds_read_b128 v[24:27], v5 offset:8192\n\t"
    "v_smfmac_f32_16x16x64_bf16 v[20:23], v[36:39], v[88:95], v6\n\t"
    "ds_read_b128 v[28:31], v5 offset:9216\n\t"
    "s_waitcnt lgkmcnt(5)\n\t"
    "v_smfmac_f32_16x16x64_bf16 v[16:19], v[40:43], v[96:103], v6\n\t"
    "ds_read_b128 v[56:59], v5 offset:10240\n\t"
    "v_smfmac_f32_16x16x64_bf16 v[20:23], v[40:43], v[104:111], v6\n\t"
    "ds_read_b128 v[60:63], v5 offset:11264\n\t"
    "ds_read_b128 v[36:39], v11 offset:36864\n\t"
    "s_waitcnt lgkmcnt(7)\n\t"
    "v_smfmac_f32_16x16x64_bf16 v[16:19], v[44:47], v[112:119], v6\n\t"
    "ds_read_b128 v[40:43], v12 offset:32768\n\t"
    "v_smfmac_f32_16x16x64_bf16 v[20:23], v[44:47], v[120:127], v6\n\t"
    "ds_read_b128 v[44:47], v12 offset:36864\n\t"
    "s_waitcnt lgkmcnt(8)\n\t"
    "v_smfmac_f32_16x16x64_bf16 v[16:19], v[48:51], v[160:167], v6\n\t"
    "v_smfmac_f32_16x16x64_bf16 v[20:23], v[48:51], v[168:175], v6\n\t"
    "ds_read_b128 v[48:51], v13 offset:32768\n\t"
    "s_waitcnt lgkmcnt(8)\n\t"
    "v_smfmac_f32_16x16x64_bf16 v[16:19], v[52:55], v[176:183], v6\n\t"
    "v_smfmac_f32_16x16x64_bf16 v[20:23], v[52:55], v[184:191], v6\n\t"
    "ds_read_b128 v[52:55], v13 offset:36864\n\t"
    "s_waitcnt lgkmcnt(8)\n\t"
    "v_smfmac_f32_16x16x64_bf16 v[16:19], v[24:27], v[192:199], v6\n\t"
    "ds_read_b32 v8, v15 offset:29184\n\t"
    "ds_read_b128 v[32:35], v5 offset:26624\n\t"
    "s_setprio 3\n\t"
    "v_smfmac_f32_16x16x64_bf16 v[20:23], v[24:27], v[200:207], v6\n\t"
    "ds_read_b128 v[24:27], v10 offset:32768\n\t"
    "s_waitcnt lgkmcnt(10)\n\t"
    "v_smfmac_f32_16x16x64_bf16 v[16:19], v[28:31], v[208:215], v6\n\t"
    "s_waitcnt lgkmcnt(9)\n\t"
    "v_smfmac_f32_16x16x64_bf16 v[16:19], v[56:59], v[224:231], v6\n\t"
    "s_waitcnt lgkmcnt(8)\n\t"
    "v_smfmac_f32_16x16x64_bf16 v[16:19], v[60:63], v[240:247], v6\n\t"
    "v_smfmac_f32_16x16x64_bf16 v[20:23], v[28:31], v[216:223], v6\n\t"
    "s_waitcnt lgkmcnt(1)\n\t"
    "21:\n\t"
    "v_cmp_ne_u32 vcc, %[sn], v8\n\t"
    "s_cbranch_vccnz 22f\n\t"
    "v_mul_f32 v32, %[lsc], v32\n\t"
    "v_mul_f32 v33, %[lsc], v33\n\t"
    "v_smfmac_f32_16x16x64_bf16 v[20:23], v[56:59], v[232:239], v6\n\t"
    "v_mul_f32 v34, %[lsc], v34\n\t"
    "v_mul_f32 v35, %[lsc], v35\n\t"
    "v_add_f32 v16, v16, v18\n\t"
    "v_add_f32 v17, v17, v19\n\t"
    "v_smfmac_f32_16x16x64_bf16 v[20:23], v[60:63], v[248:255], v6\n\t"
    "v_fma_f32 v16, v16, %[lsc], v32\n\t"
    "v_fma_f32 v17, v17, %[lsc], v33\n\t"
    "v_cvt_pk_bf16_f32 v16, v16, v17\n\t"
    "ds_write_b32 v15, v16 offset:17408\n\t"
    "s_mov_b32 %[bg], %[big]\n\t"
    "s_nop 2\n\t"
    "v_add_f32 v20, v20, v22\n\t"
    "v_add_f32 v21, v21, v23\n\t"
    "v_fma_f32 v20, v20, %[lsc], v34\n\t"
    "v_fma_f32 v21, v21, %[lsc], v35\n\t"
    "v_cvt_pk_bf16_f32 v20, v20, v21\n\t"
    "ds_write_b32 v15, v20 offset:17664\n\t"
    "ds_read_b128 v[28:31], v10 offset:36864\n\t"
    "ds_read_b128 v[32:35], v11 offset:32768\n\t"
    "s_waitcnt lgkmcnt(2)\n\t"
    "s_barrier\n\t"
    "ds_read_b64_tr_b16 v[16:17], v7 offset:16384\n\t"
    "ds_read_b64_tr_b16 v[18:19], v7 offset:16448\n\t"
    "ds_read_b64_tr_b16 v[20:21], v7 offset:16896\n\t"
    "ds_read_b64_tr_b16 v[22:23], v7 offset:16960\n\t"
    "ds_read_b64_tr_b16 v[56:57], v7 offset:17408\n\t"
    "ds_read_b64_tr_b16 v[58:59], v7 offset:17472\n\t"
    "ds_read_b64_tr_b16 v[60:61], v7 offset:17920\n\t"
    "ds_read_b64_tr_b16 v[62:63], v7 offset:17984\n\t"
    "global_load_dwordx4 v[6:9], v5, %[vs] sc1\n\t"
    "global_load_dwordx4 v[10:13], v5, %[vs] offset:1024 sc1\n\t"
    "s_waitcnt lgkmcnt(6)\n\t"
    "v_mfma_f32_16x16x32_bf16 v[24:27], v[24:27], v[16:19], 0\n\t"
    "v_mfma_f32_16x16x32_bf16 v[28:31], v[28:31], v[16:19], 0\n\t"
    "s_waitcnt lgkmcnt(4)\n\t"
    "v_mfma_f32_16x16x32_bf16 v[24:27], v[32:35], v[20:23], v[24:27]\n\t"
    "v_mfma_f32_16x16x32_bf16 v[28:31], v[36:39], v[20:23], v[28:31]\n\t"
    "v_xor_b32 v39, 32, v14\n\t"
    "ds_read_b64_tr_b16 v[32:33], v14 offset:40960\n\t"
    "ds_read_b64_tr_b16 v[34:35], v14 offset:45056\n\t"
    "ds_read_b64_tr_b16 v[36:37], v39 offset:40960\n\t"
    "ds_read_b64_tr_b16 v[38:39], v39 offset:45056\n\t"
    "s_waitcnt lgkmcnt(6)\n\t"
    "v_mfma_f32_16x16x32_bf16 v[24:27], v[40:43], v[56:59], v[24:27]\n\t"
    "v_mfma_f32_16x16x32_bf16 v[28:31], v[44:47], v[56:59], v[28:31]\n\t"
    "s_waitcnt lgkmcnt(4)\n\t"
    "v_mfma_f32_16x16x32_bf16 v[24:27], v[48:51], v[60:63], v[24:27]\n\t"
    "v_mfma_f32_16x16x32_bf16 v[28:31], v[52:55], v[60:63], v[28:31]\n\t"
    "v_mov_b32 v52, %[one]\n\t"
    "v_mov_b32 v53, %[one]\n\t"
    "v_mov_b32 v54, %[one]\n\t"
    "v_mov_b32 v55, %[one]\n\t"
    "v_mov_b32 v45, 0\n\t"
    "v_mov_b32 v47, 0\n\t"
    "v_mov_b32 v48, %[one]\n\t"
    "v_max3_f32 v14, |v24|, |v25|, |v26|\n\t"
    "v_max3_f32 v40, |v27|, |v28|, |v29|\n\t"
    "v_max3_f32 v14, v14, |v30|, |v31|\n\t"
    "v_max_f32 v14, v14, v40\n\t"
    "v_cmp_nge_f32 vcc, %[big], v14\n\t"
    "v_mov_b32 v14, 0\n\t"
    "s_cbranch_vccnz 12f\n\t"
    "13:\n\t"
    "v_lshrrev_b32 v46, 2, v5\n\t"
    "global_load_dword v44, v46, %[v2] sc1\n\t"
    "global_load_dword v46, v46, %[v2] offset:256 sc1\n\t"
    "v_exp_f32 v24, v24\n\t"
    "v_exp_f32 v25, v25\n\t"
    "v_exp_f32 v26, v26\n\t"
    "v_exp_f32 v27, v27\n\t"
    "v_exp_f32 v28, v28\n\t"
    "v_exp_f32 v29, v29\n\t"
    "v_exp_f32 v30, v30\n\t"
    "v_exp_f32 v31, v31\n\t"
    "v_cvt_pk_bf16_f32 v40, v24, v25\n\t"
    "v_cvt_pk_bf16_f32 v41, v26, v27\n\t"
    "v_cvt_pk_bf16_f32 v42, v28, v29\n\t"
    "v_cvt_pk_bf16_f32 v43, v30, v31\n\t"
    "v_mov_b32 v49, 0\n\t"
    "v_mov_b32 v51, 0\n\t"
    "s_waitcnt lgkmcnt(0)\n\t"
    "v_mfma_f32_16x16x32_bf16 v[52:55], v[52:55], v[40:43], 0\n\t"
    "v_mfma_f32_16x16x32_bf16 v[24:27], v[32:35], v[40:43], 0\n\t"
    "v_mfma_f32_16x16x32_bf16 v[28:31], v[36:39], v[40:43], 0\n\t"
    "s_waitcnt vmcnt(3)\n\t"
    "5:\n\t"
    "v_mfma_f32_16x16x32_bf16 v[32:35], v[6:9], v[16:19], 0\n\t"
    "v_mfma_f32_16x16x32_bf16 v[36:39], v[6:9], v[20:23], 0\n\t"
    "s_waitcnt vmcnt(2)\n\t"
    "v_mfma_f32_16x16x32_bf16 v[32:35], v[10:13], v[56:59], v[32:35]\n\t"
    "v_mfma_f32_16x16x32_bf16 v[36:39], v[10:13], v[60:63], v[36:39]\n\t"
    "v_max3_u32 v40, v6, v7, v8\n\t"
    "v_max3_u32 v41, v9, v10, v11\n\t"
    "v_max3_u32 v40, v40, v12, v13\n\t"
    "v_max_u32 v40, v40, v41\n\t"
    "v_cmp_eq_u32 vcc, -1, v40\n\t"
    "s_cbranch_vccnz 4f\n\t"
    "6:\n\t"
    "s_nop 0\n\t"
    "s_nop 0\n\t"
    "v_add_f32 v32, v32, v38\n\t"
    "v_add_f32 v33, v33, v39\n\t"
    "v_exp_f32 v34, v32\n\t"
    "v_max_f32 v40, v32, v33\n\t"
    "v_exp_f32 v35, v33\n\t"
    "v_cmp_nge_f32 vcc, %[bg], v40\n\t"
    "s_cbranch_vccnz 10f\n\t"
    "11:\n\t"
    "v_cvt_pk_bf16_f32 v50, v34, v35\n\t"
    "17:\n\t"
    "s_waitcnt vmcnt(0) lgkmcnt(0)\n\t"
    "v_max_u32 v40, v44, v46\n\t"
    "v_cmp_eq_u32 vcc, -1, v40\n\t"
    "s_cbranch_vccnz 18f\n\t"
    "19:\n\t"
    "v_mfma_f32_16x16x16_bf16 v[52:55], v[48:49], v[50:51], v[52:55]\n\t"
    "v_mfma_f32_16x16x16_bf16 v[24:27], v[44:45], v[50:51], v[24:27]\n\t"
    "v_mfma_f32_16x16x16_bf16 v[28:31], v[46:47], v[50:51], v[28:31]\n\t"
    "s_nop 5\n\t"
    "v_rcp_f32 v42, v52\n\t"
    "s_nop 0\n\t"
    "v_pk_mul_f32 v[24:25], v[24:25], v[42:43] op_sel_hi:[1,0]\n\t"
    "v_pk_mul_f32 v[26:27], v[26:27], v[42:43] op_sel_hi:[1,0]\n\t"
    "v_pk_mul_f32 v[28:29], v[28:29], v[42:43] op_sel_hi:[1,0]\n\t"
    "v_pk_mul_f32 v[30:31], v[30:31], v[42:43] op_sel_hi:[1,0]\n\t"
    "v_cvt_pk_bf16_f32 v36, v24, v25\n\t"
    "v_cvt_pk_bf16_f32 v37, v26, v27\n\t"
    "v_cvt_pk_bf16_f32 v38, v28, v29\n\t"
    "v_cvt_pk_bf16_f32 v39, v30, v31\n\t"
    "v_min_u32 v36, -2, v36\n\t"
    "v_min_u32 v37, -2, v37\n\t"
    "v_min_u32 v38, -2, v38\n\t"
    "v_min_u32 v39, -2, v39\n\t"
    "global_store_dwordx4 v4, v[36:39], %[od] sc1\n\t"
    "ds_write_b32 v15, v44 offset:19456\n\t"
    "ds_write_b32 v15, v46 offset:19712\n\t"
    "s_setprio 1\n\t"
    "s_waitcnt lgkmcnt(0)\n\t"
    "s_barrier\n\t"
    "s_waitcnt lgkmcnt(0)\n\t"
    "s_branch 9f\n\t"
    "1:\n\t"
    "s_add_u32 %[sp], %[sp], 1\n\t"
    "s_cmp_ge_u32 %[sp], %[cap]\n\t"
    "s_cbranch_scc1 7f\n\t"
    "global_load_dwordx4 v[24:27], v5, %[ag] sc1\n\t"
    "global_load_dwordx4 v[28:31], v5, %[ag] offset:1024 sc1\n\t"
    "s_waitcnt lgkmcnt(0)\n\t"
    "s_memtime %[tc]\n\t"
    "s_nop 7\n\t"
    "s_branch 3b\n\t"
    "7:\n\t"
    "s_mov_b32 %[cap], 0\n\t"
    "s_branch 2b\n\t"
    "4:\n\t"
    "s_add_u32 %[sp], %[sp], 0x10000\n\t"
    "s_lshr_b32 %[st], %[sp], 16\n\t"
    "s_cmp_ge_u32 %[st], %[cap]\n\t"
    "s_cbranch_scc1 8f\n\t"
    "global_load_dwordx4 v[6:9], v5, %[vs] sc1\n\t"
    "global_load_dwordx4 v[10:13], v5, %[vs] offset:1024 sc1\n\t"
    "s_waitcnt vmcnt(0) lgkmcnt(0)\n\t"
    "s_branch 5b\n\t"
    "8:\n\t"
    "s_mov_b32 %[cap], 0\n\t"
    "s_branch 6b\n\t"
    "18:\n\t"
    "s_add_u32 %[sp], %[sp], 0x10000\n\t"
    "s_lshr_b32 %[st], %[sp], 16\n\t"
    "s_cmp_ge_u32 %[st], %[cap]\n\t"
    "s_cbranch_scc1 20f\n\t"
    "v_lshrrev_b32 v46, 2, v5\n\t"
    "global_load_dword v44, v46, %[v2] sc1\n\t"
    "global_load_dword v46, v46, %[v2] offset:256 sc1\n\t"
    "s_waitcnt vmcnt(0) lgkmcnt(0)\n\t"
    "s_branch 17b\n\t"
    "20:\n\t"
    "s_mov_b32 %[cap], 0\n\t"
    "s_branch 19b\n\t"
    "22:\n\t"
    "ds_read_b32 v8, v15 offset:29184\n\t"
    "ds_read_b128 v[32:35], v5 offset:26624\n\t"
    "s_waitcnt lgkmcnt(0)\n\t"
    "s_branch 21b\n\t"
    "10:\n\t"
    "v_max_f32 v40, v40, v14\n\t"
    "v_mov_b32 v42, v40\n\t"
    "s_nop 1\n\t"
    "v_permlane16_swap_b32 v40, v42\n\t"
    "v_max_f32 v40, v40, v42\n\t"
    "v_mov_b32 v42, v40\n\t"
    "s_nop 1\n\t"
    "v_permlane32_swap_b32 v40, v42\n\t"
    "v_max_f32 v40, v40, v42\n\t"
    "v_sub_f32 v34, v32, v40\n\t"
    "v_sub_f32 v35, v33, v40\n\t"
    "v_sub_f32 v40, v14, v40\n\t"
    "v_exp_f32 v40, v40\n\t"
    "v_exp_f32 v34, v34\n\t"
    "v_exp_f32 v35, v35\n\t"
    "v_mul_f32 v24, v24, v40\n\t"
    "v_mul_f32 v25, v25, v40\n\t"
    "v_mul_f32 v26, v26, v40\n\t"
    "v_mul_f32 v27, v27, v40\n\t"
    "v_mul_f32 v28, v28, v40\n\t"
    "v_mul_f32 v29, v29, v40\n\t"
    "v_mul_f32 v30, v30, v40\n\t"
    "v_mul_f32 v31, v31, v40\n\t"
    "v_mul_f32 v52, v52, v40\n\t"
    "v_mul_f32 v53, v53, v40\n\t"
    "v_mul_f32 v54, v54, v40\n\t"
    "v_mul_f32 v55, v55, v40\n\t"
    "s_branch 11b\n\t"
    "12:\n\t"
    "v_max3_f32 v14, v24, v25, v26\n\t"
    "v_max3_f32 v40, v27, v28, v29\n\t"
    "v_max3_f32 v14, v14, v30, v31\n\t"
    "v_max3_f32 v14, v14, v40, %[neg]\n\t"
    "v_mov_b32 v40, v14\n\t"
    "s_nop 1\n\t"
    "v_permlane16_swap_b32 v14, v40\n\t"
    "v_max_f32 v14, v14, v40\n\t"
    "v_mov_b32 v40, v14\n\t"
    "s_nop 1\n\t"
    "v_permlane32_swap_b32 v14, v40\n\t"
    "v_max_f32 v14, v14, v40\n\t"
    "v_sub_f32 v24, v24, v14\n\t"
    "v_sub_f32 v25, v25, v14\n\t"
    "v_sub_f32 v26, v26, v14\n\t"
    "v_sub_f32 v27, v27, v14\n\t"
    "v_sub_f32 v28, v28, v14\n\t"
    "v_sub_f32 v29, v29, v14\n\t"
    "v_sub_f32 v30, v30, v14\n\t"
    "v_sub_f32 v31, v31, v14\n\t"
    "s_mov_b32 %[bg], 0x7fc00000\n\t"
    "s_branch 13b\n\t"
    "9:"
    : [tc] "=&s"(tc), [sp] "=&s"(sp), [cap] "+s"(cap), [st] "=&s"(st), [bg] "=&s"(bg)
    : [ag] "s"(ag), [c44] "s"(0x4444u), [lsc] "s"(LSC), [sn] "s"(sn), [pb] "s"(pb), [vs] "s"(vs), [od] "s"(od), [neg] "s"(-1.0e38f), [one] "s"(0x3F803F80u), [big] "s"(24.0f), [v2] "s"(v2), "{v[64:95]}"(W.w0), "{v[96:127]}"(W.w1), "{v[128:159]}"(W.w2), "{v[160:191]}"(W.w3), "{v[192:223]}"(W.w4), "{v[224:255]}"(W.w5)
    : "v4", "v5", "v6", "v7", "v8", "v9", "v10", "v11", "v12", "v13", "v14", "v15", "v16", "v17", "v18", "v19", "v20", "v21", "v22", "v23", "v24", "v25", "v26", "v27", "v28", "v29", "v30", "v31", "v32", "v33", "v34", "v35", "v36", "v37", "v38", "v39", "v40", "v41", "v42", "v43", "v44", "v45", "v46", "v47", "v48", "v49", "v50", "v51", "v52", "v53", "v54", "v55", "v56", "v57", "v58", "v59", "v60", "v61", "v62", "v63", "vcc", "scc", "memory");
}
template <> __device__ __forceinline__ void qk_step<3>(const QKW& W, const char* ag, const char* vs, const char* v2, const char* od, unsigned pb, unsigned sn, unsigned& cap, unsigned& sp,
    unsigned long long& tc, unsigned long long* ts, unsigned& d0, unsigned& d1) {
  unsigned st, bg;
  asm volatile("v_mbcnt_lo_u32_b32 v60, -1, 0\n\t"
    "v_mbcnt_hi_u32_b32 v60, -1, v60\n\t"
    "v_lshlrev_b32 v5, 4, v60\n\t"
    "s_mov_b32 %[sp], 0\n\t"
    "s_setprio 2\n\t"
    "global_load_dwordx4 v[24:27], v5, %[ag] sc1\n\t"
    "global_load_dwordx4 v[28:31], v5, %[ag] offset:1024 sc1\n\t"
    "s_memtime %[tc]\n\t"
    "v_lshrrev_b32 v32, 4, v60\n\t"
    "v_and_b32 v33, 15, v60\n\t"
    "v_lshrrev_b32 v34, 2, v33\n\t"
    "v_and_b32 v35, 3, v60\n\t"
    "v_and_b32 v61, 1, v60\n\t"
    "v_lshlrev_b32 v61, 3, v61\n\t"
    "v_lshl_or_b32 v7, v34, 4, v61\n\t"
    "v_lshl_or_b32 v7, v32, 7, v7\n\t"
    "v_xor_b32 v10, v32, v33\n\t"
    "v_lshlrev_b32 v10, 4, v10\n\t"
    "v_lshl_or_b32 v10, v33, 8, v10\n\t"
    "v_or_b32 v10, %[pb], v10\n\t"
    "v_xor_b32 v11, 0x40, v10\n\t"
    "v_xor_b32 v12, 0x80, v10\n\t"
    "v_xor_b32 v13, 0xc0, v10\n\t"
    "v_lshl_or_b32 v34, v32, 2, v34\n\t"
    "v_lshrrev_b32 v35, 1, v35\n\t"
    "v_or_b32 v35, 12, v35\n\t"
    "v_xor_b32 v35, v35, v34\n\t"
    "v_lshl_or_b32 v14, v35, 4, v61\n\t"
    "v_lshl_or_b32 v14, v34, 8, v14\n\t"
    "v_or_b32 v14, %[pb], v14\n\t"
    "v_and_b32 v61, 3, v60\n\t"
    "v_lshlrev_b32 v61, 6, v61\n\t"
    "v_lshl_or_b32 v4, v32, 4, v61\n\t"
    "v_and_b32 v61, 4, v60\n\t"
    "v_lshl_or_b32 v4, v61, 10, v4\n\t"
    "v_and_b32 v61, 8, v60\n\t"
    "v_lshl_or_b32 v4, v61, 14, v4\n\t"
    "v_or_b32 v4, 768, v4\n\t"
    "v_and_b32 v61, 2, v60\n\t"
    "v_mul_u32_u24 v61, 0x5555, v61\n\t"
    "v_or_b32 v6, %[c44], v61\n\t"
    "v_lshlrev_b32 v61, 2, v32\n\t"
    "v_lshl_or_b32 v15, v33, 4, v61\n\t"
    "3:\n\t"
    "v_mov_b32 v16, 0\n\t"
    "v_mov_b32 v17, 0\n\t"
    "v_mov_b32 v18, 0\n\t"
    "v_mov_b32 v19, 0\n\t"
    "v_mov_b32 v20, 0\n\t"
    "v_mov_b32 v21, 0\n\t"
    "v_mov_b32 v22, 0\n\t"
    "v_mov_b32 v23, 0\n\t"
    "s_waitcnt vmcnt(0) lgkmcnt(0)\n\t"
    "ds_write_b128 v5, v[24:27] offset:6144\n\t"
    "ds_write_b128 v5, v[28:31] offset:7168\n\t"
    "v_smfmac_f32_16x16x64_bf16 v[16:19], v[24:27], v[160:167], v6\n\t"
    "v_max3_u32 v60, v24, v25, v26\n\t"
    "v_max3_u32 v61, v27, v28, v29\n\t"
    "v_smfmac_f32_16x16x64_bf16 v[20:23], v[24:27], v[168:175], v6\n\t"
    "v_max3_u32 v60, v60, v30, v31\n\t"
    "v_max_u32 v60, v60, v61\n\t"
    "v_cmp_eq_u32 vcc, -1, v60\n\t"
    "s_cbranch_vccnz 1f\n\t"
    "2:\n\t"
    "s_waitcnt lgkmcnt(0)\n\t"
    "s_barrier\n\t"
    "ds_read_b128 v[32:35], v5 offset:0\n\t"
    "ds_read_b128 v[36:39], v5 offset:1024\n\t"
    "v_smfmac_f32_16x16x64_bf16 v[16:19], v[28:31], v[176:183], v6\n\t"
    "ds_read_b128 v[40:43], v5 offset:2048\n\t"
    "ds_read_b128 v[44:47], v5 offset:3072\n\t"
    "v_smfmac_f32_16x16x64_bf16 v[20:23], v[28:31], v[184:191], v6\n\t"
    "s_waitcnt lgkmcnt(3)\n\t"
    "v_smfmac_f32_16x16x64_bf16 v[16:19], v[32:35], v[64:71], v6\n\t"
    "ds_read_b128 v[48:51], v5 offset:4096\n\t"
    "v_smfmac_f32_16x16x64_bf16 v[20:23], v[32:35], v[72:79], v6\n\t"
    "ds_read_b128 v[52:55], v5 offset:5120\n\t"
    "s_waitcnt lgkmcnt(4)\n\t"
    "v_smfmac_f32_16x16x64_bf16 v[16:19], v[36:39], v[80:87], v6\n\t"
    "ds_read_b128 v[24:27], v5 offset:8192\n\t"
    "v_smfmac_f32_16x16x64_bf16 v[20:23], v[36:39], v[88:95], v6\n\t"
    "ds_read_b128 v[28:31], v5 offset:9216\n\t"
    "s_waitcnt lgkmcnt(5)\n\t"
    "v_smfmac_f32_16x16x64_bf16 v[16:19], v[40:43], v[96:103], v6\n\t"
    "ds_read_b128 v[56:59], v5 offset:10240\n\t"
    "v_smfmac_f32_16x16x64_bf16 v[20:23], v[40:43], v[104:111], v6\n\t"
    "ds_read_b128 v[60:63], v5 offset:11264\n\t"
    "ds_read_b128 v[36:39], v11 offset:36864\n\t"
    "s_waitcnt lgkmcnt(7)\n\t"
    "v_smfmac_f32_16x16x64_bf16 v[16:19], v[44:47], v[112:119], v6\n\t"
    "ds_read_b128 v[40:43], v12 offset:32768\n\t"
    "v_smfmac_f32_16x16x64_bf16 v[20:23], v[44:47], v[120:127], v6\n\t"
    "ds_read_b128 v[44:47], v12 offset:36864\n\t"
    "s_waitcnt lgkmcnt(8)\n\t"
    "v_smfmac_f32_16x16x64_bf16 v[16:19], v[48:51], v[128:135], v6\n\t"
    "v_smfmac_f32_16x16x64_bf16 v[20:23], v[48:51], v[136:143], v6\n\t"
    "ds_read_b128 v[48:51], v13 offset:32768\n\t"
    "s_waitcnt lgkmcnt(8)\n\t"
    "v_smfmac_f32_16x16x64_bf16 v[16:19], v[52:55], v[144:151], v6\n\t"
    "v_smfmac_f32_16x16x64_bf16 v[20:23], v[52:55], v[152:159], v6\n\t"
    "ds_read_b128 v[52:55], v13 offset:36864\n\t"
    "s_waitcnt lgkmcnt(8)\n\t"
    "v_smfmac_f32_16x16x64_bf16 v[16:19], v[24:27], v[192:199], v6\n\t"
    "ds_read_b32 v8, v15 offset:29440\n\t"
    "ds_read_b128 v[32:35], v5 offset:27648\n\t"
    "s_setprio 3\n\t"
    "v_smfmac_f32_16x16x64_bf16 v[20:23], v[24:27], v[200:207], v6\n\t"
    "ds_read_b128 v[24:27], v10 offset:32768\n\t"
    "s_waitcnt lgkmcnt(10)\n\t"
    "v_smfmac_f32_16x16x64_bf16 v[16:19], v[28:31], v[208:215], v6\n\t"
    "s_waitcnt lgkmcnt(9)\n\t"
    "v_smfmac_f32_16x16x64_bf16 v[16:19], v[56:59], v[224:231], v6\n\t"
    "s_waitcnt lgkmcnt(8)\n\t"
    "v_smfmac_f32_16x16x64_bf16 v[16:19], v[60:63], v[240:247], v6\n\t"
    "v_smfmac_f32_16x16x64_bf16 v[20:23], v[28:31], v[216:223], v6\n\t"
    "s_waitcnt lgkmcnt(1)\n\t"
    "21:\n\t"
    "v_cmp_ne_u32 vcc, %[sn], v8\n\t"
    "s_cbranch_vccnz 22f\n\t"
    "v_mul_f32 v32, %[lsc], v32\n\t"
    "v_mul_f32 v33, %[lsc], v33\n\t"
    "v_smfmac_f32_16x16x64_bf16 v[20:23], v[56:59], v[232:239], v6\n\t"
    "v_mul_f32 v34, %[lsc], v34\n\t"
    "v_mul_f32 v35, %[lsc], v35\n\t"
    "v_add_f32 v16, v16, v18\n\t"
    "v_add_f32 v17, v17, v19\n\t"
    "v_smfmac_f32_16x16x64_bf16 v[20:23], v[60:63], v[248:255], v6\n\t"
    "v_fma_f32 v16, v16, %[lsc], v32\n\t"
    "v_fma_f32 v17, v17, %[lsc], v33\n\t"
    "v_cvt_pk_bf16_f32 v16, v16, v17\n\t"
    "ds_write_b32 v15, v16 offset:17920\n\t"
    "s_mov_b32 %[bg], %[big]\n\t"
    "s_nop 2\n\t"
    "v_add_f32 v20, v20, v22\n\t"
    "v_add_f32 v21, v21, v23\n\t"
    "v_fma_f32 v20, v20, %[lsc], v34\n\t"
    "v_fma_f32 v21, v21, %[lsc], v35\n\t"
    "v_cvt_pk_bf16_f32 v20, v20, v21\n\t"
    "ds_write_b32 v15, v20 offset:18176\n\t"
    "ds_read_b128 v[28:31], v10 offset:36864\n\t"
    "ds_read_b128 v[32:35], v11 offset:32768\n\t"
    "s_waitcnt lgkmcnt(2)\n\t"
    "s_barrier\n\t"
    "ds_read_b64_tr_b16 v[16:17], v7 offset:16384\n\t"
    "ds_read_b64_tr_b16 v[18:19], v7 offset:16448\n\t"
    "ds_read_b64_tr_b16 v[20:21], v7 offset:16896\n\t"
    "ds_read_b64_tr_b16 v[22:23], v7 offset:16960\n\t"
    "ds_read_b64_tr_b16 v[56:57], v7 offset:17408\n\t"
    "ds_read_b64_tr_b16 v[58:59], v7 offset:17472\n\t"
    "ds_read_b64_tr_b16 v[60:61], v7 offset:17920\n\t"
    "ds_read_b64_tr_b16 v[62:63], v7 offset:17984\n\t"
    "global_load_dwordx4 v[6:9], v5, %[vs] sc1\n\t"
    "global_load_dwordx4 v[10:13], v5, %[vs] offset:1024 sc1\n\t"
    "s_waitcnt lgkmcnt(6)\n\t"
    "v_mfma_f32_16x16x32_bf16 v[24:27], v[24:27], v[16:19], 0\n\t"
    "v_mfma_f32_16x16x32_bf16 v[28:31], v[28:31], v[16:19], 0\n\t"
    "s_waitcnt lgkmcnt(4)\n\t"
    "v_mfma_f32_16x16x32_bf16 v[24:27], v[32:35], v[20:23], v[24:27]\n\t"
    "v_mfma_f32_16x16x32_bf16 v[28:31], v[36:39], v[20:23], v[28:31]\n\t"
    "v_xor_b32 v39, 32, v14\n\t"
    "ds_read_b64_tr_b16 v[32:33], v14 offset:40960\n\t"
    "ds_read_b64_tr_b16 v[34:35], v14 offset:45056\n\t"
    "ds_read_b64_tr_b16 v[36:37], v39 offset:40960\n\t"
    "ds_read_b64_tr_b16 v[38:39], v39 offset:45056\n\t"
    "s_waitcnt lgkmcnt(6)\n\t"
    "v_mfma_f32_16x16x32_bf16 v[24:27], v[40:43], v[56:59], v[24:27]\n\t"
    "v_mfma_f32_16x16x32_bf16 v[28:31], v[44:47], v[56:59], v[28:31]\n\t"
    "s_waitcnt lgkmcnt(4)\n\t"
    "v_mfma_f32_16x16x32_bf16 v[24:27], v[48:51], v[60:63], v[24:27]\n\t"
    "v_mfma_f32_16x16x32_bf16 v[28:31], v[52:55], v[60:63], v[28:31]\n\t"
    "v_mov_b32 v52, %[one]\n\t"
    "v_mov_b32 v53, %[one]\n\t"
    "v_mov_b32 v54, %[one]\n\t"
    "v_mov_b32 v55, %[one]\n\t"
    "v_mov_b32 v45, 0\n\t"
    "v_mov_b32 v47, 0\n\t"
    "v_mov_b32 v48, %[one]\n\t"
    "v_max3_f32 v14, |v24|, |v25|, |v26|\n\t"
    "v_max3_f32 v40, |v27|, |v28|, |v29|\n\t"
    "v_max3_f32 v14, v14, |v30|, |v31|\n\t"
    "v_max_f32 v14, v14, v40\n\t"
    "v_cmp_nge_f32 vcc, %[big], v14\n\t"
    "v_mov_b32 v14, 0\n\t"
    "s_cbranch_vccnz 12f\n\t"
    "13:\n\t"
    "v_lshrrev_b32 v46, 2, v5\n\t"
    "global_load_dword v44, v46, %[v2] sc1\n\t"
    "global_load_dword v46, v46, %[v2] offset:256 sc1\n\t"
    "v_exp_f32 v24, v24\n\t"
    "v_exp_f32 v25, v25\n\t"
    "v_exp_f32 v26, v26\n\t"
    "v_exp_f32 v27, v27\n\t"
    "v_exp_f32 v28, v28\n\t"
    "v_exp_f32 v29, v29\n\t"
    "v_exp_f32 v30, v30\n\t"
    "v_exp_f32 v31, v31\n\t"
    "v_cvt_pk_bf16_f32 v40, v24, v25\n\t"
    "v_cvt_pk_bf16_f32 v41, v26, v27\n\t"
    "v_cvt_pk_bf16_f32 v42, v28, v29\n\t"
    "v_cvt_pk_bf16_f32 v43, v30, v31\n\t"
    "v_mov_b32 v49, 0\n\t"
    "v_mov_b32 v51, 0\n\t"
    "s_waitcnt lgkmcnt(0)\n\t"
    "v_mfma_f32_16x16x32_bf16 v[52:55], v[52:55], v[40:43], 0\n\t"
    "v_mfma_f32_16x16x32_bf16 v[24:27], v[32:35], v[40:43], 0\n\t"
    "v_mfma_f32_16x16x32_bf16 v[28:31], v[36:39], v[40:43], 0\n\t"
    "s_waitcnt vmcnt(3)\n\t"
    "5:\n\t"
    "v_mfma_f32_16x16x32_bf16 v[32:35], v[6:9], v[16:19], 0\n\t"
    "v_mfma_f32_16x16x32_bf16 v[36:39], v[6:9], v[20:23], 0\n\t"
    "s_waitcnt vmcnt(2)\n\t"
    "v_mfma_f32_16x16x32_bf16 v[32:35], v[10:13], v[56:59], v[32:35]\n\t"
    "v_mfma_f32_16x16x32_bf16 v[36:39], v[10:13], v[60:63], v[36:39]\n\t"
    "v_max3_u32 v40, v6, v7, v8\n\t"
    "v_max3_u32 v41, v9, v10, v11\n\t"
    "v_max3_u32 v40, v40, v12, v13\n\t"
    "v_max_u32 v40, v40, v41\n\t"
    "v_cmp_eq_u32 vcc, -1, v40\n\t"
    "s_cbranch_vccnz 4f\n\t"
    "6:\n\t"
    "s_nop 0\n\t"
    "s_nop 0\n\t"
    "v_add_f32 v32, v32, v38\n\t"
    "v_add_f32 v33, v33, v39\n\t"
    "v_exp_f32 v34, v32\n\t"
    "v_max_f32 v40, v32, v33\n\t"
    "v_exp_f32 v35, v33\n\t"
    "v_cmp_nge_f32 vcc, %[bg], v40\n\t"
    "s_cbranch_vccnz 10f\n\t"
    "11:\n\t"
    "v_cvt_pk_bf16_f32 v50, v34, v35\n\t"
    "17:\n\t"
    "s_waitcnt vmcnt(0) lgkmcnt(0)\n\t"
    "v_max_u32 v40, v44, v46\n\t"
    "v_cmp_eq_u32 vcc, -1, v40\n\t"
    "s_cbranch_vccnz 18f\n\t"
    "19:\n\t"
    "v_mfma_f32_16x16x16_bf16 v[52:55], v[48:49], v[50:51], v[52:55]\n\t"
    "v_mfma_f32_16x16x16_bf16 v[24:27], v[44:45], v[50:51], v[24:27]\n\t"
    "v_mfma_f32_16x16x16_bf16 v[28:31], v[46:47], v[50:51], v[28:31]\n\t"
    "s_nop 5\n\t"
    "v_rcp_f32 v42, v52\n\t"
    "s_nop 0\n\t"
    "v_pk_mul_f32 v[24:25], v[24:25], v[42:43] op_sel_hi:[1,0]\n\t"
    "v_pk_mul_f32 v[26:27], v[26:27], v[42:43] op_sel_hi:[1,0]\n\t"
    "v_pk_mul_f32 v[28:29], v[28:29], v[42:43] op_sel_hi:[1,0]\n\t"
    "v_pk_mul_f32 v[30:31], v[30:31], v[42:43] op_sel_hi:[1,0]\n\t"
    "v_cvt_pk_bf16_f32 v36, v24, v25\n\t"
    "v_cvt_pk_bf16_f32 v37, v26, v27\n\t"
    "v_cvt_pk_bf16_f32 v38, v28, v29\n\t"
    "v_cvt_pk_bf16_f32 v39, v30, v31\n\t"
    "v_min_u32 v36, -2, v36\n\t"
    "v_min_u32 v37, -2, v37\n\t"
    "v_min_u32 v38, -2, v38\n\t"
    "v_min_u32 v39, -2, v39\n\t"
    "global_store_dwordx4 v4, v[36:39], %[od] sc1\n\t"
    "ds_write_b32 v15, v44 offset:19968\n\t"
    "ds_write_b32 v15, v46 offset:20224\n\t"
    "s_setprio 1\n\t"
    "s_waitcnt lgkmcnt(0)\n\t"
    "s_barrier\n\t"
    "s_waitcnt lgkmcnt(0)\n\t"
    "s_branch 9f\n\t"
    "1:\n\t"
    "s_add_u32 %[sp], %[sp], 1\n\t"
    "s_cmp_ge_u32 %[sp], %[cap]\n\t"
    "s_cbranch_scc1 7f\n\t"
    "global_load_dwordx4 v[24:27], v5, %[ag] sc1\n\t"
    "global_load_dwordx4 v[28:31], v5, %[ag] offset:1024 sc1\n\t"
    "s_waitcnt lgkmcnt(0)\n\t"
    "s_memtime %[tc]\n\t"
    "s_nop 7\n\t"
    "s_branch 3b\n\t"
    "7:\n\t"
    "s_mov_b32 %[cap], 0\n\t"
    "s_branch 2b\n\t"
    "4:\n\t"
    "s_add_u32 %[sp], %[sp], 0x10000\n\t"
    "s_lshr_b32 %[st], %[sp], 16\n\t"
    "s_cmp_ge_u32 %[st], %[cap]\n\t"
    "s_cbranch_scc1 8f\n\t"
    "global_load_dwordx4 v[6:9], v5, %[vs] sc1\n\t"
    "global_load_dwordx4 v[10:13], v5, %[vs] offset:1024 sc1\n\t"
    "s_waitcnt vmcnt(0) lgkmcnt(0)\n\t"
    "s_branch 5b\n\t"
    "8:\n\t"
    "s_mov_b32 %[cap], 0\n\t"
    "s_branch 6b\n\t"
    "18:\n\t"
    "s_add_u32 %[sp], %[sp], 0x10000\n\t"
    "s_lshr_b32 %[st], %[sp], 16\n\t"
    "s_cmp_ge_u32 %[st], %[cap]\n\t"
    "s_cbranch_scc1 20f\n\t"
    "v_lshrrev_b32 v46, 2, v5\n\t"
    "global_load_dword v44, v46, %[v2] sc1\n\t"
    "global_load_dword v46, v46, %[v2] offset:256 sc1\n\t"
    "s_waitcnt vmcnt(0) lgkmcnt(0)\n\t"
    "s_branch 17b\n\t"
    "20:\n\t"
    "s_mov_b32 %[cap], 0\n\t"
    "s_branch 19b\n\t"
    "22:\n\t"
    "ds_read_b32 v8, v15 offset:29440\n\t"
    "ds_read_b128 v[32:35], v5 offset:27648\n\t"
    "s_waitcnt lgkmcnt(0)\n\t"
    "s_branch 21b\n\t"
    "10:\n\t"
    "v_max_f32 v40, v40, v14\n\t"
    "v_mov_b32 v42, v40\n\t"
    "s_nop 1\n\t"
    "v_permlane16_swap_b32 v40, v42\n\t"
    "v_max_f32 v40, v40, v42\n\t"
    "v_mov_b32 v42, v40\n\t"
    "s_nop 1\n\t"
    "v_permlane32_swap_b32 v40, v42\n\t"
    "v_max_f32 v40, v40, v42\n\t"
    "v_sub_f32 v34, v32, v40\n\t"
    "v_sub_f32 v35, v33, v40\n\t"
    "v_sub_f32 v40, v14, v40\n\t"
    "v_exp_f32 v40, v40\n\t"
    "v_exp_f32 v34, v34\n\t"
    "v_exp_f32 v35, v35\n\t"
    "v_mul_f32 v24, v24, v40\n\t"
    "v_mul_f32 v25, v25, v40\n\t"
    "v_mul_f32 v26, v26, v40\n\t"
    "v_mul_f32 v27, v27, v40\n\t"
    "v_mul_f32 v28, v28, v40\n\t"
    "v_mul_f32 v29, v29, v40\n\t"
    "v_mul_f32 v30, v30, v40\n\t"
    "v_mul_f32 v31, v31, v40\n\t"
    "v_mul_f32 v52, v52, v40\n\t"
    "v_mul_f32 v53, v53, v40\n\t"
    "v_mul_f32 v54, v54, v40\n\t"
    "v_mul_f32 v55, v55, v40\n\t"
    "s_branch 11b\n\t"
    "12:\n\t"
    "v_max3_f32 v14, v24, v25, v26\n\t"
    "v_max3_f32 v40, v27, v28, v29\n\t"
    "v_max3_f32 v14, v14, v30, v31\n\t"
    "v_max3_f32 v14, v14, v40, %[neg]\n\t"
    "v_mov_b32 v40, v14\n\t"
    "s_nop 1\n\t"
    "v_permlane16_swap_b32 v14, v40\n\t"
    "v_max_f32 v14, v14, v40\n\t"
    "v_mov_b32 v40, v14\n\t"
    "s_nop 1\n\t"
    "v_permlane32_swap_b32 v14, v40\n\t"
    "v_max_f32 v14, v14, v40\n\t"
    "v_sub_f32 v24, v24, v14\n\t"
    "v_sub_f32 v25, v25, v14\n\t"
    "v_sub_f32 v26, v26, v14\n\t"
    "v_sub_f32 v27, v27, v14\n\t"
    "v_sub_f32 v28, v28, v14\n\t"
    "v_sub_f32 v29, v29, v14\n\t"
    "v_sub_f32 v30, v30, v14\n\t"
    "v_sub_f32 v31, v31, v14\n\t"
    "s_mov_b32 %[bg], 0x7fc00000\n\t"
    "s_branch 13b\n\t"
    "9:"
    : [tc] "=&s"(tc), [sp] "=&s"(sp), [cap] "+s"(cap), [st] "=&s"(st), [bg] "=&s"(bg)
    : [ag] "s"(ag), [c44] "s"(0x4444u), [lsc] "s"(LSC), [sn] "s"(sn), [pb] "s"(pb), [vs] "s"(vs), [od] "s"(od), [neg] "s"(-1.0e38f), [one] "s"(0x3F803F80u), [big] "s"(24.0f), [v2] "s"(v2), "{v[64:95]}"(W.w0), "{v[96:127]}"(W.w1), "{v[128:159]}"(W.w2), "{v[160:191]}"(W.w3), "{v[192:223]}"(W.w4), "{v[224:255]}"(W.w5)
    : "v4", "v5", "v6", "v7", "v8", "v9", "v10", "v11", "v12", "v13", "v14", "v15", "v16", "v17", "v18", "v19", "v20", "v21", "v22", "v23", "v24", "v25", "v26", "v27", "v28", "v29", "v30", "v31", "v32", "v33", "v34", "v35", "v36", "v37", "v38", "v39", "v40", "v41", "v42", "v43", "v44", "v45", "v46", "v47", "v48", "v49", "v50", "v51", "v52", "v53", "v54", "v55", "v56", "v57", "v58", "v59", "v60", "v61", "v62", "v63", "vcc", "scc", "memory");
}
template <> __device__ __forceinline__ void qk_step<4>(const QKW& W, const char* ag, const char* vs, const char* v2, const char* od, unsigned pb, unsigned sn, unsigned& cap, unsigned& sp,
    unsigned long long& tc, unsigned long long* ts, unsigned& d0, unsigned& d1) {
  unsigned st, bg;
  asm volatile("v_mbcnt_lo_u32_b32 v60, -1, 0\n\t"
    "v_mbcnt_hi_u32_b32 v60, -1, v60\n\t"
    "v_lshlrev_b32 v6, 4, v60\n\t"
    "s_mov_b32 %[sp], 0\n\t"
    "s_setprio 3\n\t"
    "global_load_dwordx4 v[24:27], v6, %[ag] sc1\n\t"
    "global_load_dwordx4 v[28:31], v6, %[ag] offset:-4096 sc1\n\t"
    "s_memtime %[tc]\n\t"
    "v_lshrrev_b32 v56, 4, v60\n\t"
    "v_and_b32 v57, 15, v60\n\t"
    "v_mov_b32 v20, 0\n\t"
    "v_mov_b32 v21, 0\n\t"
    "v_mov_b32 v22, 0\n\t"
    "v_mov_b32 v23, 0\n\t"
    "v_mov_b32 v11, %[sn]\n\t"
    "v_mov_b32 v10, -1\n\t"
    "v_and_b32 v61, 2, v60\n\t"
    "v_mul_u32_u24 v61, 0x5555, v61\n\t"
    "v_or_b32 v8, %[c44], v61\n\t"
    "v_lshlrev_b32 v61, 2, v56\n\t"
    "v_lshl_or_b32 v9, v57, 4, v61\n\t"
    "3:\n\t"
    "v_mov_b32 v12, 0\n\t"
    "v_mov_b32 v13, 0\n\t"
    "v_mov_b32 v14, 0\n\t"
    "v_mov_b32 v15, 0\n\t"
    "v_mov_b32 v16, 0\n\t"
    "v_mov_b32 v17, 0\n\t"
    "v_mov_b32 v18, 0\n\t"
    "v_mov_b32 v19, 0\n\t"
    "s_waitcnt vmcnt(0) lgkmcnt(0)\n\t"
    "ds_write_b128 v6, v[24:27] offset:12288\n\t"
    "ds_write_b128 v6, v[28:31] offset:8192\n\t"
    "v_smfmac_f32_16x16x64_bf16 v[12:15], v[24:27], v[64:71], v8\n\t"
    "v_max3_u32 v60, v24, v25, v26\n\t"
    "v_max3_u32 v61, v27, v28, v29\n\t"
    "v_smfmac_f32_16x16x64_bf16 v[16:19], v[24:27], v[72:79], v8\n\t"
    "v_max3_u32 v60, v60, v30, v31\n\t"
    "v_max_u32 v60, v60, v61\n\t"
    "v_cmp_eq_u32 vcc, -1, v60\n\t"
    "s_cbranch_vccnz 1f\n\t"
    "2:\n\t"
    "s_waitcnt lgkmcnt(0)\n\t"
    "s_barrier\n\t"
    "ds_read_b128 v[32:35], v6 offset:13312\n\t"
    "ds_read_b128 v[36:39], v6 offset:14336\n\t"
    "ds_read_b128 v[40:43], v6 offset:15360\n\t"
    "ds_write_b32 v9, v10 offset:18432\n\t"
    "s_waitcnt lgkmcnt(3)\n\t"
    "v_smfmac_f32_16x16x64_bf16 v[12:15], v[32:35], v[80:87], v8\n\t"
    "v_smfmac_f32_16x16x64_bf16 v[16:19], v[32:35], v[88:95], v8\n\t"
    "s_waitcnt lgkmcnt(2)\n\t"
    "v_smfmac_f32_16x16x64_bf16 v[12:15], v[36:39], v[96:103], v8\n\t"
    "v_smfmac_f32_16x16x64_bf16 v[16:19], v[36:39], v[104:111], v8\n\t"
    "s_waitcnt lgkmcnt(1)\n\t"
    "v_smfmac_f32_16x16x64_bf16 v[12:15], v[40:43], v[112:119], v8\n\t"
    "v_smfmac_f32_16x16x64_bf16 v[16:19], v[40:43], v[120:127], v8\n\t"
    "s_nop 6\n\t"
    "v_add_f32 v12, v12, v14\n\t"
    "v_add_f32 v13, v13, v15\n\t"
    "v_add_f32 v14, v16, v18\n\t"
    "v_add_f32 v15, v17, v19\n\t"
    "ds_write_b128 v6, v[12:15] offset:24576\n\t"
    "ds_write_b32 v9, v11 offset:28672\n\t"
    "s_setprio 0\n\t"
    "s_memrealtime s[36:37]\n\t"
    "s_waitcnt lgkmcnt(0)\n\t"
    "s_mov_b32 s38, s36\n\t"
    "s_min_u32 s37, %[d0], %[d1]\n\t"
    "s_add_u32 s36, s36, s37\n\t"
    "s_and_b32 s36, s36, 0xffffff\n\t"
    "s_or_b32 s36, s36, %[sn]\n\t"
    "v_lshlrev_b32 v10, 10, v6\n\t"
    "v_mov_b32 v5, s36\n\t"
    "s_mov_b64 exec, 15\n\t"
    "global_store_dword v10, v5, %[vs] offset:-4096 sc1\n\t"
    "s_mov_b64 exec, -1\n\t"
    "ds_read_b128 v[44:47], v6 offset:0\n\t"
    "ds_read_b128 v[48:51], v6 offset:1024\n\t"
    "ds_read_b128 v[52:55], v6 offset:2048\n\t"
    "ds_read_b128 v[56:59], v6 offset:3072\n\t"
    "ds_read_b128 v[60:63], v6 offset:4096\n\t"
    "s_sleep 2\n\t"
    "v_smfmac_f32_16x16x64_bf16 v[20:23], v[28:31], v[192:199], v8\n\t"
    "ds_read_b128 v[28:31], v6 offset:5120\n\t"
    "v_smfmac_f32_16x16x64_bf16 v[20:23], v[24:27], v[224:231], v8\n\t"
    "ds_read_b128 v[24:27], v6 offset:6144\n\t"
    "v_smfmac_f32_16x16x64_bf16 v[20:23], v[32:35], v[240:247], v8\n\t"
    "ds_read_b128 v[32:35], v6 offset:7168\n\t"
    "v_smfmac_f32_16x16x64_bf16 v[20:23], v[36:39], v[232:239], v8\n\t"
    "ds_read_b128 v[36:39], v6 offset:9216\n\t"
    "v_smfmac_f32_16x16x64_bf16 v[20:23], v[40:43], v[248:255], v8\n\t"
    "ds_read_b128 v[40:43], v6 offset:10240\n\t"
    "s_waitcnt lgkmcnt(9)\n\t"
    "v_smfmac_f32_16x16x64_bf16 v[20:23], v[44:47], v[128:135], v8\n\t"
    "ds_read_b128 v[44:47], v6 offset:11264\n\t"
    "s_barrier\n\t"
    "s_setprio 0\n\t"
    "s_waitcnt lgkmcnt(9)\n\t"
    "v_smfmac_f32_16x16x64_bf16 v[20:23], v[48:51], v[144:151], v8\n\t"
    "s_waitcnt lgkmcnt(8)\n\t"
    "v_smfmac_f32_16x16x64_bf16 v[20:23], v[52:55], v[136:143], v8\n\t"
    "s_waitcnt lgkmcnt(7)\n\t"
    "v_smfmac_f32_16x16x64_bf16 v[20:23], v[56:59], v[152:159], v8\n\t"
    "s_waitcnt lgkmcnt(6)\n\t"
    "v_smfmac_f32_16x16x64_bf16 v[20:23], v[60:63], v[160:167], v8\n\t"
    "s_waitcnt lgkmcnt(5)\n\t"
    "v_smfmac_f32_16x16x64_bf16 v[20:23], v[28:31], v[176:183], v8\n\t"
    "s_waitcnt lgkmcnt(4)\n\t"
    "v_smfmac_f32_16x16x64_bf16 v[20:23], v[24:27], v[168:175], v8\n\t"
    "s_waitcnt lgkmcnt(3)\n\t"
    "v_smfmac_f32_16x16x64_bf16 v[20:23], v[32:35], v[184:191], v8\n\t"
    "s_waitcnt lgkmcnt(2)\n\t"
    "v_smfmac_f32_16x16x64_bf16 v[20:23], v[36:39], v[208:215], v8\n\t"
    "s_waitcnt lgkmcnt(1)\n\t"
    "v_smfmac_f32_16x16x64_bf16 v[20:23], v[40:43], v[200:207], v8\n\t"
    "s_waitcnt lgkmcnt(0)\n\t"
    "v_smfmac_f32_16x16x64_bf16 v[20:23], v[44:47], v[216:223], v8\n\t"
    "s_nop 7\n\t"
    "v_add_f32 v20, v20, v22\n\t"
    "v_add_f32 v21, v21, v23\n\t"
    "v_cvt_pk_bf16_f32 v20, v20, v21\n\t"
    "v_min_u32 v20, -2, v20\n\t"
    "ds_write_b32 v9, v20 offset:18432\n\t"
    "s_waitcnt lgkmcnt(0)\n\t"
    "s_setprio 3\n\t"
    "s_barrier\n\t"
    "s_memrealtime s[36:37]\n\t"
    "s_mov_b32 %[d1], %[d0]\n\t"
    "s_waitcnt lgkmcnt(0)\n\t"
    "s_sub_u32 %[d0], s36, s38\n\t"
    "s_min_u32 %[d0], %[d0], 200\n\t"
    "s_and_b32 s36, s36, 0xffffff\n\t"
    "s_or_b32 s36, s36, %[sn]\n\t"
    "v_lshlrev_b32 v10, 10, v6\n\t"
    "v_mov_b32 v5, s36\n\t"
    "s_mov_b64 exec, 15\n\t"
    "global_store_dword v10, v5, %[vs] offset:0 sc1\n\t"
    "s_mov_b64 exec, -1\n\t"
    "s_waitcnt lgkmcnt(0)\n\t"
    "s_branch 9f\n\t"
    "1:\n\t"
    "s_add_u32 %[sp], %[sp], 1\n\t"
    "s_cmp_ge_u32 %[sp], %[cap]\n\t"
    "s_cbranch_scc1 7f\n\t"
    "global_load_dwordx4 v[24:27], v6, %[ag] sc1\n\t"
    "global_load_dwordx4 v[28:31], v6, %[ag] offset:-4096 sc1\n\t"
    "s_waitcnt lgkmcnt(0)\n\t"
    "s_memtime %[tc]\n\t"
    "s_nop 7\n\t"
    "s_branch 3b\n\t"
    "7:\n\t"
    "s_mov_b32 %[cap], 0\n\t"
    "s_branch 2b\n\t"
    "9:"
    : [tc] "=&s"(tc), [sp] "=&s"(sp), [cap] "+s"(cap), [d0] "+s"(d0), [d1] "+s"(d1)
    : [ag] "s"(ag), [c44] "s"(0x4444u), [lsc] "s"(LSC), [sn] "s"(sn), [vs] "s"(vs), "{v[64:95]}"(W.w0), "{v[96:127]}"(W.w1), "{v[128:159]}"(W.w2), "{v[160:191]}"(W.w3), "{v[192:223]}"(W.w4), "{v[224:255]}"(W.w5)
    : "v5", "v6", "v7", "v8", "v9", "v10", "v11", "v12", "v13", "v14", "v15", "v16", "v17", "v18", "v19", "v20", "v21", "v22", "v23", "v24", "v25", "v26", "v27", "v28", "v29", "v30", "v31", "v32", "v33", "v34", "v35", "v36", "v37", "v38", "v39", "v40", "v41", "v42", "v43", "v44", "v45", "v46", "v47", "v48", "v49", "v50", "v51", "v52", "v53", "v54", "v55", "v56", "v57", "v58", "v59", "v60", "v61", "v62", "v63", "vcc", "scc", "memory", "s36", "s37", "s38");
}
template <> __device__ __forceinline__ void qk_step<5>(const QKW& W, const char* ag, const char* vs, const char* v2, const char* od, unsigned pb, unsigned sn, unsigned& cap, unsigned& sp,
    unsigned long long& tc, unsigned long long* ts, unsigned& d0, unsigned& d1) {
  unsigned st, bg;
  asm volatile("v_mbcnt_lo_u32_b32 v60, -1, 0\n\t"
    "v_mbcnt_hi_u32_b32 v60, -1, v60\n\t"
    "v_lshlrev_b32 v6, 4, v60\n\t"
    "s_mov_b32 %[sp], 0\n\t"
    "s_setprio 3\n\t"
    "global_load_dwordx4 v[24:27], v6, %[ag] sc1\n\t"
    "global_load_dwordx4 v[28:31], v6, %[ag] offset:-4096 sc1\n\t"
    "s_memtime %[tc]\n\t"
    "v_lshrrev_b32 v56, 4, v60\n\t"
    "v_and_b32 v57, 15, v60\n\t"
    "v_mov_b32 v20, 0\n\t"
    "v_mov_b32 v21, 0\n\t"
    "v_mov_b32 v22, 0\n\t"
    "v_mov_b32 v23, 0\n\t"
    "v_mov_b32 v11, %[sn]\n\t"
    "v_mov_b32 v10, -1\n\t"
    "v_and_b32 v61, 2, v60\n\t"
    "v_mul_u32_u24 v61, 0x5555, v61\n\t"
    "v_or_b32 v8, %[c44], v61\n\t"
    "v_lshlrev_b32 v61, 2, v56\n\t"
    "v_lshl_or_b32 v9, v57, 4, v61\n\t"
    "3:\n\t"
    "v_mov_b32 v12, 0\n\t"
    "v_mov_b32 v13, 0\n\t"
    "v_mov_b32 v14, 0\n\t"
    "v_mov_b32 v15, 0\n\t"
    "v_mov_b32 v16, 0\n\t"
    "v_mov_b32 v17, 0\n\t"
    "v_mov_b32 v18, 0\n\t"
    "v_mov_b32 v19, 0\n\t"
    "s_waitcnt vmcnt(0) lgkmcnt(0)\n\t"
    "ds_write_b128 v6, v[24:27] offset:13312\n\t"
    "ds_write_b128 v6, v[28:31] offset:9216\n\t"
    "v_smfmac_f32_16x16x64_bf16 v[12:15], v[24:27], v[80:87], v8\n\t"
    "v_max3_u32 v60, v24, v25, v26\n\t"
    "v_max3_u32 v61, v27, v28, v29\n\t"
    "v_smfmac_f32_16x16x64_bf16 v[16:19], v[24:27], v[88:95], v8\n\t"
    "v_max3_u32 v60, v60, v30, v31\n\t"
    "v_max_u32 v60, v60, v61\n\t"
    "v_cmp_eq_u32 vcc, -1, v60\n\t"
    "s_cbranch_vccnz 1f\n\t"
    "2:\n\t"
    "s_waitcnt lgkmcnt(0)\n\t"
    "s_barrier\n\t"
    "ds_read_b128 v[32:35], v6 offset:12288\n\t"
    "ds_read_b128 v[36:39], v6 offset:14336\n\t"
    "ds_read_b128 v[40:43], v6 offset:15360\n\t"
    "ds_write_b32 v9, v10 offset:18688\n\t"
    "s_waitcnt lgkmcnt(3)\n\t"
    "v_smfmac_f32_16x16x64_bf16 v[12:15], v[32:35], v[64:71], v8\n\t"
    "v_smfmac_f32_16x16x64_bf16 v[16:19], v[32:35], v[72:79], v8\n\t"
    "s_waitcnt lgkmcnt(2)\n\t"
    "v_smfmac_f32_16x16x64_bf16 v[12:15], v[36:39], v[96:103], v8\n\t"
    "v_smfmac_f32_16x16x64_bf16 v[16:19], v[36:39], v[104:111], v8\n\t"
    "s_waitcnt lgkmcnt(1)\n\t"
    "v_smfmac_f32_16x16x64_bf16 v[12:15], v[40:43], v[112:119], v8\n\t"
    "v_smfmac_f32_16x16x64_bf16 v[16:19], v[40:43], v[120:127], v8\n\t"
    "s_nop 6\n\t"
    "v_add_f32 v12, v12, v14\n\t"
    "v_add_f32 v13, v13, v15\n\t"
    "v_add_f32 v14, v16, v18\n\t"
    "v_add_f32 v15, v17, v19\n\t"
    "ds_write_b128 v6, v[12:15] offset:25600\n\t"
    "ds_write_b32 v9, v11 offset:28928\n\t"
    "s_setprio 0\n\t"
    "ds_read_b128 v[44:47], v6 offset:0\n\t"
    "ds_read_b128 v[48:51], v6 offset:1024\n\t"
    "ds_read_b128 v[52:55], v6 offset:2048\n\t"
    "ds_read_b128 v[56:59], v6 offset:3072\n\t"
    "ds_read_b128 v[60:63], v6 offset:4096\n\t"
    "s_sleep 2\n\t"
    "v_smfmac_f32_16x16x64_bf16 v[20:23], v[28:31], v[208:215], v8\n\t"
    "ds_read_b128 v[28:31], v6 offset:5120\n\t"
    "v_smfmac_f32_16x16x64_bf16 v[20:23], v[24:27], v[240:247], v8\n\t"
    "ds_read_b128 v[24:27], v6 offset:6144\n\t"
    "v_smfmac_f32_16x16x64_bf16 v[20:23], v[32:35], v[224:231], v8\n\t"
    "ds_read_b128 v[32:35], v6 offset:7168\n\t"
    "v_smfmac_f32_16x16x64_bf16 v[20:23], v[36:39], v[232:239], v8\n\t"
    "ds_read_b128 v[36:39], v6 offset:8192\n\t"
    "v_smfmac_f32_16x16x64_bf16 v[20:23], v[40:43], v[248:255], v8\n\t"
    "ds_read_b128 v[40:43], v6 offset:10240\n\t"
    "s_waitcnt lgkmcnt(9)\n\t"
    "v_smfmac_f32_16x16x64_bf16 v[20:23], v[44:47], v[128:135], v8\n\t"
    "ds_read_b128 v[44:47], v6 offset:11264\n\t"
    "s_barrier\n\t"
    "s_setprio 0\n\t"
    "s_waitcnt lgkmcnt(9)\n\t"
    "v_smfmac_f32_16x16x64_bf16 v[20:23], v[48:51], v[144:151], v8\n\t"
    "s_waitcnt lgkmcnt(8)\n\t"
    "v_smfmac_f32_16x16x64_bf16 v[20:23], v[52:55], v[136:143], v8\n\t"
    "s_waitcnt lgkmcnt(7)\n\t"
    "v_smfmac_f32_16x16x64_bf16 v[20:23], v[56:59], v[152:159], v8\n\t"
    "s_waitcnt lgkmcnt(6)\n\t"
    "v_smfmac_f32_16x16x64_bf16 v[20:23], v[60:63], v[160:167], v8\n\t"
    "s_waitcnt lgkmcnt(5)\n\t"
    "v_smfmac_f32_16x16x64_bf16 v[20:23], v[28:31], v[176:183], v8\n\t"
    "s_waitcnt lgkmcnt(4)\n\t"
    "v_smfmac_f32_16x16x64_bf16 v[20:23], v[24:27], v[168:175], v8\n\t"
    "s_waitcnt lgkmcnt(3)\n\t"
    "v_smfmac_f32_16x16x64_bf16 v[20:23], v[32:35], v[184:191], v8\n\t"
    "s_waitcnt lgkmcnt(2)\n\t"
    "v_smfmac_f32_16x16x64_bf16 v[20:23], v[36:39], v[192:199], v8\n\t"
    "s_waitcnt lgkmcnt(1)\n\t"
    "v_smfmac_f32_16x16x64_bf16 v[20:23], v[40:43], v[200:207], v8\n\t"
    "s_waitcnt lgkmcnt(0)\n\t"
    "v_smfmac_f32_16x16x64_bf16 v[20:23], v[44:47], v[216:223], v8\n\t"
    "s_nop 7\n\t"
    "v_add_f32 v20, v20, v22\n\t"
    "v_add_f32 v21, v21, v23\n\t"
    "v_cvt_pk_bf16_f32 v20, v20, v21\n\t"
    "v_min_u32 v20, -2, v20\n\t"
    "ds_write_b32 v9, v20 offset:18688\n\t"
    "s_waitcnt lgkmcnt(0)\n\t"
    "s_setprio 0\n\t"
    "s_waitcnt lgkmcnt(0)\n\t"
    "s_branch 9f\n\t"
    "1:\n\t"
    "s_add_u32 %[sp], %[sp], 1\n\t"
    "s_cmp_ge_u32 %[sp], %[cap]\n\t"
    "s_cbranch_scc1 7f\n\t"
    "global_load_dwordx4 v[24:27], v6, %[ag] sc1\n\t"
    "global_load_dwordx4 v[28:31], v6, %[ag] offset:-4096 sc1\n\t"
    "s_waitcnt lgkmcnt(0)\n\t"
    "s_memtime %[tc]\n\t"
    "s_nop 7\n\t"
    "s_branch 3b\n\t"
    "7:\n\t"
    "s_mov_b32 %[cap], 0\n\t"
    "s_branch 2b\n\t"
    "9:"
    : [tc] "=&s"(tc), [sp] "=&s"(sp), [cap] "+s"(cap)
    : [ag] "s"(ag), [c44] "s"(0x4444u), [lsc] "s"(LSC), [sn] "s"(sn), "{v[64:95]}"(W.w0), "{v[96:127]}"(W.w1), "{v[128:159]}"(W.w2), "{v[160:191]}"(W.w3), "{v[192:223]}"(W.w4), "{v[224:255]}"(W.w5)
    : "v6", "v7", "v8", "v9", "v10", "v11", "v12", "v13", "v14", "v15", "v16", "v17", "v18", "v19", "v20", "v21", "v22", "v23", "v24", "v25", "v26", "v27", "v28", "v29", "v30", "v31", "v32", "v33", "v34", "v35", "v36", "v37", "v38", "v39", "v40", "v41", "v42", "v43", "v44", "v45", "v46", "v47", "v48", "v49", "v50", "v51", "v52", "v53", "v54", "v55", "v56", "v57", "v58", "v59", "v60", "v61", "v62", "v63", "vcc", "scc", "memory");
}
template <> __device__ __forceinline__ void qk_step<6>(const QKW& W, const char* ag, const char* vs, const char* v2, const char* od, unsigned pb, unsigned sn, unsigned& cap, unsigned& sp,
    unsigned long long& tc, unsigned long long* ts, unsigned& d0, unsigned& d1) {
  unsigned st, bg;
  asm volatile("v_mbcnt_lo_u32_b32 v60, -1, 0\n\t"
    "v_mbcnt_hi_u32_b32 v60, -1, v60\n\t"
    "v_lshlrev_b32 v6, 4, v60\n\t"
    "s_mov_b32 %[sp], 0\n\t"
    "s_setprio 3\n\t"
    "global_load_dwordx4 v[24:27], v6, %[ag] sc1\n\t"
    "global_load_dwordx4 v[28:31], v6, %[ag] offset:-4096 sc1\n\t"
    "s_memtime %[tc]\n\t"
    "v_lshrrev_b32 v56, 4, v60\n\t"
    "v_and_b32 v57, 15, v60\n\t"
    "v_mov_b32 v20, 0\n\t"
    "v_mov_b32 v21, 0\n\t"
    "v_mov_b32 v22, 0\n\t"
    "v_mov_b32 v23, 0\n\t"
    "v_mov_b32 v11, %[sn]\n\t"
    "v_mov_b32 v10, -1\n\t"
    "v_and_b32 v61, 2, v60\n\t"
    "v_mul_u32_u24 v61, 0x5555, v61\n\t"
    "v_or_b32 v8, %[c44], v61\n\t"
    "v_lshlrev_b32 v61, 2, v56\n\t"
    "v_lshl_or_b32 v9, v57, 4, v61\n\t"
    "3:\n\t"
    "v_mov_b32 v12, 0\n\t"
    "v_mov_b32 v13, 0\n\t"
    "v_mov_b32 v14, 0\n\t"
    "v_mov_b32 v15, 0\n\t"
    "v_mov_b32 v16, 0\n\t"
    "v_mov_b32 v17, 0\n\t"
    "v_mov_b32 v18, 0\n\t"
    "v_mov_b32 v19, 0\n\t"
    "s_waitcnt vmcnt(0) lgkmcnt(0)\n\t"
    "ds_write_b128 v6, v[24:27] offset:14336\n\t"
    "ds_write_b128 v6, v[28:31] offset:10240\n\t"
    "v_smfmac_f32_16x16x64_bf16 v[12:15], v[24:27], v[96:103], v8\n\t"
    "v_max3_u32 v60, v24, v25, v26\n\t"
    "v_max3_u32 v61, v27, v28, v29\n\t"
    "v_smfmac_f32_16x16x64_bf16 v[16:19], v[24:27], v[104:111], v8\n\t"
    "v_max3_u32 v60, v60, v30, v31\n\t"
    "v_max_u32 v60, v60, v61\n\t"
    "v_cmp_eq_u32 vcc, -1, v60\n\t"
    "s_cbranch_vccnz 1f\n\t"
    "2:\n\t"
    "s_waitcnt lgkmcnt(0)\n\t"
    "s_barrier\n\t"
    "ds_read_b128 v[32:35], v6 offset:12288\n\t"
    "ds_read_b128 v[36:39], v6 offset:13312\n\t"
    "ds_read_b128 v[40:43], v6 offset:15360\n\t"
    "ds_write_b32 v9, v10 offset:18944\n\t"
    "s_waitcnt lgkmcnt(3)\n\t"
    "v_smfmac_f32_16x16x64_bf16 v[12:15], v[32:35], v[64:71], v8\n\t"
    "v_smfmac_f32_16x16x64_bf16 v[16:19], v[32:35], v[72:79], v8\n\t"
    "s_waitcnt lgkmcnt(2)\n\t"
    "v_smfmac_f32_16x16x64_bf16 v[12:15], v[36:39], v[80:87], v8\n\t"
    "v_smfmac_f32_16x16x64_bf16 v[16:19], v[36:39], v[88:95], v8\n\t"
    "s_waitcnt lgkmcnt(1)\n\t"
    "v_smfmac_f32_16x16x64_bf16 v[12:15], v[40:43], v[112:119], v8\n\t"
    "v_smfmac_f32_16x16x64_bf16 v[16:19], v[40:43], v[120:127], v8\n\t"
    "s_nop 6\n\t"
    "v_add_f32 v12, v12, v14\n\t"
    "v_add_f32 v13, v13, v15\n\t"
    "v_add_f32 v14, v16, v18\n\t"
    "v_add_f32 v15, v17, v19\n\t"
    "ds_write_b128 v6, v[12:15] offset:26624\n\t"
    "ds_write_b32 v9, v11 offset:29184\n\t"
    "s_setprio 0\n\t"
    "ds_read_b128 v[44:47], v6 offset:0\n\t"
    "ds_read_b128 v[48:51], v6 offset:1024\n\t"
    "ds_read_b128 v[52:55], v6 offset:2048\n\t"
    "ds_read_b128 v[56:59], v6 offset:3072\n\t"
    "ds_read_b128 v[60:63], v6 offset:4096\n\t"
    "s_sleep 2\n\t"
    "v_smfmac_f32_16x16x64_bf16 v[20:23], v[28:31], v[200:207], v8\n\t"
    "ds_read_b128 v[28:31], v6 offset:5120\n\t"
    "v_smfmac_f32_16x16x64_bf16 v[20:23], v[24:27], v[232:239], v8\n\t"
    "ds_read_b128 v[24:27], v6 offset:6144\n\t"
    "v_smfmac_f32_16x16x64_bf16 v[20:23], v[32:35], v[224:231], v8\n\t"
    "ds_read_b128 v[32:35], v6 offset:7168\n\t"
    "v_smfmac_f32_16x16x64_bf16 v[20:23], v[36:39], v[240:247], v8\n\t"
    "ds_read_b128 v[36:39], v6 offset:8192\n\t"
    "v_smfmac_f32_16x16x64_bf16 v[20:23], v[40:43], v[248:255], v8\n\t"
    "ds_read_b128 v[40:43], v6 offset:9216\n\t"
    "s_waitcnt lgkmcnt(9)\n\t"
    "v_smfmac_f32_16x16x64_bf16 v[20:23], v[44:47], v[128:135], v8\n\t"
    "ds_read_b128 v[44:47], v6 offset:11264\n\t"
    "s_barrier\n\t"
    "s_setprio 0\n\t"
    "s_waitcnt lgkmcnt(9)\n\t"
    "v_smfmac_f32_16x16x64_bf16 v[20:23], v[48:51], v[144:151], v8\n\t"
    "s_waitcnt lgkmcnt(8)\n\t"
    "v_smfmac_f32_16x16x64_bf16 v[20:23], v[52:55], v[136:143], v8\n\t"
    "s_waitcnt lgkmcnt(7)\n\t"
    "v_smfmac_f32_16x16x64_bf16 v[20:23], v[56:59], v[152:159], v8\n\t"
    "s_waitcnt lgkmcnt(6)\n\t"
    "v_smfmac_f32_16x16x64_bf16 v[20:23], v[60:63], v[160:167], v8\n\t"
    "s_waitcnt lgkmcnt(5)\n\t"
    "v_smfmac_f32_16x16x64_bf16 v[20:23], v[28:31], v[176:183], v8\n\t"
    "s_waitcnt lgkmcnt(4)\n\t"
    "v_smfmac_f32_16x16x64_bf16 v[20:23], v[24:27], v[168:175], v8\n\t"
    "s_waitcnt lgkmcnt(3)\n\t"
    "v_smfmac_f32_16x16x64_bf16 v[20:23], v[32:35], v[184:191], v8\n\t"
    "s_waitcnt lgkmcnt(2)\n\t"
    "v_smfmac_f32_16x16x64_bf16 v[20:23], v[36:39], v[192:199], v8\n\t"
    "s_waitcnt lgkmcnt(1)\n\t"
    "v_smfmac_f32_16x16x64_bf16 v[20:23], v[40:43], v[208:215], v8\n\t"
    "s_waitcnt lgkmcnt(0)\n\t"
    "v_smfmac_f32_16x16x64_bf16 v[20:23], v[44:47], v[216:223], v8\n\t"
    "s_nop 7\n\t"
    "v_add_f32 v20, v20, v22\n\t"
    "v_add_f32 v21, v21, v23\n\t"
    "v_cvt_pk_bf16_f32 v20, v20, v21\n\t"
    "v_min_u32 v20, -2, v20\n\t"
    "ds_write_b32 v9, v20 offset:18944\n\t"
    "s_waitcnt lgkmcnt(0)\n\t"
    "s_setprio 0\n\t"
    "s_waitcnt lgkmcnt(0)\n\t"
    "s_branch 9f\n\t"
    "1:\n\t"
    "s_add_u32 %[sp], %[sp], 1\n\t"
    "s_cmp_ge_u32 %[sp], %[cap]\n\t"
    "s_cbranch_scc1 7f\n\t"
    "global_load_dwordx4 v[24:27], v6, %[ag] sc1\n\t"
    "global_load_dwordx4 v[28:31], v6, %[ag] offset:-4096 sc1\n\t"
    "s_waitcnt lgkmcnt(0)\n\t"
    "s_memtime %[tc]\n\t"
    "s_nop 7\n\t"
    "s_branch 3b\n\t"
    "7:\n\t"
    "s_mov_b32 %[cap], 0\n\t"
    "s_branch 2b\n\t"
    "9:"
    : [tc] "=&s"(tc), [sp] "=&s"(sp), [cap] "+s"(cap)
    : [ag] "s"(ag), [c44] "s"(0x4444u), [lsc] "s"(LSC), [sn] "s"(sn), "{v[64:95]}"(W.w0), "{v[96:127]}"(W.w1), "{v[128:159]}"(W.w2), "{v[160:191]}"(W.w3), "{v[192:223]}"(W.w4), "{v[224:255]}"(W.w5)
    : "v6", "v7", "v8", "v9", "v10", "v11", "v12", "v13", "v14", "v15", "v16", "v17", "v18", "v19", "v20", "v21", "v22", "v23", "v24", "v25", "v26", "v27", "v28", "v29", "v30", "v31", "v32", "v33", "v34", "v35", "v36", "v37", "v38", "v39", "v40", "v41", "v42", "v43", "v44", "v45", "v46", "v47", "v48", "v49", "v50", "v51", "v52", "v53", "v54", "v55", "v56", "v57", "v58", "v59", "v60", "v61", "v62", "v63", "vcc", "scc", "memory");
}
template <> __device__ __forceinline__ void qk_step<7>(const QKW& W, const char* ag, const char* vs, const char* v2, const char* od, unsigned pb, unsigned sn, unsigned& cap, unsigned& sp,
    unsigned long long& tc, unsigned long long* ts, unsigned& d0, unsigned& d1) {
  unsigned st, bg;
  asm volatile("v_mbcnt_lo_u32_b32 v60, -1, 0\n\t"
    "v_mbcnt_hi_u32_b32 v60, -1, v60\n\t"
    "v_lshlrev_b32 v6, 4, v60\n\t"
    "s_mov_b32 %[sp], 0\n\t"
    "s_setprio 3\n\t"
    "global_load_dwordx4 v[24:27], v6, %[ag] sc1\n\t"
    "global_load_dwordx4 v[28:31], v6, %[ag] offset:-4096 sc1\n\t"
    "s_memtime %[tc]\n\t"
    "v_lshrrev_b32 v56, 4, v60\n\t"
    "v_and_b32 v57, 15, v60\n\t"
    "v_mov_b32 v20, 0\n\t"
    "v_mov_b32 v21, 0\n\t"
    "v_mov_b32 v22, 0\n\t"
    "v_mov_b32 v23, 0\n\t"
    "v_mov_b32 v11, %[sn]\n\t"
    "v_mov_b32 v10, -1\n\t"
    "v_and_b32 v61, 2, v60\n\t"
    "v_mul_u32_u24 v61, 0x5555, v61\n\t"
    "v_or_b32 v8, %[c44], v61\n\t"
    "v_lshlrev_b32 v61, 2, v56\n\t"
    "v_lshl_or_b32 v9, v57, 4, v61\n\t"
    "3:\n\t"
    "v_mov_b32 v12, 0\n\t"
    "v_mov_b32 v13, 0\n\t"
    "v_mov_b32 v14, 0\n\t"
    "v_mov_b32 v15, 0\n\t"
    "v_mov_b32 v16, 0\n\t"
    "v_mov_b32 v17, 0\n\t"
    "v_mov_b32 v18, 0\n\t"
    "v_mov_b32 v19, 0\n\t"
    "s_waitcnt vmcnt(0) lgkmcnt(0)\n\t"
    "ds_write_b128 v6, v[24:27] offset:15360\n\t"
    "ds_write_b128 v6, v[28:31] offset:11264\n\t"
    "v_smfmac_f32_16x16x64_bf16 v[12:15], v[24:27], v[112:119], v8\n\t"
    "v_max3_u32 v60, v24, v25, v26\n\t"
    "v_max3_u32 v61, v27, v28, v29\n\t"
    "v_smfmac_f32_16x16x64_bf16 v[16:19], v[24:27], v[120:127], v8\n\t"
    "v_max3_u32 v60, v60, v30, v31\n\t"
    "v_max_u32 v60, v60, v61\n\t"
    "v_cmp_eq_u32 vcc, -1, v60\n\t"
    "s_cbranch_vccnz 1f\n\t"
    "2:\n\t"
    "s_waitcnt lgkmcnt(0)\n\t"
    "s_barrier\n\t"
    "ds_read_b128 v[32:35], v6 offset:12288\n\t"
    "ds_read_b128 v[36:39], v6 offset:13312\n\t"
    "ds_read_b128 v[40:43], v6 offset:14336\n\t"
    "ds_write_b32 v9, v10 offset:19200\n\t"
    "s_waitcnt lgkmcnt(3)\n\t"
    "v_smfmac_f32_16x16x64_bf16 v[12:15], v[32:35], v[64:71], v8\n\t"
    "v_smfmac_f32_16x16x64_bf16 v[16:19], v[32:35], v[72:79], v8\n\t"
    "s_waitcnt lgkmcnt(2)\n\t"
    "v_smfmac_f32_16x16x64_bf16 v[12:15], v[36:39], v[80:87], v8\n\t"
    "v_smfmac_f32_16x16x64_bf16 v[16:19], v[36:39], v[88:95], v8\n\t"
    "s_waitcnt lgkmcnt(1)\n\t"
    "v_smfmac_f32_16x16x64_bf16 v[12:15], v[40:43], v[96:103], v8\n\t"
    "v_smfmac_f32_16x16x64_bf16 v[16:19], v[40:43], v[104:111], v8\n\t"
    "s_nop 6\n\t"
    "v_add_f32 v12, v12, v14\n\t"
    "v_add_f32 v13, v13, v15\n\t"
    "v_add_f32 v14, v16, v18\n\t"
    "v_add_f32 v15, v17, v19\n\t"
    "ds_write_b128 v6, v[12:15] offset:27648\n\t"
    "ds_write_b32 v9, v11 offset:29440\n\t"
    "s_setprio 0\n\t"
    "v_lshrrev_b32 v10, 24, v11\n\t"
    "v_add_u32 v10, -1, v10\n\t"
    "s_mov_b64 exec, 1\n\t"
    "global_store_dword v6, v10, %[vs] sc1\n\t"
    "s_mov_b64 exec, -1\n\t"
    "ds_read_b128 v[44:47], v6 offset:0\n\t"
    "ds_read_b128 v[48:51], v6 offset:1024\n\t"
    "ds_read_b128 v[52:55], v6 offset:2048\n\t"
    "ds_read_b128 v[56:59], v6 offset:3072\n\t"
    "ds_read_b128 v[60:63], v6 offset:4096\n\t"
    "s_sleep 2\n\t"
    "v_smfmac_f32_16x16x64_bf16 v[20:23], v[28:31], v[216:223], v8\n\t"
    "ds_read_b128 v[28:31], v6 offset:5120\n\t"
    "v_smfmac_f32_16x16x64_bf16 v[20:23], v[24:27], v[248:255], v8\n\t"
    "ds_read_b128 v[24:27], v6 offset:6144\n\t"
    "v_smfmac_f32_16x16x64_bf16 v[20:23], v[32:35], v[224:231], v8\n\t"
    "ds_read_b128 v[32:35], v6 offset:7168\n\t"
    "v_smfmac_f32_16x16x64_bf16 v[20:23], v[36:39], v[240:247], v8\n\t"
    "ds_read_b128 v[36:39], v6 offset:8192\n\t"
    "v_smfmac_f32_16x16x64_bf16 v[20:23], v[40:43], v[232:239], v8\n\t"
    "ds_read_b128 v[40:43], v6 offset:9216\n\t"
    "s_waitcnt lgkmcnt(9)\n\t"
    "v_smfmac_f32_16x16x64_bf16 v[20:23], v[44:47], v[128:135], v8\n\t"
    "ds_read_b128 v[44:47], v6 offset:10240\n\t"
    "s_barrier\n\t"
    "s_setprio 0\n\t"
    "s_waitcnt lgkmcnt(9)\n\t"
    "v_smfmac_f32_16x16x64_bf16 v[20:23], v[48:51], v[144:151], v8\n\t"
    "s_waitcnt lgkmcnt(8)\n\t"
    "v_smfmac_f32_16x16x64_bf16 v[20:23], v[52:55], v[136:143], v8\n\t"
    "s_waitcnt lgkmcnt(7)\n\t"
    "v_smfmac_f32_16x16x64_bf16 v[20:23], v[56:59], v[152:159], v8\n\t"
    "s_waitcnt lgkmcnt(6)\n\t"
    "v_smfmac_f32_16x16x64_bf16 v[20:23], v[60:63], v[160:167], v8\n\t"
    "s_waitcnt lgkmcnt(5)\n\t"
    "v_smfmac_f32_16x16x64_bf16 v[20:23], v[28:31], v[176:183], v8\n\t"
    "s_waitcnt lgkmcnt(4)\n\t"
    "v_smfmac_f32_16x16x64_bf16 v[20:23], v[24:27], v[168:175], v8\n\t"
    "s_waitcnt lgkmcnt(3)\n\t"
    "v_smfmac_f32_16x16x64_bf16 v[20:23], v[32:35], v[184:191], v8\n\t"
    "s_waitcnt lgkmcnt(2)\n\t"
    "v_smfmac_f32_16x16x64_bf16 v[20:23], v[36:39], v[192:199], v8\n\t"
    "s_waitcnt lgkmcnt(1)\n\t"
    "v_smfmac_f32_16x16x64_bf16 v[20:23], v[40:43], v[208:215], v8\n\t"
    "s_waitcnt lgkmcnt(0)\n\t"
    "v_smfmac_f32_16x16x64_bf16 v[20:23], v[44:47], v[200:207], v8\n\t"
    "s_nop 7\n\t"
    "v_add_f32 v20, v20, v22\n\t"
    "v_add_f32 v21, v21, v23\n\t"
    "v_cvt_pk_bf16_f32 v20, v20, v21\n\t"
    "v_min_u32 v20, -2, v20\n\t"
    "ds_write_b32 v9, v20 offset:19200\n\t"
    "s_waitcnt lgkmcnt(0)\n\t"
    "s_setprio 0\n\t"
    "s_waitcnt lgkmcnt(0)\n\t"
    "s_branch 9f\n\t"
    "1:\n\t"
    "s_add_u32 %[sp], %[sp], 1\n\t"
    "s_cmp_ge_u32 %[sp], %[cap]\n\t"
    "s_cbranch_scc1 7f\n\t"
    "global_load_dwordx4 v[24:27], v6, %[ag] sc1\n\t"
    "global_load_dwordx4 v[28:31], v6, %[ag] offset:-4096 sc1\n\t"
    "s_waitcnt lgkmcnt(0)\n\t"
    "s_memtime %[tc]\n\t"
    "s_nop 7\n\t"
    "s_branch 3b\n\t"
    "7:\n\t"
    "s_mov_b32 %[cap], 0\n\t"
    "s_branch 2b\n\t"
    "9:"
    : [tc] "=&s"(tc), [sp] "=&s"(sp), [cap] "+s"(cap)
    : [ag] "s"(ag), [c44] "s"(0x4444u), [lsc] "s"(LSC), [sn] "s"(sn), [vs] "s"(vs), "{v[64:95]}"(W.w0), "{v[96:127]}"(W.w1), "{v[128:159]}"(W.w2), "{v[160:191]}"(W.w3), "{v[192:223]}"(W.w4), "{v[224:255]}"(W.w5)
    : "v6", "v7", "v8", "v9", "v10", "v11", "v12", "v13", "v14", "v15", "v16", "v17", "v18", "v19", "v20", "v21", "v22", "v23", "v24", "v25", "v26", "v27", "v28", "v29", "v30", "v31", "v32", "v33", "v34", "v35", "v36", "v37", "v38", "v39", "v40", "v41", "v42", "v43", "v44", "v45", "v46", "v47", "v48", "v49", "v50", "v51", "v52", "v53", "v54", "v55", "v56", "v57", "v58", "v59", "v60", "v61", "v62", "v63", "vcc", "scc", "memory");
}
template <int W_> __device__ __forceinline__ void seg_attn(const QKW& W, unsigned pb, const char* od, unsigned long long* ts) { }
template <> __device__ __forceinline__ void seg_attn<0>(const QKW& W, unsigned pb, const char* od, unsigned long long* ts) {
  asm volatile("v_mbcnt_lo_u32_b32 v32, -1, 0\n\t"
    "v_mbcnt_hi_u32_b32 v32, -1, v32\n\t"
    "v_lshlrev_b32 v35, 4, v32\n\t"
    "ds_read_b128 v[24:27], v35 offset:20480\n\t"
    "ds_read_b128 v[28:31], v35 offset:21504\n\t"
    "v_lshrrev_b32 v33, 4, v32\n\t"
    "v_and_b32 v34, 15, v32\n\t"
    "v_xor_b32 v4, v33, v34\n\t"
    "v_lshl_or_b32 v4, v4, 4, %[pb]\n\t"
    "v_lshl_or_b32 v4, v34, 8, v4\n\t"
    "ds_read_b128 v[8:11], v4 offset:49152\n\t"
    "v_xor_b32 v5, 0x40, v4\n\t"
    "ds_read_b128 v[12:15], v5 offset:49152\n\t"
    "v_xor_b32 v6, 0x80, v4\n\t"
    "ds_read_b128 v[16:19], v6 offset:49152\n\t"
    "v_xor_b32 v7, 0xc0, v4\n\t"
    "ds_read_b128 v[20:23], v7 offset:49152\n\t"
    "ds_read_b128 v[40:43], v4 offset:32768\n\t"
    "ds_read_b128 v[44:47], v5 offset:32768\n\t"
    "ds_read_b128 v[48:51], v6 offset:32768\n\t"
    "ds_read_b128 v[52:55], v7 offset:32768\n\t"
    "v_lshlrev_b32 v35, 2, v33\n\t"
    "v_sub_u32 v35, v34, v35\n\t"
    "v_ashrrev_i32 v60, 31, v35\n\t"
    "v_and_b32 v60, %[neg], v60\n\t"
    "v_add_u32 v61, -1, v35\n\t"
    "v_ashrrev_i32 v61, 31, v61\n\t"
    "v_and_b32 v61, %[neg], v61\n\t"
    "v_add_u32 v62, -2, v35\n\t"
    "v_ashrrev_i32 v62, 31, v62\n\t"
    "v_and_b32 v62, %[neg], v62\n\t"
    "v_add_u32 v63, -3, v35\n\t"
    "v_ashrrev_i32 v63, 31, v63\n\t"
    "v_and_b32 v63, %[neg], v63\n\t"
    "s_waitcnt lgkmcnt(6)\n\t"
    "v_mfma_f32_16x16x32_bf16 v[32:35], v[24:27], v[8:11], 0\n\t"
    "v_mfma_f32_16x16x32_bf16 v[36:39], v[24:27], v[12:15], 0\n\t"
    "s_waitcnt lgkmcnt(4)\n\t"
    "v_mfma_f32_16x16x32_bf16 v[32:35], v[28:31], v[16:19], v[32:35]\n\t"
    "v_mfma_f32_16x16x32_bf16 v[36:39], v[28:31], v[20:23], v[36:39]\n\t"
    "s_waitcnt lgkmcnt(3)\n\t"
    "v_mfma_f32_16x16x32_bf16 v[60:63], v[40:43], v[8:11], v[60:63]\n\t"
    "s_waitcnt lgkmcnt(2)\n\t"
    "v_mfma_f32_16x16x32_bf16 v[60:63], v[44:47], v[12:15], v[60:63]\n\t"
    "v_mbcnt_lo_u32_b32 v7, -1, 0\n\t"
    "v_mbcnt_hi_u32_b32 v7, -1, v7\n\t"
    "s_waitcnt lgkmcnt(1)\n\t"
    "v_mfma_f32_16x16x32_bf16 v[60:63], v[48:51], v[16:19], v[60:63]\n\t"
    "v_and_or_b32 v4, v7, 3, 0\n\t"
    "v_lshrrev_b32 v6, 2, v7\n\t"
    "s_waitcnt lgkmcnt(0)\n\t"
    "v_mfma_f32_16x16x32_bf16 v[60:63], v[52:55], v[20:23], v[60:63]\n\t"
    "v_xor_b32 v4, v4, v6\n\t"
    "v_lshl_or_b32 v4, v4, 4, %[pb]\n\t"
    "v_lshl_or_b32 v4, v6, 8, v4\n\t"
    "v_xor_b32 v5, 64, v4\n\t"
    "ds_read_b64_tr_b16 v[48:49], v4 offset:40960\n\t"
    "ds_read_b64_tr_b16 v[52:53], v4 offset:40968\n\t"
    "v_add_f32 v32, v32, v38\n\t"
    "v_add_f32 v33, v33, v39\n\t"
    "v_and_b32 v6, 12, v7\n\t"
    "v_and_b32 v4, 3, v7\n\t"
    "v_lshl_add_u32 v4, v6, 1, v4\n\t"
    "v_lshrrev_b32 v6, 4, v7\n\t"
    "v_lshl_add_u32 v4, v4, 2, v6\n\t"
    "v_lshlrev_b32 v4, 2, v4\n\t"
    "v_add_u32 v4, 18432, v4\n\t"
    "ds_read_b64_tr_b16 v[40:41], v5 offset:40960\n\t"
    "ds_read_b64_tr_b16 v[44:45], v5 offset:40968\n\t"
    "ds_read2_b32 v[36:37], v4 offset0:0 offset1:16\n\t"
    "ds_read2_b32 v[38:39], v4 offset0:128 offset1:144\n\t"
    "v_and_b32 v4, 7, v7\n\t"
    "v_xor_b32 v6, v6, v4\n\t"
    "v_max3_f32 v34, v32, v33, %[lo]\n\t"
    "v_max3_f32 v34, v34, v60, v61\n\t"
    "v_max3_f32 v34, v34, v62, v63\n\t"
    "v_mov_b32 v35, v34\n\t"
    "v_lshlrev_b32 v6, 4, v6\n\t"
    "v_and_b32 v4, 15, v7\n\t"
    "v_permlane16_swap_b32 v34, v35\n\t"
    "v_max_f32 v34, v34, v35\n\t"
    "v_mov_b32 v35, v34\n\t"
    "v_lshl_or_b32 v6, v4, 7, v6\n\t"
    "v_xor_b32 v4, 64, v6\n\t"
    "v_permlane32_swap_b32 v34, v35\n\t"
    "v_max_f32 v34, v34, v35\n\t"
    "v_mul_f32 v34, %[nl], v34\n\t"
    "v_fma_f32 v32, v32, %[lsc], v34\n\t"
    "v_fma_f32 v33, v33, %[lsc], v34\n\t"
    "v_fma_f32 v60, v60, %[lsc], v34\n\t"
    "v_fma_f32 v61, v61, %[lsc], v34\n\t"
    "v_fma_f32 v62, v62, %[lsc], v34\n\t"
    "v_fma_f32 v63, v63, %[lsc], v34\n\t"
    "v_exp_f32 v60, v60\n\t"
    "v_exp_f32 v61, v61\n\t"
    "v_exp_f32 v62, v62\n\t"
    "v_exp_f32 v63, v63\n\t"
    "v_exp_f32 v32, v32\n\t"
    "v_exp_f32 v33, v33\n\t"
    "v_cvt_pk_bf16_f32 v24, v60, v61\n\t"
    "v_cvt_pk_bf16_f32 v25, v62, v63\n\t"
    "v_cvt_pk_bf16_f32 v28, v32, v33\n\t"
    "s_waitcnt lgkmcnt(0)\n\t"
    "v_mfma_f32_16x16x16_bf16 v[8:11], v[48:49], v[24:25], 0\n\t"
    "v_mov_b32 v56, %[one]\n\t"
    "v_mov_b32 v57, %[one]\n\t"
    "v_mfma_f32_16x16x16_bf16 v[12:15], v[52:53], v[24:25], 0\n\t"
    "v_mov_b32 v58, %[one]\n\t"
    "v_mov_b32 v59, %[one]\n\t"
    "v_mfma_f32_16x16x16_bf16 v[16:19], v[40:41], v[24:25], 0\n\t"
    "v_mov_b32 v29, 0\n\t"
    "v_mov_b32 v30, 0\n\t"
    "v_mfma_f32_16x16x16_bf16 v[60:63], v[56:57], v[24:25], 0\n\t"
    "v_mov_b32 v31, v28\n\t"
    "v_mfma_f32_16x16x16_bf16 v[20:23], v[44:45], v[24:25], 0\n\t"
    "v_mfma_f32_16x16x16_bf16 v[8:11], v[36:37], v[28:29], v[8:11]\n\t"
    "v_mfma_f32_16x16x16_bf16 v[12:15], v[36:37], v[30:31], v[12:15]\n\t"
    "s_nop 1\n\t"
    "v_mfma_f32_16x16x16_bf16 v[16:19], v[38:39], v[28:29], v[16:19]\n\t"
    "v_mfma_f32_16x16x16_bf16 v[60:63], v[56:57], v[28:29], v[60:63]\n\t"
    "s_nop 1\n\t"
    "v_mfma_f32_16x16x16_bf16 v[20:23], v[38:39], v[30:31], v[20:23]\n\t"
    "s_nop 4\n\t"
    "v_rcp_f32 v60, v60\n\t"
    "s_nop 0\n\t"
    "v_pk_mul_f32 v[8:9], v[8:9], v[60:61] op_sel_hi:[1,0]\n\t"
    "v_pk_mul_f32 v[10:11], v[10:11], v[60:61] op_sel_hi:[1,0]\n\t"
    "v_pk_mul_f32 v[12:13], v[12:13], v[60:61] op_sel_hi:[1,0]\n\t"
    "v_pk_mul_f32 v[14:15], v[14:15], v[60:61] op_sel_hi:[1,0]\n\t"
    "v_cvt_pk_bf16_f32 v24, v8, v9\n\t"
    "v_cvt_pk_bf16_f32 v25, v10, v11\n\t"
    "v_cvt_pk_bf16_f32 v26, v12, v13\n\t"
    "v_cvt_pk_bf16_f32 v27, v14, v15\n\t"
    "global_store_dwordx4 v6, v[24:27], %[od] offset:0 sc1\n\t"
    "v_pk_mul_f32 v[16:17], v[16:17], v[60:61] op_sel_hi:[1,0]\n\t"
    "v_pk_mul_f32 v[18:19], v[18:19], v[60:61] op_sel_hi:[1,0]\n\t"
    "v_pk_mul_f32 v[20:21], v[20:21], v[60:61] op_sel_hi:[1,0]\n\t"
    "v_pk_mul_f32 v[22:23], v[22:23], v[60:61] op_sel_hi:[1,0]\n\t"
    "v_cvt_pk_bf16_f32 v28, v16, v17\n\t"
    "v_cvt_pk_bf16_f32 v29, v18, v19\n\t"
    "v_cvt_pk_bf16_f32 v30, v20, v21\n\t"
    "v_cvt_pk_bf16_f32 v31, v22, v23\n\t"
    "global_store_dwordx4 v4, v[28:31], %[od] offset:0 sc1\n\t"
    "s_waitcnt lgkmcnt(0)\n\t"
    "s_nop 0"
    :
    : [pb] "s"(pb), [od] "s"(od), [neg] "s"(-3.0e38f), [lo] "s"(-1.0e38f), [one] "s"(0x3F803F80u), [lsc] "s"(LSC), [nl] "s"(-LSC), "{v[64:95]}"(W.w0), "{v[96:127]}"(W.w1), "{v[128:159]}"(W.w2), "{v[160:191]}"(W.w3), "{v[192:223]}"(W.w4), "{v[224:255]}"(W.w5)
    : "v4", "v5", "v6", "v7", "v8", "v9", "v10", "v11", "v12", "v13", "v14", "v15", "v16", "v17", "v18", "v19", "v20", "v21", "v22", "v23", "v24", "v25", "v26", "v27", "v28", "v29", "v30", "v31", "v32", "v33", "v34", "v35", "v36", "v37", "v38", "v39", "v40", "v41", "v42", "v43", "v44", "v45", "v46", "v47", "v48", "v49", "v50", "v51", "v52", "v53", "v54", "v55", "v56", "v57", "v58", "v59", "v60", "v61", "v62", "v63", "memory");
}
template <> __device__ __forceinline__ void seg_attn<1>(const QKW& W, unsigned pb, const char* od, unsigned long long* ts) {
  asm volatile("v_mbcnt_lo_u32_b32 v32, -1, 0\n\t"
    "v_mbcnt_hi_u32_b32 v32, -1, v32\n\t"
    "v_lshlrev_b32 v35, 4, v32\n\t"
    "ds_read_b128 v[24:27], v35 offset:20480\n\t"
    "ds_read_b128 v[28:31], v35 offset:21504\n\t"
    "v_lshrrev_b32 v33, 4, v32\n\t"
    "v_and_b32 v34, 15, v32\n\t"
    "v_xor_b32 v4, v33, v34\n\t"
    "v_lshl_or_b32 v4, v4, 4, %[pb]\n\t"
    "v_lshl_or_b32 v4, v34, 8, v4\n\t"
    "ds_read_b128 v[8:11], v4 offset:53248\n\t"
    "v_xor_b32 v5, 0x40, v4\n\t"
    "ds_read_b128 v[12:15], v5 offset:53248\n\t"
    "v_xor_b32 v6, 0x80, v4\n\t"
    "ds_read_b128 v[16:19], v6 offset:53248\n\t"
    "v_xor_b32 v7, 0xc0, v4\n\t"
    "ds_read_b128 v[20:23], v7 offset:53248\n\t"
    "ds_read_b128 v[40:43], v4 offset:32768\n\t"
    "ds_read_b128 v[44:47], v5 offset:32768\n\t"
    "ds_read_b128 v[48:51], v6 offset:32768\n\t"
    "ds_read_b128 v[52:55], v7 offset:32768\n\t"
    "v_lshlrev_b32 v35, 2, v33\n\t"
    "v_sub_u32 v35, v34, v35\n\t"
    "v_ashrrev_i32 v60, 31, v35\n\t"
    "v_and_b32 v60, %[neg], v60\n\t"
    "v_add_u32 v61, -1, v35\n\t"
    "v_ashrrev_i32 v61, 31, v61\n\t"
    "v_and_b32 v61, %[neg], v61\n\t"
    "v_add_u32 v62, -2, v35\n\t"
    "v_ashrrev_i32 v62, 31, v62\n\t"
    "v_and_b32 v62, %[neg], v62\n\t"
    "v_add_u32 v63, -3, v35\n\t"
    "v_ashrrev_i32 v63, 31, v63\n\t"
    "v_and_b32 v63, %[neg], v63\n\t"
    "s_waitcnt lgkmcnt(6)\n\t"
    "v_mfma_f32_16x16x32_bf16 v[32:35], v[24:27], v[8:11], 0\n\t"
    "v_mfma_f32_16x16x32_bf16 v[36:39], v[24:27], v[12:15], 0\n\t"
    "s_waitcnt lgkmcnt(4)\n\t"
    "v_mfma_f32_16x16x32_bf16 v[32:35], v[28:31], v[16:19], v[32:35]\n\t"
    "v_mfma_f32_16x16x32_bf16 v[36:39], v[28:31], v[20:23], v[36:39]\n\t"
    "ds_read_b128 v[24:27], v4 offset:36864\n\t"
    "ds_read_b128 v[28:31], v5 offset:36864\n\t"
    "s_waitcnt lgkmcnt(5)\n\t"
    "v_mfma_f32_16x16x32_bf16 v[56:59], v[40:43], v[8:11], 0\n\t"
    "s_waitcnt lgkmcnt(4)\n\t"
    "v_mfma_f32_16x16x32_bf16 v[56:59], v[44:47], v[12:15], v[56:59]\n\t"
    "ds_read_b128 v[40:43], v6 offset:36864\n\t"
    "ds_read_b128 v[44:47], v7 offset:36864\n\t"
    "v_mbcnt_lo_u32_b32 v7, -1, 0\n\t"
    "v_mbcnt_hi_u32_b32 v7, -1, v7\n\t"
    "s_waitcnt lgkmcnt(5)\n\t"
    "v_mfma_f32_16x16x32_bf16 v[56:59], v[48:51], v[16:19], v[56:59]\n\t"
    "v_and_or_b32 v4, v7, 3, 0\n\t"
    "v_lshrrev_b32 v6, 2, v7\n\t"
    "s_waitcnt lgkmcnt(4)\n\t"
    "v_mfma_f32_16x16x32_bf16 v[56:59], v[52:55], v[20:23], v[56:59]\n\t"
    "v_xor_b32 v4, v4, v6\n\t"
    "v_lshl_or_b32 v4, v4, 4, %[pb]\n\t"
    "v_lshl_or_b32 v4, v6, 8, v4\n\t"
    "v_xor_b32 v5, 64, v4\n\t"
    "ds_read_b64_tr_b16 v[48:49], v4 offset:40960\n\t"
    "ds_read_b64_tr_b16 v[50:51], v4 offset:45056\n\t"
    "ds_read_b64_tr_b16 v[52:53], v4 offset:40968\n\t"
    "ds_read_b64_tr_b16 v[54:55], v4 offset:45064\n\t"
    "v_add_f32 v32, v32, v38\n\t"
    "v_add_f32 v33, v33, v39\n\t"
    "s_waitcnt lgkmcnt(7)\n\t"
    "v_mfma_f32_16x16x32_bf16 v[60:63], v[24:27], v[8:11], v[60:63]\n\t"
    "v_and_b32 v6, 12, v7\n\t"
    "v_and_b32 v4, 3, v7\n\t"
    "s_waitcnt lgkmcnt(6)\n\t"
    "v_mfma_f32_16x16x32_bf16 v[60:63], v[28:31], v[12:15], v[60:63]\n\t"
    "v_lshl_add_u32 v4, v6, 1, v4\n\t"
    "v_lshrrev_b32 v6, 4, v7\n\t"
    "s_waitcnt lgkmcnt(5)\n\t"
    "v_mfma_f32_16x16x32_bf16 v[60:63], v[40:43], v[16:19], v[60:63]\n\t"
    "v_lshl_add_u32 v4, v4, 2, v6\n\t"
    "v_lshlrev_b32 v4, 2, v4\n\t"
    "s_waitcnt lgkmcnt(4)\n\t"
    "v_mfma_f32_16x16x32_bf16 v[60:63], v[44:47], v[20:23], v[60:63]\n\t"
    "v_add_u32 v4, 18432, v4\n\t"
    "ds_read_b64_tr_b16 v[40:41], v5 offset:40960\n\t"
    "ds_read_b64_tr_b16 v[42:43], v5 offset:45056\n\t"
    "ds_read_b64_tr_b16 v[44:45], v5 offset:40968\n\t"
    "ds_read_b64_tr_b16 v[46:47], v5 offset:45064\n\t"
    "ds_read2_b32 v[36:37], v4 offset0:0 offset1:16\n\t"
    "ds_read2_b32 v[38:39], v4 offset0:128 offset1:144\n\t"
    "v_and_b32 v4, 7, v7\n\t"
    "v_xor_b32 v6, v6, v4\n\t"
    "v_max3_f32 v34, v32, v33, %[lo]\n\t"
    "v_max3_f32 v34, v34, v56, v57\n\t"
    "v_max3_f32 v34, v34, v58, v59\n\t"
    "v_max3_f32 v34, v34, v60, v61\n\t"
    "v_max3_f32 v34, v34, v62, v63\n\t"
    "v_mov_b32 v35, v34\n\t"
    "v_lshlrev_b32 v6, 4, v6\n\t"
    "v_and_b32 v4, 15, v7\n\t"
    "v_permlane16_swap_b32 v34, v35\n\t"
    "v_max_f32 v34, v34, v35\n\t"
    "v_mov_b32 v35, v34\n\t"
    "v_lshl_or_b32 v6, v4, 7, v6\n\t"
    "v_xor_b32 v4, 64, v6\n\t"
    "v_permlane32_swap_b32 v34, v35\n\t"
    "v_max_f32 v34, v34, v35\n\t"
    "v_mul_f32 v34, %[nl], v34\n\t"
    "v_fma_f32 v32, v32, %[lsc], v34\n\t"
    "v_fma_f32 v33, v33, %[lsc], v34\n\t"
    "v_fma_f32 v56, v56, %[lsc], v34\n\t"
    "v_fma_f32 v57, v57, %[lsc], v34\n\t"
    "v_fma_f32 v58, v58, %[lsc], v34\n\t"
    "v_fma_f32 v59, v59, %[lsc], v34\n\t"
    "v_fma_f32 v60, v60, %[lsc], v34\n\t"
    "v_fma_f32 v61, v61, %[lsc], v34\n\t"
    "v_fma_f32 v62, v62, %[lsc], v34\n\t"
    "v_fma_f32 v63, v63, %[lsc], v34\n\t"
    "v_exp_f32 v56, v56\n\t"
    "v_exp_f32 v57, v57\n\t"
    "v_exp_f32 v58, v58\n\t"
    "v_exp_f32 v59, v59\n\t"
    "v_exp_f32 v60, v60\n\t"
    "v_exp_f32 v61, v61\n\t"
    "v_exp_f32 v62, v62\n\t"
    "v_exp_f32 v63, v63\n\t"
    "v_exp_f32 v32, v32\n\t"
    "v_exp_f32 v33, v33\n\t"
    "v_cvt_pk_bf16_f32 v24, v56, v57\n\t"
    "v_cvt_pk_bf16_f32 v25, v58, v59\n\t"
    "v_cvt_pk_bf16_f32 v26, v60, v61\n\t"
    "v_cvt_pk_bf16_f32 v27, v62, v63\n\t"
    "v_cvt_pk_bf16_f32 v28, v32, v33\n\t"
    "s_waitcnt lgkmcnt(0)\n\t"
    "v_mfma_f32_16x16x32_bf16 v[8:11], v[48:51], v[24:27], 0\n\t"
    "v_mov_b32 v56, %[one]\n\t"
    "v_mov_b32 v57, %[one]\n\t"
    "v_mfma_f32_16x16x32_bf16 v[12:15], v[52:55], v[24:27], 0\n\t"
    "v_mov_b32 v58, %[one]\n\t"
    "v_mov_b32 v59, %[one]\n\t"
    "v_mfma_f32_16x16x32_bf16 v[16:19], v[40:43], v[24:27], 0\n\t"
    "v_mov_b32 v29, 0\n\t"
    "v_mov_b32 v30, 0\n\t"
    "v_mfma_f32_16x16x32_bf16 v[60:63], v[56:59], v[24:27], 0\n\t"
    "v_mov_b32 v31, v28\n\t"
    "v_mfma_f32_16x16x32_bf16 v[20:23], v[44:47], v[24:27], 0\n\t"
    "v_mfma_f32_16x16x16_bf16 v[8:11], v[36:37], v[28:29], v[8:11]\n\t"
    "v_mfma_f32_16x16x16_bf16 v[12:15], v[36:37], v[30:31], v[12:15]\n\t"
    "s_nop 1\n\t"
    "v_mfma_f32_16x16x16_bf16 v[16:19], v[38:39], v[28:29], v[16:19]\n\t"
    "v_mfma_f32_16x16x16_bf16 v[60:63], v[56:57], v[28:29], v[60:63]\n\t"
    "s_nop 1\n\t"
    "v_mfma_f32_16x16x16_bf16 v[20:23], v[38:39], v[30:31], v[20:23]\n\t"
    "s_nop 4\n\t"
    "v_rcp_f32 v60, v60\n\t"
    "s_nop 0\n\t"
    "v_pk_mul_f32 v[8:9], v[8:9], v[60:61] op_sel_hi:[1,0]\n\t"
    "v_pk_mul_f32 v[10:11], v[10:11], v[60:61] op_sel_hi:[1,0]\n\t"
    "v_pk_mul_f32 v[12:13], v[12:13], v[60:61] op_sel_hi:[1,0]\n\t"
    "v_pk_mul_f32 v[14:15], v[14:15], v[60:61] op_sel_hi:[1,0]\n\t"
    "v_cvt_pk_bf16_f32 v24, v8, v9\n\t"
    "v_cvt_pk_bf16_f32 v25, v10, v11\n\t"
    "v_cvt_pk_bf16_f32 v26, v12, v13\n\t"
    "v_cvt_pk_bf16_f32 v27, v14, v15\n\t"
    "global_store_dwordx4 v6, v[24:27], %[od] offset:2048 sc1\n\t"
    "v_pk_mul_f32 v[16:17], v[16:17], v[60:61] op_sel_hi:[1,0]\n\t"
    "v_pk_mul_f32 v[18:19], v[18:19], v[60:61] op_sel_hi:[1,0]\n\t"
    "v_pk_mul_f32 v[20:21], v[20:21], v[60:61] op_sel_hi:[1,0]\n\t"
    "v_pk_mul_f32 v[22:23], v[22:23], v[60:61] op_sel_hi:[1,0]\n\t"
    "v_cvt_pk_bf16_f32 v28, v16, v17\n\t"
    "v_cvt_pk_bf16_f32 v29, v18, v19\n\t"
    "v_cvt_pk_bf16_f32 v30, v20, v21\n\t"
    "v_cvt_pk_bf16_f32 v31, v22, v23\n\t"
    "global_store_dwordx4 v4, v[28:31], %[od] offset:2048 sc1\n\t"
    "s_waitcnt lgkmcnt(0)\n\t"
    "s_nop 0"
    :
    : [pb] "s"(pb), [od] "s"(od), [neg] "s"(-3.0e38f), [lo] "s"(-1.0e38f), [one] "s"(0x3F803F80u), [lsc] "s"(LSC), [nl] "s"(-LSC), "{v[64:95]}"(W.w0), "{v[96:127]}"(W.w1), "{v[128:159]}"(W.w2), "{v[160:191]}"(W.w3), "{v[192:223]}"(W.w4), "{v[224:255]}"(W.w5)
    : "v4", "v5", "v6", "v7", "v8", "v9", "v10", "v11", "v12", "v13", "v14", "v15", "v16", "v17", "v18", "v19", "v20", "v21", "v22", "v23", "v24", "v25", "v26", "v27", "v28", "v29", "v30", "v31", "v32", "v33", "v34", "v35", "v36", "v37", "v38", "v39", "v40", "v41", "v42", "v43", "v44", "v45", "v46", "v47", "v48", "v49", "v50", "v51", "v52", "v53", "v54", "v55", "v56", "v57", "v58", "v59", "v60", "v61", "v62", "v63", "memory");
}
template <> __device__ __forceinline__ void seg_attn<2>(const QKW& W, unsigned pb, const char* od, unsigned long long* ts) {
  asm volatile("v_mbcnt_lo_u32_b32 v32, -1, 0\n\t"
    "v_mbcnt_hi_u32_b32 v32, -1, v32\n\t"
    "v_lshlrev_b32 v35, 4, v32\n\t"
    "ds_read_b128 v[24:27], v35 offset:20480\n\t"
    "ds_read_b128 v[28:31], v35 offset:21504\n\t"
    "v_lshrrev_b32 v33, 4, v32\n\t"
    "v_and_b32 v34, 15, v32\n\t"
    "v_xor_b32 v4, v33, v34\n\t"
    "v_lshl_or_b32 v4, v4, 4, %[pb]\n\t"
    "v_lshl_or_b32 v4, v34, 8, v4\n\t"
    "ds_read_b128 v[8:11], v4 offset:49152\n\t"
    "v_xor_b32 v5, 0x40, v4\n\t"
    "ds_read_b128 v[12:15], v5 offset:49152\n\t"
    "v_xor_b32 v6, 0x80, v4\n\t"
    "ds_read_b128 v[16:19], v6 offset:49152\n\t"
    "v_xor_b32 v7, 0xc0, v4\n\t"
    "ds_read_b128 v[20:23], v7 offset:49152\n\t"
    "ds_read_b128 v[40:43], v4 offset:32768\n\t"
    "ds_read_b128 v[44:47], v5 offset:32768\n\t"
    "ds_read_b128 v[48:51], v6 offset:32768\n\t"
    "ds_read_b128 v[52:55], v7 offset:32768\n\t"
    "v_lshlrev_b32 v35, 2, v33\n\t"
    "v_sub_u32 v35, v34, v35\n\t"
    "v_ashrrev_i32 v60, 31, v35\n\t"
    "v_and_b32 v60, %[neg], v60\n\t"
    "v_add_u32 v61, -1, v35\n\t"
    "v_ashrrev_i32 v61, 31, v61\n\t"
    "v_and_b32 v61, %[neg], v61\n\t"
    "v_add_u32 v62, -2, v35\n\t"
    "v_ashrrev_i32 v62, 31, v62\n\t"
    "v_and_b32 v62, %[neg], v62\n\t"
    "v_add_u32 v63, -3, v35\n\t"
    "v_ashrrev_i32 v63, 31, v63\n\t"
    "v_and_b32 v63, %[neg], v63\n\t"
    "s_waitcnt lgkmcnt(6)\n\t"
    "v_mfma_f32_16x16x32_bf16 v[32:35], v[24:27], v[8:11], 0\n\t"
    "v_mfma_f32_16x16x32_bf16 v[36:39], v[24:27], v[12:15], 0\n\t"
    "s_waitcnt lgkmcnt(4)\n\t"
    "v_mfma_f32_16x16x32_bf16 v[32:35], v[28:31], v[16:19], v[32:35]\n\t"
    "v_mfma_f32_16x16x32_bf16 v[36:39], v[28:31], v[20:23], v[36:39]\n\t"
    "s_waitcnt lgkmcnt(3)\n\t"
    "v_mfma_f32_16x16x32_bf16 v[60:63], v[40:43], v[8:11], v[60:63]\n\t"
    "s_waitcnt lgkmcnt(2)\n\t"
    "v_mfma_f32_16x16x32_bf16 v[60:63], v[44:47], v[12:15], v[60:63]\n\t"
    "v_mbcnt_lo_u32_b32 v7, -1, 0\n\t"
    "v_mbcnt_hi_u32_b32 v7, -1, v7\n\t"
    "s_waitcnt lgkmcnt(1)\n\t"
    "v_mfma_f32_16x16x32_bf16 v[60:63], v[48:51], v[16:19], v[60:63]\n\t"
    "v_and_or_b32 v4, v7, 3, 8\n\t"
    "v_lshrrev_b32 v6, 2, v7\n\t"
    "s_waitcnt lgkmcnt(0)\n\t"
    "v_mfma_f32_16x16x32_bf16 v[60:63], v[52:55], v[20:23], v[60:63]\n\t"
    "v_xor_b32 v4, v4, v6\n\t"
    "v_lshl_or_b32 v4, v4, 4, %[pb]\n\t"
    "v_lshl_or_b32 v4, v6, 8, v4\n\t"
    "v_xor_b32 v5, 64, v4\n\t"
    "ds_read_b64_tr_b16 v[48:49], v4 offset:40960\n\t"
    "ds_read_b64_tr_b16 v[52:53], v4 offset:40968\n\t"
    "v_add_f32 v32, v32, v38\n\t"
    "v_add_f32 v33, v33, v39\n\t"
    "v_and_b32 v6, 12, v7\n\t"
    "v_and_b32 v4, 3, v7\n\t"
    "v_lshl_add_u32 v4, v6, 1, v4\n\t"
    "v_lshrrev_b32 v6, 4, v7\n\t"
    "v_lshl_add_u32 v4, v4, 2, v6\n\t"
    "v_lshlrev_b32 v4, 2, v4\n\t"
    "v_add_u32 v4, 19456, v4\n\t"
    "ds_read_b64_tr_b16 v[40:41], v5 offset:40960\n\t"
    "ds_read_b64_tr_b16 v[44:45], v5 offset:40968\n\t"
    "ds_read2_b32 v[36:37], v4 offset0:0 offset1:16\n\t"
    "ds_read2_b32 v[38:39], v4 offset0:128 offset1:144\n\t"
    "v_and_b32 v4, 7, v7\n\t"
    "v_xor_b32 v6, v6, v4\n\t"
    "v_max3_f32 v34, v32, v33, %[lo]\n\t"
    "v_max3_f32 v34, v34, v60, v61\n\t"
    "v_max3_f32 v34, v34, v62, v63\n\t"
    "v_mov_b32 v35, v34\n\t"
    "v_lshlrev_b32 v6, 4, v6\n\t"
    "v_and_b32 v4, 15, v7\n\t"
    "v_permlane16_swap_b32 v34, v35\n\t"
    "v_max_f32 v34, v34, v35\n\t"
    "v_mov_b32 v35, v34\n\t"
    "v_lshl_or_b32 v6, v4, 7, v6\n\t"
    "v_xor_b32 v4, 64, v6\n\t"
    "v_permlane32_swap_b32 v34, v35\n\t"
    "v_max_f32 v34, v34, v35\n\t"
    "v_mul_f32 v34, %[nl], v34\n\t"
    "v_fma_f32 v32, v32, %[lsc], v34\n\t"
    "v_fma_f32 v33, v33, %[lsc], v34\n\t"
    "v_fma_f32 v60, v60, %[lsc], v34\n\t"
    "v_fma_f32 v61, v61, %[lsc], v34\n\t"
    "v_fma_f32 v62, v62, %[lsc], v34\n\t"
    "v_fma_f32 v63, v63, %[lsc], v34\n\t"
    "v_exp_f32 v60, v60\n\t"
    "v_exp_f32 v61, v61\n\t"
    "v_exp_f32 v62, v62\n\t"
    "v_exp_f32 v63, v63\n\t"
    "v_exp_f32 v32, v32\n\t"
    "v_exp_f32 v33, v33\n\t"
    "v_cvt_pk_bf16_f32 v24, v60, v61\n\t"
    "v_cvt_pk_bf16_f32 v25, v62, v63\n\t"
    "v_cvt_pk_bf16_f32 v28, v32, v33\n\t"
    "s_waitcnt lgkmcnt(0)\n\t"
    "v_mfma_f32_16x16x16_bf16 v[8:11], v[48:49], v[24:25], 0\n\t"
    "v_mov_b32 v56, %[one]\n\t"
    "v_mov_b32 v57, %[one]\n\t"
    "v_mfma_f32_16x16x16_bf16 v[12:15], v[52:53], v[24:25], 0\n\t"
    "v_mov_b32 v58, %[one]\n\t"
    "v_mov_b32 v59, %[one]\n\t"
    "v_mfma_f32_16x16x16_bf16 v[16:19], v[40:41], v[24:25], 0\n\t"
    "v_mov_b32 v29, 0\n\t"
    "v_mov_b32 v30, 0\n\t"
    "v_mfma_f32_16x16x16_bf16 v[60:63], v[56:57], v[24:25], 0\n\t"
    "v_mov_b32 v31, v28\n\t"
    "v_mfma_f32_16x16x16_bf16 v[20:23], v[44:45], v[24:25], 0\n\t"
    "v_mfma_f32_16x16x16_bf16 v[8:11], v[36:37], v[28:29], v[8:11]\n\t"
    "v_mfma_f32_16x16x16_bf16 v[12:15], v[36:37], v[30:31], v[12:15]\n\t"
    "s_nop 1\n\t"
    "v_mfma_f32_16x16x16_bf16 v[16:19], v[38:39], v[28:29], v[16:19]\n\t"
    "v_mfma_f32_16x16x16_bf16 v[60:63], v[56:57], v[28:29], v[60:63]\n\t"
    "s_nop 1\n\t"
    "v_mfma_f32_16x16x16_bf16 v[20:23], v[38:39], v[30:31], v[20:23]\n\t"
    "s_nop 4\n\t"
    "v_rcp_f32 v60, v60\n\t"
    "s_nop 0\n\t"
    "v_pk_mul_f32 v[8:9], v[8:9], v[60:61] op_sel_hi:[1,0]\n\t"
    "v_pk_mul_f32 v[10:11], v[10:11], v[60:61] op_sel_hi:[1,0]\n\t"
    "v_pk_mul_f32 v[12:13], v[12:13], v[60:61] op_sel_hi:[1,0]\n\t"
    "v_pk_mul_f32 v[14:15], v[14:15], v[60:61] op_sel_hi:[1,0]\n\t"
    "v_cvt_pk_bf16_f32 v24, v8, v9\n\t"
    "v_cvt_pk_bf16_f32 v25, v10, v11\n\t"
    "v_cvt_pk_bf16_f32 v26, v12, v13\n\t"
    "v_cvt_pk_bf16_f32 v27, v14, v15\n\t"
    "global_store_dwordx4 v6, v[24:27], %[od] offset:0 sc1\n\t"
    "v_pk_mul_f32 v[16:17], v[16:17], v[60:61] op_sel_hi:[1,0]\n\t"
    "v_pk_mul_f32 v[18:19], v[18:19], v[60:61] op_sel_hi:[1,0]\n\t"
    "v_pk_mul_f32 v[20:21], v[20:21], v[60:61] op_sel_hi:[1,0]\n\t"
    "v_pk_mul_f32 v[22:23], v[22:23], v[60:61] op_sel_hi:[1,0]\n\t"
    "v_cvt_pk_bf16_f32 v28, v16, v17\n\t"
    "v_cvt_pk_bf16_f32 v29, v18, v19\n\t"
    "v_cvt_pk_bf16_f32 v30, v20, v21\n\t"
    "v_cvt_pk_bf16_f32 v31, v22, v23\n\t"
    "global_store_dwordx4 v4, v[28:31], %[od] offset:0 sc1\n\t"
    "s_waitcnt lgkmcnt(0)\n\t"
    "s_nop 0"
    :
    : [pb] "s"(pb), [od] "s"(od), [neg] "s"(-3.0e38f), [lo] "s"(-1.0e38f), [one] "s"(0x3F803F80u), [lsc] "s"(LSC), [nl] "s"(-LSC), "{v[64:95]}"(W.w0), "{v[96:127]}"(W.w1), "{v[128:159]}"(W.w2), "{v[160:191]}"(W.w3), "{v[192:223]}"(W.w4), "{v[224:255]}"(W.w5)
    : "v4", "v5", "v6", "v7", "v8", "v9", "v10", "v11", "v12", "v13", "v14", "v15", "v16", "v17", "v18", "v19", "v20", "v21", "v22", "v23", "v24", "v25", "v26", "v27", "v28", "v29", "v30", "v31", "v32", "v33", "v34", "v35", "v36", "v37", "v38", "v39", "v40", "v41", "v42", "v43", "v44", "v45", "v46", "v47", "v48", "v49", "v50", "v51", "v52", "v53", "v54", "v55", "v56", "v57", "v58", "v59", "v60", "v61", "v62", "v63", "memory");
}
template <> __device__ __forceinline__ void seg_attn<3>(const QKW& W, unsigned pb, const char* od, unsigned long long* ts) {
  asm volatile("v_mbcnt_lo_u32_b32 v32, -1, 0\n\t"
    "v_mbcnt_hi_u32_b32 v32, -1, v32\n\t"
    "v_lshlrev_b32 v35, 4, v32\n\t"
    "ds_read_b128 v[24:27], v35 offset:20480\n\t"
    "ds_read_b128 v[28:31], v35 offset:21504\n\t"
    "v_lshrrev_b32 v33, 4, v32\n\t"
    "v_and_b32 v34, 15, v32\n\t"
    "v_xor_b32 v4, v33, v34\n\t"
    "v_lshl_or_b32 v4, v4, 4, %[pb]\n\t"
    "v_lshl_or_b32 v4, v34, 8, v4\n\t"
    "ds_read_b128 v[8:11], v4 offset:53248\n\t"
    "v_xor_b32 v5, 0x40, v4\n\t"
    "ds_read_b128 v[12:15], v5 offset:53248\n\t"
    "v_xor_b32 v6, 0x80, v4\n\t"
    "ds_read_b128 v[16:19], v6 offset:53248\n\t"
    "v_xor_b32 v7, 0xc0, v4\n\t"
    "ds_read_b128 v[20:23], v7 offset:53248\n\t"
    "ds_read_b128 v[40:43], v4 offset:32768\n\t"
    "ds_read_b128 v[44:47], v5 offset:32768\n\t"
    "ds_read_b128 v[48:51], v6 offset:32768\n\t"
    "ds_read_b128 v[52:55], v7 offset:32768\n\t"
    "v_lshlrev_b32 v35, 2, v33\n\t"
    "v_sub_u32 v35, v34, v35\n\t"
    "v_ashrrev_i32 v60, 31, v35\n\t"
    "v_and_b32 v60, %[neg], v60\n\t"
    "v_add_u32 v61, -1, v35\n\t"
    "v_ashrrev_i32 v61, 31, v61\n\t"
    "v_and_b32 v61, %[neg], v61\n\t"
    "v_add_u32 v62, -2, v35\n\t"
    "v_ashrrev_i32 v62, 31, v62\n\t"
    "v_and_b32 v62, %[neg], v62\n\t"
    "v_add_u32 v63, -3, v35\n\t"
    "v_ashrrev_i32 v63, 31, v63\n\t"
    "v_and_b32 v63, %[neg], v63\n\t"
    "s_waitcnt lgkmcnt(6)\n\t"
    "v_mfma_f32_16x16x32_bf16 v[32:35], v[24:27], v[8:11], 0\n\t"
    "v_mfma_f32_16x16x32_bf16 v[36:39], v[24:27], v[12:15], 0\n\t"
    "s_waitcnt lgkmcnt(4)\n\t"
    "v_mfma_f32_16x16x32_bf16 v[32:35], v[28:31], v[16:19], v[32:35]\n\t"
    "v_mfma_f32_16x16x32_bf16 v[36:39], v[28:31], v[20:23], v[36:39]\n\t"
    "ds_read_b128 v[24:27], v4 offset:36864\n\t"
    "ds_read_b128 v[28:31], v5 offset:36864\n\t"
    "s_waitcnt lgkmcnt(5)\n\t"
    "v_mfma_f32_16x16x32_bf16 v[56:59], v[40:43], v[8:11], 0\n\t"
    "s_waitcnt lgkmcnt(4)\n\t"
    "v_mfma_f32_16x16x32_bf16 v[56:59], v[44:47], v[12:15], v[56:59]\n\t"
    "ds_read_b128 v[40:43], v6 offset:36864\n\t"
    "ds_read_b128 v[44:47], v7 offset:36864\n\t"
    "v_mbcnt_lo_u32_b32 v7, -1, 0\n\t"
    "v_mbcnt_hi_u32_b32 v7, -1, v7\n\t"
    "s_waitcnt lgkmcnt(5)\n\t"
    "v_mfma_f32_16x16x32_bf16 v[56:59], v[48:51], v[16:19], v[56:59]\n\t"
    "v_and_or_b32 v4, v7, 3, 8\n\t"
    "v_lshrrev_b32 v6, 2, v7\n\t"
    "s_waitcnt lgkmcnt(4)\n\t"
    "v_mfma_f32_16x16x32_bf16 v[56:59], v[52:55], v[20:23], v[56:59]\n\t"
    "v_xor_b32 v4, v4, v6\n\t"
    "v_lshl_or_b32 v4, v4, 4, %[pb]\n\t"
    "v_lshl_or_b32 v4, v6, 8, v4\n\t"
    "v_xor_b32 v5, 64, v4\n\t"
    "ds_read_b64_tr_b16 v[48:49], v4 offset:40960\n\t"
    "ds_read_b64_tr_b16 v[50:51], v4 offset:45056\n\t"
    "ds_read_b64_tr_b16 v[52:53], v4 offset:40968\n\t"
    "ds_read_b64_tr_b16 v[54:55], v4 offset:45064\n\t"
    "v_add_f32 v32, v32, v38\n\t"
    "v_add_f32 v33, v33, v39\n\t"
    "s_waitcnt lgkmcnt(7)\n\t"
    "v_mfma_f32_16x16x32_bf16 v[60:63], v[24:27], v[8:11], v[60:63]\n\t"
    "v_and_b32 v6, 12, v7\n\t"
    "v_and_b32 v4, 3, v7\n\t"
    "s_waitcnt lgkmcnt(6)\n\t"
    "v_mfma_f32_16x16x32_bf16 v[60:63], v[28:31], v[12:15], v[60:63]\n\t"
    "v_lshl_add_u32 v4, v6, 1, v4\n\t"
    "v_lshrrev_b32 v6, 4, v7\n\t"
    "s_waitcnt lgkmcnt(5)\n\t"
    "v_mfma_f32_16x16x32_bf16 v[60:63], v[40:43], v[16:19], v[60:63]\n\t"
    "v_lshl_add_u32 v4, v4, 2, v6\n\t"
    "v_lshlrev_b32 v4, 2, v4\n\t"
    "s_waitcnt lgkmcnt(4)\n\t"
    "v_mfma_f32_16x16x32_bf16 v[60:63], v[44:47], v[20:23], v[60:63]\n\t"
    "v_add_u32 v4, 19456, v4\n\t"
    "ds_read_b64_tr_b16 v[40:41], v5 offset:40960\n\t"
    "ds_read_b64_tr_b16 v[42:43], v5 offset:45056\n\t"
    "ds_read_b64_tr_b16 v[44:45], v5 offset:40968\n\t"
    "ds_read_b64_tr_b16 v[46:47], v5 offset:45064\n\t"
    "ds_read2_b32 v[36:37], v4 offset0:0 offset1:16\n\t"
    "ds_read2_b32 v[38:39], v4 offset0:128 offset1:144\n\t"
    "v_and_b32 v4, 7, v7\n\t"
    "v_xor_b32 v6, v6, v4\n\t"
    "v_max3_f32 v34, v32, v33, %[lo]\n\t"
    "v_max3_f32 v34, v34, v56, v57\n\t"
    "v_max3_f32 v34, v34, v58, v59\n\t"
    "v_max3_f32 v34, v34, v60, v61\n\t"
    "v_max3_f32 v34, v34, v62, v63\n\t"
    "v_mov_b32 v35, v34\n\t"
    "v_lshlrev_b32 v6, 4, v6\n\t"
    "v_and_b32 v4, 15, v7\n\t"
    "v_permlane16_swap_b32 v34, v35\n\t"
    "v_max_f32 v34, v34, v35\n\t"
    "v_mov_b32 v35, v34\n\t"
    "v_lshl_or_b32 v6, v4, 7, v6\n\t"
    "v_xor_b32 v4, 64, v6\n\t"
    "v_permlane32_swap_b32 v34, v35\n\t"
    "v_max_f32 v34, v34, v35\n\t"
    "v_mul_f32 v34, %[nl], v34\n\t"
    "v_fma_f32 v32, v32, %[lsc], v34\n\t"
    "v_fma_f32 v33, v33, %[lsc], v34\n\t"
    "v_fma_f32 v56, v56, %[lsc], v34\n\t"
    "v_fma_f32 v57, v57, %[lsc], v34\n\t"
    "v_fma_f32 v58, v58, %[lsc], v34\n\t"
    "v_fma_f32 v59, v59, %[lsc], v34\n\t"
    "v_fma_f32 v60, v60, %[lsc], v34\n\t"
    "v_fma_f32 v61, v61, %[lsc], v34\n\t"
    "v_fma_f32 v62, v62, %[lsc], v34\n\t"
    "v_fma_f32 v63, v63, %[lsc], v34\n\t"
    "v_exp_f32 v56, v56\n\t"
    "v_exp_f32 v57, v57\n\t"
    "v_exp_f32 v58, v58\n\t"
    "v_exp_f32 v59, v59\n\t"
    "v_exp_f32 v60, v60\n\t"
    "v_exp_f32 v61, v61\n\t"
    "v_exp_f32 v62, v62\n\t"
    "v_exp_f32 v63, v63\n\t"
    "v_exp_f32 v32, v32\n\t"
    "v_exp_f32 v33, v33\n\t"
    "v_cvt_pk_bf16_f32 v24, v56, v57\n\t"
    "v_cvt_pk_bf16_f32 v25, v58, v59\n\t"
    "v_cvt_pk_bf16_f32 v26, v60, v61\n\t"
    "v_cvt_pk_bf16_f32 v27, v62, v63\n\t"
    "v_cvt_pk_bf16_f32 v28, v32, v33\n\t"
    "s_waitcnt lgkmcnt(0)\n\t"
    "v_mfma_f32_16x16x32_bf16 v[8:11], v[48:51], v[24:27], 0\n\t"
    "v_mov_b32 v56, %[one]\n\t"
    "v_mov_b32 v57, %[one]\n\t"
    "v_mfma_f32_16x16x32_bf16 v[12:15], v[52:55], v[24:27], 0\n\t"
    "v_mov_b32 v58, %[one]\n\t"
    "v_mov_b32 v59, %[one]\n\t"
    "v_mfma_f32_16x16x32_bf16 v[16:19], v[40:43], v[24:27], 0\n\t"
    "v_mov_b32 v29, 0\n\t"
    "v_mov_b32 v30, 0\n\t"
    "v_mfma_f32_16x16x32_bf16 v[60:63], v[56:59], v[24:27], 0\n\t"
    "v_mov_b32 v31, v28\n\t"
    "v_mfma_f32_16x16x32_bf16 v[20:23], v[44:47], v[24:27], 0\n\t"
    "v_mfma_f32_16x16x16_bf16 v[8:11], v[36:37], v[28:29], v[8:11]\n\t"
    "v_mfma_f32_16x16x16_bf16 v[12:15], v[36:37], v[30:31], v[12:15]\n\t"
    "s_nop 1\n\t"
    "v_mfma_f32_16x16x16_bf16 v[16:19], v[38:39], v[28:29], v[16:19]\n\t"
    "v_mfma_f32_16x16x16_bf16 v[60:63], v[56:57], v[28:29], v[60:63]\n\t"
    "s_nop 1\n\t"
    "v_mfma_f32_16x16x16_bf16 v[20:23], v[38:39], v[30:31], v[20:23]\n\t"
    "s_nop 4\n\t"
    "v_rcp_f32 v60, v60\n\t"
    "s_nop 0\n\t"
    "v_pk_mul_f32 v[8:9], v[8:9], v[60:61] op_sel_hi:[1,0]\n\t"
    "v_pk_mul_f32 v[10:11], v[10:11], v[60:61] op_sel_hi:[1,0]\n\t"
    "v_pk_mul_f32 v[12:13], v[12:13], v[60:61] op_sel_hi:[1,0]\n\t"
    "v_pk_mul_f32 v[14:15], v[14:15], v[60:61] op_sel_hi:[1,0]\n\t"
    "v_cvt_pk_bf16_f32 v24, v8, v9\n\t"
    "v_cvt_pk_bf16_f32 v25, v10, v11\n\t"
    "v_cvt_pk_bf16_f32 v26, v12, v13\n\t"
    "v_cvt_pk_bf16_f32 v27, v14, v15\n\t"
    "global_store_dwordx4 v6, v[24:27], %[od] offset:2048 sc1\n\t"
    "v_pk_mul_f32 v[16:17], v[16:17], v[60:61] op_sel_hi:[1,0]\n\t"
    "v_pk_mul_f32 v[18:19], v[18:19], v[60:61] op_sel_hi:[1,0]\n\t"
    "v_pk_mul_f32 v[20:21], v[20:21], v[60:61] op_sel_hi:[1,0]\n\t"
    "v_pk_mul_f32 v[22:23], v[22:23], v[60:61] op_sel_hi:[1,0]\n\t"
    "v_cvt_pk_bf16_f32 v28, v16, v17\n\t"
    "v_cvt_pk_bf16_f32 v29, v18, v19\n\t"
    "v_cvt_pk_bf16_f32 v30, v20, v21\n\t"
    "v_cvt_pk_bf16_f32 v31, v22, v23\n\t"
    "global_store_dwordx4 v4, v[28:31], %[od] offset:2048 sc1\n\t"
    "s_waitcnt lgkmcnt(0)\n\t"
    "s_nop 0"
    :
    : [pb] "s"(pb), [od] "s"(od), [neg] "s"(-3.0e38f), [lo] "s"(-1.0e38f), [one] "s"(0x3F803F80u), [lsc] "s"(LSC), [nl] "s"(-LSC), "{v[64:95]}"(W.w0), "{v[96:127]}"(W.w1), "{v[128:159]}"(W.w2), "{v[160:191]}"(W.w3), "{v[192:223]}"(W.w4), "{v[224:255]}"(W.w5)
    : "v4", "v5", "v6", "v7", "v8", "v9", "v10", "v11", "v12", "v13", "v14", "v15", "v16", "v17", "v18", "v19", "v20", "v21", "v22", "v23", "v24", "v25", "v26", "v27", "v28", "v29", "v30", "v31", "v32", "v33", "v34", "v35", "v36", "v37", "v38", "v39", "v40", "v41", "v42", "v43", "v44", "v45", "v46", "v47", "v48", "v49", "v50", "v51", "v52", "v53", "v54", "v55", "v56", "v57", "v58", "v59", "v60", "v61", "v62", "v63", "memory");
}
template <int I> __device__ __forceinline__ void qk_wload(u32x32& w, unsigned a0, unsigned a1);
template <> __device__ __forceinline__ void qk_wload<0>(u32x32& w, unsigned a0, unsigned a1) {
  asm volatile("ds_read_b64_tr_b16 v[64:65], %1 offset:0\n\t"
    "ds_read_b64_tr_b16 v[66:67], %1 offset:512\n\t"
    "ds_read_b64_tr_b16 v[68:69], %1 offset:8192\n\t"
    "ds_read_b64_tr_b16 v[70:71], %1 offset:8704\n\t"
    "ds_read_b64_tr_b16 v[72:73], %2 offset:0\n\t"
    "ds_read_b64_tr_b16 v[74:75], %2 offset:512\n\t"
    "ds_read_b64_tr_b16 v[76:77], %2 offset:8192\n\t"
    "ds_read_b64_tr_b16 v[78:79], %2 offset:8704\n\t"
    "s_waitcnt lgkmcnt(0)\n\t"
    "ds_read_b64_tr_b16 v[80:81], %1 offset:16384\n\t"
    "ds_read_b64_tr_b16 v[82:83], %1 offset:16896\n\t"
    "ds_read_b64_tr_b16 v[84:85], %1 offset:24576\n\t"
    "ds_read_b64_tr_b16 v[86:87], %1 offset:25088\n\t"
    "ds_read_b64_tr_b16 v[88:89], %2 offset:16384\n\t"
    "ds_read_b64_tr_b16 v[90:91], %2 offset:16896\n\t"
    "ds_read_b64_tr_b16 v[92:93], %2 offset:24576\n\t"
    "ds_read_b64_tr_b16 v[94:95], %2 offset:25088\n\t"
    "s_waitcnt lgkmcnt(0)"
    : "=&{v[64:95]}"(w) : "v"(a0), "v"(a1) : "memory");
}
template <> __device__ __forceinline__ void qk_wload<1>(u32x32& w, unsigned a0, unsigned a1) {
  asm volatile("ds_read_b64_tr_b16 v[96:97], %1 offset:0\n\t"
    "ds_read_b64_tr_b16 v[98:99], %1 offset:512\n\t"
    "ds_read_b64_tr_b16 v[100:101], %1 offset:8192\n\t"
    "ds_read_b64_tr_b16 v[102:103], %1 offset:8704\n\t"
    "ds_read_b64_tr_b16 v[104:105], %2 offset:0\n\t"
    "ds_read_b64_tr_b16 v[106:107], %2 offset:512\n\t"
    "ds_read_b64_tr_b16 v[108:109], %2 offset:8192\n\t"
    "ds_read_b64_tr_b16 v[110:111], %2 offset:8704\n\t"
    "s_waitcnt lgkmcnt(0)\n\t"
    "ds_read_b64_tr_b16 v[112:113], %1 offset:16384\n\t"
    "ds_read_b64_tr_b16 v[114:115], %1 offset:16896\n\t"
    "ds_read_b64_tr_b16 v[116:117], %1 offset:24576\n\t"
    "ds_read_b64_tr_b16 v[118:119], %1 offset:25088\n\t"
    "ds_read_b64_tr_b16 v[120:121], %2 offset:16384\n\t"
    "ds_read_b64_tr_b16 v[122:123], %2 offset:16896\n\t"
    "ds_read_b64_tr_b16 v[124:125], %2 offset:24576\n\t"
    "ds_read_b64_tr_b16 v[126:127], %2 offset:25088\n\t"
    "s_waitcnt lgkmcnt(0)"
    : "=&{v[96:127]}"(w) : "v"(a0), "v"(a1) : "memory");
}
template <> __device__ __forceinline__ void qk_wload<2>(u32x32& w, unsigned a0, unsigned a1) {
  asm volatile("ds_read_b64_tr_b16 v[128:129], %1 offset:0\n\t"
    "ds_read_b64_tr_b16 v[130:131], %1 offset:512\n\t"
    "ds_read_b64_tr_b16 v[132:133], %1 offset:8192\n\t"
    "ds_read_b64_tr_b16 v[134:135], %1 offset:8704\n\t"
    "ds_read_b64_tr_b16 v[136:137], %2 offset:0\n\t"
    "ds_read_b64_tr_b16 v[138:139], %2 offset:512\n\t"
    "ds_read_b64_tr_b16 v[140:141], %2 offset:8192\n\t"
    "ds_read_b64_tr_b16 v[142:143], %2 offset:8704\n\t"
    "s_waitcnt lgkmcnt(0)\n\t"
    "ds_read_b64_tr_b16 v[144:145], %1 offset:16384\n\t"
    "ds_read_b64_tr_b16 v[146:147], %1 offset:16896\n\t"
    "ds_read_b64_tr_b16 v[148:149], %1 offset:24576\n\t"
    "ds_read_b64_tr_b16 v[150:151], %1 offset:25088\n\t"
    "ds_read_b64_tr_b16 v[152:153], %2 offset:16384\n\t"
    "ds_read_b64_tr_b16 v[154:155], %2 offset:16896\n\t"
    "ds_read_b64_tr_b16 v[156:157], %2 offset:24576\n\t"
    "ds_read_b64_tr_b16 v[158:159], %2 offset:25088\n\t"
    "s_waitcnt lgkmcnt(0)"
    : "=&{v[128:159]}"(w) : "v"(a0), "v"(a1) : "memory");
}
template <> __device__ __forceinline__ void qk_wload<3>(u32x32& w, unsigned a0, unsigned a1) {
  asm volatile("ds_read_b64_tr_b16 v[160:161], %1 offset:0\n\t"
    "ds_read_b64_tr_b16 v[162:163], %1 offset:512\n\t"
    "ds_read_b64_tr_b16 v[164:165], %1 offset:8192\n\t"
    "ds_read_b64_tr_b16 v[166:167], %1 offset:8704\n\t"
    "ds_read_b64_tr_b16 v[168:169], %2 offset:0\n\t"
    "ds_read_b64_tr_b16 v[170:171], %2 offset:512\n\t"
    "ds_read_b64_tr_b16 v[172:173], %2 offset:8192\n\t"
    "ds_read_b64_tr_b16 v[174:175], %2 offset:8704\n\t"
    "s_waitcnt lgkmcnt(0)\n\t"
    "ds_read_b64_tr_b16 v[176:177], %1 offset:16384\n\t"
    "ds_read_b64_tr_b16 v[178:179], %1 offset:16896\n\t"
    "ds_read_b64_tr_b16 v[180:181], %1 offset:24576\n\t"
    "ds_read_b64_tr_b16 v[182:183], %1 offset:25088\n\t"
    "ds_read_b64_tr_b16 v[184:185], %2 offset:16384\n\t"
    "ds_read_b64_tr_b16 v[186:187], %2 offset:16896\n\t"
    "ds_read_b64_tr_b16 v[188:189], %2 offset:24576\n\t"
    "ds_read_b64_tr_b16 v[190:191], %2 offset:25088\n\t"
    "s_waitcnt lgkmcnt(0)"
    : "=&{v[160:191]}"(w) : "v"(a0), "v"(a1) : "memory");
}
template <> __device__ __forceinline__ void qk_wload<4>(u32x32& w, unsigned a0, unsigned a1) {
  asm volatile("ds_read_b64_tr_b16 v[192:193], %1 offset:0\n\t"
    "ds_read_b64_tr_b16 v[194:195], %1 offset:512\n\t"
    "ds_read_b64_tr_b16 v[196:197], %1 offset:8192\n\t"
    "ds_read_b64_tr_b16 v[198:199], %1 offset:8704\n\t"
    "ds_read_b64_tr_b16 v[200:201], %2 offset:0\n\t"
    "ds_read_b64_tr_b16 v[202:203], %2 offset:512\n\t"
    "ds_read_b64_tr_b16 v[204:205], %2 offset:8192\n\t"
    "ds_read_b64_tr_b16 v[206:207], %2 offset:8704\n\t"
    "s_waitcnt lgkmcnt(0)\n\t"
    "ds_read_b64_tr_b16 v[208:209], %1 offset:16384\n\t"
    "ds_read_b64_tr_b16 v[210:211], %1 offset:16896\n\t"
    "ds_read_b64_tr_b16 v[212:213], %1 offset:24576\n\t"
    "ds_read_b64_tr_b16 v[214:215], %1 offset:25088\n\t"
    "ds_read_b64_tr_b16 v[216:217], %2 offset:16384\n\t"
    "ds_read_b64_tr_b16 v[218:219], %2 offset:16896\n\t"
    "ds_read_b64_tr_b16 v[220:221], %2 offset:24576\n\t"
    "ds_read_b64_tr_b16 v[222:223], %2 offset:25088\n\t"
    "s_waitcnt lgkmcnt(0)"
    : "=&{v[192:223]}"(w) : "v"(a0), "v"(a1) : "memory");
}
template <> __device__ __forceinline__ void qk_wload<5>(u32x32& w, unsigned a0, unsigned a1) {
  asm volatile("ds_read_b64_tr_b16 v[224:225], %1 offset:0\n\t"
    "ds_read_b64_tr_b16 v[226:227], %1 offset:512\n\t"
    "ds_read_b64_tr_b16 v[228:229], %1 offset:8192\n\t"
    "ds_read_b64_tr_b16 v[230:231], %1 offset:8704\n\t"
    "ds_read_b64_tr_b16 v[232:233], %2 offset:0\n\t"
    "ds_read_b64_tr_b16 v[234:235], %2 offset:512\n\t"
    "ds_read_b64_tr_b16 v[236:237], %2 offset:8192\n\t"
    "ds_read_b64_tr_b16 v[238:239], %2 offset:8704\n\t"
    "s_waitcnt lgkmcnt(0)\n\t"
    "ds_read_b64_tr_b16 v[240:241], %1 offset:16384\n\t"
    "ds_read_b64_tr_b16 v[242:243], %1 offset:16896\n\t"
    "ds_read_b64_tr_b16 v[244:245], %1 offset:24576\n\t"
    "ds_read_b64_tr_b16 v[246:247], %1 offset:25088\n\t"
    "ds_read_b64_tr_b16 v[248:249], %2 offset:16384\n\t"
    "ds_read_b64_tr_b16 v[250:251], %2 offset:16896\n\t"
    "ds_read_b64_tr_b16 v[252:253], %2 offset:24576\n\t"
    "ds_read_b64_tr_b16 v[254:255], %2 offset:25088\n\t"
    "s_waitcnt lgkmcnt(0)"
    : "=&{v[224:255]}"(w) : "v"(a0), "v"(a1) : "memory");
}
#define OREP 2
#define AREP 2
constexpr int V_P1 = 131072, V_PV = 149504, V_P2 = 157696;
struct VRW { u32x32 k0, k1, o0, o1, o2, o3; };
__device__ __forceinline__ void vr_v0(const VRW& W, const char* ag, const char* vd, unsigned wl, unsigned wa, unsigned wb, unsigned ra, unsigned& cap, unsigned& sp, unsigned long long& tc, unsigned long long* ts) {
  asm volatile("s_mov_b32 %[sp], 0\n\t"
    "v_mbcnt_lo_u32_b32 v20, -1, 0\n\t"
    "v_mbcnt_hi_u32_b32 v20, -1, v20\n\t"
    "v_lshlrev_b32 v21, 4, v20\n\t"
    "global_load_dwordx4 v[24:27], v21, %[ag] sc1\n\t"
    "global_load_dwordx4 v[28:31], v21, %[ag] offset:1024 sc1\n\t"
    "s_memtime %[tc]\n\t"
    "v_lshl_add_u32 v6, v20, 4, %[wl]\n\t"
    "ds_read_b128 v[32:35], v6 offset:0\n\t"
    "ds_read_b128 v[36:39], v6 offset:1024\n\t"
    "ds_read_b128 v[40:43], v6 offset:2048\n\t"
    "ds_read_b128 v[44:47], v6 offset:3072\n\t"
    "ds_read_b128 v[48:51], v6 offset:4096\n\t"
    "ds_read_b128 v[52:55], v6 offset:5120\n\t"
    "ds_read_b128 v[56:59], v6 offset:6144\n\t"
    "ds_read_b128 v[60:63], v6 offset:7168\n\t"
    "s_setprio 2\n\t"
    "v_and_b32 v6, 2, v20\n\t"
    "v_mul_u32_u24 v6, 0x5555, v6\n\t"
    "v_or_b32 v4, %[c44], v6\n\t"
    "v_and_b32 v6, 15, v20\n\t"
    "v_lshl_add_u32 v5, v6, 2, %[wa]\n\t"
    "v_and_b32 v6, 48, v20\n\t"
    "v_mad_u32_u24 v5, v6, 36, v5\n\t"
    "v_mul_u32_u24 v7, 36, v6\n\t"
    "v_and_b32 v6, 4, v20\n\t"
    "v_mul_u32_u24 v6, 72, v6\n\t"
    "v_add_u32 v7, v7, v6\n\t"
    "v_and_b32 v6, 8, v20\n\t"
    "v_lshl_add_u32 v7, v6, 4, v7\n\t"
    "v_and_b32 v6, 3, v20\n\t"
    "v_lshl_add_u32 v7, v6, 3, v7\n\t"
    "v_add_u32 v7, %[ra], v7\n\t"
    "3:\n\t"
    "v_mov_b32 v8, 0\n\t"
    "v_mov_b32 v9, 0\n\t"
    "v_mov_b32 v10, 0\n\t"
    "v_mov_b32 v11, 0\n\t"
    "v_mov_b32 v12, 0\n\t"
    "v_mov_b32 v13, 0\n\t"
    "v_mov_b32 v14, 0\n\t"
    "v_mov_b32 v15, 0\n\t"
    "v_mov_b32 v16, 0\n\t"
    "v_mov_b32 v17, 0\n\t"
    "v_mov_b32 v18, 0\n\t"
    "v_mov_b32 v19, 0\n\t"
    "v_mov_b32 v20, 0\n\t"
    "v_mov_b32 v21, 0\n\t"
    "v_mov_b32 v22, 0\n\t"
    "v_mov_b32 v23, 0\n\t"
    "s_waitcnt vmcnt(0) lgkmcnt(0)\n\t"
    "v_smfmac_f32_16x16x64_bf16 v[8:11], v[24:27], v[64:71], v4\n\t"
    "v_smfmac_f32_16x16x64_bf16 v[8:11], v[28:31], v[72:79], v4\n\t"
    "v_max3_u32 v6, v24, v25, v26\n\t"
    "v_max3_u32 v6, v6, v27, v28\n\t"
    "v_smfmac_f32_16x16x64_bf16 v[12:15], v[24:27], v[80:87], v4\n\t"
    "v_max3_u32 v6, v6, v29, v30\n\t"
    "v_max_u32 v6, v6, v31\n\t"
    "v_smfmac_f32_16x16x64_bf16 v[12:15], v[28:31], v[88:95], v4\n\t"
    "v_cmp_eq_u32 vcc, -1, v6\n\t"
    "s_cbranch_vccnz 1f\n\t"
    "2:\n\t"
    "v_smfmac_f32_16x16x64_bf16 v[16:19], v[24:27], v[96:103], v4\n\t"
    "v_add_f32 v8, v8, v10\n\t"
    "v_add_f32 v9, v9, v11\n\t"
    "ds_write2_b32 v5, v8, v9 offset0:0 offset1:72\n\t"
    "v_smfmac_f32_16x16x64_bf16 v[16:19], v[28:31], v[104:111], v4\n\t"
    "v_mov_b32 v8, 0\n\t"
    "v_mov_b32 v9, 0\n\t"
    "v_smfmac_f32_16x16x64_bf16 v[20:23], v[24:27], v[112:119], v4\n\t"
    "v_add_f32 v12, v12, v14\n\t"
    "v_add_f32 v13, v13, v15\n\t"
    "ds_write2_b32 v5, v12, v13 offset0:16 offset1:88\n\t"
    "v_smfmac_f32_16x16x64_bf16 v[20:23], v[28:31], v[120:127], v4\n\t"
    "v_mov_b32 v10, 0\n\t"
    "v_mov_b32 v11, 0\n\t"
    "v_add_f32 v16, v16, v18\n\t"
    "v_add_f32 v17, v17, v19\n\t"
    "ds_write2_b32 v5, v16, v17 offset0:32 offset1:104\n\t"
    "v_mov_b32 v12, 0\n\t"
    "v_mov_b32 v13, 0\n\t"
    "v_mov_b32 v14, 0\n\t"
    "v_mov_b32 v15, 0\n\t"
    "v_add_f32 v20, v20, v22\n\t"
    "v_add_f32 v21, v21, v23\n\t"
    "ds_write2_b32 v5, v20, v21 offset0:48 offset1:120\n\t"
    "s_setprio 1\n\t"
    "v_smfmac_f32_16x16x64_bf16 v[8:11], v[24:27], v[32:39], v4\n\t"
    "v_smfmac_f32_16x16x64_bf16 v[8:11], v[28:31], v[40:47], v4\n\t"
    "v_smfmac_f32_16x16x64_bf16 v[12:15], v[24:27], v[48:55], v4\n\t"
    "v_smfmac_f32_16x16x64_bf16 v[12:15], v[28:31], v[56:63], v4\n\t"
    "v_mbcnt_lo_u32_b32 v17, -1, 0\n\t"
    "v_mbcnt_hi_u32_b32 v17, -1, v17\n\t"
    "v_lshl_add_u32 v16, v17, 3, %[wb]\n\t"
    "v_lshlrev_b32 v17, 2, v17\n\t"
    "s_nop 1\n\t"
    "v_add_f32 v8, v8, v10\n\t"
    "v_add_f32 v9, v9, v11\n\t"
    "v_add_f32 v12, v12, v14\n\t"
    "v_add_f32 v13, v13, v15\n\t"
    "ds_write2st64_b64 v16, v[8:9], v[12:13] offset1:1\n\t"
    "s_setprio 3\n\t"
    "s_waitcnt lgkmcnt(1)\n\t"
    "s_barrier\n\t"
    "ds_read_b64 v[32:33], v7 offset:0\n\t"
    "ds_read_b64 v[34:35], v7 offset:2304\n\t"
    "ds_read_b64 v[36:37], v7 offset:4608\n\t"
    "ds_read_b64 v[38:39], v7 offset:6912\n\t"
    "ds_read_b64 v[40:41], v7 offset:9216\n\t"
    "ds_read_b64 v[42:43], v7 offset:11520\n\t"
    "ds_read_b64 v[44:45], v7 offset:13824\n\t"
    "ds_read_b64 v[46:47], v7 offset:16128\n\t"
    "s_waitcnt lgkmcnt(6)\n\t"
    "v_pk_add_f32 v[32:33], v[32:33], v[34:35]\n\t"
    "s_waitcnt lgkmcnt(4)\n\t"
    "v_pk_add_f32 v[36:37], v[36:37], v[38:39]\n\t"
    "v_pk_add_f32 v[32:33], v[32:33], v[36:37]\n\t"
    "s_waitcnt lgkmcnt(2)\n\t"
    "v_pk_add_f32 v[40:41], v[40:41], v[42:43]\n\t"
    "s_waitcnt lgkmcnt(0)\n\t"
    "v_pk_add_f32 v[44:45], v[44:45], v[46:47]\n\t"
    "v_pk_add_f32 v[40:41], v[40:41], v[44:45]\n\t"
    "v_pk_add_f32 v[32:33], v[32:33], v[40:41]\n\t"
    "v_cvt_pk_bf16_f32 v6, v32, v33\n\t"
    "v_min_u32 v6, -2, v6\n\t"
    "global_store_dword v17, v6, %[vd] sc1\n\t"
    "s_barrier\n\t"
    "s_setprio 2\n\t"
    "s_waitcnt lgkmcnt(0)\n\t"
    "s_branch 9f\n\t"
    "1:\n\t"
    "s_add_u32 %[sp], %[sp], 1\n\t"
    "s_cmp_ge_u32 %[sp], %[cap]\n\t"
    "s_cbranch_scc1 7f\n\t"
    "v_mbcnt_lo_u32_b32 v20, -1, 0\n\t"
    "v_mbcnt_hi_u32_b32 v20, -1, v20\n\t"
    "v_lshlrev_b32 v21, 4, v20\n\t"
    "global_load_dwordx4 v[24:27], v21, %[ag] sc1\n\t"
    "global_load_dwordx4 v[28:31], v21, %[ag] offset:1024 sc1\n\t"
    "s_waitcnt lgkmcnt(0)\n\t"
    "s_memtime %[tc]\n\t"
    "s_nop 7\n\t"
    "s_branch 3b\n\t"
    "7:\n\t"
    "s_mov_b32 %[cap], 0\n\t"
    "s_branch 2b\n\t"
    "9:"
    : [tc] "=&s"(tc), [sp] "=&s"(sp), [cap] "+s"(cap)
    : [ag] "s"(ag), [vd] "s"(vd), [wl] "s"(wl), [wa] "s"(wa), [wb] "s"(wb), [ra] "s"(ra), [c44] "s"(0x4444u), "{v[64:95]}"(W.k0), "{v[96:127]}"(W.k1), "{v[128:159]}"(W.o0), "{v[160:191]}"(W.o1), "{v[192:223]}"(W.o2), "{v[224:255]}"(W.o3)
    : "v4", "v5", "v6", "v7", "v8", "v9", "v10", "v11", "v12", "v13", "v14", "v15", "v16", "v17", "v18", "v19", "v20", "v21", "v22", "v23", "v24", "v25", "v26", "v27", "v28", "v29", "v30", "v31", "v32", "v33", "v34", "v35", "v36", "v37", "v38", "v39", "v40", "v41", "v42", "v43", "v44", "v45", "v46", "v47", "v48", "v49", "v50", "v51", "v52", "v53", "v54", "v55", "v56", "v57", "v58", "v59", "v60", "v61", "v62", "v63", "vcc", "scc", "memory");
}
__device__ __forceinline__ void vr_v1(const VRW& W, const char* ag, const char* vd, unsigned wl, unsigned wa, unsigned wb, unsigned ra, unsigned& cap, unsigned& sp, unsigned long long& tc, unsigned long long* ts) {
  asm volatile("s_mov_b32 %[sp], 0\n\t"
    "v_mbcnt_lo_u32_b32 v20, -1, 0\n\t"
    "v_mbcnt_hi_u32_b32 v20, -1, v20\n\t"
    "v_lshlrev_b32 v21, 4, v20\n\t"
    "global_load_dwordx4 v[24:27], v21, %[ag] sc1\n\t"
    "global_load_dwordx4 v[28:31], v21, %[ag] offset:1024 sc1\n\t"
    "s_memtime %[tc]\n\t"
    "v_lshl_add_u32 v6, v20, 4, %[wl]\n\t"
    "ds_read_b128 v[32:35], v6 offset:0\n\t"
    "ds_read_b128 v[36:39], v6 offset:1024\n\t"
    "ds_read_b128 v[40:43], v6 offset:2048\n\t"
    "ds_read_b128 v[44:47], v6 offset:3072\n\t"
    "ds_read_b128 v[48:51], v6 offset:4096\n\t"
    "ds_read_b128 v[52:55], v6 offset:5120\n\t"
    "ds_read_b128 v[56:59], v6 offset:6144\n\t"
    "ds_read_b128 v[60:63], v6 offset:7168\n\t"
    "s_setprio 3\n\t"
    "v_and_b32 v6, 2, v20\n\t"
    "v_mul_u32_u24 v6, 0x5555, v6\n\t"
    "v_or_b32 v4, %[c44], v6\n\t"
    "v_and_b32 v6, 15, v20\n\t"
    "v_lshl_add_u32 v5, v6, 2, %[wa]\n\t"
    "v_and_b32 v6, 48, v20\n\t"
    "v_mad_u32_u24 v5, v6, 36, v5\n\t"
    "v_and_b32 v6, 32, v20\n\t"
    "v_mul_u32_u24 v7, 120, v6\n\t"
    "v_lshl_add_u32 v7, v20, 3, v7\n\t"
    "v_add_u32 v7, %[ra], v7\n\t"
    "3:\n\t"
    "v_mov_b32 v8, 0\n\t"
    "v_mov_b32 v9, 0\n\t"
    "v_mov_b32 v10, 0\n\t"
    "v_mov_b32 v11, 0\n\t"
    "v_mov_b32 v12, 0\n\t"
    "v_mov_b32 v13, 0\n\t"
    "v_mov_b32 v14, 0\n\t"
    "v_mov_b32 v15, 0\n\t"
    "v_mov_b32 v16, 0\n\t"
    "v_mov_b32 v17, 0\n\t"
    "v_mov_b32 v18, 0\n\t"
    "v_mov_b32 v19, 0\n\t"
    "v_mov_b32 v20, 0\n\t"
    "v_mov_b32 v21, 0\n\t"
    "v_mov_b32 v22, 0\n\t"
    "v_mov_b32 v23, 0\n\t"
    "s_waitcnt vmcnt(0) lgkmcnt(0)\n\t"
    "v_smfmac_f32_16x16x64_bf16 v[8:11], v[24:27], v[64:71], v4\n\t"
    "v_smfmac_f32_16x16x64_bf16 v[8:11], v[28:31], v[72:79], v4\n\t"
    "v_max3_u32 v6, v24, v25, v26\n\t"
    "v_max3_u32 v6, v6, v27, v28\n\t"
    "v_smfmac_f32_16x16x64_bf16 v[12:15], v[24:27], v[80:87], v4\n\t"
    "v_max3_u32 v6, v6, v29, v30\n\t"
    "v_max_u32 v6, v6, v31\n\t"
    "v_smfmac_f32_16x16x64_bf16 v[12:15], v[28:31], v[88:95], v4\n\t"
    "v_cmp_eq_u32 vcc, -1, v6\n\t"
    "s_cbranch_vccnz 1f\n\t"
    "2:\n\t"
    "v_smfmac_f32_16x16x64_bf16 v[16:19], v[24:27], v[96:103], v4\n\t"
    "v_add_f32 v8, v8, v10\n\t"
    "v_add_f32 v9, v9, v11\n\t"
    "ds_write2_b32 v5, v8, v9 offset0:0 offset1:72\n\t"
    "v_smfmac_f32_16x16x64_bf16 v[16:19], v[28:31], v[104:111], v4\n\t"
    "v_mov_b32 v8, 0\n\t"
    "v_mov_b32 v9, 0\n\t"
    "v_smfmac_f32_16x16x64_bf16 v[20:23], v[24:27], v[112:119], v4\n\t"
    "v_add_f32 v12, v12, v14\n\t"
    "v_add_f32 v13, v13, v15\n\t"
    "ds_write2_b32 v5, v12, v13 offset0:16 offset1:88\n\t"
    "v_smfmac_f32_16x16x64_bf16 v[20:23], v[28:31], v[120:127], v4\n\t"
    "v_mov_b32 v10, 0\n\t"
    "v_mov_b32 v11, 0\n\t"
    "v_add_f32 v16, v16, v18\n\t"
    "v_add_f32 v17, v17, v19\n\t"
    "ds_write2_b32 v5, v16, v17 offset0:32 offset1:104\n\t"
    "v_mov_b32 v12, 0\n\t"
    "v_mov_b32 v13, 0\n\t"
    "v_mov_b32 v14, 0\n\t"
    "v_mov_b32 v15, 0\n\t"
    "v_add_f32 v20, v20, v22\n\t"
    "v_add_f32 v21, v21, v23\n\t"
    "ds_write2_b32 v5, v20, v21 offset0:48 offset1:120\n\t"
    "s_waitcnt lgkmcnt(0)\n\t"
    "s_barrier\n\t"
    "v_smfmac_f32_16x16x64_bf16 v[8:11], v[24:27], v[32:39], v4\n\t"
    "v_smfmac_f32_16x16x64_bf16 v[8:11], v[28:31], v[40:47], v4\n\t"
    "v_smfmac_f32_16x16x64_bf16 v[12:15], v[24:27], v[48:55], v4\n\t"
    "v_smfmac_f32_16x16x64_bf16 v[12:15], v[28:31], v[56:63], v4\n\t"
    "v_mbcnt_lo_u32_b32 v17, -1, 0\n\t"
    "v_mbcnt_hi_u32_b32 v17, -1, v17\n\t"
    "v_lshl_add_u32 v16, v17, 3, %[wb]\n\t"
    "v_lshlrev_b32 v17, 2, v17\n\t"
    "s_nop 1\n\t"
    "v_add_f32 v8, v8, v10\n\t"
    "v_add_f32 v9, v9, v11\n\t"
    "v_add_f32 v12, v12, v14\n\t"
    "v_add_f32 v13, v13, v15\n\t"
    "ds_write2st64_b64 v16, v[8:9], v[12:13] offset1:1\n\t"
    "s_waitcnt lgkmcnt(0)\n\t"
    "s_barrier\n\t"
    "ds_read_b64 v[32:33], v7 offset:0\n\t"
    "ds_read_b64 v[34:35], v7 offset:1024\n\t"
    "ds_read_b64 v[36:37], v7 offset:2048\n\t"
    "ds_read_b64 v[38:39], v7 offset:3072\n\t"
    "s_waitcnt lgkmcnt(2)\n\t"
    "v_pk_add_f32 v[32:33], v[32:33], v[34:35]\n\t"
    "s_waitcnt lgkmcnt(0)\n\t"
    "v_pk_add_f32 v[36:37], v[36:37], v[38:39]\n\t"
    "v_pk_add_f32 v[32:33], v[32:33], v[36:37]\n\t"
    "s_nop 1\n\t"
    "v_permlane32_swap_b32 v32, v34\n\t"
    "v_permlane32_swap_b32 v33, v35\n\t"
    "s_mov_b32 exec_hi, 0\n\t"
    "v_pk_add_f32 v[32:33], v[32:33], v[34:35]\n\t"
    "v_cvt_pk_bf16_f32 v6, v32, v33\n\t"
    "v_min_u32 v6, -2, v6\n\t"
    "global_store_dword v17, v6, %[vd] sc1\n\t"
    "s_mov_b32 exec_hi, -1\n\t"
    "s_setprio 2\n\t"
    "s_waitcnt lgkmcnt(0)\n\t"
    "s_branch 9f\n\t"
    "1:\n\t"
    "s_add_u32 %[sp], %[sp], 1\n\t"
    "s_cmp_ge_u32 %[sp], %[cap]\n\t"
    "s_cbranch_scc1 7f\n\t"
    "v_mbcnt_lo_u32_b32 v20, -1, 0\n\t"
    "v_mbcnt_hi_u32_b32 v20, -1, v20\n\t"
    "v_lshlrev_b32 v21, 4, v20\n\t"
    "global_load_dwordx4 v[24:27], v21, %[ag] sc1\n\t"
    "global_load_dwordx4 v[28:31], v21, %[ag] offset:1024 sc1\n\t"
    "s_waitcnt lgkmcnt(0)\n\t"
    "s_memtime %[tc]\n\t"
    "s_nop 7\n\t"
    "s_branch 3b\n\t"
    "7:\n\t"
    "s_mov_b32 %[cap], 0\n\t"
    "s_branch 2b\n\t"
    "9:"
    : [tc] "=&s"(tc), [sp] "=&s"(sp), [cap] "+s"(cap)
    : [ag] "s"(ag), [vd] "s"(vd), [wl] "s"(wl), [wa] "s"(wa), [wb] "s"(wb), [ra] "s"(ra), [c44] "s"(0x4444u), "{v[64:95]}"(W.k0), "{v[96:127]}"(W.k1), "{v[128:159]}"(W.o0), "{v[160:191]}"(W.o1), "{v[192:223]}"(W.o2), "{v[224:255]}"(W.o3)
    : "v4", "v5", "v6", "v7", "v8", "v9", "v10", "v11", "v12", "v13", "v14", "v15", "v16", "v17", "v18", "v19", "v20", "v21", "v22", "v23", "v24", "v25", "v26", "v27", "v28", "v29", "v30", "v31", "v32", "v33", "v34", "v35", "v36", "v37", "v38", "v39", "v40", "v41", "v42", "v43", "v44", "v45", "v46", "v47", "v48", "v49", "v50", "v51", "v52", "v53", "v54", "v55", "v56", "v57", "v58", "v59", "v60", "v61", "v62", "v63", "vcc", "scc", "memory");
}
__device__ __forceinline__ void vr_r(const VRW& W, const char* os, const char* ad, unsigned wa, unsigned& cap, unsigned& sp, unsigned long long* ts) {
  asm volatile("v_mbcnt_lo_u32_b32 v4, -1, 0\n\t"
    "v_mbcnt_hi_u32_b32 v4, -1, v4\n\t"
    "v_lshlrev_b32 v5, 4, v4\n\t"
    "global_load_dwordx4 v[12:15], v5, %[os] offset:0 sc1\n\t"
    "global_load_dwordx4 v[16:19], v5, %[os] offset:1024 sc1\n\t"
    "global_load_dwordx4 v[20:23], v5, %[os] offset:2048 sc1\n\t"
    "global_load_dwordx4 v[24:27], v5, %[os] offset:3072 sc1\n\t"
    "s_mov_b32 %[sp], 0\n\t"
    "v_and_b32 v10, 1, v4\n\t"
    "v_mul_u32_u24 v10, 0xaaaa, v10\n\t"
    "v_or_b32 v6, %[c44], v10\n\t"
    "v_and_b32 v10, 15, v4\n\t"
    "v_lshl_add_u32 v7, v10, 2, %[wa]\n\t"
    "v_and_b32 v10, 48, v4\n\t"
    "v_mad_u32_u24 v7, v10, 12, v7\n\t"
    "v_and_b32 v10, 16, v4\n\t"
    "v_mul_u32_u24 v8, 24, v10\n\t"
    "v_and_b32 v10, 4, v4\n\t"
    "v_mad_u32_u24 v8, v10, 48, v8\n\t"
    "v_and_b32 v10, 32, v4\n\t"
    "v_lshl_add_u32 v8, v10, 1, v8\n\t"
    "v_and_b32 v10, 8, v4\n\t"
    "v_lshl_add_u32 v8, v10, 2, v8\n\t"
    "v_and_b32 v10, 3, v4\n\t"
    "v_lshl_add_u32 v8, v10, 3, v8\n\t"
    "v_add_u32 v8, 157696, v8\n\t"
    "v_and_b32 v10, 32, v4\n\t"
    "v_add_u32 v9, v10, v4\n\t"
    "v_lshlrev_b32 v9, 2, v9\n\t"
    "3:\n\t"
    "v_mov_b32 v28, 0\n\t"
    "v_mov_b32 v29, 0\n\t"
    "v_mov_b32 v30, 0\n\t"
    "v_mov_b32 v31, 0\n\t"
    "v_mov_b32 v32, 0\n\t"
    "v_mov_b32 v33, 0\n\t"
    "v_mov_b32 v34, 0\n\t"
    "v_mov_b32 v35, 0\n\t"
    "v_mov_b32 v36, 0\n\t"
    "v_mov_b32 v37, 0\n\t"
    "v_mov_b32 v38, 0\n\t"
    "v_mov_b32 v39, 0\n\t"
    "v_mov_b32 v40, 0\n\t"
    "v_mov_b32 v41, 0\n\t"
    "v_mov_b32 v42, 0\n\t"
    "v_mov_b32 v43, 0\n\t"
    "v_mov_b32 v10, 0\n\t"
    "s_waitcnt vmcnt(3)\n\t"
    "v_smfmac_f32_16x16x64_bf16 v[28:31], v[12:15], v[128:135], v6\n\t"
    "v_max3_u32 v10, v10, v12, v13\n\t"
    "v_smfmac_f32_16x16x64_bf16 v[32:35], v[12:15], v[136:143], v6\n\t"
    "v_max3_u32 v10, v10, v14, v15\n\t"
    "v_smfmac_f32_16x16x64_bf16 v[36:39], v[12:15], v[144:151], v6\n\t"
    "v_smfmac_f32_16x16x64_bf16 v[40:43], v[12:15], v[152:159], v6\n\t"
    "s_waitcnt vmcnt(2)\n\t"
    "v_smfmac_f32_16x16x64_bf16 v[28:31], v[16:19], v[160:167], v6\n\t"
    "v_max3_u32 v10, v10, v16, v17\n\t"
    "v_smfmac_f32_16x16x64_bf16 v[32:35], v[16:19], v[168:175], v6\n\t"
    "v_max3_u32 v10, v10, v18, v19\n\t"
    "v_smfmac_f32_16x16x64_bf16 v[36:39], v[16:19], v[176:183], v6\n\t"
    "v_smfmac_f32_16x16x64_bf16 v[40:43], v[16:19], v[184:191], v6\n\t"
    "s_waitcnt vmcnt(1)\n\t"
    "v_smfmac_f32_16x16x64_bf16 v[28:31], v[20:23], v[192:199], v6\n\t"
    "v_max3_u32 v10, v10, v20, v21\n\t"
    "v_smfmac_f32_16x16x64_bf16 v[32:35], v[20:23], v[200:207], v6\n\t"
    "v_max3_u32 v10, v10, v22, v23\n\t"
    "v_smfmac_f32_16x16x64_bf16 v[36:39], v[20:23], v[208:215], v6\n\t"
    "v_smfmac_f32_16x16x64_bf16 v[40:43], v[20:23], v[216:223], v6\n\t"
    "s_waitcnt vmcnt(0)\n\t"
    "v_smfmac_f32_16x16x64_bf16 v[28:31], v[24:27], v[224:231], v6\n\t"
    "v_max3_u32 v10, v10, v24, v25\n\t"
    "v_smfmac_f32_16x16x64_bf16 v[32:35], v[24:27], v[232:239], v6\n\t"
    "v_max3_u32 v10, v10, v26, v27\n\t"
    "v_smfmac_f32_16x16x64_bf16 v[36:39], v[24:27], v[240:247], v6\n\t"
    "v_cmp_eq_u32 vcc, -1, v10\n\t"
    "v_smfmac_f32_16x16x64_bf16 v[40:43], v[24:27], v[248:255], v6\n\t"
    "s_cbranch_vccnz 4f\n\t"
    "5:\n\t"
    "s_cmp_ge_u32 %[wa], 159232\n\t"
    "s_nop 1\n\t"
    "v_add_f32 v28, v28, v29\n\t"
    "v_add_f32 v32, v32, v33\n\t"
    "v_add_f32 v36, v38, v39\n\t"
    "v_add_f32 v28, v28, v36\n\t"
    "v_add_f32 v40, v42, v43\n\t"
    "v_add_f32 v32, v32, v40\n\t"
    "ds_write2_b32 v7, v28, v32 offset1:16\n\t"
    "s_waitcnt lgkmcnt(0)\n\t"
    "s_barrier\n\t"
    "s_cbranch_scc1 9f\n\t"
    "ds_read_b64 v[44:45], v8 offset:0\n\t"
    "ds_read_b64 v[46:47], v8 offset:768\n\t"
    "ds_read_b64 v[48:49], v8 offset:1536\n\t"
    "ds_read_b64 v[50:51], v8 offset:2304\n\t"
    "ds_read_b64 v[52:53], v8 offset:3072\n\t"
    "ds_read_b64 v[54:55], v8 offset:3840\n\t"
    "ds_read_b64 v[56:57], v8 offset:4608\n\t"
    "ds_read_b64 v[58:59], v8 offset:5376\n\t"
    "s_waitcnt lgkmcnt(6)\n\t"
    "v_pk_add_f32 v[44:45], v[44:45], v[46:47]\n\t"
    "s_waitcnt lgkmcnt(4)\n\t"
    "v_pk_add_f32 v[48:49], v[48:49], v[50:51]\n\t"
    "v_pk_add_f32 v[44:45], v[44:45], v[48:49]\n\t"
    "s_waitcnt lgkmcnt(2)\n\t"
    "v_pk_add_f32 v[52:53], v[52:53], v[54:55]\n\t"
    "s_waitcnt lgkmcnt(0)\n\t"
    "v_pk_add_f32 v[56:57], v[56:57], v[58:59]\n\t"
    "v_pk_add_f32 v[52:53], v[52:53], v[56:57]\n\t"
    "v_pk_add_f32 v[44:45], v[44:45], v[52:53]\n\t"
    "v_cvt_pk_bf16_f32 v10, v44, v45\n\t"
    "v_min_u32 v10, -2, v10\n\t"
    "global_store_dword v9, v10, %[ad] sc1\n\t"
    "s_waitcnt lgkmcnt(0)\n\t"
    "s_branch 9f\n\t"
    "4:\n\t"
    "s_add_u32 %[sp], %[sp], 1\n\t"
    "s_cmp_ge_u32 %[sp], %[cap]\n\t"
    "s_cbranch_scc1 7f\n\t"
    "global_load_dwordx4 v[12:15], v5, %[os] offset:0 sc1\n\t"
    "global_load_dwordx4 v[16:19], v5, %[os] offset:1024 sc1\n\t"
    "global_load_dwordx4 v[20:23], v5, %[os] offset:2048 sc1\n\t"
    "global_load_dwordx4 v[24:27], v5, %[os] offset:3072 sc1\n\t"
    "s_nop 3\n\t"
    "s_branch 3b\n\t"
    "7:\n\t"
    "s_mov_b32 %[cap], 0\n\t"
    "s_branch 5b\n\t"
    "9:"
    : [sp] "=&s"(sp), [cap] "+s"(cap)
    : [os] "s"(os), [ad] "s"(ad), [wa] "s"(wa), [c44] "s"(0x4444u), "{v[64:95]}"(W.k0), "{v[96:127]}"(W.k1), "{v[128:159]}"(W.o0), "{v[160:191]}"(W.o1), "{v[192:223]}"(W.o2), "{v[224:255]}"(W.o3)
    : "v4", "v5", "v6", "v7", "v8", "v9", "v10", "v11", "v12", "v13", "v14", "v15", "v16", "v17", "v18", "v19", "v20", "v21", "v22", "v23", "v24", "v25", "v26", "v27", "v28", "v29", "v30", "v31", "v32", "v33", "v34", "v35", "v36", "v37", "v38", "v39", "v40", "v41", "v42", "v43", "v44", "v45", "v46", "v47", "v48", "v49", "v50", "v51", "v52", "v53", "v54", "v55", "v56", "v57", "v58", "v59", "vcc", "scc", "memory");
}
__device__ __forceinline__ void kr_load_k0(VRW& W, unsigned a) {
  asm volatile("ds_read_b64_tr_b16 v[64:65], %[a] offset:0\n\t"
    "ds_read_b64_tr_b16 v[66:67], %[a] offset:256\n\t"
    "ds_read_b64_tr_b16 v[68:69], %[a] offset:4096\n\t"
    "ds_read_b64_tr_b16 v[70:71], %[a] offset:4352\n\t"
    "ds_read_b64_tr_b16 v[80:81], %[a] offset:32\n\t"
    "ds_read_b64_tr_b16 v[82:83], %[a] offset:288\n\t"
    "ds_read_b64_tr_b16 v[84:85], %[a] offset:4128\n\t"
    "ds_read_b64_tr_b16 v[86:87], %[a] offset:4384\n\t"
    "s_waitcnt lgkmcnt(0)\n\t"
    "ds_read_b64_tr_b16 v[96:97], %[a] offset:64\n\t"
    "ds_read_b64_tr_b16 v[98:99], %[a] offset:320\n\t"
    "ds_read_b64_tr_b16 v[100:101], %[a] offset:4160\n\t"
    "ds_read_b64_tr_b16 v[102:103], %[a] offset:4416\n\t"
    "ds_read_b64_tr_b16 v[112:113], %[a] offset:96\n\t"
    "ds_read_b64_tr_b16 v[114:115], %[a] offset:352\n\t"
    "ds_read_b64_tr_b16 v[116:117], %[a] offset:4192\n\t"
    "ds_read_b64_tr_b16 v[118:119], %[a] offset:4448\n\t"
    "s_waitcnt lgkmcnt(0)"
    : "=&{v[64:95]}"(W.k0), "=&{v[96:127]}"(W.k1) : [a] "v"(a) : "memory");
}
__device__ __forceinline__ void kr_load_k1(VRW& W, unsigned a) {
  asm volatile("ds_read_b64_tr_b16 v[72:73], %[a] offset:0\n\t"
    "ds_read_b64_tr_b16 v[74:75], %[a] offset:256\n\t"
    "ds_read_b64_tr_b16 v[76:77], %[a] offset:4096\n\t"
    "ds_read_b64_tr_b16 v[78:79], %[a] offset:4352\n\t"
    "ds_read_b64_tr_b16 v[88:89], %[a] offset:32\n\t"
    "ds_read_b64_tr_b16 v[90:91], %[a] offset:288\n\t"
    "ds_read_b64_tr_b16 v[92:93], %[a] offset:4128\n\t"
    "ds_read_b64_tr_b16 v[94:95], %[a] offset:4384\n\t"
    "s_waitcnt lgkmcnt(0)\n\t"
    "ds_read_b64_tr_b16 v[104:105], %[a] offset:64\n\t"
    "ds_read_b64_tr_b16 v[106:107], %[a] offset:320\n\t"
    "ds_read_b64_tr_b16 v[108:109], %[a] offset:4160\n\t"
    "ds_read_b64_tr_b16 v[110:111], %[a] offset:4416\n\t"
    "ds_read_b64_tr_b16 v[120:121], %[a] offset:96\n\t"
    "ds_read_b64_tr_b16 v[122:123], %[a] offset:352\n\t"
    "ds_read_b64_tr_b16 v[124:125], %[a] offset:4192\n\t"
    "ds_read_b64_tr_b16 v[126:127], %[a] offset:4448\n\t"
    "s_waitcnt lgkmcnt(0)"
    : "+&{v[64:95]}"(W.k0), "+&{v[96:127]}"(W.k1) : [a] "v"(a) : "memory");
}
__device__ __forceinline__ void kr_load_o0(VRW& W, unsigned a) {
  asm volatile("ds_read_b64_tr_b16 v[128:129], %[a] offset:0\n\t"
    "ds_read_b64_tr_b16 v[130:131], %[a] offset:128\n\t"
    "ds_read_b64_tr_b16 v[132:133], %[a] offset:1024\n\t"
    "ds_read_b64_tr_b16 v[134:135], %[a] offset:1152\n\t"
    "ds_read_b64_tr_b16 v[136:137], %[a] offset:32\n\t"
    "ds_read_b64_tr_b16 v[138:139], %[a] offset:160\n\t"
    "ds_read_b64_tr_b16 v[140:141], %[a] offset:1056\n\t"
    "ds_read_b64_tr_b16 v[142:143], %[a] offset:1184\n\t"
    "s_waitcnt lgkmcnt(0)\n\t"
    "ds_read_b64_tr_b16 v[144:145], %[a] offset:4096\n\t"
    "ds_read_b64_tr_b16 v[146:147], %[a] offset:4224\n\t"
    "ds_read_b64_tr_b16 v[148:149], %[a] offset:5120\n\t"
    "ds_read_b64_tr_b16 v[150:151], %[a] offset:5248\n\t"
    "ds_read_b64_tr_b16 v[152:153], %[a] offset:4128\n\t"
    "ds_read_b64_tr_b16 v[154:155], %[a] offset:4256\n\t"
    "ds_read_b64_tr_b16 v[156:157], %[a] offset:5152\n\t"
    "ds_read_b64_tr_b16 v[158:159], %[a] offset:5280\n\t"
    "s_waitcnt lgkmcnt(0)"
    : "=&{v[128:159]}"(W.o0) : [a] "v"(a) : "memory");
}
__device__ __forceinline__ void kr_load_o1(VRW& W, unsigned a) {
  asm volatile("ds_read_b64_tr_b16 v[160:161], %[a] offset:0\n\t"
    "ds_read_b64_tr_b16 v[162:163], %[a] offset:128\n\t"
    "ds_read_b64_tr_b16 v[164:165], %[a] offset:1024\n\t"
    "ds_read_b64_tr_b16 v[166:167], %[a] offset:1152\n\t"
    "ds_read_b64_tr_b16 v[168:169], %[a] offset:32\n\t"
    "ds_read_b64_tr_b16 v[170:171], %[a] offset:160\n\t"
    "ds_read_b64_tr_b16 v[172:173], %[a] offset:1056\n\t"
    "ds_read_b64_tr_b16 v[174:175], %[a] offset:1184\n\t"
    "s_waitcnt lgkmcnt(0)\n\t"
    "ds_read_b64_tr_b16 v[176:177], %[a] offset:4096\n\t"
    "ds_read_b64_tr_b16 v[178:179], %[a] offset:4224\n\t"
    "ds_read_b64_tr_b16 v[180:181], %[a] offset:5120\n\t"
    "ds_read_b64_tr_b16 v[182:183], %[a] offset:5248\n\t"
    "ds_read_b64_tr_b16 v[184:185], %[a] offset:4128\n\t"
    "ds_read_b64_tr_b16 v[186:187], %[a] offset:4256\n\t"
    "ds_read_b64_tr_b16 v[188:189], %[a] offset:5152\n\t"
    "ds_read_b64_tr_b16 v[190:191], %[a] offset:5280\n\t"
    "s_waitcnt lgkmcnt(0)"
    : "=&{v[160:191]}"(W.o1) : [a] "v"(a) : "memory");
}
__device__ __forceinline__ void kr_load_o2(VRW& W, unsigned a) {
  asm volatile("ds_read_b64_tr_b16 v[192:193], %[a] offset:0\n\t"
    "ds_read_b64_tr_b16 v[194:195], %[a] offset:128\n\t"
    "ds_read_b64_tr_b16 v[196:197], %[a] offset:1024\n\t"
    "ds_read_b64_tr_b16 v[198:199], %[a] offset:1152\n\t"
    "ds_read_b64_tr_b16 v[200:201], %[a] offset:32\n\t"
    "ds_read_b64_tr_b16 v[202:203], %[a] offset:160\n\t"
    "ds_read_b64_tr_b16 v[204:205], %[a] offset:1056\n\t"
    "ds_read_b64_tr_b16 v[206:207], %[a] offset:1184\n\t"
    "s_waitcnt lgkmcnt(0)\n\t"
    "ds_read_b64_tr_b16 v[208:209], %[a] offset:4096\n\t"
    "ds_read_b64_tr_b16 v[210:211], %[a] offset:4224\n\t"
    "ds_read_b64_tr_b16 v[212:213], %[a] offset:5120\n\t"
    "ds_read_b64_tr_b16 v[214:215], %[a] offset:5248\n\t"
    "ds_read_b64_tr_b16 v[216:217], %[a] offset:4128\n\t"
    "ds_read_b64_tr_b16 v[218:219], %[a] offset:4256\n\t"
    "ds_read_b64_tr_b16 v[220:221], %[a] offset:5152\n\t"
    "ds_read_b64_tr_b16 v[222:223], %[a] offset:5280\n\t"
    "s_waitcnt lgkmcnt(0)"
    : "=&{v[192:223]}"(W.o2) : [a] "v"(a) : "memory");
}
__device__ __forceinline__ void kr_load_o3(VRW& W, unsigned a) {
  asm volatile("ds_read_b64_tr_b16 v[224:225], %[a] offset:0\n\t"
    "ds_read_b64_tr_b16 v[226:227], %[a] offset:128\n\t"
    "ds_read_b64_tr_b16 v[228:229], %[a] offset:1024\n\t"
    "ds_read_b64_tr_b16 v[230:231], %[a] offset:1152\n\t"
    "ds_read_b64_tr_b16 v[232:233], %[a] offset:32\n\t"
    "ds_read_b64_tr_b16 v[234:235], %[a] offset:160\n\t"
    "ds_read_b64_tr_b16 v[236:237], %[a] offset:1056\n\t"
    "ds_read_b64_tr_b16 v[238:239], %[a] offset:1184\n\t"
    "s_waitcnt lgkmcnt(0)\n\t"
    "ds_read_b64_tr_b16 v[240:241], %[a] offset:4096\n\t"
    "ds_read_b64_tr_b16 v[242:243], %[a] offset:4224\n\t"
    "ds_read_b64_tr_b16 v[244:245], %[a] offset:5120\n\t"
    "ds_read_b64_tr_b16 v[246:247], %[a] offset:5248\n\t"
    "ds_read_b64_tr_b16 v[248:249], %[a] offset:4128\n\t"
    "ds_read_b64_tr_b16 v[250:251], %[a] offset:4256\n\t"
    "ds_read_b64_tr_b16 v[252:253], %[a] offset:5152\n\t"
    "ds_read_b64_tr_b16 v[254:255], %[a] offset:5280\n\t"
    "s_waitcnt lgkmcnt(0)"
    : "=&{v[224:255]}"(W.o3) : [a] "v"(a) : "memory");
}
__device__ __forceinline__ void qk_body(char* smem, int wave_id_, int xg, int h, int wg, const u16* __restrict__ Wq, const u16* __restrict__ Wv,
                                        char* ws, unsigned* tmo, unsigned* cnt, const char* xb, const char* ob) {
  const int wave = WAVE_ID;
  const int ty = wave >> 2, w4 = wave & 3;
  const char* oh = ob + (size_t)(h >> 2) * 16384 + (h & 3) * 1024;
  const char* wq = uni((const char*)(Wq + h * DH)); const char* wv = uni((const char*)(Wv + h * DH));
  auto stage2 = [&](int i) {
    const int tid = wave * 64 + lane_id();
    if (i < 6) {
      stage_proj<false>(wq + (size_t)i * 128 * HD * 2, Q_RING + ((2 * i) & 3) * 32768, tid, wave);
      if (i < 2) stage_proj<false>(wq + (size_t)(i + 6) * 128 * HD * 2, Q_RING + ((2 * i + 1) & 3) * 32768, tid, wave);
      else stage_proj<true>(wv + (size_t)(i - 2) * 256 * HD * 2, Q_RING + ((2 * i + 1) & 3) * 32768, tid, wave);
    }
  };
  auto rearm = [&](int ps) {
    unsigned z = SENT; asm volatile("" : "+v"(z));
    const unsigned l16 = lane_id() * 16;
#pragma unroll
    for (int r = 0; r < 2 * OREP; ++r) st16s(oh + (size_t)(ps * 4 + (r >> 1)) * 131072, (r & 1) * 4096u + l16, u32x4{z, z, z, z});
  };
  const char* dbw = xb + (size_t)48 * 16384 + 4096 + h * 128;
  QKW W;
  stage2(0); stage2(1);
#define WLD(i, w) do { if ((i) < 5) WAIT_V(8); else WAIT_V(0); \
    WBAR(); { const int l_ = lane_id(); const unsigned o_ = Q_RING + ty * 32768 + ((i) & 1) * 65536; const bool v_ = ty && (i) >= 2; \
      qk_wload<i>(w, proj_base(v_ ? w4 : 2 * w4, l_) + o_, proj_base(v_ ? w4 + 4 : 2 * w4 + 1, l_) + o_); } \
    WBAR(); stage2((i) + 2); if (wave == 0) STS(ws, wg, 1 + (i)); if ((i) - 4 == RDYI && opaque_tid() == 64) st4_sc1(cnt + (PQKV + h) * 32 + 3, 1u); } while (0)
  WLD(0, W.w0); WLD(1, W.w1); WLD(2, W.w2); WLD(3, W.w3); WLD(4, W.w4); WLD(5, W.w5);
#undef WLD
  if (wave == 4) *(volatile LAS u32x4*)(smem + Q_TM) = u32x4{0u, 0u, 0u, 0u};
  if (wave == 6) *(u32x4*)(smem + Q_PQ + 4096 + lane_id() * 16) = u32x4{0u, 0u, 0u, 0u};
  BAR();
  if (wave == 0) STS(ws, wg, 0);
  unsigned avail = 0;
  auto stage_kv = [&](int s) {
    if (avail <= (unsigned)s) {
      for (int spins = 0;; ++spins) {
        avail = qkv_avail<0>(cnt + (PQKV + h) * 32);
        if (avail > (unsigned)s) break;
        __builtin_amdgcn_s_sleep(8);
        if (give_up(spins, tmo, 5)) break;
      }
    }
    head_to_lds(uni(ws + (wave == 5 ? OFF_K : (wave == 6 ? OFF_V : OFF_Q)) + ((size_t)s * NH + h) * 8192), lane_id(), Q_K + (s & 1) * 32768 + (wave - 5) * 8192);
  };
  auto loop = [&](auto w_c) __attribute__((always_inline)) {
    constexpr int WV = decltype(w_c)::value;
    if (WV >= 5) stage_kv(0);
    unsigned t0 = 0, cap = 60000, d0 = 105, d1 = 200;
    int dq = D_Q;
    int sl = 0;
    unsigned fc = 0;
    for (int s = 0; s < NSEG; ++s) {
      TSDECL;
      const char* ag = xb + (size_t)((sl * 4 + (AREP == 4 ? xg : xg >> 1)) * 2 + (WV >> 2)) * 16384 + (WV < 4 ? WV * 2048 : WV * 1024);
      const unsigned pb = (s & 1) * 32768;
      const char* vs = WV == 4 ? dbw : WV == 7 ? (const char*)(cnt + (PROG + h) * 32) : xb + (size_t)(24 + sl * 8 + (h >> 2)) * 16384 + (h & 3) * 2048;
      const char* v2 = xb + (size_t)(52 + sl * 4 + (h >> 3)) * 16384 + (h & 7) * 1024 + (WV & 1) * 512;
      const char* od = oh + (size_t)sl * 524288;
      const unsigned sn = (unsigned)(s + 1) << 24;
      if (s > 0) wait_rt(t0, dq);
      TS(0);
      unsigned long long tc; unsigned sp;
      qk_step<WV>(W, ag, vs, v2, od, pb, sn, cap, sp, tc, nullptr, d0, d1);
      dq = (sp & 0xFFFFu) ? min(dq + DQU, D_Q + DQM) : max(dq - DQD, D_Q);
      if (WV >= 5) {
        if (WV == 7) WAIT_V(0);
        const bool st0 = E_S >= 0 && s + 1 < NSEG && avail > (unsigned)(s + 1);
        if (st0) { delay64<(E_S > 0 ? E_S : 0)>(); stage_kv(s + 1); }
        if (RAE && WV == RAW && s > 0 && s < NSEG - 1) rearm(sl == 0 ? 2 : sl - 1);
        WBAR();
        if (!st0 && s + 1 < NSEG) stage_kv(s + 1);
      }
      if (!RAE && WV == RAW && s > 0 && s < NSEG - 1) rearm(sl == 0 ? 2 : sl - 1);
      unsigned long long* tsp = nullptr;
      seg_attn<WV>(W, pb, ws + OFF_O + ((size_t)s * 64 + 2 * h + (WV >> 1)) * 4096, tsp);
      if (WV == 1) TSFLUSH(0x8000);
      if (WV < 4 && s >= SPUB) {
        WAIT_V(0);
        if (lane_id() == 0 && (__hip_atomic_fetch_add((LAS unsigned*)(smem + Q_TM + 8), 1u, __ATOMIC_RELAXED, __HIP_MEMORY_SCOPE_WORKGROUP) & 3u) == 3u)
          st4_sc1(cnt + (PROG + h) * 32, (unsigned)(s + 1));
      }
      if (s < NSEG - 1) {
        if (WV == 4) {
          delay64<E_B>();
          t0 = bell_time(xb + (size_t)(48 + xg) * 16384 + 4096, (lane_id() & 31) * 128, (unsigned)(s + 1), cap);
          *(volatile LAS u32x2*)(smem + Q_TM) = u32x2{(unsigned)(s + 1), t0};
          TS(14);
          TSFLUSH(0x4000);
        } else {
          for (;;) {
            const u32x2 v = *(volatile LAS u32x2*)(smem + Q_TM);
            t0 = __builtin_amdgcn_readfirstlane(v.y);
            if (__builtin_amdgcn_readfirstlane(v.x) == (unsigned)(s + 1)) break;
            __builtin_amdgcn_s_sleep(1);
          }
        }
      }
      sl = sl == 2 ? 0 : sl + 1;
    }
    if (cap == 0 && lane_id() == 0) st4_sc1(tmo, 1u);
    FCS();
  };
  switch (wave) {
    case 0: loop(std::integral_constant<int, 0>{}); break;
    case 1: loop(std::integral_constant<int, 1>{}); break;
    case 2: loop(std::integral_constant<int, 2>{}); break;
    case 3: loop(std::integral_constant<int, 3>{}); break;
    case 4: loop(std::integral_constant<int, 4>{}); break;
    case 5: loop(std::integral_constant<int, 5>{}); break;
    case 6: loop(std::integral_constant<int, 6>{}); break;
    default: loop(std::integral_constant<int, 7>{}); break;
  }
  __builtin_amdgcn_s_setprio(0);
}

__device__ __forceinline__ void vr_body(char* smem, int wave_id_, int xg, int h, int c, int wg, const u16* __restrict__ Wk, const u16* __restrict__ Wv,
                                        const u16* __restrict__ Wo, char* ws, unsigned* tmo, unsigned* cnt, const char* xb, const char* ob) {
  const int wave = WAVE_ID;
  const unsigned po = (unsigned)(h >> 4) * 16384u + ((h >> 1) & 7) * 1024 + (h & 1) * 512 + c * 128;
  const unsigned ko_ = (unsigned)(24 + (h >> 2)) * 16384u + (h & 3) * 2048 + c * 1024;
  const unsigned vo_ = (unsigned)(52 + (h >> 3)) * 16384u + (h & 7) * 1024 + c * 512;
  auto rearm = [&](int ps) {
    const int lane = lane_id();
    unsigned z = SENT; asm volatile("" : "+v"(z));
    const u32x4 sv = u32x4{z, z, z, z};
    static_assert(AREP == 2, "");
    st16s(xb, lane < 32 ? (unsigned)(ps * 8 + (lane >> 4) * 2) * 16384u + po + ((lane >> 3) & 1) * 256 + (lane & 7) * 16 : (unsigned)ps * 65536u + vo_ + (lane & 31) * 16, sv);
    st16s(xb, (unsigned)ps * 131072u + ko_ + lane * 16, sv);
  };
  const int hg = (wave + ROTW * ((2 * h + c) & 3)) & 7;
  VRW W;
  {
    const int lane = lane_id();
    const int g = lane >> 4, q4 = (lane & 15) >> 2, p4 = lane & 3, fr = 4 * g + (q4 & 1) + 32 * (q4 >> 1);
    const unsigned lw = wave * 16384;
    const char* sk = uni((const char*)(Wk + (size_t)(128 * wave) * HD + h * DH + 64 * c));
    const char* sv_ = uni((const char*)(Wv + (size_t)(128 * wave) * HD + h * DH + 64 + 32 * c));
    const char* so = uni((const char*)(Wo + (size_t)(512 * hg) * DM + 32 * h));
    const int kp = lane >> 2, sr = 32 * (kp >> 3) + 16 * ((kp >> 2) & 1) + (kp & 3);
    auto issue = [&](int p) {
#pragma unroll
      for (int i = 0; i < 8; ++i) {
        const unsigned d = lw + (p & 1) * 8192 + i * 1024;
        if (p < 2) gldsw(sk + (size_t)(8 * p + i) * 8 * HD * 2, (unsigned)((lane >> 3) * HD * 2 + (lane & 7) * 16), d);
        else if (p < 6 && (NTM & 8)) gldsw(so + (size_t)(128 * (p - 2) + 64 * (i & 1) + 4 * (i >> 1)) * DM * 2, (unsigned)(sr * DM * 2 + p4 * 16), d);
        else if (p < 6) glds16s<0>(so + (size_t)(128 * (p - 2) + 64 * (i & 1) + 4 * (i >> 1)) * DM * 2, (unsigned)(sr * DM * 2 + p4 * 16), d);
        else if (NTM & 16) gldsw(sv_ + (size_t)i * 16 * HD * 2, (unsigned)(kp * HD * 2 + p4 * 16), d);
        else glds16s<0>(sv_ + (size_t)i * 16 * HD * 2, (unsigned)(kp * HD * 2 + p4 * 16), d);
      }
    };
#define VTS(p) do { } while (0)
    const int fm = 16 * (g >> 1) + 4 * (g & 1) + (q4 & 1) + 8 * (q4 >> 1);
    const unsigned ak = lw + fm * 128 + p4 * 8, ao = lw + fr * 64 + p4 * 8;
    issue(2); issue(3);
    WAIT_V(8); VTS(1); kr_load_o0(W, ao); issue(4);
    WAIT_V(8); VTS(2); kr_load_o1(W, ao + 8192); issue(5);
    WAIT_V(8); VTS(3); kr_load_o2(W, ao); issue(0);
    WAIT_V(8); VTS(4); kr_load_o3(W, ao + 8192); issue(1);
    WAIT_V(8); VTS(5); kr_load_k0(W, ak); issue(6);
    WAIT_V(8); VTS(6); kr_load_k1(W, ak + 8192);
    WAIT_V(0);
    u32x8 x[4];
#pragma unroll
    for (int j = 0; j < 4; ++j) {
      const unsigned a = lw + fm * 64 + p4 * 8 + (j & 1) * 4096 + (j >> 1) * 32;
      const u32x2 t0 = trread(smem, a), t1 = trread(smem, a + 128), t2 = trread(smem, a + 2048), t3 = trread(smem, a + 2176);
      x[j] = u32x8{t0.x, t0.y, t1.x, t1.y, t2.x, t2.y, t3.x, t3.y};
    }
    char* d = smem + lw + 8192 + lane * 16;
#pragma unroll
    for (int j = 0; j < 4; ++j) { *(u32x4*)(d + j * 2048) = u32x4{x[j][0], x[j][1], x[j][2], x[j][3]}; *(u32x4*)(d + j * 2048 + 1024) = u32x4{x[j][4], x[j][5], x[j][6], x[j][7]}; }
    VTS(7);
  }
  BAR();
  if (wave == 0) STS(ws, wg, 0);
  __builtin_amdgcn_s_setprio(2);
  auto loop = [&](auto cl_c) __attribute__((always_inline)) {
    constexpr int CL = decltype(cl_c)::value;
    unsigned cap = 60000, tr = 0, fc = 0;
    int da = D_A, dd = D_O;
    int sl = 0;
    for (int s = 0; s < NSEG; ++s) {
      TSDECL;
      const int sn = sl == 2 ? 0 : sl + 1;
      const char* ag = xb + (size_t)((sl * 4 + (AREP == 4 ? xg : xg >> 1)) * 2 + (wave >> 2)) * 16384 + (wave & 3) * 2048;
      const char* vd = CL == 0 ? xb + (size_t)sl * 131072 + ko_ + (wave & 3) * 256 : xb + (size_t)sl * 65536 + vo_ + (wave & 3) * 128;
      const char* os = ob + (size_t)((sl * 4 + ORSEL) * 8 + hg) * 16384 + c * 4096;
      const char* ad = xb + (size_t)((sn * 4 + (wave & 3)) * 2) * 16384 + po;
      if (s > 0) { if (wave < AREP) delay64<E_AG - E_W>(); else delay64<E_AG>(); }
      TS(0);
      unsigned long long tc; unsigned sp;
      unsigned long long* tsp = nullptr;
      if (CL == 0) vr_v0(W, ag, vd, wave * 16384 + 8192, V_P1 + wave * 2304, V_PV + wave * 1024, V_P1 + (wave & 3) * 32, cap, sp, tc, tsp);
      else vr_v1(W, ag, vd, wave * 16384 + 8192, V_P1 + wave * 2304, V_PV + wave * 1024, V_PV + (wave & 3) * 256, cap, sp, tc, tsp);
      TN(12, sp + 1);
      if (s > 3 && sp) fc += 1u;
      da = sp ? min(da + DQU, D_A + DQM) : max(da - DQD, D_A);
      if (s == NSEG - 1) break;
      if (wave == 7 && s > 0) rearm(sl == 0 ? 2 : sl - 1);
      wait_clk((unsigned)tc, E_O);
      TS(3);
      const unsigned tb = bell_time(xb + (size_t)(48 + xg) * 16384 + hg * 512, (lane_id() & 3) * 128, (unsigned)(s + 1), cap);
      tr = tb;
      wait_rt(tb, dd);
      TS(4);
      vr_r(W, os, ad, V_P2 + wave * 768, cap, sp, tsp);
      if (s > 3 && sp) fc += 0x10000u;
      dd = sp ? min(dd + DOU, D_O + DOM) : max(dd - DQD, D_O);
      TN(14, (sp & 0xFFFF) + 1);
      if (wave == 1) TSFLUSH(0x6078);
      sl = sn;
    }
    if (cap == 0 && lane_id() == 0) st4_sc1(tmo, 2u);
    FCS();
  };
  if ((wave >> 2) == CL0) loop(std::integral_constant<int, 0>{}); else loop(std::integral_constant<int, 1>{});
  __builtin_amdgcn_s_setprio(0);
}
#define GEMMX gemm7
#define PF0 0
#define NIMM 3
enum { T_NONE, T_QV, T_KR, T_WO, T_F1, T_F2, T_F2C, T_FIN, T_LN, T_SA };
__device__ __attribute__((aligned(16))) const unsigned ROLES[256][6] = {
  {0x1, 0x0, 0x0, 0x0, 0x0, 0x0},
  {0x11, 0x0, 0x0, 0x0, 0x0, 0x0},
  {0x21, 0x0, 0x0, 0x0, 0x0, 0x0},
  {0x31, 0x0, 0x0, 0x0, 0x0, 0x0},
  {0x41, 0x0, 0x0, 0x0, 0x0, 0x0},
  {0x51, 0x0, 0x0, 0x0, 0x0, 0x0},
  {0x61, 0x0, 0x0, 0x0, 0x0, 0x0},
  {0x71, 0x0, 0x0, 0x0, 0x0, 0x0},
  {0x81, 0x0, 0x0, 0x0, 0x0, 0x0},
  {0x91, 0x0, 0x0, 0x0, 0x0, 0x0},
  {0xa1, 0x0, 0x0, 0x0, 0x0, 0x0},
  {0xb1, 0x0, 0x0, 0x0, 0x0, 0x0},
  {0x2, 0x0, 0x0, 0x0, 0x0, 0x0},
  {0x12, 0x0, 0x0, 0x0, 0x0, 0x0},
  {0x22, 0x0, 0x0, 0x0, 0x0, 0x0},
  {0x32, 0x0, 0x0, 0x0, 0x0, 0x0},
  {0x42, 0x0, 0x0, 0x0, 0x0, 0x0},
  {0x52, 0x0, 0x0, 0x0, 0x0, 0x0},
  {0x62, 0x0, 0x0, 0x0, 0x0, 0x0},
  {0x72, 0x0, 0x0, 0x0, 0x0, 0x0},
  {0x82, 0x0, 0x0, 0x0, 0x0, 0x0},
  {0x92, 0x0, 0x0, 0x0, 0x0, 0x0},
  {0xa2, 0x0, 0x0, 0x0, 0x0, 0x0},
  {0xb2, 0x0, 0x0, 0x0, 0x0, 0x0},
  {0xc2, 0x0, 0x0, 0x0, 0x0, 0x0},
  {0xd2, 0x0, 0x0, 0x0, 0x0, 0x0},
  {0xe2, 0x0, 0x0, 0x0, 0x0, 0x0},
  {0xf2, 0x0, 0x0, 0x0, 0x0, 0x0},
  {0x102, 0x0, 0x0, 0x0, 0x0, 0x0},
  {0x112, 0x0, 0x0, 0x0, 0x0, 0x0},
  {0x122, 0x0, 0x0, 0x0, 0x0, 0x0},
  {0x132, 0x0, 0x0, 0x0, 0x0, 0x0},
  {0xc1, 0x0, 0x0, 0x0, 0x0, 0x0},
  {0xd1, 0x0, 0x0, 0x0, 0x0, 0x0},
  {0xe1, 0x0, 0x0, 0x0, 0x0, 0x0},
  {0xf1, 0x0, 0x0, 0x0, 0x0, 0x0},
  {0x101, 0x0, 0x0, 0x0, 0x0, 0x0},
  {0x111, 0x0, 0x0, 0x0, 0x0, 0x0},
  {0x121, 0x0, 0x0, 0x0, 0x0, 0x0},
  {0x131, 0x0, 0x0, 0x0, 0x0, 0x0},
  {0x142, 0x0, 0x0, 0x0, 0x0, 0x0},
  {0x152, 0x0, 0x0, 0x0, 0x0, 0x0},
  {0x162, 0x0, 0x0, 0x0, 0x0, 0x0},
  {0x172, 0x0, 0x0, 0x0, 0x0, 0x0},
  {0x182, 0x0, 0x0, 0x0, 0x0, 0x0},
  {0x192, 0x0, 0x0, 0x0, 0x0, 0x0},
  {0x1a2, 0x0, 0x0, 0x0, 0x0, 0x0},
  {0x1b2, 0x0, 0x0, 0x0, 0x0, 0x0},
  {0x1c2, 0x0, 0x0, 0x0, 0x0, 0x0},
  {0x1d2, 0x0, 0x0, 0x0, 0x0, 0x0},
  {0x1e2, 0x0, 0x0, 0x0, 0x0, 0x0},
  {0x1f2, 0x0, 0x0, 0x0, 0x0, 0x0},
  {0x202, 0x0, 0x0, 0x0, 0x0, 0x0},
  {0x212, 0x0, 0x0, 0x0, 0x0, 0x0},
  {0x222, 0x0, 0x0, 0x0, 0x0, 0x0},
  {0x232, 0x0, 0x0, 0x0, 0x0, 0x0},
  {0x242, 0x0, 0x0, 0x0, 0x0, 0x0},
  {0x252, 0x0, 0x0, 0x0, 0x0, 0x0},
  {0x262, 0x0, 0x0, 0x0, 0x0, 0x0},
  {0x272, 0x0, 0x0, 0x0, 0x0, 0x0},
  {0x282, 0x0, 0x0, 0x0, 0x0, 0x0},
  {0x292, 0x0, 0x0, 0x0, 0x0, 0x0},
  {0x2a2, 0x0, 0x0, 0x0, 0x0, 0x0},
  {0x2b2, 0x0, 0x0, 0x0, 0x0, 0x0},
  {0x141, 0x0, 0x0, 0x0, 0x0, 0x0},
  {0x151, 0x0, 0x0, 0x0, 0x0, 0x0},
  {0x161, 0x0, 0x0, 0x0, 0x0, 0x0},
  {0x171, 0x0, 0x0, 0x0, 0x0, 0x0},
  {0x181, 0x0, 0x0, 0x0, 0x0, 0x0},
  {0x191, 0x0, 0x0, 0x0, 0x0, 0x0},
  {0x1a1, 0x0, 0x0, 0x0, 0x0, 0x0},
  {0x1b1, 0x0, 0x0, 0x0, 0x0, 0x0},
  {0x1c1, 0x0, 0x0, 0x0, 0x0, 0x0},
  {0x1d1, 0x0, 0x0, 0x0, 0x0, 0x0},
  {0x1e1, 0x0, 0x0, 0x0, 0x0, 0x0},
  {0x1f1, 0x0, 0x0, 0x0, 0x0, 0x0},
  {0x2c2, 0x0, 0x0, 0x0, 0x0, 0x0},
  {0x2d2, 0x0, 0x0, 0x0, 0x0, 0x0},
  {0x2e2, 0x0, 0x0, 0x0, 0x0, 0x0},
  {0x2f2, 0x0, 0x0, 0x0, 0x0, 0x0},
  {0x302, 0x0, 0x0, 0x0, 0x0, 0x0},
  {0x312, 0x0, 0x0, 0x0, 0x0, 0x0},
  {0x322, 0x0, 0x0, 0x0, 0x0, 0x0},
  {0x332, 0x0, 0x0, 0x0, 0x0, 0x0},
  {0x342, 0x0, 0x0, 0x0, 0x0, 0x0},
  {0x352, 0x0, 0x0, 0x0, 0x0, 0x0},
  {0x362, 0x0, 0x0, 0x0, 0x0, 0x0},
  {0x372, 0x0, 0x0, 0x0, 0x0, 0x0},
  {0x382, 0x0, 0x0, 0x0, 0x0, 0x0},
  {0x392, 0x0, 0x0, 0x0, 0x0, 0x0},
  {0x3a2, 0x0, 0x0, 0x0, 0x0, 0x0},
  {0x3b2, 0x0, 0x0, 0x0, 0x0, 0x0},
  {0x3c2, 0x0, 0x0, 0x0, 0x0, 0x0},
  {0x3d2, 0x0, 0x0, 0x0, 0x0, 0x0},
  {0x3e2, 0x0, 0x0, 0x0, 0x0, 0x0},
  {0x3f2, 0x0, 0x0, 0x0, 0x0, 0x0},
  {0x100005, 0x300000, 0x0, 0x0, 0x0, 0x0},
  {0x100015, 0x300004, 0x0, 0x0, 0x0, 0x0},
  {0x100025, 0x300008, 0x0, 0x0, 0x0, 0x0},
  {0x100035, 0x30000c, 0x0, 0x0, 0x0, 0x0},
  {0x100045, 0x300010, 0x0, 0x0, 0x0, 0x0},
  {0x100055, 0x300014, 0x0, 0x0, 0x0, 0x0},
  {0x100065, 0x300018, 0x0, 0x0, 0x0, 0x0},
  {0x100073, 0x16001c, 0x0, 0x0, 0x0, 0x0},
  {0x100075, 0x300020, 0x0, 0x0, 0x0, 0x0},
  {0x100085, 0x300024, 0x0, 0x0, 0x0, 0x0},
  {0x100095, 0x300028, 0x0, 0x0, 0x0, 0x0},
  {0x1000a5, 0x30002c, 0x0, 0x0, 0x0, 0x0},
  {0x1000b5, 0x300030, 0x0, 0x0, 0x0, 0x0},
  {0x1000c5, 0x300034, 0x0, 0x0, 0x0, 0x0},
  {0x1000d5, 0x300038, 0x0, 0x0, 0x0, 0x0},
  {0x1000f3, 0x16003c, 0x0, 0x0, 0x0, 0x0},
  {0x1000e5, 0x300040, 0x0, 0x0, 0x0, 0x0},
  {0x1000f5, 0x300044, 0x0, 0x0, 0x0, 0x0},
  {0x100105, 0x300048, 0x0, 0x0, 0x0, 0x0},
  {0x100115, 0x30004c, 0x0, 0x0, 0x0, 0x0},
  {0x100125, 0x300050, 0x0, 0x0, 0x0, 0x0},
  {0x100135, 0x300054, 0x0, 0x0, 0x0, 0x0},
  {0x100145, 0x300058, 0x0, 0x0, 0x0, 0x0},
  {0x100173, 0x16005c, 0x0, 0x0, 0x0, 0x0},
  {0x100155, 0x300060, 0x0, 0x0, 0x0, 0x0},
  {0x100165, 0x300064, 0x0, 0x0, 0x0, 0x0},
  {0x100175, 0x300068, 0x0, 0x0, 0x0, 0x0},
  {0x100006, 0x30006c, 0x0, 0x0, 0x0, 0x0},
  {0x100016, 0x300070, 0x0, 0x0, 0x0, 0x0},
  {0x100026, 0x300074, 0x0, 0x0, 0x0, 0x0},
  {0x100036, 0x300078, 0x0, 0x0, 0x0, 0x0},
  {0x1001f3, 0x16007c, 0x0, 0x0, 0x0, 0x0},
  {0x100064, 0x5361615, 0x0, 0x0, 0x0, 0x0},
  {0x100074, 0x5361619, 0x0, 0x0, 0x0, 0x0},
  {0x100084, 0x536161d, 0x0, 0x0, 0x0, 0x0},
  {0x100094, 0x536161a, 0x0, 0x0, 0x0, 0x0},
  {0x1000a4, 0x536161e, 0x0, 0x0, 0x0, 0x0},
  {0x1000d4, 0x536161c, 0x0, 0x0, 0x0, 0x0},
  {0x100034, 0x5402812, 0x0, 0x0, 0x0, 0x0},
  {0x1000b4, 0x5402816, 0x0, 0x0, 0x0, 0x0},
  {0x200000, 0x400001, 0x1403000, 0x0, 0x0, 0x0},
  {0x200007, 0x400005, 0x1403004, 0x0, 0x0, 0x0},
  {0x200017, 0x400009, 0x1403008, 0x0, 0x0, 0x0},
  {0x200027, 0x40000d, 0x140300c, 0x0, 0x0, 0x0},
  {0x200037, 0x400011, 0x1403010, 0x0, 0x0, 0x0},
  {0x100003, 0x160015, 0x0, 0x0, 0x0, 0x0},
  {0x100013, 0x160019, 0x0, 0x0, 0x0, 0x0},
  {0x100023, 0x16001d, 0x0, 0x0, 0x0, 0x0},
  {0x100004, 0x280002, 0x0, 0x0, 0x0, 0x0},
  {0x100014, 0x280006, 0x0, 0x0, 0x0, 0x0},
  {0x100024, 0x28000a, 0x0, 0x0, 0x0, 0x0},
  {0x1000c4, 0x28000e, 0x0, 0x0, 0x0, 0x0},
  {0x100044, 0x280012, 0x0, 0x0, 0x0, 0x0},
  {0x100054, 0x280016, 0x0, 0x0, 0x0, 0x0},
  {0x100033, 0x16001a, 0x0, 0x0, 0x0, 0x0},
  {0x100043, 0x16001e, 0x0, 0x0, 0x0, 0x0},
  {0x101053, 0x2403615, 0x0, 0x0, 0x0, 0x0},
  {0x102063, 0x2403619, 0x0, 0x0, 0x0, 0x0},
  {0x303000, 0x1402802, 0x140280a, 0x1403014, 0x0, 0x0},
  {0x304000, 0x1402806, 0x140280e, 0x1403018, 0x0, 0x0},
  {0x105008, 0x240361d, 0x0, 0x0, 0x0, 0x0},
  {0x106018, 0x240361a, 0x0, 0x0, 0x0, 0x0},
  {0x107088, 0x240361e, 0x0, 0x0, 0x0, 0x0},
  {0x108098, 0x240361c, 0x0, 0x0, 0x0, 0x0},
  {0x100144, 0x5361635, 0x0, 0x0, 0x0, 0x0},
  {0x100154, 0x5361639, 0x0, 0x0, 0x0, 0x0},
  {0x100164, 0x536163d, 0x0, 0x0, 0x0, 0x0},
  {0x100174, 0x536163a, 0x0, 0x0, 0x0, 0x0},
  {0x100184, 0x536163e, 0x0, 0x0, 0x0, 0x0},
  {0x100194, 0x536163c, 0x0, 0x0, 0x0, 0x0},
  {0x100134, 0x5402832, 0x0, 0x0, 0x0, 0x0},
  {0x1001b4, 0x5402836, 0x0, 0x0, 0x0, 0x0},
  {0x200000, 0x400021, 0x1403020, 0x0, 0x0, 0x0},
  {0x200047, 0x400025, 0x1403024, 0x0, 0x0, 0x0},
  {0x200057, 0x400029, 0x1403028, 0x0, 0x0, 0x0},
  {0x200067, 0x40002d, 0x140302c, 0x0, 0x0, 0x0},
  {0x200077, 0x400031, 0x1403030, 0x0, 0x0, 0x0},
  {0x100083, 0x160035, 0x0, 0x0, 0x0, 0x0},
  {0x100093, 0x160039, 0x0, 0x0, 0x0, 0x0},
  {0x1000a3, 0x16003d, 0x0, 0x0, 0x0, 0x0},
  {0x1000e4, 0x280022, 0x0, 0x0, 0x0, 0x0},
  {0x1000f4, 0x280026, 0x0, 0x0, 0x0, 0x0},
  {0x100104, 0x28002a, 0x0, 0x0, 0x0, 0x0},
  {0x100114, 0x28002e, 0x0, 0x0, 0x0, 0x0},
  {0x100124, 0x280032, 0x0, 0x0, 0x0, 0x0},
  {0x1001a4, 0x280036, 0x0, 0x0, 0x0, 0x0},
  {0x1000b3, 0x16003a, 0x0, 0x0, 0x0, 0x0},
  {0x1000c3, 0x16003e, 0x0, 0x0, 0x0, 0x0},
  {0x1090d3, 0x2403635, 0x0, 0x0, 0x0, 0x0},
  {0x10a0e3, 0x2403639, 0x0, 0x0, 0x0, 0x0},
  {0x30b000, 0x1402822, 0x140282a, 0x1403034, 0x0, 0x0},
  {0x30c000, 0x1402826, 0x140282e, 0x1403038, 0x0, 0x0},
  {0x10d028, 0x240363d, 0x0, 0x0, 0x0, 0x0},
  {0x10e038, 0x240363a, 0x0, 0x0, 0x0, 0x0},
  {0x10f0a8, 0x240363e, 0x0, 0x0, 0x0, 0x0},
  {0x1100b8, 0x240363c, 0x0, 0x0, 0x0, 0x0},
  {0x100224, 0x5361655, 0x0, 0x0, 0x0, 0x0},
  {0x100284, 0x5361659, 0x0, 0x0, 0x0, 0x0},
  {0x100244, 0x536165d, 0x0, 0x0, 0x0, 0x0},
  {0x100254, 0x536165a, 0x0, 0x0, 0x0, 0x0},
  {0x100264, 0x536165e, 0x0, 0x0, 0x0, 0x0},
  {0x100274, 0x536165c, 0x0, 0x0, 0x0, 0x0},
  {0x100234, 0x5402852, 0x0, 0x0, 0x0, 0x0},
  {0x100294, 0x5402856, 0x0, 0x0, 0x0, 0x0},
  {0x200000, 0x400041, 0x1403040, 0x0, 0x0, 0x0},
  {0x200087, 0x400045, 0x1403044, 0x0, 0x0, 0x0},
  {0x200097, 0x400049, 0x1403048, 0x0, 0x0, 0x0},
  {0x2000a7, 0x40004d, 0x140304c, 0x0, 0x0, 0x0},
  {0x2000b7, 0x400051, 0x1403050, 0x0, 0x0, 0x0},
  {0x100103, 0x160055, 0x0, 0x0, 0x0, 0x0},
  {0x100113, 0x160059, 0x0, 0x0, 0x0, 0x0},
  {0x100123, 0x16005d, 0x0, 0x0, 0x0, 0x0},
  {0x1001c4, 0x280042, 0x0, 0x0, 0x0, 0x0},
  {0x1001d4, 0x280046, 0x0, 0x0, 0x0, 0x0},
  {0x1001e4, 0x28004a, 0x0, 0x0, 0x0, 0x0},
  {0x1001f4, 0x28004e, 0x0, 0x0, 0x0, 0x0},
  {0x100204, 0x280052, 0x0, 0x0, 0x0, 0x0},
  {0x100214, 0x280056, 0x0, 0x0, 0x0, 0x0},
  {0x100133, 0x16005a, 0x0, 0x0, 0x0, 0x0},
  {0x100143, 0x16005e, 0x0, 0x0, 0x0, 0x0},
  {0x111153, 0x2403655, 0x0, 0x0, 0x0, 0x0},
  {0x112163, 0x2403659, 0x0, 0x0, 0x0, 0x0},
  {0x313000, 0x1402842, 0x140284a, 0x1403054, 0x0, 0x0},
  {0x314000, 0x1402846, 0x140284e, 0x1403058, 0x0, 0x0},
  {0x115048, 0x240365d, 0x0, 0x0, 0x0, 0x0},
  {0x116058, 0x240365a, 0x0, 0x0, 0x0, 0x0},
  {0x1170c8, 0x240365e, 0x0, 0x0, 0x0, 0x0},
  {0x1180d8, 0x240365c, 0x0, 0x0, 0x0, 0x0},
  {0x100304, 0x5361675, 0x0, 0x0, 0x0, 0x0},
  {0x100314, 0x5361679, 0x0, 0x0, 0x0, 0x0},
  {0x100324, 0x536167d, 0x0, 0x0, 0x0, 0x0},
  {0x100374, 0x536167a, 0x0, 0x0, 0x0, 0x0},
  {0x100344, 0x536167e, 0x0, 0x0, 0x0, 0x0},
  {0x100354, 0x536167c, 0x0, 0x0, 0x0, 0x0},
  {0x1002b4, 0x5402872, 0x0, 0x0, 0x0, 0x0},
  {0x100334, 0x5402876, 0x0, 0x0, 0x0, 0x0},
  {0x200000, 0x400061, 0x1403060, 0x0, 0x0, 0x0},
  {0x2000c7, 0x400065, 0x1403064, 0x0, 0x0, 0x0},
  {0x2000d7, 0x400069, 0x1403068, 0x0, 0x0, 0x0},
  {0x2000e7, 0x40006d, 0x140306c, 0x0, 0x0, 0x0},
  {0x2000f7, 0x400071, 0x1403070, 0x0, 0x0, 0x0},
  {0x100183, 0x160075, 0x0, 0x0, 0x0, 0x0},
  {0x100193, 0x160079, 0x0, 0x0, 0x0, 0x0},
  {0x1001a3, 0x16007d, 0x0, 0x0, 0x0, 0x0},
  {0x1002a4, 0x280062, 0x0, 0x0, 0x0, 0x0},
  {0x100364, 0x280066, 0x0, 0x0, 0x0, 0x0},
  {0x1002c4, 0x28006a, 0x0, 0x0, 0x0, 0x0},
  {0x1002d4, 0x28006e, 0x0, 0x0, 0x0, 0x0},
  {0x1002e4, 0x280072, 0x0, 0x0, 0x0, 0x0},
  {0x1002f4, 0x280076, 0x0, 0x0, 0x0, 0x0},
  {0x1001b3, 0x16007a, 0x0, 0x0, 0x0, 0x0},
  {0x1001c3, 0x16007e, 0x0, 0x0, 0x0, 0x0},
  {0x1191d3, 0x2403675, 0x0, 0x0, 0x0, 0x0},
  {0x11a1e3, 0x2403679, 0x0, 0x0, 0x0, 0x0},
  {0x31b000, 0x1402862, 0x140286a, 0x1403074, 0x0, 0x0},
  {0x31c000, 0x1402866, 0x140286e, 0x1403078, 0x0, 0x0},
  {0x11d068, 0x240367d, 0x0, 0x0, 0x0, 0x0},
  {0x11e078, 0x240367a, 0x0, 0x0, 0x0, 0x0},
  {0x11f0e8, 0x240367e, 0x0, 0x0, 0x0, 0x0},
  {0x1200f8, 0x240367c, 0x0, 0x0, 0x0, 0x0},
};
constexpr int PFN = 32;
__device__ const unsigned PFT[8][PFN + 1] = {
  {0x40000f, 0x41000f, 0x42000f, 0x43000f, 0x44000f, 0x45000f, 0x46000f, 0x47000f, 0x48000f, 0x49000f, 0x4a000f, 0x4b000f, 0x4c000f, 0x4d000f, 0x4e000f, 0x4f000f, 0x40000c, 0x41000c, 0x42000c, 0x43000c, 0x40002c, 0x41002c, 0x42002c, 0x43002c, 0x40004c, 0x41004c, 0x42004c, 0x43004c, 0x40000d, 0x41000d, 0x42000d, 0x43000d, 0x0},
  {0x40006c, 0x41006c, 0x42006c, 0x43006c, 0x40008c, 0x41008c, 0x42008c, 0x43008c, 0x40006d, 0x41006d, 0x42006d, 0x43006d, 0x40006e, 0x41006e, 0x42006e, 0x43006e, 0x40008d, 0x41008d, 0x42008d, 0x43008d, 0x40008e, 0x41008e, 0x42008e, 0x43008e, 0x4000ad, 0x4100ad, 0x4200ad, 0x4300ad, 0x4000ae, 0x4100ae, 0x4200ae, 0x4300ae, 0x0},
  {0x40002f, 0x41002f, 0x42002f, 0x43002f, 0x44002f, 0x45002f, 0x46002f, 0x47002f, 0x48002f, 0x49002f, 0x4a002f, 0x4b002f, 0x4c002f, 0x4d002f, 0x4e002f, 0x4f002f, 0x4000ac, 0x4100ac, 0x4200ac, 0x4300ac, 0x4000cc, 0x4100cc, 0x4200cc, 0x4300cc, 0x4000ec, 0x4100ec, 0x4200ec, 0x4300ec, 0x4000cd, 0x4100cd, 0x4200cd, 0x4300cd, 0x0},
  {0x40000c, 0x41000c, 0x42000c, 0x43000c, 0x40002c, 0x41002c, 0x42002c, 0x43002c, 0x40004c, 0x41004c, 0x42004c, 0x43004c, 0x40006c, 0x41006c, 0x42006c, 0x43006c, 0x40008c, 0x41008c, 0x42008c, 0x43008c, 0x4000ac, 0x4100ac, 0x4200ac, 0x4300ac, 0x4000cc, 0x4100cc, 0x4200cc, 0x4300cc, 0x4000ec, 0x4100ec, 0x4200ec, 0x4300ec, 0x0},
  {0x40000d, 0x41000d, 0x42000d, 0x43000d, 0x40002d, 0x41002d, 0x42002d, 0x43002d, 0x40000e, 0x41000e, 0x42000e, 0x43000e, 0x40002e, 0x41002e, 0x42002e, 0x43002e, 0x600001, 0x600002, 0x620001, 0x620002, 0x601001, 0x601002, 0x621001, 0x621002, 0x602001, 0x602002, 0x622001, 0x622002, 0x603001, 0x603002, 0x623001, 0x623002, 0x0},
  {0x40004d, 0x41004d, 0x42004d, 0x43004d, 0x40006d, 0x41006d, 0x42006d, 0x43006d, 0x40004e, 0x41004e, 0x42004e, 0x43004e, 0x40006e, 0x41006e, 0x42006e, 0x43006e, 0x600041, 0x600042, 0x620041, 0x620042, 0x601041, 0x601042, 0x621041, 0x621042, 0x602041, 0x602042, 0x622041, 0x622042, 0x603041, 0x603042, 0x623041, 0x623042, 0x0},
  {0x40008d, 0x41008d, 0x42008d, 0x43008d, 0x4000ad, 0x4100ad, 0x4200ad, 0x4300ad, 0x40008e, 0x41008e, 0x42008e, 0x43008e, 0x4000ae, 0x4100ae, 0x4200ae, 0x4300ae, 0x600081, 0x600082, 0x620081, 0x620082, 0x601081, 0x601082, 0x621081, 0x621082, 0x602081, 0x602082, 0x622081, 0x622082, 0x603081, 0x603082, 0x623081, 0x623082, 0x0},
  {0x4000cd, 0x4100cd, 0x4200cd, 0x4300cd, 0x4000ed, 0x4100ed, 0x4200ed, 0x4300ed, 0x4000ce, 0x4100ce, 0x4200ce, 0x4300ce, 0x4000ee, 0x4100ee, 0x4200ee, 0x4300ee, 0x6000c1, 0x6000c2, 0x6200c1, 0x6200c2, 0x6010c1, 0x6010c2, 0x6210c1, 0x6210c2, 0x6020c1, 0x6020c2, 0x6220c1, 0x6220c2, 0x6030c1, 0x6030c2, 0x6230c1, 0x6230c2, 0x0},
};
__global__ void __launch_bounds__(512, 2) k_mega(const u16* __restrict__ Wq, const u16* __restrict__ Wk, const u16* __restrict__ Wv,
                                                 const u16* __restrict__ Wo, const u16* __restrict__ w1, const u16* __restrict__ w3,
                                                 const u16* __restrict__ w2, const u16* __restrict__ x, const u16* __restrict__ cvec,
                                                 const u16* __restrict__ wmod, char* ws, u16* out) {
  extern __shared__ __attribute__((aligned(16))) char smem[];
  int wave_id_ = __builtin_amdgcn_readfirstlane((int)threadIdx.x >> 6);
  asm volatile("" : "+s"(wave_id_));
  unsigned* tmo = (unsigned*)(ws + OFF_TMO); unsigned* tick = (unsigned*)(ws + OFF_CNT);
  unsigned long long* mts = nullptr;
  const float* mod = (const float*)(ws + OFF_MOD);
  volatile LAS unsigned* tslot = (volatile LAS unsigned*)(smem + L_SLOT + 768);
  constexpr int L_TAB = L_GB, L_CAL = L_GB + 6144;
  {
    const int tid = opaque_tid();
    if (tid == 0) {
      unsigned xcc; asm volatile("s_getreg_b32 %0, hwreg(HW_REG_XCC_ID)" : "=s"(xcc));
      unsigned sl = 0xFFFFu;
      for (int i = 0; i < 8; ++i) {
        const unsigned xg = (xcc + i) & 7;
        const unsigned t = __hip_atomic_fetch_add(tick + (TICK + xg) * 32, 1u, __ATOMIC_RELAXED, __HIP_MEMORY_SCOPE_AGENT);
        if (t < 32) { sl = xg * 32 + t; break; }
      }
      *tslot = sl;
    } else if (tid >= 128) *(u32x4*)(smem + L_TAB + (tid - 128) * 16) = ((const u32x4*)ROLES)[tid - 128];
    else if (tid >= 64) {
      const int c = (tid >> 4) & 3;
      int v = ((const int*)(ws + OFF_CAL))[(c == 0 ? 0 : (c == 3 ? 4 : c + 2)) * NCALW + (tid & 15)];
      v += (int)dppu<0x128>(v); v += (int)dppu<0x124>(v); v += (int)dppu<0x122>(v); v += (int)dppu<0x121>(v);
      if ((tid & 15) == 0) *(int*)(smem + L_CAL + c * 4) = v;
    }
  }
  __syncthreads();
  const int slot = (int)__builtin_amdgcn_readfirstlane(*tslot);
  unsigned rt_[6], par = 0;
  {
    const u32x4 cv = *(const u32x4*)(smem + L_CAL);
    par = __builtin_amdgcn_readfirstlane(((int)cv[0] > 0 ? 0u : 1u) | ((int)cv[1] > 0 ? 0u : 2u) | ((int)cv[2] > 0 ? 0u : 4u));
#pragma unroll
    for (int i = 0; i < 6; ++i) rt_[i] = __builtin_amdgcn_readfirstlane(*(const unsigned*)(smem + L_TAB + (slot & 255) * 24 + i * 4));
  }
  __syncthreads();
  unsigned* cnt = (unsigned*)(xch_base(ws) + ((size_t)4 << 21) + (size_t)((par >> 2) << 13));
  if (slot >= 256) return;
  const int xg = slot >> 5;
  const unsigned rw = rt_[0];
  const int role = rw & 15, idx = (rw >> 4) & 255;
  const char* cb = uni((const char*)cnt);
  if (role == T_QV || role == T_KR) {
    char* xch = xch_base(ws);
    const char* xb = uni(xch + ((size_t)3 << 21) + (((par >> 1) & 1) << 13));
    const char* ob = uni(xch + ((par & 1) << 13));
    const int wg = role == T_QV ? (idx >> 3) * 32 + (idx & 7) : (idx >> 4) * 32 + 8 + (idx & 15);
    if (role == T_QV) qk_body(smem, wave_id_, xg, idx, wg, Wq, Wv, ws, tmo, cnt, xb, ob);
    else vr_body(smem, wave_id_, xg, idx >> 1, idx & 1, wg, Wk, Wv, Wo, ws, tmo, cnt, xb, ob);
    return;
  }
  if ((rw >> 12) & 255) l1_role(smem, wave_id_, ((rw >> 12) & 255) - 1, x, mod, cvec, wmod, ws + OFF_H1, cnt, tmo, mts);
  if (rw >> 24) {
    const int tid = opaque_tid();
    const size_t o = (size_t)(tid >> 4) * HD + ((rw >> 24) - 1) * DH + (tid & 15) * 8;
    u32x4 acc = u32x4{0u, 0u, 0u, 0u};
#pragma unroll 1
    for (int i = PF0; i < 4; ++i) {
      u32x4 v[8];
#pragma unroll
      for (int j = 0; j < 8; ++j) v[j] = ldnt<4>(Wv + o + (size_t)(i * 256 + 32 * j) * HD);
#pragma unroll
      for (int j = 0; j < 8; ++j) acc ^= v[j];
    }
    asm volatile("" :: "v"(acc));
  }
  const int ntask = (rw >> 20) & 15;
  if (ntask == 0 && role == T_WO) { if (wave_id_ == 0) wait_k0(cnt, idx, tmo); __syncthreads(); }
  for (int it = 0; it < ntask; ++it) {
    const unsigned tk = it == 0 ? rt_[1] : ROLES[slot][1 + it];
    const int m = tk & 3, h = (tk >> 2) & 31, bo = (tk >> 8) & 255, ns = ((tk >> 16) & 255) - bo, part = (tk >> 24) & 3;
    const int lane = lane_id();
    const Dep d{cb, (unsigned)(PL1 + (lane & 31)) * 128, 8u, (unsigned)-bo};
    if (wave_id_ == 0) {
      if ((tk >> 26) & 1) wait_k0(cnt, h, tmo);
      if (bo) wait_l1(cnt, 8u, tmo);
    }
    __syncthreads();
    gemm7<R_QKV>(smem, wave_id_, (m == 0 ? Wq : (m == 1 ? Wk : Wv)) + h * DH, nullptr, HD, ws + OFF_H1 + (size_t)bo * 65536, 65536, d,
                    ws + (m == 0 ? OFF_Q : (m == 1 ? OFF_K : OFF_V)) + h * 8192 + (size_t)bo * 32 * 8192, 32 * 8192,
                    cnt + (PQKV + h) * 32 + 4 * part + m, tmo, part ? 319 : (h >> 3) * 32 + m * 8 + (h & 7), mts, false,
                    bo ? 0 : NIMM, ns, bo ? ns : 0, bo * 257);
  }
  __syncthreads();
  if (role == T_WO || role == T_F2) {
    const bool wo = role == T_WO;
    const int ks = idx >> 3, cn = idx & 7;
    const int lane = lane_id();
    const Dep d{cb, (unsigned)(wo ? PROG + 8 * ks + (lane & 7) : PF1 + 16 * ks + (lane & 15)) * 128, 1u, 0u};
    gemm7<R_PART>(smem, wave_id_, (wo ? Wo : w2) + (size_t)ks * 1024 * DM + cn * 128, nullptr, DM, ws + (wo ? OFF_O : OFF_G) + ks * 16 * 4096, (wo ? 64 : 56) * 4096, d,
                  ws + (wo ? OFF_P : OFF_P2) + (ks * DM + cn * 128) * (TLP ? 64 : 2), (size_t)32 * PROW, cnt + ((wo ? PWO : PF2) + idx) * 32, tmo,
                  wo ? 128 + ks * 32 + cn : 128 + (idx / 7) * 32 + 22 + idx % 7, mts, !wo);
  } else if (role == T_F2C) {
    const int lane = lane_id();
    const Dep d{cb, (unsigned)(PF1 + 48 + (lane & 7)) * 128, 1u, 0u};
    gemm7<R_PART2>(smem, wave_id_, w2 + (size_t)3072 * DM + idx * 256, nullptr, DM, ws + OFF_G + 48 * 4096, 56 * 4096, d,
                   ws + OFF_P2 + (3 * DM + idx * 256) * (TLP ? 64 : 2), (size_t)32 * PROW, cnt + (PF2 + 24 + idx) * 32, tmo, 128 + 96 + 25 + idx, mts, true);
  } else if (role == T_F1) {
    const int lane = lane_id();
    const Dep d{cb, (unsigned)(PLN + (lane & 15)) * 128, 2u, (unsigned)((lane >> 3) & 1)};
    gemm7<R_F1>(smem, wave_id_, w1 + 64 * idx, w3 + 64 * idx, FF, ws + OFF_H2, 65536, d, ws + OFF_G + idx * 4096, 56 * 4096, cnt + (PF1 + idx) * 32, tmo,
                128 + (idx / 14) * 32 + 8 + idx % 14, mts, false);
  } else if (role == T_FIN) {
    fin_role(smem, wave_id_, idx, mod, ws + OFF_P2, (const u16*)(ws + OFF_X1), out, cnt, tmo, mts);
  } else if (role == T_LN) {
    row_role(smem, wave_id_, idx & 7, idx >> 3, mod, ws + OFF_P, x, (u16*)(ws + OFF_X1), ws + OFF_H2, cnt, tmo, 256 + ((idx >> 1) & 3), mts);
  }
}

#define PFNT 0
constexpr int NARM = 4 + 108;
#define ARMWT true
#define NCS 6
__global__ void __launch_bounds__(256) k_prep(const u16* __restrict__ c, const u16* __restrict__ wmod, const u16* __restrict__ mem0,
                                              float* __restrict__ mod, char* __restrict__ ws, unsigned* __restrict__ cnt,
                                              const u16* __restrict__ Wq, const u16* __restrict__ Wk, const u16* __restrict__ Wv, const u16* __restrict__ Wo) {
  const int t = threadIdx.x;
  int b = blockIdx.x;
  char* xch = xch_base(ws);
  if (b < NCALB) {
    if (t >= 64) return;
    const int ci = b % 3, w = b / 3, chunk = ci == 0 ? 0 : ci + 2;
    char* base = xch + ((size_t)chunk << 21) + 127 * 16384 + w * 512;
    st16_sc1(base + (t >> 5) * 8192 + (t & 31) * 16, u32x4{SENT, SENT, SENT, SENT});
    asm volatile("s_waitcnt vmcnt(0)" ::: "memory");
    unsigned best[2] = {1u << 30, 1u << 30};
    const char* sb = uni(base);
#pragma unroll
    for (int r = 1; r < NCS; ++r) {
      unsigned long long t0, t1; unsigned v;
      asm volatile("s_memtime %0\n\ts_waitcnt lgkmcnt(0)\n\ts_load_dword %2, %3, %4 glc\n\ts_waitcnt lgkmcnt(0)\n\ts_memtime %1\n\ts_waitcnt lgkmcnt(0)"
                   : "=&s"(t0), "=&s"(t1), "=&s"(v) : "s"(sb), "i"((r & 1) * 8192 + (r >> 1) * 128) : "memory");
      if (r > 1) best[r & 1] = min(best[r & 1], (unsigned)(t1 - t0));
    }
    unsigned xcc; asm volatile("s_getreg_b32 %0, hwreg(HW_REG_XCC_ID)" : "=s"(xcc));
    const int d = max(-250, min(250, (int)best[1] - (int)best[0]));
    if (t == 0) ((int*)(ws + OFF_CAL))[chunk * NCALW + w] = (xcc & 4) ? -d : d;
    return;
  }
  b -= NCALB;
  if (b < NMODB) {
    __shared__ float red[32][65];
    const int cgp = b & 31, ksl = b >> 5;
    const int cg = t & 7, r0 = t >> 3, n0 = cgp * 64 + cg * 8;
    float acc[8] = {0, 0, 0, 0, 0, 0, 0, 0};
    u32x4 w[4]; u16 cv16[4];
#pragma unroll
    for (int i = 0; i < 4; ++i) {
      const int k = ksl * 128 + r0 + 32 * i;
      w[i] = ldnt<1>(wmod + (size_t)k * (6 * DM) + n0);
      cv16[i] = c[k];
    }
#pragma unroll
    for (int i = 0; i < 4; ++i) {
      const float cv = bf2f(cv16[i]);
      const float sv = bfr(cv / (1.0f + __expf(-cv)));
      acc[0] += sv * bflo(w[i].x); acc[1] += sv * bfhi(w[i].x); acc[2] += sv * bflo(w[i].y); acc[3] += sv * bfhi(w[i].y);
      acc[4] += sv * bflo(w[i].z); acc[5] += sv * bfhi(w[i].z); acc[6] += sv * bflo(w[i].w); acc[7] += sv * bfhi(w[i].w);
    }
#pragma unroll
    for (int e = 0; e < 8; ++e) red[r0][cg * 8 + e] = acc[e];
    __syncthreads();
    if (t < 64) {
      float s = 0;
#pragma unroll
      for (int r = 0; r < 32; ++r) s += red[r][t];
      mod[ksl * (2 * DM) + cgp * 64 + t] = s;
    }
    return;
  }
  b -= NMODB;
  if (b >= NARM) {
    unsigned xcc; asm volatile("s_getreg_b32 %0, hwreg(HW_REG_XCC_ID)" : "=s"(xcc));
    const int j = (b - NARM) >> 3;
    unsigned acc = 0;
#pragma unroll
    for (int i = 0; i < PFN / 32; ++i) {
      const unsigned e = PFT[xcc & 7][i * 32 + j];
      if (e) {
        const int m = e & 3, wide = (e >> 21) & 1, row = ((e >> 8) & 8191) + (wide ? t >> 4 : t), cl = ((e >> 2) & 63) + (wide ? t & 15 : 0);
        const unsigned* p = (const unsigned*)((m == 0 ? Wq : (m == 1 ? Wk : (m == 2 ? Wv : Wo))) + (size_t)row * (m == 3 ? DM : HD) + cl * 64);
        acc ^= PFNT ? __builtin_nontemporal_load(p) : *p;
      }
    }
    asm volatile("" :: "v"(acc));
    return;
  }
  if (b >= 4) {
    b -= 4;
    static_assert(OREP == 2 && AREP == 2, "");
    char* p = b < 48 ? xch + (size_t)((b >> 4) * 32 + (b & 15)) * 16384
            : (b < 56 ? xch + ((size_t)3 << 21) + (size_t)(8 + ((b - 48) >> 2) * 8 + (b & 3)) * 16384
            : (b < 96 ? xch + ((size_t)3 << 21) + (size_t)(b - 56 + 24) * 16384 : xch + ((size_t)4 << 21) + (size_t)(b - 96) * 16384));
    if (b >= 101) return;
    const unsigned v = b < 80 || (b >= 84 && b < 96) ? SENT : 0u;
#pragma unroll
    for (int i = 0; i < 4; ++i) st16<ARMWT>(p + i * 4096 + t * 16, u32x4{v, v, v, v});
    return;
  }
  {
    const int rep = b & 1, par = b >> 1;
    if (b == 2 && t < 64) *(u32x4*)(cnt + t * 4) = u32x4{0u, 0u, 0u, 0u};
    if (b == 1 && t == 0) *(unsigned*)(ws + OFF_TMO) = 0;
    for (int i = 0; i < 4; ++i) {
      const int u = t + 256 * i, pairidx = u >> 6, lane = u & 63, g = lane >> 4, hf = (lane >> 1) & 1, row = ((lane >> 1) & 6) + (lane & 1);
      const int k0 = 64 * pairidx + 16 * g + 8 * hf;
      u32x4 v = *(const u32x4*)(mem0 + row * DM + k0);
      v.x = nosent(v.x); v.y = nosent(v.y); v.z = nosent(v.z); v.w = nosent(v.w);
      *(u32x4*)(xch + ((size_t)3 << 21) + (size_t)(rep * 2 + (u >> 9)) * 16384 + par * 8192 + (u & 511) * 16) = v;
    }
  }
}
extern "C" void kernel_launch(void* const* d_in, const int* in_sizes, int n_in, void* d_out, int out_size, void* d_ws, size_t ws_size,
                              hipStream_t stream) {
  const u16* x    = (const u16*)d_in[0];
  const u16* mem0 = (const u16*)d_in[1];
  const u16* Wq   = (const u16*)d_in[2];
  const u16* Wk   = (const u16*)d_in[3];
  const u16* Wv   = (const u16*)d_in[4];
  const u16* Wo   = (const u16*)d_in[5];
  const u16* w1   = (const u16*)d_in[8];
  const u16* w3   = (const u16*)d_in[9];
  const u16* w2   = (const u16*)d_in[10];
  const u16* c    = (const u16*)d_in[11];
  const u16* wmod = (const u16*)d_in[12];
#define WSHIFT 0
  char* ws = (char*)d_ws + ((size_t)WSHIFT << 20);
  k_prep<<<NCALB + NMODB + NARM + (PFN > 0 ? 256 : 0), 256, 0, stream>>>(c, wmod, mem0, (float*)(ws + OFF_MOD), ws, (unsigned*)(ws + OFF_CNT), Wq, Wk, Wv, Wo);
  hipFuncSetAttribute((const void*)k_mega, hipFuncAttributeMaxDynamicSharedMemorySize, MEGA_LDS);
  k_mega<<<256, 512, MEGA_LDS, stream>>>(Wq, Wk, Wv, Wo, w1, w3, w2, x, c, wmod, ws, (u16*)d_out);
}
```

```cpp
#include <hip/hip_runtime.h>
#include <hip/hip_bf16.h>
#include <stdint.h>
#include <type_traits>
#pragma clang diagnostic ignored "-Winline-asm"
#pragma clang diagnostic ignored "-Wunused-value"

typedef unsigned short u16;
typedef short s16x4 __attribute__((ext_vector_type(4)));
typedef __bf16 bf16x8 __attribute__((ext_vector_type(8)));
typedef float f32x4 __attribute__((ext_vector_type(4)));
typedef float f32x2 __attribute__((ext_vector_type(2)));
typedef unsigned u32x4 __attribute__((ext_vector_type(4)));
typedef unsigned u32x2 __attribute__((ext_vector_type(2)));
#define LAS __attribute__((address_space(3)))

constexpr int SEQ = 2048, DM = 1024, NH = 32, DH = 128, HD = 4096, NMEM = 8, SEG = 32, NSEG = 64, FF = 3584;
constexpr unsigned SENT = 0xFFFFFFFFu;

__device__ __forceinline__ int lane_id() { int l; asm volatile("v_mbcnt_lo_u32_b32 %0, -1, 0\n\tv_mbcnt_hi_u32_b32 %0, -1, %0" : "=v"(l)); return l; }
#define WAVE_ID wave_id_
#define opaque_tid() (WAVE_ID * 64 + lane_id())
__device__ __forceinline__ float bf2f(u16 v) { return __builtin_bit_cast(float, (unsigned)v << 16); }
__device__ __forceinline__ float bflo(unsigned v) { return __builtin_bit_cast(float, v << 16); }
__device__ __forceinline__ float bfhi(unsigned v) { return __builtin_bit_cast(float, v & 0xffff0000u); }
__device__ __forceinline__ float bfr(float x) { return (float)(__bf16)x; }
__device__ __forceinline__ unsigned pack2(float a, float b) {
  typedef __bf16 bf16x2 __attribute__((ext_vector_type(2)));
  bf16x2 v; v[0] = (__bf16)a; v[1] = (__bf16)b; return __builtin_bit_cast(unsigned, v);
}
__device__ __forceinline__ unsigned nosent(unsigned v) { return v == SENT ? 0x7FC07FC0u : v; }
__device__ __forceinline__ bf16x8 asbf8(u32x4 v) { return __builtin_bit_cast(bf16x8, v); }
__device__ __forceinline__ s16x4 ass4(u32x2 v) { return __builtin_bit_cast(s16x4, v); }
__device__ __forceinline__ u32x2 trread(const char* lds_base, unsigned off) {
  s16x4 v = __builtin_amdgcn_ds_read_tr16_b64_v4i16((LAS s16x4*)(lds_base + off));
  return __builtin_bit_cast(u32x2, v);
}
__device__ __forceinline__ void glds16(const void* gsrc, unsigned lds_dst) {
  unsigned keep;
  asm volatile("s_mov_b32 %0, m0\n\ts_mov_b32 m0, %2\n\ts_nop 0\n\tglobal_load_lds_dwordx4 %1, off\n\ts_mov_b32 m0, %0"
               : "=&s"(keep) : "v"(gsrc), "s"(lds_dst) : "memory");
}
#define CNT_ " nt"
#define WNT_ " nt"
__device__ __forceinline__ void glds16_w(const void* gsrc, unsigned lds_dst) {
  unsigned keep;
  asm volatile("s_mov_b32 %0, m0\n\ts_mov_b32 m0, %2\n\ts_nop 0\n\tglobal_load_lds_dwordx4 %1, off" CNT_ "\n\ts_mov_b32 m0, %0"
               : "=&s"(keep) : "v"(gsrc), "s"(lds_dst) : "memory");
}
__device__ __forceinline__ void glds16_sc1(const void* gsrc, unsigned lds_dst) {
  unsigned keep;
  asm volatile("s_mov_b32 %0, m0\n\ts_mov_b32 m0, %2\n\ts_nop 0\n\tglobal_load_lds_dwordx4 %1, off sc1\n\ts_mov_b32 m0, %0"
               : "=&s"(keep) : "v"(gsrc), "s"(lds_dst) : "memory");
}
#define NTM 17
template <int B> __device__ __forceinline__ u32x4 ldnt(const void* p) { if (NTM & B) return __builtin_nontemporal_load((const u32x4*)p); else return *(const u32x4*)p; }
#define MFMA32(a, b, c) __builtin_amdgcn_mfma_f32_16x16x32_bf16((a), (b), (c), 0, 0, 0)

__device__ __forceinline__ void st16_sc1(void* p, u32x4 v) { asm volatile("global_store_dwordx4 %0, %1, off sc1\n\ts_nop 1" :: "v"(p), "v"(v) : "memory"); }
__device__ __forceinline__ void st8_sc1(void* p, u32x2 v) { asm volatile("global_store_dwordx2 %0, %1, off sc1\n\ts_nop 1" :: "v"(p), "v"(v) : "memory"); }
template <bool WT> __device__ __forceinline__ void st8(void* p, u32x2 v) { if (WT) st8_sc1(p, v); else *(u32x2*)p = v; }
template <bool WT> __device__ __forceinline__ void st16(void* p, u32x4 v) { if (WT) st16_sc1(p, v); else *(u32x4*)p = v; }
__device__ __forceinline__ u32x4 ld16_sc1(const void* p) {
  u32x4 r; asm volatile("global_load_dwordx4 %0, %1, off sc1\n\ts_waitcnt vmcnt(0)" : "=&v"(r) : "v"(p) : "memory"); return r;
}
__device__ __forceinline__ void ld16x4_sc1(const void* p, u32x4& a, u32x4& b, u32x4& c, u32x4& d) {
  asm volatile("global_load_dwordx4 %0, %4, off sc1\n\tglobal_load_dwordx4 %1, %4, off offset:1024 sc1\n\t"
               "global_load_dwordx4 %2, %4, off offset:2048 sc1\n\tglobal_load_dwordx4 %3, %4, off offset:3072 sc1\n\ts_waitcnt vmcnt(0)"
               : "=&v"(a), "=&v"(b), "=&v"(c), "=&v"(d) : "v"(p) : "memory");
}
typedef __amdgpu_buffer_rsrc_t rsrc_t;
__device__ __forceinline__ rsrc_t mkrs(const void* p) { return __builtin_amdgcn_make_buffer_rsrc((void*)p, 0, 0x7FFFFFFF, 0x00020000); }
__device__ __forceinline__ u32x4 bld16(rsrc_t r, unsigned off) { return __builtin_amdgcn_raw_buffer_load_b128(r, off, 0, 16); }
__device__ __forceinline__ u32x2 bld8(rsrc_t r, unsigned off) { return __builtin_amdgcn_raw_buffer_load_b64(r, off, 0, 16); }
__device__ __forceinline__ bool ok4(u32x4 v) { return v.x != SENT && v.y != SENT && v.z != SENT && v.w != SENT; }
constexpr int SPIN_CAP = 2000000;
__device__ __forceinline__ bool give_up(int spins, unsigned* tmo, unsigned code) {
  if ((spins & 4095) != 4095) return false;
  if (spins > SPIN_CAP) { __hip_atomic_store(tmo, code, __ATOMIC_RELAXED, __HIP_MEMORY_SCOPE_AGENT); return true; }
  return __hip_atomic_load(tmo, __ATOMIC_RELAXED, __HIP_MEMORY_SCOPE_AGENT) != 0u;
}
template <bool ACQ = true>
__device__ __forceinline__ void wait_cnt(unsigned* c, unsigned n, unsigned* tmo) {
  for (int spins = 0;; ++spins) {
    const unsigned v = __hip_atomic_load(c, __ATOMIC_RELAXED, __HIP_MEMORY_SCOPE_AGENT);
    if (v >= n) break;
    __builtin_amdgcn_s_sleep(8);
    if (give_up(spins, tmo, 5)) break;
  }
  if (ACQ) {
    __builtin_amdgcn_fence(__ATOMIC_ACQUIRE, "agent");
    asm volatile("s_waitcnt vmcnt(0)" ::: "memory");
  }
}

__device__ __forceinline__ const char* uni(const char* p) {
  const unsigned long long v = (unsigned long long)p;
  const unsigned lo = __builtin_amdgcn_readfirstlane((unsigned)v), hi = __builtin_amdgcn_readfirstlane((unsigned)(v >> 32));
  return (const char*)(((unsigned long long)hi << 32) | lo);
}
#define TSCLK "s_memrealtime"
#define TC(k) do { } while (0)
#define TS(k) do { } while (0)
#define TSDECL do { } while (0)
#define TSFLUSH(mask) do { } while (0)

constexpr size_t OFF_MOD  = 0;
constexpr size_t OFF_MOD2 = 65536;
constexpr size_t OFF_TMO  = 98304;
constexpr size_t OFF_EP   = 98304 + 256;
constexpr size_t OFF_CLM  = 102400;
constexpr size_t OFF_INI  = 131072;
constexpr size_t OFF_H1   = 262144;
constexpr size_t OFF_Q    = OFF_H1 + (size_t)SEQ * DM * 2;
constexpr size_t OFF_K    = OFF_Q + (size_t)SEQ * HD * 2;
constexpr size_t OFF_V    = OFF_K + (size_t)SEQ * HD * 2;
constexpr size_t OFF_O    = OFF_V + (size_t)SEQ * HD * 2;
constexpr size_t OFF_X1   = OFF_O + (size_t)SEQ * HD * 2;
constexpr size_t OFF_H2   = OFF_X1 + (size_t)SEQ * DM * 2;
constexpr size_t OFF_G    = OFF_H2 + (size_t)SEQ * DM * 2;
constexpr size_t OFF_XCH  = (size_t)100 << 20;
constexpr size_t OFF_P    = (size_t)112 << 20;
constexpr size_t OFF_P2   = (size_t)128 << 20;
constexpr size_t OFF_DUMP = (size_t)144 << 20;
constexpr int PROW = 4 * DM * 2;
#define TLQ 1
#define TLP 1
static_assert(OFF_G + (size_t)SEQ * FF * 2 <= OFF_XCH, "layout");
__device__ __forceinline__ char* xch_base(char* ws) { return (char*)(((size_t)(ws + OFF_XCH) + ((2u << 20) - 1)) & ~(size_t)((2u << 20) - 1)); }
constexpr int PROG = 16;
constexpr int PQKV = 64;
constexpr int PL1 = 96;
constexpr int PWO = 192;
constexpr int PLN = 224;
constexpr int PF2 = 320;
constexpr int PF1 = 448;
constexpr int L_RED = 131072;
constexpr int L_SLOT = L_RED + 16384;
constexpr int L_GB = L_SLOT + 1024;
constexpr int MEGA_LDS = 163840;
static_assert(L_GB + 12288 + 2048 <= MEGA_LDS, "lds");
__device__ __forceinline__ int bswz(int row) { return (((row >> 1) & 1) | (((row >> 3) & 1) << 1)) << 1; }
template <int OFF, bool SC1 = false>
__device__ __forceinline__ void glds16s(const char* sbase, unsigned voff, unsigned lds_base) {
  unsigned keep;
  if (SC1)
    asm volatile("s_mov_b32 %0, m0\n\ts_add_u32 m0, %3, %4\n\ts_nop 0\n\tglobal_load_lds_dwordx4 %1, %2 sc1\n\ts_mov_b32 m0, %0"
                 : "=&s"(keep) : "v"(voff), "s"(sbase), "s"(lds_base), "i"(OFF) : "memory", "scc");
  else
    asm volatile("s_mov_b32 %0, m0\n\ts_add_u32 m0, %3, %4\n\ts_nop 0\n\tglobal_load_lds_dwordx4 %1, %2\n\ts_mov_b32 m0, %0"
                 : "=&s"(keep) : "v"(voff), "s"(sbase), "s"(lds_base), "i"(OFF) : "memory", "scc");
}
template <int OFF>
__device__ __forceinline__ void glds16x2s(const char* sbase, unsigned voff, unsigned lds_base) {
  unsigned keep;
  asm volatile("s_mov_b32 %0, m0\n\ts_add_u32 m0, %3, %4\n\ts_nop 0\n\tglobal_load_lds_dwordx4 %1, %2 sc1\n\tglobal_load_lds_dwordx4 %1, %2 offset:1024 sc1\n\ts_mov_b32 m0, %0"
               : "=&s"(keep) : "v"(voff), "s"(sbase), "s"(lds_base), "i"(OFF) : "memory", "scc");
}
__device__ __forceinline__ void st16s(const char* sb, unsigned vo, u32x4 v) { asm volatile("global_store_dwordx4 %0, %1, %2 sc1\n\ts_nop 1" :: "v"(vo), "v"(v), "s"(sb) : "memory"); }
__device__ __forceinline__ void head_to_lds(const char* src, int lane, unsigned lds) {
  if (TLQ) {
    const int c = (lane & 15) ^ (lane >> 4);
    const unsigned v0 = (unsigned)((c >> 2) * 2048 + (lane >> 4) * 64 + (c & 3) * 16);
#pragma unroll
    for (int i = 0; i < 8; ++i) glds16s<0, true>(src + i * 256, v0 ^ ((i & 3) * 2048), lds + i * 1024);
  } else {
#pragma unroll
    for (int i = 0; i < 4; ++i) glds16x2s<0>(src + i * 2048, (unsigned)lane * 16, lds + i * 2048);
  }
}
#define WAIT_V(n) asm volatile("s_waitcnt vmcnt(" #n ")" ::: "memory")
#define BAR() do { asm volatile("s_waitcnt lgkmcnt(0)" ::: "memory"); __builtin_amdgcn_s_barrier(); asm volatile("" ::: "memory"); } while (0)
__device__ __forceinline__ void st4_sc1(void* p, unsigned v) { asm volatile("global_store_dword %0, %1, off sc1" :: "v"(p), "v"(v) : "memory"); }
__device__ __forceinline__ unsigned ld4_sc1(const void* p) {
  unsigned r; asm volatile("global_load_dword %0, %1, off sc1\n\ts_waitcnt vmcnt(0)" : "=&v"(r) : "v"(p) : "memory"); return r;
}
template <int CTRL> __device__ __forceinline__ unsigned dppu(unsigned x) { return (unsigned)__builtin_amdgcn_update_dpp(0, (int)x, CTRL, 0xf, 0xf, false); }
__device__ __forceinline__ unsigned rowmin16(unsigned v) {
  v = min(v, dppu<0x128>(v)); v = min(v, dppu<0x124>(v)); v = min(v, dppu<0x122>(v)); v = min(v, dppu<0x121>(v));
  return v;
}
__device__ __forceinline__ unsigned rdl(unsigned v, int l) { return (unsigned)__builtin_amdgcn_readlane((int)v, l); }
constexpr float SCALE = 0.08838834764831845f;
constexpr float LOG2E = 1.4426950408889634f;
template <int N> __device__ __forceinline__ void delay64() {
  if (N >= 16) { __builtin_amdgcn_s_sleep(15); delay64<(N >= 16 ? N - 15 : 0)>(); } else if (N > 0) __builtin_amdgcn_s_sleep(N);
}
__device__ __forceinline__ void sw16(float x, float& a, float& b) {
  a = x; b = x; asm volatile("s_nop 1\n\tv_permlane16_swap_b32 %0, %1" : "+v"(a), "+v"(b));
}
__device__ __forceinline__ void sw32(float x, float& a, float& b) {
  a = x; b = x; asm volatile("s_nop 1\n\tv_permlane32_swap_b32 %0, %1" : "+v"(a), "+v"(b));
}
__device__ __forceinline__ float red16_32_max(float x) {
  float a, b; sw16(x, a, b); x = fmaxf(a, b); sw32(x, a, b); return fmaxf(a, b);
}
__device__ __forceinline__ float red16_32_add(float x) {
  float a, b; sw16(x, a, b); x = a + b; sw32(x, a, b); return a + b;
}
__device__ __forceinline__ unsigned cvtpk(float a, float b) { unsigned r; asm("v_cvt_pk_bf16_f32 %0, %1, %2" : "=v"(r) : "v"(a), "v"(b)); return r; }
__device__ __forceinline__ float max3(float a, float b, float c) { float r; asm("v_max3_f32 %0, %1, %2, %3" : "=v"(r) : "v"(a), "v"(b), "v"(c)); return r; }
__device__ __forceinline__ float max2(float a, float b) { float r; asm("v_max_f32 %0, %1, %2" : "=v"(r) : "v"(a), "v"(b)); return r; }
__device__ __forceinline__ unsigned ror8u(unsigned x) { return (unsigned)__builtin_amdgcn_update_dpp(0, (int)x, 0x128, 0xf, 0xf, false); }
__device__ __forceinline__ float ror8(float x) { return __builtin_bit_cast(float, ror8u(__builtin_bit_cast(unsigned, x))); }
__device__ __forceinline__ float add_ror8(float x) {
  return x + __builtin_bit_cast(float, __builtin_amdgcn_update_dpp(0, __builtin_bit_cast(int, x), 0x128, 0xf, 0xf, false));
}

__device__ __forceinline__ void ini_request(const char* ws, unsigned lds) {
  const char* p = uni(ws + OFF_INI);
  asm volatile("s_mov_b32 m0, %2\n\ts_nop 3\n\tglobal_load_lds_dword %0, %1 sc1\n\ts_add_u32 m0, %2, 252\n\ts_nop 0\n\tglobal_load_lds_dword %0, %1 offset:4 sc1"
               :: "v"(lane_id() * 128), "s"(p), "s"(lds) : "memory", "m0", "scc");
}
__device__ __forceinline__ unsigned ini_resolve(const char* ws, const char* l, unsigned long long tag) {
  const int lane = lane_id();
  u32x2 v = u32x2{~(unsigned)tag, 0u};
  if (l) v = u32x2{*(const volatile LAS unsigned*)(l + lane * 4), *(const volatile LAS unsigned*)(l + 256 + lane * 4)};
  for (int spins = 0; !__all(v.x == (unsigned)tag && (v.y >> 16) == (unsigned)(tag >> 48)) && spins < SPIN_CAP; ++spins) {
    if (spins) __builtin_amdgcn_s_sleep(8);
    asm volatile("global_load_dwordx2 %0, %1, off sc1\n\ts_waitcnt vmcnt(0)" : "=&v"(v) : "v"(ws + OFF_INI + lane * 128) : "memory");
  }
  const unsigned long long up = __ballot((v.y & 3u) == 1u), dn = __ballot((v.y & 3u) == 2u);
  unsigned par = 0;
#pragma unroll
  for (int c = 0; c < 3; ++c) { const unsigned long long m = (0x249ull << c) * 0x0001000100010001ull; par |= (__popcll(up & m) > __popcll(dn & m) ? 0u : 1u) << c; }
  par = __builtin_amdgcn_readfirstlane(par);
  asm volatile("" : "+s"(par));
  return par;
}
template <int M0> __device__ __forceinline__ unsigned qkv_avail(const unsigned* line) {
  const unsigned v = ld4_sc1(line + (lane_id() & 15));
  unsigned r = NSEG;
#pragma unroll
  for (int m = M0; m < 3; ++m) {
    unsigned a = rdl(v, m);
#pragma unroll
    for (int p = 1; p < 3; ++p) { const unsigned w = rdl(v, 4 * p + m); if (a >= (w >> 8)) a = max(a, w & 255u); }
    r = min(r, a);
  }
  return r;
}
__device__ __forceinline__ void wait_l1(const unsigned* cnt, unsigned n, unsigned* tmo) {
  const int lane = lane_id();
  for (int spins = 0;; ++spins) {
    const unsigned v = ld4_sc1(cnt + (PL1 + (lane & 31)) * 32);
    if (__all(v >= n)) break;
    __builtin_amdgcn_s_sleep(8);
    if (give_up(spins, tmo, 12)) break;
  }
}
#define GKV 1
__device__ __forceinline__ void wait_k0(const unsigned* cnt, int h, unsigned* tmo, bool kv = true) {
  for (int spins = 0;; ++spins) {
    const int l = lane_id();
    const unsigned v = ld4_sc1(cnt + (PQKV + h) * 32 + (l == 0 || !kv ? 3 : 1 + (l & 1)));
    if (__all(v >= (l == 0 ? 1u : (unsigned)GKV))) break;
    __builtin_amdgcn_s_sleep(32);
    if (give_up(spins, tmo, 13)) break;
  }
}
#define KTON 0
#define WCLK(slot, b) do { } while (0)
#define WTM(slot, b, k) do { } while (0)
#define WTS(slot, b, k) do { } while (0)
#define KTS(k_) do { } while (0)
#define STS(ws_, slot_, k_) do { } while (0)
typedef __bf16 bf16x2 __attribute__((ext_vector_type(2)));
__device__ __forceinline__ f32x2 pkfma(f32x2 a, f32x2 b, f32x2 c) { return __builtin_elementwise_fma(a, b, c); }
__device__ __forceinline__ float wavesum(float v) {
  v += __builtin_bit_cast(float, dppu<0x128>(__builtin_bit_cast(unsigned, v))); v += __builtin_bit_cast(float, dppu<0x124>(__builtin_bit_cast(unsigned, v)));
  v += __builtin_bit_cast(float, dppu<0x122>(__builtin_bit_cast(unsigned, v))); v += __builtin_bit_cast(float, dppu<0x121>(__builtin_bit_cast(unsigned, v)));
  return red16_32_add(v);
}
__device__ __forceinline__ void bst16(rsrc_t r, unsigned off, unsigned soff, u32x4 v) { __builtin_amdgcn_raw_buffer_store_b128(v, r, off + soff, 0, 16); }
__device__ __forceinline__ float adds(unsigned v, unsigned sel, float c) { return __builtin_amdgcn_fdot2_f32_bf16(__builtin_bit_cast(bf16x2, v), __builtin_bit_cast(bf16x2, sel), c, false); }
__device__ __forceinline__ void sum4(const u32x4 (&p)[4], f32x2 (&a)[4]) {
  unsigned slo = 0x00003F80u, shi = 0x3F800000u;
  asm("" : "+s"(slo), "+s"(shi));
#pragma unroll
  for (int e = 0; e < 4; ++e) {
    float lo = bflo(p[0][e]), hi = bfhi(p[0][e]);
#pragma unroll
    for (int q = 1; q < 4; ++q) { lo = adds(p[q][e], slo, lo); hi = adds(p[q][e], shi, hi); }
    a[e] = f32x2{lo, hi};
  }
}
__device__ __forceinline__ u32x4 norm8(const f32x2 (&v)[4], const char* mt, float mb, float rden) {
  const f32x4 h0 = *(const f32x4*)(mt + 4096), h1 = *(const f32x4*)(mt + 4096 + 1024), c0 = *(const f32x4*)(mt + 8192), c1 = *(const f32x4*)(mt + 8192 + 1024);
  const f32x2 m2 = f32x2{mb, mb}, d2 = f32x2{rden, rden};
  const f32x2 r0 = pkfma(v[0] - m2, f32x2{c0[0], c0[1]} * d2, f32x2{h0[0], h0[1]}), r1 = pkfma(v[1] - m2, f32x2{c0[2], c0[3]} * d2, f32x2{h0[2], h0[3]});
  const f32x2 r2 = pkfma(v[2] - m2, f32x2{c1[0], c1[1]} * d2, f32x2{h1[0], h1[1]}), r3 = pkfma(v[3] - m2, f32x2{c1[2], c1[3]} * d2, f32x2{h1[2], h1[3]});
  return u32x4{pack2(r0[0], r0[1]), pack2(r1[0], r1[1]), pack2(r2[0], r2[1]), pack2(r3[0], r3[1])};
}
__device__ __forceinline__ void half_load(const char* P, const u16* res, int row, int hf, int lane, f32x2 (&a)[4], u32x4& rv) {
  const rsrc_t rp = mkrs(P), rr = mkrs(res);
  const unsigned po = TLP ? (unsigned)((row >> 5) * 32 * PROW + (hf * 16 + (lane >> 2)) * 2048 + (row & 31) * 64 + (lane & 3) * 16) : (unsigned)(row * PROW + hf * 1024 + lane * 16), ro = (unsigned)(row * DM * 2 + hf * 1024 + lane * 16);
  u32x4 p[4];
#pragma unroll
  for (int q = 0; q < 4; ++q) p[q] = bld16(rp, po + q * (TLP ? 65536 : 2048));
  if (res) rv = bld16(rr, ro);
  sum4(p, a);
}
__device__ __forceinline__ void fin_half(const char* P, const u16* res, const char* gbt, u16* __restrict__ o1, int row, int hf, int lane) {
  f32x2 a[4]; u32x4 rv;
  half_load(P, res, row, hf, lane, a, rv);
  const u32x4 gv = *(const u32x4*)(gbt + hf * 1024 + lane * 16);
  u32x4 o;
#pragma unroll
  for (int e = 0; e < 4; ++e) { const f32x2 s = pkfma(f32x2{bflo(gv[e]), bfhi(gv[e])}, a[e], f32x2{bflo(rv[e]), bfhi(rv[e])}); o[e] = pack2(s[0], s[1]); }
  st16_sc1(o1 + (size_t)row * DM + hf * 512 + lane * 8, o);
}
__device__ __forceinline__ void ln_half(const u32x4 (&p)[4], u32x4 rv, const f32x4 (&tb)[6], volatile LAS float* xs, const char* x1, unsigned xo, const char* h2, unsigned ho,
                                        int wave, int lane) {
  f32x2 a[4], v[4];
  sum4(p, a);
  u32x4 o;
#pragma unroll
  for (int e = 0; e < 4; ++e) {
    const f32x2 s = pkfma(f32x2{tb[e >> 1][2 * (e & 1)], tb[e >> 1][2 * (e & 1) + 1]}, a[e], f32x2{bflo(rv[e]), bfhi(rv[e])});
    o[e] = pack2(s[0], s[1]);
    v[e] = f32x2{bflo(o[e]), bfhi(o[e])};
  }
  const float c = __builtin_bit_cast(float, __builtin_amdgcn_readfirstlane(__builtin_bit_cast(int, v[0][0])));
  f32x2 s2 = f32x2{0, 0}, q2 = s2;
#pragma unroll
  for (int i = 0; i < 4; ++i) { const f32x2 d = v[i] - f32x2{c, c}; s2 += d; q2 = pkfma(d, d, q2); }
  float s = s2[0] + s2[1], q = q2[0] + q2[1];
#define DA(x, k) x += __builtin_bit_cast(float, dppu<k>(__builtin_bit_cast(unsigned, x)))
  DA(s, 0x128); DA(q, 0x128); DA(s, 0x124); DA(q, 0x124); DA(s, 0x122); DA(q, 0x122); DA(s, 0x121); DA(q, 0x121);
#undef DA
  {
    asm volatile("s_nop 1\n\tv_permlane16_swap_b32 %0, %1" : "+v"(s), "+v"(q));
    s += q; q = s;
    asm volatile("s_nop 1\n\tv_permlane32_swap_b32 %0, %1" : "+v"(s), "+v"(q));
    s += q;
  }
  if (lane < 48 && (lane & 15) == 0) xs[wave * 4 + (lane >> 4)] = lane == 32 ? c : s;
  st16s(x1, xo, o);
  BAR();
  const f32x4 u0 = *(const volatile LAS f32x4*)(xs + (wave & 6) * 4), u1 = *(const volatile LAS f32x4*)(xs + (wave | 1) * 4);
  const float m0 = u0[0] * (2.0f / DM), m1 = u1[0] * (2.0f / DM);
  const float dm = (u0[2] - u1[2]) + (m0 - m1);
  const float mean = ((u0[2] + m0) + (u1[2] + m1)) * 0.5f;
  const float qq = ((u0[1] - u0[0] * m0) + (u1[1] - u1[0] * m1)) + dm * dm * (DM / 4);
  const float mb = bfr(mean), vb = bfr(fmaxf(qq, 0.0f) * (1.0f / DM));
  const float rden = __builtin_amdgcn_rcpf(bfr(__builtin_amdgcn_sqrtf(bfr(vb + 1e-5f))));
  const f32x2 m2 = f32x2{mb, mb}, d2 = f32x2{rden, rden};
  u32x4 r;
#pragma unroll
  for (int e = 0; e < 4; ++e) {
    const f32x2 t = pkfma(v[e] - m2, f32x2{tb[4 + (e >> 1)][2 * (e & 1)], tb[4 + (e >> 1)][2 * (e & 1) + 1]} * d2, f32x2{tb[2 + (e >> 1)][2 * (e & 1)], tb[2 + (e >> 1)][2 * (e & 1) + 1]});
    r[e] = pack2(t[0], t[1]);
  }
  st16s(h2, ho, r);
}
__device__ __forceinline__ void row_table(char* tab, const float* mod, const unsigned* cnt, unsigned* tmo, int tid) {
  wait_l1(cnt, 9u, tmo);
  const rsrc_t rm = mkrs((const char*)mod + OFF_MOD2);
#pragma unroll
  for (int i = 0; i < 2; ++i) {
    const int u = tid + 512 * i, a = u >> 8, c = u & 255;
    f32x4 v = __builtin_bit_cast(f32x4, bld16(rm, (unsigned)u * 16));
    v = f32x4{bfr(v[0]), bfr(v[1]), bfr(v[2]), bfr(v[3])};
    if (a == 2) v = f32x4{bfr(1.0f + v[0]), bfr(1.0f + v[1]), bfr(1.0f + v[2]), bfr(1.0f + v[3])};
    if (a < 3) *(f32x4*)(tab + a * 4096 + ((c >> 7) * 2 + (c & 1)) * 1024 + ((c >> 1) & 63) * 16) = v;
    else *(u32x2*)(tab + 12288 + c * 8) = u32x2{pack2(v[0], v[1]), pack2(v[2], v[3])};
  }
  __syncthreads();
}
#define LS(i) do { } while (0)
#define LNSL 2
__device__ __forceinline__ void row_role(char* smem, int wave_id_, int grp, int set, const float* mod, const char* P, const u16* x, u16* x1, char* h2c,
                                         unsigned* cnt, unsigned* tmo, int slot, unsigned long long* mts) {
  const int tid = opaque_tid(), lane = tid & 63, wave = WAVE_ID;
  if (tid == 0) *(volatile LAS unsigned*)(smem + 16384 + 256) = 0u;
  row_table(smem, mod, cnt, tmo, tid);
  static_assert(TLP, "");
  const int hf = wave & 1, r4 = 4 * grp + (wave >> 1);
  const unsigned pw2 = (unsigned)(PWO + ((lane >> 2) & 3) * 8 + hf * 4 + (lane & 3)) * 128;
  unsigned* myp = cnt + (PLN + 8 * set + grp) * 32;
  unsigned pv = 0;
  u32x4 p[4], rv, rn;
  bool req = false;
  const char* pb = uni(P); const char* cb = uni((const char*)cnt); const char* xs_ = uni((const char*)x);
  auto publish = [&](int n) {
    if (lane == 0 && (__hip_atomic_fetch_add((LAS unsigned*)(smem + 16384 + 256), 1u, __ATOMIC_RELAXED, __HIP_MEMORY_SCOPE_WORKGROUP) & 7u) == 7u) st4_sc1(myp, (unsigned)n);
  };
  f32x4 tb[6];
#pragma unroll
  for (int i = 0; i < 6; ++i) tb[i] = *(const f32x4*)(smem + (i >> 1) * 4096 + hf * 2048 + (i & 1) * 1024 + lane * 16);
  const unsigned po = (unsigned)((hf * 16 + (lane >> 2)) * 2048 + r4 * 64 + (lane & 3) * 16), xo = (unsigned)(r4 * DM * 2 + hf * 1024 + lane * 16);
  const unsigned ho = (unsigned)(r4 * 128 + (((lane & 7) ^ (r4 & 7)) << 4) + (lane >> 3) * 4096 + hf * 32768);
  rn = *(const u32x4*)(xs_ + set * 65536 + xo);
  for (int bb = set; bb < NSEG; bb += 2) {
    if (!req) {
      for (int spins = 0; !__all(pv > (unsigned)bb); ++spins) {
        pv = ld4_sc1((const char*)cnt + pw2);
        if (__all(pv > (unsigned)bb)) break;
        __builtin_amdgcn_s_sleep(LNSL);
        if (give_up(spins, tmo, 11)) break;
      }
    }
    WTM(slot, bb, 0);
    LS(0);
    asm volatile("s_nop 4\n\tglobal_load_dwordx4 %0, %5, %9 sc1\n\tglobal_load_dwordx4 %1, %6, %9 sc1\n\tglobal_load_dwordx4 %2, %7, %9 sc1\n\tglobal_load_dwordx4 %3, %8, %9 sc1\n\t"
                 "global_load_dword %4, %10, %11 sc1\n\ts_waitcnt vmcnt(0)"
                 : "=&v"(p[0]), "=&v"(p[1]), "=&v"(p[2]), "=&v"(p[3]), "=&v"(pv)
                 : "v"(po), "v"(po + 65536), "v"(po + 131072), "v"(po + 196608), "s"(pb + (size_t)bb * 32 * PROW), "v"(pw2), "s"(cb) : "memory");
    asm volatile("v_mov_b32 %0, %4\n\tv_mov_b32 %1, %5\n\tv_mov_b32 %2, %6\n\tv_mov_b32 %3, %7" : "=&v"(rv.x), "=&v"(rv.y), "=&v"(rv.z), "=&v"(rv.w) : "v"(rn.x), "v"(rn.y), "v"(rn.z), "v"(rn.w) : "memory");
    if (bb + 2 < NSEG) rn = *(const u32x4*)(xs_ + (size_t)(bb + 2) * 65536 + xo);
    LS(1);
    if (req) publish(bb >> 1);
    ln_half(p, rv, tb, (volatile LAS float*)(smem + 16384 + ((bb >> 1) & 1) * 128), uni((const char*)x1) + (size_t)bb * 65536, xo, uni(h2c) + (size_t)bb * 65536, ho, wave, lane);
    LS(2);
    req = bb + 2 < NSEG && __all(pv > (unsigned)(bb + 2));
    if (!req) {
      if (bb + 2 < NSEG) pv = ld4_sc1((const char*)cnt + pw2);
      WAIT_V(0);
      LS(3);
      publish((bb >> 1) + 1);
    }
    WTM(slot, bb, 2);
  }
}
#define L1G 0
__device__ __forceinline__ void l1_role(char* smem, int wave_id_, int c, const u16* __restrict__ x, const float* __restrict__ mod, const u16* __restrict__ cvec,
                                        const u16* __restrict__ wmod, char* __restrict__ h1c, unsigned* cnt, unsigned* tmo, unsigned long long* mts) {
  const int tid = opaque_tid(), lane = tid & 63, wave = WAVE_ID;
  u32x4 xr[8][2];
  const u16* src = x + (size_t)(8 * c + wave) * DM + lane * 8;
  xr[0][0] = ldnt<2>(src); xr[0][1] = ldnt<2>(src + 512);
  {
    f32x4 m[4];
    const rsrc_t rm = mkrs(mod);
#pragma unroll
    for (int k = 0; k < 4; ++k) m[k] = __builtin_bit_cast(f32x4, bld16(rm, (unsigned)(k * (2 * DM) + tid * 4) * 4));
    const float sva = bf2f(cvec[tid]), svb = bf2f(cvec[tid + 512]);
    f32x4 v = (m[0] + m[1]) + (m[2] + m[3]);
    v = f32x4{bfr(v[0]), bfr(v[1]), bfr(v[2]), bfr(v[3])};
    const int a = tid >> 8, cc = tid & 255;
    if (a) v = f32x4{bfr(1.0f + v[0]), bfr(1.0f + v[1]), bfr(1.0f + v[2]), bfr(1.0f + v[3])};
    *(f32x4*)(smem + 4096 + a * 4096 + ((cc >> 7) * 2 + (cc & 1)) * 1024 + ((cc >> 1) & 63) * 16) = v;
    float* sv = (float*)(smem + 12288);
    sv[tid] = bfr(sva / (1.0f + __expf(-sva))); sv[tid + 512] = bfr(svb / (1.0f + __expf(-svb)));
  }
  __syncthreads();
#pragma unroll
  for (int p = 0; p < 8; ++p) {
    f32x2 v[2][4];
#pragma unroll
    for (int i = 0; i < 2; ++i)
#pragma unroll
      for (int e = 0; e < 4; ++e) v[i][e] = f32x2{bflo(xr[p][i][e]), bfhi(xr[p][i][e])};
    const f32x2 s2 = ((v[0][0] + v[0][1]) + (v[0][2] + v[0][3])) + ((v[1][0] + v[1][1]) + (v[1][2] + v[1][3]));
    const float mean = wavesum(s2[0] + s2[1]) * (1.0f / DM);
    f32x2 q2 = f32x2{0, 0};
#pragma unroll
    for (int i = 0; i < 8; ++i) { const f32x2 d = v[i >> 2][i & 3] - f32x2{mean, mean}; q2 = pkfma(d, d, q2); }
    const float q = wavesum(q2[0] + q2[1]);
    if (p == 0 && wave == 0) STS((char*)tmo - OFF_TMO, 128 + (c >> 3) * 32 + 24 + (c & 7), 0);
    const float mb = bfr(mean), vb = bfr(q * (1.0f / DM));
    const float rden = 1.0f / bfr(sqrtf(bfr(vb + 1e-5f)));
    const int row = 256 * p + 8 * c + wave, rb = row & 31;
    char* dst = h1c + (size_t)(row >> 5) * 65536 + (lane >> 3) * 4096 + rb * 128 + (((lane & 7) ^ (rb & 7)) << 4);
    const char* ml = smem + lane * 16;
#pragma unroll
    for (int i = 0; i < 2; ++i) st16_sc1(dst + i * 32768, norm8(v[i], ml + i * 2048, mb, rden));
    WAIT_V(0);
    __builtin_amdgcn_s_barrier();
    if (tid == 0) st4_sc1(cnt + (PL1 + c) * 32, (unsigned)(p + 1));
    if (p == 0) {
      if (L1G) wait_k0(cnt, c, tmo, L1G == 1);
#pragma unroll
      for (int pp = 1; pp < 8; ++pp) { xr[pp][0] = ldnt<2>(src + (size_t)pp * 256 * DM); xr[pp][1] = ldnt<2>(src + (size_t)pp * 256 * DM + 512); }
    }
    if (wave == 0) WTS(c, p, 0);
    if (p == 0 && wave == 0) STS((char*)tmo - OFF_TMO, 128 + (c >> 3) * 32 + 24 + (c & 7), 1);
  }
  wait_k0(cnt, c, tmo);
  {
    const int cg = tid & 15, r0 = tid >> 4;
    const u16* wp = wmod + (size_t)r0 * (6 * DM) + 2 * DM + 128 * c + cg * 8;
    const float* sv = (const float*)(smem + 12288) + r0;
    f32x2 acc[4] = {f32x2{0, 0}, f32x2{0, 0}, f32x2{0, 0}, f32x2{0, 0}};
#pragma unroll 1
    for (int o = 0; o < 4; ++o) {
      u32x4 w[8];
#pragma unroll
      for (int i = 0; i < 8; ++i) w[i] = ldnt<1>(wp + (size_t)(o * 8 + i) * 32 * (6 * DM));
#pragma unroll
      for (int i = 0; i < 8; ++i) {
        const float s = sv[(o * 8 + i) * 32];
#pragma unroll
        for (int e = 0; e < 4; ++e) acc[e] = pkfma(f32x2{s, s}, f32x2{bflo(w[i][e]), bfhi(w[i][e])}, acc[e]);
      }
    }
    float* red = (float*)(smem + 16384);
    *(f32x4*)(red + r0 * 128 + cg * 8) = f32x4{acc[0][0], acc[0][1], acc[1][0], acc[1][1]};
    *(f32x4*)(red + r0 * 128 + cg * 8 + 4) = f32x4{acc[2][0], acc[2][1], acc[3][0], acc[3][1]};
    __syncthreads();
    if (tid < 128) {
      float s = 0;
#pragma unroll
      for (int r = 0; r < 32; ++r) s += red[r * 128 + tid];
      st4_sc1((char*)mod + OFF_MOD2 + (128 * c + tid) * 4, __builtin_bit_cast(unsigned, s));
    }
    WAIT_V(0);
    __builtin_amdgcn_s_barrier();
    if (tid == 0) st4_sc1(cnt + (PL1 + c) * 32, 9u);
  }
}
enum { R_QKV = 0, R_PART = 1, R_F1 = 2, R_PART2 = 3 };
struct Dep {
  const char* base; unsigned voff; unsigned mul, add;
};
__device__ __forceinline__ unsigned dep_eval(unsigned v, const Dep& d) {
  v = rowmin16(v * d.mul + d.add);
  return min(min(rdl(v, 0), rdl(v, 16)), min(rdl(v, 32), rdl(v, 48)));
}
typedef unsigned u32x32 __attribute__((ext_vector_type(32)));
typedef unsigned u32x16 __attribute__((ext_vector_type(16)));
__device__ __forceinline__ void fast_ld0(const u32x32 (&W)[4], u32x16& pd, unsigned ea, unsigned da, int& b, int& lf, int cp, unsigned sa, unsigned mu, unsigned ad,
    unsigned rw, unsigned ab, const char* p1, const char* p0, unsigned l1, unsigned l0, const unsigned (&av)[8]) {
  asm volatile("s_setprio 0\n\t"
    "s_waitcnt vmcnt(12)\n\t"
    "s_barrier\n\t"
    "ds_read_b128 v[96:99], %[ea] offset:0\n\t"
    "ds_read_b128 v[100:103], %[ea] offset:2048\n\t"
    "ds_read_b128 v[104:107], %[da] offset:0\n\t"
    "ds_read_b128 v[108:111], %[da] offset:2048\n\t"
    "ds_read_b128 v[112:115], %[ea] offset:4096\n\t"
    "ds_read_b128 v[116:119], %[ea] offset:6144\n\t"
    "s_branch 2f\n\t"
    "1:\n\t"
    "s_waitcnt vmcnt(12)\n\t"
    "s_nop 1\n\t"
    "s_barrier\n\t"
    "2:\n\t"
    "s_waitcnt lgkmcnt(4)\n\t"
    "v_mfma_f32_16x16x32_bf16 v[64:67], v[128:131], v[96:99], 0\n\t"
    "ds_read_b128 v[120:123], %[da] offset:4096\n\t"
    "s_add_u32 m0, %[l1], 0\n\t"
    "ds_write_b128 %[rw], v[80:83] offset:0\n\t"
    "v_mfma_f32_16x16x32_bf16 v[68:71], v[132:135], v[96:99], 0\n\t"
    "ds_read_b128 v[124:127], %[da] offset:6144\n\t"
    "global_load_lds_dwordx4 %[av0], s[36:37] sc1\n\t"
    "ds_write_b128 %[rw], v[84:87] offset:1024\n\t"
    "v_mfma_f32_16x16x32_bf16 v[72:75], v[128:131], v[100:103], 0\n\t"
    "s_add_u32 m0, %[l1], 32768\n\t"
    "ds_write_b128 %[rw], v[88:91] offset:2048\n\t"
    "v_mfma_f32_16x16x32_bf16 v[76:79], v[132:135], v[100:103], 0\n\t"
    "global_load_lds_dwordx4 %[av1], s[36:37] sc1\n\t"
    "ds_write_b128 %[rw], v[92:95] offset:3072\n\t"
    "s_waitcnt lgkmcnt(8)\n\t"
    "v_mfma_f32_16x16x32_bf16 v[64:67], v[136:139], v[104:107], v[64:67]\n\t"
    "ds_read_b128 v[96:99], %[ea] offset:8192\n\t"
    "s_add_u32 m0, %[l1], 4096\n\t"
    "v_mfma_f32_16x16x32_bf16 v[68:71], v[140:143], v[104:107], v[68:71]\n\t"
    "ds_read_b128 v[100:103], %[ea] offset:10240\n\t"
    "global_load_lds_dwordx4 %[av2], s[36:37] sc1\n\t"
    "v_mfma_f32_16x16x32_bf16 v[72:75], v[136:139], v[108:111], v[72:75]\n\t"
    "s_add_u32 m0, %[l1], 36864\n\t"
    "v_mfma_f32_16x16x32_bf16 v[76:79], v[140:143], v[108:111], v[76:79]\n\t"
    "global_load_lds_dwordx4 %[av3], s[36:37] sc1\n\t"
    "s_waitcnt lgkmcnt(8)\n\t"
    "v_mfma_f32_16x16x32_bf16 v[64:67], v[144:147], v[112:115], v[64:67]\n\t"
    "ds_read_b128 v[104:107], %[da] offset:8192\n\t"
    "s_add_u32 m0, %[l1], 8192\n\t"
    "v_mfma_f32_16x16x32_bf16 v[68:71], v[148:151], v[112:115], v[68:71]\n\t"
    "ds_read_b128 v[108:111], %[da] offset:10240\n\t"
    "global_load_lds_dwordx4 %[av4], s[36:37] sc1\n\t"
    "v_mfma_f32_16x16x32_bf16 v[72:75], v[144:147], v[116:119], v[72:75]\n\t"
    "s_add_u32 m0, %[l1], 40960\n\t"
    "v_mfma_f32_16x16x32_bf16 v[76:79], v[148:151], v[116:119], v[76:79]\n\t"
    "global_load_lds_dwordx4 %[av5], s[36:37] sc1\n\t"
    "s_waitcnt lgkmcnt(7)\n\t"
    "v_mfma_f32_16x16x32_bf16 v[64:67], v[152:155], v[120:123], v[64:67]\n\t"
    "ds_read_b128 v[112:115], %[ea] offset:12288\n\t"
    "s_add_u32 m0, %[l1], 12288\n\t"
    "v_mfma_f32_16x16x32_bf16 v[68:71], v[156:159], v[120:123], v[68:71]\n\t"
    "ds_read_b128 v[116:119], %[ea] offset:14336\n\t"
    "global_load_lds_dwordx4 %[av6], s[36:37] sc1\n\t"
    "v_mfma_f32_16x16x32_bf16 v[72:75], v[152:155], v[124:127], v[72:75]\n\t"
    "s_add_u32 m0, %[l1], 45056\n\t"
    "v_mfma_f32_16x16x32_bf16 v[76:79], v[156:159], v[124:127], v[76:79]\n\t"
    "global_load_lds_dwordx4 %[av7], s[36:37] sc1\n\t"
    "s_waitcnt lgkmcnt(4)\n\t"
    "v_mfma_f32_16x16x32_bf16 v[64:67], v[160:163], v[96:99], v[64:67]\n\t"
    "ds_read_b128 v[120:123], %[da] offset:12288\n\t"
    "v_mfma_f32_16x16x32_bf16 v[68:71], v[164:167], v[96:99], v[68:71]\n\t"
    "ds_read_b128 v[124:127], %[da] offset:14336\n\t"
    "v_mfma_f32_16x16x32_bf16 v[72:75], v[160:163], v[100:103], v[72:75]\n\t"
    "v_mfma_f32_16x16x32_bf16 v[76:79], v[164:167], v[100:103], v[76:79]\n\t"
    "s_waitcnt lgkmcnt(4)\n\t"
    "v_mfma_f32_16x16x32_bf16 v[64:67], v[168:171], v[104:107], v[64:67]\n\t"
    "ds_read_b128 v[96:99], %[ea] offset:16384\n\t"
    "v_mfma_f32_16x16x32_bf16 v[68:71], v[172:175], v[104:107], v[68:71]\n\t"
    "ds_read_b128 v[100:103], %[ea] offset:18432\n\t"
    "v_mfma_f32_16x16x32_bf16 v[72:75], v[168:171], v[108:111], v[72:75]\n\t"
    "v_mfma_f32_16x16x32_bf16 v[76:79], v[172:175], v[108:111], v[76:79]\n\t"
    "s_waitcnt lgkmcnt(4)\n\t"
    "v_mfma_f32_16x16x32_bf16 v[64:67], v[176:179], v[112:115], v[64:67]\n\t"
    "ds_read_b128 v[104:107], %[da] offset:16384\n\t"
    "v_mfma_f32_16x16x32_bf16 v[68:71], v[180:183], v[112:115], v[68:71]\n\t"
    "ds_read_b128 v[108:111], %[da] offset:18432\n\t"
    "v_mfma_f32_16x16x32_bf16 v[72:75], v[176:179], v[116:119], v[72:75]\n\t"
    "v_mfma_f32_16x16x32_bf16 v[76:79], v[180:183], v[116:119], v[76:79]\n\t"
    "s_waitcnt lgkmcnt(4)\n\t"
    "v_mfma_f32_16x16x32_bf16 v[64:67], v[184:187], v[120:123], v[64:67]\n\t"
    "ds_read_b128 v[112:115], %[ea] offset:20480\n\t"
    "v_mfma_f32_16x16x32_bf16 v[68:71], v[188:191], v[120:123], v[68:71]\n\t"
    "ds_read_b128 v[116:119], %[ea] offset:22528\n\t"
    "v_mfma_f32_16x16x32_bf16 v[72:75], v[184:187], v[124:127], v[72:75]\n\t"
    "v_mfma_f32_16x16x32_bf16 v[76:79], v[188:191], v[124:127], v[76:79]\n\t"
    "s_waitcnt vmcnt(12)\n\t"
    "s_barrier\n\t"
    "s_waitcnt lgkmcnt(4)\n\t"
    "v_mfma_f32_16x16x32_bf16 v[64:67], v[192:195], v[96:99], v[64:67]\n\t"
    "ds_read_b128 v[120:123], %[da] offset:20480\n\t"
    "s_add_u32 m0, %[l0], 0\n\t"
    "v_mfma_f32_16x16x32_bf16 v[68:71], v[196:199], v[96:99], v[68:71]\n\t"
    "ds_read_b128 v[124:127], %[da] offset:22528\n\t"
    "global_load_lds_dwordx4 %[av0], s[38:39] sc1\n\t"
    "v_mfma_f32_16x16x32_bf16 v[72:75], v[192:195], v[100:103], v[72:75]\n\t"
    "s_add_u32 m0, %[l0], 32768\n\t"
    "v_mfma_f32_16x16x32_bf16 v[76:79], v[196:199], v[100:103], v[76:79]\n\t"
    "global_load_lds_dwordx4 %[av1], s[38:39] sc1\n\t"
    "s_waitcnt lgkmcnt(4)\n\t"
    "v_mfma_f32_16x16x32_bf16 v[64:67], v[200:203], v[104:107], v[64:67]\n\t"
    "ds_read_b128 v[96:99], %[ea] offset:24576\n\t"
    "s_add_u32 m0, %[l0], 4096\n\t"
    "v_mfma_f32_16x16x32_bf16 v[68:71], v[204:207], v[104:107], v[68:71]\n\t"
    "ds_read_b128 v[100:103], %[ea] offset:26624\n\t"
    "global_load_lds_dwordx4 %[av2], s[38:39] sc1\n\t"
    "v_mfma_f32_16x16x32_bf16 v[72:75], v[200:203], v[108:111], v[72:75]\n\t"
    "s_add_u32 m0, %[l0], 36864\n\t"
    "v_mfma_f32_16x16x32_bf16 v[76:79], v[204:207], v[108:111], v[76:79]\n\t"
    "global_load_lds_dwordx4 %[av3], s[38:39] sc1\n\t"
    "s_add_u32 s36, s36, %[ab]\n\t"
    "s_addc_u32 s37, s37, 0\n\t"
    "s_waitcnt lgkmcnt(4)\n\t"
    "v_mfma_f32_16x16x32_bf16 v[64:67], v[208:211], v[112:115], v[64:67]\n\t"
    "ds_read_b128 v[104:107], %[da] offset:24576\n\t"
    "s_add_u32 m0, %[l0], 8192\n\t"
    "v_mfma_f32_16x16x32_bf16 v[68:71], v[212:215], v[112:115], v[68:71]\n\t"
    "ds_read_b128 v[108:111], %[da] offset:26624\n\t"
    "global_load_lds_dwordx4 %[av4], s[38:39] sc1\n\t"
    "v_mfma_f32_16x16x32_bf16 v[72:75], v[208:211], v[116:119], v[72:75]\n\t"
    "ds_read_b32 v39, %[sa]\n\t"
    "s_add_u32 m0, %[l0], 40960\n\t"
    "v_mfma_f32_16x16x32_bf16 v[76:79], v[212:215], v[116:119], v[76:79]\n\t"
    "v_xor_b32 %[sa], 0x100, %[sa]\n\t"
    "global_load_lds_dwordx4 %[av5], s[38:39] sc1\n\t"
    "s_xor_b32 %[l1], %[l1], 0x10000\n\t"
    "s_waitcnt lgkmcnt(5)\n\t"
    "v_mfma_f32_16x16x32_bf16 v[64:67], v[216:219], v[120:123], v[64:67]\n\t"
    "ds_read_b128 v[112:115], %[ea] offset:28672\n\t"
    "s_add_u32 m0, %[l0], 12288\n\t"
    "v_mfma_f32_16x16x32_bf16 v[68:71], v[220:223], v[120:123], v[68:71]\n\t"
    "ds_read_b128 v[116:119], %[ea] offset:30720\n\t"
    "global_load_lds_dwordx4 %[av6], s[38:39] sc1\n\t"
    "v_mfma_f32_16x16x32_bf16 v[72:75], v[216:219], v[124:127], v[72:75]\n\t"
    "v_xor_b32 %[ea], 0x10000, %[ea]\n\t"
    "s_add_u32 m0, %[l0], 45056\n\t"
    "v_mfma_f32_16x16x32_bf16 v[76:79], v[220:223], v[124:127], v[76:79]\n\t"
    "global_load_lds_dwordx4 %[av7], s[38:39] sc1\n\t"
    "s_waitcnt lgkmcnt(5)\n\t"
    "v_mfma_f32_16x16x32_bf16 v[64:67], v[224:227], v[96:99], v[64:67]\n\t"
    "ds_read_b128 v[120:123], %[da] offset:28672\n\t"
    "v_mfma_f32_16x16x32_bf16 v[68:71], v[228:231], v[96:99], v[68:71]\n\t"
    "ds_read_b128 v[124:127], %[da] offset:30720\n\t"
    "v_mfma_f32_16x16x32_bf16 v[72:75], v[224:227], v[100:103], v[72:75]\n\t"
    "v_xor_b32 %[da], 0x10000, %[da]\n\t"
    "v_mfma_f32_16x16x32_bf16 v[76:79], v[228:231], v[100:103], v[76:79]\n\t"
    "s_waitcnt lgkmcnt(5)\n\t"
    "v_mfma_f32_16x16x32_bf16 v[64:67], v[232:235], v[104:107], v[64:67]\n\t"
    "ds_read_b128 v[96:99], %[ea] offset:0\n\t"
    "v_mfma_f32_16x16x32_bf16 v[68:71], v[236:239], v[104:107], v[68:71]\n\t"
    "ds_read_b128 v[100:103], %[ea] offset:2048\n\t"
    "v_mfma_f32_16x16x32_bf16 v[72:75], v[232:235], v[108:111], v[72:75]\n\t"
    "v_mfma_f32_16x16x32_bf16 v[76:79], v[236:239], v[108:111], v[76:79]\n\t"
    "s_waitcnt lgkmcnt(4)\n\t"
    "v_mfma_f32_16x16x32_bf16 v[64:67], v[240:243], v[112:115], v[64:67]\n\t"
    "ds_read_b128 v[104:107], %[da] offset:0\n\t"
    "v_mad_u32_u24 v39, v39, %[mu], %[ad]\n\t"
    "v_mfma_f32_16x16x32_bf16 v[68:71], v[244:247], v[112:115], v[68:71]\n\t"
    "ds_read_b128 v[108:111], %[da] offset:2048\n\t"
    "v_cmp_ge_i32 vcc, %[lf], v39\n\t"
    "s_cmp_eq_u64 vcc, 0\n\t"
    "s_addc_u32 %[lf], %[lf], 0\n\t"
    "v_mfma_f32_16x16x32_bf16 v[72:75], v[240:243], v[116:119], v[72:75]\n\t"
    "v_mfma_f32_16x16x32_bf16 v[76:79], v[244:247], v[116:119], v[76:79]\n\t"
    "v_cmp_ge_i32 vcc, %[lf], v39\n\t"
    "s_cmp_eq_u64 vcc, 0\n\t"
    "s_addc_u32 %[lf], %[lf], 0\n\t"
    "s_waitcnt lgkmcnt(4)\n\t"
    "v_mfma_f32_16x16x32_bf16 v[80:83], v[248:251], v[120:123], v[64:67]\n\t"
    "ds_read_b128 v[112:115], %[ea] offset:4096\n\t"
    "s_min_i32 %[lf], %[lf], %[cp]\n\t"
    "s_add_u32 %[b], %[b], 1\n\t"
    "v_mfma_f32_16x16x32_bf16 v[84:87], v[252:255], v[120:123], v[68:71]\n\t"
    "ds_read_b128 v[116:119], %[ea] offset:6144\n\t"
    "v_mfma_f32_16x16x32_bf16 v[88:91], v[248:251], v[124:127], v[72:75]\n\t"
    "v_mfma_f32_16x16x32_bf16 v[92:95], v[252:255], v[124:127], v[76:79]\n\t"
    "s_add_u32 s38, s38, %[ab]\n\t"
    "s_addc_u32 s39, s39, 0\n\t"
    "s_xor_b32 %[l0], %[l0], 0x10000\n\t"
    "s_cmp_lt_i32 %[b], %[lf]\n\t"
    "s_cbranch_scc1 1b\n\t"
    "s_waitcnt lgkmcnt(0)\n\t"
    "s_nop 4"
    : "=&{v[80:95]}"(pd), [ea] "+&v"(ea), [da] "+&v"(da), [b] "+&s"(b), [lf] "+&s"(lf), "+{s[36:37]}"(p1), "+{s[38:39]}"(p0), [sa] "+&v"(sa), [l1] "+&s"(l1), [l0] "+&s"(l0)
    : [rw] "v"(rw), [ab] "s"(ab), [cp] "s"(cp), [mu] "s"(mu), [ad] "v"(ad), "{v[128:159]}"(W[0]), "{v[160:191]}"(W[1]), "{v[192:223]}"(W[2]), "{v[224:255]}"(W[3]), [av0] "v"(av[0]), [av1] "v"(av[1]), [av2] "v"(av[2]), [av3] "v"(av[3]), [av4] "v"(av[4]), [av5] "v"(av[5]), [av6] "v"(av[6]), [av7] "v"(av[7])
    : "v39", "v64", "v65", "v66", "v67", "v68", "v69", "v70", "v71", "v72", "v73", "v74", "v75", "v76", "v77", "v78", "v79", "v96", "v97", "v98", "v99", "v100", "v101", "v102", "v103", "v104", "v105", "v106", "v107", "v108", "v109", "v110", "v111", "v112", "v113", "v114", "v115", "v116", "v117", "v118", "v119", "v120", "v121", "v122", "v123", "v124", "v125", "v126", "v127", "vcc", "scc", "m0", "memory");
}
__device__ __forceinline__ void fast_ld2(const u32x32 (&W)[4], u32x16& pd, unsigned ea, unsigned da, int& b, int& lf, int cp, unsigned sa, unsigned mu, unsigned ad,
    unsigned rw, unsigned ab, const char* p1, const char* p0, unsigned l1, unsigned l0, const unsigned (&av)[8]) {
  asm volatile("s_setprio 0\n\t"
    "s_waitcnt vmcnt(6)\n\t"
    "s_barrier\n\t"
    "ds_read_b128 v[96:99], %[ea] offset:0\n\t"
    "ds_read_b128 v[100:103], %[ea] offset:2048\n\t"
    "ds_read_b128 v[104:107], %[da] offset:0\n\t"
    "ds_read_b128 v[108:111], %[da] offset:2048\n\t"
    "ds_read_b128 v[112:115], %[ea] offset:4096\n\t"
    "ds_read_b128 v[116:119], %[ea] offset:6144\n\t"
    "s_branch 2f\n\t"
    "1:\n\t"
    "s_waitcnt vmcnt(6)\n\t"
    "s_nop 1\n\t"
    "s_barrier\n\t"
    "2:\n\t"
    "s_waitcnt lgkmcnt(4)\n\t"
    "v_mfma_f32_16x16x32_bf16 v[64:67], v[128:131], v[96:99], 0\n\t"
    "ds_read_b128 v[120:123], %[da] offset:4096\n\t"
    "s_add_u32 m0, %[l1], 0\n\t"
    "ds_write_b128 %[rw], v[80:83] offset:0\n\t"
    "v_mfma_f32_16x16x32_bf16 v[68:71], v[132:135], v[96:99], 0\n\t"
    "ds_read_b128 v[124:127], %[da] offset:6144\n\t"
    "global_load_lds_dwordx4 %[av0], s[36:37] sc1\n\t"
    "ds_write_b128 %[rw], v[84:87] offset:1024\n\t"
    "v_mfma_f32_16x16x32_bf16 v[72:75], v[128:131], v[100:103], 0\n\t"
    "ds_write_b128 %[rw], v[88:91] offset:2048\n\t"
    "v_mfma_f32_16x16x32_bf16 v[76:79], v[132:135], v[100:103], 0\n\t"
    "ds_write_b128 %[rw], v[92:95] offset:3072\n\t"
    "s_waitcnt lgkmcnt(8)\n\t"
    "v_mfma_f32_16x16x32_bf16 v[64:67], v[136:139], v[104:107], v[64:67]\n\t"
    "ds_read_b128 v[96:99], %[ea] offset:8192\n\t"
    "s_add_u32 m0, %[l1], 4096\n\t"
    "v_mfma_f32_16x16x32_bf16 v[68:71], v[140:143], v[104:107], v[68:71]\n\t"
    "ds_read_b128 v[100:103], %[ea] offset:10240\n\t"
    "global_load_lds_dwordx4 %[av2], s[36:37] sc1\n\t"
    "v_mfma_f32_16x16x32_bf16 v[72:75], v[136:139], v[108:111], v[72:75]\n\t"
    "v_mfma_f32_16x16x32_bf16 v[76:79], v[140:143], v[108:111], v[76:79]\n\t"
    "s_waitcnt lgkmcnt(8)\n\t"
    "v_mfma_f32_16x16x32_bf16 v[64:67], v[144:147], v[112:115], v[64:67]\n\t"
    "ds_read_b128 v[104:107], %[da] offset:8192\n\t"
    "s_add_u32 m0, %[l1], 8192\n\t"
    "v_mfma_f32_16x16x32_bf16 v[68:71], v[148:151], v[112:115], v[68:71]\n\t"
    "ds_read_b128 v[108:111], %[da] offset:10240\n\t"
    "global_load_lds_dwordx4 %[av4], s[36:37] sc1\n\t"
    "v_mfma_f32_16x16x32_bf16 v[72:75], v[144:147], v[116:119], v[72:75]\n\t"
    "v_mfma_f32_16x16x32_bf16 v[76:79], v[148:151], v[116:119], v[76:79]\n\t"
    "s_waitcnt lgkmcnt(7)\n\t"
    "v_mfma_f32_16x16x32_bf16 v[64:67], v[152:155], v[120:123], v[64:67]\n\t"
    "ds_read_b128 v[112:115], %[ea] offset:12288\n\t"
    "s_add_u32 m0, %[l1], 12288\n\t"
    "v_mfma_f32_16x16x32_bf16 v[68:71], v[156:159], v[120:123], v[68:71]\n\t"
    "ds_read_b128 v[116:119], %[ea] offset:14336\n\t"
    "global_load_lds_dwordx4 %[av6], s[36:37] sc1\n\t"
    "v_mfma_f32_16x16x32_bf16 v[72:75], v[152:155], v[124:127], v[72:75]\n\t"
    "v_mfma_f32_16x16x32_bf16 v[76:79], v[156:159], v[124:127], v[76:79]\n\t"
    "s_waitcnt lgkmcnt(4)\n\t"
    "v_mfma_f32_16x16x32_bf16 v[64:67], v[160:163], v[96:99], v[64:67]\n\t"
    "ds_read_b128 v[120:123], %[da] offset:12288\n\t"
    "v_mfma_f32_16x16x32_bf16 v[68:71], v[164:167], v[96:99], v[68:71]\n\t"
    "ds_read_b128 v[124:127], %[da] offset:14336\n\t"
    "v_mfma_f32_16x16x32_bf16 v[72:75], v[160:163], v[100:103], v[72:75]\n\t"
    "v_mfma_f32_16x16x32_bf16 v[76:79], v[164:167], v[100:103], v[76:79]\n\t"
    "s_waitcnt lgkmcnt(4)\n\t"
    "v_mfma_f32_16x16x32_bf16 v[64:67], v[168:171], v[104:107], v[64:67]\n\t"
    "ds_read_b128 v[96:99], %[ea] offset:16384\n\t"
    "v_mfma_f32_16x16x32_bf16 v[68:71], v[172:175], v[104:107], v[68:71]\n\t"
    "ds_read_b128 v[100:103], %[ea] offset:18432\n\t"
    "v_mfma_f32_16x16x32_bf16 v[72:75], v[168:171], v[108:111], v[72:75]\n\t"
    "v_mfma_f32_16x16x32_bf16 v[76:79], v[172:175], v[108:111], v[76:79]\n\t"
    "s_waitcnt lgkmcnt(4)\n\t"
    "v_mfma_f32_16x16x32_bf16 v[64:67], v[176:179], v[112:115], v[64:67]\n\t"
    "ds_read_b128 v[104:107], %[da] offset:16384\n\t"
    "v_mfma_f32_16x16x32_bf16 v[68:71], v[180:183], v[112:115], v[68:71]\n\t"
    "ds_read_b128 v[108:111], %[da] offset:18432\n\t"
    "v_mfma_f32_16x16x32_bf16 v[72:75], v[176:179], v[116:119], v[72:75]\n\t"
    "v_mfma_f32_16x16x32_bf16 v[76:79], v[180:183], v[116:119], v[76:79]\n\t"
    "s_waitcnt lgkmcnt(4)\n\t"
    "v_mfma_f32_16x16x32_bf16 v[64:67], v[184:187], v[120:123], v[64:67]\n\t"
    "ds_read_b128 v[112:115], %[ea] offset:20480\n\t"
    "v_mfma_f32_16x16x32_bf16 v[68:71], v[188:191], v[120:123], v[68:71]\n\t"
    "ds_read_b128 v[116:119], %[ea] offset:22528\n\t"
    "v_mfma_f32_16x16x32_bf16 v[72:75], v[184:187], v[124:127], v[72:75]\n\t"
    "v_mfma_f32_16x16x32_bf16 v[76:79], v[188:191], v[124:127], v[76:79]\n\t"
    "s_waitcnt vmcnt(6)\n\t"
    "s_barrier\n\t"
    "s_waitcnt lgkmcnt(4)\n\t"
    "v_mfma_f32_16x16x32_bf16 v[64:67], v[192:195], v[96:99], v[64:67]\n\t"
    "ds_read_b128 v[120:123], %[da] offset:20480\n\t"
    "s_add_u32 m0, %[l0], 0\n\t"
    "v_mfma_f32_16x16x32_bf16 v[68:71], v[196:199], v[96:99], v[68:71]\n\t"
    "ds_read_b128 v[124:127], %[da] offset:22528\n\t"
    "global_load_lds_dwordx4 %[av0], s[38:39] sc1\n\t"
    "v_mfma_f32_16x16x32_bf16 v[72:75], v[192:195], v[100:103], v[72:75]\n\t"
    "v_mfma_f32_16x16x32_bf16 v[76:79], v[196:199], v[100:103], v[76:79]\n\t"
    "s_waitcnt lgkmcnt(4)\n\t"
    "v_mfma_f32_16x16x32_bf16 v[64:67], v[200:203], v[104:107], v[64:67]\n\t"
    "ds_read_b128 v[96:99], %[ea] offset:24576\n\t"
    "s_add_u32 m0, %[l0], 4096\n\t"
    "v_mfma_f32_16x16x32_bf16 v[68:71], v[204:207], v[104:107], v[68:71]\n\t"
    "ds_read_b128 v[100:103], %[ea] offset:26624\n\t"
    "global_load_lds_dwordx4 %[av2], s[38:39] sc1\n\t"
    "v_mfma_f32_16x16x32_bf16 v[72:75], v[200:203], v[108:111], v[72:75]\n\t"
    "v_mfma_f32_16x16x32_bf16 v[76:79], v[204:207], v[108:111], v[76:79]\n\t"
    "s_add_u32 s36, s36, %[ab]\n\t"
    "s_addc_u32 s37, s37, 0\n\t"
    "s_waitcnt lgkmcnt(4)\n\t"
    "v_mfma_f32_16x16x32_bf16 v[64:67], v[208:211], v[112:115], v[64:67]\n\t"
    "ds_read_b128 v[104:107], %[da] offset:24576\n\t"
    "s_add_u32 m0, %[l0], 8192\n\t"
    "v_mfma_f32_16x16x32_bf16 v[68:71], v[212:215], v[112:115], v[68:71]\n\t"
    "ds_read_b128 v[108:111], %[da] offset:26624\n\t"
    "global_load_lds_dwordx4 %[av4], s[38:39] sc1\n\t"
    "v_mfma_f32_16x16x32_bf16 v[72:75], v[208:211], v[116:119], v[72:75]\n\t"
    "ds_read_b32 v39, %[sa]\n\t"
    "v_mfma_f32_16x16x32_bf16 v[76:79], v[212:215], v[116:119], v[76:79]\n\t"
    "v_xor_b32 %[sa], 0x100, %[sa]\n\t"
    "s_xor_b32 %[l1], %[l1], 0x10000\n\t"
    "s_waitcnt lgkmcnt(5)\n\t"
    "v_mfma_f32_16x16x32_bf16 v[64:67], v[216:219], v[120:123], v[64:67]\n\t"
    "ds_read_b128 v[112:115], %[ea] offset:28672\n\t"
    "s_add_u32 m0, %[l0], 12288\n\t"
    "v_mfma_f32_16x16x32_bf16 v[68:71], v[220:223], v[120:123], v[68:71]\n\t"
    "ds_read_b128 v[116:119], %[ea] offset:30720\n\t"
    "global_load_lds_dwordx4 %[av6], s[38:39] sc1\n\t"
    "v_mfma_f32_16x16x32_bf16 v[72:75], v[216:219], v[124:127], v[72:75]\n\t"
    "v_xor_b32 %[ea], 0x10000, %[ea]\n\t"
    "v_mfma_f32_16x16x32_bf16 v[76:79], v[220:223], v[124:127], v[76:79]\n\t"
    "s_waitcnt lgkmcnt(5)\n\t"
    "v_mfma_f32_16x16x32_bf16 v[64:67], v[224:227], v[96:99], v[64:67]\n\t"
    "ds_read_b128 v[120:123], %[da] offset:28672\n\t"
    "v_mfma_f32_16x16x32_bf16 v[68:71], v[228:231], v[96:99], v[68:71]\n\t"
    "ds_read_b128 v[124:127], %[da] offset:30720\n\t"
    "v_mfma_f32_16x16x32_bf16 v[72:75], v[224:227], v[100:103], v[72:75]\n\t"
    "v_xor_b32 %[da], 0x10000, %[da]\n\t"
    "v_mfma_f32_16x16x32_bf16 v[76:79], v[228:231], v[100:103], v[76:79]\n\t"
    "s_waitcnt lgkmcnt(5)\n\t"
    "v_mfma_f32_16x16x32_bf16 v[64:67], v[232:235], v[104:107], v[64:67]\n\t"
    "ds_read_b128 v[96:99], %[ea] offset:0\n\t"
    "v_mfma_f32_16x16x32_bf16 v[68:71], v[236:239], v[104:107], v[68:71]\n\t"
    "ds_read_b128 v[100:103], %[ea] offset:2048\n\t"
    "v_mfma_f32_16x16x32_bf16 v[72:75], v[232:235], v[108:111], v[72:75]\n\t"
    "v_mfma_f32_16x16x32_bf16 v[76:79], v[236:239], v[108:111], v[76:79]\n\t"
    "s_waitcnt lgkmcnt(4)\n\t"
    "v_mfma_f32_16x16x32_bf16 v[64:67], v[240:243], v[112:115], v[64:67]\n\t"
    "ds_read_b128 v[104:107], %[da] offset:0\n\t"
    "v_mad_u32_u24 v39, v39, %[mu], %[ad]\n\t"
    "v_mfma_f32_16x16x32_bf16 v[68:71], v[244:247], v[112:115], v[68:71]\n\t"
    "ds_read_b128 v[108:111], %[da] offset:2048\n\t"
    "v_cmp_ge_i32 vcc, %[lf], v39\n\t"
    "s_cmp_eq_u64 vcc, 0\n\t"
    "s_addc_u32 %[lf], %[lf], 0\n\t"
    "v_mfma_f32_16x16x32_bf16 v[72:75], v[240:243], v[116:119], v[72:75]\n\t"
    "v_mfma_f32_16x16x32_bf16 v[76:79], v[244:247], v[116:119], v[76:79]\n\t"
    "v_cmp_ge_i32 vcc, %[lf], v39\n\t"
    "s_cmp_eq_u64 vcc, 0\n\t"
    "s_addc_u32 %[lf], %[lf], 0\n\t"
    "s_waitcnt lgkmcnt(4)\n\t"
    "v_mfma_f32_16x16x32_bf16 v[80:83], v[248:251], v[120:123], v[64:67]\n\t"
    "ds_read_b128 v[112:115], %[ea] offset:4096\n\t"
    "s_min_i32 %[lf], %[lf], %[cp]\n\t"
    "s_add_u32 %[b], %[b], 1\n\t"
    "v_mfma_f32_16x16x32_bf16 v[84:87], v[252:255], v[120:123], v[68:71]\n\t"
    "ds_read_b128 v[116:119], %[ea] offset:6144\n\t"
    "v_mfma_f32_16x16x32_bf16 v[88:91], v[248:251], v[124:127], v[72:75]\n\t"
    "v_mfma_f32_16x16x32_bf16 v[92:95], v[252:255], v[124:127], v[76:79]\n\t"
    "s_add_u32 s38, s38, %[ab]\n\t"
    "s_addc_u32 s39, s39, 0\n\t"
    "s_xor_b32 %[l0], %[l0], 0x10000\n\t"
    "s_cmp_lt_i32 %[b], %[lf]\n\t"
    "s_cbranch_scc1 1b\n\t"
    "s_waitcnt lgkmcnt(0)\n\t"
    "s_nop 4"
    : "=&{v[80:95]}"(pd), [ea] "+&v"(ea), [da] "+&v"(da), [b] "+&s"(b), [lf] "+&s"(lf), "+{s[36:37]}"(p1), "+{s[38:39]}"(p0), [sa] "+&v"(sa), [l1] "+&s"(l1), [l0] "+&s"(l0)
    : [rw] "v"(rw), [ab] "s"(ab), [cp] "s"(cp), [mu] "s"(mu), [ad] "v"(ad), "{v[128:159]}"(W[0]), "{v[160:191]}"(W[1]), "{v[192:223]}"(W[2]), "{v[224:255]}"(W[3]), [av0] "v"(av[0]), [av1] "v"(av[1]), [av2] "v"(av[2]), [av3] "v"(av[3]), [av4] "v"(av[4]), [av5] "v"(av[5]), [av6] "v"(av[6]), [av7] "v"(av[7])
    : "v39", "v64", "v65", "v66", "v67", "v68", "v69", "v70", "v71", "v72", "v73", "v74", "v75", "v76", "v77", "v78", "v79", "v96", "v97", "v98", "v99", "v100", "v101", "v102", "v103", "v104", "v105", "v106", "v107", "v108", "v109", "v110", "v111", "v112", "v113", "v114", "v115", "v116", "v117", "v118", "v119", "v120", "v121", "v122", "v123", "v124", "v125", "v126", "v127", "vcc", "scc", "m0", "memory");
}
__device__ __forceinline__ void fast_st0(const u32x32 (&W)[4], u32x16& pd, unsigned ea, unsigned da, int& b, int& lf, int cp, unsigned sa, unsigned mu, unsigned ad,
    unsigned rw, unsigned ab, const char* p1, const char* p0, unsigned co0, unsigned co1, int po, int pf, unsigned w0, unsigned sl, unsigned dv, const char* db, const char* pg) {
  unsigned st;
  asm volatile("s_barrier\n\t"
    "ds_read_b128 v[96:99], %[ea] offset:0\n\t"
    "ds_read_b128 v[100:103], %[ea] offset:2048\n\t"
    "ds_read_b128 v[104:107], %[da] offset:0\n\t"
    "ds_read_b128 v[108:111], %[da] offset:2048\n\t"
    "ds_read_b128 v[112:115], %[ea] offset:4096\n\t"
    "ds_read_b128 v[116:119], %[ea] offset:6144\n\t"
    "s_branch 2f\n\t"
    "1:\n\t"
    "s_barrier\n\t"
    "2:\n\t"
    "s_waitcnt lgkmcnt(4)\n\t"
    "v_mfma_f32_16x16x32_bf16 v[64:67], v[128:131], v[96:99], 0\n\t"
    "ds_read_b128 v[120:123], %[da] offset:4096\n\t"
    "v_mfma_f32_16x16x32_bf16 v[68:71], v[132:135], v[96:99], 0\n\t"
    "ds_read_b128 v[124:127], %[da] offset:6144\n\t"
    "v_mfma_f32_16x16x32_bf16 v[72:75], v[128:131], v[100:103], 0\n\t"
    "v_mfma_f32_16x16x32_bf16 v[76:79], v[132:135], v[100:103], 0\n\t"
    "s_waitcnt lgkmcnt(4)\n\t"
    "v_mfma_f32_16x16x32_bf16 v[64:67], v[136:139], v[104:107], v[64:67]\n\t"
    "ds_read_b128 v[96:99], %[ea] offset:8192\n\t"
    "v_mfma_f32_16x16x32_bf16 v[68:71], v[140:143], v[104:107], v[68:71]\n\t"
    "ds_read_b128 v[100:103], %[ea] offset:10240\n\t"
    "v_mfma_f32_16x16x32_bf16 v[72:75], v[136:139], v[108:111], v[72:75]\n\t"
    "v_mfma_f32_16x16x32_bf16 v[76:79], v[140:143], v[108:111], v[76:79]\n\t"
    "s_waitcnt lgkmcnt(4)\n\t"
    "v_mfma_f32_16x16x32_bf16 v[64:67], v[144:147], v[112:115], v[64:67]\n\t"
    "ds_read_b128 v[104:107], %[da] offset:8192\n\t"
    "v_mfma_f32_16x16x32_bf16 v[68:71], v[148:151], v[112:115], v[68:71]\n\t"
    "ds_read_b128 v[108:111], %[da] offset:10240\n\t"
    "v_mfma_f32_16x16x32_bf16 v[72:75], v[144:147], v[116:119], v[72:75]\n\t"
    "v_mfma_f32_16x16x32_bf16 v[76:79], v[148:151], v[116:119], v[76:79]\n\t"
    "s_waitcnt lgkmcnt(4)\n\t"
    "v_mfma_f32_16x16x32_bf16 v[64:67], v[152:155], v[120:123], v[64:67]\n\t"
    "ds_read_b128 v[112:115], %[ea] offset:12288\n\t"
    "v_mfma_f32_16x16x32_bf16 v[68:71], v[156:159], v[120:123], v[68:71]\n\t"
    "ds_read_b128 v[116:119], %[ea] offset:14336\n\t"
    "v_mfma_f32_16x16x32_bf16 v[72:75], v[152:155], v[124:127], v[72:75]\n\t"
    "v_mfma_f32_16x16x32_bf16 v[76:79], v[156:159], v[124:127], v[76:79]\n\t"
    "s_waitcnt lgkmcnt(4)\n\t"
    "v_mfma_f32_16x16x32_bf16 v[64:67], v[160:163], v[96:99], v[64:67]\n\t"
    "ds_read_b128 v[120:123], %[da] offset:12288\n\t"
    "v_mfma_f32_16x16x32_bf16 v[68:71], v[164:167], v[96:99], v[68:71]\n\t"
    "ds_read_b128 v[124:127], %[da] offset:14336\n\t"
    "v_mfma_f32_16x16x32_bf16 v[72:75], v[160:163], v[100:103], v[72:75]\n\t"
    "v_mfma_f32_16x16x32_bf16 v[76:79], v[164:167], v[100:103], v[76:79]\n\t"
    "s_waitcnt lgkmcnt(4)\n\t"
    "v_mfma_f32_16x16x32_bf16 v[64:67], v[168:171], v[104:107], v[64:67]\n\t"
    "ds_read_b128 v[96:99], %[ea] offset:16384\n\t"
    "v_mfma_f32_16x16x32_bf16 v[68:71], v[172:175], v[104:107], v[68:71]\n\t"
    "ds_read_b128 v[100:103], %[ea] offset:18432\n\t"
    "v_mfma_f32_16x16x32_bf16 v[72:75], v[168:171], v[108:111], v[72:75]\n\t"
    "v_mfma_f32_16x16x32_bf16 v[76:79], v[172:175], v[108:111], v[76:79]\n\t"
    "s_waitcnt lgkmcnt(4)\n\t"
    "v_mfma_f32_16x16x32_bf16 v[64:67], v[176:179], v[112:115], v[64:67]\n\t"
    "ds_read_b128 v[104:107], %[da] offset:16384\n\t"
    "v_mfma_f32_16x16x32_bf16 v[68:71], v[180:183], v[112:115], v[68:71]\n\t"
    "ds_read_b128 v[108:111], %[da] offset:18432\n\t"
    "v_mfma_f32_16x16x32_bf16 v[72:75], v[176:179], v[116:119], v[72:75]\n\t"
    "v_mfma_f32_16x16x32_bf16 v[76:79], v[180:183], v[116:119], v[76:79]\n\t"
    "s_waitcnt lgkmcnt(4)\n\t"
    "v_mfma_f32_16x16x32_bf16 v[64:67], v[184:187], v[120:123], v[64:67]\n\t"
    "ds_read_b128 v[112:115], %[ea] offset:20480\n\t"
    "v_mfma_f32_16x16x32_bf16 v[68:71], v[188:191], v[120:123], v[68:71]\n\t"
    "ds_read_b128 v[116:119], %[ea] offset:22528\n\t"
    "v_mfma_f32_16x16x32_bf16 v[72:75], v[184:187], v[124:127], v[72:75]\n\t"
    "v_mfma_f32_16x16x32_bf16 v[76:79], v[188:191], v[124:127], v[76:79]\n\t"
    "s_waitcnt vmcnt(0)\n\t"
    "s_barrier\n\t"
    "s_waitcnt lgkmcnt(4)\n\t"
    "v_mfma_f32_16x16x32_bf16 v[64:67], v[192:195], v[96:99], v[64:67]\n\t"
    "ds_read_b128 v[120:123], %[da] offset:20480\n\t"
    "v_mfma_f32_16x16x32_bf16 v[68:71], v[196:199], v[96:99], v[68:71]\n\t"
    "ds_read_b128 v[124:127], %[da] offset:22528\n\t"
    "v_mfma_f32_16x16x32_bf16 v[72:75], v[192:195], v[100:103], v[72:75]\n\t"
    "ds_read_b128 v[48:51], %[rw] offset:0\n\t"
    "v_mfma_f32_16x16x32_bf16 v[76:79], v[196:199], v[100:103], v[76:79]\n\t"
    "ds_read_b128 v[52:55], %[rw] offset:1024\n\t"
    "s_cmp_eq_u32 %[w0], 0\n\t"
    "s_cbranch_scc1 5f\n\t"
    "6:\n\t"
    "s_waitcnt lgkmcnt(6)\n\t"
    "v_mfma_f32_16x16x32_bf16 v[64:67], v[200:203], v[104:107], v[64:67]\n\t"
    "ds_read_b128 v[96:99], %[ea] offset:24576\n\t"
    "v_mfma_f32_16x16x32_bf16 v[68:71], v[204:207], v[104:107], v[68:71]\n\t"
    "ds_read_b128 v[100:103], %[ea] offset:26624\n\t"
    "v_mfma_f32_16x16x32_bf16 v[72:75], v[200:203], v[108:111], v[72:75]\n\t"
    "ds_read_b128 v[56:59], %[rw] offset:2048\n\t"
    "v_mfma_f32_16x16x32_bf16 v[76:79], v[204:207], v[108:111], v[76:79]\n\t"
    "ds_read_b128 v[60:63], %[rw] offset:3072\n\t"
    "s_waitcnt lgkmcnt(0)\n\t"
    "v_mfma_f32_16x16x32_bf16 v[64:67], v[208:211], v[112:115], v[64:67]\n\t"
    "ds_read_b128 v[104:107], %[da] offset:24576\n\t"
    "v_add_f32 v48, v48, v80\n\t"
    "v_add_f32 v49, v49, v81\n\t"
    "v_mfma_f32_16x16x32_bf16 v[68:71], v[212:215], v[112:115], v[68:71]\n\t"
    "ds_read_b128 v[108:111], %[da] offset:26624\n\t"
    "v_add_f32 v50, v50, v82\n\t"
    "v_mfma_f32_16x16x32_bf16 v[72:75], v[208:211], v[116:119], v[72:75]\n\t"
    "ds_read_b32 v39, %[sa]\n\t"
    "v_add_f32 v51, v51, v83\n\t"
    "v_mfma_f32_16x16x32_bf16 v[76:79], v[212:215], v[116:119], v[76:79]\n\t"
    "v_xor_b32 %[sa], 0x100, %[sa]\n\t"
    "v_add_f32 v52, v52, v84\n\t"
    "v_add_f32 v53, v53, v85\n\t"
    "v_mfma_f32_16x16x32_bf16 v[64:67], v[216:219], v[120:123], v[64:67]\n\t"
    "ds_read_b128 v[112:115], %[ea] offset:28672\n\t"
    "v_add_f32 v54, v54, v86\n\t"
    "v_mfma_f32_16x16x32_bf16 v[68:71], v[220:223], v[120:123], v[68:71]\n\t"
    "ds_read_b128 v[116:119], %[ea] offset:30720\n\t"
    "v_add_f32 v55, v55, v87\n\t"
    "v_mfma_f32_16x16x32_bf16 v[72:75], v[216:219], v[124:127], v[72:75]\n\t"
    "v_xor_b32 %[ea], 0x10000, %[ea]\n\t"
    "v_add_f32 v56, v56, v88\n\t"
    "v_add_f32 v57, v57, v89\n\t"
    "v_mfma_f32_16x16x32_bf16 v[76:79], v[220:223], v[124:127], v[76:79]\n\t"
    "v_add_f32 v58, v58, v90\n\t"
    "v_mfma_f32_16x16x32_bf16 v[64:67], v[224:227], v[96:99], v[64:67]\n\t"
    "ds_read_b128 v[120:123], %[da] offset:28672\n\t"
    "v_add_f32 v59, v59, v91\n\t"
    "v_mfma_f32_16x16x32_bf16 v[68:71], v[228:231], v[96:99], v[68:71]\n\t"
    "ds_read_b128 v[124:127], %[da] offset:30720\n\t"
    "v_add_f32 v60, v60, v92\n\t"
    "v_mfma_f32_16x16x32_bf16 v[72:75], v[224:227], v[100:103], v[72:75]\n\t"
    "v_xor_b32 %[da], 0x10000, %[da]\n\t"
    "v_add_f32 v61, v61, v93\n\t"
    "v_add_f32 v62, v62, v94\n\t"
    "v_mfma_f32_16x16x32_bf16 v[76:79], v[228:231], v[100:103], v[76:79]\n\t"
    "v_add_f32 v63, v63, v95\n\t"
    "s_waitcnt lgkmcnt(5)\n\t"
    "v_mfma_f32_16x16x32_bf16 v[64:67], v[232:235], v[104:107], v[64:67]\n\t"
    "ds_read_b128 v[96:99], %[ea] offset:0\n\t"
    "v_cvt_pk_bf16_f32 v40, v48, v49\n\t"
    "v_mfma_f32_16x16x32_bf16 v[68:71], v[236:239], v[104:107], v[68:71]\n\t"
    "ds_read_b128 v[100:103], %[ea] offset:2048\n\t"
    "v_cvt_pk_bf16_f32 v41, v50, v51\n\t"
    "v_cvt_pk_bf16_f32 v42, v52, v53\n\t"
    "v_mfma_f32_16x16x32_bf16 v[72:75], v[232:235], v[108:111], v[72:75]\n\t"
    "v_cvt_pk_bf16_f32 v43, v54, v55\n\t"
    "v_mfma_f32_16x16x32_bf16 v[76:79], v[236:239], v[108:111], v[76:79]\n\t"
    "global_store_dwordx4 %[co0], v[40:43], s[36:37] sc1\n\t"
    "s_waitcnt lgkmcnt(4)\n\t"
    "v_mfma_f32_16x16x32_bf16 v[64:67], v[240:243], v[112:115], v[64:67]\n\t"
    "ds_read_b128 v[104:107], %[da] offset:0\n\t"
    "v_mad_u32_u24 v39, v39, %[mu], %[ad]\n\t"
    "v_cvt_pk_bf16_f32 v44, v56, v57\n\t"
    "v_cvt_pk_bf16_f32 v45, v58, v59\n\t"
    "v_mfma_f32_16x16x32_bf16 v[68:71], v[244:247], v[112:115], v[68:71]\n\t"
    "ds_read_b128 v[108:111], %[da] offset:2048\n\t"
    "v_cmp_ge_i32 vcc, %[lf], v39\n\t"
    "s_cmp_eq_u64 vcc, 0\n\t"
    "s_addc_u32 %[lf], %[lf], 0\n\t"
    "v_cvt_pk_bf16_f32 v46, v60, v61\n\t"
    "v_mfma_f32_16x16x32_bf16 v[72:75], v[240:243], v[116:119], v[72:75]\n\t"
    "v_cvt_pk_bf16_f32 v47, v62, v63\n\t"
    "v_mfma_f32_16x16x32_bf16 v[76:79], v[244:247], v[116:119], v[76:79]\n\t"
    "v_cmp_ge_i32 vcc, %[lf], v39\n\t"
    "s_cmp_eq_u64 vcc, 0\n\t"
    "s_addc_u32 %[lf], %[lf], 0\n\t"
    "global_store_dwordx4 %[co1], v[44:47], s[36:37] sc1\n\t"
    "s_waitcnt lgkmcnt(4)\n\t"
    "v_mfma_f32_16x16x32_bf16 v[80:83], v[248:251], v[120:123], v[64:67]\n\t"
    "ds_read_b128 v[112:115], %[ea] offset:4096\n\t"
    "s_min_i32 %[lf], %[lf], %[cp]\n\t"
    "s_add_u32 %[b], %[b], 1\n\t"
    "v_mfma_f32_16x16x32_bf16 v[84:87], v[252:255], v[120:123], v[68:71]\n\t"
    "ds_read_b128 v[116:119], %[ea] offset:6144\n\t"
    "v_mfma_f32_16x16x32_bf16 v[88:91], v[248:251], v[124:127], v[72:75]\n\t"
    "v_mfma_f32_16x16x32_bf16 v[92:95], v[252:255], v[124:127], v[76:79]\n\t"
    "s_mov_b64 s[36:37], s[38:39]\n\t"
    "s_add_u32 s38, s38, %[ab]\n\t"
    "s_addc_u32 s39, s39, 0\n\t"
    "s_cmp_lt_i32 %[b], %[lf]\n\t"
    "s_cbranch_scc1 1b\n\t"
    "s_waitcnt lgkmcnt(0)\n\t"
    "s_nop 4\n\t"
    "s_branch 9f\n\t"
    "5:\n\t"
    "s_add_u32 %[st], %[b], %[po]\n\t"
    "s_max_i32 %[st], %[st], %[p0]\n\t"
    "v_mov_b32 v39, %[st]\n\t"
    "v_mov_b32 v38, 0\n\t"
    "s_mov_b64 exec, 1\n\t"
    "global_store_dword v38, v39, %[pg] sc1\n\t"
    "s_mov_b64 exec, -1\n\t"
    "s_mov_b32 %[p0], %[p1]\n\t"
    "s_xor_b32 %[sl], %[sl], 0x100\n\t"
    "s_cmp_ge_i32 %[lf], %[cp]\n\t"
    "s_cbranch_scc1 6b\n\t"
    "s_mov_b32 m0, %[sl]\n\t"
    "s_nop 0\n\t"
    "global_load_lds_dword %[dv], %[db] sc1\n\t"
    "s_branch 6b\n\t"
    "9:"
    : "=&{v[80:95]}"(pd), [ea] "+&v"(ea), [da] "+&v"(da), [b] "+&s"(b), [lf] "+&s"(lf), "+{s[36:37]}"(p1), "+{s[38:39]}"(p0), [sa] "+&v"(sa), [st] "=&s"(st), [sl] "+&s"(sl), [p0] "+&s"(pf)
    : [rw] "v"(rw), [ab] "s"(ab), [cp] "s"(cp), [mu] "s"(mu), [ad] "v"(ad), "{v[128:159]}"(W[0]), "{v[160:191]}"(W[1]), "{v[192:223]}"(W[2]), "{v[224:255]}"(W[3]), [co0] "v"(co0), [w0] "s"(w0), [dv] "v"(dv), [db] "s"(db), [pg] "s"(pg), [po] "s"(po - 1), [p1] "s"(b + po), [co1] "v"(co1)
    : "v38", "v39", "v40", "v41", "v42", "v43", "v44", "v45", "v46", "v47", "v48", "v49", "v50", "v51", "v52", "v53", "v54", "v55", "v56", "v57", "v58", "v59", "v60", "v61", "v62", "v63", "v64", "v65", "v66", "v67", "v68", "v69", "v70", "v71", "v72", "v73", "v74", "v75", "v76", "v77", "v78", "v79", "v96", "v97", "v98", "v99", "v100", "v101", "v102", "v103", "v104", "v105", "v106", "v107", "v108", "v109", "v110", "v111", "v112", "v113", "v114", "v115", "v116", "v117", "v118", "v119", "v120", "v121", "v122", "v123", "v124", "v125", "v126", "v127", "vcc", "scc", "m0", "memory");
}
__device__ __forceinline__ void fast_st1(const u32x32 (&W)[4], u32x16& pd, unsigned ea, unsigned da, int& b, int& lf, int cp, unsigned sa, unsigned mu, unsigned ad,
    unsigned rw, unsigned ab, const char* p1, const char* p0, unsigned co0, unsigned co1, int po, int pf, unsigned w0, unsigned sl, unsigned dv, const char* db, const char* pg) {
  unsigned st;
  asm volatile("s_barrier\n\t"
    "ds_read_b128 v[96:99], %[ea] offset:0\n\t"
    "ds_read_b128 v[100:103], %[ea] offset:2048\n\t"
    "ds_read_b128 v[104:107], %[da] offset:0\n\t"
    "ds_read_b128 v[108:111], %[da] offset:2048\n\t"
    "ds_read_b128 v[112:115], %[ea] offset:4096\n\t"
    "ds_read_b128 v[116:119], %[ea] offset:6144\n\t"
    "s_branch 2f\n\t"
    "1:\n\t"
    "s_barrier\n\t"
    "2:\n\t"
    "s_waitcnt lgkmcnt(4)\n\t"
    "v_mfma_f32_16x16x32_bf16 v[64:67], v[128:131], v[96:99], 0\n\t"
    "ds_read_b128 v[120:123], %[da] offset:4096\n\t"
    "v_mfma_f32_16x16x32_bf16 v[68:71], v[132:135], v[96:99], 0\n\t"
    "ds_read_b128 v[124:127], %[da] offset:6144\n\t"
    "v_mfma_f32_16x16x32_bf16 v[72:75], v[128:131], v[100:103], 0\n\t"
    "v_mfma_f32_16x16x32_bf16 v[76:79], v[132:135], v[100:103], 0\n\t"
    "s_waitcnt lgkmcnt(4)\n\t"
    "v_mfma_f32_16x16x32_bf16 v[64:67], v[136:139], v[104:107], v[64:67]\n\t"
    "ds_read_b128 v[96:99], %[ea] offset:8192\n\t"
    "v_mfma_f32_16x16x32_bf16 v[68:71], v[140:143], v[104:107], v[68:71]\n\t"
    "ds_read_b128 v[100:103], %[ea] offset:10240\n\t"
    "v_mfma_f32_16x16x32_bf16 v[72:75], v[136:139], v[108:111], v[72:75]\n\t"
    "v_mfma_f32_16x16x32_bf16 v[76:79], v[140:143], v[108:111], v[76:79]\n\t"
    "s_waitcnt lgkmcnt(4)\n\t"
    "v_mfma_f32_16x16x32_bf16 v[64:67], v[144:147], v[112:115], v[64:67]\n\t"
    "ds_read_b128 v[104:107], %[da] offset:8192\n\t"
    "v_mfma_f32_16x16x32_bf16 v[68:71], v[148:151], v[112:115], v[68:71]\n\t"
    "ds_read_b128 v[108:111], %[da] offset:10240\n\t"
    "v_mfma_f32_16x16x32_bf16 v[72:75], v[144:147], v[116:119], v[72:75]\n\t"
    "v_mfma_f32_16x16x32_bf16 v[76:79], v[148:151], v[116:119], v[76:79]\n\t"
    "s_waitcnt lgkmcnt(4)\n\t"
    "v_mfma_f32_16x16x32_bf16 v[64:67], v[152:155], v[120:123], v[64:67]\n\t"
    "ds_read_b128 v[112:115], %[ea] offset:12288\n\t"
    "v_mfma_f32_16x16x32_bf16 v[68:71], v[156:159], v[120:123], v[68:71]\n\t"
    "ds_read_b128 v[116:119], %[ea] offset:14336\n\t"
    "v_mfma_f32_16x16x32_bf16 v[72:75], v[152:155], v[124:127], v[72:75]\n\t"
    "v_mfma_f32_16x16x32_bf16 v[76:79], v[156:159], v[124:127], v[76:79]\n\t"
    "s_waitcnt lgkmcnt(4)\n\t"
    "v_mfma_f32_16x16x32_bf16 v[64:67], v[160:163], v[96:99], v[64:67]\n\t"
    "ds_read_b128 v[120:123], %[da] offset:12288\n\t"
    "v_mfma_f32_16x16x32_bf16 v[68:71], v[164:167], v[96:99], v[68:71]\n\t"
    "ds_read_b128 v[124:127], %[da] offset:14336\n\t"
    "v_mfma_f32_16x16x32_bf16 v[72:75], v[160:163], v[100:103], v[72:75]\n\t"
    "v_mfma_f32_16x16x32_bf16 v[76:79], v[164:167], v[100:103], v[76:79]\n\t"
    "s_waitcnt lgkmcnt(4)\n\t"
    "v_mfma_f32_16x16x32_bf16 v[64:67], v[168:171], v[104:107], v[64:67]\n\t"
    "ds_read_b128 v[96:99], %[ea] offset:16384\n\t"
    "v_mfma_f32_16x16x32_bf16 v[68:71], v[172:175], v[104:107], v[68:71]\n\t"
    "ds_read_b128 v[100:103], %[ea] offset:18432\n\t"
    "v_mfma_f32_16x16x32_bf16 v[72:75], v[168:171], v[108:111], v[72:75]\n\t"
    "v_mfma_f32_16x16x32_bf16 v[76:79], v[172:175], v[108:111], v[76:79]\n\t"
    "s_waitcnt lgkmcnt(4)\n\t"
    "v_mfma_f32_16x16x32_bf16 v[64:67], v[176:179], v[112:115], v[64:67]\n\t"
    "ds_read_b128 v[104:107], %[da] offset:16384\n\t"
    "v_mfma_f32_16x16x32_bf16 v[68:71], v[180:183], v[112:115], v[68:71]\n\t"
    "ds_read_b128 v[108:111], %[da] offset:18432\n\t"
    "v_mfma_f32_16x16x32_bf16 v[72:75], v[176:179], v[116:119], v[72:75]\n\t"
    "v_mfma_f32_16x16x32_bf16 v[76:79], v[180:183], v[116:119], v[76:79]\n\t"
    "s_waitcnt lgkmcnt(4)\n\t"
    "v_mfma_f32_16x16x32_bf16 v[64:67], v[184:187], v[120:123], v[64:67]\n\t"
    "ds_read_b128 v[112:115], %[ea] offset:20480\n\t"
    "v_mfma_f32_16x16x32_bf16 v[68:71], v[188:191], v[120:123], v[68:71]\n\t"
    "ds_read_b128 v[116:119], %[ea] offset:22528\n\t"
    "v_mfma_f32_16x16x32_bf16 v[72:75], v[184:187], v[124:127], v[72:75]\n\t"
    "v_mfma_f32_16x16x32_bf16 v[76:79], v[188:191], v[124:127], v[76:79]\n\t"
    "s_waitcnt vmcnt(0)\n\t"
    "s_barrier\n\t"
    "s_waitcnt lgkmcnt(4)\n\t"
    "v_mfma_f32_16x16x32_bf16 v[64:67], v[192:195], v[96:99], v[64:67]\n\t"
    "ds_read_b128 v[120:123], %[da] offset:20480\n\t"
    "v_mfma_f32_16x16x32_bf16 v[68:71], v[196:199], v[96:99], v[68:71]\n\t"
    "ds_read_b128 v[124:127], %[da] offset:22528\n\t"
    "v_mfma_f32_16x16x32_bf16 v[72:75], v[192:195], v[100:103], v[72:75]\n\t"
    "ds_read_b128 v[48:51], %[rw] offset:0\n\t"
    "v_mfma_f32_16x16x32_bf16 v[76:79], v[196:199], v[100:103], v[76:79]\n\t"
    "ds_read_b128 v[52:55], %[rw] offset:1024\n\t"
    "s_cmp_eq_u32 %[w0], 0\n\t"
    "s_cbranch_scc1 5f\n\t"
    "6:\n\t"
    "s_waitcnt lgkmcnt(6)\n\t"
    "v_mfma_f32_16x16x32_bf16 v[64:67], v[200:203], v[104:107], v[64:67]\n\t"
    "ds_read_b128 v[96:99], %[ea] offset:24576\n\t"
    "v_mfma_f32_16x16x32_bf16 v[68:71], v[204:207], v[104:107], v[68:71]\n\t"
    "ds_read_b128 v[100:103], %[ea] offset:26624\n\t"
    "v_mfma_f32_16x16x32_bf16 v[72:75], v[200:203], v[108:111], v[72:75]\n\t"
    "ds_read_b128 v[56:59], %[rw] offset:2048\n\t"
    "v_mfma_f32_16x16x32_bf16 v[76:79], v[204:207], v[108:111], v[76:79]\n\t"
    "ds_read_b128 v[60:63], %[rw] offset:3072\n\t"
    "s_waitcnt lgkmcnt(0)\n\t"
    "v_mfma_f32_16x16x32_bf16 v[64:67], v[208:211], v[112:115], v[64:67]\n\t"
    "ds_read_b128 v[104:107], %[da] offset:24576\n\t"
    "v_add_f32 v48, v48, v80\n\t"
    "v_add_f32 v49, v49, v81\n\t"
    "v_add_f32 v50, v50, v82\n\t"
    "v_mfma_f32_16x16x32_bf16 v[68:71], v[212:215], v[112:115], v[68:71]\n\t"
    "ds_read_b128 v[108:111], %[da] offset:26624\n\t"
    "v_add_f32 v51, v51, v83\n\t"
    "v_add_f32 v52, v52, v84\n\t"
    "v_add_f32 v53, v53, v85\n\t"
    "v_mfma_f32_16x16x32_bf16 v[72:75], v[208:211], v[116:119], v[72:75]\n\t"
    "ds_read_b32 v39, %[sa]\n\t"
    "v_add_f32 v54, v54, v86\n\t"
    "v_add_f32 v55, v55, v87\n\t"
    "v_add_f32 v56, v56, v88\n\t"
    "v_mfma_f32_16x16x32_bf16 v[76:79], v[212:215], v[116:119], v[76:79]\n\t"
    "v_xor_b32 %[sa], 0x100, %[sa]\n\t"
    "v_add_f32 v57, v57, v89\n\t"
    "v_add_f32 v58, v58, v90\n\t"
    "v_add_f32 v59, v59, v91\n\t"
    "v_mfma_f32_16x16x32_bf16 v[64:67], v[216:219], v[120:123], v[64:67]\n\t"
    "ds_read_b128 v[112:115], %[ea] offset:28672\n\t"
    "v_add_f32 v60, v60, v92\n\t"
    "v_add_f32 v61, v61, v93\n\t"
    "v_add_f32 v62, v62, v94\n\t"
    "v_mfma_f32_16x16x32_bf16 v[68:71], v[220:223], v[120:123], v[68:71]\n\t"
    "ds_read_b128 v[116:119], %[ea] offset:30720\n\t"
    "v_add_f32 v63, v63, v95\n\t"
    "v_mul_f32 v40, %[nl], v48\n\t"
    "v_mul_f32 v41, %[nl], v49\n\t"
    "v_mfma_f32_16x16x32_bf16 v[72:75], v[216:219], v[124:127], v[72:75]\n\t"
    "v_xor_b32 %[ea], 0x10000, %[ea]\n\t"
    "v_mul_f32 v42, %[nl], v50\n\t"
    "v_mul_f32 v43, %[nl], v51\n\t"
    "v_mul_f32 v44, %[nl], v56\n\t"
    "v_mfma_f32_16x16x32_bf16 v[76:79], v[220:223], v[124:127], v[76:79]\n\t"
    "v_mul_f32 v45, %[nl], v57\n\t"
    "v_mul_f32 v46, %[nl], v58\n\t"
    "v_mul_f32 v47, %[nl], v59\n\t"
    "v_mfma_f32_16x16x32_bf16 v[64:67], v[224:227], v[96:99], v[64:67]\n\t"
    "ds_read_b128 v[120:123], %[da] offset:28672\n\t"
    "v_exp_f32 v40, v40\n\t"
    "v_exp_f32 v41, v41\n\t"
    "v_exp_f32 v42, v42\n\t"
    "v_mfma_f32_16x16x32_bf16 v[68:71], v[228:231], v[96:99], v[68:71]\n\t"
    "ds_read_b128 v[124:127], %[da] offset:30720\n\t"
    "v_exp_f32 v43, v43\n\t"
    "v_exp_f32 v44, v44\n\t"
    "v_exp_f32 v45, v45\n\t"
    "v_mfma_f32_16x16x32_bf16 v[72:75], v[224:227], v[100:103], v[72:75]\n\t"
    "v_xor_b32 %[da], 0x10000, %[da]\n\t"
    "v_exp_f32 v46, v46\n\t"
    "v_exp_f32 v47, v47\n\t"
    "v_add_f32 v40, 1.0, v40\n\t"
    "v_mfma_f32_16x16x32_bf16 v[76:79], v[228:231], v[100:103], v[76:79]\n\t"
    "v_add_f32 v41, 1.0, v41\n\t"
    "v_add_f32 v42, 1.0, v42\n\t"
    "v_add_f32 v43, 1.0, v43\n\t"
    "s_waitcnt lgkmcnt(5)\n\t"
    "v_mfma_f32_16x16x32_bf16 v[64:67], v[232:235], v[104:107], v[64:67]\n\t"
    "ds_read_b128 v[96:99], %[ea] offset:0\n\t"
    "v_add_f32 v44, 1.0, v44\n\t"
    "v_add_f32 v45, 1.0, v45\n\t"
    "v_add_f32 v46, 1.0, v46\n\t"
    "v_mfma_f32_16x16x32_bf16 v[68:71], v[236:239], v[104:107], v[68:71]\n\t"
    "ds_read_b128 v[100:103], %[ea] offset:2048\n\t"
    "v_add_f32 v47, 1.0, v47\n\t"
    "v_rcp_f32 v40, v40\n\t"
    "v_rcp_f32 v41, v41\n\t"
    "v_mfma_f32_16x16x32_bf16 v[72:75], v[232:235], v[108:111], v[72:75]\n\t"
    "v_rcp_f32 v42, v42\n\t"
    "v_rcp_f32 v43, v43\n\t"
    "v_rcp_f32 v44, v44\n\t"
    "v_mfma_f32_16x16x32_bf16 v[76:79], v[236:239], v[108:111], v[76:79]\n\t"
    "v_rcp_f32 v45, v45\n\t"
    "v_rcp_f32 v46, v46\n\t"
    "v_rcp_f32 v47, v47\n\t"
    "s_waitcnt lgkmcnt(4)\n\t"
    "v_mfma_f32_16x16x32_bf16 v[64:67], v[240:243], v[112:115], v[64:67]\n\t"
    "ds_read_b128 v[104:107], %[da] offset:0\n\t"
    "v_mad_u32_u24 v39, v39, %[mu], %[ad]\n\t"
    "v_mul_f32 v40, v48, v40\n\t"
    "v_mul_f32 v41, v49, v41\n\t"
    "v_mul_f32 v42, v50, v42\n\t"
    "v_mfma_f32_16x16x32_bf16 v[68:71], v[244:247], v[112:115], v[68:71]\n\t"
    "ds_read_b128 v[108:111], %[da] offset:2048\n\t"
    "v_cmp_ge_i32 vcc, %[lf], v39\n\t"
    "s_cmp_eq_u64 vcc, 0\n\t"
    "s_addc_u32 %[lf], %[lf], 0\n\t"
    "v_mul_f32 v43, v51, v43\n\t"
    "v_mul_f32 v44, v56, v44\n\t"
    "v_mul_f32 v45, v57, v45\n\t"
    "v_mfma_f32_16x16x32_bf16 v[72:75], v[240:243], v[116:119], v[72:75]\n\t"
    "v_mul_f32 v46, v58, v46\n\t"
    "v_mul_f32 v47, v59, v47\n\t"
    "v_mul_f32 v40, v40, v52\n\t"
    "v_mfma_f32_16x16x32_bf16 v[76:79], v[244:247], v[116:119], v[76:79]\n\t"
    "v_cmp_ge_i32 vcc, %[lf], v39\n\t"
    "s_cmp_eq_u64 vcc, 0\n\t"
    "s_addc_u32 %[lf], %[lf], 0\n\t"
    "v_mul_f32 v41, v41, v53\n\t"
    "v_mul_f32 v42, v42, v54\n\t"
    "v_mul_f32 v43, v43, v55\n\t"
    "s_waitcnt lgkmcnt(4)\n\t"
    "v_mfma_f32_16x16x32_bf16 v[80:83], v[248:251], v[120:123], v[64:67]\n\t"
    "ds_read_b128 v[112:115], %[ea] offset:4096\n\t"
    "s_min_i32 %[lf], %[lf], %[cp]\n\t"
    "s_add_u32 %[b], %[b], 1\n\t"
    "v_mul_f32 v44, v44, v60\n\t"
    "v_mul_f32 v45, v45, v61\n\t"
    "v_mul_f32 v46, v46, v62\n\t"
    "v_mfma_f32_16x16x32_bf16 v[84:87], v[252:255], v[120:123], v[68:71]\n\t"
    "ds_read_b128 v[116:119], %[ea] offset:6144\n\t"
    "v_mul_f32 v47, v47, v63\n\t"
    "v_cvt_pk_bf16_f32 v48, v40, v41\n\t"
    "v_cvt_pk_bf16_f32 v49, v42, v43\n\t"
    "v_mfma_f32_16x16x32_bf16 v[88:91], v[248:251], v[124:127], v[72:75]\n\t"
    "v_cvt_pk_bf16_f32 v50, v44, v45\n\t"
    "v_cvt_pk_bf16_f32 v51, v46, v47\n\t"
    "s_nop 1\n\t"
    "v_mfma_f32_16x16x32_bf16 v[92:95], v[252:255], v[124:127], v[76:79]\n\t"
    "v_permlane16_swap_b32 v48, v50\n\t"
    "v_permlane16_swap_b32 v49, v51\n\t"
    "global_store_dwordx4 %[co0], v[48:51], s[36:37] sc1\n\t"
    "s_mov_b64 s[36:37], s[38:39]\n\t"
    "s_add_u32 s38, s38, %[ab]\n\t"
    "s_addc_u32 s39, s39, 0\n\t"
    "s_cmp_lt_i32 %[b], %[lf]\n\t"
    "s_cbranch_scc1 1b\n\t"
    "s_waitcnt lgkmcnt(0)\n\t"
    "s_nop 4\n\t"
    "s_branch 9f\n\t"
    "5:\n\t"
    "s_add_u32 %[st], %[b], %[po]\n\t"
    "s_max_i32 %[st], %[st], %[p0]\n\t"
    "v_mov_b32 v39, %[st]\n\t"
    "v_mov_b32 v38, 0\n\t"
    "s_mov_b64 exec, 1\n\t"
    "global_store_dword v38, v39, %[pg] sc1\n\t"
    "s_mov_b64 exec, -1\n\t"
    "s_mov_b32 %[p0], %[p1]\n\t"
    "s_xor_b32 %[sl], %[sl], 0x100\n\t"
    "s_cmp_ge_i32 %[lf], %[cp]\n\t"
    "s_cbranch_scc1 6b\n\t"
    "s_mov_b32 m0, %[sl]\n\t"
    "s_nop 0\n\t"
    "global_load_lds_dword %[dv], %[db] sc1\n\t"
    "s_branch 6b\n\t"
    "9:"
    : "=&{v[80:95]}"(pd), [ea] "+&v"(ea), [da] "+&v"(da), [b] "+&s"(b), [lf] "+&s"(lf), "+{s[36:37]}"(p1), "+{s[38:39]}"(p0), [sa] "+&v"(sa), [st] "=&s"(st), [sl] "+&s"(sl), [p0] "+&s"(pf)
    : [rw] "v"(rw), [ab] "s"(ab), [cp] "s"(cp), [mu] "s"(mu), [ad] "v"(ad), "{v[128:159]}"(W[0]), "{v[160:191]}"(W[1]), "{v[192:223]}"(W[2]), "{v[224:255]}"(W[3]), [co0] "v"(co0), [w0] "s"(w0), [dv] "v"(dv), [db] "s"(db), [pg] "s"(pg), [po] "s"(po - 1), [p1] "s"(b + po), [nl] "s"(-LOG2E)
    : "v38", "v39", "v40", "v41", "v42", "v43", "v44", "v45", "v46", "v47", "v48", "v49", "v50", "v51", "v52", "v53", "v54", "v55", "v56", "v57", "v58", "v59", "v60", "v61", "v62", "v63", "v64", "v65", "v66", "v67", "v68", "v69", "v70", "v71", "v72", "v73", "v74", "v75", "v76", "v77", "v78", "v79", "v96", "v97", "v98", "v99", "v100", "v101", "v102", "v103", "v104", "v105", "v106", "v107", "v108", "v109", "v110", "v111", "v112", "v113", "v114", "v115", "v116", "v117", "v118", "v119", "v120", "v121", "v122", "v123", "v124", "v125", "v126", "v127", "vcc", "scc", "m0", "memory");
}
__device__ __forceinline__ void fast_st2(const u32x32 (&W)[4], u32x16& pd, unsigned ea, unsigned da, int& b, int& lf, int cp, unsigned sa, unsigned mu, unsigned ad,
    unsigned rw, unsigned ab, const char* p1, const char* p0, unsigned co0, unsigned co1, int po, int pf, unsigned w0, unsigned sl, unsigned dv, const char* db, const char* pg) {
  unsigned st;
  asm volatile("s_barrier\n\t"
    "ds_read_b128 v[96:99], %[ea] offset:0\n\t"
    "ds_read_b128 v[100:103], %[ea] offset:2048\n\t"
    "ds_read_b128 v[104:107], %[da] offset:0\n\t"
    "ds_read_b128 v[108:111], %[da] offset:2048\n\t"
    "ds_read_b128 v[112:115], %[ea] offset:4096\n\t"
    "ds_read_b128 v[116:119], %[ea] offset:6144\n\t"
    "s_branch 2f\n\t"
    "1:\n\t"
    "s_barrier\n\t"
    "2:\n\t"
    "s_waitcnt lgkmcnt(4)\n\t"
    "v_mfma_f32_16x16x32_bf16 v[64:67], v[128:131], v[96:99], 0\n\t"
    "ds_read_b128 v[120:123], %[da] offset:4096\n\t"
    "v_mfma_f32_16x16x32_bf16 v[68:71], v[132:135], v[96:99], 0\n\t"
    "ds_read_b128 v[124:127], %[da] offset:6144\n\t"
    "v_mfma_f32_16x16x32_bf16 v[72:75], v[128:131], v[100:103], 0\n\t"
    "v_mfma_f32_16x16x32_bf16 v[76:79], v[132:135], v[100:103], 0\n\t"
    "s_waitcnt lgkmcnt(4)\n\t"
    "v_mfma_f32_16x16x32_bf16 v[64:67], v[136:139], v[104:107], v[64:67]\n\t"
    "ds_read_b128 v[96:99], %[ea] offset:8192\n\t"
    "v_mfma_f32_16x16x32_bf16 v[68:71], v[140:143], v[104:107], v[68:71]\n\t"
    "ds_read_b128 v[100:103], %[ea] offset:10240\n\t"
    "v_mfma_f32_16x16x32_bf16 v[72:75], v[136:139], v[108:111], v[72:75]\n\t"
    "v_mfma_f32_16x16x32_bf16 v[76:79], v[140:143], v[108:111], v[76:79]\n\t"
    "s_waitcnt lgkmcnt(4)\n\t"
    "v_mfma_f32_16x16x32_bf16 v[64:67], v[144:147], v[112:115], v[64:67]\n\t"
    "ds_read_b128 v[104:107], %[da] offset:8192\n\t"
    "v_mfma_f32_16x16x32_bf16 v[68:71], v[148:151], v[112:115], v[68:71]\n\t"
    "ds_read_b128 v[108:111], %[da] offset:10240\n\t"
    "v_mfma_f32_16x16x32_bf16 v[72:75], v[144:147], v[116:119], v[72:75]\n\t"
    "v_mfma_f32_16x16x32_bf16 v[76:79], v[148:151], v[116:119], v[76:79]\n\t"
    "s_waitcnt lgkmcnt(4)\n\t"
    "v_mfma_f32_16x16x32_bf16 v[64:67], v[152:155], v[120:123], v[64:67]\n\t"
    "ds_read_b128 v[112:115], %[ea] offset:12288\n\t"
    "v_mfma_f32_16x16x32_bf16 v[68:71], v[156:159], v[120:123], v[68:71]\n\t"
    "ds_read_b128 v[116:119], %[ea] offset:14336\n\t"
    "v_mfma_f32_16x16x32_bf16 v[72:75], v[152:155], v[124:127], v[72:75]\n\t"
    "v_mfma_f32_16x16x32_bf16 v[76:79], v[156:159], v[124:127], v[76:79]\n\t"
    "s_waitcnt lgkmcnt(4)\n\t"
    "v_mfma_f32_16x16x32_bf16 v[64:67], v[160:163], v[96:99], v[64:67]\n\t"
    "ds_read_b128 v[120:123], %[da] offset:12288\n\t"
    "v_mfma_f32_16x16x32_bf16 v[68:71], v[164:167], v[96:99], v[68:71]\n\t"
    "ds_read_b128 v[124:127], %[da] offset:14336\n\t"
    "v_mfma_f32_16x16x32_bf16 v[72:75], v[160:163], v[100:103], v[72:75]\n\t"
    "v_mfma_f32_16x16x32_bf16 v[76:79], v[164:167], v[100:103], v[76:79]\n\t"
    "s_waitcnt lgkmcnt(4)\n\t"
    "v_mfma_f32_16x16x32_bf16 v[64:67], v[168:171], v[104:107], v[64:67]\n\t"
    "ds_read_b128 v[96:99], %[ea] offset:16384\n\t"
    "v_mfma_f32_16x16x32_bf16 v[68:71], v[172:175], v[104:107], v[68:71]\n\t"
    "ds_read_b128 v[100:103], %[ea] offset:18432\n\t"
    "v_mfma_f32_16x16x32_bf16 v[72:75], v[168:171], v[108:111], v[72:75]\n\t"
    "v_mfma_f32_16x16x32_bf16 v[76:79], v[172:175], v[108:111], v[76:79]\n\t"
    "s_waitcnt lgkmcnt(4)\n\t"
    "v_mfma_f32_16x16x32_bf16 v[64:67], v[176:179], v[112:115], v[64:67]\n\t"
    "ds_read_b128 v[104:107], %[da] offset:16384\n\t"
    "v_mfma_f32_16x16x32_bf16 v[68:71], v[180:183], v[112:115], v[68:71]\n\t"
    "ds_read_b128 v[108:111], %[da] offset:18432\n\t"
    "v_mfma_f32_16x16x32_bf16 v[72:75], v[176:179], v[116:119], v[72:75]\n\t"
    "v_mfma_f32_16x16x32_bf16 v[76:79], v[180:183], v[116:119], v[76:79]\n\t"
    "s_waitcnt lgkmcnt(4)\n\t"
    "v_mfma_f32_16x16x32_bf16 v[64:67], v[184:187], v[120:123], v[64:67]\n\t"
    "ds_read_b128 v[112:115], %[ea] offset:20480\n\t"
    "v_mfma_f32_16x16x32_bf16 v[68:71], v[188:191], v[120:123], v[68:71]\n\t"
    "ds_read_b128 v[116:119], %[ea] offset:22528\n\t"
    "v_mfma_f32_16x16x32_bf16 v[72:75], v[184:187], v[124:127], v[72:75]\n\t"
    "v_mfma_f32_16x16x32_bf16 v[76:79], v[188:191], v[124:127], v[76:79]\n\t"
    "s_waitcnt vmcnt(0)\n\t"
    "s_barrier\n\t"
    "s_waitcnt lgkmcnt(4)\n\t"
    "v_mfma_f32_16x16x32_bf16 v[64:67], v[192:195], v[96:99], v[64:67]\n\t"
    "ds_read_b128 v[120:123], %[da] offset:20480\n\t"
    "v_mfma_f32_16x16x32_bf16 v[68:71], v[196:199], v[96:99], v[68:71]\n\t"
    "ds_read_b128 v[124:127], %[da] offset:22528\n\t"
    "v_mfma_f32_16x16x32_bf16 v[72:75], v[192:195], v[100:103], v[72:75]\n\t"
    "ds_read_b128 v[48:51], %[rw] offset:0\n\t"
    "v_mfma_f32_16x16x32_bf16 v[76:79], v[196:199], v[100:103], v[76:79]\n\t"
    "ds_read_b128 v[52:55], %[rw] offset:1024\n\t"
    "s_cmp_eq_u32 %[w0], 0\n\t"
    "s_cbranch_scc1 5f\n\t"
    "6:\n\t"
    "s_waitcnt lgkmcnt(6)\n\t"
    "v_mfma_f32_16x16x32_bf16 v[64:67], v[200:203], v[104:107], v[64:67]\n\t"
    "ds_read_b128 v[96:99], %[ea] offset:24576\n\t"
    "v_mfma_f32_16x16x32_bf16 v[68:71], v[204:207], v[104:107], v[68:71]\n\t"
    "ds_read_b128 v[100:103], %[ea] offset:26624\n\t"
    "v_mfma_f32_16x16x32_bf16 v[72:75], v[200:203], v[108:111], v[72:75]\n\t"
    "ds_read_b128 v[56:59], %[rw] offset:2048\n\t"
    "v_mfma_f32_16x16x32_bf16 v[76:79], v[204:207], v[108:111], v[76:79]\n\t"
    "ds_read_b128 v[60:63], %[rw] offset:3072\n\t"
    "s_waitcnt lgkmcnt(0)\n\t"
    "v_mfma_f32_16x16x32_bf16 v[64:67], v[208:211], v[112:115], v[64:67]\n\t"
    "ds_read_b128 v[104:107], %[da] offset:24576\n\t"
    "v_cvt_pk_bf16_f32 v40, v80, v81\n\t"
    "v_mfma_f32_16x16x32_bf16 v[68:71], v[212:215], v[112:115], v[68:71]\n\t"
    "ds_read_b128 v[108:111], %[da] offset:26624\n\t"
    "v_cvt_pk_bf16_f32 v41, v82, v83\n\t"
    "v_mfma_f32_16x16x32_bf16 v[72:75], v[208:211], v[116:119], v[72:75]\n\t"
    "ds_read_b32 v39, %[sa]\n\t"
    "v_cvt_pk_bf16_f32 v42, v84, v85\n\t"
    "v_mfma_f32_16x16x32_bf16 v[76:79], v[212:215], v[116:119], v[76:79]\n\t"
    "v_xor_b32 %[sa], 0x100, %[sa]\n\t"
    "v_cvt_pk_bf16_f32 v43, v86, v87\n\t"
    "v_mfma_f32_16x16x32_bf16 v[64:67], v[216:219], v[120:123], v[64:67]\n\t"
    "ds_read_b128 v[112:115], %[ea] offset:28672\n\t"
    "global_store_dwordx4 %[co0], v[40:43], s[36:37] sc1\n\t"
    "v_mfma_f32_16x16x32_bf16 v[68:71], v[220:223], v[120:123], v[68:71]\n\t"
    "ds_read_b128 v[116:119], %[ea] offset:30720\n\t"
    "v_cvt_pk_bf16_f32 v44, v88, v89\n\t"
    "v_mfma_f32_16x16x32_bf16 v[72:75], v[216:219], v[124:127], v[72:75]\n\t"
    "v_xor_b32 %[ea], 0x10000, %[ea]\n\t"
    "v_cvt_pk_bf16_f32 v45, v90, v91\n\t"
    "v_mfma_f32_16x16x32_bf16 v[76:79], v[220:223], v[124:127], v[76:79]\n\t"
    "v_cvt_pk_bf16_f32 v46, v92, v93\n\t"
    "v_mfma_f32_16x16x32_bf16 v[64:67], v[224:227], v[96:99], v[64:67]\n\t"
    "ds_read_b128 v[120:123], %[da] offset:28672\n\t"
    "v_cvt_pk_bf16_f32 v47, v94, v95\n\t"
    "v_mfma_f32_16x16x32_bf16 v[68:71], v[228:231], v[96:99], v[68:71]\n\t"
    "ds_read_b128 v[124:127], %[da] offset:30720\n\t"
    "global_store_dwordx4 %[co1], v[44:47], s[36:37] sc1\n\t"
    "v_mfma_f32_16x16x32_bf16 v[72:75], v[224:227], v[100:103], v[72:75]\n\t"
    "v_xor_b32 %[da], 0x10000, %[da]\n\t"
    "v_cvt_pk_bf16_f32 v48, v48, v49\n\t"
    "v_mfma_f32_16x16x32_bf16 v[76:79], v[228:231], v[100:103], v[76:79]\n\t"
    "v_cvt_pk_bf16_f32 v49, v50, v51\n\t"
    "s_waitcnt lgkmcnt(5)\n\t"
    "v_mfma_f32_16x16x32_bf16 v[64:67], v[232:235], v[104:107], v[64:67]\n\t"
    "ds_read_b128 v[96:99], %[ea] offset:0\n\t"
    "v_cvt_pk_bf16_f32 v50, v52, v53\n\t"
    "v_mfma_f32_16x16x32_bf16 v[68:71], v[236:239], v[104:107], v[68:71]\n\t"
    "ds_read_b128 v[100:103], %[ea] offset:2048\n\t"
    "v_cvt_pk_bf16_f32 v51, v54, v55\n\t"
    "v_mfma_f32_16x16x32_bf16 v[72:75], v[232:235], v[108:111], v[72:75]\n\t"
    "global_store_dwordx4 %[co0], v[48:51], s[36:37] offset:2048 sc1\n\t"
    "v_mfma_f32_16x16x32_bf16 v[76:79], v[236:239], v[108:111], v[76:79]\n\t"
    "v_cvt_pk_bf16_f32 v56, v56, v57\n\t"
    "s_waitcnt lgkmcnt(4)\n\t"
    "v_mfma_f32_16x16x32_bf16 v[64:67], v[240:243], v[112:115], v[64:67]\n\t"
    "ds_read_b128 v[104:107], %[da] offset:0\n\t"
    "v_mad_u32_u24 v39, v39, %[mu], %[ad]\n\t"
    "v_cvt_pk_bf16_f32 v57, v58, v59\n\t"
    "v_mfma_f32_16x16x32_bf16 v[68:71], v[244:247], v[112:115], v[68:71]\n\t"
    "ds_read_b128 v[108:111], %[da] offset:2048\n\t"
    "v_cmp_ge_i32 vcc, %[lf], v39\n\t"
    "s_cmp_eq_u64 vcc, 0\n\t"
    "s_addc_u32 %[lf], %[lf], 0\n\t"
    "v_cvt_pk_bf16_f32 v58, v60, v61\n\t"
    "v_mfma_f32_16x16x32_bf16 v[72:75], v[240:243], v[116:119], v[72:75]\n\t"
    "v_cvt_pk_bf16_f32 v59, v62, v63\n\t"
    "v_mfma_f32_16x16x32_bf16 v[76:79], v[244:247], v[116:119], v[76:79]\n\t"
    "v_cmp_ge_i32 vcc, %[lf], v39\n\t"
    "s_cmp_eq_u64 vcc, 0\n\t"
    "s_addc_u32 %[lf], %[lf], 0\n\t"
    "global_store_dwordx4 %[co1], v[56:59], s[36:37] offset:2048 sc1\n\t"
    "s_waitcnt lgkmcnt(4)\n\t"
    "v_mfma_f32_16x16x32_bf16 v[80:83], v[248:251], v[120:123], v[64:67]\n\t"
    "ds_read_b128 v[112:115], %[ea] offset:4096\n\t"
    "s_min_i32 %[lf], %[lf], %[cp]\n\t"
    "s_add_u32 %[b], %[b], 1\n\t"
    "v_mfma_f32_16x16x32_bf16 v[84:87], v[252:255], v[120:123], v[68:71]\n\t"
    "ds_read_b128 v[116:119], %[ea] offset:6144\n\t"
    "v_mfma_f32_16x16x32_bf16 v[88:91], v[248:251], v[124:127], v[72:75]\n\t"
    "v_mfma_f32_16x16x32_bf16 v[92:95], v[252:255], v[124:127], v[76:79]\n\t"
    "s_mov_b64 s[36:37], s[38:39]\n\t"
    "s_add_u32 s38, s38, %[ab]\n\t"
    "s_addc_u32 s39, s39, 0\n\t"
    "s_cmp_lt_i32 %[b], %[lf]\n\t"
    "s_cbranch_scc1 1b\n\t"
    "s_waitcnt lgkmcnt(0)\n\t"
    "s_nop 4\n\t"
    "s_branch 9f\n\t"
    "5:\n\t"
    "s_add_u32 %[st], %[b], %[po]\n\t"
    "s_max_i32 %[st], %[st], %[p0]\n\t"
    "v_mov_b32 v39, %[st]\n\t"
    "v_mov_b32 v38, 0\n\t"
    "s_mov_b64 exec, 1\n\t"
    "global_store_dword v38, v39, %[pg] sc1\n\t"
    "s_mov_b64 exec, -1\n\t"
    "s_mov_b32 %[p0], %[p1]\n\t"
    "s_xor_b32 %[sl], %[sl], 0x100\n\t"
    "s_cmp_ge_i32 %[lf], %[cp]\n\t"
    "s_cbranch_scc1 6b\n\t"
    "s_mov_b32 m0, %[sl]\n\t"
    "s_nop 0\n\t"
    "global_load_lds_dword %[dv], %[db] sc1\n\t"
    "s_branch 6b\n\t"
    "9:"
    : "=&{v[80:95]}"(pd), [ea] "+&v"(ea), [da] "+&v"(da), [b] "+&s"(b), [lf] "+&s"(lf), "+{s[36:37]}"(p1), "+{s[38:39]}"(p0), [sa] "+&v"(sa), [st] "=&s"(st), [sl] "+&s"(sl), [p0] "+&s"(pf)
    : [rw] "v"(rw), [ab] "s"(ab), [cp] "s"(cp), [mu] "s"(mu), [ad] "v"(ad), "{v[128:159]}"(W[0]), "{v[160:191]}"(W[1]), "{v[192:223]}"(W[2]), "{v[224:255]}"(W[3]), [co0] "v"(co0), [w0] "s"(w0), [dv] "v"(dv), [db] "s"(db), [pg] "s"(pg), [po] "s"(po - 1), [p1] "s"(b + po), [co1] "v"(co1)
    : "v38", "v39", "v40", "v41", "v42", "v43", "v44", "v45", "v46", "v47", "v48", "v49", "v50", "v51", "v52", "v53", "v54", "v55", "v56", "v57", "v58", "v59", "v60", "v61", "v62", "v63", "v64", "v65", "v66", "v67", "v68", "v69", "v70", "v71", "v72", "v73", "v74", "v75", "v76", "v77", "v78", "v79", "v96", "v97", "v98", "v99", "v100", "v101", "v102", "v103", "v104", "v105", "v106", "v107", "v108", "v109", "v110", "v111", "v112", "v113", "v114", "v115", "v116", "v117", "v118", "v119", "v120", "v121", "v122", "v123", "v124", "v125", "v126", "v127", "vcc", "scc", "m0", "memory");
}
__device__ __forceinline__ void near_ld0(const u32x32 (&W)[4], u32x16& pd, unsigned ea, unsigned da, int& b, int& lf, int cp, unsigned sa, unsigned mu, unsigned ad,
    unsigned rw, unsigned ab, const char* p1, const char* p0, unsigned l1, unsigned l0, const unsigned (&av)[8]) {
  asm volatile("s_setprio 0\n\t"
    "s_waitcnt vmcnt(4)\n\t"
    "s_barrier\n\t"
    "ds_read_b128 v[96:99], %[ea] offset:0\n\t"
    "ds_read_b128 v[100:103], %[ea] offset:2048\n\t"
    "ds_read_b128 v[104:107], %[da] offset:0\n\t"
    "ds_read_b128 v[108:111], %[da] offset:2048\n\t"
    "ds_read_b128 v[112:115], %[ea] offset:4096\n\t"
    "ds_read_b128 v[116:119], %[ea] offset:6144\n\t"
    "s_branch 2f\n\t"
    "1:\n\t"
    "s_waitcnt vmcnt(4)\n\t"
    "s_nop 1\n\t"
    "s_barrier\n\t"
    "2:\n\t"
    "s_waitcnt lgkmcnt(4)\n\t"
    "v_mfma_f32_16x16x32_bf16 v[64:67], v[128:131], v[96:99], 0\n\t"
    "ds_read_b128 v[120:123], %[da] offset:4096\n\t"
    "s_add_u32 m0, %[l1], 0\n\t"
    "ds_write_b128 %[rw], v[80:83] offset:0\n\t"
    "v_mfma_f32_16x16x32_bf16 v[68:71], v[132:135], v[96:99], 0\n\t"
    "ds_read_b128 v[124:127], %[da] offset:6144\n\t"
    "global_load_lds_dwordx4 %[av0], s[36:37] sc1\n\t"
    "ds_write_b128 %[rw], v[84:87] offset:1024\n\t"
    "v_mfma_f32_16x16x32_bf16 v[72:75], v[128:131], v[100:103], 0\n\t"
    "s_add_u32 m0, %[l1], 32768\n\t"
    "ds_write_b128 %[rw], v[88:91] offset:2048\n\t"
    "v_mfma_f32_16x16x32_bf16 v[76:79], v[132:135], v[100:103], 0\n\t"
    "global_load_lds_dwordx4 %[av1], s[36:37] sc1\n\t"
    "ds_write_b128 %[rw], v[92:95] offset:3072\n\t"
    "s_waitcnt lgkmcnt(8)\n\t"
    "v_mfma_f32_16x16x32_bf16 v[64:67], v[136:139], v[104:107], v[64:67]\n\t"
    "ds_read_b128 v[96:99], %[ea] offset:8192\n\t"
    "s_add_u32 m0, %[l1], 4096\n\t"
    "v_mfma_f32_16x16x32_bf16 v[68:71], v[140:143], v[104:107], v[68:71]\n\t"
    "ds_read_b128 v[100:103], %[ea] offset:10240\n\t"
    "global_load_lds_dwordx4 %[av2], s[36:37] sc1\n\t"
    "v_mfma_f32_16x16x32_bf16 v[72:75], v[136:139], v[108:111], v[72:75]\n\t"
    "s_add_u32 m0, %[l1], 36864\n\t"
    "v_mfma_f32_16x16x32_bf16 v[76:79], v[140:143], v[108:111], v[76:79]\n\t"
    "global_load_lds_dwordx4 %[av3], s[36:37] sc1\n\t"
    "s_waitcnt lgkmcnt(2)\n\t"
    "v_mfma_f32_16x16x32_bf16 v[64:67], v[144:147], v[112:115], v[64:67]\n\t"
    "ds_read_b128 v[104:107], %[da] offset:8192\n\t"
    "s_add_u32 m0, %[l1], 8192\n\t"
    "v_mfma_f32_16x16x32_bf16 v[68:71], v[148:151], v[112:115], v[68:71]\n\t"
    "ds_read_b128 v[108:111], %[da] offset:10240\n\t"
    "global_load_lds_dwordx4 %[av4], s[36:37] sc1\n\t"
    "v_mfma_f32_16x16x32_bf16 v[72:75], v[144:147], v[116:119], v[72:75]\n\t"
    "s_add_u32 m0, %[l1], 40960\n\t"
    "v_mfma_f32_16x16x32_bf16 v[76:79], v[148:151], v[116:119], v[76:79]\n\t"
    "global_load_lds_dwordx4 %[av5], s[36:37] sc1\n\t"
    "s_waitcnt vmcnt(6)\n\t"
    "s_barrier\n\t"
    "v_mfma_f32_16x16x32_bf16 v[64:67], v[152:155], v[120:123], v[64:67]\n\t"
    "ds_read_b128 v[112:115], %[ea] offset:12288\n\t"
    "s_add_u32 m0, %[l1], 12288\n\t"
    "v_mfma_f32_16x16x32_bf16 v[68:71], v[156:159], v[120:123], v[68:71]\n\t"
    "ds_read_b128 v[116:119], %[ea] offset:14336\n\t"
    "global_load_lds_dwordx4 %[av6], s[36:37] sc1\n\t"
    "v_mfma_f32_16x16x32_bf16 v[72:75], v[152:155], v[124:127], v[72:75]\n\t"
    "s_add_u32 m0, %[l1], 45056\n\t"
    "v_mfma_f32_16x16x32_bf16 v[76:79], v[156:159], v[124:127], v[76:79]\n\t"
    "global_load_lds_dwordx4 %[av7], s[36:37] sc1\n\t"
    "s_waitcnt lgkmcnt(4)\n\t"
    "v_mfma_f32_16x16x32_bf16 v[64:67], v[160:163], v[96:99], v[64:67]\n\t"
    "ds_read_b128 v[120:123], %[da] offset:12288\n\t"
    "s_add_u32 m0, %[l0], 0\n\t"
    "v_mfma_f32_16x16x32_bf16 v[68:71], v[164:167], v[96:99], v[68:71]\n\t"
    "ds_read_b128 v[124:127], %[da] offset:14336\n\t"
    "global_load_lds_dwordx4 %[av0], s[38:39] sc1\n\t"
    "v_mfma_f32_16x16x32_bf16 v[72:75], v[160:163], v[100:103], v[72:75]\n\t"
    "s_add_u32 m0, %[l0], 32768\n\t"
    "v_mfma_f32_16x16x32_bf16 v[76:79], v[164:167], v[100:103], v[76:79]\n\t"
    "global_load_lds_dwordx4 %[av1], s[38:39] sc1\n\t"
    "s_waitcnt lgkmcnt(4)\n\t"
    "v_mfma_f32_16x16x32_bf16 v[64:67], v[168:171], v[104:107], v[64:67]\n\t"
    "ds_read_b128 v[96:99], %[ea] offset:16384\n\t"
    "s_add_u32 m0, %[l0], 4096\n\t"
    "v_mfma_f32_16x16x32_bf16 v[68:71], v[172:175], v[104:107], v[68:71]\n\t"
    "ds_read_b128 v[100:103], %[ea] offset:18432\n\t"
    "global_load_lds_dwordx4 %[av2], s[38:39] sc1\n\t"
    "v_mfma_f32_16x16x32_bf16 v[72:75], v[168:171], v[108:111], v[72:75]\n\t"
    "s_add_u32 m0, %[l0], 36864\n\t"
    "v_mfma_f32_16x16x32_bf16 v[76:79], v[172:175], v[108:111], v[76:79]\n\t"
    "global_load_lds_dwordx4 %[av3], s[38:39] sc1\n\t"
    "s_waitcnt lgkmcnt(4)\n\t"
    "v_mfma_f32_16x16x32_bf16 v[64:67], v[176:179], v[112:115], v[64:67]\n\t"
    "ds_read_b128 v[104:107], %[da] offset:16384\n\t"
    "s_add_u32 m0, %[l0], 8192\n\t"
    "v_mfma_f32_16x16x32_bf16 v[68:71], v[180:183], v[112:115], v[68:71]\n\t"
    "ds_read_b128 v[108:111], %[da] offset:18432\n\t"
    "global_load_lds_dwordx4 %[av4], s[38:39] sc1\n\t"
    "v_mfma_f32_16x16x32_bf16 v[72:75], v[176:179], v[116:119], v[72:75]\n\t"
    "s_add_u32 m0, %[l0], 40960\n\t"
    "v_mfma_f32_16x16x32_bf16 v[76:79], v[180:183], v[116:119], v[76:79]\n\t"
    "global_load_lds_dwordx4 %[av5], s[38:39] sc1\n\t"
    "s_waitcnt lgkmcnt(4)\n\t"
    "v_mfma_f32_16x16x32_bf16 v[64:67], v[184:187], v[120:123], v[64:67]\n\t"
    "ds_read_b128 v[112:115], %[ea] offset:20480\n\t"
    "s_add_u32 m0, %[l0], 12288\n\t"
    "v_mfma_f32_16x16x32_bf16 v[68:71], v[188:191], v[120:123], v[68:71]\n\t"
    "ds_read_b128 v[116:119], %[ea] offset:22528\n\t"
    "global_load_lds_dwordx4 %[av6], s[38:39] sc1\n\t"
    "v_mfma_f32_16x16x32_bf16 v[72:75], v[184:187], v[124:127], v[72:75]\n\t"
    "s_add_u32 m0, %[l0], 45056\n\t"
    "v_mfma_f32_16x16x32_bf16 v[76:79], v[188:191], v[124:127], v[76:79]\n\t"
    "global_load_lds_dwordx4 %[av7], s[38:39] sc1\n\t"
    "s_waitcnt lgkmcnt(4)\n\t"
    "v_mfma_f32_16x16x32_bf16 v[64:67], v[192:195], v[96:99], v[64:67]\n\t"
    "ds_read_b128 v[120:123], %[da] offset:20480\n\t"
    "v_mfma_f32_16x16x32_bf16 v[68:71], v[196:199], v[96:99], v[68:71]\n\t"
    "ds_read_b128 v[124:127], %[da] offset:22528\n\t"
    "v_mfma_f32_16x16x32_bf16 v[72:75], v[192:195], v[100:103], v[72:75]\n\t"
    "v_mfma_f32_16x16x32_bf16 v[76:79], v[196:199], v[100:103], v[76:79]\n\t"
    "s_waitcnt lgkmcnt(4)\n\t"
    "v_mfma_f32_16x16x32_bf16 v[64:67], v[200:203], v[104:107], v[64:67]\n\t"
    "ds_read_b128 v[96:99], %[ea] offset:24576\n\t"
    "v_mfma_f32_16x16x32_bf16 v[68:71], v[204:207], v[104:107], v[68:71]\n\t"
    "ds_read_b128 v[100:103], %[ea] offset:26624\n\t"
    "v_mfma_f32_16x16x32_bf16 v[72:75], v[200:203], v[108:111], v[72:75]\n\t"
    "v_mfma_f32_16x16x32_bf16 v[76:79], v[204:207], v[108:111], v[76:79]\n\t"
    "s_add_u32 s36, s36, %[ab]\n\t"
    "s_addc_u32 s37, s37, 0\n\t"
    "s_waitcnt lgkmcnt(4)\n\t"
    "v_mfma_f32_16x16x32_bf16 v[64:67], v[208:211], v[112:115], v[64:67]\n\t"
    "ds_read_b128 v[104:107], %[da] offset:24576\n\t"
    "v_mfma_f32_16x16x32_bf16 v[68:71], v[212:215], v[112:115], v[68:71]\n\t"
    "ds_read_b128 v[108:111], %[da] offset:26624\n\t"
    "v_mfma_f32_16x16x32_bf16 v[72:75], v[208:211], v[116:119], v[72:75]\n\t"
    "v_mfma_f32_16x16x32_bf16 v[76:79], v[212:215], v[116:119], v[76:79]\n\t"
    "s_xor_b32 %[l1], %[l1], 0x10000\n\t"
    "s_waitcnt lgkmcnt(4)\n\t"
    "v_mfma_f32_16x16x32_bf16 v[64:67], v[216:219], v[120:123], v[64:67]\n\t"
    "ds_read_b128 v[112:115], %[ea] offset:28672\n\t"
    "v_mfma_f32_16x16x32_bf16 v[68:71], v[220:223], v[120:123], v[68:71]\n\t"
    "ds_read_b128 v[116:119], %[ea] offset:30720\n\t"
    "v_mfma_f32_16x16x32_bf16 v[72:75], v[216:219], v[124:127], v[72:75]\n\t"
    "v_xor_b32 %[ea], 0x10000, %[ea]\n\t"
    "v_mfma_f32_16x16x32_bf16 v[76:79], v[220:223], v[124:127], v[76:79]\n\t"
    "s_waitcnt vmcnt(12)\n\t"
    "s_barrier\n\t"
    "s_waitcnt lgkmcnt(4)\n\t"
    "v_mfma_f32_16x16x32_bf16 v[64:67], v[224:227], v[96:99], v[64:67]\n\t"
    "ds_read_b32 v39, %[sa]\n\t"
    "ds_read_b128 v[120:123], %[da] offset:28672\n\t"
    "v_mfma_f32_16x16x32_bf16 v[68:71], v[228:231], v[96:99], v[68:71]\n\t"
    "ds_read_b128 v[124:127], %[da] offset:30720\n\t"
    "v_mfma_f32_16x16x32_bf16 v[72:75], v[224:227], v[100:103], v[72:75]\n\t"
    "v_xor_b32 %[da], 0x10000, %[da]\n\t"
    "v_mfma_f32_16x16x32_bf16 v[76:79], v[228:231], v[100:103], v[76:79]\n\t"
    "s_waitcnt lgkmcnt(5)\n\t"
    "v_mfma_f32_16x16x32_bf16 v[64:67], v[232:235], v[104:107], v[64:67]\n\t"
    "ds_read_b128 v[96:99], %[ea] offset:0\n\t"
    "v_mfma_f32_16x16x32_bf16 v[68:71], v[236:239], v[104:107], v[68:71]\n\t"
    "ds_read_b128 v[100:103], %[ea] offset:2048\n\t"
    "v_mfma_f32_16x16x32_bf16 v[72:75], v[232:235], v[108:111], v[72:75]\n\t"
    "v_mfma_f32_16x16x32_bf16 v[76:79], v[236:239], v[108:111], v[76:79]\n\t"
    "s_waitcnt lgkmcnt(4)\n\t"
    "v_mfma_f32_16x16x32_bf16 v[64:67], v[240:243], v[112:115], v[64:67]\n\t"
    "ds_read_b128 v[104:107], %[da] offset:0\n\t"
    "v_mad_u32_u24 v39, v39, %[mu], %[ad]\n\t"
    "v_mfma_f32_16x16x32_bf16 v[68:71], v[244:247], v[112:115], v[68:71]\n\t"
    "ds_read_b128 v[108:111], %[da] offset:2048\n\t"
    "v_cmp_ge_i32 vcc, %[lf], v39\n\t"
    "s_cmp_eq_u64 vcc, 0\n\t"
    "s_addc_u32 %[lf], %[lf], 0\n\t"
    "v_mfma_f32_16x16x32_bf16 v[72:75], v[240:243], v[116:119], v[72:75]\n\t"
    "v_mfma_f32_16x16x32_bf16 v[76:79], v[244:247], v[116:119], v[76:79]\n\t"
    "v_cmp_ge_i32 vcc, %[lf], v39\n\t"
    "s_cmp_eq_u64 vcc, 0\n\t"
    "s_addc_u32 %[lf], %[lf], 0\n\t"
    "s_waitcnt lgkmcnt(4)\n\t"
    "v_mfma_f32_16x16x32_bf16 v[80:83], v[248:251], v[120:123], v[64:67]\n\t"
    "ds_read_b128 v[112:115], %[ea] offset:4096\n\t"
    "s_min_i32 %[lf], %[lf], %[cp]\n\t"
    "s_add_u32 %[b], %[b], 1\n\t"
    "v_mfma_f32_16x16x32_bf16 v[84:87], v[252:255], v[120:123], v[68:71]\n\t"
    "ds_read_b128 v[116:119], %[ea] offset:6144\n\t"
    "v_mfma_f32_16x16x32_bf16 v[88:91], v[248:251], v[124:127], v[72:75]\n\t"
    "v_mfma_f32_16x16x32_bf16 v[92:95], v[252:255], v[124:127], v[76:79]\n\t"
    "s_add_u32 s38, s38, %[ab]\n\t"
    "s_addc_u32 s39, s39, 0\n\t"
    "s_xor_b32 %[l0], %[l0], 0x10000\n\t"
    "s_cmp_lt_i32 %[b], %[lf]\n\t"
    "s_cbranch_scc1 1b\n\t"
    "s_waitcnt lgkmcnt(0)\n\t"
    "s_nop 4"
    : "=&{v[80:95]}"(pd), [ea] "+&v"(ea), [da] "+&v"(da), [b] "+&s"(b), [lf] "+&s"(lf), "+{s[36:37]}"(p1), "+{s[38:39]}"(p0), [sa] "+&v"(sa), [l1] "+&s"(l1), [l0] "+&s"(l0)
    : [rw] "v"(rw), [ab] "s"(ab), [cp] "s"(cp), [mu] "s"(mu), [ad] "v"(ad), "{v[128:159]}"(W[0]), "{v[160:191]}"(W[1]), "{v[192:223]}"(W[2]), "{v[224:255]}"(W[3]), [av0] "v"(av[0]), [av1] "v"(av[1]), [av2] "v"(av[2]), [av3] "v"(av[3]), [av4] "v"(av[4]), [av5] "v"(av[5]), [av6] "v"(av[6]), [av7] "v"(av[7])
    : "v39", "v64", "v65", "v66", "v67", "v68", "v69", "v70", "v71", "v72", "v73", "v74", "v75", "v76", "v77", "v78", "v79", "v96", "v97", "v98", "v99", "v100", "v101", "v102", "v103", "v104", "v105", "v106", "v107", "v108", "v109", "v110", "v111", "v112", "v113", "v114", "v115", "v116", "v117", "v118", "v119", "v120", "v121", "v122", "v123", "v124", "v125", "v126", "v127", "vcc", "scc", "m0", "memory");
}
__device__ __forceinline__ void near_ld2(const u32x32 (&W)[4], u32x16& pd, unsigned ea, unsigned da, int& b, int& lf, int cp, unsigned sa, unsigned mu, unsigned ad,
    unsigned rw, unsigned ab, const char* p1, const char* p0, unsigned l1, unsigned l0, const unsigned (&av)[8]) {
  asm volatile("s_setprio 0\n\t"
    "s_waitcnt vmcnt(2)\n\t"
    "s_barrier\n\t"
    "ds_read_b128 v[96:99], %[ea] offset:0\n\t"
    "ds_read_b128 v[100:103], %[ea] offset:2048\n\t"
    "ds_read_b128 v[104:107], %[da] offset:0\n\t"
    "ds_read_b128 v[108:111], %[da] offset:2048\n\t"
    "ds_read_b128 v[112:115], %[ea] offset:4096\n\t"
    "ds_read_b128 v[116:119], %[ea] offset:6144\n\t"
    "s_branch 2f\n\t"
    "1:\n\t"
    "s_waitcnt vmcnt(2)\n\t"
    "s_nop 1\n\t"
    "s_barrier\n\t"
    "2:\n\t"
    "s_waitcnt lgkmcnt(4)\n\t"
    "v_mfma_f32_16x16x32_bf16 v[64:67], v[128:131], v[96:99], 0\n\t"
    "ds_read_b128 v[120:123], %[da] offset:4096\n\t"
    "s_add_u32 m0, %[l1], 0\n\t"
    "ds_write_b128 %[rw], v[80:83] offset:0\n\t"
    "v_mfma_f32_16x16x32_bf16 v[68:71], v[132:135], v[96:99], 0\n\t"
    "ds_read_b128 v[124:127], %[da] offset:6144\n\t"
    "global_load_lds_dwordx4 %[av0], s[36:37] sc1\n\t"
    "ds_write_b128 %[rw], v[84:87] offset:1024\n\t"
    "v_mfma_f32_16x16x32_bf16 v[72:75], v[128:131], v[100:103], 0\n\t"
    "ds_write_b128 %[rw], v[88:91] offset:2048\n\t"
    "v_mfma_f32_16x16x32_bf16 v[76:79], v[132:135], v[100:103], 0\n\t"
    "ds_write_b128 %[rw], v[92:95] offset:3072\n\t"
    "s_waitcnt lgkmcnt(8)\n\t"
    "v_mfma_f32_16x16x32_bf16 v[64:67], v[136:139], v[104:107], v[64:67]\n\t"
    "ds_read_b128 v[96:99], %[ea] offset:8192\n\t"
    "s_add_u32 m0, %[l1], 4096\n\t"
    "v_mfma_f32_16x16x32_bf16 v[68:71], v[140:143], v[104:107], v[68:71]\n\t"
    "ds_read_b128 v[100:103], %[ea] offset:10240\n\t"
    "global_load_lds_dwordx4 %[av2], s[36:37] sc1\n\t"
    "v_mfma_f32_16x16x32_bf16 v[72:75], v[136:139], v[108:111], v[72:75]\n\t"
    "v_mfma_f32_16x16x32_bf16 v[76:79], v[140:143], v[108:111], v[76:79]\n\t"
    "s_waitcnt lgkmcnt(2)\n\t"
    "v_mfma_f32_16x16x32_bf16 v[64:67], v[144:147], v[112:115], v[64:67]\n\t"
    "ds_read_b128 v[104:107], %[da] offset:8192\n\t"
    "s_add_u32 m0, %[l1], 8192\n\t"
    "v_mfma_f32_16x16x32_bf16 v[68:71], v[148:151], v[112:115], v[68:71]\n\t"
    "ds_read_b128 v[108:111], %[da] offset:10240\n\t"
    "global_load_lds_dwordx4 %[av4], s[36:37] sc1\n\t"
    "v_mfma_f32_16x16x32_bf16 v[72:75], v[144:147], v[116:119], v[72:75]\n\t"
    "v_mfma_f32_16x16x32_bf16 v[76:79], v[148:151], v[116:119], v[76:79]\n\t"
    "s_waitcnt vmcnt(3)\n\t"
    "s_barrier\n\t"
    "v_mfma_f32_16x16x32_bf16 v[64:67], v[152:155], v[120:123], v[64:67]\n\t"
    "ds_read_b128 v[112:115], %[ea] offset:12288\n\t"
    "s_add_u32 m0, %[l1], 12288\n\t"
    "v_mfma_f32_16x16x32_bf16 v[68:71], v[156:159], v[120:123], v[68:71]\n\t"
    "ds_read_b128 v[116:119], %[ea] offset:14336\n\t"
    "global_load_lds_dwordx4 %[av6], s[36:37] sc1\n\t"
    "v_mfma_f32_16x16x32_bf16 v[72:75], v[152:155], v[124:127], v[72:75]\n\t"
    "v_mfma_f32_16x16x32_bf16 v[76:79], v[156:159], v[124:127], v[76:79]\n\t"
    "s_waitcnt lgkmcnt(4)\n\t"
    "v_mfma_f32_16x16x32_bf16 v[64:67], v[160:163], v[96:99], v[64:67]\n\t"
    "ds_read_b128 v[120:123], %[da] offset:12288\n\t"
    "s_add_u32 m0, %[l0], 0\n\t"
    "v_mfma_f32_16x16x32_bf16 v[68:71], v[164:167], v[96:99], v[68:71]\n\t"
    "ds_read_b128 v[124:127], %[da] offset:14336\n\t"
    "global_load_lds_dwordx4 %[av0], s[38:39] sc1\n\t"
    "v_mfma_f32_16x16x32_bf16 v[72:75], v[160:163], v[100:103], v[72:75]\n\t"
    "v_mfma_f32_16x16x32_bf16 v[76:79], v[164:167], v[100:103], v[76:79]\n\t"
    "s_waitcnt lgkmcnt(4)\n\t"
    "v_mfma_f32_16x16x32_bf16 v[64:67], v[168:171], v[104:107], v[64:67]\n\t"
    "ds_read_b128 v[96:99], %[ea] offset:16384\n\t"
    "s_add_u32 m0, %[l0], 4096\n\t"
    "v_mfma_f32_16x16x32_bf16 v[68:71], v[172:175], v[104:107], v[68:71]\n\t"
    "ds_read_b128 v[100:103], %[ea] offset:18432\n\t"
    "global_load_lds_dwordx4 %[av2], s[38:39] sc1\n\t"
    "v_mfma_f32_16x16x32_bf16 v[72:75], v[168:171], v[108:111], v[72:75]\n\t"
    "v_mfma_f32_16x16x32_bf16 v[76:79], v[172:175], v[108:111], v[76:79]\n\t"
    "s_waitcnt lgkmcnt(4)\n\t"
    "v_mfma_f32_16x16x32_bf16 v[64:67], v[176:179], v[112:115], v[64:67]\n\t"
    "ds_read_b128 v[104:107], %[da] offset:16384\n\t"
    "s_add_u32 m0, %[l0], 8192\n\t"
    "v_mfma_f32_16x16x32_bf16 v[68:71], v[180:183], v[112:115], v[68:71]\n\t"
    "ds_read_b128 v[108:111], %[da] offset:18432\n\t"
    "global_load_lds_dwordx4 %[av4], s[38:39] sc1\n\t"
    "v_mfma_f32_16x16x32_bf16 v[72:75], v[176:179], v[116:119], v[72:75]\n\t"
    "v_mfma_f32_16x16x32_bf16 v[76:79], v[180:183], v[116:119], v[76:79]\n\t"
    "s_waitcnt lgkmcnt(4)\n\t"
    "v_mfma_f32_16x16x32_bf16 v[64:67], v[184:187], v[120:123], v[64:67]\n\t"
    "ds_read_b128 v[112:115], %[ea] offset:20480\n\t"
    "s_add_u32 m0, %[l0], 12288\n\t"
    "v_mfma_f32_16x16x32_bf16 v[68:71], v[188:191], v[120:123], v[68:71]\n\t"
    "ds_read_b128 v[116:119], %[ea] offset:22528\n\t"
    "global_load_lds_dwordx4 %[av6], s[38:39] sc1\n\t"
    "v_mfma_f32_16x16x32_bf16 v[72:75], v[184:187], v[124:127], v[72:75]\n\t"
    "v_mfma_f32_16x16x32_bf16 v[76:79], v[188:191], v[124:127], v[76:79]\n\t"
    "s_waitcnt lgkmcnt(4)\n\t"
    "v_mfma_f32_16x16x32_bf16 v[64:67], v[192:195], v[96:99], v[64:67]\n\t"
    "ds_read_b128 v[120:123], %[da] offset:20480\n\t"
    "v_mfma_f32_16x16x32_bf16 v[68:71], v[196:199], v[96:99], v[68:71]\n\t"
    "ds_read_b128 v[124:127], %[da] offset:22528\n\t"
    "v_mfma_f32_16x16x32_bf16 v[72:75], v[192:195], v[100:103], v[72:75]\n\t"
    "v_mfma_f32_16x16x32_bf16 v[76:79], v[196:199], v[100:103], v[76:79]\n\t"
    "s_waitcnt lgkmcnt(4)\n\t"
    "v_mfma_f32_16x16x32_bf16 v[64:67], v[200:203], v[104:107], v[64:67]\n\t"
    "ds_read_b128 v[96:99], %[ea] offset:24576\n\t"
    "v_mfma_f32_16x16x32_bf16 v[68:71], v[204:207], v[104:107], v[68:71]\n\t"
    "ds_read_b128 v[100:103], %[ea] offset:26624\n\t"
    "v_mfma_f32_16x16x32_bf16 v[72:75], v[200:203], v[108:111], v[72:75]\n\t"
    "v_mfma_f32_16x16x32_bf16 v[76:79], v[204:207], v[108:111], v[76:79]\n\t"
    "s_add_u32 s36, s36, %[ab]\n\t"
    "s_addc_u32 s37, s37, 0\n\t"
    "s_waitcnt lgkmcnt(4)\n\t"
    "v_mfma_f32_16x16x32_bf16 v[64:67], v[208:211], v[112:115], v[64:67]\n\t"
    "ds_read_b128 v[104:107], %[da] offset:24576\n\t"
    "v_mfma_f32_16x16x32_bf16 v[68:71], v[212:215], v[112:115], v[68:71]\n\t"
    "ds_read_b128 v[108:111], %[da] offset:26624\n\t"
    "v_mfma_f32_16x16x32_bf16 v[72:75], v[208:211], v[116:119], v[72:75]\n\t"
    "v_mfma_f32_16x16x32_bf16 v[76:79], v[212:215], v[116:119], v[76:79]\n\t"
    "s_xor_b32 %[l1], %[l1], 0x10000\n\t"
    "s_waitcnt lgkmcnt(4)\n\t"
    "v_mfma_f32_16x16x32_bf16 v[64:67], v[216:219], v[120:123], v[64:67]\n\t"
    "ds_read_b128 v[112:115], %[ea] offset:28672\n\t"
    "v_mfma_f32_16x16x32_bf16 v[68:71], v[220:223], v[120:123], v[68:71]\n\t"
    "ds_read_b128 v[116:119], %[ea] offset:30720\n\t"
    "v_mfma_f32_16x16x32_bf16 v[72:75], v[216:219], v[124:127], v[72:75]\n\t"
    "v_xor_b32 %[ea], 0x10000, %[ea]\n\t"
    "v_mfma_f32_16x16x32_bf16 v[76:79], v[220:223], v[124:127], v[76:79]\n\t"
    "s_waitcnt vmcnt(6)\n\t"
    "s_barrier\n\t"
    "s_waitcnt lgkmcnt(4)\n\t"
    "v_mfma_f32_16x16x32_bf16 v[64:67], v[224:227], v[96:99], v[64:67]\n\t"
    "ds_read_b32 v39, %[sa]\n\t"
    "ds_read_b128 v[120:123], %[da] offset:28672\n\t"
    "v_mfma_f32_16x16x32_bf16 v[68:71], v[228:231], v[96:99], v[68:71]\n\t"
    "ds_read_b128 v[124:127], %[da] offset:30720\n\t"
    "v_mfma_f32_16x16x32_bf16 v[72:75], v[224:227], v[100:103], v[72:75]\n\t"
    "v_xor_b32 %[da], 0x10000, %[da]\n\t"
    "v_mfma_f32_16x16x32_bf16 v[76:79], v[228:231], v[100:103], v[76:79]\n\t"
    "s_waitcnt lgkmcnt(5)\n\t"
    "v_mfma_f32_16x16x32_bf16 v[64:67], v[232:235], v[104:107], v[64:67]\n\t"
    "ds_read_b128 v[96:99], %[ea] offset:0\n\t"
    "v_mfma_f32_16x16x32_bf16 v[68:71], v[236:239], v[104:107], v[68:71]\n\t"
    "ds_read_b128 v[100:103], %[ea] offset:2048\n\t"
    "v_mfma_f32_16x16x32_bf16 v[72:75], v[232:235], v[108:111], v[72:75]\n\t"
    "v_mfma_f32_16x16x32_bf16 v[76:79], v[236:239], v[108:111], v[76:79]\n\t"
    "s_waitcnt lgkmcnt(4)\n\t"
    "v_mfma_f32_16x16x32_bf16 v[64:67], v[240:243], v[112:115], v[64:67]\n\t"
    "ds_read_b128 v[104:107], %[da] offset:0\n\t"
    "v_mad_u32_u24 v39, v39, %[mu], %[ad]\n\t"
    "v_mfma_f32_16x16x32_bf16 v[68:71], v[244:247], v[112:115], v[68:71]\n\t"
    "ds_read_b128 v[108:111], %[da] offset:2048\n\t"
    "v_cmp_ge_i32 vcc, %[lf], v39\n\t"
    "s_cmp_eq_u64 vcc, 0\n\t"
    "s_addc_u32 %[lf], %[lf], 0\n\t"
    "v_mfma_f32_16x16x32_bf16 v[72:75], v[240:243], v[116:119], v[72:75]\n\t"
    "v_mfma_f32_16x16x32_bf16 v[76:79], v[244:247], v[116:119], v[76:79]\n\t"
    "v_cmp_ge_i32 vcc, %[lf], v39\n\t"
    "s_cmp_eq_u64 vcc, 0\n\t"
    "s_addc_u32 %[lf], %[lf], 0\n\t"
    "s_waitcnt lgkmcnt(4)\n\t"
    "v_mfma_f32_16x16x32_bf16 v[80:83], v[248:251], v[120:123], v[64:67]\n\t"
    "ds_read_b128 v[112:115], %[ea] offset:4096\n\t"
    "s_min_i32 %[lf], %[lf], %[cp]\n\t"
    "s_add_u32 %[b], %[b], 1\n\t"
    "v_mfma_f32_16x16x32_bf16 v[84:87], v[252:255], v[120:123], v[68:71]\n\t"
    "ds_read_b128 v[116:119], %[ea] offset:6144\n\t"
    "v_mfma_f32_16x16x32_bf16 v[88:91], v[248:251], v[124:127], v[72:75]\n\t"
    "v_mfma_f32_16x16x32_bf16 v[92:95], v[252:255], v[124:127], v[76:79]\n\t"
    "s_add_u32 s38, s38, %[ab]\n\t"
    "s_addc_u32 s39, s39, 0\n\t"
    "s_xor_b32 %[l0], %[l0], 0x10000\n\t"
    "s_cmp_lt_i32 %[b], %[lf]\n\t"
    "s_cbranch_scc1 1b\n\t"
    "s_waitcnt lgkmcnt(0)\n\t"
    "s_nop 4"
    : "=&{v[80:95]}"(pd), [ea] "+&v"(ea), [da] "+&v"(da), [b] "+&s"(b), [lf] "+&s"(lf), "+{s[36:37]}"(p1), "+{s[38:39]}"(p0), [sa] "+&v"(sa), [l1] "+&s"(l1), [l0] "+&s"(l0)
    : [rw] "v"(rw), [ab] "s"(ab), [cp] "s"(cp), [mu] "s"(mu), [ad] "v"(ad), "{v[128:159]}"(W[0]), "{v[160:191]}"(W[1]), "{v[192:223]}"(W[2]), "{v[224:255]}"(W[3]), [av0] "v"(av[0]), [av1] "v"(av[1]), [av2] "v"(av[2]), [av3] "v"(av[3]), [av4] "v"(av[4]), [av5] "v"(av[5]), [av6] "v"(av[6]), [av7] "v"(av[7])
    : "v39", "v64", "v65", "v66", "v67", "v68", "v69", "v70", "v71", "v72", "v73", "v74", "v75", "v76", "v77", "v78", "v79", "v96", "v97", "v98", "v99", "v100", "v101", "v102", "v103", "v104", "v105", "v106", "v107", "v108", "v109", "v110", "v111", "v112", "v113", "v114", "v115", "v116", "v117", "v118", "v119", "v120", "v121", "v122", "v123", "v124", "v125", "v126", "v127", "vcc", "scc", "m0", "memory");
}
__device__ __forceinline__ void near_st0(const u32x32 (&W)[4], u32x16& pd, unsigned ea, unsigned da, int& b, int& lf, int cp, unsigned sa, unsigned mu, unsigned ad,
    unsigned rw, unsigned ab, const char* p1, const char* p0, unsigned co0, unsigned co1, int po, int pf, unsigned w0, unsigned sl, unsigned dv, const char* db, const char* pg) {
  unsigned st;
  asm volatile("s_waitcnt vmcnt(0)\n\t"
    "s_barrier\n\t"
    "ds_read_b128 v[96:99], %[ea] offset:0\n\t"
    "ds_read_b128 v[100:103], %[ea] offset:2048\n\t"
    "ds_read_b128 v[104:107], %[da] offset:0\n\t"
    "ds_read_b128 v[108:111], %[da] offset:2048\n\t"
    "ds_read_b128 v[112:115], %[ea] offset:4096\n\t"
    "ds_read_b128 v[116:119], %[ea] offset:6144\n\t"
    "s_branch 2f\n\t"
    "1:\n\t"
    "s_waitcnt vmcnt(0)\n\t"
    "s_barrier\n\t"
    "2:\n\t"
    "s_waitcnt lgkmcnt(4)\n\t"
    "v_mfma_f32_16x16x32_bf16 v[64:67], v[128:131], v[96:99], 0\n\t"
    "ds_read_b128 v[120:123], %[da] offset:4096\n\t"
    "v_mfma_f32_16x16x32_bf16 v[68:71], v[132:135], v[96:99], 0\n\t"
    "ds_read_b128 v[124:127], %[da] offset:6144\n\t"
    "v_mfma_f32_16x16x32_bf16 v[72:75], v[128:131], v[100:103], 0\n\t"
    "v_mfma_f32_16x16x32_bf16 v[76:79], v[132:135], v[100:103], 0\n\t"
    "s_cmp_eq_u32 %[w0], 0\n\t"
    "s_cbranch_scc1 5f\n\t"
    "6:\n\t"
    "s_waitcnt lgkmcnt(4)\n\t"
    "v_mfma_f32_16x16x32_bf16 v[64:67], v[136:139], v[104:107], v[64:67]\n\t"
    "ds_read_b128 v[96:99], %[ea] offset:8192\n\t"
    "v_mfma_f32_16x16x32_bf16 v[68:71], v[140:143], v[104:107], v[68:71]\n\t"
    "ds_read_b128 v[100:103], %[ea] offset:10240\n\t"
    "v_mfma_f32_16x16x32_bf16 v[72:75], v[136:139], v[108:111], v[72:75]\n\t"
    "v_mfma_f32_16x16x32_bf16 v[76:79], v[140:143], v[108:111], v[76:79]\n\t"
    "s_waitcnt lgkmcnt(4)\n\t"
    "v_mfma_f32_16x16x32_bf16 v[64:67], v[144:147], v[112:115], v[64:67]\n\t"
    "ds_read_b128 v[104:107], %[da] offset:8192\n\t"
    "v_mfma_f32_16x16x32_bf16 v[68:71], v[148:151], v[112:115], v[68:71]\n\t"
    "ds_read_b128 v[108:111], %[da] offset:10240\n\t"
    "v_mfma_f32_16x16x32_bf16 v[72:75], v[144:147], v[116:119], v[72:75]\n\t"
    "v_mfma_f32_16x16x32_bf16 v[76:79], v[148:151], v[116:119], v[76:79]\n\t"
    "s_barrier\n\t"
    "s_waitcnt lgkmcnt(4)\n\t"
    "v_mfma_f32_16x16x32_bf16 v[64:67], v[152:155], v[120:123], v[64:67]\n\t"
    "ds_read_b128 v[112:115], %[ea] offset:12288\n\t"
    "v_mfma_f32_16x16x32_bf16 v[68:71], v[156:159], v[120:123], v[68:71]\n\t"
    "ds_read_b128 v[116:119], %[ea] offset:14336\n\t"
    "v_mfma_f32_16x16x32_bf16 v[72:75], v[152:155], v[124:127], v[72:75]\n\t"
    "ds_read_b128 v[48:51], %[rw] offset:0\n\t"
    "v_mfma_f32_16x16x32_bf16 v[76:79], v[156:159], v[124:127], v[76:79]\n\t"
    "ds_read_b128 v[52:55], %[rw] offset:1024\n\t"
    "s_waitcnt lgkmcnt(6)\n\t"
    "v_mfma_f32_16x16x32_bf16 v[64:67], v[160:163], v[96:99], v[64:67]\n\t"
    "ds_read_b128 v[120:123], %[da] offset:12288\n\t"
    "v_mfma_f32_16x16x32_bf16 v[68:71], v[164:167], v[96:99], v[68:71]\n\t"
    "ds_read_b128 v[124:127], %[da] offset:14336\n\t"
    "v_mfma_f32_16x16x32_bf16 v[72:75], v[160:163], v[100:103], v[72:75]\n\t"
    "ds_read_b128 v[56:59], %[rw] offset:2048\n\t"
    "v_mfma_f32_16x16x32_bf16 v[76:79], v[164:167], v[100:103], v[76:79]\n\t"
    "ds_read_b128 v[60:63], %[rw] offset:3072\n\t"
    "s_waitcnt lgkmcnt(0)\n\t"
    "v_mfma_f32_16x16x32_bf16 v[64:67], v[168:171], v[104:107], v[64:67]\n\t"
    "ds_read_b128 v[96:99], %[ea] offset:16384\n\t"
    "v_add_f32 v48, v48, v80\n\t"
    "v_add_f32 v49, v49, v81\n\t"
    "v_mfma_f32_16x16x32_bf16 v[68:71], v[172:175], v[104:107], v[68:71]\n\t"
    "ds_read_b128 v[100:103], %[ea] offset:18432\n\t"
    "v_add_f32 v50, v50, v82\n\t"
    "v_mfma_f32_16x16x32_bf16 v[72:75], v[168:171], v[108:111], v[72:75]\n\t"
    "v_add_f32 v51, v51, v83\n\t"
    "v_mfma_f32_16x16x32_bf16 v[76:79], v[172:175], v[108:111], v[76:79]\n\t"
    "v_add_f32 v52, v52, v84\n\t"
    "v_add_f32 v53, v53, v85\n\t"
    "v_mfma_f32_16x16x32_bf16 v[64:67], v[176:179], v[112:115], v[64:67]\n\t"
    "ds_read_b128 v[104:107], %[da] offset:16384\n\t"
    "v_add_f32 v54, v54, v86\n\t"
    "v_mfma_f32_16x16x32_bf16 v[68:71], v[180:183], v[112:115], v[68:71]\n\t"
    "ds_read_b128 v[108:111], %[da] offset:18432\n\t"
    "v_add_f32 v55, v55, v87\n\t"
    "v_mfma_f32_16x16x32_bf16 v[72:75], v[176:179], v[116:119], v[72:75]\n\t"
    "v_add_f32 v56, v56, v88\n\t"
    "v_add_f32 v57, v57, v89\n\t"
    "v_mfma_f32_16x16x32_bf16 v[76:79], v[180:183], v[116:119], v[76:79]\n\t"
    "v_add_f32 v58, v58, v90\n\t"
    "v_mfma_f32_16x16x32_bf16 v[64:67], v[184:187], v[120:123], v[64:67]\n\t"
    "ds_read_b128 v[112:115], %[ea] offset:20480\n\t"
    "v_add_f32 v59, v59, v91\n\t"
    "v_mfma_f32_16x16x32_bf16 v[68:71], v[188:191], v[120:123], v[68:71]\n\t"
    "ds_read_b128 v[116:119], %[ea] offset:22528\n\t"
    "v_add_f32 v60, v60, v92\n\t"
    "v_mfma_f32_16x16x32_bf16 v[72:75], v[184:187], v[124:127], v[72:75]\n\t"
    "v_add_f32 v61, v61, v93\n\t"
    "v_add_f32 v62, v62, v94\n\t"
    "v_mfma_f32_16x16x32_bf16 v[76:79], v[188:191], v[124:127], v[76:79]\n\t"
    "v_add_f32 v63, v63, v95\n\t"
    "s_waitcnt lgkmcnt(4)\n\t"
    "v_mfma_f32_16x16x32_bf16 v[64:67], v[192:195], v[96:99], v[64:67]\n\t"
    "ds_read_b128 v[120:123], %[da] offset:20480\n\t"
    "v_cvt_pk_bf16_f32 v40, v48, v49\n\t"
    "v_mfma_f32_16x16x32_bf16 v[68:71], v[196:199], v[96:99], v[68:71]\n\t"
    "ds_read_b128 v[124:127], %[da] offset:22528\n\t"
    "v_cvt_pk_bf16_f32 v41, v50, v51\n\t"
    "v_cvt_pk_bf16_f32 v42, v52, v53\n\t"
    "v_mfma_f32_16x16x32_bf16 v[72:75], v[192:195], v[100:103], v[72:75]\n\t"
    "v_cvt_pk_bf16_f32 v43, v54, v55\n\t"
    "v_mfma_f32_16x16x32_bf16 v[76:79], v[196:199], v[100:103], v[76:79]\n\t"
    "global_store_dwordx4 %[co0], v[40:43], s[36:37] sc1\n\t"
    "s_waitcnt lgkmcnt(4)\n\t"
    "v_mfma_f32_16x16x32_bf16 v[64:67], v[200:203], v[104:107], v[64:67]\n\t"
    "ds_read_b128 v[96:99], %[ea] offset:24576\n\t"
    "v_cvt_pk_bf16_f32 v44, v56, v57\n\t"
    "v_cvt_pk_bf16_f32 v45, v58, v59\n\t"
    "v_mfma_f32_16x16x32_bf16 v[68:71], v[204:207], v[104:107], v[68:71]\n\t"
    "ds_read_b128 v[100:103], %[ea] offset:26624\n\t"
    "v_cvt_pk_bf16_f32 v46, v60, v61\n\t"
    "v_mfma_f32_16x16x32_bf16 v[72:75], v[200:203], v[108:111], v[72:75]\n\t"
    "v_cvt_pk_bf16_f32 v47, v62, v63\n\t"
    "v_mfma_f32_16x16x32_bf16 v[76:79], v[204:207], v[108:111], v[76:79]\n\t"
    "global_store_dwordx4 %[co1], v[44:47], s[36:37] sc1\n\t"
    "s_waitcnt lgkmcnt(4)\n\t"
    "v_mfma_f32_16x16x32_bf16 v[64:67], v[208:211], v[112:115], v[64:67]\n\t"
    "ds_read_b128 v[104:107], %[da] offset:24576\n\t"
    "v_mfma_f32_16x16x32_bf16 v[68:71], v[212:215], v[112:115], v[68:71]\n\t"
    "ds_read_b128 v[108:111], %[da] offset:26624\n\t"
    "v_mfma_f32_16x16x32_bf16 v[72:75], v[208:211], v[116:119], v[72:75]\n\t"
    "v_mfma_f32_16x16x32_bf16 v[76:79], v[212:215], v[116:119], v[76:79]\n\t"
    "s_waitcnt lgkmcnt(4)\n\t"
    "v_mfma_f32_16x16x32_bf16 v[64:67], v[216:219], v[120:123], v[64:67]\n\t"
    "ds_read_b128 v[112:115], %[ea] offset:28672\n\t"
    "v_mfma_f32_16x16x32_bf16 v[68:71], v[220:223], v[120:123], v[68:71]\n\t"
    "ds_read_b128 v[116:119], %[ea] offset:30720\n\t"
    "v_mfma_f32_16x16x32_bf16 v[72:75], v[216:219], v[124:127], v[72:75]\n\t"
    "v_xor_b32 %[ea], 0x10000, %[ea]\n\t"
    "v_mfma_f32_16x16x32_bf16 v[76:79], v[220:223], v[124:127], v[76:79]\n\t"
    "s_waitcnt vmcnt(2)\n\t"
    "s_barrier\n\t"
    "s_waitcnt lgkmcnt(4)\n\t"
    "v_mfma_f32_16x16x32_bf16 v[64:67], v[224:227], v[96:99], v[64:67]\n\t"
    "ds_read_b32 v39, %[sa]\n\t"
    "ds_read_b128 v[120:123], %[da] offset:28672\n\t"
    "v_mfma_f32_16x16x32_bf16 v[68:71], v[228:231], v[96:99], v[68:71]\n\t"
    "ds_read_b128 v[124:127], %[da] offset:30720\n\t"
    "v_mfma_f32_16x16x32_bf16 v[72:75], v[224:227], v[100:103], v[72:75]\n\t"
    "v_xor_b32 %[da], 0x10000, %[da]\n\t"
    "v_mfma_f32_16x16x32_bf16 v[76:79], v[228:231], v[100:103], v[76:79]\n\t"
    "s_waitcnt lgkmcnt(5)\n\t"
    "v_mfma_f32_16x16x32_bf16 v[64:67], v[232:235], v[104:107], v[64:67]\n\t"
    "ds_read_b128 v[96:99], %[ea] offset:0\n\t"
    "v_mfma_f32_16x16x32_bf16 v[68:71], v[236:239], v[104:107], v[68:71]\n\t"
    "ds_read_b128 v[100:103], %[ea] offset:2048\n\t"
    "v_mfma_f32_16x16x32_bf16 v[72:75], v[232:235], v[108:111], v[72:75]\n\t"
    "v_mfma_f32_16x16x32_bf16 v[76:79], v[236:239], v[108:111], v[76:79]\n\t"
    "s_waitcnt lgkmcnt(4)\n\t"
    "v_mfma_f32_16x16x32_bf16 v[64:67], v[240:243], v[112:115], v[64:67]\n\t"
    "ds_read_b128 v[104:107], %[da] offset:0\n\t"
    "v_mad_u32_u24 v39, v39, %[mu], %[ad]\n\t"
    "v_mfma_f32_16x16x32_bf16 v[68:71], v[244:247], v[112:115], v[68:71]\n\t"
    "ds_read_b128 v[108:111], %[da] offset:2048\n\t"
    "v_cmp_ge_i32 vcc, %[lf], v39\n\t"
    "s_cmp_eq_u64 vcc, 0\n\t"
    "s_addc_u32 %[lf], %[lf], 0\n\t"
    "v_mfma_f32_16x16x32_bf16 v[72:75], v[240:243], v[116:119], v[72:75]\n\t"
    "v_mfma_f32_16x16x32_bf16 v[76:79], v[244:247], v[116:119], v[76:79]\n\t"
    "v_cmp_ge_i32 vcc, %[lf], v39\n\t"
    "s_cmp_eq_u64 vcc, 0\n\t"
    "s_addc_u32 %[lf], %[lf], 0\n\t"
    "s_waitcnt lgkmcnt(4)\n\t"
    "v_mfma_f32_16x16x32_bf16 v[80:83], v[248:251], v[120:123], v[64:67]\n\t"
    "ds_read_b128 v[112:115], %[ea] offset:4096\n\t"
    "s_min_i32 %[lf], %[lf], %[cp]\n\t"
    "s_add_u32 %[b], %[b], 1\n\t"
    "v_mfma_f32_16x16x32_bf16 v[84:87], v[252:255], v[120:123], v[68:71]\n\t"
    "ds_read_b128 v[116:119], %[ea] offset:6144\n\t"
    "v_mfma_f32_16x16x32_bf16 v[88:91], v[248:251], v[124:127], v[72:75]\n\t"
    "v_mfma_f32_16x16x32_bf16 v[92:95], v[252:255], v[124:127], v[76:79]\n\t"
    "s_mov_b64 s[36:37], s[38:39]\n\t"
    "s_add_u32 s38, s38, %[ab]\n\t"
    "s_addc_u32 s39, s39, 0\n\t"
    "s_cmp_lt_i32 %[b], %[lf]\n\t"
    "s_cbranch_scc1 1b\n\t"
    "s_waitcnt lgkmcnt(0)\n\t"
    "s_nop 4\n\t"
    "s_branch 9f\n\t"
    "5:\n\t"
    "s_add_u32 %[st], %[b], %[po]\n\t"
    "s_max_i32 %[st], %[st], %[p0]\n\t"
    "v_mov_b32 v39, %[st]\n\t"
    "v_mov_b32 v38, 0\n\t"
    "s_mov_b64 exec, 1\n\t"
    "global_store_dword v38, v39, %[pg] sc1\n\t"
    "s_mov_b64 exec, -1\n\t"
    "s_mov_b32 %[p0], %[p1]\n\t"
    "s_cmp_ge_i32 %[lf], %[cp]\n\t"
    "s_cbranch_scc1 6b\n\t"
    "s_mov_b32 m0, %[sl]\n\t"
    "s_nop 0\n\t"
    "global_load_lds_dword %[dv], %[db] sc1\n\t"
    "s_branch 6b\n\t"
    "9:"
    : "=&{v[80:95]}"(pd), [ea] "+&v"(ea), [da] "+&v"(da), [b] "+&s"(b), [lf] "+&s"(lf), "+{s[36:37]}"(p1), "+{s[38:39]}"(p0), [sa] "+&v"(sa), [st] "=&s"(st), [sl] "+&s"(sl), [p0] "+&s"(pf)
    : [rw] "v"(rw), [ab] "s"(ab), [cp] "s"(cp), [mu] "s"(mu), [ad] "v"(ad), "{v[128:159]}"(W[0]), "{v[160:191]}"(W[1]), "{v[192:223]}"(W[2]), "{v[224:255]}"(W[3]), [co0] "v"(co0), [w0] "s"(w0), [dv] "v"(dv), [db] "s"(db), [pg] "s"(pg), [po] "s"(po - 1), [p1] "s"(b + po), [co1] "v"(co1)
    : "v38", "v39", "v40", "v41", "v42", "v43", "v44", "v45", "v46", "v47", "v48", "v49", "v50", "v51", "v52", "v53", "v54", "v55", "v56", "v57", "v58", "v59", "v60", "v61", "v62", "v63", "v64", "v65", "v66", "v67", "v68", "v69", "v70", "v71", "v72", "v73", "v74", "v75", "v76", "v77", "v78", "v79", "v96", "v97", "v98", "v99", "v100", "v101", "v102", "v103", "v104", "v105", "v106", "v107", "v108", "v109", "v110", "v111", "v112", "v113", "v114", "v115", "v116", "v117", "v118", "v119", "v120", "v121", "v122", "v123", "v124", "v125", "v126", "v127", "vcc", "scc", "m0", "memory");
}
__device__ __forceinline__ void near_st1(const u32x32 (&W)[4], u32x16& pd, unsigned ea, unsigned da, int& b, int& lf, int cp, unsigned sa, unsigned mu, unsigned ad,
    unsigned rw, unsigned ab, const char* p1, const char* p0, unsigned co0, unsigned co1, int po, int pf, unsigned w0, unsigned sl, unsigned dv, const char* db, const char* pg) {
  unsigned st;
  asm volatile("s_waitcnt vmcnt(0)\n\t"
    "s_barrier\n\t"
    "ds_read_b128 v[96:99], %[ea] offset:0\n\t"
    "ds_read_b128 v[100:103], %[ea] offset:2048\n\t"
    "ds_read_b128 v[104:107], %[da] offset:0\n\t"
    "ds_read_b128 v[108:111], %[da] offset:2048\n\t"
    "ds_read_b128 v[112:115], %[ea] offset:4096\n\t"
    "ds_read_b128 v[116:119], %[ea] offset:6144\n\t"
    "s_branch 2f\n\t"
    "1:\n\t"
    "s_waitcnt vmcnt(0)\n\t"
    "s_barrier\n\t"
    "2:\n\t"
    "s_waitcnt lgkmcnt(4)\n\t"
    "v_mfma_f32_16x16x32_bf16 v[64:67], v[128:131], v[96:99], 0\n\t"
    "ds_read_b128 v[120:123], %[da] offset:4096\n\t"
    "v_mfma_f32_16x16x32_bf16 v[68:71], v[132:135], v[96:99], 0\n\t"
    "ds_read_b128 v[124:127], %[da] offset:6144\n\t"
    "v_mfma_f32_16x16x32_bf16 v[72:75], v[128:131], v[100:103], 0\n\t"
    "v_mfma_f32_16x16x32_bf16 v[76:79], v[132:135], v[100:103], 0\n\t"
    "s_cmp_eq_u32 %[w0], 0\n\t"
    "s_cbranch_scc1 5f\n\t"
    "6:\n\t"
    "s_waitcnt lgkmcnt(4)\n\t"
    "v_mfma_f32_16x16x32_bf16 v[64:67], v[136:139], v[104:107], v[64:67]\n\t"
    "ds_read_b128 v[96:99], %[ea] offset:8192\n\t"
    "v_mfma_f32_16x16x32_bf16 v[68:71], v[140:143], v[104:107], v[68:71]\n\t"
    "ds_read_b128 v[100:103], %[ea] offset:10240\n\t"
    "v_mfma_f32_16x16x32_bf16 v[72:75], v[136:139], v[108:111], v[72:75]\n\t"
    "v_mfma_f32_16x16x32_bf16 v[76:79], v[140:143], v[108:111], v[76:79]\n\t"
    "s_waitcnt lgkmcnt(4)\n\t"
    "v_mfma_f32_16x16x32_bf16 v[64:67], v[144:147], v[112:115], v[64:67]\n\t"
    "ds_read_b128 v[104:107], %[da] offset:8192\n\t"
    "v_mfma_f32_16x16x32_bf16 v[68:71], v[148:151], v[112:115], v[68:71]\n\t"
    "ds_read_b128 v[108:111], %[da] offset:10240\n\t"
    "v_mfma_f32_16x16x32_bf16 v[72:75], v[144:147], v[116:119], v[72:75]\n\t"
    "v_mfma_f32_16x16x32_bf16 v[76:79], v[148:151], v[116:119], v[76:79]\n\t"
    "s_barrier\n\t"
    "s_waitcnt lgkmcnt(4)\n\t"
    "v_mfma_f32_16x16x32_bf16 v[64:67], v[152:155], v[120:123], v[64:67]\n\t"
    "ds_read_b128 v[112:115], %[ea] offset:12288\n\t"
    "v_mfma_f32_16x16x32_bf16 v[68:71], v[156:159], v[120:123], v[68:71]\n\t"
    "ds_read_b128 v[116:119], %[ea] offset:14336\n\t"
    "v_mfma_f32_16x16x32_bf16 v[72:75], v[152:155], v[124:127], v[72:75]\n\t"
    "ds_read_b128 v[48:51], %[rw] offset:0\n\t"
    "v_mfma_f32_16x16x32_bf16 v[76:79], v[156:159], v[124:127], v[76:79]\n\t"
    "ds_read_b128 v[52:55], %[rw] offset:1024\n\t"
    "s_waitcnt lgkmcnt(6)\n\t"
    "v_mfma_f32_16x16x32_bf16 v[64:67], v[160:163], v[96:99], v[64:67]\n\t"
    "ds_read_b128 v[120:123], %[da] offset:12288\n\t"
    "v_mfma_f32_16x16x32_bf16 v[68:71], v[164:167], v[96:99], v[68:71]\n\t"
    "ds_read_b128 v[124:127], %[da] offset:14336\n\t"
    "v_mfma_f32_16x16x32_bf16 v[72:75], v[160:163], v[100:103], v[72:75]\n\t"
    "ds_read_b128 v[56:59], %[rw] offset:2048\n\t"
    "v_mfma_f32_16x16x32_bf16 v[76:79], v[164:167], v[100:103], v[76:79]\n\t"
    "ds_read_b128 v[60:63], %[rw] offset:3072\n\t"
    "s_waitcnt lgkmcnt(0)\n\t"
    "v_mfma_f32_16x16x32_bf16 v[64:67], v[168:171], v[104:107], v[64:67]\n\t"
    "ds_read_b128 v[96:99], %[ea] offset:16384\n\t"
    "v_add_f32 v48, v48, v80\n\t"
    "v_add_f32 v49, v49, v81\n\t"
    "v_add_f32 v50, v50, v82\n\t"
    "v_mfma_f32_16x16x32_bf16 v[68:71], v[172:175], v[104:107], v[68:71]\n\t"
    "ds_read_b128 v[100:103], %[ea] offset:18432\n\t"
    "v_add_f32 v51, v51, v83\n\t"
    "v_add_f32 v52, v52, v84\n\t"
    "v_add_f32 v53, v53, v85\n\t"
    "v_mfma_f32_16x16x32_bf16 v[72:75], v[168:171], v[108:111], v[72:75]\n\t"
    "v_add_f32 v54, v54, v86\n\t"
    "v_add_f32 v55, v55, v87\n\t"
    "v_add_f32 v56, v56, v88\n\t"
    "v_mfma_f32_16x16x32_bf16 v[76:79], v[172:175], v[108:111], v[76:79]\n\t"
    "v_add_f32 v57, v57, v89\n\t"
    "v_add_f32 v58, v58, v90\n\t"
    "v_add_f32 v59, v59, v91\n\t"
    "v_mfma_f32_16x16x32_bf16 v[64:67], v[176:179], v[112:115], v[64:67]\n\t"
    "ds_read_b128 v[104:107], %[da] offset:16384\n\t"
    "v_add_f32 v60, v60, v92\n\t"
    "v_add_f32 v61, v61, v93\n\t"
    "v_add_f32 v62, v62, v94\n\t"
    "v_mfma_f32_16x16x32_bf16 v[68:71], v[180:183], v[112:115], v[68:71]\n\t"
    "ds_read_b128 v[108:111], %[da] offset:18432\n\t"
    "v_add_f32 v63, v63, v95\n\t"
    "v_mul_f32 v40, %[nl], v48\n\t"
    "v_mul_f32 v41, %[nl], v49\n\t"
    "v_mfma_f32_16x16x32_bf16 v[72:75], v[176:179], v[116:119], v[72:75]\n\t"
    "v_mul_f32 v42, %[nl], v50\n\t"
    "v_mul_f32 v43, %[nl], v51\n\t"
    "v_mul_f32 v44, %[nl], v56\n\t"
    "v_mfma_f32_16x16x32_bf16 v[76:79], v[180:183], v[116:119], v[76:79]\n\t"
    "v_mul_f32 v45, %[nl], v57\n\t"
    "v_mul_f32 v46, %[nl], v58\n\t"
    "v_mul_f32 v47, %[nl], v59\n\t"
    "v_mfma_f32_16x16x32_bf16 v[64:67], v[184:187], v[120:123], v[64:67]\n\t"
    "ds_read_b128 v[112:115], %[ea] offset:20480\n\t"
    "v_exp_f32 v40, v40\n\t"
    "v_exp_f32 v41, v41\n\t"
    "v_exp_f32 v42, v42\n\t"
    "v_mfma_f32_16x16x32_bf16 v[68:71], v[188:191], v[120:123], v[68:71]\n\t"
    "ds_read_b128 v[116:119], %[ea] offset:22528\n\t"
    "v_exp_f32 v43, v43\n\t"
    "v_exp_f32 v44, v44\n\t"
    "v_exp_f32 v45, v45\n\t"
    "v_mfma_f32_16x16x32_bf16 v[72:75], v[184:187], v[124:127], v[72:75]\n\t"
    "v_exp_f32 v46, v46\n\t"
    "v_exp_f32 v47, v47\n\t"
    "v_add_f32 v40, 1.0, v40\n\t"
    "v_mfma_f32_16x16x32_bf16 v[76:79], v[188:191], v[124:127], v[76:79]\n\t"
    "v_add_f32 v41, 1.0, v41\n\t"
    "v_add_f32 v42, 1.0, v42\n\t"
    "v_add_f32 v43, 1.0, v43\n\t"
    "s_waitcnt lgkmcnt(4)\n\t"
    "v_mfma_f32_16x16x32_bf16 v[64:67], v[192:195], v[96:99], v[64:67]\n\t"
    "ds_read_b128 v[120:123], %[da] offset:20480\n\t"
    "v_add_f32 v44, 1.0, v44\n\t"
    "v_add_f32 v45, 1.0, v45\n\t"
    "v_add_f32 v46, 1.0, v46\n\t"
    "v_mfma_f32_16x16x32_bf16 v[68:71], v[196:199], v[96:99], v[68:71]\n\t"
    "ds_read_b128 v[124:127], %[da] offset:22528\n\t"
    "v_add_f32 v47, 1.0, v47\n\t"
    "v_rcp_f32 v40, v40\n\t"
    "v_rcp_f32 v41, v41\n\t"
    "v_mfma_f32_16x16x32_bf16 v[72:75], v[192:195], v[100:103], v[72:75]\n\t"
    "v_rcp_f32 v42, v42\n\t"
    "v_rcp_f32 v43, v43\n\t"
    "v_rcp_f32 v44, v44\n\t"
    "v_mfma_f32_16x16x32_bf16 v[76:79], v[196:199], v[100:103], v[76:79]\n\t"
    "v_rcp_f32 v45, v45\n\t"
    "v_rcp_f32 v46, v46\n\t"
    "v_rcp_f32 v47, v47\n\t"
    "s_waitcnt lgkmcnt(4)\n\t"
    "v_mfma_f32_16x16x32_bf16 v[64:67], v[200:203], v[104:107], v[64:67]\n\t"
    "ds_read_b128 v[96:99], %[ea] offset:24576\n\t"
    "v_mul_f32 v40, v48, v40\n\t"
    "v_mul_f32 v41, v49, v41\n\t"
    "v_mul_f32 v42, v50, v42\n\t"
    "v_mfma_f32_16x16x32_bf16 v[68:71], v[204:207], v[104:107], v[68:71]\n\t"
    "ds_read_b128 v[100:103], %[ea] offset:26624\n\t"
    "v_mul_f32 v43, v51, v43\n\t"
    "v_mul_f32 v44, v56, v44\n\t"
    "v_mul_f32 v45, v57, v45\n\t"
    "v_mfma_f32_16x16x32_bf16 v[72:75], v[200:203], v[108:111], v[72:75]\n\t"
    "v_mul_f32 v46, v58, v46\n\t"
    "v_mul_f32 v47, v59, v47\n\t"
    "v_mul_f32 v40, v40, v52\n\t"
    "v_mfma_f32_16x16x32_bf16 v[76:79], v[204:207], v[108:111], v[76:79]\n\t"
    "v_mul_f32 v41, v41, v53\n\t"
    "v_mul_f32 v42, v42, v54\n\t"
    "v_mul_f32 v43, v43, v55\n\t"
    "s_waitcnt lgkmcnt(4)\n\t"
    "v_mfma_f32_16x16x32_bf16 v[64:67], v[208:211], v[112:115], v[64:67]\n\t"
    "ds_read_b128 v[104:107], %[da] offset:24576\n\t"
    "v_mul_f32 v44, v44, v60\n\t"
    "v_mul_f32 v45, v45, v61\n\t"
    "v_mul_f32 v46, v46, v62\n\t"
    "v_mfma_f32_16x16x32_bf16 v[68:71], v[212:215], v[112:115], v[68:71]\n\t"
    "ds_read_b128 v[108:111], %[da] offset:26624\n\t"
    "v_mul_f32 v47, v47, v63\n\t"
    "v_cvt_pk_bf16_f32 v48, v40, v41\n\t"
    "v_cvt_pk_bf16_f32 v49, v42, v43\n\t"
    "v_mfma_f32_16x16x32_bf16 v[72:75], v[208:211], v[116:119], v[72:75]\n\t"
    "v_cvt_pk_bf16_f32 v50, v44, v45\n\t"
    "v_cvt_pk_bf16_f32 v51, v46, v47\n\t"
    "s_nop 1\n\t"
    "v_mfma_f32_16x16x32_bf16 v[76:79], v[212:215], v[116:119], v[76:79]\n\t"
    "v_permlane16_swap_b32 v48, v50\n\t"
    "v_permlane16_swap_b32 v49, v51\n\t"
    "global_store_dwordx4 %[co0], v[48:51], s[36:37] sc1\n\t"
    "s_waitcnt lgkmcnt(4)\n\t"
    "v_mfma_f32_16x16x32_bf16 v[64:67], v[216:219], v[120:123], v[64:67]\n\t"
    "ds_read_b128 v[112:115], %[ea] offset:28672\n\t"
    "v_mfma_f32_16x16x32_bf16 v[68:71], v[220:223], v[120:123], v[68:71]\n\t"
    "ds_read_b128 v[116:119], %[ea] offset:30720\n\t"
    "v_mfma_f32_16x16x32_bf16 v[72:75], v[216:219], v[124:127], v[72:75]\n\t"
    "v_xor_b32 %[ea], 0x10000, %[ea]\n\t"
    "v_mfma_f32_16x16x32_bf16 v[76:79], v[220:223], v[124:127], v[76:79]\n\t"
    "s_waitcnt vmcnt(1)\n\t"
    "s_barrier\n\t"
    "s_waitcnt lgkmcnt(4)\n\t"
    "v_mfma_f32_16x16x32_bf16 v[64:67], v[224:227], v[96:99], v[64:67]\n\t"
    "ds_read_b32 v39, %[sa]\n\t"
    "ds_read_b128 v[120:123], %[da] offset:28672\n\t"
    "v_mfma_f32_16x16x32_bf16 v[68:71], v[228:231], v[96:99], v[68:71]\n\t"
    "ds_read_b128 v[124:127], %[da] offset:30720\n\t"
    "v_mfma_f32_16x16x32_bf16 v[72:75], v[224:227], v[100:103], v[72:75]\n\t"
    "v_xor_b32 %[da], 0x10000, %[da]\n\t"
    "v_mfma_f32_16x16x32_bf16 v[76:79], v[228:231], v[100:103], v[76:79]\n\t"
    "s_waitcnt lgkmcnt(5)\n\t"
    "v_mfma_f32_16x16x32_bf16 v[64:67], v[232:235], v[104:107], v[64:67]\n\t"
    "ds_read_b128 v[96:99], %[ea] offset:0\n\t"
    "v_mfma_f32_16x16x32_bf16 v[68:71], v[236:239], v[104:107], v[68:71]\n\t"
    "ds_read_b128 v[100:103], %[ea] offset:2048\n\t"
    "v_mfma_f32_16x16x32_bf16 v[72:75], v[232:235], v[108:111], v[72:75]\n\t"
    "v_mfma_f32_16x16x32_bf16 v[76:79], v[236:239], v[108:111], v[76:79]\n\t"
    "s_waitcnt lgkmcnt(4)\n\t"
    "v_mfma_f32_16x16x32_bf16 v[64:67], v[240:243], v[112:115], v[64:67]\n\t"
    "ds_read_b128 v[104:107], %[da] offset:0\n\t"
    "v_mad_u32_u24 v39, v39, %[mu], %[ad]\n\t"
    "v_mfma_f32_16x16x32_bf16 v[68:71], v[244:247], v[112:115], v[68:71]\n\t"
    "ds_read_b128 v[108:111], %[da] offset:2048\n\t"
    "v_cmp_ge_i32 vcc, %[lf], v39\n\t"
    "s_cmp_eq_u64 vcc, 0\n\t"
    "s_addc_u32 %[lf], %[lf], 0\n\t"
    "v_mfma_f32_16x16x32_bf16 v[72:75], v[240:243], v[116:119], v[72:75]\n\t"
    "v_mfma_f32_16x16x32_bf16 v[76:79], v[244:247], v[116:119], v[76:79]\n\t"
    "v_cmp_ge_i32 vcc, %[lf], v39\n\t"
    "s_cmp_eq_u64 vcc, 0\n\t"
    "s_addc_u32 %[lf], %[lf], 0\n\t"
    "s_waitcnt lgkmcnt(4)\n\t"
    "v_mfma_f32_16x16x32_bf16 v[80:83], v[248:251], v[120:123], v[64:67]\n\t"
    "ds_read_b128 v[112:115], %[ea] offset:4096\n\t"
    "s_min_i32 %[lf], %[lf], %[cp]\n\t"
    "s_add_u32 %[b], %[b], 1\n\t"
    "v_mfma_f32_16x16x32_bf16 v[84:87], v[252:255], v[120:123], v[68:71]\n\t"
    "ds_read_b128 v[116:119], %[ea] offset:6144\n\t"
    "v_mfma_f32_16x16x32_bf16 v[88:91], v[248:251], v[124:127], v[72:75]\n\t"
    "v_mfma_f32_16x16x32_bf16 v[92:95], v[252:255], v[124:127], v[76:79]\n\t"
    "s_mov_b64 s[36:37], s[38:39]\n\t"
    "s_add_u32 s38, s38, %[ab]\n\t"
    "s_addc_u32 s39, s39, 0\n\t"
    "s_cmp_lt_i32 %[b], %[lf]\n\t"
    "s_cbranch_scc1 1b\n\t"
    "s_waitcnt lgkmcnt(0)\n\t"
    "s_nop 4\n\t"
    "s_branch 9f\n\t"
    "5:\n\t"
    "s_add_u32 %[st], %[b], %[po]\n\t"
    "s_max_i32 %[st], %[st], %[p0]\n\t"
    "v_mov_b32 v39, %[st]\n\t"
    "v_mov_b32 v38, 0\n\t"
    "s_mov_b64 exec, 1\n\t"
    "global_store_dword v38, v39, %[pg] sc1\n\t"
    "s_mov_b64 exec, -1\n\t"
    "s_mov_b32 %[p0], %[p1]\n\t"
    "s_cmp_ge_i32 %[lf], %[cp]\n\t"
    "s_cbranch_scc1 6b\n\t"
    "s_mov_b32 m0, %[sl]\n\t"
    "s_nop 0\n\t"
    "global_load_lds_dword %[dv], %[db] sc1\n\t"
    "s_branch 6b\n\t"
    "9:"
    : "=&{v[80:95]}"(pd), [ea] "+&v"(ea), [da] "+&v"(da), [b] "+&s"(b), [lf] "+&s"(lf), "+{s[36:37]}"(p1), "+{s[38:39]}"(p0), [sa] "+&v"(sa), [st] "=&s"(st), [sl] "+&s"(sl), [p0] "+&s"(pf)
    : [rw] "v"(rw), [ab] "s"(ab), [cp] "s"(cp), [mu] "s"(mu), [ad] "v"(ad), "{v[128:159]}"(W[0]), "{v[160:191]}"(W[1]), "{v[192:223]}"(W[2]), "{v[224:255]}"(W[3]), [co0] "v"(co0), [w0] "s"(w0), [dv] "v"(dv), [db] "s"(db), [pg] "s"(pg), [po] "s"(po - 1), [p1] "s"(b + po), [nl] "s"(-LOG2E)
    : "v38", "v39", "v40", "v41", "v42", "v43", "v44", "v45", "v46", "v47", "v48", "v49", "v50", "v51", "v52", "v53", "v54", "v55", "v56", "v57", "v58", "v59", "v60", "v61", "v62", "v63", "v64", "v65", "v66", "v67", "v68", "v69", "v70", "v71", "v72", "v73", "v74", "v75", "v76", "v77", "v78", "v79", "v96", "v97", "v98", "v99", "v100", "v101", "v102", "v103", "v104", "v105", "v106", "v107", "v108", "v109", "v110", "v111", "v112", "v113", "v114", "v115", "v116", "v117", "v118", "v119", "v120", "v121", "v122", "v123", "v124", "v125", "v126", "v127", "vcc", "scc", "m0", "memory");
}
__device__ __forceinline__ void near_st2(const u32x32 (&W)[4], u32x16& pd, unsigned ea, unsigned da, int& b, int& lf, int cp, unsigned sa, unsigned mu, unsigned ad,
    unsigned rw, unsigned ab, const char* p1, const char* p0, unsigned co0, unsigned co1, int po, int pf, unsigned w0, unsigned sl, unsigned dv, const char* db, const char* pg) {
  unsigned st;
  asm volatile("s_waitcnt vmcnt(0)\n\t"
    "s_barrier\n\t"
    "ds_read_b128 v[96:99], %[ea] offset:0\n\t"
    "ds_read_b128 v[100:103], %[ea] offset:2048\n\t"
    "ds_read_b128 v[104:107], %[da] offset:0\n\t"
    "ds_read_b128 v[108:111], %[da] offset:2048\n\t"
    "ds_read_b128 v[112:115], %[ea] offset:4096\n\t"
    "ds_read_b128 v[116:119], %[ea] offset:6144\n\t"
    "s_branch 2f\n\t"
    "1:\n\t"
    "s_waitcnt vmcnt(0)\n\t"
    "s_barrier\n\t"
    "2:\n\t"
    "s_waitcnt lgkmcnt(4)\n\t"
    "v_mfma_f32_16x16x32_bf16 v[64:67], v[128:131], v[96:99], 0\n\t"
    "ds_read_b128 v[120:123], %[da] offset:4096\n\t"
    "v_mfma_f32_16x16x32_bf16 v[68:71], v[132:135], v[96:99], 0\n\t"
    "ds_read_b128 v[124:127], %[da] offset:6144\n\t"
    "v_mfma_f32_16x16x32_bf16 v[72:75], v[128:131], v[100:103], 0\n\t"
    "v_mfma_f32_16x16x32_bf16 v[76:79], v[132:135], v[100:103], 0\n\t"
    "s_cmp_eq_u32 %[w0], 0\n\t"
    "s_cbranch_scc1 5f\n\t"
    "6:\n\t"
    "s_waitcnt lgkmcnt(4)\n\t"
    "v_mfma_f32_16x16x32_bf16 v[64:67], v[136:139], v[104:107], v[64:67]\n\t"
    "ds_read_b128 v[96:99], %[ea] offset:8192\n\t"
    "v_mfma_f32_16x16x32_bf16 v[68:71], v[140:143], v[104:107], v[68:71]\n\t"
    "ds_read_b128 v[100:103], %[ea] offset:10240\n\t"
    "v_mfma_f32_16x16x32_bf16 v[72:75], v[136:139], v[108:111], v[72:75]\n\t"
    "v_mfma_f32_16x16x32_bf16 v[76:79], v[140:143], v[108:111], v[76:79]\n\t"
    "s_waitcnt lgkmcnt(4)\n\t"
    "v_mfma_f32_16x16x32_bf16 v[64:67], v[144:147], v[112:115], v[64:67]\n\t"
    "ds_read_b128 v[104:107], %[da] offset:8192\n\t"
    "v_mfma_f32_16x16x32_bf16 v[68:71], v[148:151], v[112:115], v[68:71]\n\t"
    "ds_read_b128 v[108:111], %[da] offset:10240\n\t"
    "v_mfma_f32_16x16x32_bf16 v[72:75], v[144:147], v[116:119], v[72:75]\n\t"
    "v_mfma_f32_16x16x32_bf16 v[76:79], v[148:151], v[116:119], v[76:79]\n\t"
    "s_barrier\n\t"
    "s_waitcnt lgkmcnt(4)\n\t"
    "v_mfma_f32_16x16x32_bf16 v[64:67], v[152:155], v[120:123], v[64:67]\n\t"
    "ds_read_b128 v[112:115], %[ea] offset:12288\n\t"
    "v_mfma_f32_16x16x32_bf16 v[68:71], v[156:159], v[120:123], v[68:71]\n\t"
    "ds_read_b128 v[116:119], %[ea] offset:14336\n\t"
    "v_mfma_f32_16x16x32_bf16 v[72:75], v[152:155], v[124:127], v[72:75]\n\t"
    "ds_read_b128 v[48:51], %[rw] offset:0\n\t"
    "v_mfma_f32_16x16x32_bf16 v[76:79], v[156:159], v[124:127], v[76:79]\n\t"
    "ds_read_b128 v[52:55], %[rw] offset:1024\n\t"
    "s_waitcnt lgkmcnt(6)\n\t"
    "v_mfma_f32_16x16x32_bf16 v[64:67], v[160:163], v[96:99], v[64:67]\n\t"
    "ds_read_b128 v[120:123], %[da] offset:12288\n\t"
    "v_mfma_f32_16x16x32_bf16 v[68:71], v[164:167], v[96:99], v[68:71]\n\t"
    "ds_read_b128 v[124:127], %[da] offset:14336\n\t"
    "v_mfma_f32_16x16x32_bf16 v[72:75], v[160:163], v[100:103], v[72:75]\n\t"
    "ds_read_b128 v[56:59], %[rw] offset:2048\n\t"
    "v_mfma_f32_16x16x32_bf16 v[76:79], v[164:167], v[100:103], v[76:79]\n\t"
    "ds_read_b128 v[60:63], %[rw] offset:3072\n\t"
    "s_waitcnt lgkmcnt(0)\n\t"
    "v_mfma_f32_16x16x32_bf16 v[64:67], v[168:171], v[104:107], v[64:67]\n\t"
    "ds_read_b128 v[96:99], %[ea] offset:16384\n\t"
    "v_cvt_pk_bf16_f32 v40, v80, v81\n\t"
    "v_mfma_f32_16x16x32_bf16 v[68:71], v[172:175], v[104:107], v[68:71]\n\t"
    "ds_read_b128 v[100:103], %[ea] offset:18432\n\t"
    "v_cvt_pk_bf16_f32 v41, v82, v83\n\t"
    "v_mfma_f32_16x16x32_bf16 v[72:75], v[168:171], v[108:111], v[72:75]\n\t"
    "v_cvt_pk_bf16_f32 v42, v84, v85\n\t"
    "v_mfma_f32_16x16x32_bf16 v[76:79], v[172:175], v[108:111], v[76:79]\n\t"
    "v_cvt_pk_bf16_f32 v43, v86, v87\n\t"
    "v_mfma_f32_16x16x32_bf16 v[64:67], v[176:179], v[112:115], v[64:67]\n\t"
    "ds_read_b128 v[104:107], %[da] offset:16384\n\t"
    "global_store_dwordx4 %[co0], v[40:43], s[36:37] sc1\n\t"
    "v_mfma_f32_16x16x32_bf16 v[68:71], v[180:183], v[112:115], v[68:71]\n\t"
    "ds_read_b128 v[108:111], %[da] offset:18432\n\t"
    "v_cvt_pk_bf16_f32 v44, v88, v89\n\t"
    "v_mfma_f32_16x16x32_bf16 v[72:75], v[176:179], v[116:119], v[72:75]\n\t"
    "v_cvt_pk_bf16_f32 v45, v90, v91\n\t"
    "v_mfma_f32_16x16x32_bf16 v[76:79], v[180:183], v[116:119], v[76:79]\n\t"
    "v_cvt_pk_bf16_f32 v46, v92, v93\n\t"
    "v_mfma_f32_16x16x32_bf16 v[64:67], v[184:187], v[120:123], v[64:67]\n\t"
    "ds_read_b128 v[112:115], %[ea] offset:20480\n\t"
    "v_cvt_pk_bf16_f32 v47, v94, v95\n\t"
    "v_mfma_f32_16x16x32_bf16 v[68:71], v[188:191], v[120:123], v[68:71]\n\t"
    "ds_read_b128 v[116:119], %[ea] offset:22528\n\t"
    "global_store_dwordx4 %[co1], v[44:47], s[36:37] sc1\n\t"
    "v_mfma_f32_16x16x32_bf16 v[72:75], v[184:187], v[124:127], v[72:75]\n\t"
    "v_cvt_pk_bf16_f32 v48, v48, v49\n\t"
    "v_mfma_f32_16x16x32_bf16 v[76:79], v[188:191], v[124:127], v[76:79]\n\t"
    "v_cvt_pk_bf16_f32 v49, v50, v51\n\t"
    "s_waitcnt lgkmcnt(4)\n\t"
    "v_mfma_f32_16x16x32_bf16 v[64:67], v[192:195], v[96:99], v[64:67]\n\t"
    "ds_read_b128 v[120:123], %[da] offset:20480\n\t"
    "v_cvt_pk_bf16_f32 v50, v52, v53\n\t"
    "v_mfma_f32_16x16x32_bf16 v[68:71], v[196:199], v[96:99], v[68:71]\n\t"
    "ds_read_b128 v[124:127], %[da] offset:22528\n\t"
    "v_cvt_pk_bf16_f32 v51, v54, v55\n\t"
    "v_mfma_f32_16x16x32_bf16 v[72:75], v[192:195], v[100:103], v[72:75]\n\t"
    "global_store_dwordx4 %[co0], v[48:51], s[36:37] offset:2048 sc1\n\t"
    "v_mfma_f32_16x16x32_bf16 v[76:79], v[196:199], v[100:103], v[76:79]\n\t"
    "v_cvt_pk_bf16_f32 v56, v56, v57\n\t"
    "s_waitcnt lgkmcnt(4)\n\t"
    "v_mfma_f32_16x16x32_bf16 v[64:67], v[200:203], v[104:107], v[64:67]\n\t"
    "ds_read_b128 v[96:99], %[ea] offset:24576\n\t"
    "v_cvt_pk_bf16_f32 v57, v58, v59\n\t"
    "v_mfma_f32_16x16x32_bf16 v[68:71], v[204:207], v[104:107], v[68:71]\n\t"
    "ds_read_b128 v[100:103], %[ea] offset:26624\n\t"
    "v_cvt_pk_bf16_f32 v58, v60, v61\n\t"
    "v_mfma_f32_16x16x32_bf16 v[72:75], v[200:203], v[108:111], v[72:75]\n\t"
    "v_cvt_pk_bf16_f32 v59, v62, v63\n\t"
    "v_mfma_f32_16x16x32_bf16 v[76:79], v[204:207], v[108:111], v[76:79]\n\t"
    "global_store_dwordx4 %[co1], v[56:59], s[36:37] offset:2048 sc1\n\t"
    "s_waitcnt lgkmcnt(4)\n\t"
    "v_mfma_f32_16x16x32_bf16 v[64:67], v[208:211], v[112:115], v[64:67]\n\t"
    "ds_read_b128 v[104:107], %[da] offset:24576\n\t"
    "v_mfma_f32_16x16x32_bf16 v[68:71], v[212:215], v[112:115], v[68:71]\n\t"
    "ds_read_b128 v[108:111], %[da] offset:26624\n\t"
    "v_mfma_f32_16x16x32_bf16 v[72:75], v[208:211], v[116:119], v[72:75]\n\t"
    "v_mfma_f32_16x16x32_bf16 v[76:79], v[212:215], v[116:119], v[76:79]\n\t"
    "s_waitcnt lgkmcnt(4)\n\t"
    "v_mfma_f32_16x16x32_bf16 v[64:67], v[216:219], v[120:123], v[64:67]\n\t"
    "ds_read_b128 v[112:115], %[ea] offset:28672\n\t"
    "v_mfma_f32_16x16x32_bf16 v[68:71], v[220:223], v[120:123], v[68:71]\n\t"
    "ds_read_b128 v[116:119], %[ea] offset:30720\n\t"
    "v_mfma_f32_16x16x32_bf16 v[72:75], v[216:219], v[124:127], v[72:75]\n\t"
    "v_xor_b32 %[ea], 0x10000, %[ea]\n\t"
    "v_mfma_f32_16x16x32_bf16 v[76:79], v[220:223], v[124:127], v[76:79]\n\t"
    "s_waitcnt vmcnt(4)\n\t"
    "s_barrier\n\t"
    "s_waitcnt lgkmcnt(4)\n\t"
    "v_mfma_f32_16x16x32_bf16 v[64:67], v[224:227], v[96:99], v[64:67]\n\t"
    "ds_read_b32 v39, %[sa]\n\t"
    "ds_read_b128 v[120:123], %[da] offset:28672\n\t"
    "v_mfma_f32_16x16x32_bf16 v[68:71], v[228:231], v[96:99], v[68:71]\n\t"
    "ds_read_b128 v[124:127], %[da] offset:30720\n\t"
    "v_mfma_f32_16x16x32_bf16 v[72:75], v[224:227], v[100:103], v[72:75]\n\t"
    "v_xor_b32 %[da], 0x10000, %[da]\n\t"
    "v_mfma_f32_16x16x32_bf16 v[76:79], v[228:231], v[100:103], v[76:79]\n\t"
    "s_waitcnt lgkmcnt(5)\n\t"
    "v_mfma_f32_16x16x32_bf16 v[64:67], v[232:235], v[104:107], v[64:67]\n\t"
    "ds_read_b128 v[96:99], %[ea] offset:0\n\t"
    "v_mfma_f32_16x16x32_bf16 v[68:71], v[236:239], v[104:107], v[68:71]\n\t"
    "ds_read_b128 v[100:103], %[ea] offset:2048\n\t"
    "v_mfma_f32_16x16x32_bf16 v[72:75], v[232:235], v[108:111], v[72:75]\n\t"
    "v_mfma_f32_16x16x32_bf16 v[76:79], v[236:239], v[108:111], v[76:79]\n\t"
    "s_waitcnt lgkmcnt(4)\n\t"
    "v_mfma_f32_16x16x32_bf16 v[64:67], v[240:243], v[112:115], v[64:67]\n\t"
    "ds_read_b128 v[104:107], %[da] offset:0\n\t"
    "v_mad_u32_u24 v39, v39, %[mu], %[ad]\n\t"
    "v_mfma_f32_16x16x32_bf16 v[68:71], v[244:247], v[112:115], v[68:71]\n\t"
    "ds_read_b128 v[108:111], %[da] offset:2048\n\t"
    "v_cmp_ge_i32 vcc, %[lf], v39\n\t"
    "s_cmp_eq_u64 vcc, 0\n\t"
    "s_addc_u32 %[lf], %[lf], 0\n\t"
    "v_mfma_f32_16x16x32_bf16 v[72:75], v[240:243], v[116:119], v[72:75]\n\t"
    "v_mfma_f32_16x16x32_bf16 v[76:79], v[244:247], v[116:119], v[76:79]\n\t"
    "v_cmp_ge_i32 vcc, %[lf], v39\n\t"
    "s_cmp_eq_u64 vcc, 0\n\t"
    "s_addc_u32 %[lf], %[lf], 0\n\t"
    "s_waitcnt lgkmcnt(4)\n\t"
    "v_mfma_f32_16x16x32_bf16 v[80:83], v[248:251], v[120:123], v[64:67]\n\t"
    "ds_read_b128 v[112:115], %[ea] offset:4096\n\t"
    "s_min_i32 %[lf], %[lf], %[cp]\n\t"
    "s_add_u32 %[b], %[b], 1\n\t"
    "v_mfma_f32_16x16x32_bf16 v[84:87], v[252:255], v[120:123], v[68:71]\n\t"
    "ds_read_b128 v[116:119], %[ea] offset:6144\n\t"
    "v_mfma_f32_16x16x32_bf16 v[88:91], v[248:251], v[124:127], v[72:75]\n\t"
    "v_mfma_f32_16x16x32_bf16 v[92:95], v[252:255], v[124:127], v[76:79]\n\t"
    "s_mov_b64 s[36:37], s[38:39]\n\t"
    "s_add_u32 s38, s38, %[ab]\n\t"
    "s_addc_u32 s39, s39, 0\n\t"
    "s_cmp_lt_i32 %[b], %[lf]\n\t"
    "s_cbranch_scc1 1b\n\t"
    "s_waitcnt lgkmcnt(0)\n\t"
    "s_nop 4\n\t"
    "s_branch 9f\n\t"
    "5:\n\t"
    "s_add_u32 %[st], %[b], %[po]\n\t"
    "s_max_i32 %[st], %[st], %[p0]\n\t"
    "v_mov_b32 v39, %[st]\n\t"
    "v_mov_b32 v38, 0\n\t"
    "s_mov_b64 exec, 1\n\t"
    "global_store_dword v38, v39, %[pg] sc1\n\t"
    "s_mov_b64 exec, -1\n\t"
    "s_mov_b32 %[p0], %[p1]\n\t"
    "s_cmp_ge_i32 %[lf], %[cp]\n\t"
    "s_cbranch_scc1 6b\n\t"
    "s_mov_b32 m0, %[sl]\n\t"
    "s_nop 0\n\t"
    "global_load_lds_dword %[dv], %[db] sc1\n\t"
    "s_branch 6b\n\t"
    "9:"
    : "=&{v[80:95]}"(pd), [ea] "+&v"(ea), [da] "+&v"(da), [b] "+&s"(b), [lf] "+&s"(lf), "+{s[36:37]}"(p1), "+{s[38:39]}"(p0), [sa] "+&v"(sa), [st] "=&s"(st), [sl] "+&s"(sl), [p0] "+&s"(pf)
    : [rw] "v"(rw), [ab] "s"(ab), [cp] "s"(cp), [mu] "s"(mu), [ad] "v"(ad), "{v[128:159]}"(W[0]), "{v[160:191]}"(W[1]), "{v[192:223]}"(W[2]), "{v[224:255]}"(W[3]), [co0] "v"(co0), [w0] "s"(w0), [dv] "v"(dv), [db] "s"(db), [pg] "s"(pg), [po] "s"(po - 1), [p1] "s"(b + po), [co1] "v"(co1)
    : "v38", "v39", "v40", "v41", "v42", "v43", "v44", "v45", "v46", "v47", "v48", "v49", "v50", "v51", "v52", "v53", "v54", "v55", "v56", "v57", "v58", "v59", "v60", "v61", "v62", "v63", "v64", "v65", "v66", "v67", "v68", "v69", "v70", "v71", "v72", "v73", "v74", "v75", "v76", "v77", "v78", "v79", "v96", "v97", "v98", "v99", "v100", "v101", "v102", "v103", "v104", "v105", "v106", "v107", "v108", "v109", "v110", "v111", "v112", "v113", "v114", "v115", "v116", "v117", "v118", "v119", "v120", "v121", "v122", "v123", "v124", "v125", "v126", "v127", "vcc", "scc", "m0", "memory");
}
__device__ __forceinline__ void lean_ld0(const u32x32 (&W)[4], u32x16& pd, unsigned ea, unsigned da, unsigned x, unsigned lb, const char* s0, const char* s1, const unsigned (&av)[8], unsigned sa, unsigned mu, unsigned ad, int th, unsigned s2, unsigned dv, const char* db) {
  asm volatile("s_setprio 2\n\t"
    "ds_read_b128 v[96:99], %[ea] offset:0\n\t"
    "ds_read_b128 v[100:103], %[ea] offset:2048\n\t"
    "ds_read_b128 v[104:107], %[da] offset:0\n\t"
    "ds_read_b128 v[108:111], %[da] offset:2048\n\t"
    "ds_read_b128 v[112:115], %[ea] offset:4096\n\t"
    "ds_read_b128 v[116:119], %[ea] offset:6144\n\t"
    "s_waitcnt lgkmcnt(4)\n\t"
    "v_mfma_f32_16x16x32_bf16 v[80:83], v[128:131], v[96:99], 0\n\t"
    "ds_read_b128 v[120:123], %[da] offset:4096\n\t"
    "v_mfma_f32_16x16x32_bf16 v[84:87], v[132:135], v[96:99], 0\n\t"
    "ds_read_b128 v[124:127], %[da] offset:6144\n\t"
    "v_mfma_f32_16x16x32_bf16 v[88:91], v[128:131], v[100:103], 0\n\t"
    "v_mfma_f32_16x16x32_bf16 v[92:95], v[132:135], v[100:103], 0\n\t"
    "s_waitcnt vmcnt(24)\n\t"
    "s_cmp_lt_u32 %[x], 2\n\t"
    "s_cbranch_scc0 41f\n\t"
    "s_waitcnt vmcnt(8)\n\t"
    "41:\n\t"
    "s_barrier\n\t"
    "s_waitcnt lgkmcnt(4)\n\t"
    "v_mfma_f32_16x16x32_bf16 v[80:83], v[136:139], v[104:107], v[80:83]\n\t"
    "ds_read_b128 v[96:99], %[ea] offset:8192\n\t"
    "v_mfma_f32_16x16x32_bf16 v[84:87], v[140:143], v[104:107], v[84:87]\n\t"
    "ds_read_b128 v[100:103], %[ea] offset:10240\n\t"
    "v_mfma_f32_16x16x32_bf16 v[88:91], v[136:139], v[108:111], v[88:91]\n\t"
    "v_mfma_f32_16x16x32_bf16 v[92:95], v[140:143], v[108:111], v[92:95]\n\t"
    "s_waitcnt lgkmcnt(4)\n\t"
    "v_mfma_f32_16x16x32_bf16 v[80:83], v[144:147], v[112:115], v[80:83]\n\t"
    "ds_read_b128 v[104:107], %[da] offset:8192\n\t"
    "v_mfma_f32_16x16x32_bf16 v[84:87], v[148:151], v[112:115], v[84:87]\n\t"
    "ds_read_b128 v[108:111], %[da] offset:10240\n\t"
    "v_mfma_f32_16x16x32_bf16 v[88:91], v[144:147], v[116:119], v[88:91]\n\t"
    "v_mfma_f32_16x16x32_bf16 v[92:95], v[148:151], v[116:119], v[92:95]\n\t"
    "s_waitcnt lgkmcnt(4)\n\t"
    "v_mfma_f32_16x16x32_bf16 v[80:83], v[152:155], v[120:123], v[80:83]\n\t"
    "ds_read_b128 v[112:115], %[ea] offset:12288\n\t"
    "v_mfma_f32_16x16x32_bf16 v[84:87], v[156:159], v[120:123], v[84:87]\n\t"
    "ds_read_b128 v[116:119], %[ea] offset:14336\n\t"
    "v_mfma_f32_16x16x32_bf16 v[88:91], v[152:155], v[124:127], v[88:91]\n\t"
    "v_mfma_f32_16x16x32_bf16 v[92:95], v[156:159], v[124:127], v[92:95]\n\t"
    "s_waitcnt lgkmcnt(4)\n\t"
    "v_mfma_f32_16x16x32_bf16 v[80:83], v[160:163], v[96:99], v[80:83]\n\t"
    "ds_read_b128 v[120:123], %[da] offset:12288\n\t"
    "v_mfma_f32_16x16x32_bf16 v[84:87], v[164:167], v[96:99], v[84:87]\n\t"
    "ds_read_b128 v[124:127], %[da] offset:14336\n\t"
    "v_mfma_f32_16x16x32_bf16 v[88:91], v[160:163], v[100:103], v[88:91]\n\t"
    "v_mfma_f32_16x16x32_bf16 v[92:95], v[164:167], v[100:103], v[92:95]\n\t"
    "s_waitcnt vmcnt(20)\n\t"
    "s_cmp_lt_u32 %[x], 2\n\t"
    "s_cbranch_scc0 45f\n\t"
    "s_waitcnt vmcnt(4)\n\t"
    "45:\n\t"
    "s_barrier\n\t"
    "s_waitcnt lgkmcnt(4)\n\t"
    "v_mfma_f32_16x16x32_bf16 v[80:83], v[168:171], v[104:107], v[80:83]\n\t"
    "ds_read_b128 v[96:99], %[ea] offset:16384\n\t"
    "v_mfma_f32_16x16x32_bf16 v[84:87], v[172:175], v[104:107], v[84:87]\n\t"
    "ds_read_b128 v[100:103], %[ea] offset:18432\n\t"
    "v_mfma_f32_16x16x32_bf16 v[88:91], v[168:171], v[108:111], v[88:91]\n\t"
    "v_mfma_f32_16x16x32_bf16 v[92:95], v[172:175], v[108:111], v[92:95]\n\t"
    "s_waitcnt lgkmcnt(4)\n\t"
    "v_mfma_f32_16x16x32_bf16 v[80:83], v[176:179], v[112:115], v[80:83]\n\t"
    "ds_read_b128 v[104:107], %[da] offset:16384\n\t"
    "v_mfma_f32_16x16x32_bf16 v[84:87], v[180:183], v[112:115], v[84:87]\n\t"
    "ds_read_b128 v[108:111], %[da] offset:18432\n\t"
    "v_mfma_f32_16x16x32_bf16 v[88:91], v[176:179], v[116:119], v[88:91]\n\t"
    "v_mfma_f32_16x16x32_bf16 v[92:95], v[180:183], v[116:119], v[92:95]\n\t"
    "s_waitcnt lgkmcnt(4)\n\t"
    "v_mfma_f32_16x16x32_bf16 v[80:83], v[184:187], v[120:123], v[80:83]\n\t"
    "ds_read_b128 v[112:115], %[ea] offset:20480\n\t"
    "v_mfma_f32_16x16x32_bf16 v[84:87], v[188:191], v[120:123], v[84:87]\n\t"
    "ds_read_b128 v[116:119], %[ea] offset:22528\n\t"
    "v_mfma_f32_16x16x32_bf16 v[88:91], v[184:187], v[124:127], v[88:91]\n\t"
    "v_mfma_f32_16x16x32_bf16 v[92:95], v[188:191], v[124:127], v[92:95]\n\t"
    "s_waitcnt lgkmcnt(4)\n\t"
    "v_mfma_f32_16x16x32_bf16 v[80:83], v[192:195], v[96:99], v[80:83]\n\t"
    "ds_read_b128 v[120:123], %[da] offset:20480\n\t"
    "v_mfma_f32_16x16x32_bf16 v[84:87], v[196:199], v[96:99], v[84:87]\n\t"
    "ds_read_b128 v[124:127], %[da] offset:22528\n\t"
    "v_mfma_f32_16x16x32_bf16 v[88:91], v[192:195], v[100:103], v[88:91]\n\t"
    "v_mfma_f32_16x16x32_bf16 v[92:95], v[196:199], v[100:103], v[92:95]\n\t"
    "s_waitcnt vmcnt(16)\n\t"
    "s_cmp_lt_u32 %[x], 2\n\t"
    "s_cbranch_scc0 49f\n\t"
    "s_waitcnt vmcnt(0)\n\t"
    "49:\n\t"
    "s_barrier\n\t"
    "ds_read_b32 v79, %[sa]\n\t"
    "s_waitcnt lgkmcnt(5)\n\t"
    "v_mfma_f32_16x16x32_bf16 v[80:83], v[200:203], v[104:107], v[80:83]\n\t"
    "ds_read_b128 v[96:99], %[ea] offset:24576\n\t"
    "v_mfma_f32_16x16x32_bf16 v[84:87], v[204:207], v[104:107], v[84:87]\n\t"
    "ds_read_b128 v[100:103], %[ea] offset:26624\n\t"
    "v_mfma_f32_16x16x32_bf16 v[88:91], v[200:203], v[108:111], v[88:91]\n\t"
    "v_mfma_f32_16x16x32_bf16 v[92:95], v[204:207], v[108:111], v[92:95]\n\t"
    "s_waitcnt lgkmcnt(5)\n\t"
    "v_mfma_f32_16x16x32_bf16 v[80:83], v[208:211], v[112:115], v[80:83]\n\t"
    "ds_read_b128 v[104:107], %[da] offset:24576\n\t"
    "v_mfma_f32_16x16x32_bf16 v[84:87], v[212:215], v[112:115], v[84:87]\n\t"
    "ds_read_b128 v[108:111], %[da] offset:26624\n\t"
    "v_mfma_f32_16x16x32_bf16 v[88:91], v[208:211], v[116:119], v[88:91]\n\t"
    "v_mfma_f32_16x16x32_bf16 v[92:95], v[212:215], v[116:119], v[92:95]\n\t"
    "s_cmp_eq_u32 %[s2], 0\n\t"
    "s_cbranch_scc1 7f\n\t"
    "s_mov_b32 m0, %[s2]\n\t"
    "s_nop 0\n\t"
    "global_load_lds_dword %[dv], %[db] sc1\n\t"
    "7:\n\t"
    "s_waitcnt lgkmcnt(4)\n\t"
    "v_mfma_f32_16x16x32_bf16 v[80:83], v[216:219], v[120:123], v[80:83]\n\t"
    "ds_read_b128 v[112:115], %[ea] offset:28672\n\t"
    "v_mfma_f32_16x16x32_bf16 v[84:87], v[220:223], v[120:123], v[84:87]\n\t"
    "ds_read_b128 v[116:119], %[ea] offset:30720\n\t"
    "v_mfma_f32_16x16x32_bf16 v[88:91], v[216:219], v[124:127], v[88:91]\n\t"
    "v_mad_u32_u24 v79, v79, %[mu], %[ad]\n\t"
    "v_cmp_ge_i32 vcc, %[th], v79\n\t"
    "v_mfma_f32_16x16x32_bf16 v[92:95], v[220:223], v[124:127], v[92:95]\n\t"
    "s_cbranch_vccz 5f\n\t"
    "s_waitcnt lgkmcnt(4)\n\t"
    "v_mfma_f32_16x16x32_bf16 v[80:83], v[224:227], v[96:99], v[80:83]\n\t"
    "ds_read_b128 v[120:123], %[da] offset:28672\n\t"
    "v_mfma_f32_16x16x32_bf16 v[84:87], v[228:231], v[96:99], v[84:87]\n\t"
    "ds_read_b128 v[124:127], %[da] offset:30720\n\t"
    "v_mfma_f32_16x16x32_bf16 v[88:91], v[224:227], v[100:103], v[88:91]\n\t"
    "v_mfma_f32_16x16x32_bf16 v[92:95], v[228:231], v[100:103], v[92:95]\n\t"
    "s_waitcnt lgkmcnt(4)\n\t"
    "v_mfma_f32_16x16x32_bf16 v[80:83], v[232:235], v[104:107], v[80:83]\n\t"
    "v_mfma_f32_16x16x32_bf16 v[84:87], v[236:239], v[104:107], v[84:87]\n\t"
    "v_mfma_f32_16x16x32_bf16 v[88:91], v[232:235], v[108:111], v[88:91]\n\t"
    "v_mfma_f32_16x16x32_bf16 v[92:95], v[236:239], v[108:111], v[92:95]\n\t"
    "s_waitcnt lgkmcnt(2)\n\t"
    "v_mfma_f32_16x16x32_bf16 v[80:83], v[240:243], v[112:115], v[80:83]\n\t"
    "v_mfma_f32_16x16x32_bf16 v[84:87], v[244:247], v[112:115], v[84:87]\n\t"
    "v_mfma_f32_16x16x32_bf16 v[88:91], v[240:243], v[116:119], v[88:91]\n\t"
    "v_mfma_f32_16x16x32_bf16 v[92:95], v[244:247], v[116:119], v[92:95]\n\t"
    "s_waitcnt lgkmcnt(0)\n\t"
    "v_mfma_f32_16x16x32_bf16 v[80:83], v[248:251], v[120:123], v[80:83]\n\t"
    "v_mfma_f32_16x16x32_bf16 v[84:87], v[252:255], v[120:123], v[84:87]\n\t"
    "v_mfma_f32_16x16x32_bf16 v[88:91], v[248:251], v[124:127], v[88:91]\n\t"
    "v_mfma_f32_16x16x32_bf16 v[92:95], v[252:255], v[124:127], v[92:95]\n\t"
    "s_branch 6f\n\t"
    "5:\n\t"
    "s_waitcnt lgkmcnt(4)\n\t"
    "v_mfma_f32_16x16x32_bf16 v[80:83], v[224:227], v[96:99], v[80:83]\n\t"
    "ds_read_b128 v[120:123], %[da] offset:28672\n\t"
    "s_add_u32 m0, %[lb], 0\n\t"
    "s_nop 0\n\t"
    "global_load_lds_dwordx4 %[av0], %[s0] sc1\n\t"
    "v_mfma_f32_16x16x32_bf16 v[84:87], v[228:231], v[96:99], v[84:87]\n\t"
    "ds_read_b128 v[124:127], %[da] offset:30720\n\t"
    "s_add_u32 m0, %[lb], 32768\n\t"
    "s_nop 0\n\t"
    "global_load_lds_dwordx4 %[av1], %[s0] sc1\n\t"
    "v_mfma_f32_16x16x32_bf16 v[88:91], v[224:227], v[100:103], v[88:91]\n\t"
    "s_add_u32 m0, %[lb], 4096\n\t"
    "s_nop 0\n\t"
    "global_load_lds_dwordx4 %[av2], %[s0] sc1\n\t"
    "v_mfma_f32_16x16x32_bf16 v[92:95], v[228:231], v[100:103], v[92:95]\n\t"
    "s_add_u32 m0, %[lb], 36864\n\t"
    "s_nop 0\n\t"
    "global_load_lds_dwordx4 %[av3], %[s0] sc1\n\t"
    "s_waitcnt lgkmcnt(4)\n\t"
    "v_mfma_f32_16x16x32_bf16 v[80:83], v[232:235], v[104:107], v[80:83]\n\t"
    "s_add_u32 m0, %[lb], 8192\n\t"
    "s_nop 0\n\t"
    "global_load_lds_dwordx4 %[av4], %[s0] sc1\n\t"
    "v_mfma_f32_16x16x32_bf16 v[84:87], v[236:239], v[104:107], v[84:87]\n\t"
    "s_add_u32 m0, %[lb], 40960\n\t"
    "s_nop 0\n\t"
    "global_load_lds_dwordx4 %[av5], %[s0] sc1\n\t"
    "v_mfma_f32_16x16x32_bf16 v[88:91], v[232:235], v[108:111], v[88:91]\n\t"
    "s_add_u32 m0, %[lb], 12288\n\t"
    "s_nop 0\n\t"
    "global_load_lds_dwordx4 %[av6], %[s0] sc1\n\t"
    "v_mfma_f32_16x16x32_bf16 v[92:95], v[236:239], v[108:111], v[92:95]\n\t"
    "s_add_u32 m0, %[lb], 45056\n\t"
    "s_nop 0\n\t"
    "global_load_lds_dwordx4 %[av7], %[s0] sc1\n\t"
    "s_waitcnt lgkmcnt(2)\n\t"
    "v_mfma_f32_16x16x32_bf16 v[80:83], v[240:243], v[112:115], v[80:83]\n\t"
    "s_add_u32 m0, %[lb], 16384\n\t"
    "s_nop 0\n\t"
    "global_load_lds_dwordx4 %[av0], %[s1] sc1\n\t"
    "v_mfma_f32_16x16x32_bf16 v[84:87], v[244:247], v[112:115], v[84:87]\n\t"
    "s_add_u32 m0, %[lb], 49152\n\t"
    "s_nop 0\n\t"
    "global_load_lds_dwordx4 %[av1], %[s1] sc1\n\t"
    "v_mfma_f32_16x16x32_bf16 v[88:91], v[240:243], v[116:119], v[88:91]\n\t"
    "s_add_u32 m0, %[lb], 20480\n\t"
    "s_nop 0\n\t"
    "global_load_lds_dwordx4 %[av2], %[s1] sc1\n\t"
    "v_mfma_f32_16x16x32_bf16 v[92:95], v[244:247], v[116:119], v[92:95]\n\t"
    "s_add_u32 m0, %[lb], 53248\n\t"
    "s_nop 0\n\t"
    "global_load_lds_dwordx4 %[av3], %[s1] sc1\n\t"
    "s_waitcnt lgkmcnt(0)\n\t"
    "v_mfma_f32_16x16x32_bf16 v[80:83], v[248:251], v[120:123], v[80:83]\n\t"
    "s_add_u32 m0, %[lb], 24576\n\t"
    "s_nop 0\n\t"
    "global_load_lds_dwordx4 %[av4], %[s1] sc1\n\t"
    "v_mfma_f32_16x16x32_bf16 v[84:87], v[252:255], v[120:123], v[84:87]\n\t"
    "s_add_u32 m0, %[lb], 57344\n\t"
    "s_nop 0\n\t"
    "global_load_lds_dwordx4 %[av5], %[s1] sc1\n\t"
    "v_mfma_f32_16x16x32_bf16 v[88:91], v[248:251], v[124:127], v[88:91]\n\t"
    "s_add_u32 m0, %[lb], 28672\n\t"
    "s_nop 0\n\t"
    "global_load_lds_dwordx4 %[av6], %[s1] sc1\n\t"
    "v_mfma_f32_16x16x32_bf16 v[92:95], v[252:255], v[124:127], v[92:95]\n\t"
    "s_add_u32 m0, %[lb], 61440\n\t"
    "s_nop 0\n\t"
    "global_load_lds_dwordx4 %[av7], %[s1] sc1\n\t"
    "6:\n\t"
    "s_nop 6\n\t"
    "s_branch 9f\n\t"
    "3:\n\t"
    "9:"
    : "=&{v[80:95]}"(pd)
    : [ea] "v"(ea), [da] "v"(da), [x] "s"(x), "{v[128:159]}"(W[0]), "{v[160:191]}"(W[1]), "{v[192:223]}"(W[2]), "{v[224:255]}"(W[3]), [lb] "s"(lb), [s0] "s"(s0), [s1] "s"(s1), [av0] "v"(av[0]), [av1] "v"(av[1]), [av2] "v"(av[2]), [av3] "v"(av[3]), [av4] "v"(av[4]), [av5] "v"(av[5]), [av6] "v"(av[6]), [av7] "v"(av[7]), [sa] "v"(sa), [mu] "s"(mu), [ad] "v"(ad), [th] "s"(th), [s2] "s"(s2), [dv] "v"(dv), [db] "s"(db)
    : "v96", "v97", "v98", "v99", "v100", "v101", "v102", "v103", "v104", "v105", "v106", "v107", "v108", "v109", "v110", "v111", "v112", "v113", "v114", "v115", "v116", "v117", "v118", "v119", "v120", "v121", "v122", "v123", "v124", "v125", "v126", "v127", "v79", "vcc", "scc", "m0", "memory");
}
__device__ __forceinline__ void lean_dm0(const u32x32 (&W)[4], u32x16& pd, unsigned ea, unsigned da, unsigned x, unsigned lb, const char* s0, const char* s1, const unsigned (&av)[8]) {
  asm volatile("s_setprio 2\n\t"
    "ds_read_b128 v[96:99], %[ea] offset:0\n\t"
    "ds_read_b128 v[100:103], %[ea] offset:2048\n\t"
    "ds_read_b128 v[104:107], %[da] offset:0\n\t"
    "ds_read_b128 v[108:111], %[da] offset:2048\n\t"
    "ds_read_b128 v[112:115], %[ea] offset:4096\n\t"
    "ds_read_b128 v[116:119], %[ea] offset:6144\n\t"
    "s_waitcnt lgkmcnt(4)\n\t"
    "v_mfma_f32_16x16x32_bf16 v[80:83], v[128:131], v[96:99], 0\n\t"
    "ds_read_b128 v[120:123], %[da] offset:4096\n\t"
    "s_add_u32 m0, %[lb], 0\n\t"
    "v_mfma_f32_16x16x32_bf16 v[84:87], v[132:135], v[96:99], 0\n\t"
    "ds_read_b128 v[124:127], %[da] offset:6144\n\t"
    "global_load_lds_dwordx4 %[av0], %[s0] sc1\n\t"
    "v_mfma_f32_16x16x32_bf16 v[88:91], v[128:131], v[100:103], 0\n\t"
    "s_add_u32 m0, %[lb], 32768\n\t"
    "v_mfma_f32_16x16x32_bf16 v[92:95], v[132:135], v[100:103], 0\n\t"
    "global_load_lds_dwordx4 %[av1], %[s0] sc1\n\t"
    "s_waitcnt vmcnt(10)\n\t"
    "s_barrier\n\t"
    "s_waitcnt lgkmcnt(4)\n\t"
    "v_mfma_f32_16x16x32_bf16 v[80:83], v[136:139], v[104:107], v[80:83]\n\t"
    "ds_read_b128 v[96:99], %[ea] offset:8192\n\t"
    "s_add_u32 m0, %[lb], 4096\n\t"
    "v_mfma_f32_16x16x32_bf16 v[84:87], v[140:143], v[104:107], v[84:87]\n\t"
    "ds_read_b128 v[100:103], %[ea] offset:10240\n\t"
    "global_load_lds_dwordx4 %[av2], %[s0] sc1\n\t"
    "v_mfma_f32_16x16x32_bf16 v[88:91], v[136:139], v[108:111], v[88:91]\n\t"
    "s_add_u32 m0, %[lb], 36864\n\t"
    "v_mfma_f32_16x16x32_bf16 v[92:95], v[140:143], v[108:111], v[92:95]\n\t"
    "global_load_lds_dwordx4 %[av3], %[s0] sc1\n\t"
    "s_waitcnt lgkmcnt(4)\n\t"
    "v_mfma_f32_16x16x32_bf16 v[80:83], v[144:147], v[112:115], v[80:83]\n\t"
    "ds_read_b128 v[104:107], %[da] offset:8192\n\t"
    "s_add_u32 m0, %[lb], 8192\n\t"
    "v_mfma_f32_16x16x32_bf16 v[84:87], v[148:151], v[112:115], v[84:87]\n\t"
    "ds_read_b128 v[108:111], %[da] offset:10240\n\t"
    "global_load_lds_dwordx4 %[av4], %[s0] sc1\n\t"
    "v_mfma_f32_16x16x32_bf16 v[88:91], v[144:147], v[116:119], v[88:91]\n\t"
    "s_add_u32 m0, %[lb], 40960\n\t"
    "v_mfma_f32_16x16x32_bf16 v[92:95], v[148:151], v[116:119], v[92:95]\n\t"
    "global_load_lds_dwordx4 %[av5], %[s0] sc1\n\t"
    "s_waitcnt lgkmcnt(4)\n\t"
    "v_mfma_f32_16x16x32_bf16 v[80:83], v[152:155], v[120:123], v[80:83]\n\t"
    "ds_read_b128 v[112:115], %[ea] offset:12288\n\t"
    "s_add_u32 m0, %[lb], 12288\n\t"
    "v_mfma_f32_16x16x32_bf16 v[84:87], v[156:159], v[120:123], v[84:87]\n\t"
    "ds_read_b128 v[116:119], %[ea] offset:14336\n\t"
    "global_load_lds_dwordx4 %[av6], %[s0] sc1\n\t"
    "v_mfma_f32_16x16x32_bf16 v[88:91], v[152:155], v[124:127], v[88:91]\n\t"
    "s_add_u32 m0, %[lb], 45056\n\t"
    "v_mfma_f32_16x16x32_bf16 v[92:95], v[156:159], v[124:127], v[92:95]\n\t"
    "global_load_lds_dwordx4 %[av7], %[s0] sc1\n\t"
    "s_waitcnt lgkmcnt(4)\n\t"
    "v_mfma_f32_16x16x32_bf16 v[80:83], v[160:163], v[96:99], v[80:83]\n\t"
    "ds_read_b128 v[120:123], %[da] offset:12288\n\t"
    "s_add_u32 m0, %[lb], 16384\n\t"
    "v_mfma_f32_16x16x32_bf16 v[84:87], v[164:167], v[96:99], v[84:87]\n\t"
    "ds_read_b128 v[124:127], %[da] offset:14336\n\t"
    "global_load_lds_dwordx4 %[av0], %[s1] sc1\n\t"
    "v_mfma_f32_16x16x32_bf16 v[88:91], v[160:163], v[100:103], v[88:91]\n\t"
    "s_add_u32 m0, %[lb], 49152\n\t"
    "v_mfma_f32_16x16x32_bf16 v[92:95], v[164:167], v[100:103], v[92:95]\n\t"
    "global_load_lds_dwordx4 %[av1], %[s1] sc1\n\t"
    "s_waitcnt vmcnt(14)\n\t"
    "s_barrier\n\t"
    "s_waitcnt lgkmcnt(4)\n\t"
    "v_mfma_f32_16x16x32_bf16 v[80:83], v[168:171], v[104:107], v[80:83]\n\t"
    "ds_read_b128 v[96:99], %[ea] offset:16384\n\t"
    "s_add_u32 m0, %[lb], 20480\n\t"
    "v_mfma_f32_16x16x32_bf16 v[84:87], v[172:175], v[104:107], v[84:87]\n\t"
    "ds_read_b128 v[100:103], %[ea] offset:18432\n\t"
    "global_load_lds_dwordx4 %[av2], %[s1] sc1\n\t"
    "v_mfma_f32_16x16x32_bf16 v[88:91], v[168:171], v[108:111], v[88:91]\n\t"
    "s_add_u32 m0, %[lb], 53248\n\t"
    "v_mfma_f32_16x16x32_bf16 v[92:95], v[172:175], v[108:111], v[92:95]\n\t"
    "global_load_lds_dwordx4 %[av3], %[s1] sc1\n\t"
    "s_waitcnt lgkmcnt(4)\n\t"
    "v_mfma_f32_16x16x32_bf16 v[80:83], v[176:179], v[112:115], v[80:83]\n\t"
    "ds_read_b128 v[104:107], %[da] offset:16384\n\t"
    "s_add_u32 m0, %[lb], 24576\n\t"
    "v_mfma_f32_16x16x32_bf16 v[84:87], v[180:183], v[112:115], v[84:87]\n\t"
    "ds_read_b128 v[108:111], %[da] offset:18432\n\t"
    "global_load_lds_dwordx4 %[av4], %[s1] sc1\n\t"
    "v_mfma_f32_16x16x32_bf16 v[88:91], v[176:179], v[116:119], v[88:91]\n\t"
    "s_add_u32 m0, %[lb], 57344\n\t"
    "v_mfma_f32_16x16x32_bf16 v[92:95], v[180:183], v[116:119], v[92:95]\n\t"
    "global_load_lds_dwordx4 %[av5], %[s1] sc1\n\t"
    "s_waitcnt lgkmcnt(4)\n\t"
    "v_mfma_f32_16x16x32_bf16 v[80:83], v[184:187], v[120:123], v[80:83]\n\t"
    "ds_read_b128 v[112:115], %[ea] offset:20480\n\t"
    "s_add_u32 m0, %[lb], 28672\n\t"
    "v_mfma_f32_16x16x32_bf16 v[84:87], v[188:191], v[120:123], v[84:87]\n\t"
    "ds_read_b128 v[116:119], %[ea] offset:22528\n\t"
    "global_load_lds_dwordx4 %[av6], %[s1] sc1\n\t"
    "v_mfma_f32_16x16x32_bf16 v[88:91], v[184:187], v[124:127], v[88:91]\n\t"
    "s_add_u32 m0, %[lb], 61440\n\t"
    "v_mfma_f32_16x16x32_bf16 v[92:95], v[188:191], v[124:127], v[92:95]\n\t"
    "global_load_lds_dwordx4 %[av7], %[s1] sc1\n\t"
    "s_waitcnt lgkmcnt(4)\n\t"
    "v_mfma_f32_16x16x32_bf16 v[80:83], v[192:195], v[96:99], v[80:83]\n\t"
    "ds_read_b128 v[120:123], %[da] offset:20480\n\t"
    "v_mfma_f32_16x16x32_bf16 v[84:87], v[196:199], v[96:99], v[84:87]\n\t"
    "ds_read_b128 v[124:127], %[da] offset:22528\n\t"
    "v_mfma_f32_16x16x32_bf16 v[88:91], v[192:195], v[100:103], v[88:91]\n\t"
    "v_mfma_f32_16x16x32_bf16 v[92:95], v[196:199], v[100:103], v[92:95]\n\t"
    "s_waitcnt vmcnt(16)\n\t"
    "s_barrier\n\t"
    "s_waitcnt lgkmcnt(4)\n\t"
    "v_mfma_f32_16x16x32_bf16 v[80:83], v[200:203], v[104:107], v[80:83]\n\t"
    "ds_read_b128 v[96:99], %[ea] offset:24576\n\t"
    "v_mfma_f32_16x16x32_bf16 v[84:87], v[204:207], v[104:107], v[84:87]\n\t"
    "ds_read_b128 v[100:103], %[ea] offset:26624\n\t"
    "v_mfma_f32_16x16x32_bf16 v[88:91], v[200:203], v[108:111], v[88:91]\n\t"
    "v_mfma_f32_16x16x32_bf16 v[92:95], v[204:207], v[108:111], v[92:95]\n\t"
    "s_waitcnt lgkmcnt(4)\n\t"
    "v_mfma_f32_16x16x32_bf16 v[80:83], v[208:211], v[112:115], v[80:83]\n\t"
    "ds_read_b128 v[104:107], %[da] offset:24576\n\t"
    "v_mfma_f32_16x16x32_bf16 v[84:87], v[212:215], v[112:115], v[84:87]\n\t"
    "ds_read_b128 v[108:111], %[da] offset:26624\n\t"
    "v_mfma_f32_16x16x32_bf16 v[88:91], v[208:211], v[116:119], v[88:91]\n\t"
    "v_mfma_f32_16x16x32_bf16 v[92:95], v[212:215], v[116:119], v[92:95]\n\t"
    "s_waitcnt lgkmcnt(4)\n\t"
    "v_mfma_f32_16x16x32_bf16 v[80:83], v[216:219], v[120:123], v[80:83]\n\t"
    "ds_read_b128 v[112:115], %[ea] offset:28672\n\t"
    "v_mfma_f32_16x16x32_bf16 v[84:87], v[220:223], v[120:123], v[84:87]\n\t"
    "ds_read_b128 v[116:119], %[ea] offset:30720\n\t"
    "v_mfma_f32_16x16x32_bf16 v[88:91], v[216:219], v[124:127], v[88:91]\n\t"
    "v_mfma_f32_16x16x32_bf16 v[92:95], v[220:223], v[124:127], v[92:95]\n\t"
    "s_waitcnt lgkmcnt(4)\n\t"
    "v_mfma_f32_16x16x32_bf16 v[80:83], v[224:227], v[96:99], v[80:83]\n\t"
    "ds_read_b128 v[120:123], %[da] offset:28672\n\t"
    "v_mfma_f32_16x16x32_bf16 v[84:87], v[228:231], v[96:99], v[84:87]\n\t"
    "ds_read_b128 v[124:127], %[da] offset:30720\n\t"
    "v_mfma_f32_16x16x32_bf16 v[88:91], v[224:227], v[100:103], v[88:91]\n\t"
    "v_mfma_f32_16x16x32_bf16 v[92:95], v[228:231], v[100:103], v[92:95]\n\t"
    "s_waitcnt lgkmcnt(4)\n\t"
    "v_mfma_f32_16x16x32_bf16 v[80:83], v[232:235], v[104:107], v[80:83]\n\t"
    "v_mfma_f32_16x16x32_bf16 v[84:87], v[236:239], v[104:107], v[84:87]\n\t"
    "v_mfma_f32_16x16x32_bf16 v[88:91], v[232:235], v[108:111], v[88:91]\n\t"
    "v_mfma_f32_16x16x32_bf16 v[92:95], v[236:239], v[108:111], v[92:95]\n\t"
    "s_waitcnt lgkmcnt(2)\n\t"
    "v_mfma_f32_16x16x32_bf16 v[80:83], v[240:243], v[112:115], v[80:83]\n\t"
    "v_mfma_f32_16x16x32_bf16 v[84:87], v[244:247], v[112:115], v[84:87]\n\t"
    "v_mfma_f32_16x16x32_bf16 v[88:91], v[240:243], v[116:119], v[88:91]\n\t"
    "v_mfma_f32_16x16x32_bf16 v[92:95], v[244:247], v[116:119], v[92:95]\n\t"
    "s_waitcnt lgkmcnt(0)\n\t"
    "v_mfma_f32_16x16x32_bf16 v[80:83], v[248:251], v[120:123], v[80:83]\n\t"
    "v_mfma_f32_16x16x32_bf16 v[84:87], v[252:255], v[120:123], v[84:87]\n\t"
    "v_mfma_f32_16x16x32_bf16 v[88:91], v[248:251], v[124:127], v[88:91]\n\t"
    "v_mfma_f32_16x16x32_bf16 v[92:95], v[252:255], v[124:127], v[92:95]\n\t"
    "s_nop 6\n\t"
    "s_branch 9f\n\t"
    "3:\n\t"
    "9:"
    : "=&{v[80:95]}"(pd)
    : [ea] "v"(ea), [da] "v"(da), [x] "s"(x), "{v[128:159]}"(W[0]), "{v[160:191]}"(W[1]), "{v[192:223]}"(W[2]), "{v[224:255]}"(W[3]), [lb] "s"(lb), [s0] "s"(s0), [s1] "s"(s1), [av0] "v"(av[0]), [av1] "v"(av[1]), [av2] "v"(av[2]), [av3] "v"(av[3]), [av4] "v"(av[4]), [av5] "v"(av[5]), [av6] "v"(av[6]), [av7] "v"(av[7])
    : "v96", "v97", "v98", "v99", "v100", "v101", "v102", "v103", "v104", "v105", "v106", "v107", "v108", "v109", "v110", "v111", "v112", "v113", "v114", "v115", "v116", "v117", "v118", "v119", "v120", "v121", "v122", "v123", "v124", "v125", "v126", "v127", "vcc", "scc", "m0", "memory");
}
__device__ __forceinline__ void lean_ld1(const u32x32 (&W)[4], u32x16& pd, unsigned ea, unsigned da, unsigned x, unsigned lb, const char* s0, const char* s1, const unsigned (&av)[8], unsigned sa, unsigned mu, unsigned ad, int th, unsigned s2, unsigned dv, const char* db) {
  asm volatile("s_setprio 2\n\t"
    "ds_read_b128 v[96:99], %[ea] offset:0\n\t"
    "ds_read_b128 v[100:103], %[ea] offset:2048\n\t"
    "ds_read_b128 v[104:107], %[da] offset:0\n\t"
    "ds_read_b128 v[108:111], %[da] offset:2048\n\t"
    "ds_read_b128 v[112:115], %[ea] offset:4096\n\t"
    "ds_read_b128 v[116:119], %[ea] offset:6144\n\t"
    "s_waitcnt lgkmcnt(4)\n\t"
    "v_mfma_f32_16x16x32_bf16 v[80:83], v[128:131], v[96:99], 0\n\t"
    "ds_read_b128 v[120:123], %[da] offset:4096\n\t"
    "v_mfma_f32_16x16x32_bf16 v[84:87], v[132:135], v[96:99], 0\n\t"
    "ds_read_b128 v[124:127], %[da] offset:6144\n\t"
    "v_mfma_f32_16x16x32_bf16 v[88:91], v[128:131], v[100:103], 0\n\t"
    "v_mfma_f32_16x16x32_bf16 v[92:95], v[132:135], v[100:103], 0\n\t"
    "s_waitcnt vmcnt(12)\n\t"
    "s_cmp_lt_u32 %[x], 2\n\t"
    "s_cbranch_scc0 41f\n\t"
    "s_waitcnt vmcnt(4)\n\t"
    "41:\n\t"
    "s_barrier\n\t"
    "s_waitcnt lgkmcnt(4)\n\t"
    "v_mfma_f32_16x16x32_bf16 v[80:83], v[136:139], v[104:107], v[80:83]\n\t"
    "ds_read_b128 v[96:99], %[ea] offset:8192\n\t"
    "v_mfma_f32_16x16x32_bf16 v[84:87], v[140:143], v[104:107], v[84:87]\n\t"
    "ds_read_b128 v[100:103], %[ea] offset:10240\n\t"
    "v_mfma_f32_16x16x32_bf16 v[88:91], v[136:139], v[108:111], v[88:91]\n\t"
    "v_mfma_f32_16x16x32_bf16 v[92:95], v[140:143], v[108:111], v[92:95]\n\t"
    "s_waitcnt lgkmcnt(4)\n\t"
    "v_mfma_f32_16x16x32_bf16 v[80:83], v[144:147], v[112:115], v[80:83]\n\t"
    "ds_read_b128 v[104:107], %[da] offset:8192\n\t"
    "v_mfma_f32_16x16x32_bf16 v[84:87], v[148:151], v[112:115], v[84:87]\n\t"
    "ds_read_b128 v[108:111], %[da] offset:10240\n\t"
    "v_mfma_f32_16x16x32_bf16 v[88:91], v[144:147], v[116:119], v[88:91]\n\t"
    "v_mfma_f32_16x16x32_bf16 v[92:95], v[148:151], v[116:119], v[92:95]\n\t"
    "s_waitcnt lgkmcnt(4)\n\t"
    "v_mfma_f32_16x16x32_bf16 v[80:83], v[152:155], v[120:123], v[80:83]\n\t"
    "ds_read_b128 v[112:115], %[ea] offset:12288\n\t"
    "v_mfma_f32_16x16x32_bf16 v[84:87], v[156:159], v[120:123], v[84:87]\n\t"
    "ds_read_b128 v[116:119], %[ea] offset:14336\n\t"
    "v_mfma_f32_16x16x32_bf16 v[88:91], v[152:155], v[124:127], v[88:91]\n\t"
    "v_mfma_f32_16x16x32_bf16 v[92:95], v[156:159], v[124:127], v[92:95]\n\t"
    "s_waitcnt lgkmcnt(4)\n\t"
    "v_mfma_f32_16x16x32_bf16 v[80:83], v[160:163], v[96:99], v[80:83]\n\t"
    "ds_read_b128 v[120:123], %[da] offset:12288\n\t"
    "v_mfma_f32_16x16x32_bf16 v[84:87], v[164:167], v[96:99], v[84:87]\n\t"
    "ds_read_b128 v[124:127], %[da] offset:14336\n\t"
    "v_mfma_f32_16x16x32_bf16 v[88:91], v[160:163], v[100:103], v[88:91]\n\t"
    "v_mfma_f32_16x16x32_bf16 v[92:95], v[164:167], v[100:103], v[92:95]\n\t"
    "s_waitcnt vmcnt(10)\n\t"
    "s_cmp_lt_u32 %[x], 2\n\t"
    "s_cbranch_scc0 45f\n\t"
    "s_waitcnt vmcnt(2)\n\t"
    "45:\n\t"
    "s_barrier\n\t"
    "s_waitcnt lgkmcnt(4)\n\t"
    "v_mfma_f32_16x16x32_bf16 v[80:83], v[168:171], v[104:107], v[80:83]\n\t"
    "ds_read_b128 v[96:99], %[ea] offset:16384\n\t"
    "v_mfma_f32_16x16x32_bf16 v[84:87], v[172:175], v[104:107], v[84:87]\n\t"
    "ds_read_b128 v[100:103], %[ea] offset:18432\n\t"
    "v_mfma_f32_16x16x32_bf16 v[88:91], v[168:171], v[108:111], v[88:91]\n\t"
    "v_mfma_f32_16x16x32_bf16 v[92:95], v[172:175], v[108:111], v[92:95]\n\t"
    "s_waitcnt lgkmcnt(4)\n\t"
    "v_mfma_f32_16x16x32_bf16 v[80:83], v[176:179], v[112:115], v[80:83]\n\t"
    "ds_read_b128 v[104:107], %[da] offset:16384\n\t"
    "v_mfma_f32_16x16x32_bf16 v[84:87], v[180:183], v[112:115], v[84:87]\n\t"
    "ds_read_b128 v[108:111], %[da] offset:18432\n\t"
    "v_mfma_f32_16x16x32_bf16 v[88:91], v[176:179], v[116:119], v[88:91]\n\t"
    "v_mfma_f32_16x16x32_bf16 v[92:95], v[180:183], v[116:119], v[92:95]\n\t"
    "s_waitcnt lgkmcnt(4)\n\t"
    "v_mfma_f32_16x16x32_bf16 v[80:83], v[184:187], v[120:123], v[80:83]\n\t"
    "ds_read_b128 v[112:115], %[ea] offset:20480\n\t"
    "v_mfma_f32_16x16x32_bf16 v[84:87], v[188:191], v[120:123], v[84:87]\n\t"
    "ds_read_b128 v[116:119], %[ea] offset:22528\n\t"
    "v_mfma_f32_16x16x32_bf16 v[88:91], v[184:187], v[124:127], v[88:91]\n\t"
    "v_mfma_f32_16x16x32_bf16 v[92:95], v[188:191], v[124:127], v[92:95]\n\t"
    "s_waitcnt lgkmcnt(4)\n\t"
    "v_mfma_f32_16x16x32_bf16 v[80:83], v[192:195], v[96:99], v[80:83]\n\t"
    "ds_read_b128 v[120:123], %[da] offset:20480\n\t"
    "v_mfma_f32_16x16x32_bf16 v[84:87], v[196:199], v[96:99], v[84:87]\n\t"
    "ds_read_b128 v[124:127], %[da] offset:22528\n\t"
    "v_mfma_f32_16x16x32_bf16 v[88:91], v[192:195], v[100:103], v[88:91]\n\t"
    "v_mfma_f32_16x16x32_bf16 v[92:95], v[196:199], v[100:103], v[92:95]\n\t"
    "s_waitcnt vmcnt(8)\n\t"
    "s_cmp_lt_u32 %[x], 2\n\t"
    "s_cbranch_scc0 49f\n\t"
    "s_waitcnt vmcnt(0)\n\t"
    "49:\n\t"
    "s_barrier\n\t"
    "ds_read_b32 v79, %[sa]\n\t"
    "s_waitcnt lgkmcnt(5)\n\t"
    "v_mfma_f32_16x16x32_bf16 v[80:83], v[200:203], v[104:107], v[80:83]\n\t"
    "ds_read_b128 v[96:99], %[ea] offset:24576\n\t"
    "v_mfma_f32_16x16x32_bf16 v[84:87], v[204:207], v[104:107], v[84:87]\n\t"
    "ds_read_b128 v[100:103], %[ea] offset:26624\n\t"
    "v_mfma_f32_16x16x32_bf16 v[88:91], v[200:203], v[108:111], v[88:91]\n\t"
    "v_mfma_f32_16x16x32_bf16 v[92:95], v[204:207], v[108:111], v[92:95]\n\t"
    "s_waitcnt lgkmcnt(5)\n\t"
    "v_mfma_f32_16x16x32_bf16 v[80:83], v[208:211], v[112:115], v[80:83]\n\t"
    "ds_read_b128 v[104:107], %[da] offset:24576\n\t"
    "v_mfma_f32_16x16x32_bf16 v[84:87], v[212:215], v[112:115], v[84:87]\n\t"
    "ds_read_b128 v[108:111], %[da] offset:26624\n\t"
    "v_mfma_f32_16x16x32_bf16 v[88:91], v[208:211], v[116:119], v[88:91]\n\t"
    "v_mfma_f32_16x16x32_bf16 v[92:95], v[212:215], v[116:119], v[92:95]\n\t"
    "s_cmp_eq_u32 %[s2], 0\n\t"
    "s_cbranch_scc1 7f\n\t"
    "s_mov_b32 m0, %[s2]\n\t"
    "s_nop 0\n\t"
    "global_load_lds_dword %[dv], %[db] sc1\n\t"
    "7:\n\t"
    "s_waitcnt lgkmcnt(4)\n\t"
    "v_mfma_f32_16x16x32_bf16 v[80:83], v[216:219], v[120:123], v[80:83]\n\t"
    "ds_read_b128 v[112:115], %[ea] offset:28672\n\t"
    "v_mfma_f32_16x16x32_bf16 v[84:87], v[220:223], v[120:123], v[84:87]\n\t"
    "ds_read_b128 v[116:119], %[ea] offset:30720\n\t"
    "v_mfma_f32_16x16x32_bf16 v[88:91], v[216:219], v[124:127], v[88:91]\n\t"
    "v_mad_u32_u24 v79, v79, %[mu], %[ad]\n\t"
    "v_cmp_ge_i32 vcc, %[th], v79\n\t"
    "v_mfma_f32_16x16x32_bf16 v[92:95], v[220:223], v[124:127], v[92:95]\n\t"
    "s_cbranch_vccz 5f\n\t"
    "s_waitcnt lgkmcnt(4)\n\t"
    "v_mfma_f32_16x16x32_bf16 v[80:83], v[224:227], v[96:99], v[80:83]\n\t"
    "ds_read_b128 v[120:123], %[da] offset:28672\n\t"
    "v_mfma_f32_16x16x32_bf16 v[84:87], v[228:231], v[96:99], v[84:87]\n\t"
    "ds_read_b128 v[124:127], %[da] offset:30720\n\t"
    "v_mfma_f32_16x16x32_bf16 v[88:91], v[224:227], v[100:103], v[88:91]\n\t"
    "v_mfma_f32_16x16x32_bf16 v[92:95], v[228:231], v[100:103], v[92:95]\n\t"
    "s_waitcnt lgkmcnt(4)\n\t"
    "v_mfma_f32_16x16x32_bf16 v[80:83], v[232:235], v[104:107], v[80:83]\n\t"
    "v_mfma_f32_16x16x32_bf16 v[84:87], v[236:239], v[104:107], v[84:87]\n\t"
    "v_mfma_f32_16x16x32_bf16 v[88:91], v[232:235], v[108:111], v[88:91]\n\t"
    "v_mfma_f32_16x16x32_bf16 v[92:95], v[236:239], v[108:111], v[92:95]\n\t"
    "s_waitcnt lgkmcnt(2)\n\t"
    "v_mfma_f32_16x16x32_bf16 v[80:83], v[240:243], v[112:115], v[80:83]\n\t"
    "v_mfma_f32_16x16x32_bf16 v[84:87], v[244:247], v[112:115], v[84:87]\n\t"
    "v_mfma_f32_16x16x32_bf16 v[88:91], v[240:243], v[116:119], v[88:91]\n\t"
    "v_mfma_f32_16x16x32_bf16 v[92:95], v[244:247], v[116:119], v[92:95]\n\t"
    "s_waitcnt lgkmcnt(0)\n\t"
    "v_mfma_f32_16x16x32_bf16 v[80:83], v[248:251], v[120:123], v[80:83]\n\t"
    "v_mfma_f32_16x16x32_bf16 v[84:87], v[252:255], v[120:123], v[84:87]\n\t"
    "v_mfma_f32_16x16x32_bf16 v[88:91], v[248:251], v[124:127], v[88:91]\n\t"
    "v_mfma_f32_16x16x32_bf16 v[92:95], v[252:255], v[124:127], v[92:95]\n\t"
    "s_branch 6f\n\t"
    "5:\n\t"
    "s_waitcnt lgkmcnt(4)\n\t"
    "v_mfma_f32_16x16x32_bf16 v[80:83], v[224:227], v[96:99], v[80:83]\n\t"
    "ds_read_b128 v[120:123], %[da] offset:28672\n\t"
    "s_add_u32 m0, %[lb], 0\n\t"
    "s_nop 0\n\t"
    "global_load_lds_dwordx4 %[av0], %[s0] sc1\n\t"
    "v_mfma_f32_16x16x32_bf16 v[84:87], v[228:231], v[96:99], v[84:87]\n\t"
    "ds_read_b128 v[124:127], %[da] offset:30720\n\t"
    "v_mfma_f32_16x16x32_bf16 v[88:91], v[224:227], v[100:103], v[88:91]\n\t"
    "s_add_u32 m0, %[lb], 4096\n\t"
    "s_nop 0\n\t"
    "global_load_lds_dwordx4 %[av2], %[s0] sc1\n\t"
    "v_mfma_f32_16x16x32_bf16 v[92:95], v[228:231], v[100:103], v[92:95]\n\t"
    "s_waitcnt lgkmcnt(4)\n\t"
    "v_mfma_f32_16x16x32_bf16 v[80:83], v[232:235], v[104:107], v[80:83]\n\t"
    "s_add_u32 m0, %[lb], 8192\n\t"
    "s_nop 0\n\t"
    "global_load_lds_dwordx4 %[av4], %[s0] sc1\n\t"
    "v_mfma_f32_16x16x32_bf16 v[84:87], v[236:239], v[104:107], v[84:87]\n\t"
    "v_mfma_f32_16x16x32_bf16 v[88:91], v[232:235], v[108:111], v[88:91]\n\t"
    "s_add_u32 m0, %[lb], 12288\n\t"
    "s_nop 0\n\t"
    "global_load_lds_dwordx4 %[av6], %[s0] sc1\n\t"
    "v_mfma_f32_16x16x32_bf16 v[92:95], v[236:239], v[108:111], v[92:95]\n\t"
    "s_waitcnt lgkmcnt(2)\n\t"
    "v_mfma_f32_16x16x32_bf16 v[80:83], v[240:243], v[112:115], v[80:83]\n\t"
    "s_add_u32 m0, %[lb], 16384\n\t"
    "s_nop 0\n\t"
    "global_load_lds_dwordx4 %[av0], %[s1] sc1\n\t"
    "v_mfma_f32_16x16x32_bf16 v[84:87], v[244:247], v[112:115], v[84:87]\n\t"
    "v_mfma_f32_16x16x32_bf16 v[88:91], v[240:243], v[116:119], v[88:91]\n\t"
    "s_add_u32 m0, %[lb], 20480\n\t"
    "s_nop 0\n\t"
    "global_load_lds_dwordx4 %[av2], %[s1] sc1\n\t"
    "v_mfma_f32_16x16x32_bf16 v[92:95], v[244:247], v[116:119], v[92:95]\n\t"
    "s_waitcnt lgkmcnt(0)\n\t"
    "v_mfma_f32_16x16x32_bf16 v[80:83], v[248:251], v[120:123], v[80:83]\n\t"
    "s_add_u32 m0, %[lb], 24576\n\t"
    "s_nop 0\n\t"
    "global_load_lds_dwordx4 %[av4], %[s1] sc1\n\t"
    "v_mfma_f32_16x16x32_bf16 v[84:87], v[252:255], v[120:123], v[84:87]\n\t"
    "v_mfma_f32_16x16x32_bf16 v[88:91], v[248:251], v[124:127], v[88:91]\n\t"
    "s_add_u32 m0, %[lb], 28672\n\t"
    "s_nop 0\n\t"
    "global_load_lds_dwordx4 %[av6], %[s1] sc1\n\t"
    "v_mfma_f32_16x16x32_bf16 v[92:95], v[252:255], v[124:127], v[92:95]\n\t"
    "6:\n\t"
    "s_nop 6\n\t"
    "s_branch 9f\n\t"
    "3:\n\t"
    "9:"
    : "=&{v[80:95]}"(pd)
    : [ea] "v"(ea), [da] "v"(da), [x] "s"(x), "{v[128:159]}"(W[0]), "{v[160:191]}"(W[1]), "{v[192:223]}"(W[2]), "{v[224:255]}"(W[3]), [lb] "s"(lb), [s0] "s"(s0), [s1] "s"(s1), [av0] "v"(av[0]), [av1] "v"(av[1]), [av2] "v"(av[2]), [av3] "v"(av[3]), [av4] "v"(av[4]), [av5] "v"(av[5]), [av6] "v"(av[6]), [av7] "v"(av[7]), [sa] "v"(sa), [mu] "s"(mu), [ad] "v"(ad), [th] "s"(th), [s2] "s"(s2), [dv] "v"(dv), [db] "s"(db)
    : "v96", "v97", "v98", "v99", "v100", "v101", "v102", "v103", "v104", "v105", "v106", "v107", "v108", "v109", "v110", "v111", "v112", "v113", "v114", "v115", "v116", "v117", "v118", "v119", "v120", "v121", "v122", "v123", "v124", "v125", "v126", "v127", "v79", "vcc", "scc", "m0", "memory");
}
__device__ __forceinline__ void lean_dm1(const u32x32 (&W)[4], u32x16& pd, unsigned ea, unsigned da, unsigned x, unsigned lb, const char* s0, const char* s1, const unsigned (&av)[8]) {
  asm volatile("s_setprio 2\n\t"
    "ds_read_b128 v[96:99], %[ea] offset:0\n\t"
    "ds_read_b128 v[100:103], %[ea] offset:2048\n\t"
    "ds_read_b128 v[104:107], %[da] offset:0\n\t"
    "ds_read_b128 v[108:111], %[da] offset:2048\n\t"
    "ds_read_b128 v[112:115], %[ea] offset:4096\n\t"
    "ds_read_b128 v[116:119], %[ea] offset:6144\n\t"
    "s_waitcnt lgkmcnt(4)\n\t"
    "v_mfma_f32_16x16x32_bf16 v[80:83], v[128:131], v[96:99], 0\n\t"
    "ds_read_b128 v[120:123], %[da] offset:4096\n\t"
    "s_add_u32 m0, %[lb], 0\n\t"
    "v_mfma_f32_16x16x32_bf16 v[84:87], v[132:135], v[96:99], 0\n\t"
    "ds_read_b128 v[124:127], %[da] offset:6144\n\t"
    "global_load_lds_dwordx4 %[av0], %[s0] sc1\n\t"
    "v_mfma_f32_16x16x32_bf16 v[88:91], v[128:131], v[100:103], 0\n\t"
    "v_mfma_f32_16x16x32_bf16 v[92:95], v[132:135], v[100:103], 0\n\t"
    "s_waitcnt vmcnt(5)\n\t"
    "s_barrier\n\t"
    "s_waitcnt lgkmcnt(4)\n\t"
    "v_mfma_f32_16x16x32_bf16 v[80:83], v[136:139], v[104:107], v[80:83]\n\t"
    "ds_read_b128 v[96:99], %[ea] offset:8192\n\t"
    "s_add_u32 m0, %[lb], 4096\n\t"
    "v_mfma_f32_16x16x32_bf16 v[84:87], v[140:143], v[104:107], v[84:87]\n\t"
    "ds_read_b128 v[100:103], %[ea] offset:10240\n\t"
    "global_load_lds_dwordx4 %[av2], %[s0] sc1\n\t"
    "v_mfma_f32_16x16x32_bf16 v[88:91], v[136:139], v[108:111], v[88:91]\n\t"
    "v_mfma_f32_16x16x32_bf16 v[92:95], v[140:143], v[108:111], v[92:95]\n\t"
    "s_waitcnt lgkmcnt(4)\n\t"
    "v_mfma_f32_16x16x32_bf16 v[80:83], v[144:147], v[112:115], v[80:83]\n\t"
    "ds_read_b128 v[104:107], %[da] offset:8192\n\t"
    "s_add_u32 m0, %[lb], 8192\n\t"
    "v_mfma_f32_16x16x32_bf16 v[84:87], v[148:151], v[112:115], v[84:87]\n\t"
    "ds_read_b128 v[108:111], %[da] offset:10240\n\t"
    "global_load_lds_dwordx4 %[av4], %[s0] sc1\n\t"
    "v_mfma_f32_16x16x32_bf16 v[88:91], v[144:147], v[116:119], v[88:91]\n\t"
    "v_mfma_f32_16x16x32_bf16 v[92:95], v[148:151], v[116:119], v[92:95]\n\t"
    "s_waitcnt lgkmcnt(4)\n\t"
    "v_mfma_f32_16x16x32_bf16 v[80:83], v[152:155], v[120:123], v[80:83]\n\t"
    "ds_read_b128 v[112:115], %[ea] offset:12288\n\t"
    "s_add_u32 m0, %[lb], 12288\n\t"
    "v_mfma_f32_16x16x32_bf16 v[84:87], v[156:159], v[120:123], v[84:87]\n\t"
    "ds_read_b128 v[116:119], %[ea] offset:14336\n\t"
    "global_load_lds_dwordx4 %[av6], %[s0] sc1\n\t"
    "v_mfma_f32_16x16x32_bf16 v[88:91], v[152:155], v[124:127], v[88:91]\n\t"
    "v_mfma_f32_16x16x32_bf16 v[92:95], v[156:159], v[124:127], v[92:95]\n\t"
    "s_waitcnt lgkmcnt(4)\n\t"
    "v_mfma_f32_16x16x32_bf16 v[80:83], v[160:163], v[96:99], v[80:83]\n\t"
    "ds_read_b128 v[120:123], %[da] offset:12288\n\t"
    "s_add_u32 m0, %[lb], 16384\n\t"
    "v_mfma_f32_16x16x32_bf16 v[84:87], v[164:167], v[96:99], v[84:87]\n\t"
    "ds_read_b128 v[124:127], %[da] offset:14336\n\t"
    "global_load_lds_dwordx4 %[av0], %[s1] sc1\n\t"
    "v_mfma_f32_16x16x32_bf16 v[88:91], v[160:163], v[100:103], v[88:91]\n\t"
    "v_mfma_f32_16x16x32_bf16 v[92:95], v[164:167], v[100:103], v[92:95]\n\t"
    "s_waitcnt vmcnt(7)\n\t"
    "s_barrier\n\t"
    "s_waitcnt lgkmcnt(4)\n\t"
    "v_mfma_f32_16x16x32_bf16 v[80:83], v[168:171], v[104:107], v[80:83]\n\t"
    "ds_read_b128 v[96:99], %[ea] offset:16384\n\t"
    "s_add_u32 m0, %[lb], 20480\n\t"
    "v_mfma_f32_16x16x32_bf16 v[84:87], v[172:175], v[104:107], v[84:87]\n\t"
    "ds_read_b128 v[100:103], %[ea] offset:18432\n\t"
    "global_load_lds_dwordx4 %[av2], %[s1] sc1\n\t"
    "v_mfma_f32_16x16x32_bf16 v[88:91], v[168:171], v[108:111], v[88:91]\n\t"
    "v_mfma_f32_16x16x32_bf16 v[92:95], v[172:175], v[108:111], v[92:95]\n\t"
    "s_waitcnt lgkmcnt(4)\n\t"
    "v_mfma_f32_16x16x32_bf16 v[80:83], v[176:179], v[112:115], v[80:83]\n\t"
    "ds_read_b128 v[104:107], %[da] offset:16384\n\t"
    "s_add_u32 m0, %[lb], 24576\n\t"
    "v_mfma_f32_16x16x32_bf16 v[84:87], v[180:183], v[112:115], v[84:87]\n\t"
    "ds_read_b128 v[108:111], %[da] offset:18432\n\t"
    "global_load_lds_dwordx4 %[av4], %[s1] sc1\n\t"
    "v_mfma_f32_16x16x32_bf16 v[88:91], v[176:179], v[116:119], v[88:91]\n\t"
    "v_mfma_f32_16x16x32_bf16 v[92:95], v[180:183], v[116:119], v[92:95]\n\t"
    "s_waitcnt lgkmcnt(4)\n\t"
    "v_mfma_f32_16x16x32_bf16 v[80:83], v[184:187], v[120:123], v[80:83]\n\t"
    "ds_read_b128 v[112:115], %[ea] offset:20480\n\t"
    "s_add_u32 m0, %[lb], 28672\n\t"
    "v_mfma_f32_16x16x32_bf16 v[84:87], v[188:191], v[120:123], v[84:87]\n\t"
    "ds_read_b128 v[116:119], %[ea] offset:22528\n\t"
    "global_load_lds_dwordx4 %[av6], %[s1] sc1\n\t"
    "v_mfma_f32_16x16x32_bf16 v[88:91], v[184:187], v[124:127], v[88:91]\n\t"
    "v_mfma_f32_16x16x32_bf16 v[92:95], v[188:191], v[124:127], v[92:95]\n\t"
    "s_waitcnt lgkmcnt(4)\n\t"
    "v_mfma_f32_16x16x32_bf16 v[80:83], v[192:195], v[96:99], v[80:83]\n\t"
    "ds_read_b128 v[120:123], %[da] offset:20480\n\t"
    "v_mfma_f32_16x16x32_bf16 v[84:87], v[196:199], v[96:99], v[84:87]\n\t"
    "ds_read_b128 v[124:127], %[da] offset:22528\n\t"
    "v_mfma_f32_16x16x32_bf16 v[88:91], v[192:195], v[100:103], v[88:91]\n\t"
    "v_mfma_f32_16x16x32_bf16 v[92:95], v[196:199], v[100:103], v[92:95]\n\t"
    "s_waitcnt vmcnt(8)\n\t"
    "s_barrier\n\t"
    "s_waitcnt lgkmcnt(4)\n\t"
    "v_mfma_f32_16x16x32_bf16 v[80:83], v[200:203], v[104:107], v[80:83]\n\t"
    "ds_read_b128 v[96:99], %[ea] offset:24576\n\t"
    "v_mfma_f32_16x16x32_bf16 v[84:87], v[204:207], v[104:107], v[84:87]\n\t"
    "ds_read_b128 v[100:103], %[ea] offset:26624\n\t"
    "v_mfma_f32_16x16x32_bf16 v[88:91], v[200:203], v[108:111], v[88:91]\n\t"
    "v_mfma_f32_16x16x32_bf16 v[92:95], v[204:207], v[108:111], v[92:95]\n\t"
    "s_waitcnt lgkmcnt(4)\n\t"
    "v_mfma_f32_16x16x32_bf16 v[80:83], v[208:211], v[112:115], v[80:83]\n\t"
    "ds_read_b128 v[104:107], %[da] offset:24576\n\t"
    "v_mfma_f32_16x16x32_bf16 v[84:87], v[212:215], v[112:115], v[84:87]\n\t"
    "ds_read_b128 v[108:111], %[da] offset:26624\n\t"
    "v_mfma_f32_16x16x32_bf16 v[88:91], v[208:211], v[116:119], v[88:91]\n\t"
    "v_mfma_f32_16x16x32_bf16 v[92:95], v[212:215], v[116:119], v[92:95]\n\t"
    "s_waitcnt lgkmcnt(4)\n\t"
    "v_mfma_f32_16x16x32_bf16 v[80:83], v[216:219], v[120:123], v[80:83]\n\t"
    "ds_read_b128 v[112:115], %[ea] offset:28672\n\t"
    "v_mfma_f32_16x16x32_bf16 v[84:87], v[220:223], v[120:123], v[84:87]\n\t"
    "ds_read_b128 v[116:119], %[ea] offset:30720\n\t"
    "v_mfma_f32_16x16x32_bf16 v[88:91], v[216:219], v[124:127], v[88:91]\n\t"
    "v_mfma_f32_16x16x32_bf16 v[92:95], v[220:223], v[124:127], v[92:95]\n\t"
    "s_waitcnt lgkmcnt(4)\n\t"
    "v_mfma_f32_16x16x32_bf16 v[80:83], v[224:227], v[96:99], v[80:83]\n\t"
    "ds_read_b128 v[120:123], %[da] offset:28672\n\t"
    "v_mfma_f32_16x16x32_bf16 v[84:87], v[228:231], v[96:99], v[84:87]\n\t"
    "ds_read_b128 v[124:127], %[da] offset:30720\n\t"
    "v_mfma_f32_16x16x32_bf16 v[88:91], v[224:227], v[100:103], v[88:91]\n\t"
    "v_mfma_f32_16x16x32_bf16 v[92:95], v[228:231], v[100:103], v[92:95]\n\t"
    "s_waitcnt lgkmcnt(4)\n\t"
    "v_mfma_f32_16x16x32_bf16 v[80:83], v[232:235], v[104:107], v[80:83]\n\t"
    "v_mfma_f32_16x16x32_bf16 v[84:87], v[236:239], v[104:107], v[84:87]\n\t"
    "v_mfma_f32_16x16x32_bf16 v[88:91], v[232:235], v[108:111], v[88:91]\n\t"
    "v_mfma_f32_16x16x32_bf16 v[92:95], v[236:239], v[108:111], v[92:95]\n\t"
    "s_waitcnt lgkmcnt(2)\n\t"
    "v_mfma_f32_16x16x32_bf16 v[80:83], v[240:243], v[112:115], v[80:83]\n\t"
    "v_mfma_f32_16x16x32_bf16 v[84:87], v[244:247], v[112:115], v[84:87]\n\t"
    "v_mfma_f32_16x16x32_bf16 v[88:91], v[240:243], v[116:119], v[88:91]\n\t"
    "v_mfma_f32_16x16x32_bf16 v[92:95], v[244:247], v[116:119], v[92:95]\n\t"
    "s_waitcnt lgkmcnt(0)\n\t"
    "v_mfma_f32_16x16x32_bf16 v[80:83], v[248:251], v[120:123], v[80:83]\n\t"
    "v_mfma_f32_16x16x32_bf16 v[84:87], v[252:255], v[120:123], v[84:87]\n\t"
    "v_mfma_f32_16x16x32_bf16 v[88:91], v[248:251], v[124:127], v[88:91]\n\t"
    "v_mfma_f32_16x16x32_bf16 v[92:95], v[252:255], v[124:127], v[92:95]\n\t"
    "s_nop 6\n\t"
    "s_branch 9f\n\t"
    "3:\n\t"
    "9:"
    : "=&{v[80:95]}"(pd)
    : [ea] "v"(ea), [da] "v"(da), [x] "s"(x), "{v[128:159]}"(W[0]), "{v[160:191]}"(W[1]), "{v[192:223]}"(W[2]), "{v[224:255]}"(W[3]), [lb] "s"(lb), [s0] "s"(s0), [s1] "s"(s1), [av0] "v"(av[0]), [av1] "v"(av[1]), [av2] "v"(av[2]), [av3] "v"(av[3]), [av4] "v"(av[4]), [av5] "v"(av[5]), [av6] "v"(av[6]), [av7] "v"(av[7])
    : "v96", "v97", "v98", "v99", "v100", "v101", "v102", "v103", "v104", "v105", "v106", "v107", "v108", "v109", "v110", "v111", "v112", "v113", "v114", "v115", "v116", "v117", "v118", "v119", "v120", "v121", "v122", "v123", "v124", "v125", "v126", "v127", "vcc", "scc", "m0", "memory");
}
__device__ __forceinline__ void lean_st0(const u32x32 (&W)[4], u32x16& pd, unsigned ea, unsigned da, unsigned x, unsigned pv, unsigned cn, unsigned sl, unsigned dv, const char* db, const char* pg) {
  unsigned st;
  asm volatile("ds_read_b128 v[96:99], %[ea] offset:0\n\t"
    "ds_read_b128 v[100:103], %[ea] offset:2048\n\t"
    "ds_read_b128 v[104:107], %[da] offset:0\n\t"
    "ds_read_b128 v[108:111], %[da] offset:2048\n\t"
    "ds_read_b128 v[112:115], %[ea] offset:4096\n\t"
    "ds_read_b128 v[116:119], %[ea] offset:6144\n\t"
    "s_waitcnt lgkmcnt(4)\n\t"
    "v_mfma_f32_16x16x32_bf16 v[80:83], v[128:131], v[96:99], 0\n\t"
    "ds_read_b128 v[120:123], %[da] offset:4096\n\t"
    "v_mfma_f32_16x16x32_bf16 v[84:87], v[132:135], v[96:99], 0\n\t"
    "ds_read_b128 v[124:127], %[da] offset:6144\n\t"
    "v_mfma_f32_16x16x32_bf16 v[88:91], v[128:131], v[100:103], 0\n\t"
    "v_mfma_f32_16x16x32_bf16 v[92:95], v[132:135], v[100:103], 0\n\t"
    "s_barrier\n\t"
    "s_waitcnt lgkmcnt(4)\n\t"
    "v_mfma_f32_16x16x32_bf16 v[80:83], v[136:139], v[104:107], v[80:83]\n\t"
    "ds_read_b128 v[96:99], %[ea] offset:8192\n\t"
    "v_mfma_f32_16x16x32_bf16 v[84:87], v[140:143], v[104:107], v[84:87]\n\t"
    "ds_read_b128 v[100:103], %[ea] offset:10240\n\t"
    "v_mfma_f32_16x16x32_bf16 v[88:91], v[136:139], v[108:111], v[88:91]\n\t"
    "v_mfma_f32_16x16x32_bf16 v[92:95], v[140:143], v[108:111], v[92:95]\n\t"
    "s_waitcnt lgkmcnt(4)\n\t"
    "v_mfma_f32_16x16x32_bf16 v[80:83], v[144:147], v[112:115], v[80:83]\n\t"
    "ds_read_b128 v[104:107], %[da] offset:8192\n\t"
    "v_mfma_f32_16x16x32_bf16 v[84:87], v[148:151], v[112:115], v[84:87]\n\t"
    "ds_read_b128 v[108:111], %[da] offset:10240\n\t"
    "v_mfma_f32_16x16x32_bf16 v[88:91], v[144:147], v[116:119], v[88:91]\n\t"
    "v_mfma_f32_16x16x32_bf16 v[92:95], v[148:151], v[116:119], v[92:95]\n\t"
    "s_waitcnt lgkmcnt(4)\n\t"
    "v_mfma_f32_16x16x32_bf16 v[80:83], v[152:155], v[120:123], v[80:83]\n\t"
    "ds_read_b128 v[112:115], %[ea] offset:12288\n\t"
    "v_mfma_f32_16x16x32_bf16 v[84:87], v[156:159], v[120:123], v[84:87]\n\t"
    "ds_read_b128 v[116:119], %[ea] offset:14336\n\t"
    "v_mfma_f32_16x16x32_bf16 v[88:91], v[152:155], v[124:127], v[88:91]\n\t"
    "v_mfma_f32_16x16x32_bf16 v[92:95], v[156:159], v[124:127], v[92:95]\n\t"
    "s_bitcmp1_b32 %[x], 2\n\t"
    "s_cbranch_scc1 7f\n\t"
    "8:\n\t"
    "s_waitcnt lgkmcnt(4)\n\t"
    "v_mfma_f32_16x16x32_bf16 v[80:83], v[160:163], v[96:99], v[80:83]\n\t"
    "ds_read_b128 v[120:123], %[da] offset:12288\n\t"
    "v_mfma_f32_16x16x32_bf16 v[84:87], v[164:167], v[96:99], v[84:87]\n\t"
    "ds_read_b128 v[124:127], %[da] offset:14336\n\t"
    "v_mfma_f32_16x16x32_bf16 v[88:91], v[160:163], v[100:103], v[88:91]\n\t"
    "v_mfma_f32_16x16x32_bf16 v[92:95], v[164:167], v[100:103], v[92:95]\n\t"
    "s_barrier\n\t"
    "s_waitcnt lgkmcnt(4)\n\t"
    "v_mfma_f32_16x16x32_bf16 v[80:83], v[168:171], v[104:107], v[80:83]\n\t"
    "ds_read_b128 v[96:99], %[ea] offset:16384\n\t"
    "v_mfma_f32_16x16x32_bf16 v[84:87], v[172:175], v[104:107], v[84:87]\n\t"
    "ds_read_b128 v[100:103], %[ea] offset:18432\n\t"
    "v_mfma_f32_16x16x32_bf16 v[88:91], v[168:171], v[108:111], v[88:91]\n\t"
    "v_mfma_f32_16x16x32_bf16 v[92:95], v[172:175], v[108:111], v[92:95]\n\t"
    "s_waitcnt lgkmcnt(4)\n\t"
    "v_mfma_f32_16x16x32_bf16 v[80:83], v[176:179], v[112:115], v[80:83]\n\t"
    "ds_read_b128 v[104:107], %[da] offset:16384\n\t"
    "v_mfma_f32_16x16x32_bf16 v[84:87], v[180:183], v[112:115], v[84:87]\n\t"
    "ds_read_b128 v[108:111], %[da] offset:18432\n\t"
    "v_mfma_f32_16x16x32_bf16 v[88:91], v[176:179], v[116:119], v[88:91]\n\t"
    "v_mfma_f32_16x16x32_bf16 v[92:95], v[180:183], v[116:119], v[92:95]\n\t"
    "s_waitcnt lgkmcnt(4)\n\t"
    "v_mfma_f32_16x16x32_bf16 v[80:83], v[184:187], v[120:123], v[80:83]\n\t"
    "ds_read_b128 v[112:115], %[ea] offset:20480\n\t"
    "v_mfma_f32_16x16x32_bf16 v[84:87], v[188:191], v[120:123], v[84:87]\n\t"
    "ds_read_b128 v[116:119], %[ea] offset:22528\n\t"
    "v_mfma_f32_16x16x32_bf16 v[88:91], v[184:187], v[124:127], v[88:91]\n\t"
    "v_mfma_f32_16x16x32_bf16 v[92:95], v[188:191], v[124:127], v[92:95]\n\t"
    "s_waitcnt lgkmcnt(4)\n\t"
    "v_mfma_f32_16x16x32_bf16 v[80:83], v[192:195], v[96:99], v[80:83]\n\t"
    "ds_read_b128 v[120:123], %[da] offset:20480\n\t"
    "v_mfma_f32_16x16x32_bf16 v[84:87], v[196:199], v[96:99], v[84:87]\n\t"
    "ds_read_b128 v[124:127], %[da] offset:22528\n\t"
    "v_mfma_f32_16x16x32_bf16 v[88:91], v[192:195], v[100:103], v[88:91]\n\t"
    "v_mfma_f32_16x16x32_bf16 v[92:95], v[196:199], v[100:103], v[92:95]\n\t"
    "s_barrier\n\t"
    "s_and_b32 %[st], %[x], 3\n\t"
    "s_cbranch_scc1 3f\n\t"
    "4:\n\t"
    "s_waitcnt lgkmcnt(4)\n\t"
    "v_mfma_f32_16x16x32_bf16 v[80:83], v[200:203], v[104:107], v[80:83]\n\t"
    "ds_read_b128 v[96:99], %[ea] offset:24576\n\t"
    "v_mfma_f32_16x16x32_bf16 v[84:87], v[204:207], v[104:107], v[84:87]\n\t"
    "ds_read_b128 v[100:103], %[ea] offset:26624\n\t"
    "v_mfma_f32_16x16x32_bf16 v[88:91], v[200:203], v[108:111], v[88:91]\n\t"
    "v_mfma_f32_16x16x32_bf16 v[92:95], v[204:207], v[108:111], v[92:95]\n\t"
    "s_waitcnt lgkmcnt(4)\n\t"
    "v_mfma_f32_16x16x32_bf16 v[80:83], v[208:211], v[112:115], v[80:83]\n\t"
    "ds_read_b128 v[104:107], %[da] offset:24576\n\t"
    "v_mfma_f32_16x16x32_bf16 v[84:87], v[212:215], v[112:115], v[84:87]\n\t"
    "ds_read_b128 v[108:111], %[da] offset:26624\n\t"
    "v_mfma_f32_16x16x32_bf16 v[88:91], v[208:211], v[116:119], v[88:91]\n\t"
    "v_mfma_f32_16x16x32_bf16 v[92:95], v[212:215], v[116:119], v[92:95]\n\t"
    "s_waitcnt lgkmcnt(4)\n\t"
    "v_mfma_f32_16x16x32_bf16 v[80:83], v[216:219], v[120:123], v[80:83]\n\t"
    "ds_read_b128 v[112:115], %[ea] offset:28672\n\t"
    "v_mfma_f32_16x16x32_bf16 v[84:87], v[220:223], v[120:123], v[84:87]\n\t"
    "ds_read_b128 v[116:119], %[ea] offset:30720\n\t"
    "v_mfma_f32_16x16x32_bf16 v[88:91], v[216:219], v[124:127], v[88:91]\n\t"
    "v_mfma_f32_16x16x32_bf16 v[92:95], v[220:223], v[124:127], v[92:95]\n\t"
    "s_waitcnt lgkmcnt(4)\n\t"
    "v_mfma_f32_16x16x32_bf16 v[80:83], v[224:227], v[96:99], v[80:83]\n\t"
    "ds_read_b128 v[120:123], %[da] offset:28672\n\t"
    "v_mfma_f32_16x16x32_bf16 v[84:87], v[228:231], v[96:99], v[84:87]\n\t"
    "ds_read_b128 v[124:127], %[da] offset:30720\n\t"
    "v_mfma_f32_16x16x32_bf16 v[88:91], v[224:227], v[100:103], v[88:91]\n\t"
    "v_mfma_f32_16x16x32_bf16 v[92:95], v[228:231], v[100:103], v[92:95]\n\t"
    "s_waitcnt lgkmcnt(4)\n\t"
    "v_mfma_f32_16x16x32_bf16 v[80:83], v[232:235], v[104:107], v[80:83]\n\t"
    "v_mfma_f32_16x16x32_bf16 v[84:87], v[236:239], v[104:107], v[84:87]\n\t"
    "v_mfma_f32_16x16x32_bf16 v[88:91], v[232:235], v[108:111], v[88:91]\n\t"
    "v_mfma_f32_16x16x32_bf16 v[92:95], v[236:239], v[108:111], v[92:95]\n\t"
    "s_waitcnt lgkmcnt(2)\n\t"
    "v_mfma_f32_16x16x32_bf16 v[80:83], v[240:243], v[112:115], v[80:83]\n\t"
    "v_mfma_f32_16x16x32_bf16 v[84:87], v[244:247], v[112:115], v[84:87]\n\t"
    "v_mfma_f32_16x16x32_bf16 v[88:91], v[240:243], v[116:119], v[88:91]\n\t"
    "v_mfma_f32_16x16x32_bf16 v[92:95], v[244:247], v[116:119], v[92:95]\n\t"
    "s_waitcnt lgkmcnt(0)\n\t"
    "v_mfma_f32_16x16x32_bf16 v[80:83], v[248:251], v[120:123], v[80:83]\n\t"
    "v_mfma_f32_16x16x32_bf16 v[84:87], v[252:255], v[120:123], v[84:87]\n\t"
    "v_mfma_f32_16x16x32_bf16 v[88:91], v[248:251], v[124:127], v[88:91]\n\t"
    "v_mfma_f32_16x16x32_bf16 v[92:95], v[252:255], v[124:127], v[92:95]\n\t"
    "s_nop 6\n\t"
    "s_branch 9f\n\t"
    "3:\n\t"
    "s_bitcmp0_b32 %[x], 0\n\t"
    "s_cbranch_scc1 5f\n\t"
    "7:\n\t"
    "v_mov_b32 v76, %[cn]\n\t"
    "v_mov_b32 v77, 1\n\t"
    "v_mov_b32 v78, %[pv]\n\t"
    "v_mov_b32 v79, 0\n\t"
    "s_waitcnt vmcnt(0)\n\t"
    "s_mov_b64 exec, 1\n\t"
    "ds_add_rtn_u32 v77, v76, v77\n\t"
    "s_waitcnt lgkmcnt(0)\n\t"
    "v_readfirstlane_b32 %[st], v77\n\t"
    "s_and_b32 %[st], %[st], 3\n\t"
    "s_cmp_lg_u32 %[st], 3\n\t"
    "s_cbranch_scc1 6f\n\t"
    "global_store_dword v79, v78, %[pg] sc1\n\t"
    "6:\n\t"
    "s_mov_b64 exec, -1\n\t"
    "s_bitcmp1_b32 %[x], 2\n\t"
    "s_cbranch_scc1 8b\n\t"
    "5:\n\t"
    "s_bitcmp0_b32 %[x], 1\n\t"
    "s_cbranch_scc1 4b\n\t"
    "s_mov_b32 m0, %[sl]\n\t"
    "s_nop 0\n\t"
    "global_load_lds_dword %[dv], %[db] sc1\n\t"
    "s_branch 4b\n\t"
    "9:"
    : "=&{v[80:95]}"(pd), [st] "=&s"(st)
    : [ea] "v"(ea), [da] "v"(da), [x] "s"(x), "{v[128:159]}"(W[0]), "{v[160:191]}"(W[1]), "{v[192:223]}"(W[2]), "{v[224:255]}"(W[3]), [pv] "s"(pv), [cn] "s"(cn), [sl] "s"(sl), [dv] "v"(dv), [db] "s"(db), [pg] "s"(pg)
    : "v96", "v97", "v98", "v99", "v100", "v101", "v102", "v103", "v104", "v105", "v106", "v107", "v108", "v109", "v110", "v111", "v112", "v113", "v114", "v115", "v116", "v117", "v118", "v119", "v120", "v121", "v122", "v123", "v124", "v125", "v126", "v127", "v76", "v77", "v78", "v79", "vcc", "scc", "m0", "memory");
}
#define LEAN_QB 1
__device__ __forceinline__ void paced_ld0(const u32x32 (&W)[4], unsigned ea, unsigned da, int b, int ns, unsigned ab, unsigned rw, unsigned fa,
    const char* p0, const char* p1, unsigned lb, int& cap, int wm, unsigned rq, unsigned mu, unsigned ad, unsigned dv, const char* db, unsigned sl, unsigned sa, const unsigned (&av)[8]) {
  unsigned st;
  asm volatile("s_setprio 2\n\t"
    "v_mov_b32 v78, %[wm]\n\t"
    "s_cmp_lg_u32 %[rq], 0\n\t"
    "s_cbranch_scc1 3f\n\t"
    "2:\n\t"
    "v_cmp_ge_i32 vcc, %[b], v78\n\t"
    "s_cbranch_vccz 4f\n\t"
    "21:\n\t"
    "s_cmp_eq_u32 %[sl], 0\n\t"
    "s_cbranch_scc1 71f\n\t"
    "s_mov_b32 m0, %[sl]\n\t"
    "s_nop 0\n\t"
    "global_load_lds_dword %[dv], %[db] sc1\n\t"
    "71:\n\t"
    "s_sub_u32 %[cap], %[cap], 1\n\t"
    "s_max_i32 %[cap], %[cap], 0\n\t"
    "s_waitcnt vmcnt(0)\n\t"
    "ds_read_b32 v79, %[sa]\n\t"
    "s_waitcnt lgkmcnt(0)\n\t"
    "v_mad_u32_u24 v79, v79, %[mu], %[ad]\n\t"
    "v_max_i32 v78, v78, v79\n\t"
    "v_cmp_ge_i32 vcc, %[b], v78\n\t"
    "s_cbranch_vccz 4f\n\t"
    "s_sleep 1\n\t"
    "s_cmp_lg_u32 %[cap], 0\n\t"
    "s_cbranch_scc1 21b\n\t"
    "4:\n\t"
    "s_add_u32 m0, %[lb], 0\n\t"
    "s_nop 0\n\t"
    "global_load_lds_dwordx4 %[av0], s[36:37] sc1\n\t"
    "s_add_u32 m0, %[lb], 32768\n\t"
    "s_nop 0\n\t"
    "global_load_lds_dwordx4 %[av1], s[36:37] sc1\n\t"
    "s_add_u32 m0, %[lb], 4096\n\t"
    "s_nop 0\n\t"
    "global_load_lds_dwordx4 %[av2], s[36:37] sc1\n\t"
    "s_add_u32 m0, %[lb], 36864\n\t"
    "s_nop 0\n\t"
    "global_load_lds_dwordx4 %[av3], s[36:37] sc1\n\t"
    "s_add_u32 m0, %[lb], 8192\n\t"
    "s_nop 0\n\t"
    "global_load_lds_dwordx4 %[av4], s[36:37] sc1\n\t"
    "s_add_u32 m0, %[lb], 40960\n\t"
    "s_nop 0\n\t"
    "global_load_lds_dwordx4 %[av5], s[36:37] sc1\n\t"
    "s_add_u32 m0, %[lb], 12288\n\t"
    "s_nop 0\n\t"
    "global_load_lds_dwordx4 %[av6], s[36:37] sc1\n\t"
    "s_add_u32 m0, %[lb], 45056\n\t"
    "s_nop 0\n\t"
    "global_load_lds_dwordx4 %[av7], s[36:37] sc1\n\t"
    "s_add_u32 m0, %[lb], 16384\n\t"
    "s_nop 0\n\t"
    "global_load_lds_dwordx4 %[av0], s[38:39] sc1\n\t"
    "s_add_u32 m0, %[lb], 49152\n\t"
    "s_nop 0\n\t"
    "global_load_lds_dwordx4 %[av1], s[38:39] sc1\n\t"
    "s_add_u32 m0, %[lb], 20480\n\t"
    "s_nop 0\n\t"
    "global_load_lds_dwordx4 %[av2], s[38:39] sc1\n\t"
    "s_add_u32 m0, %[lb], 53248\n\t"
    "s_nop 0\n\t"
    "global_load_lds_dwordx4 %[av3], s[38:39] sc1\n\t"
    "s_add_u32 m0, %[lb], 24576\n\t"
    "s_nop 0\n\t"
    "global_load_lds_dwordx4 %[av4], s[38:39] sc1\n\t"
    "s_add_u32 m0, %[lb], 57344\n\t"
    "s_nop 0\n\t"
    "global_load_lds_dwordx4 %[av5], s[38:39] sc1\n\t"
    "s_add_u32 m0, %[lb], 28672\n\t"
    "s_nop 0\n\t"
    "global_load_lds_dwordx4 %[av6], s[38:39] sc1\n\t"
    "s_add_u32 m0, %[lb], 61440\n\t"
    "s_nop 0\n\t"
    "global_load_lds_dwordx4 %[av7], s[38:39] sc1\n\t"
    "s_add_u32 s36, s36, %[ab]\n\t"
    "s_addc_u32 s37, s37, 0\n\t"
    "s_add_u32 s38, s38, %[ab]\n\t"
    "s_addc_u32 s39, s39, 0\n\t"
    "s_xor_b32 %[lb], %[lb], 0x10000\n\t"
    "3:\n\t"
    "s_add_u32 %[st], %[b], 1\n\t"
    "v_min_i32 v78, %[ns], v78\n\t"
    "v_cmp_ge_i32 vcc, %[st], v78\n\t"
    "s_waitcnt vmcnt(12) lgkmcnt(0)\n\t"
    "s_barrier\n\t"
    "ds_read_b128 v[96:99], %[ea] offset:0\n\t"
    "ds_read_b128 v[100:103], %[ea] offset:2048\n\t"
    "ds_read_b128 v[104:107], %[da] offset:0\n\t"
    "ds_read_b128 v[108:111], %[da] offset:2048\n\t"
    "ds_read_b128 v[112:115], %[ea] offset:4096\n\t"
    "ds_read_b128 v[116:119], %[ea] offset:6144\n\t"
    "s_cbranch_vccz 5f\n\t"
    "s_waitcnt lgkmcnt(4)\n\t"
    "v_mfma_f32_16x16x32_bf16 v[80:83], v[128:131], v[96:99], 0\n\t"
    "ds_read_b128 v[120:123], %[da] offset:4096\n\t"
    "v_mfma_f32_16x16x32_bf16 v[84:87], v[132:135], v[96:99], 0\n\t"
    "ds_read_b128 v[124:127], %[da] offset:6144\n\t"
    "v_mfma_f32_16x16x32_bf16 v[88:91], v[128:131], v[100:103], 0\n\t"
    "v_mfma_f32_16x16x32_bf16 v[92:95], v[132:135], v[100:103], 0\n\t"
    "s_waitcnt vmcnt(8)\n\t"
    "s_barrier\n\t"
    "s_waitcnt lgkmcnt(4)\n\t"
    "v_mfma_f32_16x16x32_bf16 v[80:83], v[136:139], v[104:107], v[80:83]\n\t"
    "ds_read_b128 v[96:99], %[ea] offset:8192\n\t"
    "v_mfma_f32_16x16x32_bf16 v[84:87], v[140:143], v[104:107], v[84:87]\n\t"
    "ds_read_b128 v[100:103], %[ea] offset:10240\n\t"
    "v_mfma_f32_16x16x32_bf16 v[88:91], v[136:139], v[108:111], v[88:91]\n\t"
    "v_mfma_f32_16x16x32_bf16 v[92:95], v[140:143], v[108:111], v[92:95]\n\t"
    "s_waitcnt lgkmcnt(4)\n\t"
    "v_mfma_f32_16x16x32_bf16 v[80:83], v[144:147], v[112:115], v[80:83]\n\t"
    "ds_read_b128 v[104:107], %[da] offset:8192\n\t"
    "v_mfma_f32_16x16x32_bf16 v[84:87], v[148:151], v[112:115], v[84:87]\n\t"
    "ds_read_b128 v[108:111], %[da] offset:10240\n\t"
    "v_mfma_f32_16x16x32_bf16 v[88:91], v[144:147], v[116:119], v[88:91]\n\t"
    "v_mfma_f32_16x16x32_bf16 v[92:95], v[148:151], v[116:119], v[92:95]\n\t"
    "s_waitcnt lgkmcnt(4)\n\t"
    "v_mfma_f32_16x16x32_bf16 v[80:83], v[152:155], v[120:123], v[80:83]\n\t"
    "ds_read_b128 v[112:115], %[ea] offset:12288\n\t"
    "v_mfma_f32_16x16x32_bf16 v[84:87], v[156:159], v[120:123], v[84:87]\n\t"
    "ds_read_b128 v[116:119], %[ea] offset:14336\n\t"
    "v_mfma_f32_16x16x32_bf16 v[88:91], v[152:155], v[124:127], v[88:91]\n\t"
    "v_mfma_f32_16x16x32_bf16 v[92:95], v[156:159], v[124:127], v[92:95]\n\t"
    "s_waitcnt lgkmcnt(4)\n\t"
    "v_mfma_f32_16x16x32_bf16 v[80:83], v[160:163], v[96:99], v[80:83]\n\t"
    "ds_read_b128 v[120:123], %[da] offset:12288\n\t"
    "v_mfma_f32_16x16x32_bf16 v[84:87], v[164:167], v[96:99], v[84:87]\n\t"
    "ds_read_b128 v[124:127], %[da] offset:14336\n\t"
    "v_mfma_f32_16x16x32_bf16 v[88:91], v[160:163], v[100:103], v[88:91]\n\t"
    "v_mfma_f32_16x16x32_bf16 v[92:95], v[164:167], v[100:103], v[92:95]\n\t"
    "s_waitcnt vmcnt(4)\n\t"
    "s_barrier\n\t"
    "s_waitcnt lgkmcnt(4)\n\t"
    "v_mfma_f32_16x16x32_bf16 v[80:83], v[168:171], v[104:107], v[80:83]\n\t"
    "ds_read_b128 v[96:99], %[ea] offset:16384\n\t"
    "v_mfma_f32_16x16x32_bf16 v[84:87], v[172:175], v[104:107], v[84:87]\n\t"
    "ds_read_b128 v[100:103], %[ea] offset:18432\n\t"
    "v_mfma_f32_16x16x32_bf16 v[88:91], v[168:171], v[108:111], v[88:91]\n\t"
    "v_mfma_f32_16x16x32_bf16 v[92:95], v[172:175], v[108:111], v[92:95]\n\t"
    "s_waitcnt lgkmcnt(4)\n\t"
    "v_mfma_f32_16x16x32_bf16 v[80:83], v[176:179], v[112:115], v[80:83]\n\t"
    "ds_read_b128 v[104:107], %[da] offset:16384\n\t"
    "v_mfma_f32_16x16x32_bf16 v[84:87], v[180:183], v[112:115], v[84:87]\n\t"
    "ds_read_b128 v[108:111], %[da] offset:18432\n\t"
    "v_mfma_f32_16x16x32_bf16 v[88:91], v[176:179], v[116:119], v[88:91]\n\t"
    "v_mfma_f32_16x16x32_bf16 v[92:95], v[180:183], v[116:119], v[92:95]\n\t"
    "s_waitcnt lgkmcnt(4)\n\t"
    "v_mfma_f32_16x16x32_bf16 v[80:83], v[184:187], v[120:123], v[80:83]\n\t"
    "ds_read_b128 v[112:115], %[ea] offset:20480\n\t"
    "v_mfma_f32_16x16x32_bf16 v[84:87], v[188:191], v[120:123], v[84:87]\n\t"
    "ds_read_b128 v[116:119], %[ea] offset:22528\n\t"
    "v_mfma_f32_16x16x32_bf16 v[88:91], v[184:187], v[124:127], v[88:91]\n\t"
    "v_mfma_f32_16x16x32_bf16 v[92:95], v[188:191], v[124:127], v[92:95]\n\t"
    "s_waitcnt lgkmcnt(4)\n\t"
    "v_mfma_f32_16x16x32_bf16 v[80:83], v[192:195], v[96:99], v[80:83]\n\t"
    "ds_read_b128 v[120:123], %[da] offset:20480\n\t"
    "v_mfma_f32_16x16x32_bf16 v[84:87], v[196:199], v[96:99], v[84:87]\n\t"
    "ds_read_b128 v[124:127], %[da] offset:22528\n\t"
    "v_mfma_f32_16x16x32_bf16 v[88:91], v[192:195], v[100:103], v[88:91]\n\t"
    "v_mfma_f32_16x16x32_bf16 v[92:95], v[196:199], v[100:103], v[92:95]\n\t"
    "s_waitcnt vmcnt(0)\n\t"
    "s_barrier\n\t"
    "s_waitcnt lgkmcnt(4)\n\t"
    "v_mfma_f32_16x16x32_bf16 v[80:83], v[200:203], v[104:107], v[80:83]\n\t"
    "ds_read_b128 v[96:99], %[ea] offset:24576\n\t"
    "v_mfma_f32_16x16x32_bf16 v[84:87], v[204:207], v[104:107], v[84:87]\n\t"
    "ds_read_b128 v[100:103], %[ea] offset:26624\n\t"
    "v_mfma_f32_16x16x32_bf16 v[88:91], v[200:203], v[108:111], v[88:91]\n\t"
    "v_mfma_f32_16x16x32_bf16 v[92:95], v[204:207], v[108:111], v[92:95]\n\t"
    "s_waitcnt lgkmcnt(4)\n\t"
    "v_mfma_f32_16x16x32_bf16 v[80:83], v[208:211], v[112:115], v[80:83]\n\t"
    "ds_read_b128 v[104:107], %[da] offset:24576\n\t"
    "v_mfma_f32_16x16x32_bf16 v[84:87], v[212:215], v[112:115], v[84:87]\n\t"
    "ds_read_b128 v[108:111], %[da] offset:26624\n\t"
    "v_mfma_f32_16x16x32_bf16 v[88:91], v[208:211], v[116:119], v[88:91]\n\t"
    "v_mfma_f32_16x16x32_bf16 v[92:95], v[212:215], v[116:119], v[92:95]\n\t"
    "s_cmp_eq_u32 %[sl], 0\n\t"
    "s_cbranch_scc1 72f\n\t"
    "s_mov_b32 m0, %[sl]\n\t"
    "s_nop 0\n\t"
    "global_load_lds_dword %[dv], %[db] sc1\n\t"
    "72:\n\t"
    "s_waitcnt lgkmcnt(4)\n\t"
    "v_mfma_f32_16x16x32_bf16 v[80:83], v[216:219], v[120:123], v[80:83]\n\t"
    "ds_read_b128 v[112:115], %[ea] offset:28672\n\t"
    "v_mfma_f32_16x16x32_bf16 v[84:87], v[220:223], v[120:123], v[84:87]\n\t"
    "ds_read_b128 v[116:119], %[ea] offset:30720\n\t"
    "v_mfma_f32_16x16x32_bf16 v[88:91], v[216:219], v[124:127], v[88:91]\n\t"
    "v_mfma_f32_16x16x32_bf16 v[92:95], v[220:223], v[124:127], v[92:95]\n\t"
    "s_waitcnt lgkmcnt(4)\n\t"
    "v_mfma_f32_16x16x32_bf16 v[80:83], v[224:227], v[96:99], v[80:83]\n\t"
    "ds_read_b128 v[120:123], %[da] offset:28672\n\t"
    "v_mfma_f32_16x16x32_bf16 v[84:87], v[228:231], v[96:99], v[84:87]\n\t"
    "ds_read_b128 v[124:127], %[da] offset:30720\n\t"
    "v_mfma_f32_16x16x32_bf16 v[88:91], v[224:227], v[100:103], v[88:91]\n\t"
    "v_mfma_f32_16x16x32_bf16 v[92:95], v[228:231], v[100:103], v[92:95]\n\t"
    "s_waitcnt lgkmcnt(4)\n\t"
    "v_mfma_f32_16x16x32_bf16 v[80:83], v[232:235], v[104:107], v[80:83]\n\t"
    "v_mfma_f32_16x16x32_bf16 v[84:87], v[236:239], v[104:107], v[84:87]\n\t"
    "v_mfma_f32_16x16x32_bf16 v[88:91], v[232:235], v[108:111], v[88:91]\n\t"
    "v_xor_b32 %[ea], 0x10000, %[ea]\n\t"
    "v_mfma_f32_16x16x32_bf16 v[92:95], v[236:239], v[108:111], v[92:95]\n\t"
    "v_xor_b32 %[da], 0x10000, %[da]\n\t"
    "s_waitcnt lgkmcnt(2)\n\t"
    "v_mfma_f32_16x16x32_bf16 v[80:83], v[240:243], v[112:115], v[80:83]\n\t"
    "v_mfma_f32_16x16x32_bf16 v[84:87], v[244:247], v[112:115], v[84:87]\n\t"
    "v_mfma_f32_16x16x32_bf16 v[88:91], v[240:243], v[116:119], v[88:91]\n\t"
    "s_add_u32 %[b], %[b], 1\n\t"
    "v_mfma_f32_16x16x32_bf16 v[92:95], v[244:247], v[116:119], v[92:95]\n\t"
    "s_waitcnt lgkmcnt(0)\n\t"
    "v_mfma_f32_16x16x32_bf16 v[80:83], v[248:251], v[120:123], v[80:83]\n\t"
    "v_mfma_f32_16x16x32_bf16 v[84:87], v[252:255], v[120:123], v[84:87]\n\t"
    "v_mfma_f32_16x16x32_bf16 v[88:91], v[248:251], v[124:127], v[88:91]\n\t"
    "v_mfma_f32_16x16x32_bf16 v[92:95], v[252:255], v[124:127], v[92:95]\n\t"
    "s_nop 4\n\t"
    "ds_write_b128 %[rw], v[80:83] offset:0\n\t"
    "ds_write_b128 %[rw], v[84:87] offset:1024\n\t"
    "ds_write_b128 %[rw], v[88:91] offset:2048\n\t"
    "ds_write_b128 %[rw], v[92:95] offset:3072\n\t"
    "s_waitcnt vmcnt(0)\n\t"
    "ds_read_b32 v79, %[sa]\n\t"
    "s_waitcnt lgkmcnt(0)\n\t"
    "v_mad_u32_u24 v79, v79, %[mu], %[ad]\n\t"
    "v_max_i32 v78, v78, v79\n\t"
    "v_mov_b32 v77, %[b]\n\t"
    "v_cmp_ge_i32 vcc, %[b], v78\n\t"
    "s_cmp_ge_i32 %[b], %[ns]\n\t"
    "s_cbranch_scc1 6f\n\t"
    "s_cbranch_vccnz 6f\n\t"
    "v_or_b32 v77, 0x100, v77\n\t"
    "ds_write_b32 %[fa], v77\n\t"
    "s_branch 4b\n\t"
    "6:\n\t"
    "ds_write_b32 %[fa], v77\n\t"
    "s_cbranch_scc0 21b\n\t"
    "s_branch 9f\n\t"
    "5:\n\t"
    "s_waitcnt lgkmcnt(4)\n\t"
    "v_mfma_f32_16x16x32_bf16 v[80:83], v[128:131], v[96:99], 0\n\t"
    "ds_read_b128 v[120:123], %[da] offset:4096\n\t"
    "s_add_u32 m0, %[lb], 0\n\t"
    "v_mfma_f32_16x16x32_bf16 v[84:87], v[132:135], v[96:99], 0\n\t"
    "ds_read_b128 v[124:127], %[da] offset:6144\n\t"
    "global_load_lds_dwordx4 %[av0], s[36:37] sc1\n\t"
    "v_mfma_f32_16x16x32_bf16 v[88:91], v[128:131], v[100:103], 0\n\t"
    "s_add_u32 m0, %[lb], 32768\n\t"
    "v_mfma_f32_16x16x32_bf16 v[92:95], v[132:135], v[100:103], 0\n\t"
    "global_load_lds_dwordx4 %[av1], s[36:37] sc1\n\t"
    "s_waitcnt vmcnt(10)\n\t"
    "s_barrier\n\t"
    "s_waitcnt lgkmcnt(4)\n\t"
    "v_mfma_f32_16x16x32_bf16 v[80:83], v[136:139], v[104:107], v[80:83]\n\t"
    "ds_read_b128 v[96:99], %[ea] offset:8192\n\t"
    "s_add_u32 m0, %[lb], 4096\n\t"
    "v_mfma_f32_16x16x32_bf16 v[84:87], v[140:143], v[104:107], v[84:87]\n\t"
    "ds_read_b128 v[100:103], %[ea] offset:10240\n\t"
    "global_load_lds_dwordx4 %[av2], s[36:37] sc1\n\t"
    "v_mfma_f32_16x16x32_bf16 v[88:91], v[136:139], v[108:111], v[88:91]\n\t"
    "s_add_u32 m0, %[lb], 36864\n\t"
    "v_mfma_f32_16x16x32_bf16 v[92:95], v[140:143], v[108:111], v[92:95]\n\t"
    "global_load_lds_dwordx4 %[av3], s[36:37] sc1\n\t"
    "s_waitcnt lgkmcnt(4)\n\t"
    "v_mfma_f32_16x16x32_bf16 v[80:83], v[144:147], v[112:115], v[80:83]\n\t"
    "ds_read_b128 v[104:107], %[da] offset:8192\n\t"
    "s_add_u32 m0, %[lb], 8192\n\t"
    "v_mfma_f32_16x16x32_bf16 v[84:87], v[148:151], v[112:115], v[84:87]\n\t"
    "ds_read_b128 v[108:111], %[da] offset:10240\n\t"
    "global_load_lds_dwordx4 %[av4], s[36:37] sc1\n\t"
    "v_mfma_f32_16x16x32_bf16 v[88:91], v[144:147], v[116:119], v[88:91]\n\t"
    "s_add_u32 m0, %[lb], 40960\n\t"
    "v_mfma_f32_16x16x32_bf16 v[92:95], v[148:151], v[116:119], v[92:95]\n\t"
    "global_load_lds_dwordx4 %[av5], s[36:37] sc1\n\t"
    "s_waitcnt lgkmcnt(4)\n\t"
    "v_mfma_f32_16x16x32_bf16 v[80:83], v[152:155], v[120:123], v[80:83]\n\t"
    "ds_read_b128 v[112:115], %[ea] offset:12288\n\t"
    "s_add_u32 m0, %[lb], 12288\n\t"
    "v_mfma_f32_16x16x32_bf16 v[84:87], v[156:159], v[120:123], v[84:87]\n\t"
    "ds_read_b128 v[116:119], %[ea] offset:14336\n\t"
    "global_load_lds_dwordx4 %[av6], s[36:37] sc1\n\t"
    "v_mfma_f32_16x16x32_bf16 v[88:91], v[152:155], v[124:127], v[88:91]\n\t"
    "s_add_u32 m0, %[lb], 45056\n\t"
    "v_mfma_f32_16x16x32_bf16 v[92:95], v[156:159], v[124:127], v[92:95]\n\t"
    "global_load_lds_dwordx4 %[av7], s[36:37] sc1\n\t"
    "s_waitcnt lgkmcnt(4)\n\t"
    "v_mfma_f32_16x16x32_bf16 v[80:83], v[160:163], v[96:99], v[80:83]\n\t"
    "ds_read_b128 v[120:123], %[da] offset:12288\n\t"
    "s_add_u32 m0, %[lb], 16384\n\t"
    "v_mfma_f32_16x16x32_bf16 v[84:87], v[164:167], v[96:99], v[84:87]\n\t"
    "ds_read_b128 v[124:127], %[da] offset:14336\n\t"
    "global_load_lds_dwordx4 %[av0], s[38:39] sc1\n\t"
    "v_mfma_f32_16x16x32_bf16 v[88:91], v[160:163], v[100:103], v[88:91]\n\t"
    "s_add_u32 m0, %[lb], 49152\n\t"
    "v_mfma_f32_16x16x32_bf16 v[92:95], v[164:167], v[100:103], v[92:95]\n\t"
    "global_load_lds_dwordx4 %[av1], s[38:39] sc1\n\t"
    "s_waitcnt vmcnt(14)\n\t"
    "s_barrier\n\t"
    "s_waitcnt lgkmcnt(4)\n\t"
    "v_mfma_f32_16x16x32_bf16 v[80:83], v[168:171], v[104:107], v[80:83]\n\t"
    "ds_read_b128 v[96:99], %[ea] offset:16384\n\t"
    "s_add_u32 m0, %[lb], 20480\n\t"
    "v_mfma_f32_16x16x32_bf16 v[84:87], v[172:175], v[104:107], v[84:87]\n\t"
    "ds_read_b128 v[100:103], %[ea] offset:18432\n\t"
    "global_load_lds_dwordx4 %[av2], s[38:39] sc1\n\t"
    "v_mfma_f32_16x16x32_bf16 v[88:91], v[168:171], v[108:111], v[88:91]\n\t"
    "s_add_u32 m0, %[lb], 53248\n\t"
    "v_mfma_f32_16x16x32_bf16 v[92:95], v[172:175], v[108:111], v[92:95]\n\t"
    "global_load_lds_dwordx4 %[av3], s[38:39] sc1\n\t"
    "s_waitcnt lgkmcnt(4)\n\t"
    "v_mfma_f32_16x16x32_bf16 v[80:83], v[176:179], v[112:115], v[80:83]\n\t"
    "ds_read_b128 v[104:107], %[da] offset:16384\n\t"
    "s_add_u32 m0, %[lb], 24576\n\t"
    "v_mfma_f32_16x16x32_bf16 v[84:87], v[180:183], v[112:115], v[84:87]\n\t"
    "ds_read_b128 v[108:111], %[da] offset:18432\n\t"
    "global_load_lds_dwordx4 %[av4], s[38:39] sc1\n\t"
    "v_mfma_f32_16x16x32_bf16 v[88:91], v[176:179], v[116:119], v[88:91]\n\t"
    "s_add_u32 m0, %[lb], 57344\n\t"
    "v_mfma_f32_16x16x32_bf16 v[92:95], v[180:183], v[116:119], v[92:95]\n\t"
    "global_load_lds_dwordx4 %[av5], s[38:39] sc1\n\t"
    "s_waitcnt lgkmcnt(4)\n\t"
    "v_mfma_f32_16x16x32_bf16 v[80:83], v[184:187], v[120:123], v[80:83]\n\t"
    "ds_read_b128 v[112:115], %[ea] offset:20480\n\t"
    "s_add_u32 m0, %[lb], 28672\n\t"
    "v_mfma_f32_16x16x32_bf16 v[84:87], v[188:191], v[120:123], v[84:87]\n\t"
    "ds_read_b128 v[116:119], %[ea] offset:22528\n\t"
    "global_load_lds_dwordx4 %[av6], s[38:39] sc1\n\t"
    "v_mfma_f32_16x16x32_bf16 v[88:91], v[184:187], v[124:127], v[88:91]\n\t"
    "s_add_u32 m0, %[lb], 61440\n\t"
    "v_mfma_f32_16x16x32_bf16 v[92:95], v[188:191], v[124:127], v[92:95]\n\t"
    "global_load_lds_dwordx4 %[av7], s[38:39] sc1\n\t"
    "s_waitcnt lgkmcnt(4)\n\t"
    "v_mfma_f32_16x16x32_bf16 v[80:83], v[192:195], v[96:99], v[80:83]\n\t"
    "ds_read_b128 v[120:123], %[da] offset:20480\n\t"
    "v_mfma_f32_16x16x32_bf16 v[84:87], v[196:199], v[96:99], v[84:87]\n\t"
    "ds_read_b128 v[124:127], %[da] offset:22528\n\t"
    "v_mfma_f32_16x16x32_bf16 v[88:91], v[192:195], v[100:103], v[88:91]\n\t"
    "v_mfma_f32_16x16x32_bf16 v[92:95], v[196:199], v[100:103], v[92:95]\n\t"
    "s_add_u32 s36, s36, %[ab]\n\t"
    "s_addc_u32 s37, s37, 0\n\t"
    "s_add_u32 s38, s38, %[ab]\n\t"
    "s_addc_u32 s39, s39, 0\n\t"
    "s_xor_b32 %[lb], %[lb], 0x10000\n\t"
    "s_waitcnt vmcnt(16)\n\t"
    "s_barrier\n\t"
    "s_waitcnt lgkmcnt(4)\n\t"
    "v_mfma_f32_16x16x32_bf16 v[80:83], v[200:203], v[104:107], v[80:83]\n\t"
    "ds_read_b128 v[96:99], %[ea] offset:24576\n\t"
    "v_mfma_f32_16x16x32_bf16 v[84:87], v[204:207], v[104:107], v[84:87]\n\t"
    "ds_read_b128 v[100:103], %[ea] offset:26624\n\t"
    "v_mfma_f32_16x16x32_bf16 v[88:91], v[200:203], v[108:111], v[88:91]\n\t"
    "v_mfma_f32_16x16x32_bf16 v[92:95], v[204:207], v[108:111], v[92:95]\n\t"
    "s_waitcnt lgkmcnt(4)\n\t"
    "v_mfma_f32_16x16x32_bf16 v[80:83], v[208:211], v[112:115], v[80:83]\n\t"
    "ds_read_b128 v[104:107], %[da] offset:24576\n\t"
    "v_mfma_f32_16x16x32_bf16 v[84:87], v[212:215], v[112:115], v[84:87]\n\t"
    "ds_read_b128 v[108:111], %[da] offset:26624\n\t"
    "v_mfma_f32_16x16x32_bf16 v[88:91], v[208:211], v[116:119], v[88:91]\n\t"
    "v_mfma_f32_16x16x32_bf16 v[92:95], v[212:215], v[116:119], v[92:95]\n\t"
    "s_cmp_eq_u32 %[sl], 0\n\t"
    "s_cbranch_scc1 73f\n\t"
    "s_mov_b32 m0, %[sl]\n\t"
    "s_nop 0\n\t"
    "global_load_lds_dword %[dv], %[db] sc1\n\t"
    "73:\n\t"
    "s_waitcnt lgkmcnt(4)\n\t"
    "v_mfma_f32_16x16x32_bf16 v[80:83], v[216:219], v[120:123], v[80:83]\n\t"
    "ds_read_b128 v[112:115], %[ea] offset:28672\n\t"
    "v_mfma_f32_16x16x32_bf16 v[84:87], v[220:223], v[120:123], v[84:87]\n\t"
    "ds_read_b128 v[116:119], %[ea] offset:30720\n\t"
    "v_mfma_f32_16x16x32_bf16 v[88:91], v[216:219], v[124:127], v[88:91]\n\t"
    "v_mfma_f32_16x16x32_bf16 v[92:95], v[220:223], v[124:127], v[92:95]\n\t"
    "s_waitcnt lgkmcnt(4)\n\t"
    "v_mfma_f32_16x16x32_bf16 v[80:83], v[224:227], v[96:99], v[80:83]\n\t"
    "ds_read_b128 v[120:123], %[da] offset:28672\n\t"
    "v_mfma_f32_16x16x32_bf16 v[84:87], v[228:231], v[96:99], v[84:87]\n\t"
    "ds_read_b128 v[124:127], %[da] offset:30720\n\t"
    "v_mfma_f32_16x16x32_bf16 v[88:91], v[224:227], v[100:103], v[88:91]\n\t"
    "v_mfma_f32_16x16x32_bf16 v[92:95], v[228:231], v[100:103], v[92:95]\n\t"
    "s_waitcnt lgkmcnt(4)\n\t"
    "v_mfma_f32_16x16x32_bf16 v[80:83], v[232:235], v[104:107], v[80:83]\n\t"
    "v_mfma_f32_16x16x32_bf16 v[84:87], v[236:239], v[104:107], v[84:87]\n\t"
    "v_mfma_f32_16x16x32_bf16 v[88:91], v[232:235], v[108:111], v[88:91]\n\t"
    "v_xor_b32 %[ea], 0x10000, %[ea]\n\t"
    "v_mfma_f32_16x16x32_bf16 v[92:95], v[236:239], v[108:111], v[92:95]\n\t"
    "v_xor_b32 %[da], 0x10000, %[da]\n\t"
    "s_waitcnt lgkmcnt(2)\n\t"
    "v_mfma_f32_16x16x32_bf16 v[80:83], v[240:243], v[112:115], v[80:83]\n\t"
    "v_mfma_f32_16x16x32_bf16 v[84:87], v[244:247], v[112:115], v[84:87]\n\t"
    "v_mfma_f32_16x16x32_bf16 v[88:91], v[240:243], v[116:119], v[88:91]\n\t"
    "s_add_u32 %[b], %[b], 1\n\t"
    "v_mfma_f32_16x16x32_bf16 v[92:95], v[244:247], v[116:119], v[92:95]\n\t"
    "s_waitcnt lgkmcnt(0)\n\t"
    "v_mfma_f32_16x16x32_bf16 v[80:83], v[248:251], v[120:123], v[80:83]\n\t"
    "v_mfma_f32_16x16x32_bf16 v[84:87], v[252:255], v[120:123], v[84:87]\n\t"
    "v_mfma_f32_16x16x32_bf16 v[88:91], v[248:251], v[124:127], v[88:91]\n\t"
    "v_mfma_f32_16x16x32_bf16 v[92:95], v[252:255], v[124:127], v[92:95]\n\t"
    "v_mov_b32 v77, %[b]\n\t"
    "v_or_b32 v77, 0x100, v77\n\t"
    "s_nop 2\n\t"
    "ds_write_b128 %[rw], v[80:83] offset:0\n\t"
    "ds_write_b128 %[rw], v[84:87] offset:1024\n\t"
    "ds_write_b128 %[rw], v[88:91] offset:2048\n\t"
    "ds_write_b128 %[rw], v[92:95] offset:3072\n\t"
    "ds_write_b32 %[fa], v77\n\t"
    "s_waitcnt vmcnt(0)\n\t"
    "ds_read_b32 v79, %[sa]\n\t"
    "s_waitcnt lgkmcnt(0)\n\t"
    "v_mad_u32_u24 v79, v79, %[mu], %[ad]\n\t"
    "v_max_i32 v78, v78, v79\n\t"
    "s_branch 3b\n\t"
    "9:\n\t"
    "s_waitcnt lgkmcnt(0)"
    : [ea] "+&v"(ea), [da] "+&v"(da), [b] "+&s"(b), [st] "=&s"(st), "+{s[36:37]}"(p0), "+{s[38:39]}"(p1), [lb] "+&s"(lb), [cap] "+&s"(cap)
    : [ns] "s"(ns), [ab] "s"(ab), [rw] "v"(rw), [fa] "v"(fa), "{v[128:159]}"(W[0]), "{v[160:191]}"(W[1]), "{v[192:223]}"(W[2]), "{v[224:255]}"(W[3]), [wm] "s"(wm), [rq] "s"(rq), [mu] "s"(mu), [ad] "v"(ad), [dv] "v"(dv), [db] "s"(db), [sl] "s"(sl), [sa] "v"(sa), [av0] "v"(av[0]), [av1] "v"(av[1]), [av2] "v"(av[2]), [av3] "v"(av[3]), [av4] "v"(av[4]), [av5] "v"(av[5]), [av6] "v"(av[6]), [av7] "v"(av[7])
    : "v77", "v78", "v79", "v80", "v81", "v82", "v83", "v84", "v85", "v86", "v87", "v88", "v89", "v90", "v91", "v92", "v93", "v94", "v95", "v96", "v97", "v98", "v99", "v100", "v101", "v102", "v103", "v104", "v105", "v106", "v107", "v108", "v109", "v110", "v111", "v112", "v113", "v114", "v115", "v116", "v117", "v118", "v119", "v120", "v121", "v122", "v123", "v124", "v125", "v126", "v127", "vcc", "scc", "m0", "memory");
}
__device__ __forceinline__ void paced_ld2(const u32x32 (&W)[4], unsigned ea, unsigned da, int b, int ns, unsigned ab, unsigned rw, unsigned fa,
    const char* p0, const char* p1, unsigned lb, int& cap, int wm, unsigned rq, unsigned mu, unsigned ad, unsigned dv, const char* db, unsigned sl, unsigned sa, const unsigned (&av)[8]) {
  unsigned st;
  asm volatile("s_setprio 2\n\t"
    "v_mov_b32 v78, %[wm]\n\t"
    "s_cmp_lg_u32 %[rq], 0\n\t"
    "s_cbranch_scc1 3f\n\t"
    "2:\n\t"
    "v_cmp_ge_i32 vcc, %[b], v78\n\t"
    "s_cbranch_vccz 4f\n\t"
    "21:\n\t"
    "s_cmp_eq_u32 %[sl], 0\n\t"
    "s_cbranch_scc1 71f\n\t"
    "s_mov_b32 m0, %[sl]\n\t"
    "s_nop 0\n\t"
    "global_load_lds_dword %[dv], %[db] sc1\n\t"
    "71:\n\t"
    "s_sub_u32 %[cap], %[cap], 1\n\t"
    "s_max_i32 %[cap], %[cap], 0\n\t"
    "s_waitcnt vmcnt(0)\n\t"
    "ds_read_b32 v79, %[sa]\n\t"
    "s_waitcnt lgkmcnt(0)\n\t"
    "v_mad_u32_u24 v79, v79, %[mu], %[ad]\n\t"
    "v_max_i32 v78, v78, v79\n\t"
    "v_cmp_ge_i32 vcc, %[b], v78\n\t"
    "s_cbranch_vccz 4f\n\t"
    "s_sleep 1\n\t"
    "s_cmp_lg_u32 %[cap], 0\n\t"
    "s_cbranch_scc1 21b\n\t"
    "4:\n\t"
    "s_add_u32 m0, %[lb], 0\n\t"
    "s_nop 0\n\t"
    "global_load_lds_dwordx4 %[av0], s[36:37] sc1\n\t"
    "s_add_u32 m0, %[lb], 4096\n\t"
    "s_nop 0\n\t"
    "global_load_lds_dwordx4 %[av2], s[36:37] sc1\n\t"
    "s_add_u32 m0, %[lb], 8192\n\t"
    "s_nop 0\n\t"
    "global_load_lds_dwordx4 %[av4], s[36:37] sc1\n\t"
    "s_add_u32 m0, %[lb], 12288\n\t"
    "s_nop 0\n\t"
    "global_load_lds_dwordx4 %[av6], s[36:37] sc1\n\t"
    "s_add_u32 m0, %[lb], 16384\n\t"
    "s_nop 0\n\t"
    "global_load_lds_dwordx4 %[av0], s[38:39] sc1\n\t"
    "s_add_u32 m0, %[lb], 20480\n\t"
    "s_nop 0\n\t"
    "global_load_lds_dwordx4 %[av2], s[38:39] sc1\n\t"
    "s_add_u32 m0, %[lb], 24576\n\t"
    "s_nop 0\n\t"
    "global_load_lds_dwordx4 %[av4], s[38:39] sc1\n\t"
    "s_add_u32 m0, %[lb], 28672\n\t"
    "s_nop 0\n\t"
    "global_load_lds_dwordx4 %[av6], s[38:39] sc1\n\t"
    "s_add_u32 s36, s36, %[ab]\n\t"
    "s_addc_u32 s37, s37, 0\n\t"
    "s_add_u32 s38, s38, %[ab]\n\t"
    "s_addc_u32 s39, s39, 0\n\t"
    "s_xor_b32 %[lb], %[lb], 0x10000\n\t"
    "3:\n\t"
    "s_add_u32 %[st], %[b], 1\n\t"
    "v_min_i32 v78, %[ns], v78\n\t"
    "v_cmp_ge_i32 vcc, %[st], v78\n\t"
    "s_waitcnt vmcnt(6) lgkmcnt(0)\n\t"
    "s_barrier\n\t"
    "ds_read_b128 v[96:99], %[ea] offset:0\n\t"
    "ds_read_b128 v[100:103], %[ea] offset:2048\n\t"
    "ds_read_b128 v[104:107], %[da] offset:0\n\t"
    "ds_read_b128 v[108:111], %[da] offset:2048\n\t"
    "ds_read_b128 v[112:115], %[ea] offset:4096\n\t"
    "ds_read_b128 v[116:119], %[ea] offset:6144\n\t"
    "s_cbranch_vccz 5f\n\t"
    "s_waitcnt lgkmcnt(4)\n\t"
    "v_mfma_f32_16x16x32_bf16 v[80:83], v[128:131], v[96:99], 0\n\t"
    "ds_read_b128 v[120:123], %[da] offset:4096\n\t"
    "v_mfma_f32_16x16x32_bf16 v[84:87], v[132:135], v[96:99], 0\n\t"
    "ds_read_b128 v[124:127], %[da] offset:6144\n\t"
    "v_mfma_f32_16x16x32_bf16 v[88:91], v[128:131], v[100:103], 0\n\t"
    "v_mfma_f32_16x16x32_bf16 v[92:95], v[132:135], v[100:103], 0\n\t"
    "s_waitcnt vmcnt(4)\n\t"
    "s_barrier\n\t"
    "s_waitcnt lgkmcnt(4)\n\t"
    "v_mfma_f32_16x16x32_bf16 v[80:83], v[136:139], v[104:107], v[80:83]\n\t"
    "ds_read_b128 v[96:99], %[ea] offset:8192\n\t"
    "v_mfma_f32_16x16x32_bf16 v[84:87], v[140:143], v[104:107], v[84:87]\n\t"
    "ds_read_b128 v[100:103], %[ea] offset:10240\n\t"
    "v_mfma_f32_16x16x32_bf16 v[88:91], v[136:139], v[108:111], v[88:91]\n\t"
    "v_mfma_f32_16x16x32_bf16 v[92:95], v[140:143], v[108:111], v[92:95]\n\t"
    "s_waitcnt lgkmcnt(4)\n\t"
    "v_mfma_f32_16x16x32_bf16 v[80:83], v[144:147], v[112:115], v[80:83]\n\t"
    "ds_read_b128 v[104:107], %[da] offset:8192\n\t"
    "v_mfma_f32_16x16x32_bf16 v[84:87], v[148:151], v[112:115], v[84:87]\n\t"
    "ds_read_b128 v[108:111], %[da] offset:10240\n\t"
    "v_mfma_f32_16x16x32_bf16 v[88:91], v[144:147], v[116:119], v[88:91]\n\t"
    "v_mfma_f32_16x16x32_bf16 v[92:95], v[148:151], v[116:119], v[92:95]\n\t"
    "s_waitcnt lgkmcnt(4)\n\t"
    "v_mfma_f32_16x16x32_bf16 v[80:83], v[152:155], v[120:123], v[80:83]\n\t"
    "ds_read_b128 v[112:115], %[ea] offset:12288\n\t"
    "v_mfma_f32_16x16x32_bf16 v[84:87], v[156:159], v[120:123], v[84:87]\n\t"
    "ds_read_b128 v[116:119], %[ea] offset:14336\n\t"
    "v_mfma_f32_16x16x32_bf16 v[88:91], v[152:155], v[124:127], v[88:91]\n\t"
    "v_mfma_f32_16x16x32_bf16 v[92:95], v[156:159], v[124:127], v[92:95]\n\t"
    "s_waitcnt lgkmcnt(4)\n\t"
    "v_mfma_f32_16x16x32_bf16 v[80:83], v[160:163], v[96:99], v[80:83]\n\t"
    "ds_read_b128 v[120:123], %[da] offset:12288\n\t"
    "v_mfma_f32_16x16x32_bf16 v[84:87], v[164:167], v[96:99], v[84:87]\n\t"
    "ds_read_b128 v[124:127], %[da] offset:14336\n\t"
    "v_mfma_f32_16x16x32_bf16 v[88:91], v[160:163], v[100:103], v[88:91]\n\t"
    "v_mfma_f32_16x16x32_bf16 v[92:95], v[164:167], v[100:103], v[92:95]\n\t"
    "s_waitcnt vmcnt(2)\n\t"
    "s_barrier\n\t"
    "s_waitcnt lgkmcnt(4)\n\t"
    "v_mfma_f32_16x16x32_bf16 v[80:83], v[168:171], v[104:107], v[80:83]\n\t"
    "ds_read_b128 v[96:99], %[ea] offset:16384\n\t"
    "v_mfma_f32_16x16x32_bf16 v[84:87], v[172:175], v[104:107], v[84:87]\n\t"
    "ds_read_b128 v[100:103], %[ea] offset:18432\n\t"
    "v_mfma_f32_16x16x32_bf16 v[88:91], v[168:171], v[108:111], v[88:91]\n\t"
    "v_mfma_f32_16x16x32_bf16 v[92:95], v[172:175], v[108:111], v[92:95]\n\t"
    "s_waitcnt lgkmcnt(4)\n\t"
    "v_mfma_f32_16x16x32_bf16 v[80:83], v[176:179], v[112:115], v[80:83]\n\t"
    "ds_read_b128 v[104:107], %[da] offset:16384\n\t"
    "v_mfma_f32_16x16x32_bf16 v[84:87], v[180:183], v[112:115], v[84:87]\n\t"
    "ds_read_b128 v[108:111], %[da] offset:18432\n\t"
    "v_mfma_f32_16x16x32_bf16 v[88:91], v[176:179], v[116:119], v[88:91]\n\t"
    "v_mfma_f32_16x16x32_bf16 v[92:95], v[180:183], v[116:119], v[92:95]\n\t"
    "s_waitcnt lgkmcnt(4)\n\t"
    "v_mfma_f32_16x16x32_bf16 v[80:83], v[184:187], v[120:123], v[80:83]\n\t"
    "ds_read_b128 v[112:115], %[ea] offset:20480\n\t"
    "v_mfma_f32_16x16x32_bf16 v[84:87], v[188:191], v[120:123], v[84:87]\n\t"
    "ds_read_b128 v[116:119], %[ea] offset:22528\n\t"
    "v_mfma_f32_16x16x32_bf16 v[88:91], v[184:187], v[124:127], v[88:91]\n\t"
    "v_mfma_f32_16x16x32_bf16 v[92:95], v[188:191], v[124:127], v[92:95]\n\t"
    "s_waitcnt lgkmcnt(4)\n\t"
    "v_mfma_f32_16x16x32_bf16 v[80:83], v[192:195], v[96:99], v[80:83]\n\t"
    "ds_read_b128 v[120:123], %[da] offset:20480\n\t"
    "v_mfma_f32_16x16x32_bf16 v[84:87], v[196:199], v[96:99], v[84:87]\n\t"
    "ds_read_b128 v[124:127], %[da] offset:22528\n\t"
    "v_mfma_f32_16x16x32_bf16 v[88:91], v[192:195], v[100:103], v[88:91]\n\t"
    "v_mfma_f32_16x16x32_bf16 v[92:95], v[196:199], v[100:103], v[92:95]\n\t"
    "s_waitcnt vmcnt(0)\n\t"
    "s_barrier\n\t"
    "s_waitcnt lgkmcnt(4)\n\t"
    "v_mfma_f32_16x16x32_bf16 v[80:83], v[200:203], v[104:107], v[80:83]\n\t"
    "ds_read_b128 v[96:99], %[ea] offset:24576\n\t"
    "v_mfma_f32_16x16x32_bf16 v[84:87], v[204:207], v[104:107], v[84:87]\n\t"
    "ds_read_b128 v[100:103], %[ea] offset:26624\n\t"
    "v_mfma_f32_16x16x32_bf16 v[88:91], v[200:203], v[108:111], v[88:91]\n\t"
    "v_mfma_f32_16x16x32_bf16 v[92:95], v[204:207], v[108:111], v[92:95]\n\t"
    "s_waitcnt lgkmcnt(4)\n\t"
    "v_mfma_f32_16x16x32_bf16 v[80:83], v[208:211], v[112:115], v[80:83]\n\t"
    "ds_read_b128 v[104:107], %[da] offset:24576\n\t"
    "v_mfma_f32_16x16x32_bf16 v[84:87], v[212:215], v[112:115], v[84:87]\n\t"
    "ds_read_b128 v[108:111], %[da] offset:26624\n\t"
    "v_mfma_f32_16x16x32_bf16 v[88:91], v[208:211], v[116:119], v[88:91]\n\t"
    "v_mfma_f32_16x16x32_bf16 v[92:95], v[212:215], v[116:119], v[92:95]\n\t"
    "s_cmp_eq_u32 %[sl], 0\n\t"
    "s_cbranch_scc1 72f\n\t"
    "s_mov_b32 m0, %[sl]\n\t"
    "s_nop 0\n\t"
    "global_load_lds_dword %[dv], %[db] sc1\n\t"
    "72:\n\t"
    "s_waitcnt lgkmcnt(4)\n\t"
    "v_mfma_f32_16x16x32_bf16 v[80:83], v[216:219], v[120:123], v[80:83]\n\t"
    "ds_read_b128 v[112:115], %[ea] offset:28672\n\t"
    "v_mfma_f32_16x16x32_bf16 v[84:87], v[220:223], v[120:123], v[84:87]\n\t"
    "ds_read_b128 v[116:119], %[ea] offset:30720\n\t"
    "v_mfma_f32_16x16x32_bf16 v[88:91], v[216:219], v[124:127], v[88:91]\n\t"
    "v_mfma_f32_16x16x32_bf16 v[92:95], v[220:223], v[124:127], v[92:95]\n\t"
    "s_waitcnt lgkmcnt(4)\n\t"
    "v_mfma_f32_16x16x32_bf16 v[80:83], v[224:227], v[96:99], v[80:83]\n\t"
    "ds_read_b128 v[120:123], %[da] offset:28672\n\t"
    "v_mfma_f32_16x16x32_bf16 v[84:87], v[228:231], v[96:99], v[84:87]\n\t"
    "ds_read_b128 v[124:127], %[da] offset:30720\n\t"
    "v_mfma_f32_16x16x32_bf16 v[88:91], v[224:227], v[100:103], v[88:91]\n\t"
    "v_mfma_f32_16x16x32_bf16 v[92:95], v[228:231], v[100:103], v[92:95]\n\t"
    "s_waitcnt lgkmcnt(4)\n\t"
    "v_mfma_f32_16x16x32_bf16 v[80:83], v[232:235], v[104:107], v[80:83]\n\t"
    "v_mfma_f32_16x16x32_bf16 v[84:87], v[236:239], v[104:107], v[84:87]\n\t"
    "v_mfma_f32_16x16x32_bf16 v[88:91], v[232:235], v[108:111], v[88:91]\n\t"
    "v_xor_b32 %[ea], 0x10000, %[ea]\n\t"
    "v_mfma_f32_16x16x32_bf16 v[92:95], v[236:239], v[108:111], v[92:95]\n\t"
    "v_xor_b32 %[da], 0x10000, %[da]\n\t"
    "s_waitcnt lgkmcnt(2)\n\t"
    "v_mfma_f32_16x16x32_bf16 v[80:83], v[240:243], v[112:115], v[80:83]\n\t"
    "v_mfma_f32_16x16x32_bf16 v[84:87], v[244:247], v[112:115], v[84:87]\n\t"
    "v_mfma_f32_16x16x32_bf16 v[88:91], v[240:243], v[116:119], v[88:91]\n\t"
    "s_add_u32 %[b], %[b], 1\n\t"
    "v_mfma_f32_16x16x32_bf16 v[92:95], v[244:247], v[116:119], v[92:95]\n\t"
    "s_waitcnt lgkmcnt(0)\n\t"
    "v_mfma_f32_16x16x32_bf16 v[80:83], v[248:251], v[120:123], v[80:83]\n\t"
    "v_mfma_f32_16x16x32_bf16 v[84:87], v[252:255], v[120:123], v[84:87]\n\t"
    "v_mfma_f32_16x16x32_bf16 v[88:91], v[248:251], v[124:127], v[88:91]\n\t"
    "v_mfma_f32_16x16x32_bf16 v[92:95], v[252:255], v[124:127], v[92:95]\n\t"
    "s_nop 4\n\t"
    "ds_write_b128 %[rw], v[80:83] offset:0\n\t"
    "ds_write_b128 %[rw], v[84:87] offset:1024\n\t"
    "ds_write_b128 %[rw], v[88:91] offset:2048\n\t"
    "ds_write_b128 %[rw], v[92:95] offset:3072\n\t"
    "s_waitcnt vmcnt(0)\n\t"
    "ds_read_b32 v79, %[sa]\n\t"
    "s_waitcnt lgkmcnt(0)\n\t"
    "v_mad_u32_u24 v79, v79, %[mu], %[ad]\n\t"
    "v_max_i32 v78, v78, v79\n\t"
    "v_mov_b32 v77, %[b]\n\t"
    "v_cmp_ge_i32 vcc, %[b], v78\n\t"
    "s_cmp_ge_i32 %[b], %[ns]\n\t"
    "s_cbranch_scc1 6f\n\t"
    "s_cbranch_vccnz 6f\n\t"
    "v_or_b32 v77, 0x100, v77\n\t"
    "ds_write_b32 %[fa], v77\n\t"
    "s_branch 4b\n\t"
    "6:\n\t"
    "ds_write_b32 %[fa], v77\n\t"
    "s_cbranch_scc0 21b\n\t"
    "s_branch 9f\n\t"
    "5:\n\t"
    "s_waitcnt lgkmcnt(4)\n\t"
    "v_mfma_f32_16x16x32_bf16 v[80:83], v[128:131], v[96:99], 0\n\t"
    "ds_read_b128 v[120:123], %[da] offset:4096\n\t"
    "s_add_u32 m0, %[lb], 0\n\t"
    "v_mfma_f32_16x16x32_bf16 v[84:87], v[132:135], v[96:99], 0\n\t"
    "ds_read_b128 v[124:127], %[da] offset:6144\n\t"
    "global_load_lds_dwordx4 %[av0], s[36:37] sc1\n\t"
    "v_mfma_f32_16x16x32_bf16 v[88:91], v[128:131], v[100:103], 0\n\t"
    "v_mfma_f32_16x16x32_bf16 v[92:95], v[132:135], v[100:103], 0\n\t"
    "s_waitcnt vmcnt(5)\n\t"
    "s_barrier\n\t"
    "s_waitcnt lgkmcnt(4)\n\t"
    "v_mfma_f32_16x16x32_bf16 v[80:83], v[136:139], v[104:107], v[80:83]\n\t"
    "ds_read_b128 v[96:99], %[ea] offset:8192\n\t"
    "s_add_u32 m0, %[lb], 4096\n\t"
    "v_mfma_f32_16x16x32_bf16 v[84:87], v[140:143], v[104:107], v[84:87]\n\t"
    "ds_read_b128 v[100:103], %[ea] offset:10240\n\t"
    "global_load_lds_dwordx4 %[av2], s[36:37] sc1\n\t"
    "v_mfma_f32_16x16x32_bf16 v[88:91], v[136:139], v[108:111], v[88:91]\n\t"
    "v_mfma_f32_16x16x32_bf16 v[92:95], v[140:143], v[108:111], v[92:95]\n\t"
    "s_waitcnt lgkmcnt(4)\n\t"
    "v_mfma_f32_16x16x32_bf16 v[80:83], v[144:147], v[112:115], v[80:83]\n\t"
    "ds_read_b128 v[104:107], %[da] offset:8192\n\t"
    "s_add_u32 m0, %[lb], 8192\n\t"
    "v_mfma_f32_16x16x32_bf16 v[84:87], v[148:151], v[112:115], v[84:87]\n\t"
    "ds_read_b128 v[108:111], %[da] offset:10240\n\t"
    "global_load_lds_dwordx4 %[av4], s[36:37] sc1\n\t"
    "v_mfma_f32_16x16x32_bf16 v[88:91], v[144:147], v[116:119], v[88:91]\n\t"
    "v_mfma_f32_16x16x32_bf16 v[92:95], v[148:151], v[116:119], v[92:95]\n\t"
    "s_waitcnt lgkmcnt(4)\n\t"
    "v_mfma_f32_16x16x32_bf16 v[80:83], v[152:155], v[120:123], v[80:83]\n\t"
    "ds_read_b128 v[112:115], %[ea] offset:12288\n\t"
    "s_add_u32 m0, %[lb], 12288\n\t"
    "v_mfma_f32_16x16x32_bf16 v[84:87], v[156:159], v[120:123], v[84:87]\n\t"
    "ds_read_b128 v[116:119], %[ea] offset:14336\n\t"
    "global_load_lds_dwordx4 %[av6], s[36:37] sc1\n\t"
    "v_mfma_f32_16x16x32_bf16 v[88:91], v[152:155], v[124:127], v[88:91]\n\t"
    "v_mfma_f32_16x16x32_bf16 v[92:95], v[156:159], v[124:127], v[92:95]\n\t"
    "s_waitcnt lgkmcnt(4)\n\t"
    "v_mfma_f32_16x16x32_bf16 v[80:83], v[160:163], v[96:99], v[80:83]\n\t"
    "ds_read_b128 v[120:123], %[da] offset:12288\n\t"
    "s_add_u32 m0, %[lb], 16384\n\t"
    "v_mfma_f32_16x16x32_bf16 v[84:87], v[164:167], v[96:99], v[84:87]\n\t"
    "ds_read_b128 v[124:127], %[da] offset:14336\n\t"
    "global_load_lds_dwordx4 %[av0], s[38:39] sc1\n\t"
    "v_mfma_f32_16x16x32_bf16 v[88:91], v[160:163], v[100:103], v[88:91]\n\t"
    "v_mfma_f32_16x16x32_bf16 v[92:95], v[164:167], v[100:103], v[92:95]\n\t"
    "s_waitcnt vmcnt(7)\n\t"
    "s_barrier\n\t"
    "s_waitcnt lgkmcnt(4)\n\t"
    "v_mfma_f32_16x16x32_bf16 v[80:83], v[168:171], v[104:107], v[80:83]\n\t"
    "ds_read_b128 v[96:99], %[ea] offset:16384\n\t"
    "s_add_u32 m0, %[lb], 20480\n\t"
    "v_mfma_f32_16x16x32_bf16 v[84:87], v[172:175], v[104:107], v[84:87]\n\t"
    "ds_read_b128 v[100:103], %[ea] offset:18432\n\t"
    "global_load_lds_dwordx4 %[av2], s[38:39] sc1\n\t"
    "v_mfma_f32_16x16x32_bf16 v[88:91], v[168:171], v[108:111], v[88:91]\n\t"
    "v_mfma_f32_16x16x32_bf16 v[92:95], v[172:175], v[108:111], v[92:95]\n\t"
    "s_waitcnt lgkmcnt(4)\n\t"
    "v_mfma_f32_16x16x32_bf16 v[80:83], v[176:179], v[112:115], v[80:83]\n\t"
    "ds_read_b128 v[104:107], %[da] offset:16384\n\t"
    "s_add_u32 m0, %[lb], 24576\n\t"
    "v_mfma_f32_16x16x32_bf16 v[84:87], v[180:183], v[112:115], v[84:87]\n\t"
    "ds_read_b128 v[108:111], %[da] offset:18432\n\t"
    "global_load_lds_dwordx4 %[av4], s[38:39] sc1\n\t"
    "v_mfma_f32_16x16x32_bf16 v[88:91], v[176:179], v[116:119], v[88:91]\n\t"
    "v_mfma_f32_16x16x32_bf16 v[92:95], v[180:183], v[116:119], v[92:95]\n\t"
    "s_waitcnt lgkmcnt(4)\n\t"
    "v_mfma_f32_16x16x32_bf16 v[80:83], v[184:187], v[120:123], v[80:83]\n\t"
    "ds_read_b128 v[112:115], %[ea] offset:20480\n\t"
    "s_add_u32 m0, %[lb], 28672\n\t"
    "v_mfma_f32_16x16x32_bf16 v[84:87], v[188:191], v[120:123], v[84:87]\n\t"
    "ds_read_b128 v[116:119], %[ea] offset:22528\n\t"
    "global_load_lds_dwordx4 %[av6], s[38:39] sc1\n\t"
    "v_mfma_f32_16x16x32_bf16 v[88:91], v[184:187], v[124:127], v[88:91]\n\t"
    "v_mfma_f32_16x16x32_bf16 v[92:95], v[188:191], v[124:127], v[92:95]\n\t"
    "s_waitcnt lgkmcnt(4)\n\t"
    "v_mfma_f32_16x16x32_bf16 v[80:83], v[192:195], v[96:99], v[80:83]\n\t"
    "ds_read_b128 v[120:123], %[da] offset:20480\n\t"
    "v_mfma_f32_16x16x32_bf16 v[84:87], v[196:199], v[96:99], v[84:87]\n\t"
    "ds_read_b128 v[124:127], %[da] offset:22528\n\t"
    "v_mfma_f32_16x16x32_bf16 v[88:91], v[192:195], v[100:103], v[88:91]\n\t"
    "v_mfma_f32_16x16x32_bf16 v[92:95], v[196:199], v[100:103], v[92:95]\n\t"
    "s_add_u32 s36, s36, %[ab]\n\t"
    "s_addc_u32 s37, s37, 0\n\t"
    "s_add_u32 s38, s38, %[ab]\n\t"
    "s_addc_u32 s39, s39, 0\n\t"
    "s_xor_b32 %[lb], %[lb], 0x10000\n\t"
    "s_waitcnt vmcnt(8)\n\t"
    "s_barrier\n\t"
    "s_waitcnt lgkmcnt(4)\n\t"
    "v_mfma_f32_16x16x32_bf16 v[80:83], v[200:203], v[104:107], v[80:83]\n\t"
    "ds_read_b128 v[96:99], %[ea] offset:24576\n\t"
    "v_mfma_f32_16x16x32_bf16 v[84:87], v[204:207], v[104:107], v[84:87]\n\t"
    "ds_read_b128 v[100:103], %[ea] offset:26624\n\t"
    "v_mfma_f32_16x16x32_bf16 v[88:91], v[200:203], v[108:111], v[88:91]\n\t"
    "v_mfma_f32_16x16x32_bf16 v[92:95], v[204:207], v[108:111], v[92:95]\n\t"
    "s_waitcnt lgkmcnt(4)\n\t"
    "v_mfma_f32_16x16x32_bf16 v[80:83], v[208:211], v[112:115], v[80:83]\n\t"
    "ds_read_b128 v[104:107], %[da] offset:24576\n\t"
    "v_mfma_f32_16x16x32_bf16 v[84:87], v[212:215], v[112:115], v[84:87]\n\t"
    "ds_read_b128 v[108:111], %[da] offset:26624\n\t"
    "v_mfma_f32_16x16x32_bf16 v[88:91], v[208:211], v[116:119], v[88:91]\n\t"
    "v_mfma_f32_16x16x32_bf16 v[92:95], v[212:215], v[116:119], v[92:95]\n\t"
    "s_cmp_eq_u32 %[sl], 0\n\t"
    "s_cbranch_scc1 73f\n\t"
    "s_mov_b32 m0, %[sl]\n\t"
    "s_nop 0\n\t"
    "global_load_lds_dword %[dv], %[db] sc1\n\t"
    "73:\n\t"
    "s_waitcnt lgkmcnt(4)\n\t"
    "v_mfma_f32_16x16x32_bf16 v[80:83], v[216:219], v[120:123], v[80:83]\n\t"
    "ds_read_b128 v[112:115], %[ea] offset:28672\n\t"
    "v_mfma_f32_16x16x32_bf16 v[84:87], v[220:223], v[120:123], v[84:87]\n\t"
    "ds_read_b128 v[116:119], %[ea] offset:30720\n\t"
    "v_mfma_f32_16x16x32_bf16 v[88:91], v[216:219], v[124:127], v[88:91]\n\t"
    "v_mfma_f32_16x16x32_bf16 v[92:95], v[220:223], v[124:127], v[92:95]\n\t"
    "s_waitcnt lgkmcnt(4)\n\t"
    "v_mfma_f32_16x16x32_bf16 v[80:83], v[224:227], v[96:99], v[80:83]\n\t"
    "ds_read_b128 v[120:123], %[da] offset:28672\n\t"
    "v_mfma_f32_16x16x32_bf16 v[84:87], v[228:231], v[96:99], v[84:87]\n\t"
    "ds_read_b128 v[124:127], %[da] offset:30720\n\t"
    "v_mfma_f32_16x16x32_bf16 v[88:91], v[224:227], v[100:103], v[88:91]\n\t"
    "v_mfma_f32_16x16x32_bf16 v[92:95], v[228:231], v[100:103], v[92:95]\n\t"
    "s_waitcnt lgkmcnt(4)\n\t"
    "v_mfma_f32_16x16x32_bf16 v[80:83], v[232:235], v[104:107], v[80:83]\n\t"
    "v_mfma_f32_16x16x32_bf16 v[84:87], v[236:239], v[104:107], v[84:87]\n\t"
    "v_mfma_f32_16x16x32_bf16 v[88:91], v[232:235], v[108:111], v[88:91]\n\t"
    "v_xor_b32 %[ea], 0x10000, %[ea]\n\t"
    "v_mfma_f32_16x16x32_bf16 v[92:95], v[236:239], v[108:111], v[92:95]\n\t"
    "v_xor_b32 %[da], 0x10000, %[da]\n\t"
    "s_waitcnt lgkmcnt(2)\n\t"
    "v_mfma_f32_16x16x32_bf16 v[80:83], v[240:243], v[112:115], v[80:83]\n\t"
    "v_mfma_f32_16x16x32_bf16 v[84:87], v[244:247], v[112:115], v[84:87]\n\t"
    "v_mfma_f32_16x16x32_bf16 v[88:91], v[240:243], v[116:119], v[88:91]\n\t"
    "s_add_u32 %[b], %[b], 1\n\t"
    "v_mfma_f32_16x16x32_bf16 v[92:95], v[244:247], v[116:119], v[92:95]\n\t"
    "s_waitcnt lgkmcnt(0)\n\t"
    "v_mfma_f32_16x16x32_bf16 v[80:83], v[248:251], v[120:123], v[80:83]\n\t"
    "v_mfma_f32_16x16x32_bf16 v[84:87], v[252:255], v[120:123], v[84:87]\n\t"
    "v_mfma_f32_16x16x32_bf16 v[88:91], v[248:251], v[124:127], v[88:91]\n\t"
    "v_mfma_f32_16x16x32_bf16 v[92:95], v[252:255], v[124:127], v[92:95]\n\t"
    "v_mov_b32 v77, %[b]\n\t"
    "v_or_b32 v77, 0x100, v77\n\t"
    "s_nop 2\n\t"
    "ds_write_b128 %[rw], v[80:83] offset:0\n\t"
    "ds_write_b128 %[rw], v[84:87] offset:1024\n\t"
    "ds_write_b128 %[rw], v[88:91] offset:2048\n\t"
    "ds_write_b128 %[rw], v[92:95] offset:3072\n\t"
    "ds_write_b32 %[fa], v77\n\t"
    "s_waitcnt vmcnt(0)\n\t"
    "ds_read_b32 v79, %[sa]\n\t"
    "s_waitcnt lgkmcnt(0)\n\t"
    "v_mad_u32_u24 v79, v79, %[mu], %[ad]\n\t"
    "v_max_i32 v78, v78, v79\n\t"
    "s_branch 3b\n\t"
    "9:\n\t"
    "s_waitcnt lgkmcnt(0)"
    : [ea] "+&v"(ea), [da] "+&v"(da), [b] "+&s"(b), [st] "=&s"(st), "+{s[36:37]}"(p0), "+{s[38:39]}"(p1), [lb] "+&s"(lb), [cap] "+&s"(cap)
    : [ns] "s"(ns), [ab] "s"(ab), [rw] "v"(rw), [fa] "v"(fa), "{v[128:159]}"(W[0]), "{v[160:191]}"(W[1]), "{v[192:223]}"(W[2]), "{v[224:255]}"(W[3]), [wm] "s"(wm), [rq] "s"(rq), [mu] "s"(mu), [ad] "v"(ad), [dv] "v"(dv), [db] "s"(db), [sl] "s"(sl), [sa] "v"(sa), [av0] "v"(av[0]), [av1] "v"(av[1]), [av2] "v"(av[2]), [av3] "v"(av[3]), [av4] "v"(av[4]), [av5] "v"(av[5]), [av6] "v"(av[6]), [av7] "v"(av[7])
    : "v77", "v78", "v79", "v80", "v81", "v82", "v83", "v84", "v85", "v86", "v87", "v88", "v89", "v90", "v91", "v92", "v93", "v94", "v95", "v96", "v97", "v98", "v99", "v100", "v101", "v102", "v103", "v104", "v105", "v106", "v107", "v108", "v109", "v110", "v111", "v112", "v113", "v114", "v115", "v116", "v117", "v118", "v119", "v120", "v121", "v122", "v123", "v124", "v125", "v126", "v127", "vcc", "scc", "m0", "memory");
}
__device__ __forceinline__ void paced_st0(const u32x32 (&W)[4], unsigned ea, unsigned da, int b, int ns, unsigned ab, unsigned rw, unsigned fa,
    const char* p0, unsigned pp, int po, unsigned cn, const char* pg, unsigned co0, unsigned co1) {
  unsigned st;
  asm volatile("1:\n\t"
    "s_barrier\n\t"
    "ds_read_b128 v[96:99], %[ea] offset:0\n\t"
    "ds_read_b128 v[100:103], %[ea] offset:2048\n\t"
    "ds_read_b128 v[104:107], %[da] offset:0\n\t"
    "ds_read_b128 v[108:111], %[da] offset:2048\n\t"
    "ds_read_b128 v[112:115], %[ea] offset:4096\n\t"
    "ds_read_b128 v[116:119], %[ea] offset:6144\n\t"
    "s_waitcnt lgkmcnt(4)\n\t"
    "v_mfma_f32_16x16x32_bf16 v[80:83], v[128:131], v[96:99], 0\n\t"
    "ds_read_b128 v[120:123], %[da] offset:4096\n\t"
    "v_mfma_f32_16x16x32_bf16 v[84:87], v[132:135], v[96:99], 0\n\t"
    "ds_read_b128 v[124:127], %[da] offset:6144\n\t"
    "v_mfma_f32_16x16x32_bf16 v[88:91], v[128:131], v[100:103], 0\n\t"
    "v_mfma_f32_16x16x32_bf16 v[92:95], v[132:135], v[100:103], 0\n\t"
    "s_barrier\n\t"
    "s_waitcnt lgkmcnt(4)\n\t"
    "v_mfma_f32_16x16x32_bf16 v[80:83], v[136:139], v[104:107], v[80:83]\n\t"
    "ds_read_b128 v[96:99], %[ea] offset:8192\n\t"
    "v_mfma_f32_16x16x32_bf16 v[84:87], v[140:143], v[104:107], v[84:87]\n\t"
    "ds_read_b128 v[100:103], %[ea] offset:10240\n\t"
    "v_mfma_f32_16x16x32_bf16 v[88:91], v[136:139], v[108:111], v[88:91]\n\t"
    "v_mfma_f32_16x16x32_bf16 v[92:95], v[140:143], v[108:111], v[92:95]\n\t"
    "s_waitcnt lgkmcnt(4)\n\t"
    "v_mfma_f32_16x16x32_bf16 v[80:83], v[144:147], v[112:115], v[80:83]\n\t"
    "ds_read_b128 v[104:107], %[da] offset:8192\n\t"
    "v_mfma_f32_16x16x32_bf16 v[84:87], v[148:151], v[112:115], v[84:87]\n\t"
    "ds_read_b128 v[108:111], %[da] offset:10240\n\t"
    "v_mfma_f32_16x16x32_bf16 v[88:91], v[144:147], v[116:119], v[88:91]\n\t"
    "v_mfma_f32_16x16x32_bf16 v[92:95], v[148:151], v[116:119], v[92:95]\n\t"
    "s_waitcnt lgkmcnt(4)\n\t"
    "v_mfma_f32_16x16x32_bf16 v[80:83], v[152:155], v[120:123], v[80:83]\n\t"
    "ds_read_b128 v[112:115], %[ea] offset:12288\n\t"
    "v_mfma_f32_16x16x32_bf16 v[84:87], v[156:159], v[120:123], v[84:87]\n\t"
    "ds_read_b128 v[116:119], %[ea] offset:14336\n\t"
    "v_mfma_f32_16x16x32_bf16 v[88:91], v[152:155], v[124:127], v[88:91]\n\t"
    "v_mfma_f32_16x16x32_bf16 v[92:95], v[156:159], v[124:127], v[92:95]\n\t"
    "s_cmp_lg_u32 %[pp], 0\n\t"
    "s_cbranch_scc1 7f\n\t"
    "8:\n\t"
    "s_waitcnt lgkmcnt(4)\n\t"
    "v_mfma_f32_16x16x32_bf16 v[80:83], v[160:163], v[96:99], v[80:83]\n\t"
    "ds_read_b128 v[120:123], %[da] offset:12288\n\t"
    "v_mfma_f32_16x16x32_bf16 v[84:87], v[164:167], v[96:99], v[84:87]\n\t"
    "ds_read_b128 v[124:127], %[da] offset:14336\n\t"
    "v_mfma_f32_16x16x32_bf16 v[88:91], v[160:163], v[100:103], v[88:91]\n\t"
    "v_mfma_f32_16x16x32_bf16 v[92:95], v[164:167], v[100:103], v[92:95]\n\t"
    "s_barrier\n\t"
    "s_waitcnt lgkmcnt(4)\n\t"
    "v_mfma_f32_16x16x32_bf16 v[80:83], v[168:171], v[104:107], v[80:83]\n\t"
    "ds_read_b128 v[96:99], %[ea] offset:16384\n\t"
    "v_mfma_f32_16x16x32_bf16 v[84:87], v[172:175], v[104:107], v[84:87]\n\t"
    "ds_read_b128 v[100:103], %[ea] offset:18432\n\t"
    "v_mfma_f32_16x16x32_bf16 v[88:91], v[168:171], v[108:111], v[88:91]\n\t"
    "v_mfma_f32_16x16x32_bf16 v[92:95], v[172:175], v[108:111], v[92:95]\n\t"
    "s_waitcnt lgkmcnt(4)\n\t"
    "v_mfma_f32_16x16x32_bf16 v[80:83], v[176:179], v[112:115], v[80:83]\n\t"
    "ds_read_b128 v[104:107], %[da] offset:16384\n\t"
    "v_mfma_f32_16x16x32_bf16 v[84:87], v[180:183], v[112:115], v[84:87]\n\t"
    "ds_read_b128 v[108:111], %[da] offset:18432\n\t"
    "v_mfma_f32_16x16x32_bf16 v[88:91], v[176:179], v[116:119], v[88:91]\n\t"
    "v_mfma_f32_16x16x32_bf16 v[92:95], v[180:183], v[116:119], v[92:95]\n\t"
    "s_waitcnt lgkmcnt(4)\n\t"
    "v_mfma_f32_16x16x32_bf16 v[80:83], v[184:187], v[120:123], v[80:83]\n\t"
    "ds_read_b128 v[112:115], %[ea] offset:20480\n\t"
    "v_mfma_f32_16x16x32_bf16 v[84:87], v[188:191], v[120:123], v[84:87]\n\t"
    "ds_read_b128 v[116:119], %[ea] offset:22528\n\t"
    "v_mfma_f32_16x16x32_bf16 v[88:91], v[184:187], v[124:127], v[88:91]\n\t"
    "v_mfma_f32_16x16x32_bf16 v[92:95], v[188:191], v[124:127], v[92:95]\n\t"
    "s_waitcnt lgkmcnt(4)\n\t"
    "v_mfma_f32_16x16x32_bf16 v[80:83], v[192:195], v[96:99], v[80:83]\n\t"
    "ds_read_b128 v[120:123], %[da] offset:20480\n\t"
    "v_mfma_f32_16x16x32_bf16 v[84:87], v[196:199], v[96:99], v[84:87]\n\t"
    "ds_read_b128 v[124:127], %[da] offset:22528\n\t"
    "v_mfma_f32_16x16x32_bf16 v[88:91], v[192:195], v[100:103], v[88:91]\n\t"
    "v_mfma_f32_16x16x32_bf16 v[92:95], v[196:199], v[100:103], v[92:95]\n\t"
    "s_barrier\n\t"
    "s_waitcnt lgkmcnt(4)\n\t"
    "v_mfma_f32_16x16x32_bf16 v[80:83], v[200:203], v[104:107], v[80:83]\n\t"
    "ds_read_b128 v[96:99], %[ea] offset:24576\n\t"
    "v_mfma_f32_16x16x32_bf16 v[84:87], v[204:207], v[104:107], v[84:87]\n\t"
    "ds_read_b128 v[100:103], %[ea] offset:26624\n\t"
    "v_mfma_f32_16x16x32_bf16 v[88:91], v[200:203], v[108:111], v[88:91]\n\t"
    "v_mfma_f32_16x16x32_bf16 v[92:95], v[204:207], v[108:111], v[92:95]\n\t"
    "s_waitcnt lgkmcnt(4)\n\t"
    "v_mfma_f32_16x16x32_bf16 v[80:83], v[208:211], v[112:115], v[80:83]\n\t"
    "ds_read_b128 v[104:107], %[da] offset:24576\n\t"
    "v_mfma_f32_16x16x32_bf16 v[84:87], v[212:215], v[112:115], v[84:87]\n\t"
    "ds_read_b128 v[108:111], %[da] offset:26624\n\t"
    "v_mfma_f32_16x16x32_bf16 v[88:91], v[208:211], v[116:119], v[88:91]\n\t"
    "v_mfma_f32_16x16x32_bf16 v[92:95], v[212:215], v[116:119], v[92:95]\n\t"
    "s_waitcnt lgkmcnt(4)\n\t"
    "v_mfma_f32_16x16x32_bf16 v[80:83], v[216:219], v[120:123], v[80:83]\n\t"
    "ds_read_b128 v[112:115], %[ea] offset:28672\n\t"
    "v_mfma_f32_16x16x32_bf16 v[84:87], v[220:223], v[120:123], v[84:87]\n\t"
    "ds_read_b128 v[116:119], %[ea] offset:30720\n\t"
    "v_mfma_f32_16x16x32_bf16 v[88:91], v[216:219], v[124:127], v[88:91]\n\t"
    "v_mfma_f32_16x16x32_bf16 v[92:95], v[220:223], v[124:127], v[92:95]\n\t"
    "s_waitcnt lgkmcnt(4)\n\t"
    "v_mfma_f32_16x16x32_bf16 v[80:83], v[224:227], v[96:99], v[80:83]\n\t"
    "ds_read_b128 v[120:123], %[da] offset:28672\n\t"
    "v_mfma_f32_16x16x32_bf16 v[84:87], v[228:231], v[96:99], v[84:87]\n\t"
    "ds_read_b128 v[124:127], %[da] offset:30720\n\t"
    "v_mfma_f32_16x16x32_bf16 v[88:91], v[224:227], v[100:103], v[88:91]\n\t"
    "v_mfma_f32_16x16x32_bf16 v[92:95], v[228:231], v[100:103], v[92:95]\n\t"
    "s_waitcnt lgkmcnt(4)\n\t"
    "v_mfma_f32_16x16x32_bf16 v[80:83], v[232:235], v[104:107], v[80:83]\n\t"
    "v_mfma_f32_16x16x32_bf16 v[84:87], v[236:239], v[104:107], v[84:87]\n\t"
    "v_mfma_f32_16x16x32_bf16 v[88:91], v[232:235], v[108:111], v[88:91]\n\t"
    "v_xor_b32 %[ea], 0x10000, %[ea]\n\t"
    "v_mfma_f32_16x16x32_bf16 v[92:95], v[236:239], v[108:111], v[92:95]\n\t"
    "v_xor_b32 %[da], 0x10000, %[da]\n\t"
    "s_waitcnt lgkmcnt(2)\n\t"
    "v_mfma_f32_16x16x32_bf16 v[80:83], v[240:243], v[112:115], v[80:83]\n\t"
    "v_mfma_f32_16x16x32_bf16 v[84:87], v[244:247], v[112:115], v[84:87]\n\t"
    "v_mfma_f32_16x16x32_bf16 v[88:91], v[240:243], v[116:119], v[88:91]\n\t"
    "s_add_u32 %[b], %[b], 1\n\t"
    "v_mfma_f32_16x16x32_bf16 v[92:95], v[244:247], v[116:119], v[92:95]\n\t"
    "s_waitcnt lgkmcnt(0)\n\t"
    "v_mfma_f32_16x16x32_bf16 v[80:83], v[248:251], v[120:123], v[80:83]\n\t"
    "v_mfma_f32_16x16x32_bf16 v[84:87], v[252:255], v[120:123], v[84:87]\n\t"
    "v_mfma_f32_16x16x32_bf16 v[88:91], v[248:251], v[124:127], v[88:91]\n\t"
    "v_mfma_f32_16x16x32_bf16 v[92:95], v[252:255], v[124:127], v[92:95]\n\t"
    "2:\n\t"
    "ds_read_b32 v39, %[fa]\n\t"
    "ds_read_b128 v[48:51], %[rw] offset:0\n\t"
    "ds_read_b128 v[52:55], %[rw] offset:1024\n\t"
    "ds_read_b128 v[56:59], %[rw] offset:2048\n\t"
    "ds_read_b128 v[60:63], %[rw] offset:3072\n\t"
    "s_waitcnt lgkmcnt(2)\n\t"
    "v_readfirstlane_b32 %[pp], v39\n\t"
    "s_and_b32 %[st], %[pp], 0xff\n\t"
    "s_cmp_eq_u32 %[st], %[b]\n\t"
    "s_cbranch_scc1 3f\n\t"
    "s_waitcnt lgkmcnt(0)\n\t"
    "s_branch 2b\n\t"
    "3:\n\t"
    "v_add_f32 v48, v48, v80\n\t"
    "v_add_f32 v49, v49, v81\n\t"
    "v_add_f32 v50, v50, v82\n\t"
    "v_add_f32 v51, v51, v83\n\t"
    "v_add_f32 v52, v52, v84\n\t"
    "v_add_f32 v53, v53, v85\n\t"
    "v_add_f32 v54, v54, v86\n\t"
    "v_add_f32 v55, v55, v87\n\t"
    "v_cvt_pk_bf16_f32 v40, v48, v49\n\t"
    "v_cvt_pk_bf16_f32 v41, v50, v51\n\t"
    "v_cvt_pk_bf16_f32 v42, v52, v53\n\t"
    "v_cvt_pk_bf16_f32 v43, v54, v55\n\t"
    "global_store_dwordx4 %[co0], v[40:43], s[36:37] sc1\n\t"
    "s_waitcnt lgkmcnt(0)\n\t"
    "v_add_f32 v56, v56, v88\n\t"
    "v_add_f32 v57, v57, v89\n\t"
    "v_add_f32 v58, v58, v90\n\t"
    "v_add_f32 v59, v59, v91\n\t"
    "v_add_f32 v60, v60, v92\n\t"
    "v_add_f32 v61, v61, v93\n\t"
    "v_add_f32 v62, v62, v94\n\t"
    "v_add_f32 v63, v63, v95\n\t"
    "v_cvt_pk_bf16_f32 v44, v56, v57\n\t"
    "v_cvt_pk_bf16_f32 v45, v58, v59\n\t"
    "v_cvt_pk_bf16_f32 v46, v60, v61\n\t"
    "v_cvt_pk_bf16_f32 v47, v62, v63\n\t"
    "global_store_dwordx4 %[co1], v[44:47], s[36:37] sc1\n\t"
    "s_add_u32 s36, s36, %[ab]\n\t"
    "s_addc_u32 s37, s37, 0\n\t"
    "s_lshr_b32 %[pp], %[pp], 8\n\t"
    "s_cbranch_scc1 1b\n\t"
    "v_mov_b32 v72, %[cn]\n\t"
    "v_mov_b32 v73, 1\n\t"
    "s_add_u32 %[st], %[b], %[po]\n\t"
    "v_mov_b32 v74, %[st]\n\t"
    "v_mov_b32 v75, 0\n\t"
    "s_waitcnt vmcnt(0)\n\t"
    "s_mov_b64 exec, 1\n\t"
    "ds_add_rtn_u32 v73, v72, v73\n\t"
    "s_waitcnt lgkmcnt(0)\n\t"
    "v_readfirstlane_b32 %[st], v73\n\t"
    "s_and_b32 %[st], %[st], 3\n\t"
    "s_cmp_lg_u32 %[st], 3\n\t"
    "s_cbranch_scc1 61f\n\t"
    "global_store_dword v75, v74, %[pg] sc1\n\t"
    "61:\n\t"
    "s_mov_b64 exec, -1\n\t"
    "s_mov_b32 %[pp], 0\n\t"
    "s_cmp_lt_i32 %[b], %[ns]\n\t"
    "s_cbranch_scc1 1b\n\t"
    "s_branch 9f\n\t"
    "7:\n\t"
    "v_mov_b32 v72, %[cn]\n\t"
    "v_mov_b32 v73, 1\n\t"
    "s_add_u32 %[st], %[b], %[po]\n\t"
    "v_mov_b32 v74, %[st]\n\t"
    "v_mov_b32 v75, 0\n\t"
    "s_waitcnt vmcnt(0)\n\t"
    "s_mov_b64 exec, 1\n\t"
    "ds_add_rtn_u32 v73, v72, v73\n\t"
    "s_waitcnt lgkmcnt(0)\n\t"
    "v_readfirstlane_b32 %[st], v73\n\t"
    "s_and_b32 %[st], %[st], 3\n\t"
    "s_cmp_lg_u32 %[st], 3\n\t"
    "s_cbranch_scc1 61f\n\t"
    "global_store_dword v75, v74, %[pg] sc1\n\t"
    "61:\n\t"
    "s_mov_b64 exec, -1\n\t"
    "s_mov_b32 %[pp], 0\n\t"
    "s_branch 8b\n\t"
    "9:"
    : [ea] "+&v"(ea), [da] "+&v"(da), [b] "+&s"(b), [st] "=&s"(st), "+{s[36:37]}"(p0), [pp] "+&s"(pp)
    : [ns] "s"(ns), [ab] "s"(ab), [rw] "v"(rw), [fa] "v"(fa), "{v[128:159]}"(W[0]), "{v[160:191]}"(W[1]), "{v[192:223]}"(W[2]), "{v[224:255]}"(W[3]), [po] "s"(po), [cn] "s"(cn), [pg] "s"(pg), [co0] "v"(co0), [co1] "v"(co1)
    : "v39", "v40", "v41", "v42", "v43", "v44", "v45", "v46", "v47", "v48", "v49", "v50", "v51", "v52", "v53", "v54", "v55", "v56", "v57", "v58", "v59", "v60", "v61", "v62", "v63", "v72", "v73", "v74", "v75", "v80", "v81", "v82", "v83", "v84", "v85", "v86", "v87", "v88", "v89", "v90", "v91", "v92", "v93", "v94", "v95", "v96", "v97", "v98", "v99", "v100", "v101", "v102", "v103", "v104", "v105", "v106", "v107", "v108", "v109", "v110", "v111", "v112", "v113", "v114", "v115", "v116", "v117", "v118", "v119", "v120", "v121", "v122", "v123", "v124", "v125", "v126", "v127", "vcc", "scc", "m0", "memory");
}
__device__ __forceinline__ void paced_st1(const u32x32 (&W)[4], unsigned ea, unsigned da, int b, int ns, unsigned ab, unsigned rw, unsigned fa,
    const char* p0, unsigned pp, int po, unsigned cn, const char* pg, unsigned co0, unsigned co1) {
  unsigned st;
  asm volatile("1:\n\t"
    "s_barrier\n\t"
    "ds_read_b128 v[96:99], %[ea] offset:0\n\t"
    "ds_read_b128 v[100:103], %[ea] offset:2048\n\t"
    "ds_read_b128 v[104:107], %[da] offset:0\n\t"
    "ds_read_b128 v[108:111], %[da] offset:2048\n\t"
    "ds_read_b128 v[112:115], %[ea] offset:4096\n\t"
    "ds_read_b128 v[116:119], %[ea] offset:6144\n\t"
    "s_waitcnt lgkmcnt(4)\n\t"
    "v_mfma_f32_16x16x32_bf16 v[80:83], v[128:131], v[96:99], 0\n\t"
    "ds_read_b128 v[120:123], %[da] offset:4096\n\t"
    "v_mfma_f32_16x16x32_bf16 v[84:87], v[132:135], v[96:99], 0\n\t"
    "ds_read_b128 v[124:127], %[da] offset:6144\n\t"
    "v_mfma_f32_16x16x32_bf16 v[88:91], v[128:131], v[100:103], 0\n\t"
    "v_mfma_f32_16x16x32_bf16 v[92:95], v[132:135], v[100:103], 0\n\t"
    "s_barrier\n\t"
    "s_waitcnt lgkmcnt(4)\n\t"
    "v_mfma_f32_16x16x32_bf16 v[80:83], v[136:139], v[104:107], v[80:83]\n\t"
    "ds_read_b128 v[96:99], %[ea] offset:8192\n\t"
    "v_mfma_f32_16x16x32_bf16 v[84:87], v[140:143], v[104:107], v[84:87]\n\t"
    "ds_read_b128 v[100:103], %[ea] offset:10240\n\t"
    "v_mfma_f32_16x16x32_bf16 v[88:91], v[136:139], v[108:111], v[88:91]\n\t"
    "v_mfma_f32_16x16x32_bf16 v[92:95], v[140:143], v[108:111], v[92:95]\n\t"
    "s_waitcnt lgkmcnt(4)\n\t"
    "v_mfma_f32_16x16x32_bf16 v[80:83], v[144:147], v[112:115], v[80:83]\n\t"
    "ds_read_b128 v[104:107], %[da] offset:8192\n\t"
    "v_mfma_f32_16x16x32_bf16 v[84:87], v[148:151], v[112:115], v[84:87]\n\t"
    "ds_read_b128 v[108:111], %[da] offset:10240\n\t"
    "v_mfma_f32_16x16x32_bf16 v[88:91], v[144:147], v[116:119], v[88:91]\n\t"
    "v_mfma_f32_16x16x32_bf16 v[92:95], v[148:151], v[116:119], v[92:95]\n\t"
    "s_waitcnt lgkmcnt(4)\n\t"
    "v_mfma_f32_16x16x32_bf16 v[80:83], v[152:155], v[120:123], v[80:83]\n\t"
    "ds_read_b128 v[112:115], %[ea] offset:12288\n\t"
    "v_mfma_f32_16x16x32_bf16 v[84:87], v[156:159], v[120:123], v[84:87]\n\t"
    "ds_read_b128 v[116:119], %[ea] offset:14336\n\t"
    "v_mfma_f32_16x16x32_bf16 v[88:91], v[152:155], v[124:127], v[88:91]\n\t"
    "v_mfma_f32_16x16x32_bf16 v[92:95], v[156:159], v[124:127], v[92:95]\n\t"
    "s_cmp_lg_u32 %[pp], 0\n\t"
    "s_cbranch_scc1 7f\n\t"
    "8:\n\t"
    "s_waitcnt lgkmcnt(4)\n\t"
    "v_mfma_f32_16x16x32_bf16 v[80:83], v[160:163], v[96:99], v[80:83]\n\t"
    "ds_read_b128 v[120:123], %[da] offset:12288\n\t"
    "v_mfma_f32_16x16x32_bf16 v[84:87], v[164:167], v[96:99], v[84:87]\n\t"
    "ds_read_b128 v[124:127], %[da] offset:14336\n\t"
    "v_mfma_f32_16x16x32_bf16 v[88:91], v[160:163], v[100:103], v[88:91]\n\t"
    "v_mfma_f32_16x16x32_bf16 v[92:95], v[164:167], v[100:103], v[92:95]\n\t"
    "s_barrier\n\t"
    "s_waitcnt lgkmcnt(4)\n\t"
    "v_mfma_f32_16x16x32_bf16 v[80:83], v[168:171], v[104:107], v[80:83]\n\t"
    "ds_read_b128 v[96:99], %[ea] offset:16384\n\t"
    "v_mfma_f32_16x16x32_bf16 v[84:87], v[172:175], v[104:107], v[84:87]\n\t"
    "ds_read_b128 v[100:103], %[ea] offset:18432\n\t"
    "v_mfma_f32_16x16x32_bf16 v[88:91], v[168:171], v[108:111], v[88:91]\n\t"
    "v_mfma_f32_16x16x32_bf16 v[92:95], v[172:175], v[108:111], v[92:95]\n\t"
    "s_waitcnt lgkmcnt(4)\n\t"
    "v_mfma_f32_16x16x32_bf16 v[80:83], v[176:179], v[112:115], v[80:83]\n\t"
    "ds_read_b128 v[104:107], %[da] offset:16384\n\t"
    "v_mfma_f32_16x16x32_bf16 v[84:87], v[180:183], v[112:115], v[84:87]\n\t"
    "ds_read_b128 v[108:111], %[da] offset:18432\n\t"
    "v_mfma_f32_16x16x32_bf16 v[88:91], v[176:179], v[116:119], v[88:91]\n\t"
    "v_mfma_f32_16x16x32_bf16 v[92:95], v[180:183], v[116:119], v[92:95]\n\t"
    "s_waitcnt lgkmcnt(4)\n\t"
    "v_mfma_f32_16x16x32_bf16 v[80:83], v[184:187], v[120:123], v[80:83]\n\t"
    "ds_read_b128 v[112:115], %[ea] offset:20480\n\t"
    "v_mfma_f32_16x16x32_bf16 v[84:87], v[188:191], v[120:123], v[84:87]\n\t"
    "ds_read_b128 v[116:119], %[ea] offset:22528\n\t"
    "v_mfma_f32_16x16x32_bf16 v[88:91], v[184:187], v[124:127], v[88:91]\n\t"
    "v_mfma_f32_16x16x32_bf16 v[92:95], v[188:191], v[124:127], v[92:95]\n\t"
    "s_waitcnt lgkmcnt(4)\n\t"
    "v_mfma_f32_16x16x32_bf16 v[80:83], v[192:195], v[96:99], v[80:83]\n\t"
    "ds_read_b128 v[120:123], %[da] offset:20480\n\t"
    "v_mfma_f32_16x16x32_bf16 v[84:87], v[196:199], v[96:99], v[84:87]\n\t"
    "ds_read_b128 v[124:127], %[da] offset:22528\n\t"
    "v_mfma_f32_16x16x32_bf16 v[88:91], v[192:195], v[100:103], v[88:91]\n\t"
    "v_mfma_f32_16x16x32_bf16 v[92:95], v[196:199], v[100:103], v[92:95]\n\t"
    "s_barrier\n\t"
    "s_waitcnt lgkmcnt(4)\n\t"
    "v_mfma_f32_16x16x32_bf16 v[80:83], v[200:203], v[104:107], v[80:83]\n\t"
    "ds_read_b128 v[96:99], %[ea] offset:24576\n\t"
    "v_mfma_f32_16x16x32_bf16 v[84:87], v[204:207], v[104:107], v[84:87]\n\t"
    "ds_read_b128 v[100:103], %[ea] offset:26624\n\t"
    "v_mfma_f32_16x16x32_bf16 v[88:91], v[200:203], v[108:111], v[88:91]\n\t"
    "v_mfma_f32_16x16x32_bf16 v[92:95], v[204:207], v[108:111], v[92:95]\n\t"
    "s_waitcnt lgkmcnt(4)\n\t"
    "v_mfma_f32_16x16x32_bf16 v[80:83], v[208:211], v[112:115], v[80:83]\n\t"
    "ds_read_b128 v[104:107], %[da] offset:24576\n\t"
    "v_mfma_f32_16x16x32_bf16 v[84:87], v[212:215], v[112:115], v[84:87]\n\t"
    "ds_read_b128 v[108:111], %[da] offset:26624\n\t"
    "v_mfma_f32_16x16x32_bf16 v[88:91], v[208:211], v[116:119], v[88:91]\n\t"
    "v_mfma_f32_16x16x32_bf16 v[92:95], v[212:215], v[116:119], v[92:95]\n\t"
    "s_waitcnt lgkmcnt(4)\n\t"
    "v_mfma_f32_16x16x32_bf16 v[80:83], v[216:219], v[120:123], v[80:83]\n\t"
    "ds_read_b128 v[112:115], %[ea] offset:28672\n\t"
    "v_mfma_f32_16x16x32_bf16 v[84:87], v[220:223], v[120:123], v[84:87]\n\t"
    "ds_read_b128 v[116:119], %[ea] offset:30720\n\t"
    "v_mfma_f32_16x16x32_bf16 v[88:91], v[216:219], v[124:127], v[88:91]\n\t"
    "v_mfma_f32_16x16x32_bf16 v[92:95], v[220:223], v[124:127], v[92:95]\n\t"
    "s_waitcnt lgkmcnt(4)\n\t"
    "v_mfma_f32_16x16x32_bf16 v[80:83], v[224:227], v[96:99], v[80:83]\n\t"
    "ds_read_b128 v[120:123], %[da] offset:28672\n\t"
    "v_mfma_f32_16x16x32_bf16 v[84:87], v[228:231], v[96:99], v[84:87]\n\t"
    "ds_read_b128 v[124:127], %[da] offset:30720\n\t"
    "v_mfma_f32_16x16x32_bf16 v[88:91], v[224:227], v[100:103], v[88:91]\n\t"
    "v_mfma_f32_16x16x32_bf16 v[92:95], v[228:231], v[100:103], v[92:95]\n\t"
    "s_waitcnt lgkmcnt(4)\n\t"
    "v_mfma_f32_16x16x32_bf16 v[80:83], v[232:235], v[104:107], v[80:83]\n\t"
    "v_mfma_f32_16x16x32_bf16 v[84:87], v[236:239], v[104:107], v[84:87]\n\t"
    "v_mfma_f32_16x16x32_bf16 v[88:91], v[232:235], v[108:111], v[88:91]\n\t"
    "v_xor_b32 %[ea], 0x10000, %[ea]\n\t"
    "v_mfma_f32_16x16x32_bf16 v[92:95], v[236:239], v[108:111], v[92:95]\n\t"
    "v_xor_b32 %[da], 0x10000, %[da]\n\t"
    "s_waitcnt lgkmcnt(2)\n\t"
    "v_mfma_f32_16x16x32_bf16 v[80:83], v[240:243], v[112:115], v[80:83]\n\t"
    "v_mfma_f32_16x16x32_bf16 v[84:87], v[244:247], v[112:115], v[84:87]\n\t"
    "v_mfma_f32_16x16x32_bf16 v[88:91], v[240:243], v[116:119], v[88:91]\n\t"
    "s_add_u32 %[b], %[b], 1\n\t"
    "v_mfma_f32_16x16x32_bf16 v[92:95], v[244:247], v[116:119], v[92:95]\n\t"
    "s_waitcnt lgkmcnt(0)\n\t"
    "v_mfma_f32_16x16x32_bf16 v[80:83], v[248:251], v[120:123], v[80:83]\n\t"
    "v_mfma_f32_16x16x32_bf16 v[84:87], v[252:255], v[120:123], v[84:87]\n\t"
    "v_mfma_f32_16x16x32_bf16 v[88:91], v[248:251], v[124:127], v[88:91]\n\t"
    "v_mfma_f32_16x16x32_bf16 v[92:95], v[252:255], v[124:127], v[92:95]\n\t"
    "2:\n\t"
    "ds_read_b32 v39, %[fa]\n\t"
    "ds_read_b128 v[48:51], %[rw] offset:0\n\t"
    "ds_read_b128 v[52:55], %[rw] offset:1024\n\t"
    "ds_read_b128 v[56:59], %[rw] offset:2048\n\t"
    "ds_read_b128 v[60:63], %[rw] offset:3072\n\t"
    "s_waitcnt lgkmcnt(2)\n\t"
    "v_readfirstlane_b32 %[pp], v39\n\t"
    "s_and_b32 %[st], %[pp], 0xff\n\t"
    "s_cmp_eq_u32 %[st], %[b]\n\t"
    "s_cbranch_scc1 3f\n\t"
    "s_waitcnt lgkmcnt(0)\n\t"
    "s_branch 2b\n\t"
    "3:\n\t"
    "v_add_f32 v48, v48, v80\n\t"
    "v_add_f32 v49, v49, v81\n\t"
    "v_add_f32 v50, v50, v82\n\t"
    "v_add_f32 v51, v51, v83\n\t"
    "v_add_f32 v52, v52, v84\n\t"
    "v_add_f32 v53, v53, v85\n\t"
    "v_add_f32 v54, v54, v86\n\t"
    "v_add_f32 v55, v55, v87\n\t"
    "s_waitcnt lgkmcnt(0)\n\t"
    "v_add_f32 v56, v56, v88\n\t"
    "v_add_f32 v57, v57, v89\n\t"
    "v_add_f32 v58, v58, v90\n\t"
    "v_add_f32 v59, v59, v91\n\t"
    "v_add_f32 v60, v60, v92\n\t"
    "v_add_f32 v61, v61, v93\n\t"
    "v_add_f32 v62, v62, v94\n\t"
    "v_add_f32 v63, v63, v95\n\t"
    "v_mul_f32 v40, %[nl], v48\n\t"
    "v_mul_f32 v41, %[nl], v49\n\t"
    "v_mul_f32 v42, %[nl], v50\n\t"
    "v_mul_f32 v43, %[nl], v51\n\t"
    "v_mul_f32 v44, %[nl], v56\n\t"
    "v_mul_f32 v45, %[nl], v57\n\t"
    "v_mul_f32 v46, %[nl], v58\n\t"
    "v_mul_f32 v47, %[nl], v59\n\t"
    "v_exp_f32 v40, v40\n\t"
    "v_exp_f32 v41, v41\n\t"
    "v_exp_f32 v42, v42\n\t"
    "v_exp_f32 v43, v43\n\t"
    "v_exp_f32 v44, v44\n\t"
    "v_exp_f32 v45, v45\n\t"
    "v_exp_f32 v46, v46\n\t"
    "v_exp_f32 v47, v47\n\t"
    "v_add_f32 v40, 1.0, v40\n\t"
    "v_add_f32 v41, 1.0, v41\n\t"
    "v_add_f32 v42, 1.0, v42\n\t"
    "v_add_f32 v43, 1.0, v43\n\t"
    "v_add_f32 v44, 1.0, v44\n\t"
    "v_add_f32 v45, 1.0, v45\n\t"
    "v_add_f32 v46, 1.0, v46\n\t"
    "v_add_f32 v47, 1.0, v47\n\t"
    "v_rcp_f32 v40, v40\n\t"
    "v_rcp_f32 v41, v41\n\t"
    "v_rcp_f32 v42, v42\n\t"
    "v_rcp_f32 v43, v43\n\t"
    "v_rcp_f32 v44, v44\n\t"
    "v_rcp_f32 v45, v45\n\t"
    "v_rcp_f32 v46, v46\n\t"
    "v_rcp_f32 v47, v47\n\t"
    "v_mul_f32 v40, v48, v40\n\t"
    "v_mul_f32 v41, v49, v41\n\t"
    "v_mul_f32 v42, v50, v42\n\t"
    "v_mul_f32 v43, v51, v43\n\t"
    "v_mul_f32 v44, v56, v44\n\t"
    "v_mul_f32 v45, v57, v45\n\t"
    "v_mul_f32 v46, v58, v46\n\t"
    "v_mul_f32 v47, v59, v47\n\t"
    "v_mul_f32 v40, v40, v52\n\t"
    "v_mul_f32 v41, v41, v53\n\t"
    "v_mul_f32 v42, v42, v54\n\t"
    "v_mul_f32 v43, v43, v55\n\t"
    "v_mul_f32 v44, v44, v60\n\t"
    "v_mul_f32 v45, v45, v61\n\t"
    "v_mul_f32 v46, v46, v62\n\t"
    "v_mul_f32 v47, v47, v63\n\t"
    "v_cvt_pk_bf16_f32 v48, v40, v41\n\t"
    "v_cvt_pk_bf16_f32 v49, v42, v43\n\t"
    "v_cvt_pk_bf16_f32 v50, v44, v45\n\t"
    "v_cvt_pk_bf16_f32 v51, v46, v47\n\t"
    "s_nop 1\n\t"
    "v_permlane16_swap_b32 v48, v50\n\t"
    "v_permlane16_swap_b32 v49, v51\n\t"
    "global_store_dwordx4 %[co0], v[48:51], s[36:37] sc1\n\t"
    "s_add_u32 s36, s36, %[ab]\n\t"
    "s_addc_u32 s37, s37, 0\n\t"
    "s_lshr_b32 %[pp], %[pp], 8\n\t"
    "s_cbranch_scc1 1b\n\t"
    "v_mov_b32 v72, %[cn]\n\t"
    "v_mov_b32 v73, 1\n\t"
    "s_add_u32 %[st], %[b], %[po]\n\t"
    "v_mov_b32 v74, %[st]\n\t"
    "v_mov_b32 v75, 0\n\t"
    "s_waitcnt vmcnt(0)\n\t"
    "s_mov_b64 exec, 1\n\t"
    "ds_add_rtn_u32 v73, v72, v73\n\t"
    "s_waitcnt lgkmcnt(0)\n\t"
    "v_readfirstlane_b32 %[st], v73\n\t"
    "s_and_b32 %[st], %[st], 3\n\t"
    "s_cmp_lg_u32 %[st], 3\n\t"
    "s_cbranch_scc1 61f\n\t"
    "global_store_dword v75, v74, %[pg] sc1\n\t"
    "61:\n\t"
    "s_mov_b64 exec, -1\n\t"
    "s_mov_b32 %[pp], 0\n\t"
    "s_cmp_lt_i32 %[b], %[ns]\n\t"
    "s_cbranch_scc1 1b\n\t"
    "s_branch 9f\n\t"
    "7:\n\t"
    "v_mov_b32 v72, %[cn]\n\t"
    "v_mov_b32 v73, 1\n\t"
    "s_add_u32 %[st], %[b], %[po]\n\t"
    "v_mov_b32 v74, %[st]\n\t"
    "v_mov_b32 v75, 0\n\t"
    "s_waitcnt vmcnt(0)\n\t"
    "s_mov_b64 exec, 1\n\t"
    "ds_add_rtn_u32 v73, v72, v73\n\t"
    "s_waitcnt lgkmcnt(0)\n\t"
    "v_readfirstlane_b32 %[st], v73\n\t"
    "s_and_b32 %[st], %[st], 3\n\t"
    "s_cmp_lg_u32 %[st], 3\n\t"
    "s_cbranch_scc1 61f\n\t"
    "global_store_dword v75, v74, %[pg] sc1\n\t"
    "61:\n\t"
    "s_mov_b64 exec, -1\n\t"
    "s_mov_b32 %[pp], 0\n\t"
    "s_branch 8b\n\t"
    "9:"
    : [ea] "+&v"(ea), [da] "+&v"(da), [b] "+&s"(b), [st] "=&s"(st), "+{s[36:37]}"(p0), [pp] "+&s"(pp)
    : [ns] "s"(ns), [ab] "s"(ab), [rw] "v"(rw), [fa] "v"(fa), "{v[128:159]}"(W[0]), "{v[160:191]}"(W[1]), "{v[192:223]}"(W[2]), "{v[224:255]}"(W[3]), [po] "s"(po), [cn] "s"(cn), [pg] "s"(pg), [co0] "v"(co0), [nl] "s"(-LOG2E)
    : "v39", "v40", "v41", "v42", "v43", "v44", "v45", "v46", "v47", "v48", "v49", "v50", "v51", "v52", "v53", "v54", "v55", "v56", "v57", "v58", "v59", "v60", "v61", "v62", "v63", "v72", "v73", "v74", "v75", "v80", "v81", "v82", "v83", "v84", "v85", "v86", "v87", "v88", "v89", "v90", "v91", "v92", "v93", "v94", "v95", "v96", "v97", "v98", "v99", "v100", "v101", "v102", "v103", "v104", "v105", "v106", "v107", "v108", "v109", "v110", "v111", "v112", "v113", "v114", "v115", "v116", "v117", "v118", "v119", "v120", "v121", "v122", "v123", "v124", "v125", "v126", "v127", "vcc", "scc", "m0", "memory");
}
__device__ __forceinline__ void paced_st2(const u32x32 (&W)[4], unsigned ea, unsigned da, int b, int ns, unsigned ab, unsigned rw, unsigned fa,
    const char* p0, unsigned pp, int po, unsigned cn, const char* pg, unsigned co0, unsigned co1) {
  unsigned st;
  asm volatile("1:\n\t"
    "s_barrier\n\t"
    "ds_read_b128 v[96:99], %[ea] offset:0\n\t"
    "ds_read_b128 v[100:103], %[ea] offset:2048\n\t"
    "ds_read_b128 v[104:107], %[da] offset:0\n\t"
    "ds_read_b128 v[108:111], %[da] offset:2048\n\t"
    "ds_read_b128 v[112:115], %[ea] offset:4096\n\t"
    "ds_read_b128 v[116:119], %[ea] offset:6144\n\t"
    "s_waitcnt lgkmcnt(4)\n\t"
    "v_mfma_f32_16x16x32_bf16 v[80:83], v[128:131], v[96:99], 0\n\t"
    "ds_read_b128 v[120:123], %[da] offset:4096\n\t"
    "v_mfma_f32_16x16x32_bf16 v[84:87], v[132:135], v[96:99], 0\n\t"
    "ds_read_b128 v[124:127], %[da] offset:6144\n\t"
    "v_mfma_f32_16x16x32_bf16 v[88:91], v[128:131], v[100:103], 0\n\t"
    "v_mfma_f32_16x16x32_bf16 v[92:95], v[132:135], v[100:103], 0\n\t"
    "s_barrier\n\t"
    "s_waitcnt lgkmcnt(4)\n\t"
    "v_mfma_f32_16x16x32_bf16 v[80:83], v[136:139], v[104:107], v[80:83]\n\t"
    "ds_read_b128 v[96:99], %[ea] offset:8192\n\t"
    "v_mfma_f32_16x16x32_bf16 v[84:87], v[140:143], v[104:107], v[84:87]\n\t"
    "ds_read_b128 v[100:103], %[ea] offset:10240\n\t"
    "v_mfma_f32_16x16x32_bf16 v[88:91], v[136:139], v[108:111], v[88:91]\n\t"
    "v_mfma_f32_16x16x32_bf16 v[92:95], v[140:143], v[108:111], v[92:95]\n\t"
    "s_waitcnt lgkmcnt(4)\n\t"
    "v_mfma_f32_16x16x32_bf16 v[80:83], v[144:147], v[112:115], v[80:83]\n\t"
    "ds_read_b128 v[104:107], %[da] offset:8192\n\t"
    "v_mfma_f32_16x16x32_bf16 v[84:87], v[148:151], v[112:115], v[84:87]\n\t"
    "ds_read_b128 v[108:111], %[da] offset:10240\n\t"
    "v_mfma_f32_16x16x32_bf16 v[88:91], v[144:147], v[116:119], v[88:91]\n\t"
    "v_mfma_f32_16x16x32_bf16 v[92:95], v[148:151], v[116:119], v[92:95]\n\t"
    "s_waitcnt lgkmcnt(4)\n\t"
    "v_mfma_f32_16x16x32_bf16 v[80:83], v[152:155], v[120:123], v[80:83]\n\t"
    "ds_read_b128 v[112:115], %[ea] offset:12288\n\t"
    "v_mfma_f32_16x16x32_bf16 v[84:87], v[156:159], v[120:123], v[84:87]\n\t"
    "ds_read_b128 v[116:119], %[ea] offset:14336\n\t"
    "v_mfma_f32_16x16x32_bf16 v[88:91], v[152:155], v[124:127], v[88:91]\n\t"
    "v_mfma_f32_16x16x32_bf16 v[92:95], v[156:159], v[124:127], v[92:95]\n\t"
    "s_cmp_lg_u32 %[pp], 0\n\t"
    "s_cbranch_scc1 7f\n\t"
    "8:\n\t"
    "s_waitcnt lgkmcnt(4)\n\t"
    "v_mfma_f32_16x16x32_bf16 v[80:83], v[160:163], v[96:99], v[80:83]\n\t"
    "ds_read_b128 v[120:123], %[da] offset:12288\n\t"
    "v_mfma_f32_16x16x32_bf16 v[84:87], v[164:167], v[96:99], v[84:87]\n\t"
    "ds_read_b128 v[124:127], %[da] offset:14336\n\t"
    "v_mfma_f32_16x16x32_bf16 v[88:91], v[160:163], v[100:103], v[88:91]\n\t"
    "v_mfma_f32_16x16x32_bf16 v[92:95], v[164:167], v[100:103], v[92:95]\n\t"
    "s_barrier\n\t"
    "s_waitcnt lgkmcnt(4)\n\t"
    "v_mfma_f32_16x16x32_bf16 v[80:83], v[168:171], v[104:107], v[80:83]\n\t"
    "ds_read_b128 v[96:99], %[ea] offset:16384\n\t"
    "v_mfma_f32_16x16x32_bf16 v[84:87], v[172:175], v[104:107], v[84:87]\n\t"
    "ds_read_b128 v[100:103], %[ea] offset:18432\n\t"
    "v_mfma_f32_16x16x32_bf16 v[88:91], v[168:171], v[108:111], v[88:91]\n\t"
    "v_mfma_f32_16x16x32_bf16 v[92:95], v[172:175], v[108:111], v[92:95]\n\t"
    "s_waitcnt lgkmcnt(4)\n\t"
    "v_mfma_f32_16x16x32_bf16 v[80:83], v[176:179], v[112:115], v[80:83]\n\t"
    "ds_read_b128 v[104:107], %[da] offset:16384\n\t"
    "v_mfma_f32_16x16x32_bf16 v[84:87], v[180:183], v[112:115], v[84:87]\n\t"
    "ds_read_b128 v[108:111], %[da] offset:18432\n\t"
    "v_mfma_f32_16x16x32_bf16 v[88:91], v[176:179], v[116:119], v[88:91]\n\t"
    "v_mfma_f32_16x16x32_bf16 v[92:95], v[180:183], v[116:119], v[92:95]\n\t"
    "s_waitcnt lgkmcnt(4)\n\t"
    "v_mfma_f32_16x16x32_bf16 v[80:83], v[184:187], v[120:123], v[80:83]\n\t"
    "ds_read_b128 v[112:115], %[ea] offset:20480\n\t"
    "v_mfma_f32_16x16x32_bf16 v[84:87], v[188:191], v[120:123], v[84:87]\n\t"
    "ds_read_b128 v[116:119], %[ea] offset:22528\n\t"
    "v_mfma_f32_16x16x32_bf16 v[88:91], v[184:187], v[124:127], v[88:91]\n\t"
    "v_mfma_f32_16x16x32_bf16 v[92:95], v[188:191], v[124:127], v[92:95]\n\t"
    "s_waitcnt lgkmcnt(4)\n\t"
    "v_mfma_f32_16x16x32_bf16 v[80:83], v[192:195], v[96:99], v[80:83]\n\t"
    "ds_read_b128 v[120:123], %[da] offset:20480\n\t"
    "v_mfma_f32_16x16x32_bf16 v[84:87], v[196:199], v[96:99], v[84:87]\n\t"
    "ds_read_b128 v[124:127], %[da] offset:22528\n\t"
    "v_mfma_f32_16x16x32_bf16 v[88:91], v[192:195], v[100:103], v[88:91]\n\t"
    "v_mfma_f32_16x16x32_bf16 v[92:95], v[196:199], v[100:103], v[92:95]\n\t"
    "s_barrier\n\t"
    "s_waitcnt lgkmcnt(4)\n\t"
    "v_mfma_f32_16x16x32_bf16 v[80:83], v[200:203], v[104:107], v[80:83]\n\t"
    "ds_read_b128 v[96:99], %[ea] offset:24576\n\t"
    "v_mfma_f32_16x16x32_bf16 v[84:87], v[204:207], v[104:107], v[84:87]\n\t"
    "ds_read_b128 v[100:103], %[ea] offset:26624\n\t"
    "v_mfma_f32_16x16x32_bf16 v[88:91], v[200:203], v[108:111], v[88:91]\n\t"
    "v_mfma_f32_16x16x32_bf16 v[92:95], v[204:207], v[108:111], v[92:95]\n\t"
    "s_waitcnt lgkmcnt(4)\n\t"
    "v_mfma_f32_16x16x32_bf16 v[80:83], v[208:211], v[112:115], v[80:83]\n\t"
    "ds_read_b128 v[104:107], %[da] offset:24576\n\t"
    "v_mfma_f32_16x16x32_bf16 v[84:87], v[212:215], v[112:115], v[84:87]\n\t"
    "ds_read_b128 v[108:111], %[da] offset:26624\n\t"
    "v_mfma_f32_16x16x32_bf16 v[88:91], v[208:211], v[116:119], v[88:91]\n\t"
    "v_mfma_f32_16x16x32_bf16 v[92:95], v[212:215], v[116:119], v[92:95]\n\t"
    "s_waitcnt lgkmcnt(4)\n\t"
    "v_mfma_f32_16x16x32_bf16 v[80:83], v[216:219], v[120:123], v[80:83]\n\t"
    "ds_read_b128 v[112:115], %[ea] offset:28672\n\t"
    "v_mfma_f32_16x16x32_bf16 v[84:87], v[220:223], v[120:123], v[84:87]\n\t"
    "ds_read_b128 v[116:119], %[ea] offset:30720\n\t"
    "v_mfma_f32_16x16x32_bf16 v[88:91], v[216:219], v[124:127], v[88:91]\n\t"
    "v_mfma_f32_16x16x32_bf16 v[92:95], v[220:223], v[124:127], v[92:95]\n\t"
    "s_waitcnt lgkmcnt(4)\n\t"
    "v_mfma_f32_16x16x32_bf16 v[80:83], v[224:227], v[96:99], v[80:83]\n\t"
    "ds_read_b128 v[120:123], %[da] offset:28672\n\t"
    "v_mfma_f32_16x16x32_bf16 v[84:87], v[228:231], v[96:99], v[84:87]\n\t"
    "ds_read_b128 v[124:127], %[da] offset:30720\n\t"
    "v_mfma_f32_16x16x32_bf16 v[88:91], v[224:227], v[100:103], v[88:91]\n\t"
    "v_mfma_f32_16x16x32_bf16 v[92:95], v[228:231], v[100:103], v[92:95]\n\t"
    "s_waitcnt lgkmcnt(4)\n\t"
    "v_mfma_f32_16x16x32_bf16 v[80:83], v[232:235], v[104:107], v[80:83]\n\t"
    "v_mfma_f32_16x16x32_bf16 v[84:87], v[236:239], v[104:107], v[84:87]\n\t"
    "v_mfma_f32_16x16x32_bf16 v[88:91], v[232:235], v[108:111], v[88:91]\n\t"
    "v_xor_b32 %[ea], 0x10000, %[ea]\n\t"
    "v_mfma_f32_16x16x32_bf16 v[92:95], v[236:239], v[108:111], v[92:95]\n\t"
    "v_xor_b32 %[da], 0x10000, %[da]\n\t"
    "s_waitcnt lgkmcnt(2)\n\t"
    "v_mfma_f32_16x16x32_bf16 v[80:83], v[240:243], v[112:115], v[80:83]\n\t"
    "v_mfma_f32_16x16x32_bf16 v[84:87], v[244:247], v[112:115], v[84:87]\n\t"
    "v_mfma_f32_16x16x32_bf16 v[88:91], v[240:243], v[116:119], v[88:91]\n\t"
    "s_add_u32 %[b], %[b], 1\n\t"
    "v_mfma_f32_16x16x32_bf16 v[92:95], v[244:247], v[116:119], v[92:95]\n\t"
    "s_waitcnt lgkmcnt(0)\n\t"
    "v_mfma_f32_16x16x32_bf16 v[80:83], v[248:251], v[120:123], v[80:83]\n\t"
    "v_mfma_f32_16x16x32_bf16 v[84:87], v[252:255], v[120:123], v[84:87]\n\t"
    "v_mfma_f32_16x16x32_bf16 v[88:91], v[248:251], v[124:127], v[88:91]\n\t"
    "v_mfma_f32_16x16x32_bf16 v[92:95], v[252:255], v[124:127], v[92:95]\n\t"
    "s_nop 6\n\t"
    "v_cvt_pk_bf16_f32 v40, v80, v81\n\t"
    "v_cvt_pk_bf16_f32 v41, v82, v83\n\t"
    "v_cvt_pk_bf16_f32 v42, v84, v85\n\t"
    "v_cvt_pk_bf16_f32 v43, v86, v87\n\t"
    "global_store_dwordx4 %[co0], v[40:43], s[36:37] sc1\n\t"
    "v_cvt_pk_bf16_f32 v44, v88, v89\n\t"
    "v_cvt_pk_bf16_f32 v45, v90, v91\n\t"
    "v_cvt_pk_bf16_f32 v46, v92, v93\n\t"
    "v_cvt_pk_bf16_f32 v47, v94, v95\n\t"
    "global_store_dwordx4 %[co1], v[44:47], s[36:37] sc1\n\t"
    "2:\n\t"
    "ds_read_b32 v39, %[fa]\n\t"
    "ds_read_b128 v[48:51], %[rw] offset:0\n\t"
    "ds_read_b128 v[52:55], %[rw] offset:1024\n\t"
    "ds_read_b128 v[56:59], %[rw] offset:2048\n\t"
    "ds_read_b128 v[60:63], %[rw] offset:3072\n\t"
    "s_waitcnt lgkmcnt(2)\n\t"
    "v_readfirstlane_b32 %[pp], v39\n\t"
    "s_and_b32 %[st], %[pp], 0xff\n\t"
    "s_cmp_eq_u32 %[st], %[b]\n\t"
    "s_cbranch_scc1 3f\n\t"
    "s_waitcnt lgkmcnt(0)\n\t"
    "s_branch 2b\n\t"
    "3:\n\t"
    "v_cvt_pk_bf16_f32 v48, v48, v49\n\t"
    "v_cvt_pk_bf16_f32 v49, v50, v51\n\t"
    "v_cvt_pk_bf16_f32 v50, v52, v53\n\t"
    "v_cvt_pk_bf16_f32 v51, v54, v55\n\t"
    "global_store_dwordx4 %[co0], v[48:51], s[36:37] offset:2048 sc1\n\t"
    "s_waitcnt lgkmcnt(0)\n\t"
    "v_cvt_pk_bf16_f32 v56, v56, v57\n\t"
    "v_cvt_pk_bf16_f32 v57, v58, v59\n\t"
    "v_cvt_pk_bf16_f32 v58, v60, v61\n\t"
    "v_cvt_pk_bf16_f32 v59, v62, v63\n\t"
    "global_store_dwordx4 %[co1], v[56:59], s[36:37] offset:2048 sc1\n\t"
    "s_add_u32 s36, s36, %[ab]\n\t"
    "s_addc_u32 s37, s37, 0\n\t"
    "s_lshr_b32 %[pp], %[pp], 8\n\t"
    "s_cbranch_scc1 1b\n\t"
    "v_mov_b32 v72, %[cn]\n\t"
    "v_mov_b32 v73, 1\n\t"
    "s_add_u32 %[st], %[b], %[po]\n\t"
    "v_mov_b32 v74, %[st]\n\t"
    "v_mov_b32 v75, 0\n\t"
    "s_waitcnt vmcnt(0)\n\t"
    "s_mov_b64 exec, 1\n\t"
    "ds_add_rtn_u32 v73, v72, v73\n\t"
    "s_waitcnt lgkmcnt(0)\n\t"
    "v_readfirstlane_b32 %[st], v73\n\t"
    "s_and_b32 %[st], %[st], 3\n\t"
    "s_cmp_lg_u32 %[st], 3\n\t"
    "s_cbranch_scc1 61f\n\t"
    "global_store_dword v75, v74, %[pg] sc1\n\t"
    "61:\n\t"
    "s_mov_b64 exec, -1\n\t"
    "s_mov_b32 %[pp], 0\n\t"
    "s_cmp_lt_i32 %[b], %[ns]\n\t"
    "s_cbranch_scc1 1b\n\t"
    "s_branch 9f\n\t"
    "7:\n\t"
    "v_mov_b32 v72, %[cn]\n\t"
    "v_mov_b32 v73, 1\n\t"
    "s_add_u32 %[st], %[b], %[po]\n\t"
    "v_mov_b32 v74, %[st]\n\t"
    "v_mov_b32 v75, 0\n\t"
    "s_waitcnt vmcnt(0)\n\t"
    "s_mov_b64 exec, 1\n\t"
    "ds_add_rtn_u32 v73, v72, v73\n\t"
    "s_waitcnt lgkmcnt(0)\n\t"
    "v_readfirstlane_b32 %[st], v73\n\t"
    "s_and_b32 %[st], %[st], 3\n\t"
    "s_cmp_lg_u32 %[st], 3\n\t"
    "s_cbranch_scc1 61f\n\t"
    "global_store_dword v75, v74, %[pg] sc1\n\t"
    "61:\n\t"
    "s_mov_b64 exec, -1\n\t"
    "s_mov_b32 %[pp], 0\n\t"
    "s_branch 8b\n\t"
    "9:"
    : [ea] "+&v"(ea), [da] "+&v"(da), [b] "+&s"(b), [st] "=&s"(st), "+{s[36:37]}"(p0), [pp] "+&s"(pp)
    : [ns] "s"(ns), [ab] "s"(ab), [rw] "v"(rw), [fa] "v"(fa), "{v[128:159]}"(W[0]), "{v[160:191]}"(W[1]), "{v[192:223]}"(W[2]), "{v[224:255]}"(W[3]), [po] "s"(po), [cn] "s"(cn), [pg] "s"(pg), [co0] "v"(co0), [co1] "v"(co1)
    : "v39", "v40", "v41", "v42", "v43", "v44", "v45", "v46", "v47", "v48", "v49", "v50", "v51", "v52", "v53", "v54", "v55", "v56", "v57", "v58", "v59", "v60", "v61", "v62", "v63", "v72", "v73", "v74", "v75", "v80", "v81", "v82", "v83", "v84", "v85", "v86", "v87", "v88", "v89", "v90", "v91", "v92", "v93", "v94", "v95", "v96", "v97", "v98", "v99", "v100", "v101", "v102", "v103", "v104", "v105", "v106", "v107", "v108", "v109", "v110", "v111", "v112", "v113", "v114", "v115", "v116", "v117", "v118", "v119", "v120", "v121", "v122", "v123", "v124", "v125", "v126", "v127", "vcc", "scc", "m0", "memory");
}
__device__ __forceinline__ void fin_ld(const u32x16& pd, unsigned rw, unsigned fa, unsigned fv) {
  asm volatile("ds_write_b128 %[rw], v[80:83] offset:0\n\t"
    "ds_write_b128 %[rw], v[84:87] offset:1024\n\t"
    "ds_write_b128 %[rw], v[88:91] offset:2048\n\t"
    "ds_write_b128 %[rw], v[92:95] offset:3072\n\t"
    "ds_write_b32 %[fa], %[fv]"
    :
    : [rw] "v"(rw), "{v[80:95]}"(pd), [fa] "v"(fa), [fv] "v"(fv)
    : "memory");
}
__device__ __forceinline__ void fin_st0(const u32x16& pd, unsigned rw, unsigned fa, unsigned fv, const char* cb, unsigned co0, unsigned co1) {
  asm volatile("1:\n\t"
    "ds_read_b32 v39, %[fa]\n\t"
    "ds_read_b128 v[48:51], %[rw] offset:0\n\t"
    "ds_read_b128 v[52:55], %[rw] offset:1024\n\t"
    "ds_read_b128 v[56:59], %[rw] offset:2048\n\t"
    "ds_read_b128 v[60:63], %[rw] offset:3072\n\t"
    "s_waitcnt lgkmcnt(2)\n\t"
    "v_cmp_ne_u32 vcc, v39, %[fv]\n\t"
    "s_cbranch_vccz 2f\n\t"
    "s_waitcnt lgkmcnt(0)\n\t"
    "s_branch 1b\n\t"
    "2:\n\t"
    "v_add_f32 v48, v48, v80\n\t"
    "v_add_f32 v49, v49, v81\n\t"
    "v_add_f32 v50, v50, v82\n\t"
    "v_add_f32 v51, v51, v83\n\t"
    "v_add_f32 v52, v52, v84\n\t"
    "v_add_f32 v53, v53, v85\n\t"
    "v_add_f32 v54, v54, v86\n\t"
    "v_add_f32 v55, v55, v87\n\t"
    "v_cvt_pk_bf16_f32 v40, v48, v49\n\t"
    "v_cvt_pk_bf16_f32 v41, v50, v51\n\t"
    "v_cvt_pk_bf16_f32 v42, v52, v53\n\t"
    "v_cvt_pk_bf16_f32 v43, v54, v55\n\t"
    "global_store_dwordx4 %[co0], v[40:43], %[cb] sc1\n\t"
    "s_waitcnt lgkmcnt(0)\n\t"
    "v_add_f32 v56, v56, v88\n\t"
    "v_add_f32 v57, v57, v89\n\t"
    "v_add_f32 v58, v58, v90\n\t"
    "v_add_f32 v59, v59, v91\n\t"
    "v_add_f32 v60, v60, v92\n\t"
    "v_add_f32 v61, v61, v93\n\t"
    "v_add_f32 v62, v62, v94\n\t"
    "v_add_f32 v63, v63, v95\n\t"
    "v_cvt_pk_bf16_f32 v44, v56, v57\n\t"
    "v_cvt_pk_bf16_f32 v45, v58, v59\n\t"
    "v_cvt_pk_bf16_f32 v46, v60, v61\n\t"
    "v_cvt_pk_bf16_f32 v47, v62, v63\n\t"
    "global_store_dwordx4 %[co1], v[44:47], %[cb] sc1\n\t"
    "s_nop 1"
    :
    : [rw] "v"(rw), "{v[80:95]}"(pd), [fa] "v"(fa), [fv] "v"(fv), [cb] "s"(cb), [co0] "v"(co0), [co1] "v"(co1)
    : "v39", "v40", "v41", "v42", "v43", "v44", "v45", "v46", "v47", "v48", "v49", "v50", "v51", "v52", "v53", "v54", "v55", "v56", "v57", "v58", "v59", "v60", "v61", "v62", "v63", "vcc", "memory");
}
__device__ __forceinline__ void fin_st1(const u32x16& pd, unsigned rw, unsigned fa, unsigned fv, const char* cb, unsigned co0, unsigned co1) {
  asm volatile("1:\n\t"
    "ds_read_b32 v39, %[fa]\n\t"
    "ds_read_b128 v[48:51], %[rw] offset:0\n\t"
    "ds_read_b128 v[52:55], %[rw] offset:1024\n\t"
    "ds_read_b128 v[56:59], %[rw] offset:2048\n\t"
    "ds_read_b128 v[60:63], %[rw] offset:3072\n\t"
    "s_waitcnt lgkmcnt(2)\n\t"
    "v_cmp_ne_u32 vcc, v39, %[fv]\n\t"
    "s_cbranch_vccz 2f\n\t"
    "s_waitcnt lgkmcnt(0)\n\t"
    "s_branch 1b\n\t"
    "2:\n\t"
    "v_add_f32 v48, v48, v80\n\t"
    "v_add_f32 v49, v49, v81\n\t"
    "v_add_f32 v50, v50, v82\n\t"
    "v_add_f32 v51, v51, v83\n\t"
    "v_add_f32 v52, v52, v84\n\t"
    "v_add_f32 v53, v53, v85\n\t"
    "v_add_f32 v54, v54, v86\n\t"
    "v_add_f32 v55, v55, v87\n\t"
    "s_waitcnt lgkmcnt(0)\n\t"
    "v_add_f32 v56, v56, v88\n\t"
    "v_add_f32 v57, v57, v89\n\t"
    "v_add_f32 v58, v58, v90\n\t"
    "v_add_f32 v59, v59, v91\n\t"
    "v_add_f32 v60, v60, v92\n\t"
    "v_add_f32 v61, v61, v93\n\t"
    "v_add_f32 v62, v62, v94\n\t"
    "v_add_f32 v63, v63, v95\n\t"
    "v_mul_f32 v40, %[nl], v48\n\t"
    "v_mul_f32 v41, %[nl], v49\n\t"
    "v_mul_f32 v42, %[nl], v50\n\t"
    "v_mul_f32 v43, %[nl], v51\n\t"
    "v_mul_f32 v44, %[nl], v56\n\t"
    "v_mul_f32 v45, %[nl], v57\n\t"
    "v_mul_f32 v46, %[nl], v58\n\t"
    "v_mul_f32 v47, %[nl], v59\n\t"
    "v_exp_f32 v40, v40\n\t"
    "v_exp_f32 v41, v41\n\t"
    "v_exp_f32 v42, v42\n\t"
    "v_exp_f32 v43, v43\n\t"
    "v_exp_f32 v44, v44\n\t"
    "v_exp_f32 v45, v45\n\t"
    "v_exp_f32 v46, v46\n\t"
    "v_exp_f32 v47, v47\n\t"
    "v_add_f32 v40, 1.0, v40\n\t"
    "v_add_f32 v41, 1.0, v41\n\t"
    "v_add_f32 v42, 1.0, v42\n\t"
    "v_add_f32 v43, 1.0, v43\n\t"
    "v_add_f32 v44, 1.0, v44\n\t"
    "v_add_f32 v45, 1.0, v45\n\t"
    "v_add_f32 v46, 1.0, v46\n\t"
    "v_add_f32 v47, 1.0, v47\n\t"
    "v_rcp_f32 v40, v40\n\t"
    "v_rcp_f32 v41, v41\n\t"
    "v_rcp_f32 v42, v42\n\t"
    "v_rcp_f32 v43, v43\n\t"
    "v_rcp_f32 v44, v44\n\t"
    "v_rcp_f32 v45, v45\n\t"
    "v_rcp_f32 v46, v46\n\t"
    "v_rcp_f32 v47, v47\n\t"
    "v_mul_f32 v40, v48, v40\n\t"
    "v_mul_f32 v41, v49, v41\n\t"
    "v_mul_f32 v42, v50, v42\n\t"
    "v_mul_f32 v43, v51, v43\n\t"
    "v_mul_f32 v44, v56, v44\n\t"
    "v_mul_f32 v45, v57, v45\n\t"
    "v_mul_f32 v46, v58, v46\n\t"
    "v_mul_f32 v47, v59, v47\n\t"
    "v_mul_f32 v40, v40, v52\n\t"
    "v_mul_f32 v41, v41, v53\n\t"
    "v_mul_f32 v42, v42, v54\n\t"
    "v_mul_f32 v43, v43, v55\n\t"
    "v_mul_f32 v44, v44, v60\n\t"
    "v_mul_f32 v45, v45, v61\n\t"
    "v_mul_f32 v46, v46, v62\n\t"
    "v_mul_f32 v47, v47, v63\n\t"
    "v_cvt_pk_bf16_f32 v48, v40, v41\n\t"
    "v_cvt_pk_bf16_f32 v49, v42, v43\n\t"
    "v_cvt_pk_bf16_f32 v50, v44, v45\n\t"
    "v_cvt_pk_bf16_f32 v51, v46, v47\n\t"
    "s_nop 1\n\t"
    "v_permlane16_swap_b32 v48, v50\n\t"
    "v_permlane16_swap_b32 v49, v51\n\t"
    "global_store_dwordx4 %[co0], v[48:51], %[cb] sc1\n\t"
    "s_nop 1"
    :
    : [rw] "v"(rw), "{v[80:95]}"(pd), [fa] "v"(fa), [fv] "v"(fv), [cb] "s"(cb), [co0] "v"(co0), [nl] "s"(-LOG2E)
    : "v39", "v40", "v41", "v42", "v43", "v44", "v45", "v46", "v47", "v48", "v49", "v50", "v51", "v52", "v53", "v54", "v55", "v56", "v57", "v58", "v59", "v60", "v61", "v62", "v63", "vcc", "memory");
}
__device__ __forceinline__ void fin_st2(const u32x16& pd, unsigned rw, unsigned fa, unsigned fv, const char* cb, unsigned co0, unsigned co1) {
  asm volatile("s_nop 1\n\t"
    "v_cvt_pk_bf16_f32 v40, v80, v81\n\t"
    "v_cvt_pk_bf16_f32 v41, v82, v83\n\t"
    "v_cvt_pk_bf16_f32 v42, v84, v85\n\t"
    "v_cvt_pk_bf16_f32 v43, v86, v87\n\t"
    "global_store_dwordx4 %[co0], v[40:43], %[cb] sc1\n\t"
    "v_cvt_pk_bf16_f32 v44, v88, v89\n\t"
    "v_cvt_pk_bf16_f32 v45, v90, v91\n\t"
    "v_cvt_pk_bf16_f32 v46, v92, v93\n\t"
    "v_cvt_pk_bf16_f32 v47, v94, v95\n\t"
    "global_store_dwordx4 %[co1], v[44:47], %[cb] sc1\n\t"
    "1:\n\t"
    "ds_read_b32 v39, %[fa]\n\t"
    "ds_read_b128 v[48:51], %[rw] offset:0\n\t"
    "ds_read_b128 v[52:55], %[rw] offset:1024\n\t"
    "ds_read_b128 v[56:59], %[rw] offset:2048\n\t"
    "ds_read_b128 v[60:63], %[rw] offset:3072\n\t"
    "s_waitcnt lgkmcnt(2)\n\t"
    "v_cmp_ne_u32 vcc, v39, %[fv]\n\t"
    "s_cbranch_vccz 2f\n\t"
    "s_waitcnt lgkmcnt(0)\n\t"
    "s_branch 1b\n\t"
    "2:\n\t"
    "v_cvt_pk_bf16_f32 v48, v48, v49\n\t"
    "v_cvt_pk_bf16_f32 v49, v50, v51\n\t"
    "v_cvt_pk_bf16_f32 v50, v52, v53\n\t"
    "v_cvt_pk_bf16_f32 v51, v54, v55\n\t"
    "global_store_dwordx4 %[co0], v[48:51], %[cb] offset:2048 sc1\n\t"
    "s_waitcnt lgkmcnt(0)\n\t"
    "v_cvt_pk_bf16_f32 v56, v56, v57\n\t"
    "v_cvt_pk_bf16_f32 v57, v58, v59\n\t"
    "v_cvt_pk_bf16_f32 v58, v60, v61\n\t"
    "v_cvt_pk_bf16_f32 v59, v62, v63\n\t"
    "global_store_dwordx4 %[co1], v[56:59], %[cb] offset:2048 sc1\n\t"
    "s_nop 1"
    :
    : [rw] "v"(rw), "{v[80:95]}"(pd), [fa] "v"(fa), [fv] "v"(fv), [cb] "s"(cb), [co0] "v"(co0), [co1] "v"(co1)
    : "v39", "v40", "v41", "v42", "v43", "v44", "v45", "v46", "v47", "v48", "v49", "v50", "v51", "v52", "v53", "v54", "v55", "v56", "v57", "v58", "v59", "v60", "v61", "v62", "v63", "vcc", "memory");
}
template <int R> __device__ __forceinline__ void gw_load(u32x32& w, unsigned a0, unsigned a1);
template <> __device__ __forceinline__ void gw_load<0>(u32x32& w, unsigned a0, unsigned a1) {
  asm volatile("ds_read_b64_tr_b16 v[128:129], %1 offset:0\n\t"
    "ds_read_b64_tr_b16 v[130:131], %1 offset:512\n\t"
    "ds_read_b64_tr_b16 v[132:133], %2 offset:0\n\t"
    "ds_read_b64_tr_b16 v[134:135], %2 offset:512\n\t"
    "ds_read_b64_tr_b16 v[136:137], %1 offset:4096\n\t"
    "ds_read_b64_tr_b16 v[138:139], %1 offset:4608\n\t"
    "ds_read_b64_tr_b16 v[140:141], %2 offset:4096\n\t"
    "ds_read_b64_tr_b16 v[142:143], %2 offset:4608\n\t"
    "s_waitcnt lgkmcnt(0)\n\t"
    "ds_read_b64_tr_b16 v[144:145], %1 offset:16384\n\t"
    "ds_read_b64_tr_b16 v[146:147], %1 offset:16896\n\t"
    "ds_read_b64_tr_b16 v[148:149], %2 offset:16384\n\t"
    "ds_read_b64_tr_b16 v[150:151], %2 offset:16896\n\t"
    "ds_read_b64_tr_b16 v[152:153], %1 offset:20480\n\t"
    "ds_read_b64_tr_b16 v[154:155], %1 offset:20992\n\t"
    "ds_read_b64_tr_b16 v[156:157], %2 offset:20480\n\t"
    "ds_read_b64_tr_b16 v[158:159], %2 offset:20992\n\t"
    "s_waitcnt lgkmcnt(0)"
    : "=&{v[128:159]}"(w) : "v"(a0), "v"(a1) : "memory");
}
template <> __device__ __forceinline__ void gw_load<1>(u32x32& w, unsigned a0, unsigned a1) {
  asm volatile("ds_read_b64_tr_b16 v[160:161], %1 offset:0\n\t"
    "ds_read_b64_tr_b16 v[162:163], %1 offset:512\n\t"
    "ds_read_b64_tr_b16 v[164:165], %2 offset:0\n\t"
    "ds_read_b64_tr_b16 v[166:167], %2 offset:512\n\t"
    "ds_read_b64_tr_b16 v[168:169], %1 offset:4096\n\t"
    "ds_read_b64_tr_b16 v[170:171], %1 offset:4608\n\t"
    "ds_read_b64_tr_b16 v[172:173], %2 offset:4096\n\t"
    "ds_read_b64_tr_b16 v[174:175], %2 offset:4608\n\t"
    "s_waitcnt lgkmcnt(0)\n\t"
    "ds_read_b64_tr_b16 v[176:177], %1 offset:16384\n\t"
    "ds_read_b64_tr_b16 v[178:179], %1 offset:16896\n\t"
    "ds_read_b64_tr_b16 v[180:181], %2 offset:16384\n\t"
    "ds_read_b64_tr_b16 v[182:183], %2 offset:16896\n\t"
    "ds_read_b64_tr_b16 v[184:185], %1 offset:20480\n\t"
    "ds_read_b64_tr_b16 v[186:187], %1 offset:20992\n\t"
    "ds_read_b64_tr_b16 v[188:189], %2 offset:20480\n\t"
    "ds_read_b64_tr_b16 v[190:191], %2 offset:20992\n\t"
    "s_waitcnt lgkmcnt(0)"
    : "=&{v[160:191]}"(w) : "v"(a0), "v"(a1) : "memory");
}
template <> __device__ __forceinline__ void gw_load<2>(u32x32& w, unsigned a0, unsigned a1) {
  asm volatile("ds_read_b64_tr_b16 v[192:193], %1 offset:0\n\t"
    "ds_read_b64_tr_b16 v[194:195], %1 offset:512\n\t"
    "ds_read_b64_tr_b16 v[196:197], %2 offset:0\n\t"
    "ds_read_b64_tr_b16 v[198:199], %2 offset:512\n\t"
    "ds_read_b64_tr_b16 v[200:201], %1 offset:4096\n\t"
    "ds_read_b64_tr_b16 v[202:203], %1 offset:4608\n\t"
    "ds_read_b64_tr_b16 v[204:205], %2 offset:4096\n\t"
    "ds_read_b64_tr_b16 v[206:207], %2 offset:4608\n\t"
    "s_waitcnt lgkmcnt(0)\n\t"
    "ds_read_b64_tr_b16 v[208:209], %1 offset:16384\n\t"
    "ds_read_b64_tr_b16 v[210:211], %1 offset:16896\n\t"
    "ds_read_b64_tr_b16 v[212:213], %2 offset:16384\n\t"
    "ds_read_b64_tr_b16 v[214:215], %2 offset:16896\n\t"
    "ds_read_b64_tr_b16 v[216:217], %1 offset:20480\n\t"
    "ds_read_b64_tr_b16 v[218:219], %1 offset:20992\n\t"
    "ds_read_b64_tr_b16 v[220:221], %2 offset:20480\n\t"
    "ds_read_b64_tr_b16 v[222:223], %2 offset:20992\n\t"
    "s_waitcnt lgkmcnt(0)"
    : "=&{v[192:223]}"(w) : "v"(a0), "v"(a1) : "memory");
}
template <> __device__ __forceinline__ void gw_load<3>(u32x32& w, unsigned a0, unsigned a1) {
  asm volatile("ds_read_b64_tr_b16 v[224:225], %1 offset:0\n\t"
    "ds_read_b64_tr_b16 v[226:227], %1 offset:512\n\t"
    "ds_read_b64_tr_b16 v[228:229], %2 offset:0\n\t"
    "ds_read_b64_tr_b16 v[230:231], %2 offset:512\n\t"
    "ds_read_b64_tr_b16 v[232:233], %1 offset:4096\n\t"
    "ds_read_b64_tr_b16 v[234:235], %1 offset:4608\n\t"
    "ds_read_b64_tr_b16 v[236:237], %2 offset:4096\n\t"
    "ds_read_b64_tr_b16 v[238:239], %2 offset:4608\n\t"
    "s_waitcnt lgkmcnt(0)\n\t"
    "ds_read_b64_tr_b16 v[240:241], %1 offset:16384\n\t"
    "ds_read_b64_tr_b16 v[242:243], %1 offset:16896\n\t"
    "ds_read_b64_tr_b16 v[244:245], %2 offset:16384\n\t"
    "ds_read_b64_tr_b16 v[246:247], %2 offset:16896\n\t"
    "ds_read_b64_tr_b16 v[248:249], %1 offset:20480\n\t"
    "ds_read_b64_tr_b16 v[250:251], %1 offset:20992\n\t"
    "ds_read_b64_tr_b16 v[252:253], %2 offset:20480\n\t"
    "ds_read_b64_tr_b16 v[254:255], %2 offset:20992\n\t"
    "s_waitcnt lgkmcnt(0)"
    : "=&{v[224:255]}"(w) : "v"(a0), "v"(a1) : "memory");
}
template <int I, int E, class F> __device__ __forceinline__ void sfor(F&& f) { if constexpr (I < E) { f(std::integral_constant<int, I>{}); sfor<I + 1, E>(f); } }
template <int ROLE, int KQ, class F0, class F>
__device__ __forceinline__ void wload(char* smem, int wave_id_, const u16* W0, const u16* W1, int ldb, u32x32 (&bw)[4], int rot, F0&& hook0, F&& hook) {
  constexpr int ABUF = 65536;
  const int tid = opaque_tid(), lane = tid & 63, wave = WAVE_ID;
  const int kq = KQ == 2 ? wave >> 2 : 0, nq = KQ == 2 ? wave & 3 : wave;
  const int g = lane >> 4, r16 = lane & 15, q4 = r16 >> 2, p4 = lane & 3;
  const unsigned ldsb = __builtin_amdgcn_readfirstlane((unsigned)(size_t)(LAS char*)smem);
  {
    const int srow = 8 * wave + (lane >> 3), ch = (lane & 7) ^ bswz(srow);
    const unsigned voff = (unsigned)(srow * ldb + ch * 8) * 2;
    const char* w0 = uni((const char*)W0); const char* w1 = uni((const char*)(ROLE == R_F1 ? W1 : W0 + 64));
    const size_t kb = (size_t)64 * ldb * 2;
    auto wsrc = [&](int r, int i) {
      const int kbi = KQ == 2 ? (ROLE == R_PART2 ? 0 : (i >> 2) * 8) + ((2 * r + ((i >> 1) & 1)) ^ rot) : 2 * r + (i >> 2);
      return KQ == 2 ? ((i & 1) ? w1 : w0) + kbi * kb + (ROLE == R_PART2 ? (i >> 2) * 256 : 0) : w0 + kbi * kb + (i & 3) * 128;
    };
    auto wstage = [&](int r) {
      const unsigned ld = ldsb + (r & 1) * ABUF + wave * 1024;
#pragma unroll
      for (int i = 0; i < 8; ++i) {
        unsigned keep;
        asm volatile("s_mov_b32 %0, m0\n\ts_mov_b32 m0, %3\n\ts_nop 0\n\tglobal_load_lds_dwordx4 %1, %2" WNT_ "\n\ts_mov_b32 m0, %0"
                     : "=&s"(keep) : "v"(voff), "s"(wsrc(r, i)), "s"(ld + i * 8192) : "memory");
      }
    };
    const int brow = 8 * g + q4, bs = bswz(brow);
    unsigned la[2];
#pragma unroll
    for (int j = 0; j < 2; ++j) {
      int blk, c16, sub;
      if (ROLE == R_F1) { blk = j; c16 = nq * 2 + (p4 >> 1); sub = (p4 & 1) * 8; }
      else { blk = nq >> 1; c16 = (nq & 1) * 4 + p4; sub = j * 8; }
      int set = kq;
      if (ROLE == R_PART2 && TLP) { set = nq >> 1; blk = nq & 1; c16 = kq * 4 + p4; }
      la[j] = (unsigned)((KQ == 2 ? set * 4 + blk : blk) * 8192 + brow * 128 + ((c16 ^ bs) << 4) + sub);
    }
    wstage(0); wstage(1);
    u32x4 raw[2][8];
#pragma unroll
    for (int r = 0; r < 2; ++r)
#pragma unroll
      for (int i = 0; i < 8; ++i) asm volatile("global_load_dwordx4 %0, %1, %2" WNT_ : "=&v"(raw[r][i]) : "v"(voff), "s"(wsrc(r + 2, i)) : "memory");
    sfor<0, 4>([&](auto r_c) __attribute__((always_inline)) {
      constexpr int r = decltype(r_c)::value;
      if (r == 0) WAIT_V(24); else if (r == 1) WAIT_V(16);
      if (r == 3) BAR(); else if (r < 2) { __builtin_amdgcn_s_barrier(); asm volatile("" ::: "memory"); }
      gw_load<r>(bw[r], ldsb + (r & 1) * ABUF + la[0], ldsb + (r & 1) * ABUF + la[1]);
      if (r == 1 || r == 2) {
        u32x4 (&q)[8] = raw[(r + 1) & 1];
        if (r == 1) hook0();
        if (r == 1) asm volatile("s_waitcnt vmcnt(8)" : "+v"(q[0]), "+v"(q[1]), "+v"(q[2]), "+v"(q[3]), "+v"(q[4]), "+v"(q[5]), "+v"(q[6]), "+v"(q[7]) :: "memory");
        else asm volatile("s_waitcnt vmcnt(0)" : "+v"(q[0]), "+v"(q[1]), "+v"(q[2]), "+v"(q[3]), "+v"(q[4]), "+v"(q[5]), "+v"(q[6]), "+v"(q[7]) :: "memory");
#pragma unroll
        for (int i = 0; i < 8; ++i) *(u32x4*)(smem + ((r + 1) & 1) * ABUF + wave * 1024 + i * 8192 + lane * 16) = q[i];
        if (r == 1) BAR();
      }
      if (r == 3) { hook(); BAR(); }
    });
  }
}
#define DSR2(d0, d1, a0_, a1_, off) asm volatile("ds_read_b128 %0, %2 offset:%4\n\tds_read_b128 %1, %3 offset:%4" : "=&v"(d0), "=&v"(d1) : "v"(a0_), "v"(a1_), "i"(off))
template <int N> __device__ __forceinline__ void waitl(u32x4& a, u32x4& b) { asm volatile("s_waitcnt lgkmcnt(%2)" : "+v"(a), "+v"(b) : "i"(N)); }
__device__ __forceinline__ void glds1(const char* sbase, unsigned voff, unsigned lds) {
  unsigned keep;
  asm volatile("s_mov_b32 %0, m0\n\ts_mov_b32 m0, %3\n\ts_nop 0\n\tglobal_load_lds_dwordx4 %1, %2 sc1\n\ts_mov_b32 m0, %0"
               : "=&s"(keep) : "v"(voff), "s"(sbase), "s"(lds) : "memory");
}
#define XS(i) do { } while (0)
#define XFL(b_) do { } while (0)
#define XA_ 0
#define XF_ 0
#define XE_ 0
#define XP_ 0
#define PUBK 9
#define LATE 0
#define NOWP 2
#define NEARM 15
#define PNEAR 2
#define LATEQ 0
#define DEFP 1
#define ROTM 0
#define ROTK 7
#define BPACE 40
#define W2 1
#define PDEEP 4
template <int ROLE>
__device__ __forceinline__ void gemm7(char* smem, int wave_id_, const u16* W0, const u16* W1, int ldb, const char* A_, size_t a_blk, Dep dep,
                                      char* C_, size_t c_blk, unsigned* myprog, unsigned* tmo, int slot, unsigned long long* mts,
                                      bool gl, int nimm = 0, int nseg = NSEG, int wm0 = 0, int pofs = 0, unsigned long long tag = 0, unsigned* par = nullptr) {
  volatile LAS unsigned* wsl = (volatile LAS unsigned*)(smem + L_SLOT + 512);
  if (opaque_tid() < 144) *(volatile LAS unsigned*)(smem + L_SLOT + opaque_tid() * 4) = 0u;
  const int wave = WAVE_ID;
  const int kq = wave >> 2, nq = wave & 3;
  const unsigned ldsb = __builtin_amdgcn_readfirstlane((unsigned)(size_t)(LAS char*)smem);
  const int rot = (ROTM >> ROLE) & 1 ? slot & ROTK : 0;
  const char* A = uni(A_) + nq * 1024;
  constexpr bool NEAR = (NEARM >> ROLE) & 1;
  constexpr int AH = NEAR ? 2 : 3, PD = NEAR ? PNEAR : PDEEP;
  constexpr bool P2C = ROLE == R_PART2;
  int pw = 0;
  int wm = wm0, nh = 0;
  auto lpoll = [&](int b, bool hs = false) {
    unsigned w;
    if (wave == 4) {
      for (int spins = 0;; ++spins) {
        unsigned v;
        if (hs) { WAIT_V(0); v = *(volatile LAS unsigned*)(smem + L_SLOT + 768 + lane_id() * 4); }
        else asm volatile("s_nop 4\n\tglobal_load_dword %0, %1, %2 sc1\n\ts_waitcnt vmcnt(0)" : "=&v"(v) : "v"(dep.voff), "s"(dep.base) : "memory");
        w = dep_eval(v, dep);
        if ((int)w > b) break;
        if (!hs) __builtin_amdgcn_s_sleep(2);
        hs = false;
        if (give_up(spins, tmo, 9)) { w = NSEG; break; }
      }
      w = w < 255u ? w : 255u;
      if (lane_id() == 0) wsl[0] = ((unsigned)(b + 1) << 8) | w;
    } else {
      for (;;) {
        w = __builtin_amdgcn_readfirstlane(wsl[0]);
        if ((w >> 8) == (unsigned)(b + 1)) break;
        __builtin_amdgcn_s_sleep(1);
      }
      w &= 255u;
    }
    return (int)w;
  };
  auto pubw = [&](int n) {
    WAIT_V(0);
    if (lane_id() == 0) {
      const unsigned o = __hip_atomic_fetch_add((LAS unsigned*)(smem + L_SLOT + 516), 1u, __ATOMIC_RELAXED, __HIP_MEMORY_SCOPE_WORKGROUP);
      if ((o & 3u) == 3u) {
        st4_sc1(myprog, (unsigned)(n + pofs));
      }
    }
    asm volatile("s_waitcnt lgkmcnt(0)" ::: "memory");
  };
  auto hsrc = [&](int hh) { return A + (size_t)(hh >> 1) * a_blk + ((hh ^ (rot >> 2)) & 1) * 16384; };
  auto hlds = [&](int hh) { return ldsb + (unsigned)(((hh >> 1) & 1) * 65536 + (hh & 1) * 16384 + nq * 1024); };
#define POFF(i_) (((i_) & 1) * 32768 + ((i_) >> 1) * 4096)
#define PIECE(s_, l_, i_) asm volatile("s_add_u32 m0, %2, %3\n\ts_nop 0\n\tglobal_load_lds_dwordx4 %0, %1 sc1" :: "v"(av[i_]), "s"(s_), "s"(l_), "i"(POFF(i_)) : "memory", "scc", "m0")
  u32x32 BW[4];
  KTS(1);
  wload<ROLE, 2>(smem, wave_id_, W0, W1, ldb, BW, rot, [&]() {
    if (ROLE == R_QKV && nimm > 0 && wave == 7) ini_request((const char*)tmo - OFF_TMO, ldsb + L_GB);
  }, [&]() {
    if (ROLE == R_QKV && nimm > 0) {
      *par = ini_resolve((const char*)tmo - OFF_TMO, smem + L_GB, tag);
      dep.base += (*par >> 2) << 13; myprog += (*par >> 2) << 11;
      if (kq) {
        wm = lpoll(0);
        const unsigned l16 = lane_id() * 16;
#pragma unroll
        for (int i = 0; i < 16; ++i) glds1(hsrc(i >> 3) + (i & 1) * 32768 + ((((i & 7) >> 1) ^ rot) & 3) * 4096, l16, hlds(i >> 3) + POFF(i & 7));
      }
      nh = 2;
    }
  });
  KTS(2);
  if (ROLE == R_QKV && nimm > 0 && !kq) wm = (int)(__builtin_amdgcn_readfirstlane(wsl[0]) & 255u);
  if (ROLE == R_QKV && opaque_tid() == 0) STS((char*)tmo - OFF_TMO, 128 + slot, 0);
  auto body = [&](auto ldr_c) __attribute__((always_inline)) {
    constexpr bool LDR = decltype(ldr_c)::value;
    if (XP_ == 0 ? !LDR : (XP_ == 1 ? LDR : false)) __builtin_amdgcn_s_setprio(1);
    unsigned av[8];
    const int lane = lane_id();
#pragma unroll
    for (int i = 0; i < 8; ++i) av[i] = lane * 16 + (P2C ? 0 : (i & 1) * 32768) + (((i >> 1) ^ rot) & 3) * 4096;
    auto issue_all = [&](int hh) {
      if (LDR) {
        const char* s_ = hsrc(hh); const unsigned l_ = hlds(hh);
#pragma unroll
        for (int i = 0; i < 8; ++i) if (!(P2C && (i & 1))) PIECE(s_, l_, i);
      }
    };
    int b = 0;
    bool hs = false;
    while (b < nseg) {
      const int ln = lane_id(), g = ln >> 4, r16 = ln & 15;
      const unsigned e0 = ldsb + (unsigned)((P2C ? 0 : kq * 32768) + (b & 1) * 65536 + (gl ? (g >> 1) * 512 + r16 * 32 + (g & 1) * 16 : r16 * 128 + ((g ^ (r16 & 7)) << 4)));
      const unsigned d0 = e0 ^ (gl ? 1024u : 64u);
      const unsigned rw = ldsb + L_RED + nq * 4096 + ln * 16;
      const unsigned co0 = ROLE == R_F1 ? (unsigned)((g & 1) * 2048 + nq * 512 + r16 * 32 + (g >> 1) * 16)
                         : ((ROLE == R_QKV ? TLQ : TLP) ? (unsigned)(nq * (P2C ? 4096 : 2048) + r16 * 64 + g * 16)
                         : (ROLE == R_QKV ? (unsigned)(r16 * 256 + (((4 * nq + g) ^ r16) << 4)) : (unsigned)(r16 * PROW + (32 * nq + 8 * g) * 2)));
      const unsigned co1 = co0 + ((ROLE == R_QKV ? TLQ : TLP) ? 1024u : (ROLE == R_QKV ? 4096u : 16u * PROW));
      u32x16 pv_;
      bool smp = false, lt0 = false;
      if ((wm < nseg ? wm : nseg) >= b + PD && b >= nimm && nh <= 2 * b + AH) {
        __builtin_amdgcn_s_barrier(); asm volatile("" ::: "memory");
        for (; nh < 2 * b + AH; ++nh) issue_all(nh);
        int lf = (wm < nseg ? wm : nseg) - (AH - 1), cp = nseg - (AH - 1);
        if (!LDR) KTS(128 + b);
        const unsigned sa = ldsb + L_SLOT + ln * 4, ad = dep.add - (AH - 1);
        if constexpr (LDR) (NEAR ? (P2C ? near_ld2 : near_ld0) : (P2C ? fast_ld2 : fast_ld0))(BW, pv_, e0, d0, b, lf, cp, sa, dep.mul, ad, rw, (unsigned)a_blk, hsrc(2 * b + AH), hsrc(2 * b + AH + 1), hlds(2 * b + AH), hlds(2 * b + AH + 1), av);
        else {
          const char* c0_ = uni((const char*)tmo - OFF_TMO + OFF_DUMP); const char* c1_ = uni(C_ + (size_t)b * c_blk);
          const int sf = b + pofs - (pw > 0 ? 1 : 0);
          const char* pg = uni((const char*)myprog);
          (NEAR ? (ROLE == R_F1 ? near_st1 : (P2C ? near_st2 : near_st0)) : (ROLE == R_F1 ? fast_st1 : (P2C ? fast_st2 : fast_st0)))(BW, pv_, e0, d0, b, lf, cp, sa, dep.mul, ad, rw, (unsigned)c_blk, c0_, c1_, co0, co1, pofs, sf, (unsigned)wave,
                                                                   ldsb + L_SLOT, dep.voff, dep.base, pg);
        }
        if (!LDR) KTS(64 + b);
        wm = lf + (AH - 1) > wm ? lf + (AH - 1) : wm;
        nh = 2 * b + AH;
        --b;
        __builtin_amdgcn_s_barrier(); asm volatile("" ::: "memory");
      } else if (NEAR && b >= BPACE) {
        if constexpr (LDR) {
          int cap = wave == 4 ? SPIN_CAP : 8 * SPIN_CAP;
          if (hs) WAIT_V(0);
          (P2C ? paced_ld2 : paced_ld0)(BW, e0, d0, b, nseg, (unsigned)a_blk, rw, ldsb + L_SLOT + 528 + nq * 4, hsrc(nh), hsrc(nh + 1), hlds(nh), cap, wm, (unsigned)(nh - 2 * b),
                                        dep.mul, dep.add, dep.voff, dep.base, wave == 4 ? ldsb + L_SLOT : 0u, ldsb + L_SLOT + ln * 4, av);
          if (cap == 0 && ln == 0) st4_sc1(tmo, 15u);
        } else (ROLE == R_F1 ? paced_st1 : (P2C ? paced_st2 : paced_st0))(BW, e0, d0, b, nseg, (unsigned)c_blk, rw, ldsb + L_SLOT + 528 + nq * 4, uni(C_ + (size_t)b * c_blk),
                                                                          __builtin_amdgcn_readfirstlane(pw > 0 ? 1u : 0u), pofs, ldsb + L_SLOT + 516, uni((const char*)myprog), co0, co1);
        break;
      } else {
        const bool pl = wm <= b;
        XS(0);
        if (pl && LDR) wm = lpoll(b, hs);
        XS(1);
        const bool cold = nh < 2 * b + 2;
        { const int upto = nh == 2 * b + 3 ? 2 * b + 4 : 2 * b + 2; for (; nh < upto; ++nh) issue_all(nh); }
        smp = nh == 2 * b + 2 && b + 1 < nseg && wm0 < nseg;
        if (LDR && wave == 4 && smp)
          asm volatile("s_mov_b32 m0, %0\n\ts_nop 3\n\tglobal_load_lds_dword %1, %2 sc1" :: "s"(ldsb + L_SLOT + (b & 1) * 256), "v"(dep.voff), "s"(dep.base) : "memory", "m0");
        XS(2);
        if (LDR && LEAN_QB) { if (nh >= 2 * b + 4) { if (P2C) WAIT_V(14); else WAIT_V(28); } else { if (P2C) WAIT_V(6); else WAIT_V(12); } }
        else if (LDR) { if (nh >= 2 * b + 4) { if (P2C) WAIT_V(12); else WAIT_V(24); } else { if (P2C) WAIT_V(4); else WAIT_V(8); } }
        BAR();
        XS(3);
        if (pl && !LDR) wm = (int)(__builtin_amdgcn_readfirstlane(wsl[0]) & 255u);
        const bool dm = nh == 2 * b + 2 && wm > b + 1 && b + 1 < nseg;
        if (ROLE == R_QKV && b < 2 && opaque_tid() == 0) STS((char*)tmo - OFF_TMO, 128 + slot, 1 + b);
        lt0 = smp && !dm;
        if constexpr (LDR) {
          if (dm) (P2C ? lean_dm1 : lean_dm0)(BW, pv_, e0, d0, 0u, hlds(nh), hsrc(nh), hsrc(nh + 1), av);
          else (P2C ? lean_ld1 : lean_ld0)(BW, pv_, e0, d0, (unsigned)(nh - 2 * b - 2), hlds(nh), hsrc(nh), hsrc(nh + 1), av, ldsb + L_SLOT + (b & 1) * 256 + ln * 4, dep.mul, dep.add, LATEQ && lt0 ? b + 1 : 0x7FFFFFFF,
                                           __builtin_amdgcn_readfirstlane(lt0 && wave == 4 ? ldsb + L_SLOT + 768 : 0u), dep.voff, dep.base);
        }
        else lean_st0(BW, pv_, e0, d0, __builtin_amdgcn_readfirstlane(pw > 0 ? (cold ? 4u : 1u) : 0u), (unsigned)(pw + pofs), ldsb + L_SLOT + 516, ldsb + L_SLOT + 256,
                     dep.voff, dep.base, uni((const char*)myprog));
        if (dm) nh += 2;
        XS(5);
      }
      if constexpr (LDR) fin_ld(pv_, rw, ldsb + L_SLOT + 528 + nq * 4, (unsigned)(b + 1));
      else (ROLE == R_F1 ? fin_st1 : (P2C ? fin_st2 : fin_st0))(pv_, rw, ldsb + L_SLOT + 528 + nq * 4, (unsigned)(b + 1), uni(C_ + (size_t)b * c_blk), co0, co1);
      XS(6);
      if (smp) {
        const int w = (int)dep_eval(*(volatile LAS unsigned*)(smem + L_SLOT + (b & 1) * 256 + lane_id() * 4), dep);
        if (LATEQ && lt0 && w > b + 1) nh += 2;
        wm = w > wm ? w : wm;
      }
      bool now = (NOWP == 2 ? nh < 2 * b + 4 && wm < b + 1 + PD : wm <= b + 1) || b + 1 >= nseg || b < nimm;
      if (!LDR && now) {
        if (DEFP && b >= nimm && b + 1 < nseg && (wm > b + 1 || (__builtin_amdgcn_readfirstlane(wsl[0]) >> 8) == (unsigned)(b + 2))) now = false;
        else pubw(b + 1);
      }
      XS(7);
      XFL(b);
      pw = now ? 0 : b + 1;
      hs = W2 && lt0;
      ++b;
    }
    __builtin_amdgcn_s_setprio(0);
  };
  if (kq) body(std::true_type{}); else body(std::false_type{});
#undef PIECE
#undef POFF
  WAIT_V(0);
  BAR();
  KTS(3);
}
__device__ __forceinline__ void fin_role(char* smem, int wave_id_, int fidx, const float* mod, const char* P2, const u16* x1, u16* out, unsigned* cnt, unsigned* tmo,
                                         unsigned long long* mts) {
  const int tid = opaque_tid(), lane = tid & 63, wave = WAVE_ID;
  row_table(smem + L_GB, mod, cnt, tmo, tid);
  volatile LAS unsigned* wsl = (volatile LAS unsigned*)(smem + L_SLOT + 512);
  int wf = 0, par = 0;
  for (int nf = fidx >> 3; nf < NSEG; nf += 2) {
    if (wf <= nf) {
      if (wave == 0) {
        unsigned w;
        for (int spins = 0;; ++spins) {
          const unsigned v = rowmin16(ld4_sc1(cnt + (PF2 + ((lane & 31) < 28 ? (lane & 31) : 0)) * 32));
          w = min(rdl(v, 0), rdl(v, 16));
          if ((int)w > nf) break;
          __builtin_amdgcn_s_sleep(2);
          if (give_up(spins, tmo, 9)) { w = NSEG; break; }
        }
        if (lane == 0) wsl[par] = w;
      }
      BAR();
      wf = (int)__builtin_amdgcn_readfirstlane(wsl[par]);
      par ^= 1;
    }
    int ln = lane; asm volatile("" : "+v"(ln));
    if (wave == 0) WTS(256 + 8 + fidx, nf / 2, 0);
    fin_half(P2, x1, smem + L_GB + 12288, out, nf * 32 + 4 * (fidx & 7) + (wave >> 1), wave & 1, ln);
    if (wave == 0) WTS(256 + 8 + fidx, nf / 2, 2);
  }
}
constexpr int Q_MEM = 0;
constexpr int Q_QK  = 16384;
constexpr int Q_TM  = 22528;
constexpr int Q_PQ  = 24576;
constexpr int Q_K   = 32768;
constexpr int Q_RING = 32768;
constexpr int NCHAIN = 96;
#define NQQ 6
#define NQK 4
#define E_AG 8
#define RDYI -1
#define ROTW 2
#define DQU 6
#define DQM 18
#define DQD 1
#define ORSEL (xg >> 1)
#define D_Q 85
#define D_O 5
#define D_A 0
#define WOF 1
#define E_S -1
#define RAE 0
#define SPUB 60
#define RAW 5
#define E_MIN 3
#define E_W 3
#define DOU 0
#define DOM 12
#define CL0 1
#define TSW 0
#define E_B 8
#define E_O 1900
#define KDEL 5
#define VPOS 0

typedef __bf16 bf16x16 __attribute__((ext_vector_type(16)));
typedef unsigned u32x8 __attribute__((ext_vector_type(8)));
__device__ __forceinline__ f32x4 SMF(u32x4 a, u32x8 b, f32x4 c, int idx) {
  return __builtin_amdgcn_smfmac_f32_16x16x64_bf16(__builtin_bit_cast(bf16x8, a), __builtin_bit_cast(bf16x16, b), c, idx, 0, 0);
}
__device__ __forceinline__ float fold32(float a, float b) {
  const auto p = __builtin_amdgcn_permlane32_swap(__float_as_uint(a), __float_as_uint(b), false, false);
  return __uint_as_float(p[0]) + __uint_as_float(p[1]);
}
__device__ __forceinline__ f32x2 rowsum(f32x4 x) { return f32x2{fold32(x[0], x[2]), fold32(x[1], x[3])}; }
__device__ __forceinline__ unsigned nosent1(unsigned v) { return v < 0xFFFFFFFEu ? v : 0xFFFFFFFEu; }
__device__ __forceinline__ void st4s(const char* sb, unsigned vo, unsigned v) { asm volatile("global_store_dword %0, %1, %2 sc1\n\ts_nop 0" :: "v"(vo), "v"(v), "s"(sb) : "memory"); }
__device__ __forceinline__ void st8s(const char* sb, unsigned vo, u32x2 v) { asm volatile("global_store_dwordx2 %0, %1, %2 sc1\n\ts_nop 1" :: "v"(vo), "v"(v), "s"(sb) : "memory"); }
__device__ __forceinline__ void gldsw(const char* sbase, unsigned voff, unsigned lds) {
  unsigned keep;
  asm volatile("s_mov_b32 %0, m0\n\ts_mov_b32 m0, %3\n\ts_nop 0\n\tglobal_load_lds_dwordx4 %1, %2" CNT_ "\n\ts_mov_b32 m0, %0"
               : "=&s"(keep) : "v"(voff), "s"(sbase), "s"(lds) : "memory");
}
__device__ __forceinline__ void wait_clk(unsigned t0, unsigned d) {
  while ((unsigned)__builtin_amdgcn_s_memtime() - t0 < d) __builtin_amdgcn_s_sleep(1);
}
__device__ __forceinline__ unsigned bell_time(const char* a, unsigned vo, unsigned tag, unsigned cap, unsigned v = 0) {
  for (unsigned spins = 0; !__all((v >> 24) >= tag); ++spins) {
    asm volatile("s_nop 4\n\tglobal_load_dword %0, %1, %2 sc1\n\ts_waitcnt vmcnt(0)" : "=&v"(v) : "v"(vo), "s"(a) : "memory");
    if (__all((v >> 24) >= tag) || spins >= cap) break;
  }
  const unsigned now = (unsigned)__builtin_amdgcn_s_memrealtime() + 250u;
  const unsigned age = rowmin16((now - v) & 0xFFFFFFu);
  unsigned r = __builtin_amdgcn_readfirstlane(now - min(min(rdl(age, 0), rdl(age, 16)), 500u));
  asm volatile("" : "+s"(r));
  return r;
}
__device__ __forceinline__ void wait_rt(unsigned t0, int d) {
  while ((int)((unsigned)__builtin_amdgcn_s_memrealtime() - t0) < d) { }
}
#define WBAR() do { __builtin_amdgcn_s_barrier(); asm volatile("" ::: "memory"); } while (0)
#define SB() __builtin_amdgcn_sched_barrier(0)
#define TN(k, v) do { } while (0)
template <bool HALF>
__device__ __forceinline__ void stage_proj(const char* src, unsigned lds, int tid, int wave) {
  const int row = tid >> 4, f = 2 * ((row & 1) | (((row >> 2) & 3) << 1));
#pragma unroll
  for (int j = 0; j < 4; ++j) {
    const int pc = (tid & 15) ^ f;
    gldsw(src + (size_t)j * 32 * HD * 2, (unsigned)(HALF ? (row + (pc >> 3) * 128) * HD * 2 + ((pc & 7) << 4) : row * HD * 2 + (pc << 4)), lds + (wave * 64 + 512 * j) * 16);
  }
}
__device__ __forceinline__ unsigned proj_base(int T, int lane) {
  const int g = lane >> 4, q4 = (lane & 15) >> 2, p4 = lane & 3, fs = 2 * ((q4 & 1) | ((g & 1) << 1) | ((q4 >> 1) << 2));
  return (unsigned)((16 * (g >> 1) + 4 * (g & 1) + (q4 & 1) + 8 * (q4 >> 1)) * 256 + (((2 * T) ^ fs) << 4) + p4 * 8);
}
__device__ __forceinline__ f32x4 zero4() { float z = 0.0f; asm volatile("" : "+v"(z)); return f32x4{z, z, z, z}; }
__device__ __forceinline__ unsigned lds_word(const char* p) { return __builtin_amdgcn_readfirstlane(*(volatile LAS unsigned*)p); }

#define MFMA16(a, b, c) __builtin_amdgcn_mfma_f32_16x16x16bf16_1k((a), (b), (c), 0, 0, 0)
#define UF(x) __uint_as_float(x)
__device__ __forceinline__ unsigned umax3(unsigned a, unsigned b, unsigned c) { return max(max(a, b), c); }
__device__ __forceinline__ f32x2 logit2(float a, float b) {
  return f32x2{a, b};
}
constexpr float LSC = LOG2E * SCALE;

#define FCS() do { } while (0)
struct QKW { u32x32 w0, w1, w2, w3, w4, w5; };
template <int W_> __device__ __forceinline__ void qk_step(const QKW& W, const char* ag, const char* vs, const char* v2, const char* od, unsigned pb, unsigned sn, unsigned& cap, unsigned& sp,
    unsigned long long& tc, unsigned long long* ts, unsigned& d0, unsigned& d1);
template <> __device__ __forceinline__ void qk_step<0>(const QKW& W, const char* ag, const char* vs, const char* v2, const char* od, unsigned pb, unsigned sn, unsigned& cap, unsigned& sp,
    unsigned long long& tc, unsigned long long* ts, unsigned& d0, unsigned& d1) {
  unsigned st, bg;
  asm volatile("v_mbcnt_lo_u32_b32 v60, -1, 0\n\t"
    "v_mbcnt_hi_u32_b32 v60, -1, v60\n\t"
    "v_lshlrev_b32 v5, 4, v60\n\t"
    "s_mov_b32 %[sp], 0\n\t"
    "s_setprio 2\n\t"
    "global_load_dwordx4 v[24:27], v5, %[ag] sc1\n\t"
    "global_load_dwordx4 v[28:31], v5, %[ag] offset:1024 sc1\n\t"
    "s_memtime %[tc]\n\t"
    "v_lshrrev_b32 v32, 4, v60\n\t"
    "v_and_b32 v33, 15, v60\n\t"
    "v_lshrrev_b32 v34, 2, v33\n\t"
    "v_and_b32 v35, 3, v60\n\t"
    "v_and_b32 v61, 1, v60\n\t"
    "v_lshlrev_b32 v61, 3, v61\n\t"
    "v_lshl_or_b32 v7, v34, 4, v61\n\t"
    "v_lshl_or_b32 v7, v32, 7, v7\n\t"
    "v_xor_b32 v10, v32, v33\n\t"
    "v_lshlrev_b32 v10, 4, v10\n\t"
    "v_lshl_or_b32 v10, v33, 8, v10\n\t"
    "v_or_b32 v10, %[pb], v10\n\t"
    "v_xor_b32 v11, 0x40, v10\n\t"
    "v_xor_b32 v12, 0x80, v10\n\t"
    "v_xor_b32 v13, 0xc0, v10\n\t"
    "v_lshl_or_b32 v34, v32, 2, v34\n\t"
    "v_lshrrev_b32 v35, 1, v35\n\t"
    "v_or_b32 v35, 0, v35\n\t"
    "v_xor_b32 v35, v35, v34\n\t"
    "v_lshl_or_b32 v14, v35, 4, v61\n\t"
    "v_lshl_or_b32 v14, v34, 8, v14\n\t"
    "v_or_b32 v14, %[pb], v14\n\t"
    "v_and_b32 v61, 3, v60\n\t"
    "v_lshlrev_b32 v61, 6, v61\n\t"
    "v_lshl_or_b32 v4, v32, 4, v61\n\t"
    "v_and_b32 v61, 4, v60\n\t"
    "v_lshl_or_b32 v4, v61, 10, v4\n\t"
    "v_and_b32 v61, 8, v60\n\t"
    "v_lshl_or_b32 v4, v61, 14, v4\n\t"
    "v_or_b32 v4, 0, v4\n\t"
    "v_and_b32 v61, 2, v60\n\t"
    "v_mul_u32_u24 v61, 0x5555, v61\n\t"
    "v_or_b32 v6, %[c44], v61\n\t"
    "v_lshlrev_b32 v61, 2, v32\n\t"
    "v_lshl_or_b32 v15, v33, 4, v61\n\t"
    "3:\n\t"
    "v_mov_b32 v16, 0\n\t"
    "v_mov_b32 v17, 0\n\t"
    "v_mov_b32 v18, 0\n\t"
    "v_mov_b32 v19, 0\n\t"
    "v_mov_b32 v20, 0\n\t"
    "v_mov_b32 v21, 0\n\t"
    "v_mov_b32 v22, 0\n\t"
    "v_mov_b32 v23, 0\n\t"
    "s_waitcnt vmcnt(0) lgkmcnt(0)\n\t"
    "ds_write_b128 v5, v[24:27] offset:0\n\t"
    "ds_write_b128 v5, v[28:31] offset:1024\n\t"
    "v_smfmac_f32_16x16x64_bf16 v[16:19], v[24:27], v[64:71], v6\n\t"
    "v_max3_u32 v60, v24, v25, v26\n\t"
    "v_max3_u32 v61, v27, v28, v29\n\t"
    "v_smfmac_f32_16x16x64_bf16 v[20:23], v[24:27], v[72:79], v6\n\t"
    "v_max3_u32 v60, v60, v30, v31\n\t"
    "v_max_u32 v60, v60, v61\n\t"
    "v_cmp_eq_u32 vcc, -1, v60\n\t"
    "s_cbranch_vccnz 1f\n\t"
    "2:\n\t"
    "s_waitcnt lgkmcnt(0)\n\t"
    "s_barrier\n\t"
    "ds_read_b128 v[32:35], v5 offset:2048\n\t"
    "ds_read_b128 v[36:39], v5 offset:3072\n\t"
    "v_smfmac_f32_16x16x64_bf16 v[16:19], v[28:31], v[80:87], v6\n\t"
    "ds_read_b128 v[40:43], v5 offset:4096\n\t"
    "ds_read_b128 v[44:47], v5 offset:5120\n\t"
    "v_smfmac_f32_16x16x64_bf16 v[20:23], v[28:31], v[88:95], v6\n\t"
    "s_waitcnt lgkmcnt(3)\n\t"
    "v_smfmac_f32_16x16x64_bf16 v[16:19], v[32:35], v[96:103], v6\n\t"
    "ds_read_b128 v[48:51], v5 offset:6144\n\t"
    "v_smfmac_f32_16x16x64_bf16 v[20:23], v[32:35], v[104:111], v6\n\t"
    "ds_read_b128 v[52:55], v5 offset:7168\n\t"
    "s_waitcnt lgkmcnt(4)\n\t"
    "v_smfmac_f32_16x16x64_bf16 v[16:19], v[36:39], v[112:119], v6\n\t"
    "ds_read_b128 v[24:27], v5 offset:8192\n\t"
    "v_smfmac_f32_16x16x64_bf16 v[20:23], v[36:39], v[120:127], v6\n\t"
    "ds_read_b128 v[28:31], v5 offset:9216\n\t"
    "s_waitcnt lgkmcnt(5)\n\t"
    "v_smfmac_f32_16x16x64_bf16 v[16:19], v[40:43], v[128:135], v6\n\t"
    "ds_read_b128 v[56:59], v5 offset:10240\n\t"
    "v_smfmac_f32_16x16x64_bf16 v[20:23], v[40:43], v[136:143], v6\n\t"
    "ds_read_b128 v[60:63], v5 offset:11264\n\t"
    "ds_read_b128 v[36:39], v11 offset:36864\n\t"
    "s_waitcnt lgkmcnt(7)\n\t"
    "v_smfmac_f32_16x16x64_bf16 v[16:19], v[44:47], v[144:151], v6\n\t"
    "ds_read_b128 v[40:43], v12 offset:32768\n\t"
    "v_smfmac_f32_16x16x64_bf16 v[20:23], v[44:47], v[152:159], v6\n\t"
    "ds_read_b128 v[44:47], v12 offset:36864\n\t"
    "s_waitcnt lgkmcnt(8)\n\t"
    "v_smfmac_f32_16x16x64_bf16 v[16:19], v[48:51], v[160:167], v6\n\t"
    "v_smfmac_f32_16x16x64_bf16 v[20:23], v[48:51], v[168:175], v6\n\t"
    "ds_read_b128 v[48:51], v13 offset:32768\n\t"
    "s_waitcnt lgkmcnt(8)\n\t"
    "v_smfmac_f32_16x16x64_bf16 v[16:19], v[52:55], v[176:183], v6\n\t"
    "v_smfmac_f32_16x16x64_bf16 v[20:23], v[52:55], v[184:191], v6\n\t"
    "ds_read_b128 v[52:55], v13 offset:36864\n\t"
    "s_waitcnt lgkmcnt(8)\n\t"
    "v_smfmac_f32_16x16x64_bf16 v[16:19], v[24:27], v[192:199], v6\n\t"
    "ds_read_b32 v8, v15 offset:28672\n\t"
    "ds_read_b128 v[32:35], v5 offset:24576\n\t"
    "s_setprio 3\n\t"
    "v_smfmac_f32_16x16x64_bf16 v[20:23], v[24:27], v[200:207], v6\n\t"
    "ds_read_b128 v[24:27], v10 offset:32768\n\t"
    "s_waitcnt lgkmcnt(10)\n\t"
    "v_smfmac_f32_16x16x64_bf16 v[16:19], v[28:31], v[208:215], v6\n\t"
    "s_waitcnt lgkmcnt(9)\n\t"
    "v_smfmac_f32_16x16x64_bf16 v[16:19], v[56:59], v[224:231], v6\n\t"
    "s_waitcnt lgkmcnt(8)\n\t"
    "v_smfmac_f32_16x16x64_bf16 v[16:19], v[60:63], v[240:247], v6\n\t"
    "v_smfmac_f32_16x16x64_bf16 v[20:23], v[28:31], v[216:223], v6\n\t"
    "s_waitcnt lgkmcnt(1)\n\t"
    "21:\n\t"
    "v_cmp_ne_u32 vcc, %[sn], v8\n\t"
    "s_cbranch_vccnz 22f\n\t"
    "v_mul_f32 v32, %[lsc], v32\n\t"
    "v_mul_f32 v33, %[lsc], v33\n\t"
    "v_smfmac_f32_16x16x64_bf16 v[20:23], v[56:59], v[232:239], v6\n\t"
    "v_mul_f32 v34, %[lsc], v34\n\t"
    "v_mul_f32 v35, %[lsc], v35\n\t"
    "v_add_f32 v16, v16, v18\n\t"
    "v_add_f32 v17, v17, v19\n\t"
    "v_smfmac_f32_16x16x64_bf16 v[20:23], v[60:63], v[248:255], v6\n\t"
    "v_fma_f32 v16, v16, %[lsc], v32\n\t"
    "v_fma_f32 v17, v17, %[lsc], v33\n\t"
    "v_cvt_pk_bf16_f32 v16, v16, v17\n\t"
    "ds_write_b32 v15, v16 offset:16384\n\t"
    "s_mov_b32 %[bg], %[big]\n\t"
    "s_nop 2\n\t"
    "v_add_f32 v20, v20, v22\n\t"
    "v_add_f32 v21, v21, v23\n\t"
    "v_fma_f32 v20, v20, %[lsc], v34\n\t"
    "v_fma_f32 v21, v21, %[lsc], v35\n\t"
    "v_cvt_pk_bf16_f32 v20, v20, v21\n\t"
    "ds_write_b32 v15, v20 offset:16640\n\t"
    "ds_read_b128 v[28:31], v10 offset:36864\n\t"
    "ds_read_b128 v[32:35], v11 offset:32768\n\t"
    "s_waitcnt lgkmcnt(2)\n\t"
    "s_barrier\n\t"
    "ds_read_b64_tr_b16 v[16:17], v7 offset:16384\n\t"
    "ds_read_b64_tr_b16 v[18:19], v7 offset:16448\n\t"
    "ds_read_b64_tr_b16 v[20:21], v7 offset:16896\n\t"
    "ds_read_b64_tr_b16 v[22:23], v7 offset:16960\n\t"
    "ds_read_b64_tr_b16 v[56:57], v7 offset:17408\n\t"
    "ds_read_b64_tr_b16 v[58:59], v7 offset:17472\n\t"
    "ds_read_b64_tr_b16 v[60:61], v7 offset:17920\n\t"
    "ds_read_b64_tr_b16 v[62:63], v7 offset:17984\n\t"
    "global_load_dwordx4 v[6:9], v5, %[vs] sc1\n\t"
    "global_load_dwordx4 v[10:13], v5, %[vs] offset:1024 sc1\n\t"
    "s_waitcnt lgkmcnt(6)\n\t"
    "v_mfma_f32_16x16x32_bf16 v[24:27], v[24:27], v[16:19], 0\n\t"
    "v_mfma_f32_16x16x32_bf16 v[28:31], v[28:31], v[16:19], 0\n\t"
    "s_waitcnt lgkmcnt(4)\n\t"
    "v_mfma_f32_16x16x32_bf16 v[24:27], v[32:35], v[20:23], v[24:27]\n\t"
    "v_mfma_f32_16x16x32_bf16 v[28:31], v[36:39], v[20:23], v[28:31]\n\t"
    "v_xor_b32 v39, 32, v14\n\t"
    "ds_read_b64_tr_b16 v[32:33], v14 offset:40960\n\t"
    "ds_read_b64_tr_b16 v[34:35], v14 offset:45056\n\t"
    "ds_read_b64_tr_b16 v[36:37], v39 offset:40960\n\t"
    "ds_read_b64_tr_b16 v[38:39], v39 offset:45056\n\t"
    "s_waitcnt lgkmcnt(6)\n\t"
    "v_mfma_f32_16x16x32_bf16 v[24:27], v[40:43], v[56:59], v[24:27]\n\t"
    "v_mfma_f32_16x16x32_bf16 v[28:31], v[44:47], v[56:59], v[28:31]\n\t"
    "s_waitcnt lgkmcnt(4)\n\t"
    "v_mfma_f32_16x16x32_bf16 v[24:27], v[48:51], v[60:63], v[24:27]\n\t"
    "v_mfma_f32_16x16x32_bf16 v[28:31], v[52:55], v[60:63], v[28:31]\n\t"
    "v_mov_b32 v52, %[one]\n\t"
    "v_mov_b32 v53, %[one]\n\t"
    "v_mov_b32 v54, %[one]\n\t"
    "v_mov_b32 v55, %[one]\n\t"
    "v_mov_b32 v45, 0\n\t"
    "v_mov_b32 v47, 0\n\t"
    "v_mov_b32 v48, %[one]\n\t"
    "v_max3_f32 v14, |v24|, |v25|, |v26|\n\t"
    "v_max3_f32 v40, |v27|, |v28|, |v29|\n\t"
    "v_max3_f32 v14, v14, |v30|, |v31|\n\t"
    "v_max_f32 v14, v14, v40\n\t"
    "v_cmp_nge_f32 vcc, %[big], v14\n\t"
    "v_mov_b32 v14, 0\n\t"
    "s_cbranch_vccnz 12f\n\t"
    "13:\n\t"
    "v_exp_f32 v24, v24\n\t"
    "v_exp_f32 v25, v25\n\t"
    "v_exp_f32 v26, v26\n\t"
    "v_exp_f32 v27, v27\n\t"
    "v_exp_f32 v28, v28\n\t"
    "v_exp_f32 v29, v29\n\t"
    "v_exp_f32 v30, v30\n\t"
    "v_exp_f32 v31, v31\n\t"
    "v_cvt_pk_bf16_f32 v40, v24, v25\n\t"
    "v_cvt_pk_bf16_f32 v41, v26, v27\n\t"
    "v_cvt_pk_bf16_f32 v42, v28, v29\n\t"
    "v_cvt_pk_bf16_f32 v43, v30, v31\n\t"
    "v_mov_b32 v49, 0\n\t"
    "v_mov_b32 v51, 0\n\t"
    "s_waitcnt lgkmcnt(0)\n\t"
    "v_mfma_f32_16x16x32_bf16 v[52:55], v[52:55], v[40:43], 0\n\t"
    "v_mfma_f32_16x16x32_bf16 v[24:27], v[32:35], v[40:43], 0\n\t"
    "v_mfma_f32_16x16x32_bf16 v[28:31], v[36:39], v[40:43], 0\n\t"
    "s_waitcnt vmcnt(1)\n\t"
    "5:\n\t"
    "v_mfma_f32_16x16x32_bf16 v[32:35], v[6:9], v[16:19], 0\n\t"
    "v_mfma_f32_16x16x32_bf16 v[36:39], v[6:9], v[20:23], 0\n\t"
    "s_waitcnt vmcnt(0)\n\t"
    "v_mfma_f32_16x16x32_bf16 v[32:35], v[10:13], v[56:59], v[32:35]\n\t"
    "v_mfma_f32_16x16x32_bf16 v[36:39], v[10:13], v[60:63], v[36:39]\n\t"
    "ds_read_b32 v44, v15 offset:18432\n\t"
    "ds_read_b32 v46, v15 offset:18688\n\t"
    "v_max3_u32 v40, v6, v7, v8\n\t"
    "v_max3_u32 v41, v9, v10, v11\n\t"
    "v_max3_u32 v40, v40, v12, v13\n\t"
    "v_max_u32 v40, v40, v41\n\t"
    "v_cmp_eq_u32 vcc, -1, v40\n\t"
    "s_cbranch_vccnz 4f\n\t"
    "6:\n\t"
    "s_nop 0\n\t"
    "v_add_f32 v32, v32, v38\n\t"
    "v_add_f32 v33, v33, v39\n\t"
    "v_exp_f32 v34, v32\n\t"
    "v_max_f32 v40, v32, v33\n\t"
    "v_exp_f32 v35, v33\n\t"
    "v_cmp_nge_f32 vcc, %[bg], v40\n\t"
    "s_cbranch_vccnz 10f\n\t"
    "11:\n\t"
    "v_cvt_pk_bf16_f32 v50, v34, v35\n\t"
    "17:\n\t"
    "s_waitcnt vmcnt(0) lgkmcnt(0)\n\t"
    "v_max_u32 v40, v44, v46\n\t"
    "v_cmp_eq_u32 vcc, -1, v40\n\t"
    "s_cbranch_vccnz 18f\n\t"
    "19:\n\t"
    "v_mfma_f32_16x16x16_bf16 v[52:55], v[48:49], v[50:51], v[52:55]\n\t"
    "v_mfma_f32_16x16x16_bf16 v[24:27], v[44:45], v[50:51], v[24:27]\n\t"
    "v_mfma_f32_16x16x16_bf16 v[28:31], v[46:47], v[50:51], v[28:31]\n\t"
    "s_nop 5\n\t"
    "v_rcp_f32 v42, v52\n\t"
    "s_nop 0\n\t"
    "v_pk_mul_f32 v[24:25], v[24:25], v[42:43] op_sel_hi:[1,0]\n\t"
    "v_pk_mul_f32 v[26:27], v[26:27], v[42:43] op_sel_hi:[1,0]\n\t"
    "v_pk_mul_f32 v[28:29], v[28:29], v[42:43] op_sel_hi:[1,0]\n\t"
    "v_pk_mul_f32 v[30:31], v[30:31], v[42:43] op_sel_hi:[1,0]\n\t"
    "v_cvt_pk_bf16_f32 v36, v24, v25\n\t"
    "v_cvt_pk_bf16_f32 v37, v26, v27\n\t"
    "v_cvt_pk_bf16_f32 v38, v28, v29\n\t"
    "v_cvt_pk_bf16_f32 v39, v30, v31\n\t"
    "v_min_u32 v36, -2, v36\n\t"
    "v_min_u32 v37, -2, v37\n\t"
    "v_min_u32 v38, -2, v38\n\t"
    "v_min_u32 v39, -2, v39\n\t"
    "global_store_dwordx4 v4, v[36:39], %[od] sc1\n\t"
    "ds_write_b128 v5, v[6:9] offset:20480\n\t"
    "s_setprio 1\n\t"
    "s_waitcnt lgkmcnt(0)\n\t"
    "s_barrier\n\t"
    "s_waitcnt lgkmcnt(0)\n\t"
    "s_branch 9f\n\t"
    "1:\n\t"
    "s_add_u32 %[sp], %[sp], 1\n\t"
    "s_cmp_ge_u32 %[sp], %[cap]\n\t"
    "s_cbranch_scc1 7f\n\t"
    "global_load_dwordx4 v[24:27], v5, %[ag] sc1\n\t"
    "global_load_dwordx4 v[28:31], v5, %[ag] offset:1024 sc1\n\t"
    "s_waitcnt lgkmcnt(0)\n\t"
    "s_memtime %[tc]\n\t"
    "s_nop 7\n\t"
    "s_branch 3b\n\t"
    "7:\n\t"
    "s_mov_b32 %[cap], 0\n\t"
    "s_branch 2b\n\t"
    "4:\n\t"
    "s_add_u32 %[sp], %[sp], 0x10000\n\t"
    "s_lshr_b32 %[st], %[sp], 16\n\t"
    "s_cmp_ge_u32 %[st], %[cap]\n\t"
    "s_cbranch_scc1 8f\n\t"
    "global_load_dwordx4 v[6:9], v5, %[vs] sc1\n\t"
    "global_load_dwordx4 v[10:13], v5, %[vs] offset:1024 sc1\n\t"
    "s_waitcnt vmcnt(0) lgkmcnt(0)\n\t"
    "s_branch 5b\n\t"
    "8:\n\t"
    "s_mov_b32 %[cap], 0\n\t"
    "s_branch 6b\n\t"
    "18:\n\t"
    "s_add_u32 %[sp], %[sp], 0x10000\n\t"
    "s_lshr_b32 %[st], %[sp], 16\n\t"
    "s_cmp_ge_u32 %[st], %[cap]\n\t"
    "s_cbranch_scc1 20f\n\t"
    "ds_read_b32 v44, v15 offset:18432\n\t"
    "ds_read_b32 v46, v15 offset:18688\n\t"
    "s_waitcnt vmcnt(0) lgkmcnt(0)\n\t"
    "s_branch 17b\n\t"
    "20:\n\t"
    "s_mov_b32 %[cap], 0\n\t"
    "s_branch 19b\n\t"
    "22:\n\t"
    "ds_read_b32 v8, v15 offset:28672\n\t"
    "ds_read_b128 v[32:35], v5 offset:24576\n\t"
    "s_waitcnt lgkmcnt(0)\n\t"
    "s_branch 21b\n\t"
    "10:\n\t"
    "v_max_f32 v40, v40, v14\n\t"
    "v_mov_b32 v42, v40\n\t"
    "s_nop 1\n\t"
    "v_permlane16_swap_b32 v40, v42\n\t"
    "v_max_f32 v40, v40, v42\n\t"
    "v_mov_b32 v42, v40\n\t"
    "s_nop 1\n\t"
    "v_permlane32_swap_b32 v40, v42\n\t"
    "v_max_f32 v40, v40, v42\n\t"
    "v_sub_f32 v34, v32, v40\n\t"
    "v_sub_f32 v35, v33, v40\n\t"
    "v_sub_f32 v40, v14, v40\n\t"
    "v_exp_f32 v40, v40\n\t"
    "v_exp_f32 v34, v34\n\t"
    "v_exp_f32 v35, v35\n\t"
    "v_mul_f32 v24, v24, v40\n\t"
    "v_mul_f32 v25, v25, v40\n\t"
    "v_mul_f32 v26, v26, v40\n\t"
    "v_mul_f32 v27, v27, v40\n\t"
    "v_mul_f32 v28, v28, v40\n\t"
    "v_mul_f32 v29, v29, v40\n\t"
    "v_mul_f32 v30, v30, v40\n\t"
    "v_mul_f32 v31, v31, v40\n\t"
    "v_mul_f32 v52, v52, v40\n\t"
    "v_mul_f32 v53, v53, v40\n\t"
    "v_mul_f32 v54, v54, v40\n\t"
    "v_mul_f32 v55, v55, v40\n\t"
    "s_branch 11b\n\t"
    "12:\n\t"
    "v_max3_f32 v14, v24, v25, v26\n\t"
    "v_max3_f32 v40, v27, v28, v29\n\t"
    "v_max3_f32 v14, v14, v30, v31\n\t"
    "v_max3_f32 v14, v14, v40, %[neg]\n\t"
    "v_mov_b32 v40, v14\n\t"
    "s_nop 1\n\t"
    "v_permlane16_swap_b32 v14, v40\n\t"
    "v_max_f32 v14, v14, v40\n\t"
    "v_mov_b32 v40, v14\n\t"
    "s_nop 1\n\t"
    "v_permlane32_swap_b32 v14, v40\n\t"
    "v_max_f32 v14, v14, v40\n\t"
    "v_sub_f32 v24, v24, v14\n\t"
    "v_sub_f32 v25, v25, v14\n\t"
    "v_sub_f32 v26, v26, v14\n\t"
    "v_sub_f32 v27, v27, v14\n\t"
    "v_sub_f32 v28, v28, v14\n\t"
    "v_sub_f32 v29, v29, v14\n\t"
    "v_sub_f32 v30, v30, v14\n\t"
    "v_sub_f32 v31, v31, v14\n\t"
    "s_mov_b32 %[bg], 0x7fc00000\n\t"
    "s_branch 13b\n\t"
    "9:"
    : [tc] "=&s"(tc), [sp] "=&s"(sp), [cap] "+s"(cap), [st] "=&s"(st), [bg] "=&s"(bg)
    : [ag] "s"(ag), [c44] "s"(0x4444u), [lsc] "s"(LSC), [sn] "s"(sn), [pb] "s"(pb), [vs] "s"(vs), [od] "s"(od), [neg] "s"(-1.0e38f), [one] "s"(0x3F803F80u), [big] "s"(24.0f), "{v[64:95]}"(W.w0), "{v[96:127]}"(W.w1), "{v[128:159]}"(W.w2), "{v[160:191]}"(W.w3), "{v[192:223]}"(W.w4), "{v[224:255]}"(W.w5)
    : "v4", "v5", "v6", "v7", "v8", "v9", "v10", "v11", "v12", "v13", "v14", "v15", "v16", "v17", "v18", "v19", "v20", "v21", "v22", "v23", "v24", "v25", "v26", "v27", "v28", "v29", "v30", "v31", "v32", "v33", "v34", "v35", "v36", "v37", "v38", "v39", "v40", "v41", "v42", "v43", "v44", "v45", "v46", "v47", "v48", "v49", "v50", "v51", "v52", "v53", "v54", "v55", "v56", "v57", "v58", "v59", "v60", "v61", "v62", "v63", "vcc", "scc", "memory");
}
template <> __device__ __forceinline__ void qk_step<1>(const QKW& W, const char* ag, const char* vs, const char* v2, const char* od, unsigned pb, unsigned sn, unsigned& cap, unsigned& sp,
    unsigned long long& tc, unsigned long long* ts, unsigned& d0, unsigned& d1) {
  unsigned st, bg;
  asm volatile("v_mbcnt_lo_u32_b32 v60, -1, 0\n\t"
    "v_mbcnt_hi_u32_b32 v60, -1, v60\n\t"
    "v_lshlrev_b32 v5, 4, v60\n\t"
    "s_mov_b32 %[sp], 0\n\t"
    "s_setprio 2\n\t"
    "global_load_dwordx4 v[24:27], v5, %[ag] sc1\n\t"
    "global_load_dwordx4 v[28:31], v5, %[ag] offset:1024 sc1\n\t"
    "s_memtime %[tc]\n\t"
    "v_lshrrev_b32 v32, 4, v60\n\t"
    "v_and_b32 v33, 15, v60\n\t"
    "v_lshrrev_b32 v34, 2, v33\n\t"
    "v_and_b32 v35, 3, v60\n\t"
    "v_and_b32 v61, 1, v60\n\t"
    "v_lshlrev_b32 v61, 3, v61\n\t"
    "v_lshl_or_b32 v7, v34, 4, v61\n\t"
    "v_lshl_or_b32 v7, v32, 7, v7\n\t"
    "v_xor_b32 v10, v32, v33\n\t"
    "v_lshlrev_b32 v10, 4, v10\n\t"
    "v_lshl_or_b32 v10, v33, 8, v10\n\t"
    "v_or_b32 v10, %[pb], v10\n\t"
    "v_xor_b32 v11, 0x40, v10\n\t"
    "v_xor_b32 v12, 0x80, v10\n\t"
    "v_xor_b32 v13, 0xc0, v10\n\t"
    "v_lshl_or_b32 v34, v32, 2, v34\n\t"
    "v_lshrrev_b32 v35, 1, v35\n\t"
    "v_or_b32 v35, 4, v35\n\t"
    "v_xor_b32 v35, v35, v34\n\t"
    "v_lshl_or_b32 v14, v35, 4, v61\n\t"
    "v_lshl_or_b32 v14, v34, 8, v14\n\t"
    "v_or_b32 v14, %[pb], v14\n\t"
    "v_and_b32 v61, 3, v60\n\t"
    "v_lshlrev_b32 v61, 6, v61\n\t"
    "v_lshl_or_b32 v4, v32, 4, v61\n\t"
    "v_and_b32 v61, 4, v60\n\t"
    "v_lshl_or_b32 v4, v61, 10, v4\n\t"
    "v_and_b32 v61, 8, v60\n\t"
    "v_lshl_or_b32 v4, v61, 14, v4\n\t"
    "v_or_b32 v4, 256, v4\n\t"
    "v_and_b32 v61, 2, v60\n\t"
    "v_mul_u32_u24 v61, 0x5555, v61\n\t"
    "v_or_b32 v6, %[c44], v61\n\t"
    "v_lshlrev_b32 v61, 2, v32\n\t"
    "v_lshl_or_b32 v15, v33, 4, v61\n\t"
    "3:\n\t"
    "v_mov_b32 v16, 0\n\t"
    "v_mov_b32 v17, 0\n\t"
    "v_mov_b32 v18, 0\n\t"
    "v_mov_b32 v19, 0\n\t"
    "v_mov_b32 v20, 0\n\t"
    "v_mov_b32 v21, 0\n\t"
    "v_mov_b32 v22, 0\n\t"
    "v_mov_b32 v23, 0\n\t"
    "s_waitcnt vmcnt(0) lgkmcnt(0)\n\t"
    "ds_write_b128 v5, v[24:27] offset:2048\n\t"
    "ds_write_b128 v5, v[28:31] offset:3072\n\t"
    "v_smfmac_f32_16x16x64_bf16 v[16:19], v[24:27], v[96:103], v6\n\t"
    "v_max3_u32 v60, v24, v25, v26\n\t"
    "v_max3_u32 v61, v27, v28, v29\n\t"
    "v_smfmac_f32_16x16x64_bf16 v[20:23], v[24:27], v[104:111], v6\n\t"
    "v_max3_u32 v60, v60, v30, v31\n\t"
    "v_max_u32 v60, v60, v61\n\t"
    "v_cmp_eq_u32 vcc, -1, v60\n\t"
    "s_cbranch_vccnz 1f\n\t"
    "2:\n\t"
    "s_waitcnt lgkmcnt(0)\n\t"
    "s_barrier\n\t"
    "ds_read_b128 v[32:35], v5 offset:0\n\t"
    "ds_read_b128 v[36:39], v5 offset:1024\n\t"
    "v_smfmac_f32_16x16x64_bf16 v[16:19], v[28:31], v[112:119], v6\n\t"
    "ds_read_b128 v[40:43], v5 offset:4096\n\t"
    "ds_read_b128 v[44:47], v5 offset:5120\n\t"
    "v_smfmac_f32_16x16x64_bf16 v[20:23], v[28:31], v[120:127], v6\n\t"
    "s_waitcnt lgkmcnt(3)\n\t"
    "v_smfmac_f32_16x16x64_bf16 v[16:19], v[32:35], v[64:71], v6\n\t"
    "ds_read_b128 v[48:51], v5 offset:6144\n\t"
    "v_smfmac_f32_16x16x64_bf16 v[20:23], v[32:35], v[72:79], v6\n\t"
    "ds_read_b128 v[52:55], v5 offset:7168\n\t"
    "s_waitcnt lgkmcnt(4)\n\t"
    "v_smfmac_f32_16x16x64_bf16 v[16:19], v[36:39], v[80:87], v6\n\t"
    "ds_read_b128 v[24:27], v5 offset:8192\n\t"
    "v_smfmac_f32_16x16x64_bf16 v[20:23], v[36:39], v[88:95], v6\n\t"
    "ds_read_b128 v[28:31], v5 offset:9216\n\t"
    "s_waitcnt lgkmcnt(5)\n\t"
    "v_smfmac_f32_16x16x64_bf16 v[16:19], v[40:43], v[128:135], v6\n\t"
    "ds_read_b128 v[56:59], v5 offset:10240\n\t"
    "v_smfmac_f32_16x16x64_bf16 v[20:23], v[40:43], v[136:143], v6\n\t"
    "ds_read_b128 v[60:63], v5 offset:11264\n\t"
    "ds_read_b128 v[36:39], v11 offset:36864\n\t"
    "s_waitcnt lgkmcnt(7)\n\t"
    "v_smfmac_f32_16x16x64_bf16 v[16:19], v[44:47], v[144:151], v6\n\t"
    "ds_read_b128 v[40:43], v12 offset:32768\n\t"
    "v_smfmac_f32_16x16x64_bf16 v[20:23], v[44:47], v[152:159], v6\n\t"
    "ds_read_b128 v[44:47], v12 offset:36864\n\t"
    "s_waitcnt lgkmcnt(8)\n\t"
    "v_smfmac_f32_16x16x64_bf16 v[16:19], v[48:51], v[160:167], v6\n\t"
    "v_smfmac_f32_16x16x64_bf16 v[20:23], v[48:51], v[168:175], v6\n\t"
    "ds_read_b128 v[48:51], v13 offset:32768\n\t"
    "s_waitcnt lgkmcnt(8)\n\t"
    "v_smfmac_f32_16x16x64_bf16 v[16:19], v[52:55], v[176:183], v6\n\t"
    "v_smfmac_f32_16x16x64_bf16 v[20:23], v[52:55], v[184:191], v6\n\t"
    "ds_read_b128 v[52:55], v13 offset:36864\n\t"
    "s_waitcnt lgkmcnt(8)\n\t"
    "v_smfmac_f32_16x16x64_bf16 v[16:19], v[24:27], v[192:199], v6\n\t"
    "ds_read_b32 v8, v15 offset:28928\n\t"
    "ds_read_b128 v[32:35], v5 offset:25600\n\t"
    "s_setprio 3\n\t"
    "v_smfmac_f32_16x16x64_bf16 v[20:23], v[24:27], v[200:207], v6\n\t"
    "ds_read_b128 v[24:27], v10 offset:32768\n\t"
    "s_waitcnt lgkmcnt(10)\n\t"
    "v_smfmac_f32_16x16x64_bf16 v[16:19], v[28:31], v[208:215], v6\n\t"
    "s_waitcnt lgkmcnt(9)\n\t"
    "v_smfmac_f32_16x16x64_bf16 v[16:19], v[56:59], v[224:231], v6\n\t"
    "s_waitcnt lgkmcnt(8)\n\t"
    "v_smfmac_f32_16x16x64_bf16 v[16:19], v[60:63], v[240:247], v6\n\t"
    "v_smfmac_f32_16x16x64_bf16 v[20:23], v[28:31], v[216:223], v6\n\t"
    "s_waitcnt lgkmcnt(1)\n\t"
    "21:\n\t"
    "v_cmp_ne_u32 vcc, %[sn], v8\n\t"
    "s_cbranch_vccnz 22f\n\t"
    "v_mul_f32 v32, %[lsc], v32\n\t"
    "v_mul_f32 v33, %[lsc], v33\n\t"
    "v_smfmac_f32_16x16x64_bf16 v[20:23], v[56:59], v[232:239], v6\n\t"
    "v_mul_f32 v34, %[lsc], v34\n\t"
    "v_mul_f32 v35, %[lsc], v35\n\t"
    "v_add_f32 v16, v16, v18\n\t"
    "v_add_f32 v17, v17, v19\n\t"
    "v_smfmac_f32_16x16x64_bf16 v[20:23], v[60:63], v[248:255], v6\n\t"
    "v_fma_f32 v16, v16, %[lsc], v32\n\t"
    "v_fma_f32 v17, v17, %[lsc], v33\n\t"
    "v_cvt_pk_bf16_f32 v16, v16, v17\n\t"
    "ds_write_b32 v15, v16 offset:16896\n\t"
    "s_mov_b32 %[bg], %[big]\n\t"
    "s_nop 2\n\t"
    "v_add_f32 v20, v20, v22\n\t"
    "v_add_f32 v21, v21, v23\n\t"
    "v_fma_f32 v20, v20, %[lsc], v34\n\t"
    "v_fma_f32 v21, v21, %[lsc], v35\n\t"
    "v_cvt_pk_bf16_f32 v20, v20, v21\n\t"
    "ds_write_b32 v15, v20 offset:17152\n\t"
    "ds_read_b128 v[28:31], v10 offset:36864\n\t"
    "ds_read_b128 v[32:35], v11 offset:32768\n\t"
    "s_waitcnt lgkmcnt(2)\n\t"
    "s_barrier\n\t"
    "ds_read_b64_tr_b16 v[16:17], v7 offset:16384\n\t"
    "ds_read_b64_tr_b16 v[18:19], v7 offset:16448\n\t"
    "ds_read_b64_tr_b16 v[20:21], v7 offset:16896\n\t"
    "ds_read_b64_tr_b16 v[22:23], v7 offset:16960\n\t"
    "ds_read_b64_tr_b16 v[56:57], v7 offset:17408\n\t"
    "ds_read_b64_tr_b16 v[58:59], v7 offset:17472\n\t"
    "ds_read_b64_tr_b16 v[60:61], v7 offset:17920\n\t"
    "ds_read_b64_tr_b16 v[62:63], v7 offset:17984\n\t"
    "global_load_dwordx4 v[6:9], v5, %[vs] sc1\n\t"
    "global_load_dwordx4 v[10:13], v5, %[vs] offset:1024 sc1\n\t"
    "s_waitcnt lgkmcnt(6)\n\t"
    "v_mfma_f32_16x16x32_bf16 v[24:27], v[24:27], v[16:19], 0\n\t"
    "v_mfma_f32_16x16x32_bf16 v[28:31], v[28:31], v[16:19], 0\n\t"
    "s_waitcnt lgkmcnt(4)\n\t"
    "v_mfma_f32_16x16x32_bf16 v[24:27], v[32:35], v[20:23], v[24:27]\n\t"
    "v_mfma_f32_16x16x32_bf16 v[28:31], v[36:39], v[20:23], v[28:31]\n\t"
    "v_xor_b32 v39, 32, v14\n\t"
    "ds_read_b64_tr_b16 v[32:33], v14 offset:40960\n\t"
    "ds_read_b64_tr_b16 v[34:35], v14 offset:45056\n\t"
    "ds_read_b64_tr_b16 v[36:37], v39 offset:40960\n\t"
    "ds_read_b64_tr_b16 v[38:39], v39 offset:45056\n\t"
    "s_waitcnt lgkmcnt(6)\n\t"
    "v_mfma_f32_16x16x32_bf16 v[24:27], v[40:43], v[56:59], v[24:27]\n\t"
    "v_mfma_f32_16x16x32_bf16 v[28:31], v[44:47], v[56:59], v[28:31]\n\t"
    "s_waitcnt lgkmcnt(4)\n\t"
    "v_mfma_f32_16x16x32_bf16 v[24:27], v[48:51], v[60:63], v[24:27]\n\t"
    "v_mfma_f32_16x16x32_bf16 v[28:31], v[52:55], v[60:63], v[28:31]\n\t"
    "v_mov_b32 v52, %[one]\n\t"
    "v_mov_b32 v53, %[one]\n\t"
    "v_mov_b32 v54, %[one]\n\t"
    "v_mov_b32 v55, %[one]\n\t"
    "v_mov_b32 v45, 0\n\t"
    "v_mov_b32 v47, 0\n\t"
    "v_mov_b32 v48, %[one]\n\t"
    "v_max3_f32 v14, |v24|, |v25|, |v26|\n\t"
    "v_max3_f32 v40, |v27|, |v28|, |v29|\n\t"
    "v_max3_f32 v14, v14, |v30|, |v31|\n\t"
    "v_max_f32 v14, v14, v40\n\t"
    "v_cmp_nge_f32 vcc, %[big], v14\n\t"
    "v_mov_b32 v14, 0\n\t"
    "s_cbranch_vccnz 12f\n\t"
    "13:\n\t"
    "v_exp_f32 v24, v24\n\t"
    "v_exp_f32 v25, v25\n\t"
    "v_exp_f32 v26, v26\n\t"
    "v_exp_f32 v27, v27\n\t"
    "v_exp_f32 v28, v28\n\t"
    "v_exp_f32 v29, v29\n\t"
    "v_exp_f32 v30, v30\n\t"
    "v_exp_f32 v31, v31\n\t"
    "v_cvt_pk_bf16_f32 v40, v24, v25\n\t"
    "v_cvt_pk_bf16_f32 v41, v26, v27\n\t"
    "v_cvt_pk_bf16_f32 v42, v28, v29\n\t"
    "v_cvt_pk_bf16_f32 v43, v30, v31\n\t"
    "v_mov_b32 v49, 0\n\t"
    "v_mov_b32 v51, 0\n\t"
    "s_waitcnt lgkmcnt(0)\n\t"
    "v_mfma_f32_16x16x32_bf16 v[52:55], v[52:55], v[40:43], 0\n\t"
    "v_mfma_f32_16x16x32_bf16 v[24:27], v[32:35], v[40:43], 0\n\t"
    "v_mfma_f32_16x16x32_bf16 v[28:31], v[36:39], v[40:43], 0\n\t"
    "s_waitcnt vmcnt(1)\n\t"
    "5:\n\t"
    "v_mfma_f32_16x16x32_bf16 v[32:35], v[6:9], v[16:19], 0\n\t"
    "v_mfma_f32_16x16x32_bf16 v[36:39], v[6:9], v[20:23], 0\n\t"
    "s_waitcnt vmcnt(0)\n\t"
    "v_mfma_f32_16x16x32_bf16 v[32:35], v[10:13], v[56:59], v[32:35]\n\t"
    "v_mfma_f32_16x16x32_bf16 v[36:39], v[10:13], v[60:63], v[36:39]\n\t"
    "ds_read_b32 v44, v15 offset:18944\n\t"
    "ds_read_b32 v46, v15 offset:19200\n\t"
    "v_max3_u32 v40, v6, v7, v8\n\t"
    "v_max3_u32 v41, v9, v10, v11\n\t"
    "v_max3_u32 v40, v40, v12, v13\n\t"
    "v_max_u32 v40, v40, v41\n\t"
    "v_cmp_eq_u32 vcc, -1, v40\n\t"
    "s_cbranch_vccnz 4f\n\t"
    "6:\n\t"
    "s_nop 0\n\t"
    "v_add_f32 v32, v32, v38\n\t"
    "v_add_f32 v33, v33, v39\n\t"
    "v_exp_f32 v34, v32\n\t"
    "v_max_f32 v40, v32, v33\n\t"
    "v_exp_f32 v35, v33\n\t"
    "v_cmp_nge_f32 vcc, %[bg], v40\n\t"
    "s_cbranch_vccnz 10f\n\t"
    "11:\n\t"
    "v_cvt_pk_bf16_f32 v50, v34, v35\n\t"
    "17:\n\t"
    "s_waitcnt vmcnt(0) lgkmcnt(0)\n\t"
    "v_max_u32 v40, v44, v46\n\t"
    "v_cmp_eq_u32 vcc, -1, v40\n\t"
    "s_cbranch_vccnz 18f\n\t"
    "19:\n\t"
    "v_mfma_f32_16x16x16_bf16 v[52:55], v[48:49], v[50:51], v[52:55]\n\t"
    "v_mfma_f32_16x16x16_bf16 v[24:27], v[44:45], v[50:51], v[24:27]\n\t"
    "v_mfma_f32_16x16x16_bf16 v[28:31], v[46:47], v[50:51], v[28:31]\n\t"
    "s_nop 5\n\t"
    "v_rcp_f32 v42, v52\n\t"
    "s_nop 0\n\t"
    "v_pk_mul_f32 v[24:25], v[24:25], v[42:43] op_sel_hi:[1,0]\n\t"
    "v_pk_mul_f32 v[26:27], v[26:27], v[42:43] op_sel_hi:[1,0]\n\t"
    "v_pk_mul_f32 v[28:29], v[28:29], v[42:43] op_sel_hi:[1,0]\n\t"
    "v_pk_mul_f32 v[30:31], v[30:31], v[42:43] op_sel_hi:[1,0]\n\t"
    "v_cvt_pk_bf16_f32 v36, v24, v25\n\t"
    "v_cvt_pk_bf16_f32 v37, v26, v27\n\t"
    "v_cvt_pk_bf16_f32 v38, v28, v29\n\t"
    "v_cvt_pk_bf16_f32 v39, v30, v31\n\t"
    "v_min_u32 v36, -2, v36\n\t"
    "v_min_u32 v37, -2, v37\n\t"
    "v_min_u32 v38, -2, v38\n\t"
    "v_min_u32 v39, -2, v39\n\t"
    "global_store_dwordx4 v4, v[36:39], %[od] sc1\n\t"
    "ds_write_b128 v5, v[10:13] offset:21504\n\t"
    "s_setprio 1\n\t"
    "s_waitcnt lgkmcnt(0)\n\t"
    "s_barrier\n\t"
    "s_waitcnt lgkmcnt(0)\n\t"
    "s_branch 9f\n\t"
    "1:\n\t"
    "s_add_u32 %[sp], %[sp], 1\n\t"
    "s_cmp_ge_u32 %[sp], %[cap]\n\t"
    "s_cbranch_scc1 7f\n\t"
    "global_load_dwordx4 v[24:27], v5, %[ag] sc1\n\t"
    "global_load_dwordx4 v[28:31], v5, %[ag] offset:1024 sc1\n\t"
    "s_waitcnt lgkmcnt(0)\n\t"
    "s_memtime %[tc]\n\t"
    "s_nop 7\n\t"
    "s_branch 3b\n\t"
    "7:\n\t"
    "s_mov_b32 %[cap], 0\n\t"
    "s_branch 2b\n\t"
    "4:\n\t"
    "s_add_u32 %[sp], %[sp], 0x10000\n\t"
    "s_lshr_b32 %[st], %[sp], 16\n\t"
    "s_cmp_ge_u32 %[st], %[cap]\n\t"
    "s_cbranch_scc1 8f\n\t"
    "global_load_dwordx4 v[6:9], v5, %[vs] sc1\n\t"
    "global_load_dwordx4 v[10:13], v5, %[vs] offset:1024 sc1\n\t"
    "s_waitcnt vmcnt(0) lgkmcnt(0)\n\t"
    "s_branch 5b\n\t"
    "8:\n\t"
    "s_mov_b32 %[cap], 0\n\t"
    "s_branch 6b\n\t"
    "18:\n\t"
    "s_add_u32 %[sp], %[sp], 0x10000\n\t"
    "s_lshr_b32 %[st], %[sp], 16\n\t"
    "s_cmp_ge_u32 %[st], %[cap]\n\t"
    "s_cbranch_scc1 20f\n\t"
    "ds_read_b32 v44, v15 offset:18944\n\t"
    "ds_read_b32 v46, v15 offset:19200\n\t"
    "s_waitcnt vmcnt(0) lgkmcnt(0)\n\t"
    "s_branch 17b\n\t"
    "20:\n\t"
    "s_mov_b32 %[cap], 0\n\t"
    "s_branch 19b\n\t"
    "22:\n\t"
    "ds_read_b32 v8, v15 offset:28928\n\t"
    "ds_read_b128 v[32:35], v5 offset:25600\n\t"
    "s_waitcnt lgkmcnt(0)\n\t"
    "s_branch 21b\n\t"
    "10:\n\t"
    "v_max_f32 v40, v40, v14\n\t"
    "v_mov_b32 v42, v40\n\t"
    "s_nop 1\n\t"
    "v_permlane16_swap_b32 v40, v42\n\t"
    "v_max_f32 v40, v40, v42\n\t"
    "v_mov_b32 v42, v40\n\t"
    "s_nop 1\n\t"
    "v_permlane32_swap_b32 v40, v42\n\t"
    "v_max_f32 v40, v40, v42\n\t"
    "v_sub_f32 v34, v32, v40\n\t"
    "v_sub_f32 v35, v33, v40\n\t"
    "v_sub_f32 v40, v14, v40\n\t"
    "v_exp_f32 v40, v40\n\t"
    "v_exp_f32 v34, v34\n\t"
    "v_exp_f32 v35, v35\n\t"
    "v_mul_f32 v24, v24, v40\n\t"
    "v_mul_f32 v25, v25, v40\n\t"
    "v_mul_f32 v26, v26, v40\n\t"
    "v_mul_f32 v27, v27, v40\n\t"
    "v_mul_f32 v28, v28, v40\n\t"
    "v_mul_f32 v29, v29, v40\n\t"
    "v_mul_f32 v30, v30, v40\n\t"
    "v_mul_f32 v31, v31, v40\n\t"
    "v_mul_f32 v52, v52, v40\n\t"
    "v_mul_f32 v53, v53, v40\n\t"
    "v_mul_f32 v54, v54, v40\n\t"
    "v_mul_f32 v55, v55, v40\n\t"
    "s_branch 11b\n\t"
    "12:\n\t"
    "v_max3_f32 v14, v24, v25, v26\n\t"
    "v_max3_f32 v40, v27, v28, v29\n\t"
    "v_max3_f32 v14, v14, v30, v31\n\t"
    "v_max3_f32 v14, v14, v40, %[neg]\n\t"
    "v_mov_b32 v40, v14\n\t"
    "s_nop 1\n\t"
    "v_permlane16_swap_b32 v14, v40\n\t"
    "v_max_f32 v14, v14, v40\n\t"
    "v_mov_b32 v40, v14\n\t"
    "s_nop 1\n\t"
    "v_permlane32_swap_b32 v14, v40\n\t"
    "v_max_f32 v14, v14, v40\n\t"
    "v_sub_f32 v24, v24, v14\n\t"
    "v_sub_f32 v25, v25, v14\n\t"
    "v_sub_f32 v26, v26, v14\n\t"
    "v_sub_f32 v27, v27, v14\n\t"
    "v_sub_f32 v28, v28, v14\n\t"
    "v_sub_f32 v29, v29, v14\n\t"
    "v_sub_f32 v30, v30, v14\n\t"
    "v_sub_f32 v31, v31, v14\n\t"
    "s_mov_b32 %[bg], 0x7fc00000\n\t"
    "s_branch 13b\n\t"
    "9:"
    : [tc] "=&s"(tc), [sp] "=&s"(sp), [cap] "+s"(cap), [st] "=&s"(st), [bg] "=&s"(bg)
    : [ag] "s"(ag), [c44] "s"(0x4444u), [lsc] "s"(LSC), [sn] "s"(sn), [pb] "s"(pb), [vs] "s"(vs), [od] "s"(od), [neg] "s"(-1.0e38f), [one] "s"(0x3F803F80u), [big] "s"(24.0f), "{v[64:95]}"(W.w0), "{v[96:127]}"(W.w1), "{v[128:159]}"(W.w2), "{v[160:191]}"(W.w3), "{v[192:223]}"(W.w4), "{v[224:255]}"(W.w5)
    : "v4", "v5", "v6", "v7", "v8", "v9", "v10", "v11", "v12", "v13", "v14", "v15", "v16", "v17", "v18", "v19", "v20", "v21", "v22", "v23", "v24", "v25", "v26", "v27", "v28", "v29", "v30", "v31", "v32", "v33", "v34", "v35", "v36", "v37", "v38", "v39", "v40", "v41", "v42", "v43", "v44", "v45", "v46", "v47", "v48", "v49", "v50", "v51", "v52", "v53", "v54", "v55", "v56", "v57", "v58", "v59", "v60", "v61", "v62", "v63", "vcc", "scc", "memory");
}
template <> __device__ __forceinline__ void qk_step<2>(const QKW& W, const char* ag, const char* vs, const char* v2, const char* od, unsigned pb, unsigned sn, unsigned& cap, unsigned& sp,
    unsigned long long& tc, unsigned long long* ts, unsigned& d0, unsigned& d1) {
  unsigned st, bg;
  asm volatile("v_mbcnt_lo_u32_b32 v60, -1, 0\n\t"
    "v_mbcnt_hi_u32_b32 v60, -1, v60\n\t"
    "v_lshlrev_b32 v5, 4, v60\n\t"
    "s_mov_b32 %[sp], 0\n\t"
    "s_setprio 2\n\t"
    "global_load_dwordx4 v[24:27], v5, %[ag] sc1\n\t"
    "global_load_dwordx4 v[28:31], v5, %[ag] offset:1024 sc1\n\t"
    "s_memtime %[tc]\n\t"
    "v_lshrrev_b32 v32, 4, v60\n\t"
    "v_and_b32 v33, 15, v60\n\t"
    "v_lshrrev_b32 v34, 2, v33\n\t"
    "v_and_b32 v35, 3, v60\n\t"
    "v_and_b32 v61, 1, v60\n\t"
    "v_lshlrev_b32 v61, 3, v61\n\t"
    "v_lshl_or_b32 v7, v34, 4, v61\n\t"
    "v_lshl_or_b32 v7, v32, 7, v7\n\t"
    "v_xor_b32 v10, v32, v33\n\t"
    "v_lshlrev_b32 v10, 4, v10\n\t"
    "v_lshl_or_b32 v10, v33, 8, v10\n\t"
    "v_or_b32 v10, %[pb], v10\n\t"
    "v_xor_b32 v11, 0x40, v10\n\t"
    "v_xor_b32 v12, 0x80, v10\n\t"
    "v_xor_b32 v13, 0xc0, v10\n\t"
    "v_lshl_or_b32 v34, v32, 2, v34\n\t"
    "v_lshrrev_b32 v35, 1, v35\n\t"
    "v_or_b32 v35, 8, v35\n\t"
    "v_xor_b32 v35, v35, v34\n\t"
    "v_lshl_or_b32 v14, v35, 4, v61\n\t"
    "v_lshl_or_b32 v14, v34, 8, v14\n\t"
    "v_or_b32 v14, %[pb], v14\n\t"
    "v_and_b32 v61, 3, v60\n\t"
    "v_lshlrev_b32 v61, 6, v61\n\t"
    "v_lshl_or_b32 v4, v32, 4, v61\n\t"
    "v_and_b32 v61, 4, v60\n\t"
    "v_lshl_or_b32 v4, v61, 10, v4\n\t"
    "v_and_b32 v61, 8, v60\n\t"
    "v_lshl_or_b32 v4, v61, 14, v4\n\t"
    "v_or_b32 v4, 512, v4\n\t"
    "v_and_b32 v61, 2, v60\n\t"
    "v_mul_u32_u24 v61, 0x5555, v61\n\t"
    "v_or_b32 v6, %[c44], v61\n\t"
    "v_lshlrev_b32 v61, 2, v32\n\t"
    "v_lshl_or_b32 v15, v33, 4, v61\n\t"
    "3:\n\t"
    "v_mov_b32 v16, 0\n\t"
    "v_mov_b32 v17, 0\n\t"
    "v_mov_b32 v18, 0\n\t"
    "v_mov_b32 v19, 0\n\t"
    "v_mov_b32 v20, 0\n\t"
    "v_mov_b32 v21, 0\n\t"
    "v_mov_b32 v22, 0\n\t"
    "v_mov_b32 v23, 0\n\t"
    "s_waitcnt vmcnt(0) lgkmcnt(0)\n\t"
    "ds_write_b128 v5, v[24:27] offset:4096\n\t"
    "ds_write_b128 v5, v[28:31] offset:5120\n\t"
    "v_smfmac_f32_16x16x64_bf16 v[16:19], v[24:27], v[128:135], v6\n\t"
    "v_max3_u32 v60, v24, v25, v26\n\t"
    "v_max3_u32 v61, v27, v28, v29\n\t"
    "v_smfmac_f32_16x16x64_bf16 v[20:23], v[24:27], v[136:143], v6\n\t"
    "v_max3_u32 v60, v60, v30, v31\n\t"
    "v_max_u32 v60, v60, v61\n\t"
    "v_cmp_eq_u32 vcc, -1, v60\n\t"
    "s_cbranch_vccnz 1f\n\t"
    "2:\n\t"
    "s_waitcnt lgkmcnt(0)\n\t"
    "s_barrier\n\t"
    "ds_read_b128 v[32:35], v5 offset:0\n\t"
    "ds_read_b128 v[36:39], v5 offset:1024\n\t"
    "v_smfmac_f32_16x16x64_bf16 v[16:19], v[28:31], v[144:151], v6\n\t"
    "ds_read_b128 v[40:43], v5 offset:2048\n\t"
    "ds_read_b128 v[44:47], v5 offset:3072\n\t"
    "v_smfmac_f32_16x16x64_bf16 v[20:23], v[28:31], v[152:159], v6\n\t"
    "s_waitcnt lgkmcnt(3)\n\t"
    "v_smfmac_f32_16x16x64_bf16 v[16:19], v[32:35], v[64:71], v6\n\t"
    "ds_read_b128 v[48:51], v5 offset:6144\n\t"
    "v_smfmac_f32_16x16x64_bf16 v[20:23], v[32:35], v[72:79], v6\n\t"
    "ds_read_b128 v[52:55], v5 offset:7168\n\t"
    "s_waitcnt lgkmcnt(4)\n\t"
    "v_smfmac_f32_16x16x64_bf16 v[16:19], v[36:39], v[80:87], v6\n\t"
    "ds_read_b128 v[24:27], v5 offset:8192\n\t"
    "v_smfmac_f32_16x16x64_bf16 v[20:23], v[36:39], v[88:95], v6\n\t"
    "ds_read_b128 v[28:31], v5 offset:9216\n\t"
    "s_waitcnt lgkmcnt(5)\n\t"
    "v_smfmac_f32_16x16x64_bf16 v[16:19], v[40:43], v[96:103], v6\n\t"
    "ds_read_b128 v[56:59], v5 offset:10240\n\t"
    "v_smfmac_f32_16x16x64_bf16 v[20:23], v[40:43], v[104:111], v6\n\t"
    "ds_read_b128 v[60:63], v5 offset:11264\n\t"
    "ds_read_b128 v[36:39], v11 offset:36864\n\t"
    "s_waitcnt lgkmcnt(7)\n\t"
    "v_smfmac_f32_16x16x64_bf16 v[16:19], v[44:47], v[112:119], v6\n\t"
    "ds_read_b128 v[40:43], v12 offset:32768\n\t"
    "v_smfmac_f32_16x16x64_bf16 v[20:23], v[44:47], v[120:127], v6\n\t"
    "ds_read_b128 v[44:47], v12 offset:36864\n\t"
    "s_waitcnt lgkmcnt(8)\n\t"
    "v_smfmac_f32_16x16x64_bf16 v[16:19], v[48:51], v[160:167], v6\n\t"
    "v_smfmac_f32_16x16x64_bf16 v[20:23], v[48:51], v[168:175], v6\n\t"
    "ds_read_b128 v[48:51], v13 offset:32768\n\t"
    "s_waitcnt lgkmcnt(8)\n\t"
    "v_smfmac_f32_16x16x64_bf16 v[16:19], v[52:55], v[176:183], v6\n\t"
    "v_smfmac_f32_16x16x64_bf16 v[20:23], v[52:55], v[184:191], v6\n\t"
    "ds_read_b128 v[52:55], v13 offset:36864\n\t"
    "s_waitcnt lgkmcnt(8)\n\t"
    "v_smfmac_f32_16x16x64_bf16 v[16:19], v[24:27], v[192:199], v6\n\t"
    "ds_read_b32 v8, v15 offset:29184\n\t"
    "ds_read_b128 v[32:35], v5 offset:26624\n\t"
    "s_setprio 3\n\t"
    "v_smfmac_f32_16x16x64_bf16 v[20:23], v[24:27], v[200:207], v6\n\t"
    "ds_read_b128 v[24:27], v10 offset:32768\n\t"
    "s_waitcnt lgkmcnt(10)\n\t"
    "v_smfmac_f32_16x16x64_bf16 v[16:19], v[28:31], v[208:215], v6\n\t"
    "s_waitcnt lgkmcnt(9)\n\t"
    "v_smfmac_f32_16x16x64_bf16 v[16:19], v[56:59], v[224:231], v6\n\t"
    "s_waitcnt lgkmcnt(8)\n\t"
    "v_smfmac_f32_16x16x64_bf16 v[16:19], v[60:63], v[240:247], v6\n\t"
    "v_smfmac_f32_16x16x64_bf16 v[20:23], v[28:31], v[216:223], v6\n\t"
    "s_waitcnt lgkmcnt(1)\n\t"
    "21:\n\t"
    "v_cmp_ne_u32 vcc, %[sn], v8\n\t"
    "s_cbranch_vccnz 22f\n\t"
    "v_mul_f32 v32, %[lsc], v32\n\t"
    "v_mul_f32 v33, %[lsc], v33\n\t"
    "v_smfmac_f32_16x16x64_bf16 v[20:23], v[56:59], v[232:239], v6\n\t"
    "v_mul_f32 v34, %[lsc], v34\n\t"
    "v_mul_f32 v35, %[lsc], v35\n\t"
    "v_add_f32 v16, v16, v18\n\t"
    "v_add_f32 v17, v17, v19\n\t"
    "v_smfmac_f32_16x16x64_bf16 v[20:23], v[60:63], v[248:255], v6\n\t"
    "v_fma_f32 v16, v16, %[lsc], v32\n\t"
    "v_fma_f32 v17, v17, %[lsc], v33\n\t"
    "v_cvt_pk_bf16_f32 v16, v16, v17\n\t"
    "ds_write_b32 v15, v16 offset:17408\n\t"
    "s_mov_b32 %[bg], %[big]\n\t"
    "s_nop 2\n\t"
    "v_add_f32 v20, v20, v22\n\t"
    "v_add_f32 v21, v21, v23\n\t"
    "v_fma_f32 v20, v20, %[lsc], v34\n\t"
    "v_fma_f32 v21, v21, %[lsc], v35\n\t"
    "v_cvt_pk_bf16_f32 v20, v20, v21\n\t"
    "ds_write_b32 v15, v20 offset:17664\n\t"
    "ds_read_b128 v[28:31], v10 offset:36864\n\t"
    "ds_read_b128 v[32:35], v11 offset:32768\n\t"
    "s_waitcnt lgkmcnt(2)\n\t"
    "s_barrier\n\t"
    "ds_read_b64_tr_b16 v[16:17], v7 offset:16384\n\t"
    "ds_read_b64_tr_b16 v[18:19], v7 offset:16448\n\t"
    "ds_read_b64_tr_b16 v[20:21], v7 offset:16896\n\t"
    "ds_read_b64_tr_b16 v[22:23], v7 offset:16960\n\t"
    "ds_read_b64_tr_b16 v[56:57], v7 offset:17408\n\t"
    "ds_read_b64_tr_b16 v[58:59], v7 offset:17472\n\t"
    "ds_read_b64_tr_b16 v[60:61], v7 offset:17920\n\t"
    "ds_read_b64_tr_b16 v[62:63], v7 offset:17984\n\t"
    "global_load_dwordx4 v[6:9], v5, %[vs] sc1\n\t"
    "global_load_dwordx4 v[10:13], v5, %[vs] offset:1024 sc1\n\t"
    "s_waitcnt lgkmcnt(6)\n\t"
    "v_mfma_f32_16x16x32_bf16 v[24:27], v[24:27], v[16:19], 0\n\t"
    "v_mfma_f32_16x16x32_bf16 v[28:31], v[28:31], v[16:19], 0\n\t"
    "s_waitcnt lgkmcnt(4)\n\t"
    "v_mfma_f32_16x16x32_bf16 v[24:27], v[32:35], v[20:23], v[24:27]\n\t"
    "v_mfma_f32_16x16x32_bf16 v[28:31], v[36:39], v[20:23], v[28:31]\n\t"
    "v_xor_b32 v39, 32, v14\n\t"
    "ds_read_b64_tr_b16 v[32:33], v14 offset:40960\n\t"
    "ds_read_b64_tr_b16 v[34:35], v14 offset:45056\n\t"
    "ds_read_b64_tr_b16 v[36:37], v39 offset:40960\n\t"
    "ds_read_b64_tr_b16 v[38:39], v39 offset:45056\n\t"
    "s_waitcnt lgkmcnt(6)\n\t"
    "v_mfma_f32_16x16x32_bf16 v[24:27], v[40:43], v[56:59], v[24:27]\n\t"
    "v_mfma_f32_16x16x32_bf16 v[28:31], v[44:47], v[56:59], v[28:31]\n\t"
    "s_waitcnt lgkmcnt(4)\n\t"
    "v_mfma_f32_16x16x32_bf16 v[24:27], v[48:51], v[60:63], v[24:27]\n\t"
    "v_mfma_f32_16x16x32_bf16 v[28:31], v[52:55], v[60:63], v[28:31]\n\t"
    "v_mov_b32 v52, %[one]\n\t"
    "v_mov_b32 v53, %[one]\n\t"
    "v_mov_b32 v54, %[one]\n\t"
    "v_mov_b32 v55, %[one]\n\t"
    "v_mov_b32 v45, 0\n\t"
    "v_mov_b32 v47, 0\n\t"
    "v_mov_b32 v48, %[one]\n\t"
    "v_max3_f32 v14, |v24|, |v25|, |v26|\n\t"
    "v_max3_f32 v40, |v27|, |v28|, |v29|\n\t"
    "v_max3_f32 v14, v14, |v30|, |v31|\n\t"
    "v_max_f32 v14, v14, v40\n\t"
    "v_cmp_nge_f32 vcc, %[big], v14\n\t"
    "v_mov_b32 v14, 0\n\t"
    "s_cbranch_vccnz 12f\n\t"
    "13:\n\t"
    "v_lshrrev_b32 v46, 2, v5\n\t"
    "global_load_dword v44, v46, %[v2] sc1\n\t"
    "global_load_dword v46, v46, %[v2] offset:256 sc1\n\t"
    "v_exp_f32 v24, v24\n\t"
    "v_exp_f32 v25, v25\n\t"
    "v_exp_f32 v26, v26\n\t"
    "v_exp_f32 v27, v27\n\t"
    "v_exp_f32 v28, v28\n\t"
    "v_exp_f32 v29, v29\n\t"
    "v_exp_f32 v30, v30\n\t"
    "v_exp_f32 v31, v31\n\t"
    "v_cvt_pk_bf16_f32 v40, v24, v25\n\t"
    "v_cvt_pk_bf16_f32 v41, v26, v27\n\t"
    "v_cvt_pk_bf16_f32 v42, v28, v29\n\t"
    "v_cvt_pk_bf16_f32 v43, v30, v31\n\t"
    "v_mov_b32 v49, 0\n\t"
    "v_mov_b32 v51, 0\n\t"
    "s_waitcnt lgkmcnt(0)\n\t"
    "v_mfma_f32_16x16x32_bf16 v[52:55], v[52:55], v[40:43], 0\n\t"
    "v_mfma_f32_16x16x32_bf16 v[24:27], v[32:35], v[40:43], 0\n\t"
    "v_mfma_f32_16x16x32_bf16 v[28:31], v[36:39], v[40:43], 0\n\t"
    "s_waitcnt vmcnt(3)\n\t"
    "5:\n\t"
    "v_mfma_f32_16x16x32_bf16 v[32:35], v[6:9], v[16:19], 0\n\t"
    "v_mfma_f32_16x16x32_bf16 v[36:39], v[6:9], v[20:23], 0\n\t"
    "s_waitcnt vmcnt(2)\n\t"
    "v_mfma_f32_16x16x32_bf16 v[32:35], v[10:13], v[56:59], v[32:35]\n\t"
    "v_mfma_f32_16x16x32_bf16 v[36:39], v[10:13], v[60:63], v[36:39]\n\t"
    "v_max3_u32 v40, v6, v7, v8\n\t"
    "v_max3_u32 v41, v9, v10, v11\n\t"
    "v_max3_u32 v40, v40, v12, v13\n\t"
    "v_max_u32 v40, v40, v41\n\t"
    "v_cmp_eq_u32 vcc, -1, v40\n\t"
    "s_cbranch_vccnz 4f\n\t"
    "6:\n\t"
    "s_nop 0\n\t"
    "s_nop 0\n\t"
    "v_add_f32 v32, v32, v38\n\t"
    "v_add_f32 v33, v33, v39\n\t"
    "v_exp_f32 v34, v32\n\t"
    "v_max_f32 v40, v32, v33\n\t"
    "v_exp_f32 v35, v33\n\t"
    "v_cmp_nge_f32 vcc, %[bg], v40\n\t"
    "s_cbranch_vccnz 10f\n\t"
    "11:\n\t"
    "v_cvt_pk_bf16_f32 v50, v34, v35\n\t"
    "17:\n\t"
    "s_waitcnt vmcnt(0) lgkmcnt(0)\n\t"
    "v_max_u32 v40, v44, v46\n\t"
    "v_cmp_eq_u32 vcc, -1, v40\n\t"
    "s_cbranch_vccnz 18f\n\t"
    "19:\n\t"
    "v_mfma_f32_16x16x16_bf16 v[52:55], v[48:49], v[50:51], v[52:55]\n\t"
    "v_mfma_f32_16x16x16_bf16 v[24:27], v[44:45], v[50:51], v[24:27]\n\t"
    "v_mfma_f32_16x16x16_bf16 v[28:31], v[46:47], v[50:51], v[28:31]\n\t"
    "s_nop 5\n\t"
    "v_rcp_f32 v42, v52\n\t"
    "s_nop 0\n\t"
    "v_pk_mul_f32 v[24:25], v[24:25], v[42:43] op_sel_hi:[1,0]\n\t"
    "v_pk_mul_f32 v[26:27], v[26:27], v[42:43] op_sel_hi:[1,0]\n\t"
    "v_pk_mul_f32 v[28:29], v[28:29], v[42:43] op_sel_hi:[1,0]\n\t"
    "v_pk_mul_f32 v[30:31], v[30:31], v[42:43] op_sel_hi:[1,0]\n\t"
    "v_cvt_pk_bf16_f32 v36, v24, v25\n\t"
    "v_cvt_pk_bf16_f32 v37, v26, v27\n\t"
    "v_cvt_pk_bf16_f32 v38, v28, v29\n\t"
    "v_cvt_pk_bf16_f32 v39, v30, v31\n\t"
    "v_min_u32 v36, -2, v36\n\t"
    "v_min_u32 v37, -2, v37\n\t"
    "v_min_u32 v38, -2, v38\n\t"
    "v_min_u32 v39, -2, v39\n\t"
    "global_store_dwordx4 v4, v[36:39], %[od] sc1\n\t"
    "ds_write_b32 v15, v44 offset:19456\n\t"
    "ds_write_b32 v15, v46 offset:19712\n\t"
    "s_setprio 1\n\t"
    "s_waitcnt lgkmcnt(0)\n\t"
    "s_barrier\n\t"
    "s_waitcnt lgkmcnt(0)\n\t"
    "s_branch 9f\n\t"
    "1:\n\t"
    "s_add_u32 %[sp], %[sp], 1\n\t"
    "s_cmp_ge_u32 %[sp], %[cap]\n\t"
    "s_cbranch_scc1 7f\n\t"
    "global_load_dwordx4 v[24:27], v5, %[ag] sc1\n\t"
    "global_load_dwordx4 v[28:31], v5, %[ag] offset:1024 sc1\n\t"
    "s_waitcnt lgkmcnt(0)\n\t"
    "s_memtime %[tc]\n\t"
    "s_nop 7\n\t"
    "s_branch 3b\n\t"
    "7:\n\t"
    "s_mov_b32 %[cap], 0\n\t"
    "s_branch 2b\n\t"
    "4:\n\t"
    "s_add_u32 %[sp], %[sp], 0x10000\n\t"
    "s_lshr_b32 %[st], %[sp], 16\n\t"
    "s_cmp_ge_u32 %[st], %[cap]\n\t"
    "s_cbranch_scc1 8f\n\t"
    "global_load_dwordx4 v[6:9], v5, %[vs] sc1\n\t"
    "global_load_dwordx4 v[10:13], v5, %[vs] offset:1024 sc1\n\t"
    "s_waitcnt vmcnt(0) lgkmcnt(0)\n\t"
    "s_branch 5b\n\t"
    "8:\n\t"
    "s_mov_b32 %[cap], 0\n\t"
    "s_branch 6b\n\t"
    "18:\n\t"
    "s_add_u32 %[sp], %[sp], 0x10000\n\t"
    "s_lshr_b32 %[st], %[sp], 16\n\t"
    "s_cmp_ge_u32 %[st], %[cap]\n\t"
    "s_cbranch_scc1 20f\n\t"
    "v_lshrrev_b32 v46, 2, v5\n\t"
    "global_load_dword v44, v46, %[v2] sc1\n\t"
    "global_load_dword v46, v46, %[v2] offset:256 sc1\n\t"
    "s_waitcnt vmcnt(0) lgkmcnt(0)\n\t"
    "s_branch 17b\n\t"
    "20:\n\t"
    "s_mov_b32 %[cap], 0\n\t"
    "s_branch 19b\n\t"
    "22:\n\t"
    "ds_read_b32 v8, v15 offset:29184\n\t"
    "ds_read_b128 v[32:35], v5 offset:26624\n\t"
    "s_waitcnt lgkmcnt(0)\n\t"
    "s_branch 21b\n\t"
    "10:\n\t"
    "v_max_f32 v40, v40, v14\n\t"
    "v_mov_b32 v42, v40\n\t"
    "s_nop 1\n\t"
    "v_permlane16_swap_b32 v40, v42\n\t"
    "v_max_f32 v40, v40, v42\n\t"
    "v_mov_b32 v42, v40\n\t"
    "s_nop 1\n\t"
    "v_permlane32_swap_b32 v40, v42\n\t"
    "v_max_f32 v40, v40, v42\n\t"
    "v_sub_f32 v34, v32, v40\n\t"
    "v_sub_f32 v35, v33, v40\n\t"
    "v_sub_f32 v40, v14, v40\n\t"
    "v_exp_f32 v40, v40\n\t"
    "v_exp_f32 v34, v34\n\t"
    "v_exp_f32 v35, v35\n\t"
    "v_mul_f32 v24, v24, v40\n\t"
    "v_mul_f32 v25, v25, v40\n\t"
    "v_mul_f32 v26, v26, v40\n\t"
    "v_mul_f32 v27, v27, v40\n\t"
    "v_mul_f32 v28, v28, v40\n\t"
    "v_mul_f32 v29, v29, v40\n\t"
    "v_mul_f32 v30, v30, v40\n\t"
    "v_mul_f32 v31, v31, v40\n\t"
    "v_mul_f32 v52, v52, v40\n\t"
    "v_mul_f32 v53, v53, v40\n\t"
    "v_mul_f32 v54, v54, v40\n\t"
    "v_mul_f32 v55, v55, v40\n\t"
    "s_branch 11b\n\t"
    "12:\n\t"
    "v_max3_f32 v14, v24, v25, v26\n\t"
    "v_max3_f32 v40, v27, v28, v29\n\t"
    "v_max3_f32 v14, v14, v30, v31\n\t"
    "v_max3_f32 v14, v14, v40, %[neg]\n\t"
    "v_mov_b32 v40, v14\n\t"
    "s_nop 1\n\t"
    "v_permlane16_swap_b32 v14, v40\n\t"
    "v_max_f32 v14, v14, v40\n\t"
    "v_mov_b32 v40, v14\n\t"
    "s_nop 1\n\t"
    "v_permlane32_swap_b32 v14, v40\n\t"
    "v_max_f32 v14, v14, v40\n\t"
    "v_sub_f32 v24, v24, v14\n\t"
    "v_sub_f32 v25, v25, v14\n\t"
    "v_sub_f32 v26, v26, v14\n\t"
    "v_sub_f32 v27, v27, v14\n\t"
    "v_sub_f32 v28, v28, v14\n\t"
    "v_sub_f32 v29, v29, v14\n\t"
    "v_sub_f32 v30, v30, v14\n\t"
    "v_sub_f32 v31, v31, v14\n\t"
    "s_mov_b32 %[bg], 0x7fc00000\n\t"
    "s_branch 13b\n\t"
    "9:"
    : [tc] "=&s"(tc), [sp] "=&s"(sp), [cap] "+s"(cap), [st] "=&s"(st), [bg] "=&s"(bg)
    : [ag] "s"(ag), [c44] "s"(0x4444u), [lsc] "s"(LSC), [sn] "s"(sn), [pb] "s"(pb), [vs] "s"(vs), [od] "s"(od), [neg] "s"(-1.0e38f), [one] "s"(0x3F803F80u), [big] "s"(24.0f), [v2] "s"(v2), "{v[64:95]}"(W.w0), "{v[96:127]}"(W.w1), "{v[128:159]}"(W.w2), "{v[160:191]}"(W.w3), "{v[192:223]}"(W.w4), "{v[224:255]}"(W.w5)
    : "v4", "v5", "v6", "v7", "v8", "v9", "v10", "v11", "v12", "v13", "v14", "v15", "v16", "v17", "v18", "v19", "v20", "v21", "v22", "v23", "v24", "v25", "v26", "v27", "v28", "v29", "v30", "v31", "v32", "v33", "v34", "v35", "v36", "v37", "v38", "v39", "v40", "v41", "v42", "v43", "v44", "v45", "v46", "v47", "v48", "v49", "v50", "v51", "v52", "v53", "v54", "v55", "v56", "v57", "v58", "v59", "v60", "v61", "v62", "v63", "vcc", "scc", "memory");
}
template <> __device__ __forceinline__ void qk_step<3>(const QKW& W, const char* ag, const char* vs, const char* v2, const char* od, unsigned pb, unsigned sn, unsigned& cap, unsigned& sp,
    unsigned long long& tc, unsigned long long* ts, unsigned& d0, unsigned& d1) {
  unsigned st, bg;
  asm volatile("v_mbcnt_lo_u32_b32 v60, -1, 0\n\t"
    "v_mbcnt_hi_u32_b32 v60, -1, v60\n\t"
    "v_lshlrev_b32 v5, 4, v60\n\t"
    "s_mov_b32 %[sp], 0\n\t"
    "s_setprio 2\n\t"
    "global_load_dwordx4 v[24:27], v5, %[ag] sc1\n\t"
    "global_load_dwordx4 v[28:31], v5, %[ag] offset:1024 sc1\n\t"
    "s_memtime %[tc]\n\t"
    "v_lshrrev_b32 v32, 4, v60\n\t"
    "v_and_b32 v33, 15, v60\n\t"
    "v_lshrrev_b32 v34, 2, v33\n\t"
    "v_and_b32 v35, 3, v60\n\t"
    "v_and_b32 v61, 1, v60\n\t"
    "v_lshlrev_b32 v61, 3, v61\n\t"
    "v_lshl_or_b32 v7, v34, 4, v61\n\t"
    "v_lshl_or_b32 v7, v32, 7, v7\n\t"
    "v_xor_b32 v10, v32, v33\n\t"
    "v_lshlrev_b32 v10, 4, v10\n\t"
    "v_lshl_or_b32 v10, v33, 8, v10\n\t"
    "v_or_b32 v10, %[pb], v10\n\t"
    "v_xor_b32 v11, 0x40, v10\n\t"
    "v_xor_b32 v12, 0x80, v10\n\t"
    "v_xor_b32 v13, 0xc0, v10\n\t"
    "v_lshl_or_b32 v34, v32, 2, v34\n\t"
    "v_lshrrev_b32 v35, 1, v35\n\t"
    "v_or_b32 v35, 12, v35\n\t"
    "v_xor_b32 v35, v35, v34\n\t"
    "v_lshl_or_b32 v14, v35, 4, v61\n\t"
    "v_lshl_or_b32 v14, v34, 8, v14\n\t"
    "v_or_b32 v14, %[pb], v14\n\t"
    "v_and_b32 v61, 3, v60\n\t"
    "v_lshlrev_b32 v61, 6, v61\n\t"
    "v_lshl_or_b32 v4, v32, 4, v61\n\t"
    "v_and_b32 v61, 4, v60\n\t"
    "v_lshl_or_b32 v4, v61, 10, v4\n\t"
    "v_and_b32 v61, 8, v60\n\t"
    "v_lshl_or_b32 v4, v61, 14, v4\n\t"
    "v_or_b32 v4, 768, v4\n\t"
    "v_and_b32 v61, 2, v60\n\t"
    "v_mul_u32_u24 v61, 0x5555, v61\n\t"
    "v_or_b32 v6, %[c44], v61\n\t"
    "v_lshlrev_b32 v61, 2, v32\n\t"
    "v_lshl_or_b32 v15, v33, 4, v61\n\t"
    "3:\n\t"
    "v_mov_b32 v16, 0\n\t"
    "v_mov_b32 v17, 0\n\t"
    "v_mov_b32 v18, 0\n\t"
    "v_mov_b32 v19, 0\n\t"
    "v_mov_b32 v20, 0\n\t"
    "v_mov_b32 v21, 0\n\t"
    "v_mov_b32 v22, 0\n\t"
    "v_mov_b32 v23, 0\n\t"
    "s_waitcnt vmcnt(0) lgkmcnt(0)\n\t"
    "ds_write_b128 v5, v[24:27] offset:6144\n\t"
    "ds_write_b128 v5, v[28:31] offset:7168\n\t"
    "v_smfmac_f32_16x16x64_bf16 v[16:19], v[24:27], v[160:167], v6\n\t"
    "v_max3_u32 v60, v24, v25, v26\n\t"
    "v_max3_u32 v61, v27, v28, v29\n\t"
    "v_smfmac_f32_16x16x64_bf16 v[20:23], v[24:27], v[168:175], v6\n\t"
    "v_max3_u32 v60, v60, v30, v31\n\t"
    "v_max_u32 v60, v60, v61\n\t"
    "v_cmp_eq_u32 vcc, -1, v60\n\t"
    "s_cbranch_vccnz 1f\n\t"
    "2:\n\t"
    "s_waitcnt lgkmcnt(0)\n\t"
    "s_barrier\n\t"
    "ds_read_b128 v[32:35], v5 offset:0\n\t"
    "ds_read_b128 v[36:39], v5 offset:1024\n\t"
    "v_smfmac_f32_16x16x64_bf16 v[16:19], v[28:31], v[176:183], v6\n\t"
    "ds_read_b128 v[40:43], v5 offset:2048\n\t"
    "ds_read_b128 v[44:47], v5 offset:3072\n\t"
    "v_smfmac_f32_16x16x64_bf16 v[20:23], v[28:31], v[184:191], v6\n\t"
    "s_waitcnt lgkmcnt(3)\n\t"
    "v_smfmac_f32_16x16x64_bf16 v[16:19], v[32:35], v[64:71], v6\n\t"
    "ds_read_b128 v[48:51], v5 offset:4096\n\t"
    "v_smfmac_f32_16x16x64_bf16 v[20:23], v[32:35], v[72:79], v6\n\t"
    "ds_read_b128 v[52:55], v5 offset:5120\n\t"
    "s_waitcnt lgkmcnt(4)\n\t"
    "v_smfmac_f32_16x16x64_bf16 v[16:19], v[36:39], v[80:87], v6\n\t"
    "ds_read_b128 v[24:27], v5 offset:8192\n\t"
    "v_smfmac_f32_16x16x64_bf16 v[20:23], v[36:39], v[88:95], v6\n\t"
    "ds_read_b128 v[28:31], v5 offset:9216\n\t"
    "s_waitcnt lgkmcnt(5)\n\t"
    "v_smfmac_f32_16x16x64_bf16 v[16:19], v[40:43], v[96:103], v6\n\t"
    "ds_read_b128 v[56:59], v5 offset:10240\n\t"
    "v_smfmac_f32_16x16x64_bf16 v[20:23], v[40:43], v[104:111], v6\n\t"
    "ds_read_b128 v[60:63], v5 offset:11264\n\t"
    "ds_read_b128 v[36:39], v11 offset:36864\n\t"
    "s_waitcnt lgkmcnt(7)\n\t"
    "v_smfmac_f32_16x16x64_bf16 v[16:19], v[44:47], v[112:119], v6\n\t"
    "ds_read_b128 v[40:43], v12 offset:32768\n\t"
    "v_smfmac_f32_16x16x64_bf16 v[20:23], v[44:47], v[120:127], v6\n\t"
    "ds_read_b128 v[44:47], v12 offset:36864\n\t"
    "s_waitcnt lgkmcnt(8)\n\t"
    "v_smfmac_f32_16x16x64_bf16 v[16:19], v[48:51], v[128:135], v6\n\t"
    "v_smfmac_f32_16x16x64_bf16 v[20:23], v[48:51], v[136:143], v6\n\t"
    "ds_read_b128 v[48:51], v13 offset:32768\n\t"
    "s_waitcnt lgkmcnt(8)\n\t"
    "v_smfmac_f32_16x16x64_bf16 v[16:19], v[52:55], v[144:151], v6\n\t"
    "v_smfmac_f32_16x16x64_bf16 v[20:23], v[52:55], v[152:159], v6\n\t"
    "ds_read_b128 v[52:55], v13 offset:36864\n\t"
    "s_waitcnt lgkmcnt(8)\n\t"
    "v_smfmac_f32_16x16x64_bf16 v[16:19], v[24:27], v[192:199], v6\n\t"
    "ds_read_b32 v8, v15 offset:29440\n\t"
    "ds_read_b128 v[32:35], v5 offset:27648\n\t"
    "s_setprio 3\n\t"
    "v_smfmac_f32_16x16x64_bf16 v[20:23], v[24:27], v[200:207], v6\n\t"
    "ds_read_b128 v[24:27], v10 offset:32768\n\t"
    "s_waitcnt lgkmcnt(10)\n\t"
    "v_smfmac_f32_16x16x64_bf16 v[16:19], v[28:31], v[208:215], v6\n\t"
    "s_waitcnt lgkmcnt(9)\n\t"
    "v_smfmac_f32_16x16x64_bf16 v[16:19], v[56:59], v[224:231], v6\n\t"
    "s_waitcnt lgkmcnt(8)\n\t"
    "v_smfmac_f32_16x16x64_bf16 v[16:19], v[60:63], v[240:247], v6\n\t"
    "v_smfmac_f32_16x16x64_bf16 v[20:23], v[28:31], v[216:223], v6\n\t"
    "s_waitcnt lgkmcnt(1)\n\t"
    "21:\n\t"
    "v_cmp_ne_u32 vcc, %[sn], v8\n\t"
    "s_cbranch_vccnz 22f\n\t"
    "v_mul_f32 v32, %[lsc], v32\n\t"
    "v_mul_f32 v33, %[lsc], v33\n\t"
    "v_smfmac_f32_16x16x64_bf16 v[20:23], v[56:59], v[232:239], v6\n\t"
    "v_mul_f32 v34, %[lsc], v34\n\t"
    "v_mul_f32 v35, %[lsc], v35\n\t"
    "v_add_f32 v16, v16, v18\n\t"
    "v_add_f32 v17, v17, v19\n\t"
    "v_smfmac_f32_16x16x64_bf16 v[20:23], v[60:63], v[248:255], v6\n\t"
    "v_fma_f32 v16, v16, %[lsc], v32\n\t"
    "v_fma_f32 v17, v17, %[lsc], v33\n\t"
    "v_cvt_pk_bf16_f32 v16, v16, v17\n\t"
    "ds_write_b32 v15, v16 offset:17920\n\t"
    "s_mov_b32 %[bg], %[big]\n\t"
    "s_nop 2\n\t"
    "v_add_f32 v20, v20, v22\n\t"
    "v_add_f32 v21, v21, v23\n\t"
    "v_fma_f32 v20, v20, %[lsc], v34\n\t"
    "v_fma_f32 v21, v21, %[lsc], v35\n\t"
    "v_cvt_pk_bf16_f32 v20, v20, v21\n\t"
    "ds_write_b32 v15, v20 offset:18176\n\t"
    "ds_read_b128 v[28:31], v10 offset:36864\n\t"
    "ds_read_b128 v[32:35], v11 offset:32768\n\t"
    "s_waitcnt lgkmcnt(2)\n\t"
    "s_barrier\n\t"
    "ds_read_b64_tr_b16 v[16:17], v7 offset:16384\n\t"
    "ds_read_b64_tr_b16 v[18:19], v7 offset:16448\n\t"
    "ds_read_b64_tr_b16 v[20:21], v7 offset:16896\n\t"
    "ds_read_b64_tr_b16 v[22:23], v7 offset:16960\n\t"
    "ds_read_b64_tr_b16 v[56:57], v7 offset:17408\n\t"
    "ds_read_b64_tr_b16 v[58:59], v7 offset:17472\n\t"
    "ds_read_b64_tr_b16 v[60:61], v7 offset:17920\n\t"
    "ds_read_b64_tr_b16 v[62:63], v7 offset:17984\n\t"
    "global_load_dwordx4 v[6:9], v5, %[vs] sc1\n\t"
    "global_load_dwordx4 v[10:13], v5, %[vs] offset:1024 sc1\n\t"
    "s_waitcnt lgkmcnt(6)\n\t"
    "v_mfma_f32_16x16x32_bf16 v[24:27], v[24:27], v[16:19], 0\n\t"
    "v_mfma_f32_16x16x32_bf16 v[28:31], v[28:31], v[16:19], 0\n\t"
    "s_waitcnt lgkmcnt(4)\n\t"
    "v_mfma_f32_16x16x32_bf16 v[24:27], v[32:35], v[20:23], v[24:27]\n\t"
    "v_mfma_f32_16x16x32_bf16 v[28:31], v[36:39], v[20:23], v[28:31]\n\t"
    "v_xor_b32 v39, 32, v14\n\t"
    "ds_read_b64_tr_b16 v[32:33], v14 offset:40960\n\t"
    "ds_read_b64_tr_b16 v[34:35], v14 offset:45056\n\t"
    "ds_read_b64_tr_b16 v[36:37], v39 offset:40960\n\t"
    "ds_read_b64_tr_b16 v[38:39], v39 offset:45056\n\t"
    "s_waitcnt lgkmcnt(6)\n\t"
    "v_mfma_f32_16x16x32_bf16 v[24:27], v[40:43], v[56:59], v[24:27]\n\t"
    "v_mfma_f32_16x16x32_bf16 v[28:31], v[44:47], v[56:59], v[28:31]\n\t"
    "s_waitcnt lgkmcnt(4)\n\t"
    "v_mfma_f32_16x16x32_bf16 v[24:27], v[48:51], v[60:63], v[24:27]\n\t"
    "v_mfma_f32_16x16x32_bf16 v[28:31], v[52:55], v[60:63], v[28:31]\n\t"
    "v_mov_b32 v52, %[one]\n\t"
    "v_mov_b32 v53, %[one]\n\t"
    "v_mov_b32 v54, %[one]\n\t"
    "v_mov_b32 v55, %[one]\n\t"
    "v_mov_b32 v45, 0\n\t"
    "v_mov_b32 v47, 0\n\t"
    "v_mov_b32 v48, %[one]\n\t"
    "v_max3_f32 v14, |v24|, |v25|, |v26|\n\t"
    "v_max3_f32 v40, |v27|, |v28|, |v29|\n\t"
    "v_max3_f32 v14, v14, |v30|, |v31|\n\t"
    "v_max_f32 v14, v14, v40\n\t"
    "v_cmp_nge_f32 vcc, %[big], v14\n\t"
    "v_mov_b32 v14, 0\n\t"
    "s_cbranch_vccnz 12f\n\t"
    "13:\n\t"
    "v_lshrrev_b32 v46, 2, v5\n\t"
    "global_load_dword v44, v46, %[v2] sc1\n\t"
    "global_load_dword v46, v46, %[v2] offset:256 sc1\n\t"
    "v_exp_f32 v24, v24\n\t"
    "v_exp_f32 v25, v25\n\t"
    "v_exp_f32 v26, v26\n\t"
    "v_exp_f32 v27, v27\n\t"
    "v_exp_f32 v28, v28\n\t"
    "v_exp_f32 v29, v29\n\t"
    "v_exp_f32 v30, v30\n\t"
    "v_exp_f32 v31, v31\n\t"
    "v_cvt_pk_bf16_f32 v40, v24, v25\n\t"
    "v_cvt_pk_bf16_f32 v41, v26, v27\n\t"
    "v_cvt_pk_bf16_f32 v42, v28, v29\n\t"
    "v_cvt_pk_bf16_f32 v43, v30, v31\n\t"
    "v_mov_b32 v49, 0\n\t"
    "v_mov_b32 v51, 0\n\t"
    "s_waitcnt lgkmcnt(0)\n\t"
    "v_mfma_f32_16x16x32_bf16 v[52:55], v[52:55], v[40:43], 0\n\t"
    "v_mfma_f32_16x16x32_bf16 v[24:27], v[32:35], v[40:43], 0\n\t"
    "v_mfma_f32_16x16x32_bf16 v[28:31], v[36:39], v[40:43], 0\n\t"
    "s_waitcnt vmcnt(3)\n\t"
    "5:\n\t"
    "v_mfma_f32_16x16x32_bf16 v[32:35], v[6:9], v[16:19], 0\n\t"
    "v_mfma_f32_16x16x32_bf16 v[36:39], v[6:9], v[20:23], 0\n\t"
    "s_waitcnt vmcnt(2)\n\t"
    "v_mfma_f32_16x16x32_bf16 v[32:35], v[10:13], v[56:59], v[32:35]\n\t"
    "v_mfma_f32_16x16x32_bf16 v[36:39], v[10:13], v[60:63], v[36:39]\n\t"
    "v_max3_u32 v40, v6, v7, v8\n\t"
    "v_max3_u32 v41, v9, v10, v11\n\t"
    "v_max3_u32 v40, v40, v12, v13\n\t"
    "v_max_u32 v40, v40, v41\n\t"
    "v_cmp_eq_u32 vcc, -1, v40\n\t"
    "s_cbranch_vccnz 4f\n\t"
    "6:\n\t"
    "s_nop 0\n\t"
    "s_nop 0\n\t"
    "v_add_f32 v32, v32, v38\n\t"
    "v_add_f32 v33, v33, v39\n\t"
    "v_exp_f32 v34, v32\n\t"
    "v_max_f32 v40, v32, v33\n\t"
    "v_exp_f32 v35, v33\n\t"
    "v_cmp_nge_f32 vcc, %[bg], v40\n\t"
    "s_cbranch_vccnz 10f\n\t"
    "11:\n\t"
    "v_cvt_pk_bf16_f32 v50, v34, v35\n\t"
    "17:\n\t"
    "s_waitcnt vmcnt(0) lgkmcnt(0)\n\t"
    "v_max_u32 v40, v44, v46\n\t"
    "v_cmp_eq_u32 vcc, -1, v40\n\t"
    "s_cbranch_vccnz 18f\n\t"
    "19:\n\t"
    "v_mfma_f32_16x16x16_bf16 v[52:55], v[48:49], v[50:51], v[52:55]\n\t"
    "v_mfma_f32_16x16x16_bf16 v[24:27], v[44:45], v[50:51], v[24:27]\n\t"
    "v_mfma_f32_16x16x16_bf16 v[28:31], v[46:47], v[50:51], v[28:31]\n\t"
    "s_nop 5\n\t"
    "v_rcp_f32 v42, v52\n\t"
    "s_nop 0\n\t"
    "v_pk_mul_f32 v[24:25], v[24:25], v[42:43] op_sel_hi:[1,0]\n\t"
    "v_pk_mul_f32 v[26:27], v[26:27], v[42:43] op_sel_hi:[1,0]\n\t"
    "v_pk_mul_f32 v[28:29], v[28:29], v[42:43] op_sel_hi:[1,0]\n\t"
    "v_pk_mul_f32 v[30:31], v[30:31], v[42:43] op_sel_hi:[1,0]\n\t"
    "v_cvt_pk_bf16_f32 v36, v24, v25\n\t"
    "v_cvt_pk_bf16_f32 v37, v26, v27\n\t"
    "v_cvt_pk_bf16_f32 v38, v28, v29\n\t"
    "v_cvt_pk_bf16_f32 v39, v30, v31\n\t"
    "v_min_u32 v36, -2, v36\n\t"
    "v_min_u32 v37, -2, v37\n\t"
    "v_min_u32 v38, -2, v38\n\t"
    "v_min_u32 v39, -2, v39\n\t"
    "global_store_dwordx4 v4, v[36:39], %[od] sc1\n\t"
    "ds_write_b32 v15, v44 offset:19968\n\t"
    "ds_write_b32 v15, v46 offset:20224\n\t"
    "s_setprio 1\n\t"
    "s_waitcnt lgkmcnt(0)\n\t"
    "s_barrier\n\t"
    "s_waitcnt lgkmcnt(0)\n\t"
    "s_branch 9f\n\t"
    "1:\n\t"
    "s_add_u32 %[sp], %[sp], 1\n\t"
    "s_cmp_ge_u32 %[sp], %[cap]\n\t"
    "s_cbranch_scc1 7f\n\t"
    "global_load_dwordx4 v[24:27], v5, %[ag] sc1\n\t"
    "global_load_dwordx4 v[28:31], v5, %[ag] offset:1024 sc1\n\t"
    "s_waitcnt lgkmcnt(0)\n\t"
    "s_memtime %[tc]\n\t"
    "s_nop 7\n\t"
    "s_branch 3b\n\t"
    "7:\n\t"
    "s_mov_b32 %[cap], 0\n\t"
    "s_branch 2b\n\t"
    "4:\n\t"
    "s_add_u32 %[sp], %[sp], 0x10000\n\t"
    "s_lshr_b32 %[st], %[sp], 16\n\t"
    "s_cmp_ge_u32 %[st], %[cap]\n\t"
    "s_cbranch_scc1 8f\n\t"
    "global_load_dwordx4 v[6:9], v5, %[vs] sc1\n\t"
    "global_load_dwordx4 v[10:13], v5, %[vs] offset:1024 sc1\n\t"
    "s_waitcnt vmcnt(0) lgkmcnt(0)\n\t"
    "s_branch 5b\n\t"
    "8:\n\t"
    "s_mov_b32 %[cap], 0\n\t"
    "s_branch 6b\n\t"
    "18:\n\t"
    "s_add_u32 %[sp], %[sp], 0x10000\n\t"
    "s_lshr_b32 %[st], %[sp], 16\n\t"
    "s_cmp_ge_u32 %[st], %[cap]\n\t"
    "s_cbranch_scc1 20f\n\t"
    "v_lshrrev_b32 v46, 2, v5\n\t"
    "global_load_dword v44, v46, %[v2] sc1\n\t"
    "global_load_dword v46, v46, %[v2] offset:256 sc1\n\t"
    "s_waitcnt vmcnt(0) lgkmcnt(0)\n\t"
    "s_branch 17b\n\t"
    "20:\n\t"
    "s_mov_b32 %[cap], 0\n\t"
    "s_branch 19b\n\t"
    "22:\n\t"
    "ds_read_b32 v8, v15 offset:29440\n\t"
    "ds_read_b128 v[32:35], v5 offset:27648\n\t"
    "s_waitcnt lgkmcnt(0)\n\t"
    "s_branch 21b\n\t"
    "10:\n\t"
    "v_max_f32 v40, v40, v14\n\t"
    "v_mov_b32 v42, v40\n\t"
    "s_nop 1\n\t"
    "v_permlane16_swap_b32 v40, v42\n\t"
    "v_max_f32 v40, v40, v42\n\t"
    "v_mov_b32 v42, v40\n\t"
    "s_nop 1\n\t"
    "v_permlane32_swap_b32 v40, v42\n\t"
    "v_max_f32 v40, v40, v42\n\t"
    "v_sub_f32 v34, v32, v40\n\t"
    "v_sub_f32 v35, v33, v40\n\t"
    "v_sub_f32 v40, v14, v40\n\t"
    "v_exp_f32 v40, v40\n\t"
    "v_exp_f32 v34, v34\n\t"
    "v_exp_f32 v35, v35\n\t"
    "v_mul_f32 v24, v24, v40\n\t"
    "v_mul_f32 v25, v25, v40\n\t"
    "v_mul_f32 v26, v26, v40\n\t"
    "v_mul_f32 v27, v27, v40\n\t"
    "v_mul_f32 v28, v28, v40\n\t"
    "v_mul_f32 v29, v29, v40\n\t"
    "v_mul_f32 v30, v30, v40\n\t"
    "v_mul_f32 v31, v31, v40\n\t"
    "v_mul_f32 v52, v52, v40\n\t"
    "v_mul_f32 v53, v53, v40\n\t"
    "v_mul_f32 v54, v54, v40\n\t"
    "v_mul_f32 v55, v55, v40\n\t"
    "s_branch 11b\n\t"
    "12:\n\t"
    "v_max3_f32 v14, v24, v25, v26\n\t"
    "v_max3_f32 v40, v27, v28, v29\n\t"
    "v_max3_f32 v14, v14, v30, v31\n\t"
    "v_max3_f32 v14, v14, v40, %[neg]\n\t"
    "v_mov_b32 v40, v14\n\t"
    "s_nop 1\n\t"
    "v_permlane16_swap_b32 v14, v40\n\t"
    "v_max_f32 v14, v14, v40\n\t"
    "v_mov_b32 v40, v14\n\t"
    "s_nop 1\n\t"
    "v_permlane32_swap_b32 v14, v40\n\t"
    "v_max_f32 v14, v14, v40\n\t"
    "v_sub_f32 v24, v24, v14\n\t"
    "v_sub_f32 v25, v25, v14\n\t"
    "v_sub_f32 v26, v26, v14\n\t"
    "v_sub_f32 v27, v27, v14\n\t"
    "v_sub_f32 v28, v28, v14\n\t"
    "v_sub_f32 v29, v29, v14\n\t"
    "v_sub_f32 v30, v30, v14\n\t"
    "v_sub_f32 v31, v31, v14\n\t"
    "s_mov_b32 %[bg], 0x7fc00000\n\t"
    "s_branch 13b\n\t"
    "9:"
    : [tc] "=&s"(tc), [sp] "=&s"(sp), [cap] "+s"(cap), [st] "=&s"(st), [bg] "=&s"(bg)
    : [ag] "s"(ag), [c44] "s"(0x4444u), [lsc] "s"(LSC), [sn] "s"(sn), [pb] "s"(pb), [vs] "s"(vs), [od] "s"(od), [neg] "s"(-1.0e38f), [one] "s"(0x3F803F80u), [big] "s"(24.0f), [v2] "s"(v2), "{v[64:95]}"(W.w0), "{v[96:127]}"(W.w1), "{v[128:159]}"(W.w2), "{v[160:191]}"(W.w3), "{v[192:223]}"(W.w4), "{v[224:255]}"(W.w5)
    : "v4", "v5", "v6", "v7", "v8", "v9", "v10", "v11", "v12", "v13", "v14", "v15", "v16", "v17", "v18", "v19", "v20", "v21", "v22", "v23", "v24", "v25", "v26", "v27", "v28", "v29", "v30", "v31", "v32", "v33", "v34", "v35", "v36", "v37", "v38", "v39", "v40", "v41", "v42", "v43", "v44", "v45", "v46", "v47", "v48", "v49", "v50", "v51", "v52", "v53", "v54", "v55", "v56", "v57", "v58", "v59", "v60", "v61", "v62", "v63", "vcc", "scc", "memory");
}
template <> __device__ __forceinline__ void qk_step<4>(const QKW& W, const char* ag, const char* vs, const char* v2, const char* od, unsigned pb, unsigned sn, unsigned& cap, unsigned& sp,
    unsigned long long& tc, unsigned long long* ts, unsigned& d0, unsigned& d1) {
  unsigned st, bg;
  asm volatile("v_mbcnt_lo_u32_b32 v60, -1, 0\n\t"
    "v_mbcnt_hi_u32_b32 v60, -1, v60\n\t"
    "v_lshlrev_b32 v6, 4, v60\n\t"
    "s_mov_b32 %[sp], 0\n\t"
    "s_setprio 3\n\t"
    "global_load_dwordx4 v[24:27], v6, %[ag] sc1\n\t"
    "global_load_dwordx4 v[28:31], v6, %[ag] offset:-4096 sc1\n\t"
    "s_memtime %[tc]\n\t"
    "v_lshrrev_b32 v56, 4, v60\n\t"
    "v_and_b32 v57, 15, v60\n\t"
    "v_mov_b32 v20, 0\n\t"
    "v_mov_b32 v21, 0\n\t"
    "v_mov_b32 v22, 0\n\t"
    "v_mov_b32 v23, 0\n\t"
    "v_mov_b32 v11, %[sn]\n\t"
    "v_mov_b32 v10, -1\n\t"
    "v_and_b32 v61, 2, v60\n\t"
    "v_mul_u32_u24 v61, 0x5555, v61\n\t"
    "v_or_b32 v8, %[c44], v61\n\t"
    "v_lshlrev_b32 v61, 2, v56\n\t"
    "v_lshl_or_b32 v9, v57, 4, v61\n\t"
    "3:\n\t"
    "v_mov_b32 v12, 0\n\t"
    "v_mov_b32 v13, 0\n\t"
    "v_mov_b32 v14, 0\n\t"
    "v_mov_b32 v15, 0\n\t"
    "v_mov_b32 v16, 0\n\t"
    "v_mov_b32 v17, 0\n\t"
    "v_mov_b32 v18, 0\n\t"
    "v_mov_b32 v19, 0\n\t"
    "s_waitcnt vmcnt(0) lgkmcnt(0)\n\t"
    "ds_write_b128 v6, v[24:27] offset:12288\n\t"
    "ds_write_b128 v6, v[28:31] offset:8192\n\t"
    "v_smfmac_f32_16x16x64_bf16 v[12:15], v[24:27], v[64:71], v8\n\t"
    "v_max3_u32 v60, v24, v25, v26\n\t"
    "v_max3_u32 v61, v27, v28, v29\n\t"
    "v_smfmac_f32_16x16x64_bf16 v[16:19], v[24:27], v[72:79], v8\n\t"
    "v_max3_u32 v60, v60, v30, v31\n\t"
    "v_max_u32 v60, v60, v61\n\t"
    "v_cmp_eq_u32 vcc, -1, v60\n\t"
    "s_cbranch_vccnz 1f\n\t"
    "2:\n\t"
    "s_waitcnt lgkmcnt(0)\n\t"
    "s_barrier\n\t"
    "ds_read_b128 v[32:35], v6 offset:13312\n\t"
    "ds_read_b128 v[36:39], v6 offset:14336\n\t"
    "ds_read_b128 v[40:43], v6 offset:15360\n\t"
    "ds_write_b32 v9, v10 offset:18432\n\t"
    "s_waitcnt lgkmcnt(3)\n\t"
    "v_smfmac_f32_16x16x64_bf16 v[12:15], v[32:35], v[80:87], v8\n\t"
    "v_smfmac_f32_16x16x64_bf16 v[16:19], v[32:35], v[88:95], v8\n\t"
    "s_waitcnt lgkmcnt(2)\n\t"
    "v_smfmac_f32_16x16x64_bf16 v[12:15], v[36:39], v[96:103], v8\n\t"
    "v_smfmac_f32_16x16x64_bf16 v[16:19], v[36:39], v[104:111], v8\n\t"
    "s_waitcnt lgkmcnt(1)\n\t"
    "v_smfmac_f32_16x16x64_bf16 v[12:15], v[40:43], v[112:119], v8\n\t"
    "v_smfmac_f32_16x16x64_bf16 v[16:19], v[40:43], v[120:127], v8\n\t"
    "s_nop 6\n\t"
    "v_add_f32 v12, v12, v14\n\t"
    "v_add_f32 v13, v13, v15\n\t"
    "v_add_f32 v14, v16, v18\n\t"
    "v_add_f32 v15, v17, v19\n\t"
    "ds_write_b128 v6, v[12:15] offset:24576\n\t"
    "ds_write_b32 v9, v11 offset:28672\n\t"
    "s_setprio 0\n\t"
    "s_memrealtime s[36:37]\n\t"
    "s_waitcnt lgkmcnt(0)\n\t"
    "s_mov_b32 s38, s36\n\t"
    "s_min_u32 s37, %[d0], %[d1]\n\t"
    "s_add_u32 s36, s36, s37\n\t"
    "s_and_b32 s36, s36, 0xffffff\n\t"
    "s_or_b32 s36, s36, %[sn]\n\t"
    "v_lshlrev_b32 v10, 10, v6\n\t"
    "v_mov_b32 v5, s36\n\t"
    "s_mov_b64 exec, 15\n\t"
    "global_store_dword v10, v5, %[vs] offset:-4096 sc1\n\t"
    "s_mov_b64 exec, -1\n\t"
    "ds_read_b128 v[44:47], v6 offset:0\n\t"
    "ds_read_b128 v[48:51], v6 offset:1024\n\t"
    "ds_read_b128 v[52:55], v6 offset:2048\n\t"
    "ds_read_b128 v[56:59], v6 offset:3072\n\t"
    "ds_read_b128 v[60:63], v6 offset:4096\n\t"
    "s_sleep 2\n\t"
    "v_smfmac_f32_16x16x64_bf16 v[20:23], v[28:31], v[192:199], v8\n\t"
    "ds_read_b128 v[28:31], v6 offset:5120\n\t"
    "v_smfmac_f32_16x16x64_bf16 v[20:23], v[24:27], v[224:231], v8\n\t"
    "ds_read_b128 v[24:27], v6 offset:6144\n\t"
    "v_smfmac_f32_16x16x64_bf16 v[20:23], v[32:35], v[240:247], v8\n\t"
    "ds_read_b128 v[32:35], v6 offset:7168\n\t"
    "v_smfmac_f32_16x16x64_bf16 v[20:23], v[36:39], v[232:239], v8\n\t"
    "ds_read_b128 v[36:39], v6 offset:9216\n\t"
    "v_smfmac_f32_16x16x64_bf16 v[20:23], v[40:43], v[248:255], v8\n\t"
    "ds_read_b128 v[40:43], v6 offset:10240\n\t"
    "s_waitcnt lgkmcnt(9)\n\t"
    "v_smfmac_f32_16x16x64_bf16 v[20:23], v[44:47], v[128:135], v8\n\t"
    "ds_read_b128 v[44:47], v6 offset:11264\n\t"
    "s_barrier\n\t"
    "s_setprio 0\n\t"
    "s_waitcnt lgkmcnt(9)\n\t"
    "v_smfmac_f32_16x16x64_bf16 v[20:23], v[48:51], v[144:151], v8\n\t"
    "s_waitcnt lgkmcnt(8)\n\t"
    "v_smfmac_f32_16x16x64_bf16 v[20:23], v[52:55], v[136:143], v8\n\t"
    "s_waitcnt lgkmcnt(7)\n\t"
    "v_smfmac_f32_16x16x64_bf16 v[20:23], v[56:59], v[152:159], v8\n\t"
    "s_waitcnt lgkmcnt(6)\n\t"
    "v_smfmac_f32_16x16x64_bf16 v[20:23], v[60:63], v[160:167], v8\n\t"
    "s_waitcnt lgkmcnt(5)\n\t"
    "v_smfmac_f32_16x16x64_bf16 v[20:23], v[28:31], v[176:183], v8\n\t"
    "s_waitcnt lgkmcnt(4)\n\t"
    "v_smfmac_f32_16x16x64_bf16 v[20:23], v[24:27], v[168:175], v8\n\t"
    "s_waitcnt lgkmcnt(3)\n\t"
    "v_smfmac_f32_16x16x64_bf16 v[20:23], v[32:35], v[184:191], v8\n\t"
    "s_waitcnt lgkmcnt(2)\n\t"
    "v_smfmac_f32_16x16x64_bf16 v[20:23], v[36:39], v[208:215], v8\n\t"
    "s_waitcnt lgkmcnt(1)\n\t"
    "v_smfmac_f32_16x16x64_bf16 v[20:23], v[40:43], v[200:207], v8\n\t"
    "s_waitcnt lgkmcnt(0)\n\t"
    "v_smfmac_f32_16x16x64_bf16 v[20:23], v[44:47], v[216:223], v8\n\t"
    "s_nop 7\n\t"
    "v_add_f32 v20, v20, v22\n\t"
    "v_add_f32 v21, v21, v23\n\t"
    "v_cvt_pk_bf16_f32 v20, v20, v21\n\t"
    "v_min_u32 v20, -2, v20\n\t"
    "ds_write_b32 v9, v20 offset:18432\n\t"
    "s_waitcnt lgkmcnt(0)\n\t"
    "s_setprio 3\n\t"
    "s_barrier\n\t"
    "s_memrealtime s[36:37]\n\t"
    "s_mov_b32 %[d1], %[d0]\n\t"
    "s_waitcnt lgkmcnt(0)\n\t"
    "s_sub_u32 %[d0], s36, s38\n\t"
    "s_min_u32 %[d0], %[d0], 200\n\t"
    "s_and_b32 s36, s36, 0xffffff\n\t"
    "s_or_b32 s36, s36, %[sn]\n\t"
    "v_lshlrev_b32 v10, 10, v6\n\t"
    "v_mov_b32 v5, s36\n\t"
    "s_mov_b64 exec, 15\n\t"
    "global_store_dword v10, v5, %[vs] offset:0 sc1\n\t"
    "s_mov_b64 exec, -1\n\t"
    "s_waitcnt lgkmcnt(0)\n\t"
    "s_branch 9f\n\t"
    "1:\n\t"
    "s_add_u32 %[sp], %[sp], 1\n\t"
    "s_cmp_ge_u32 %[sp], %[cap]\n\t"
    "s_cbranch_scc1 7f\n\t"
    "global_load_dwordx4 v[24:27], v6, %[ag] sc1\n\t"
    "global_load_dwordx4 v[28:31], v6, %[ag] offset:-4096 sc1\n\t"
    "s_waitcnt lgkmcnt(0)\n\t"
    "s_memtime %[tc]\n\t"
    "s_nop 7\n\t"
    "s_branch 3b\n\t"
    "7:\n\t"
    "s_mov_b32 %[cap], 0\n\t"
    "s_branch 2b\n\t"
    "9:"
    : [tc] "=&s"(tc), [sp] "=&s"(sp), [cap] "+s"(cap), [d0] "+s"(d0), [d1] "+s"(d1)
    : [ag] "s"(ag), [c44] "s"(0x4444u), [lsc] "s"(LSC), [sn] "s"(sn), [vs] "s"(vs), "{v[64:95]}"(W.w0), "{v[96:127]}"(W.w1), "{v[128:159]}"(W.w2), "{v[160:191]}"(W.w3), "{v[192:223]}"(W.w4), "{v[224:255]}"(W.w5)
    : "v5", "v6", "v7", "v8", "v9", "v10", "v11", "v12", "v13", "v14", "v15", "v16", "v17", "v18", "v19", "v20", "v21", "v22", "v23", "v24", "v25", "v26", "v27", "v28", "v29", "v30", "v31", "v32", "v33", "v34", "v35", "v36", "v37", "v38", "v39", "v40", "v41", "v42", "v43", "v44", "v45", "v46", "v47", "v48", "v49", "v50", "v51", "v52", "v53", "v54", "v55", "v56", "v57", "v58", "v59", "v60", "v61", "v62", "v63", "vcc", "scc", "memory", "s36", "s37", "s38");
}
template <> __device__ __forceinline__ void qk_step<5>(const QKW& W, const char* ag, const char* vs, const char* v2, const char* od, unsigned pb, unsigned sn, unsigned& cap, unsigned& sp,
    unsigned long long& tc, unsigned long long* ts, unsigned& d0, unsigned& d1) {
  unsigned st, bg;
  asm volatile("v_mbcnt_lo_u32_b32 v60, -1, 0\n\t"
    "v_mbcnt_hi_u32_b32 v60, -1, v60\n\t"
    "v_lshlrev_b32 v6, 4, v60\n\t"
    "s_mov_b32 %[sp], 0\n\t"
    "s_setprio 3\n\t"
    "global_load_dwordx4 v[24:27], v6, %[ag] sc1\n\t"
    "global_load_dwordx4 v[28:31], v6, %[ag] offset:-4096 sc1\n\t"
    "s_memtime %[tc]\n\t"
    "v_lshrrev_b32 v56, 4, v60\n\t"
    "v_and_b32 v57, 15, v60\n\t"
    "v_mov_b32 v20, 0\n\t"
    "v_mov_b32 v21, 0\n\t"
    "v_mov_b32 v22, 0\n\t"
    "v_mov_b32 v23, 0\n\t"
    "v_mov_b32 v11, %[sn]\n\t"
    "v_mov_b32 v10, -1\n\t"
    "v_and_b32 v61, 2, v60\n\t"
    "v_mul_u32_u24 v61, 0x5555, v61\n\t"
    "v_or_b32 v8, %[c44], v61\n\t"
    "v_lshlrev_b32 v61, 2, v56\n\t"
    "v_lshl_or_b32 v9, v57, 4, v61\n\t"
    "3:\n\t"
    "v_mov_b32 v12, 0\n\t"
    "v_mov_b32 v13, 0\n\t"
    "v_mov_b32 v14, 0\n\t"
    "v_mov_b32 v15, 0\n\t"
    "v_mov_b32 v16, 0\n\t"
    "v_mov_b32 v17, 0\n\t"
    "v_mov_b32 v18, 0\n\t"
    "v_mov_b32 v19, 0\n\t"
    "s_waitcnt vmcnt(0) lgkmcnt(0)\n\t"
    "ds_write_b128 v6, v[24:27] offset:13312\n\t"
    "ds_write_b128 v6, v[28:31] offset:9216\n\t"
    "v_smfmac_f32_16x16x64_bf16 v[12:15], v[24:27], v[80:87], v8\n\t"
    "v_max3_u32 v60, v24, v25, v26\n\t"
    "v_max3_u32 v61, v27, v28, v29\n\t"
    "v_smfmac_f32_16x16x64_bf16 v[16:19], v[24:27], v[88:95], v8\n\t"
    "v_max3_u32 v60, v60, v30, v31\n\t"
    "v_max_u32 v60, v60, v61\n\t"
    "v_cmp_eq_u32 vcc, -1, v60\n\t"
    "s_cbranch_vccnz 1f\n\t"
    "2:\n\t"
    "s_waitcnt lgkmcnt(0)\n\t"
    "s_barrier\n\t"
    "ds_read_b128 v[32:35], v6 offset:12288\n\t"
    "ds_read_b128 v[36:39], v6 offset:14336\n\t"
    "ds_read_b128 v[40:43], v6 offset:15360\n\t"
    "ds_write_b32 v9, v10 offset:18688\n\t"
    "s_waitcnt lgkmcnt(3)\n\t"
    "v_smfmac_f32_16x16x64_bf16 v[12:15], v[32:35], v[64:71], v8\n\t"
    "v_smfmac_f32_16x16x64_bf16 v[16:19], v[32:35], v[72:79], v8\n\t"
    "s_waitcnt lgkmcnt(2)\n\t"
    "v_smfmac_f32_16x16x64_bf16 v[12:15], v[36:39], v[96:103], v8\n\t"
    "v_smfmac_f32_16x16x64_bf16 v[16:19], v[36:39], v[104:111], v8\n\t"
    "s_waitcnt lgkmcnt(1)\n\t"
    "v_smfmac_f32_16x16x64_bf16 v[12:15], v[40:43], v[112:119], v8\n\t"
    "v_smfmac_f32_16x16x64_bf16 v[16:19], v[40:43], v[120:127], v8\n\t"
    "s_nop 6\n\t"
    "v_add_f32 v12, v12, v14\n\t"
    "v_add_f32 v13, v13, v15\n\t"
    "v_add_f32 v14, v16, v18\n\t"
    "v_add_f32 v15, v17, v19\n\t"
    "ds_write_b128 v6, v[12:15] offset:25600\n\t"
    "ds_write_b32 v9, v11 offset:28928\n\t"
    "s_setprio 0\n\t"
    "ds_read_b128 v[44:47], v6 offset:0\n\t"
    "ds_read_b128 v[48:51], v6 offset:1024\n\t"
    "ds_read_b128 v[52:55], v6 offset:2048\n\t"
    "ds_read_b128 v[56:59], v6 offset:3072\n\t"
    "ds_read_b128 v[60:63], v6 offset:4096\n\t"
    "s_sleep 2\n\t"
    "v_smfmac_f32_16x16x64_bf16 v[20:23], v[28:31], v[208:215], v8\n\t"
    "ds_read_b128 v[28:31], v6 offset:5120\n\t"
    "v_smfmac_f32_16x16x64_bf16 v[20:23], v[24:27], v[240:247], v8\n\t"
    "ds_read_b128 v[24:27], v6 offset:6144\n\t"
    "v_smfmac_f32_16x16x64_bf16 v[20:23], v[32:35], v[224:231], v8\n\t"
    "ds_read_b128 v[32:35], v6 offset:7168\n\t"
    "v_smfmac_f32_16x16x64_bf16 v[20:23], v[36:39], v[232:239], v8\n\t"
    "ds_read_b128 v[36:39], v6 offset:8192\n\t"
    "v_smfmac_f32_16x16x64_bf16 v[20:23], v[40:43], v[248:255], v8\n\t"
    "ds_read_b128 v[40:43], v6 offset:10240\n\t"
    "s_waitcnt lgkmcnt(9)\n\t"
    "v_smfmac_f32_16x16x64_bf16 v[20:23], v[44:47], v[128:135], v8\n\t"
    "ds_read_b128 v[44:47], v6 offset:11264\n\t"
    "s_barrier\n\t"
    "s_setprio 0\n\t"
    "s_waitcnt lgkmcnt(9)\n\t"
    "v_smfmac_f32_16x16x64_bf16 v[20:23], v[48:51], v[144:151], v8\n\t"
    "s_waitcnt lgkmcnt(8)\n\t"
    "v_smfmac_f32_16x16x64_bf16 v[20:23], v[52:55], v[136:143], v8\n\t"
    "s_waitcnt lgkmcnt(7)\n\t"
    "v_smfmac_f32_16x16x64_bf16 v[20:23], v[56:59], v[152:159], v8\n\t"
    "s_waitcnt lgkmcnt(6)\n\t"
    "v_smfmac_f32_16x16x64_bf16 v[20:23], v[60:63], v[160:167], v8\n\t"
    "s_waitcnt lgkmcnt(5)\n\t"
    "v_smfmac_f32_16x16x64_bf16 v[20:23], v[28:31], v[176:183], v8\n\t"
    "s_waitcnt lgkmcnt(4)\n\t"
    "v_smfmac_f32_16x16x64_bf16 v[20:23], v[24:27], v[168:175], v8\n\t"
    "s_waitcnt lgkmcnt(3)\n\t"
    "v_smfmac_f32_16x16x64_bf16 v[20:23], v[32:35], v[184:191], v8\n\t"
    "s_waitcnt lgkmcnt(2)\n\t"
    "v_smfmac_f32_16x16x64_bf16 v[20:23], v[36:39], v[192:199], v8\n\t"
    "s_waitcnt lgkmcnt(1)\n\t"
    "v_smfmac_f32_16x16x64_bf16 v[20:23], v[40:43], v[200:207], v8\n\t"
    "s_waitcnt lgkmcnt(0)\n\t"
    "v_smfmac_f32_16x16x64_bf16 v[20:23], v[44:47], v[216:223], v8\n\t"
    "s_nop 7\n\t"
    "v_add_f32 v20, v20, v22\n\t"
    "v_add_f32 v21, v21, v23\n\t"
    "v_cvt_pk_bf16_f32 v20, v20, v21\n\t"
    "v_min_u32 v20, -2, v20\n\t"
    "ds_write_b32 v9, v20 offset:18688\n\t"
    "s_waitcnt lgkmcnt(0)\n\t"
    "s_setprio 0\n\t"
    "s_waitcnt lgkmcnt(0)\n\t"
    "s_branch 9f\n\t"
    "1:\n\t"
    "s_add_u32 %[sp], %[sp], 1\n\t"
    "s_cmp_ge_u32 %[sp], %[cap]\n\t"
    "s_cbranch_scc1 7f\n\t"
    "global_load_dwordx4 v[24:27], v6, %[ag] sc1\n\t"
    "global_load_dwordx4 v[28:31], v6, %[ag] offset:-4096 sc1\n\t"
    "s_waitcnt lgkmcnt(0)\n\t"
    "s_memtime %[tc]\n\t"
    "s_nop 7\n\t"
    "s_branch 3b\n\t"
    "7:\n\t"
    "s_mov_b32 %[cap], 0\n\t"
    "s_branch 2b\n\t"
    "9:"
    : [tc] "=&s"(tc), [sp] "=&s"(sp), [cap] "+s"(cap)
    : [ag] "s"(ag), [c44] "s"(0x4444u), [lsc] "s"(LSC), [sn] "s"(sn), "{v[64:95]}"(W.w0), "{v[96:127]}"(W.w1), "{v[128:159]}"(W.w2), "{v[160:191]}"(W.w3), "{v[192:223]}"(W.w4), "{v[224:255]}"(W.w5)
    : "v6", "v7", "v8", "v9", "v10", "v11", "v12", "v13", "v14", "v15", "v16", "v17", "v18", "v19", "v20", "v21", "v22", "v23", "v24", "v25", "v26", "v27", "v28", "v29", "v30", "v31", "v32", "v33", "v34", "v35", "v36", "v37", "v38", "v39", "v40", "v41", "v42", "v43", "v44", "v45", "v46", "v47", "v48", "v49", "v50", "v51", "v52", "v53", "v54", "v55", "v56", "v57", "v58", "v59", "v60", "v61", "v62", "v63", "vcc", "scc", "memory");
}
template <> __device__ __forceinline__ void qk_step<6>(const QKW& W, const char* ag, const char* vs, const char* v2, const char* od, unsigned pb, unsigned sn, unsigned& cap, unsigned& sp,
    unsigned long long& tc, unsigned long long* ts, unsigned& d0, unsigned& d1) {
  unsigned st, bg;
  asm volatile("v_mbcnt_lo_u32_b32 v60, -1, 0\n\t"
    "v_mbcnt_hi_u32_b32 v60, -1, v60\n\t"
    "v_lshlrev_b32 v6, 4, v60\n\t"
    "s_mov_b32 %[sp], 0\n\t"
    "s_setprio 3\n\t"
    "global_load_dwordx4 v[24:27], v6, %[ag] sc1\n\t"
    "global_load_dwordx4 v[28:31], v6, %[ag] offset:-4096 sc1\n\t"
    "s_memtime %[tc]\n\t"
    "v_lshrrev_b32 v56, 4, v60\n\t"
    "v_and_b32 v57, 15, v60\n\t"
    "v_mov_b32 v20, 0\n\t"
    "v_mov_b32 v21, 0\n\t"
    "v_mov_b32 v22, 0\n\t"
    "v_mov_b32 v23, 0\n\t"
    "v_mov_b32 v11, %[sn]\n\t"
    "v_mov_b32 v10, -1\n\t"
    "v_and_b32 v61, 2, v60\n\t"
    "v_mul_u32_u24 v61, 0x5555, v61\n\t"
    "v_or_b32 v8, %[c44], v61\n\t"
    "v_lshlrev_b32 v61, 2, v56\n\t"
    "v_lshl_or_b32 v9, v57, 4, v61\n\t"
    "3:\n\t"
    "v_mov_b32 v12, 0\n\t"
    "v_mov_b32 v13, 0\n\t"
    "v_mov_b32 v14, 0\n\t"
    "v_mov_b32 v15, 0\n\t"
    "v_mov_b32 v16, 0\n\t"
    "v_mov_b32 v17, 0\n\t"
    "v_mov_b32 v18, 0\n\t"
    "v_mov_b32 v19, 0\n\t"
    "s_waitcnt vmcnt(0) lgkmcnt(0)\n\t"
    "ds_write_b128 v6, v[24:27] offset:14336\n\t"
    "ds_write_b128 v6, v[28:31] offset:10240\n\t"
    "v_smfmac_f32_16x16x64_bf16 v[12:15], v[24:27], v[96:103], v8\n\t"
    "v_max3_u32 v60, v24, v25, v26\n\t"
    "v_max3_u32 v61, v27, v28, v29\n\t"
    "v_smfmac_f32_16x16x64_bf16 v[16:19], v[24:27], v[104:111], v8\n\t"
    "v_max3_u32 v60, v60, v30, v31\n\t"
    "v_max_u32 v60, v60, v61\n\t"
    "v_cmp_eq_u32 vcc, -1, v60\n\t"
    "s_cbranch_vccnz 1f\n\t"
    "2:\n\t"
    "s_waitcnt lgkmcnt(0)\n\t"
    "s_barrier\n\t"
    "ds_read_b128 v[32:35], v6 offset:12288\n\t"
    "ds_read_b128 v[36:39], v6 offset:13312\n\t"
    "ds_read_b128 v[40:43], v6 offset:15360\n\t"
    "ds_write_b32 v9, v10 offset:18944\n\t"
    "s_waitcnt lgkmcnt(3)\n\t"
    "v_smfmac_f32_16x16x64_bf16 v[12:15], v[32:35], v[64:71], v8\n\t"
    "v_smfmac_f32_16x16x64_bf16 v[16:19], v[32:35], v[72:79], v8\n\t"
    "s_waitcnt lgkmcnt(2)\n\t"
    "v_smfmac_f32_16x16x64_bf16 v[12:15], v[36:39], v[80:87], v8\n\t"
    "v_smfmac_f32_16x16x64_bf16 v[16:19], v[36:39], v[88:95], v8\n\t"
    "s_waitcnt lgkmcnt(1)\n\t"
    "v_smfmac_f32_16x16x64_bf16 v[12:15], v[40:43], v[112:119], v8\n\t"
    "v_smfmac_f32_16x16x64_bf16 v[16:19], v[40:43], v[120:127], v8\n\t"
    "s_nop 6\n\t"
    "v_add_f32 v12, v12, v14\n\t"
    "v_add_f32 v13, v13, v15\n\t"
    "v_add_f32 v14, v16, v18\n\t"
    "v_add_f32 v15, v17, v19\n\t"
    "ds_write_b128 v6, v[12:15] offset:26624\n\t"
    "ds_write_b32 v9, v11 offset:29184\n\t"
    "s_setprio 0\n\t"
    "ds_read_b128 v[44:47], v6 offset:0\n\t"
    "ds_read_b128 v[48:51], v6 offset:1024\n\t"
    "ds_read_b128 v[52:55], v6 offset:2048\n\t"
    "ds_read_b128 v[56:59], v6 offset:3072\n\t"
    "ds_read_b128 v[60:63], v6 offset:4096\n\t"
    "s_sleep 2\n\t"
    "v_smfmac_f32_16x16x64_bf16 v[20:23], v[28:31], v[200:207], v8\n\t"
    "ds_read_b128 v[28:31], v6 offset:5120\n\t"
    "v_smfmac_f32_16x16x64_bf16 v[20:23], v[24:27], v[232:239], v8\n\t"
    "ds_read_b128 v[24:27], v6 offset:6144\n\t"
    "v_smfmac_f32_16x16x64_bf16 v[20:23], v[32:35], v[224:231], v8\n\t"
    "ds_read_b128 v[32:35], v6 offset:7168\n\t"
    "v_smfmac_f32_16x16x64_bf16 v[20:23], v[36:39], v[240:247], v8\n\t"
    "ds_read_b128 v[36:39], v6 offset:8192\n\t"
    "v_smfmac_f32_16x16x64_bf16 v[20:23], v[40:43], v[248:255], v8\n\t"
    "ds_read_b128 v[40:43], v6 offset:9216\n\t"
    "s_waitcnt lgkmcnt(9)\n\t"
    "v_smfmac_f32_16x16x64_bf16 v[20:23], v[44:47], v[128:135], v8\n\t"
    "ds_read_b128 v[44:47], v6 offset:11264\n\t"
    "s_barrier\n\t"
    "s_setprio 0\n\t"
    "s_waitcnt lgkmcnt(9)\n\t"
    "v_smfmac_f32_16x16x64_bf16 v[20:23], v[48:51], v[144:151], v8\n\t"
    "s_waitcnt lgkmcnt(8)\n\t"
    "v_smfmac_f32_16x16x64_bf16 v[20:23], v[52:55], v[136:143], v8\n\t"
    "s_waitcnt lgkmcnt(7)\n\t"
    "v_smfmac_f32_16x16x64_bf16 v[20:23], v[56:59], v[152:159], v8\n\t"
    "s_waitcnt lgkmcnt(6)\n\t"
    "v_smfmac_f32_16x16x64_bf16 v[20:23], v[60:63], v[160:167], v8\n\t"
    "s_waitcnt lgkmcnt(5)\n\t"
    "v_smfmac_f32_16x16x64_bf16 v[20:23], v[28:31], v[176:183], v8\n\t"
    "s_waitcnt lgkmcnt(4)\n\t"
    "v_smfmac_f32_16x16x64_bf16 v[20:23], v[24:27], v[168:175], v8\n\t"
    "s_waitcnt lgkmcnt(3)\n\t"
    "v_smfmac_f32_16x16x64_bf16 v[20:23], v[32:35], v[184:191], v8\n\t"
    "s_waitcnt lgkmcnt(2)\n\t"
    "v_smfmac_f32_16x16x64_bf16 v[20:23], v[36:39], v[192:199], v8\n\t"
    "s_waitcnt lgkmcnt(1)\n\t"
    "v_smfmac_f32_16x16x64_bf16 v[20:23], v[40:43], v[208:215], v8\n\t"
    "s_waitcnt lgkmcnt(0)\n\t"
    "v_smfmac_f32_16x16x64_bf16 v[20:23], v[44:47], v[216:223], v8\n\t"
    "s_nop 7\n\t"
    "v_add_f32 v20, v20, v22\n\t"
    "v_add_f32 v21, v21, v23\n\t"
    "v_cvt_pk_bf16_f32 v20, v20, v21\n\t"
    "v_min_u32 v20, -2, v20\n\t"
    "ds_write_b32 v9, v20 offset:18944\n\t"
    "s_waitcnt lgkmcnt(0)\n\t"
    "s_setprio 0\n\t"
    "s_waitcnt lgkmcnt(0)\n\t"
    "s_branch 9f\n\t"
    "1:\n\t"
    "s_add_u32 %[sp], %[sp], 1\n\t"
    "s_cmp_ge_u32 %[sp], %[cap]\n\t"
    "s_cbranch_scc1 7f\n\t"
    "global_load_dwordx4 v[24:27], v6, %[ag] sc1\n\t"
    "global_load_dwordx4 v[28:31], v6, %[ag] offset:-4096 sc1\n\t"
    "s_waitcnt lgkmcnt(0)\n\t"
    "s_memtime %[tc]\n\t"
    "s_nop 7\n\t"
    "s_branch 3b\n\t"
    "7:\n\t"
    "s_mov_b32 %[cap], 0\n\t"
    "s_branch 2b\n\t"
    "9:"
    : [tc] "=&s"(tc), [sp] "=&s"(sp), [cap] "+s"(cap)
    : [ag] "s"(ag), [c44] "s"(0x4444u), [lsc] "s"(LSC), [sn] "s"(sn), "{v[64:95]}"(W.w0), "{v[96:127]}"(W.w1), "{v[128:159]}"(W.w2), "{v[160:191]}"(W.w3), "{v[192:223]}"(W.w4), "{v[224:255]}"(W.w5)
    : "v6", "v7", "v8", "v9", "v10", "v11", "v12", "v13", "v14", "v15", "v16", "v17", "v18", "v19", "v20", "v21", "v22", "v23", "v24", "v25", "v26", "v27", "v28", "v29", "v30", "v31", "v32", "v33", "v34", "v35", "v36", "v37", "v38", "v39", "v40", "v41", "v42", "v43", "v44", "v45", "v46", "v47", "v48", "v49", "v50", "v51", "v52", "v53", "v54", "v55", "v56", "v57", "v58", "v59", "v60", "v61", "v62", "v63", "vcc", "scc", "memory");
}
template <> __device__ __forceinline__ void qk_step<7>(const QKW& W, const char* ag, const char* vs, const char* v2, const char* od, unsigned pb, unsigned sn, unsigned& cap, unsigned& sp,
    unsigned long long& tc, unsigned long long* ts, unsigned& d0, unsigned& d1) {
  unsigned st, bg;
  asm volatile("v_mbcnt_lo_u32_b32 v60, -1, 0\n\t"
    "v_mbcnt_hi_u32_b32 v60, -1, v60\n\t"
    "v_lshlrev_b32 v6, 4, v60\n\t"
    "s_mov_b32 %[sp], 0\n\t"
    "s_setprio 3\n\t"
    "global_load_dwordx4 v[24:27], v6, %[ag] sc1\n\t"
    "global_load_dwordx4 v[28:31], v6, %[ag] offset:-4096 sc1\n\t"
    "s_memtime %[tc]\n\t"
    "v_lshrrev_b32 v56, 4, v60\n\t"
    "v_and_b32 v57, 15, v60\n\t"
    "v_mov_b32 v20, 0\n\t"
    "v_mov_b32 v21, 0\n\t"
    "v_mov_b32 v22, 0\n\t"
    "v_mov_b32 v23, 0\n\t"
    "v_mov_b32 v11, %[sn]\n\t"
    "v_mov_b32 v10, -1\n\t"
    "v_and_b32 v61, 2, v60\n\t"
    "v_mul_u32_u24 v61, 0x5555, v61\n\t"
    "v_or_b32 v8, %[c44], v61\n\t"
    "v_lshlrev_b32 v61, 2, v56\n\t"
    "v_lshl_or_b32 v9, v57, 4, v61\n\t"
    "3:\n\t"
    "v_mov_b32 v12, 0\n\t"
    "v_mov_b32 v13, 0\n\t"
    "v_mov_b32 v14, 0\n\t"
    "v_mov_b32 v15, 0\n\t"
    "v_mov_b32 v16, 0\n\t"
    "v_mov_b32 v17, 0\n\t"
    "v_mov_b32 v18, 0\n\t"
    "v_mov_b32 v19, 0\n\t"
    "s_waitcnt vmcnt(0) lgkmcnt(0)\n\t"
    "ds_write_b128 v6, v[24:27] offset:15360\n\t"
    "ds_write_b128 v6, v[28:31] offset:11264\n\t"
    "v_smfmac_f32_16x16x64_bf16 v[12:15], v[24:27], v[112:119], v8\n\t"
    "v_max3_u32 v60, v24, v25, v26\n\t"
    "v_max3_u32 v61, v27, v28, v29\n\t"
    "v_smfmac_f32_16x16x64_bf16 v[16:19], v[24:27], v[120:127], v8\n\t"
    "v_max3_u32 v60, v60, v30, v31\n\t"
    "v_max_u32 v60, v60, v61\n\t"
    "v_cmp_eq_u32 vcc, -1, v60\n\t"
    "s_cbranch_vccnz 1f\n\t"
    "2:\n\t"
    "s_waitcnt lgkmcnt(0)\n\t"
    "s_barrier\n\t"
    "ds_read_b128 v[32:35], v6 offset:12288\n\t"
    "ds_read_b128 v[36:39], v6 offset:13312\n\t"
    "ds_read_b128 v[40:43], v6 offset:14336\n\t"
    "ds_write_b32 v9, v10 offset:19200\n\t"
    "s_waitcnt lgkmcnt(3)\n\t"
    "v_smfmac_f32_16x16x64_bf16 v[12:15], v[32:35], v[64:71], v8\n\t"
    "v_smfmac_f32_16x16x64_bf16 v[16:19], v[32:35], v[72:79], v8\n\t"
    "s_waitcnt lgkmcnt(2)\n\t"
    "v_smfmac_f32_16x16x64_bf16 v[12:15], v[36:39], v[80:87], v8\n\t"
    "v_smfmac_f32_16x16x64_bf16 v[16:19], v[36:39], v[88:95], v8\n\t"
    "s_waitcnt lgkmcnt(1)\n\t"
    "v_smfmac_f32_16x16x64_bf16 v[12:15], v[40:43], v[96:103], v8\n\t"
    "v_smfmac_f32_16x16x64_bf16 v[16:19], v[40:43], v[104:111], v8\n\t"
    "s_nop 6\n\t"
    "v_add_f32 v12, v12, v14\n\t"
    "v_add_f32 v13, v13, v15\n\t"
    "v_add_f32 v14, v16, v18\n\t"
    "v_add_f32 v15, v17, v19\n\t"
    "ds_write_b128 v6, v[12:15] offset:27648\n\t"
    "ds_write_b32 v9, v11 offset:29440\n\t"
    "s_setprio 0\n\t"
    "v_lshrrev_b32 v10, 24, v11\n\t"
    "v_add_u32 v10, -1, v10\n\t"
    "s_mov_b64 exec, 1\n\t"
    "global_store_dword v6, v10, %[vs] sc1\n\t"
    "s_mov_b64 exec, -1\n\t"
    "ds_read_b128 v[44:47], v6 offset:0\n\t"
    "ds_read_b128 v[48:51], v6 offset:1024\n\t"
    "ds_read_b128 v[52:55], v6 offset:2048\n\t"
    "ds_read_b128 v[56:59], v6 offset:3072\n\t"
    "ds_read_b128 v[60:63], v6 offset:4096\n\t"
    "s_sleep 2\n\t"
    "v_smfmac_f32_16x16x64_bf16 v[20:23], v[28:31], v[216:223], v8\n\t"
    "ds_read_b128 v[28:31], v6 offset:5120\n\t"
    "v_smfmac_f32_16x16x64_bf16 v[20:23], v[24:27], v[248:255], v8\n\t"
    "ds_read_b128 v[24:27], v6 offset:6144\n\t"
    "v_smfmac_f32_16x16x64_bf16 v[20:23], v[32:35], v[224:231], v8\n\t"
    "ds_read_b128 v[32:35], v6 offset:7168\n\t"
    "v_smfmac_f32_16x16x64_bf16 v[20:23], v[36:39], v[240:247], v8\n\t"
    "ds_read_b128 v[36:39], v6 offset:8192\n\t"
    "v_smfmac_f32_16x16x64_bf16 v[20:23], v[40:43], v[232:239], v8\n\t"
    "ds_read_b128 v[40:43], v6 offset:9216\n\t"
    "s_waitcnt lgkmcnt(9)\n\t"
    "v_smfmac_f32_16x16x64_bf16 v[20:23], v[44:47], v[128:135], v8\n\t"
    "ds_read_b128 v[44:47], v6 offset:10240\n\t"
    "s_barrier\n\t"
    "s_setprio 0\n\t"
    "s_waitcnt lgkmcnt(9)\n\t"
    "v_smfmac_f32_16x16x64_bf16 v[20:23], v[48:51], v[144:151], v8\n\t"
    "s_waitcnt lgkmcnt(8)\n\t"
    "v_smfmac_f32_16x16x64_bf16 v[20:23], v[52:55], v[136:143], v8\n\t"
    "s_waitcnt lgkmcnt(7)\n\t"
    "v_smfmac_f32_16x16x64_bf16 v[20:23], v[56:59], v[152:159], v8\n\t"
    "s_waitcnt lgkmcnt(6)\n\t"
    "v_smfmac_f32_16x16x64_bf16 v[20:23], v[60:63], v[160:167], v8\n\t"
    "s_waitcnt lgkmcnt(5)\n\t"
    "v_smfmac_f32_16x16x64_bf16 v[20:23], v[28:31], v[176:183], v8\n\t"
    "s_waitcnt lgkmcnt(4)\n\t"
    "v_smfmac_f32_16x16x64_bf16 v[20:23], v[24:27], v[168:175], v8\n\t"
    "s_waitcnt lgkmcnt(3)\n\t"
    "v_smfmac_f32_16x16x64_bf16 v[20:23], v[32:35], v[184:191], v8\n\t"
    "s_waitcnt lgkmcnt(2)\n\t"
    "v_smfmac_f32_16x16x64_bf16 v[20:23], v[36:39], v[192:199], v8\n\t"
    "s_waitcnt lgkmcnt(1)\n\t"
    "v_smfmac_f32_16x16x64_bf16 v[20:23], v[40:43], v[208:215], v8\n\t"
    "s_waitcnt lgkmcnt(0)\n\t"
    "v_smfmac_f32_16x16x64_bf16 v[20:23], v[44:47], v[200:207], v8\n\t"
    "s_nop 7\n\t"
    "v_add_f32 v20, v20, v22\n\t"
    "v_add_f32 v21, v21, v23\n\t"
    "v_cvt_pk_bf16_f32 v20, v20, v21\n\t"
    "v_min_u32 v20, -2, v20\n\t"
    "ds_write_b32 v9, v20 offset:19200\n\t"
    "s_waitcnt lgkmcnt(0)\n\t"
    "s_setprio 0\n\t"
    "s_waitcnt lgkmcnt(0)\n\t"
    "s_branch 9f\n\t"
    "1:\n\t"
    "s_add_u32 %[sp], %[sp], 1\n\t"
    "s_cmp_ge_u32 %[sp], %[cap]\n\t"
    "s_cbranch_scc1 7f\n\t"
    "global_load_dwordx4 v[24:27], v6, %[ag] sc1\n\t"
    "global_load_dwordx4 v[28:31], v6, %[ag] offset:-4096 sc1\n\t"
    "s_waitcnt lgkmcnt(0)\n\t"
    "s_memtime %[tc]\n\t"
    "s_nop 7\n\t"
    "s_branch 3b\n\t"
    "7:\n\t"
    "s_mov_b32 %[cap], 0\n\t"
    "s_branch 2b\n\t"
    "9:"
    : [tc] "=&s"(tc), [sp] "=&s"(sp), [cap] "+s"(cap)
    : [ag] "s"(ag), [c44] "s"(0x4444u), [lsc] "s"(LSC), [sn] "s"(sn), [vs] "s"(vs), "{v[64:95]}"(W.w0), "{v[96:127]}"(W.w1), "{v[128:159]}"(W.w2), "{v[160:191]}"(W.w3), "{v[192:223]}"(W.w4), "{v[224:255]}"(W.w5)
    : "v6", "v7", "v8", "v9", "v10", "v11", "v12", "v13", "v14", "v15", "v16", "v17", "v18", "v19", "v20", "v21", "v22", "v23", "v24", "v25", "v26", "v27", "v28", "v29", "v30", "v31", "v32", "v33", "v34", "v35", "v36", "v37", "v38", "v39", "v40", "v41", "v42", "v43", "v44", "v45", "v46", "v47", "v48", "v49", "v50", "v51", "v52", "v53", "v54", "v55", "v56", "v57", "v58", "v59", "v60", "v61", "v62", "v63", "vcc", "scc", "memory");
}
template <int W_> __device__ __forceinline__ void seg_attn(const QKW& W, unsigned pb, const char* od, unsigned long long* ts) { }
template <> __device__ __forceinline__ void seg_attn<0>(const QKW& W, unsigned pb, const char* od, unsigned long long* ts) {
  asm volatile("v_mbcnt_lo_u32_b32 v32, -1, 0\n\t"
    "v_mbcnt_hi_u32_b32 v32, -1, v32\n\t"
    "v_lshlrev_b32 v35, 4, v32\n\t"
    "ds_read_b128 v[24:27], v35 offset:20480\n\t"
    "ds_read_b128 v[28:31], v35 offset:21504\n\t"
    "v_lshrrev_b32 v33, 4, v32\n\t"
    "v_and_b32 v34, 15, v32\n\t"
    "v_xor_b32 v4, v33, v34\n\t"
    "v_lshl_or_b32 v4, v4, 4, %[pb]\n\t"
    "v_lshl_or_b32 v4, v34, 8, v4\n\t"
    "ds_read_b128 v[8:11], v4 offset:49152\n\t"
    "v_xor_b32 v5, 0x40, v4\n\t"
    "ds_read_b128 v[12:15], v5 offset:49152\n\t"
    "v_xor_b32 v6, 0x80, v4\n\t"
    "ds_read_b128 v[16:19], v6 offset:49152\n\t"
    "v_xor_b32 v7, 0xc0, v4\n\t"
    "ds_read_b128 v[20:23], v7 offset:49152\n\t"
    "ds_read_b128 v[40:43], v4 offset:32768\n\t"
    "ds_read_b128 v[44:47], v5 offset:32768\n\t"
    "ds_read_b128 v[48:51], v6 offset:32768\n\t"
    "ds_read_b128 v[52:55], v7 offset:32768\n\t"
    "v_lshlrev_b32 v35, 2, v33\n\t"
    "v_sub_u32 v35, v34, v35\n\t"
    "v_ashrrev_i32 v60, 31, v35\n\t"
    "v_and_b32 v60, %[neg], v60\n\t"
    "v_add_u32 v61, -1, v35\n\t"
    "v_ashrrev_i32 v61, 31, v61\n\t"
    "v_and_b32 v61, %[neg], v61\n\t"
    "v_add_u32 v62, -2, v35\n\t"
    "v_ashrrev_i32 v62, 31, v62\n\t"
    "v_and_b32 v62, %[neg], v62\n\t"
    "v_add_u32 v63, -3, v35\n\t"
    "v_ashrrev_i32 v63, 31, v63\n\t"
    "v_and_b32 v63, %[neg], v63\n\t"
    "s_waitcnt lgkmcnt(6)\n\t"
    "v_mfma_f32_16x16x32_bf16 v[32:35], v[24:27], v[8:11], 0\n\t"
    "v_mfma_f32_16x16x32_bf16 v[36:39], v[24:27], v[12:15], 0\n\t"
    "s_waitcnt lgkmcnt(4)\n\t"
    "v_mfma_f32_16x16x32_bf16 v[32:35], v[28:31], v[16:19], v[32:35]\n\t"
    "v_mfma_f32_16x16x32_bf16 v[36:39], v[28:31], v[20:23], v[36:39]\n\t"
    "s_waitcnt lgkmcnt(3)\n\t"
    "v_mfma_f32_16x16x32_bf16 v[60:63], v[40:43], v[8:11], v[60:63]\n\t"
    "s_waitcnt lgkmcnt(2)\n\t"
    "v_mfma_f32_16x16x32_bf16 v[60:63], v[44:47], v[12:15], v[60:63]\n\t"
    "v_mbcnt_lo_u32_b32 v7, -1, 0\n\t"
    "v_mbcnt_hi_u32_b32 v7, -1, v7\n\t"
    "s_waitcnt lgkmcnt(1)\n\t"
    "v_mfma_f32_16x16x32_bf16 v[60:63], v[48:51], v[16:19], v[60:63]\n\t"
    "v_and_or_b32 v4, v7, 3, 0\n\t"
    "v_lshrrev_b32 v6, 2, v7\n\t"
    "s_waitcnt lgkmcnt(0)\n\t"
    "v_mfma_f32_16x16x32_bf16 v[60:63], v[52:55], v[20:23], v[60:63]\n\t"
    "v_xor_b32 v4, v4, v6\n\t"
    "v_lshl_or_b32 v4, v4, 4, %[pb]\n\t"
    "v_lshl_or_b32 v4, v6, 8, v4\n\t"
    "v_xor_b32 v5, 64, v4\n\t"
    "ds_read_b64_tr_b16 v[48:49], v4 offset:40960\n\t"
    "ds_read_b64_tr_b16 v[52:53], v4 offset:40968\n\t"
    "v_add_f32 v32, v32, v38\n\t"
    "v_add_f32 v33, v33, v39\n\t"
    "v_and_b32 v6, 12, v7\n\t"
    "v_and_b32 v4, 3, v7\n\t"
    "v_lshl_add_u32 v4, v6, 1, v4\n\t"
    "v_lshrrev_b32 v6, 4, v7\n\t"
    "v_lshl_add_u32 v4, v4, 2, v6\n\t"
    "v_lshlrev_b32 v4, 2, v4\n\t"
    "v_add_u32 v4, 18432, v4\n\t"
    "ds_read_b64_tr_b16 v[40:41], v5 offset:40960\n\t"
    "ds_read_b64_tr_b16 v[44:45], v5 offset:40968\n\t"
    "ds_read2_b32 v[36:37], v4 offset0:0 offset1:16\n\t"
    "ds_read2_b32 v[38:39], v4 offset0:128 offset1:144\n\t"
    "v_and_b32 v4, 7, v7\n\t"
    "v_xor_b32 v6, v6, v4\n\t"
    "v_max3_f32 v34, v32, v33, %[lo]\n\t"
    "v_max3_f32 v34, v34, v60, v61\n\t"
    "v_max3_f32 v34, v34, v62, v63\n\t"
    "v_mov_b32 v35, v34\n\t"
    "v_lshlrev_b32 v6, 4, v6\n\t"
    "v_and_b32 v4, 15, v7\n\t"
    "v_permlane16_swap_b32 v34, v35\n\t"
    "v_max_f32 v34, v34, v35\n\t"
    "v_mov_b32 v35, v34\n\t"
    "v_lshl_or_b32 v6, v4, 7, v6\n\t"
    "v_xor_b32 v4, 64, v6\n\t"
    "v_permlane32_swap_b32 v34, v35\n\t"
    "v_max_f32 v34, v34, v35\n\t"
    "v_mul_f32 v34, %[nl], v34\n\t"
    "v_fma_f32 v32, v32, %[lsc], v34\n\t"
    "v_fma_f32 v33, v33, %[lsc], v34\n\t"
    "v_fma_f32 v60, v60, %[lsc], v34\n\t"
    "v_fma_f32 v61, v61, %[lsc], v34\n\t"
    "v_fma_f32 v62, v62, %[lsc], v34\n\t"
    "v_fma_f32 v63, v63, %[lsc], v34\n\t"
    "v_exp_f32 v60, v60\n\t"
    "v_exp_f32 v61, v61\n\t"
    "v_exp_f32 v62, v62\n\t"
    "v_exp_f32 v63, v63\n\t"
    "v_exp_f32 v32, v32\n\t"
    "v_exp_f32 v33, v33\n\t"
    "v_cvt_pk_bf16_f32 v24, v60, v61\n\t"
    "v_cvt_pk_bf16_f32 v25, v62, v63\n\t"
    "v_cvt_pk_bf16_f32 v28, v32, v33\n\t"
    "s_waitcnt lgkmcnt(0)\n\t"
    "v_mfma_f32_16x16x16_bf16 v[8:11], v[48:49], v[24:25], 0\n\t"
    "v_mov_b32 v56, %[one]\n\t"
    "v_mov_b32 v57, %[one]\n\t"
    "v_mfma_f32_16x16x16_bf16 v[12:15], v[52:53], v[24:25], 0\n\t"
    "v_mov_b32 v58, %[one]\n\t"
    "v_mov_b32 v59, %[one]\n\t"
    "v_mfma_f32_16x16x16_bf16 v[16:19], v[40:41], v[24:25], 0\n\t"
    "v_mov_b32 v29, 0\n\t"
    "v_mov_b32 v30, 0\n\t"
    "v_mfma_f32_16x16x16_bf16 v[60:63], v[56:57], v[24:25], 0\n\t"
    "v_mov_b32 v31, v28\n\t"
    "v_mfma_f32_16x16x16_bf16 v[20:23], v[44:45], v[24:25], 0\n\t"
    "v_mfma_f32_16x16x16_bf16 v[8:11], v[36:37], v[28:29], v[8:11]\n\t"
    "v_mfma_f32_16x16x16_bf16 v[12:15], v[36:37], v[30:31], v[12:15]\n\t"
    "s_nop 1\n\t"
    "v_mfma_f32_16x16x16_bf16 v[16:19], v[38:39], v[28:29], v[16:19]\n\t"
    "v_mfma_f32_16x16x16_bf16 v[60:63], v[56:57], v[28:29], v[60:63]\n\t"
    "s_nop 1\n\t"
    "v_mfma_f32_16x16x16_bf16 v[20:23], v[38:39], v[30:31], v[20:23]\n\t"
    "s_nop 4\n\t"
    "v_rcp_f32 v60, v60\n\t"
    "s_nop 0\n\t"
    "v_pk_mul_f32 v[8:9], v[8:9], v[60:61] op_sel_hi:[1,0]\n\t"
    "v_pk_mul_f32 v[10:11], v[10:11], v[60:61] op_sel_hi:[1,0]\n\t"
    "v_pk_mul_f32 v[12:13], v[12:13], v[60:61] op_sel_hi:[1,0]\n\t"
    "v_pk_mul_f32 v[14:15], v[14:15], v[60:61] op_sel_hi:[1,0]\n\t"
    "v_cvt_pk_bf16_f32 v24, v8, v9\n\t"
    "v_cvt_pk_bf16_f32 v25, v10, v11\n\t"
    "v_cvt_pk_bf16_f32 v26, v12, v13\n\t"
    "v_cvt_pk_bf16_f32 v27, v14, v15\n\t"
    "global_store_dwordx4 v6, v[24:27], %[od] offset:0 sc1\n\t"
    "v_pk_mul_f32 v[16:17], v[16:17], v[60:61] op_sel_hi:[1,0]\n\t"
    "v_pk_mul_f32 v[18:19], v[18:19], v[60:61] op_sel_hi:[1,0]\n\t"
    "v_pk_mul_f32 v[20:21], v[20:21], v[60:61] op_sel_hi:[1,0]\n\t"
    "v_pk_mul_f32 v[22:23], v[22:23], v[60:61] op_sel_hi:[1,0]\n\t"
    "v_cvt_pk_bf16_f32 v28, v16, v17\n\t"
    "v_cvt_pk_bf16_f32 v29, v18, v19\n\t"
    "v_cvt_pk_bf16_f32 v30, v20, v21\n\t"
    "v_cvt_pk_bf16_f32 v31, v22, v23\n\t"
    "global_store_dwordx4 v4, v[28:31], %[od] offset:0 sc1\n\t"
    "s_waitcnt lgkmcnt(0)\n\t"
    "s_nop 0"
    :
    : [pb] "s"(pb), [od] "s"(od), [neg] "s"(-3.0e38f), [lo] "s"(-1.0e38f), [one] "s"(0x3F803F80u), [lsc] "s"(LSC), [nl] "s"(-LSC), "{v[64:95]}"(W.w0), "{v[96:127]}"(W.w1), "{v[128:159]}"(W.w2), "{v[160:191]}"(W.w3), "{v[192:223]}"(W.w4), "{v[224:255]}"(W.w5)
    : "v4", "v5", "v6", "v7", "v8", "v9", "v10", "v11", "v12", "v13", "v14", "v15", "v16", "v17", "v18", "v19", "v20", "v21", "v22", "v23", "v24", "v25", "v26", "v27", "v28", "v29", "v30", "v31", "v32", "v33", "v34", "v35", "v36", "v37", "v38", "v39", "v40", "v41", "v42", "v43", "v44", "v45", "v46", "v47", "v48", "v49", "v50", "v51", "v52", "v53", "v54", "v55", "v56", "v57", "v58", "v59", "v60", "v61", "v62", "v63", "memory");
}
template <> __device__ __forceinline__ void seg_attn<1>(const QKW& W, unsigned pb, const char* od, unsigned long long* ts) {
  asm volatile("v_mbcnt_lo_u32_b32 v32, -1, 0\n\t"
    "v_mbcnt_hi_u32_b32 v32, -1, v32\n\t"
    "v_lshlrev_b32 v35, 4, v32\n\t"
    "ds_read_b128 v[24:27], v35 offset:20480\n\t"
    "ds_read_b128 v[28:31], v35 offset:21504\n\t"
    "v_lshrrev_b32 v33, 4, v32\n\t"
    "v_and_b32 v34, 15, v32\n\t"
    "v_xor_b32 v4, v33, v34\n\t"
    "v_lshl_or_b32 v4, v4, 4, %[pb]\n\t"
    "v_lshl_or_b32 v4, v34, 8, v4\n\t"
    "ds_read_b128 v[8:11], v4 offset:53248\n\t"
    "v_xor_b32 v5, 0x40, v4\n\t"
    "ds_read_b128 v[12:15], v5 offset:53248\n\t"
    "v_xor_b32 v6, 0x80, v4\n\t"
    "ds_read_b128 v[16:19], v6 offset:53248\n\t"
    "v_xor_b32 v7, 0xc0, v4\n\t"
    "ds_read_b128 v[20:23], v7 offset:53248\n\t"
    "ds_read_b128 v[40:43], v4 offset:32768\n\t"
    "ds_read_b128 v[44:47], v5 offset:32768\n\t"
    "ds_read_b128 v[48:51], v6 offset:32768\n\t"
    "ds_read_b128 v[52:55], v7 offset:32768\n\t"
    "v_lshlrev_b32 v35, 2, v33\n\t"
    "v_sub_u32 v35, v34, v35\n\t"
    "v_ashrrev_i32 v60, 31, v35\n\t"
    "v_and_b32 v60, %[neg], v60\n\t"
    "v_add_u32 v61, -1, v35\n\t"
    "v_ashrrev_i32 v61, 31, v61\n\t"
    "v_and_b32 v61, %[neg], v61\n\t"
    "v_add_u32 v62, -2, v35\n\t"
    "v_ashrrev_i32 v62, 31, v62\n\t"
    "v_and_b32 v62, %[neg], v62\n\t"
    "v_add_u32 v63, -3, v35\n\t"
    "v_ashrrev_i32 v63, 31, v63\n\t"
    "v_and_b32 v63, %[neg], v63\n\t"
    "s_waitcnt lgkmcnt(6)\n\t"
    "v_mfma_f32_16x16x32_bf16 v[32:35], v[24:27], v[8:11], 0\n\t"
    "v_mfma_f32_16x16x32_bf16 v[36:39], v[24:27], v[12:15], 0\n\t"
    "s_waitcnt lgkmcnt(4)\n\t"
    "v_mfma_f32_16x16x32_bf16 v[32:35], v[28:31], v[16:19], v[32:35]\n\t"
    "v_mfma_f32_16x16x32_bf16 v[36:39], v[28:31], v[20:23], v[36:39]\n\t"
    "ds_read_b128 v[24:27], v4 offset:36864\n\t"
    "ds_read_b128 v[28:31], v5 offset:36864\n\t"
    "s_waitcnt lgkmcnt(5)\n\t"
    "v_mfma_f32_16x16x32_bf16 v[56:59], v[40:43], v[8:11], 0\n\t"
    "s_waitcnt lgkmcnt(4)\n\t"
    "v_mfma_f32_16x16x32_bf16 v[56:59], v[44:47], v[12:15], v[56:59]\n\t"
    "ds_read_b128 v[40:43], v6 offset:36864\n\t"
    "ds_read_b128 v[44:47], v7 offset:36864\n\t"
    "v_mbcnt_lo_u32_b32 v7, -1, 0\n\t"
    "v_mbcnt_hi_u32_b32 v7, -1, v7\n\t"
    "s_waitcnt lgkmcnt(5)\n\t"
    "v_mfma_f32_16x16x32_bf16 v[56:59], v[48:51], v[16:19], v[56:59]\n\t"
    "v_and_or_b32 v4, v7, 3, 0\n\t"
    "v_lshrrev_b32 v6, 2, v7\n\t"
    "s_waitcnt lgkmcnt(4)\n\t"
    "v_mfma_f32_16x16x32_bf16 v[56:59], v[52:55], v[20:23], v[56:59]\n\t"
    "v_xor_b32 v4, v4, v6\n\t"
    "v_lshl_or_b32 v4, v4, 4, %[pb]\n\t"
    "v_lshl_or_b32 v4, v6, 8, v4\n\t"
    "v_xor_b32 v5, 64, v4\n\t"
    "ds_read_b64_tr_b16 v[48:49], v4 offset:40960\n\t"
    "ds_read_b64_tr_b16 v[50:51], v4 offset:45056\n\t"
    "ds_read_b64_tr_b16 v[52:53], v4 offset:40968\n\t"
    "ds_read_b64_tr_b16 v[54:55], v4 offset:45064\n\t"
    "v_add_f32 v32, v32, v38\n\t"
    "v_add_f32 v33, v33, v39\n\t"
    "s_waitcnt lgkmcnt(7)\n\t"
    "v_mfma_f32_16x16x32_bf16 v[60:63], v[24:27], v[8:11], v[60:63]\n\t"
    "v_and_b32 v6, 12, v7\n\t"
    "v_and_b32 v4, 3, v7\n\t"
    "s_waitcnt lgkmcnt(6)\n\t"
    "v_mfma_f32_16x16x32_bf16 v[60:63], v[28:31], v[12:15], v[60:63]\n\t"
    "v_lshl_add_u32 v4, v6, 1, v4\n\t"
    "v_lshrrev_b32 v6, 4, v7\n\t"
    "s_waitcnt lgkmcnt(5)\n\t"
    "v_mfma_f32_16x16x32_bf16 v[60:63], v[40:43], v[16:19], v[60:63]\n\t"
    "v_lshl_add_u32 v4, v4, 2, v6\n\t"
    "v_lshlrev_b32 v4, 2, v4\n\t"
    "s_waitcnt lgkmcnt(4)\n\t"
    "v_mfma_f32_16x16x32_bf16 v[60:63], v[44:47], v[20:23], v[60:63]\n\t"
    "v_add_u32 v4, 18432, v4\n\t"
    "ds_read_b64_tr_b16 v[40:41], v5 offset:40960\n\t"
    "ds_read_b64_tr_b16 v[42:43], v5 offset:45056\n\t"
    "ds_read_b64_tr_b16 v[44:45], v5 offset:40968\n\t"
    "ds_read_b64_tr_b16 v[46:47], v5 offset:45064\n\t"
    "ds_read2_b32 v[36:37], v4 offset0:0 offset1:16\n\t"
    "ds_read2_b32 v[38:39], v4 offset0:128 offset1:144\n\t"
    "v_and_b32 v4, 7, v7\n\t"
    "v_xor_b32 v6, v6, v4\n\t"
    "v_max3_f32 v34, v32, v33, %[lo]\n\t"
    "v_max3_f32 v34, v34, v56, v57\n\t"
    "v_max3_f32 v34, v34, v58, v59\n\t"
    "v_max3_f32 v34, v34, v60, v61\n\t"
    "v_max3_f32 v34, v34, v62, v63\n\t"
    "v_mov_b32 v35, v34\n\t"
    "v_lshlrev_b32 v6, 4, v6\n\t"
    "v_and_b32 v4, 15, v7\n\t"
    "v_permlane16_swap_b32 v34, v35\n\t"
    "v_max_f32 v34, v34, v35\n\t"
    "v_mov_b32 v35, v34\n\t"
    "v_lshl_or_b32 v6, v4, 7, v6\n\t"
    "v_xor_b32 v4, 64, v6\n\t"
    "v_permlane32_swap_b32 v34, v35\n\t"
    "v_max_f32 v34, v34, v35\n\t"
    "v_mul_f32 v34, %[nl], v34\n\t"
    "v_fma_f32 v32, v32, %[lsc], v34\n\t"
    "v_fma_f32 v33, v33, %[lsc], v34\n\t"
    "v_fma_f32 v56, v56, %[lsc], v34\n\t"
    "v_fma_f32 v57, v57, %[lsc], v34\n\t"
    "v_fma_f32 v58, v58, %[lsc], v34\n\t"
    "v_fma_f32 v59, v59, %[lsc], v34\n\t"
    "v_fma_f32 v60, v60, %[lsc], v34\n\t"
    "v_fma_f32 v61, v61, %[lsc], v34\n\t"
    "v_fma_f32 v62, v62, %[lsc], v34\n\t"
    "v_fma_f32 v63, v63, %[lsc], v34\n\t"
    "v_exp_f32 v56, v56\n\t"
    "v_exp_f32 v57, v57\n\t"
    "v_exp_f32 v58, v58\n\t"
    "v_exp_f32 v59, v59\n\t"
    "v_exp_f32 v60, v60\n\t"
    "v_exp_f32 v61, v61\n\t"
    "v_exp_f32 v62, v62\n\t"
    "v_exp_f32 v63, v63\n\t"
    "v_exp_f32 v32, v32\n\t"
    "v_exp_f32 v33, v33\n\t"
    "v_cvt_pk_bf16_f32 v24, v56, v57\n\t"
    "v_cvt_pk_bf16_f32 v25, v58, v59\n\t"
    "v_cvt_pk_bf16_f32 v26, v60, v61\n\t"
    "v_cvt_pk_bf16_f32 v27, v62, v63\n\t"
    "v_cvt_pk_bf16_f32 v28, v32, v33\n\t"
    "s_waitcnt lgkmcnt(0)\n\t"
    "v_mfma_f32_16x16x32_bf16 v[8:11], v[48:51], v[24:27], 0\n\t"
    "v_mov_b32 v56, %[one]\n\t"
    "v_mov_b32 v57, %[one]\n\t"
    "v_mfma_f32_16x16x32_bf16 v[12:15], v[52:55], v[24:27], 0\n\t"
    "v_mov_b32 v58, %[one]\n\t"
    "v_mov_b32 v59, %[one]\n\t"
    "v_mfma_f32_16x16x32_bf16 v[16:19], v[40:43], v[24:27], 0\n\t"
    "v_mov_b32 v29, 0\n\t"
    "v_mov_b32 v30, 0\n\t"
    "v_mfma_f32_16x16x32_bf16 v[60:63], v[56:59], v[24:27], 0\n\t"
    "v_mov_b32 v31, v28\n\t"
    "v_mfma_f32_16x16x32_bf16 v[20:23], v[44:47], v[24:27], 0\n\t"
    "v_mfma_f32_16x16x16_bf16 v[8:11], v[36:37], v[28:29], v[8:11]\n\t"
    "v_mfma_f32_16x16x16_bf16 v[12:15], v[36:37], v[30:31], v[12:15]\n\t"
    "s_nop 1\n\t"
    "v_mfma_f32_16x16x16_bf16 v[16:19], v[38:39], v[28:29], v[16:19]\n\t"
    "v_mfma_f32_16x16x16_bf16 v[60:63], v[56:57], v[28:29], v[60:63]\n\t"
    "s_nop 1\n\t"
    "v_mfma_f32_16x16x16_bf16 v[20:23], v[38:39], v[30:31], v[20:23]\n\t"
    "s_nop 4\n\t"
    "v_rcp_f32 v60, v60\n\t"
    "s_nop 0\n\t"
    "v_pk_mul_f32 v[8:9], v[8:9], v[60:61] op_sel_hi:[1,0]\n\t"
    "v_pk_mul_f32 v[10:11], v[10:11], v[60:61] op_sel_hi:[1,0]\n\t"
    "v_pk_mul_f32 v[12:13], v[12:13], v[60:61] op_sel_hi:[1,0]\n\t"
    "v_pk_mul_f32 v[14:15], v[14:15], v[60:61] op_sel_hi:[1,0]\n\t"
    "v_cvt_pk_bf16_f32 v24, v8, v9\n\t"
    "v_cvt_pk_bf16_f32 v25, v10, v11\n\t"
    "v_cvt_pk_bf16_f32 v26, v12, v13\n\t"
    "v_cvt_pk_bf16_f32 v27, v14, v15\n\t"
    "global_store_dwordx4 v6, v[24:27], %[od] offset:2048 sc1\n\t"
    "v_pk_mul_f32 v[16:17], v[16:17], v[60:61] op_sel_hi:[1,0]\n\t"
    "v_pk_mul_f32 v[18:19], v[18:19], v[60:61] op_sel_hi:[1,0]\n\t"
    "v_pk_mul_f32 v[20:21], v[20:21], v[60:61] op_sel_hi:[1,0]\n\t"
    "v_pk_mul_f32 v[22:23], v[22:23], v[60:61] op_sel_hi:[1,0]\n\t"
    "v_cvt_pk_bf16_f32 v28, v16, v17\n\t"
    "v_cvt_pk_bf16_f32 v29, v18, v19\n\t"
    "v_cvt_pk_bf16_f32 v30, v20, v21\n\t"
    "v_cvt_pk_bf16_f32 v31, v22, v23\n\t"
    "global_store_dwordx4 v4, v[28:31], %[od] offset:2048 sc1\n\t"
    "s_waitcnt lgkmcnt(0)\n\t"
    "s_nop 0"
    :
    : [pb] "s"(pb), [od] "s"(od), [neg] "s"(-3.0e38f), [lo] "s"(-1.0e38f), [one] "s"(0x3F803F80u), [lsc] "s"(LSC), [nl] "s"(-LSC), "{v[64:95]}"(W.w0), "{v[96:127]}"(W.w1), "{v[128:159]}"(W.w2), "{v[160:191]}"(W.w3), "{v[192:223]}"(W.w4), "{v[224:255]}"(W.w5)
    : "v4", "v5", "v6", "v7", "v8", "v9", "v10", "v11", "v12", "v13", "v14", "v15", "v16", "v17", "v18", "v19", "v20", "v21", "v22", "v23", "v24", "v25", "v26", "v27", "v28", "v29", "v30", "v31", "v32", "v33", "v34", "v35", "v36", "v37", "v38", "v39", "v40", "v41", "v42", "v43", "v44", "v45", "v46", "v47", "v48", "v49", "v50", "v51", "v52", "v53", "v54", "v55", "v56", "v57", "v58", "v59", "v60", "v61", "v62", "v63", "memory");
}
template <> __device__ __forceinline__ void seg_attn<2>(const QKW& W, unsigned pb, const char* od, unsigned long long* ts) {
  asm volatile("v_mbcnt_lo_u32_b32 v32, -1, 0\n\t"
    "v_mbcnt_hi_u32_b32 v32, -1, v32\n\t"
    "v_lshlrev_b32 v35, 4, v32\n\t"
    "ds_read_b128 v[24:27], v35 offset:20480\n\t"
    "ds_read_b128 v[28:31], v35 offset:21504\n\t"
    "v_lshrrev_b32 v33, 4, v32\n\t"
    "v_and_b32 v34, 15, v32\n\t"
    "v_xor_b32 v4, v33, v34\n\t"
    "v_lshl_or_b32 v4, v4, 4, %[pb]\n\t"
    "v_lshl_or_b32 v4, v34, 8, v4\n\t"
    "ds_read_b128 v[8:11], v4 offset:49152\n\t"
    "v_xor_b32 v5, 0x40, v4\n\t"
    "ds_read_b128 v[12:15], v5 offset:49152\n\t"
    "v_xor_b32 v6, 0x80, v4\n\t"
    "ds_read_b128 v[16:19], v6 offset:49152\n\t"
    "v_xor_b32 v7, 0xc0, v4\n\t"
    "ds_read_b128 v[20:23], v7 offset:49152\n\t"
    "ds_read_b128 v[40:43], v4 offset:32768\n\t"
    "ds_read_b128 v[44:47], v5 offset:32768\n\t"
    "ds_read_b128 v[48:51], v6 offset:32768\n\t"
    "ds_read_b128 v[52:55], v7 offset:32768\n\t"
    "v_lshlrev_b32 v35, 2, v33\n\t"
    "v_sub_u32 v35, v34, v35\n\t"
    "v_ashrrev_i32 v60, 31, v35\n\t"
    "v_and_b32 v60, %[neg], v60\n\t"
    "v_add_u32 v61, -1, v35\n\t"
    "v_ashrrev_i32 v61, 31, v61\n\t"
    "v_and_b32 v61, %[neg], v61\n\t"
    "v_add_u32 v62, -2, v35\n\t"
    "v_ashrrev_i32 v62, 31, v62\n\t"
    "v_and_b32 v62, %[neg], v62\n\t"
    "v_add_u32 v63, -3, v35\n\t"
    "v_ashrrev_i32 v63, 31, v63\n\t"
    "v_and_b32 v63, %[neg], v63\n\t"
    "s_waitcnt lgkmcnt(6)\n\t"
    "v_mfma_f32_16x16x32_bf16 v[32:35], v[24:27], v[8:11], 0\n\t"
    "v_mfma_f32_16x16x32_bf16 v[36:39], v[24:27], v[12:15], 0\n\t"
    "s_waitcnt lgkmcnt(4)\n\t"
    "v_mfma_f32_16x16x32_bf16 v[32:35], v[28:31], v[16:19], v[32:35]\n\t"
    "v_mfma_f32_16x16x32_bf16 v[36:39], v[28:31], v[20:23], v[36:39]\n\t"
    "s_waitcnt lgkmcnt(3)\n\t"
    "v_mfma_f32_16x16x32_bf16 v[60:63], v[40:43], v[8:11], v[60:63]\n\t"
    "s_waitcnt lgkmcnt(2)\n\t"
    "v_mfma_f32_16x16x32_bf16 v[60:63], v[44:47], v[12:15], v[60:63]\n\t"
    "v_mbcnt_lo_u32_b32 v7, -1, 0\n\t"
    "v_mbcnt_hi_u32_b32 v7, -1, v7\n\t"
    "s_waitcnt lgkmcnt(1)\n\t"
    "v_mfma_f32_16x16x32_bf16 v[60:63], v[48:51], v[16:19], v[60:63]\n\t"
    "v_and_or_b32 v4, v7, 3, 8\n\t"
    "v_lshrrev_b32 v6, 2, v7\n\t"
    "s_waitcnt lgkmcnt(0)\n\t"
    "v_mfma_f32_16x16x32_bf16 v[60:63], v[52:55], v[20:23], v[60:63]\n\t"
    "v_xor_b32 v4, v4, v6\n\t"
    "v_lshl_or_b32 v4, v4, 4, %[pb]\n\t"
    "v_lshl_or_b32 v4, v6, 8, v4\n\t"
    "v_xor_b32 v5, 64, v4\n\t"
    "ds_read_b64_tr_b16 v[48:49], v4 offset:40960\n\t"
    "ds_read_b64_tr_b16 v[52:53], v4 offset:40968\n\t"
    "v_add_f32 v32, v32, v38\n\t"
    "v_add_f32 v33, v33, v39\n\t"
    "v_and_b32 v6, 12, v7\n\t"
    "v_and_b32 v4, 3, v7\n\t"
    "v_lshl_add_u32 v4, v6, 1, v4\n\t"
    "v_lshrrev_b32 v6, 4, v7\n\t"
    "v_lshl_add_u32 v4, v4, 2, v6\n\t"
    "v_lshlrev_b32 v4, 2, v4\n\t"
    "v_add_u32 v4, 19456, v4\n\t"
    "ds_read_b64_tr_b16 v[40:41], v5 offset:40960\n\t"
    "ds_read_b64_tr_b16 v[44:45], v5 offset:40968\n\t"
    "ds_read2_b32 v[36:37], v4 offset0:0 offset1:16\n\t"
    "ds_read2_b32 v[38:39], v4 offset0:128 offset1:144\n\t"
    "v_and_b32 v4, 7, v7\n\t"
    "v_xor_b32 v6, v6, v4\n\t"
    "v_max3_f32 v34, v32, v33, %[lo]\n\t"
    "v_max3_f32 v34, v34, v60, v61\n\t"
    "v_max3_f32 v34, v34, v62, v63\n\t"
    "v_mov_b32 v35, v34\n\t"
    "v_lshlrev_b32 v6, 4, v6\n\t"
    "v_and_b32 v4, 15, v7\n\t"
    "v_permlane16_swap_b32 v34, v35\n\t"
    "v_max_f32 v34, v34, v35\n\t"
    "v_mov_b32 v35, v34\n\t"
    "v_lshl_or_b32 v6, v4, 7, v6\n\t"
    "v_xor_b32 v4, 64, v6\n\t"
    "v_permlane32_swap_b32 v34, v35\n\t"
    "v_max_f32 v34, v34, v35\n\t"
    "v_mul_f32 v34, %[nl], v34\n\t"
    "v_fma_f32 v32, v32, %[lsc], v34\n\t"
    "v_fma_f32 v33, v33, %[lsc], v34\n\t"
    "v_fma_f32 v60, v60, %[lsc], v34\n\t"
    "v_fma_f32 v61, v61, %[lsc], v34\n\t"
    "v_fma_f32 v62, v62, %[lsc], v34\n\t"
    "v_fma_f32 v63, v63, %[lsc], v34\n\t"
    "v_exp_f32 v60, v60\n\t"
    "v_exp_f32 v61, v61\n\t"
    "v_exp_f32 v62, v62\n\t"
    "v_exp_f32 v63, v63\n\t"
    "v_exp_f32 v32, v32\n\t"
    "v_exp_f32 v33, v33\n\t"
    "v_cvt_pk_bf16_f32 v24, v60, v61\n\t"
    "v_cvt_pk_bf16_f32 v25, v62, v63\n\t"
    "v_cvt_pk_bf16_f32 v28, v32, v33\n\t"
    "s_waitcnt lgkmcnt(0)\n\t"
    "v_mfma_f32_16x16x16_bf16 v[8:11], v[48:49], v[24:25], 0\n\t"
    "v_mov_b32 v56, %[one]\n\t"
    "v_mov_b32 v57, %[one]\n\t"
    "v_mfma_f32_16x16x16_bf16 v[12:15], v[52:53], v[24:25], 0\n\t"
    "v_mov_b32 v58, %[one]\n\t"
    "v_mov_b32 v59, %[one]\n\t"
    "v_mfma_f32_16x16x16_bf16 v[16:19], v[40:41], v[24:25], 0\n\t"
    "v_mov_b32 v29, 0\n\t"
    "v_mov_b32 v30, 0\n\t"
    "v_mfma_f32_16x16x16_bf16 v[60:63], v[56:57], v[24:25], 0\n\t"
    "v_mov_b32 v31, v28\n\t"
    "v_mfma_f32_16x16x16_bf16 v[20:23], v[44:45], v[24:25], 0\n\t"
    "v_mfma_f32_16x16x16_bf16 v[8:11], v[36:37], v[28:29], v[8:11]\n\t"
    "v_mfma_f32_16x16x16_bf16 v[12:15], v[36:37], v[30:31], v[12:15]\n\t"
    "s_nop 1\n\t"
    "v_mfma_f32_16x16x16_bf16 v[16:19], v[38:39], v[28:29], v[16:19]\n\t"
    "v_mfma_f32_16x16x16_bf16 v[60:63], v[56:57], v[28:29], v[60:63]\n\t"
    "s_nop 1\n\t"
    "v_mfma_f32_16x16x16_bf16 v[20:23], v[38:39], v[30:31], v[20:23]\n\t"
    "s_nop 4\n\t"
    "v_rcp_f32 v60, v60\n\t"
    "s_nop 0\n\t"
    "v_pk_mul_f32 v[8:9], v[8:9], v[60:61] op_sel_hi:[1,0]\n\t"
    "v_pk_mul_f32 v[10:11], v[10:11], v[60:61] op_sel_hi:[1,0]\n\t"
    "v_pk_mul_f32 v[12:13], v[12:13], v[60:61] op_sel_hi:[1,0]\n\t"
    "v_pk_mul_f32 v[14:15], v[14:15], v[60:61] op_sel_hi:[1,0]\n\t"
    "v_cvt_pk_bf16_f32 v24, v8, v9\n\t"
    "v_cvt_pk_bf16_f32 v25, v10, v11\n\t"
    "v_cvt_pk_bf16_f32 v26, v12, v13\n\t"
    "v_cvt_pk_bf16_f32 v27, v14, v15\n\t"
    "global_store_dwordx4 v6, v[24:27], %[od] offset:0 sc1\n\t"
    "v_pk_mul_f32 v[16:17], v[16:17], v[60:61] op_sel_hi:[1,0]\n\t"
    "v_pk_mul_f32 v[18:19], v[18:19], v[60:61] op_sel_hi:[1,0]\n\t"
    "v_pk_mul_f32 v[20:21], v[20:21], v[60:61] op_sel_hi:[1,0]\n\t"
    "v_pk_mul_f32 v[22:23], v[22:23], v[60:61] op_sel_hi:[1,0]\n\t"
    "v_cvt_pk_bf16_f32 v28, v16, v17\n\t"
    "v_cvt_pk_bf16_f32 v29, v18, v19\n\t"
    "v_cvt_pk_bf16_f32 v30, v20, v21\n\t"
    "v_cvt_pk_bf16_f32 v31, v22, v23\n\t"
    "global_store_dwordx4 v4, v[28:31], %[od] offset:0 sc1\n\t"
    "s_waitcnt lgkmcnt(0)\n\t"
    "s_nop 0"
    :
    : [pb] "s"(pb), [od] "s"(od), [neg] "s"(-3.0e38f), [lo] "s"(-1.0e38f), [one] "s"(0x3F803F80u), [lsc] "s"(LSC), [nl] "s"(-LSC), "{v[64:95]}"(W.w0), "{v[96:127]}"(W.w1), "{v[128:159]}"(W.w2), "{v[160:191]}"(W.w3), "{v[192:223]}"(W.w4), "{v[224:255]}"(W.w5)
    : "v4", "v5", "v6", "v7", "v8", "v9", "v10", "v11", "v12", "v13", "v14", "v15", "v16", "v17", "v18", "v19", "v20", "v21", "v22", "v23", "v24", "v25", "v26", "v27", "v28", "v29", "v30", "v31", "v32", "v33", "v34", "v35", "v36", "v37", "v38", "v39", "v40", "v41", "v42", "v43", "v44", "v45", "v46", "v47", "v48", "v49", "v50", "v51", "v52", "v53", "v54", "v55", "v56", "v57", "v58", "v59", "v60", "v61", "v62", "v63", "memory");
}
template <> __device__ __forceinline__ void seg_attn<3>(const QKW& W, unsigned pb, const char* od, unsigned long long* ts) {
  asm volatile("v_mbcnt_lo_u32_b32 v32, -1, 0\n\t"
    "v_mbcnt_hi_u32_b32 v32, -1, v32\n\t"
    "v_lshlrev_b32 v35, 4, v32\n\t"
    "ds_read_b128 v[24:27], v35 offset:20480\n\t"
    "ds_read_b128 v[28:31], v35 offset:21504\n\t"
    "v_lshrrev_b32 v33, 4, v32\n\t"
    "v_and_b32 v34, 15, v32\n\t"
    "v_xor_b32 v4, v33, v34\n\t"
    "v_lshl_or_b32 v4, v4, 4, %[pb]\n\t"
    "v_lshl_or_b32 v4, v34, 8, v4\n\t"
    "ds_read_b128 v[8:11], v4 offset:53248\n\t"
    "v_xor_b32 v5, 0x40, v4\n\t"
    "ds_read_b128 v[12:15], v5 offset:53248\n\t"
    "v_xor_b32 v6, 0x80, v4\n\t"
    "ds_read_b128 v[16:19], v6 offset:53248\n\t"
    "v_xor_b32 v7, 0xc0, v4\n\t"
    "ds_read_b128 v[20:23], v7 offset:53248\n\t"
    "ds_read_b128 v[40:43], v4 offset:32768\n\t"
    "ds_read_b128 v[44:47], v5 offset:32768\n\t"
    "ds_read_b128 v[48:51], v6 offset:32768\n\t"
    "ds_read_b128 v[52:55], v7 offset:32768\n\t"
    "v_lshlrev_b32 v35, 2, v33\n\t"
    "v_sub_u32 v35, v34, v35\n\t"
    "v_ashrrev_i32 v60, 31, v35\n\t"
    "v_and_b32 v60, %[neg], v60\n\t"
    "v_add_u32 v61, -1, v35\n\t"
    "v_ashrrev_i32 v61, 31, v61\n\t"
    "v_and_b32 v61, %[neg], v61\n\t"
    "v_add_u32 v62, -2, v35\n\t"
    "v_ashrrev_i32 v62, 31, v62\n\t"
    "v_and_b32 v62, %[neg], v62\n\t"
    "v_add_u32 v63, -3, v35\n\t"
    "v_ashrrev_i32 v63, 31, v63\n\t"
    "v_and_b32 v63, %[neg], v63\n\t"
    "s_waitcnt lgkmcnt(6)\n\t"
    "v_mfma_f32_16x16x32_bf16 v[32:35], v[24:27], v[8:11], 0\n\t"
    "v_mfma_f32_16x16x32_bf16 v[36:39], v[24:27], v[12:15], 0\n\t"
    "s_waitcnt lgkmcnt(4)\n\t"
    "v_mfma_f32_16x16x32_bf16 v[32:35], v[28:31], v[16:19], v[32:35]\n\t"
    "v_mfma_f32_16x16x32_bf16 v[36:39], v[28:31], v[20:23], v[36:39]\n\t"
    "ds_read_b128 v[24:27], v4 offset:36864\n\t"
    "ds_read_b128 v[28:31], v5 offset:36864\n\t"
    "s_waitcnt lgkmcnt(5)\n\t"
    "v_mfma_f32_16x16x32_bf16 v[56:59], v[40:43], v[8:11], 0\n\t"
    "s_waitcnt lgkmcnt(4)\n\t"
    "v_mfma_f32_16x16x32_bf16 v[56:59], v[44:47], v[12:15], v[56:59]\n\t"
    "ds_read_b128 v[40:43], v6 offset:36864\n\t"
    "ds_read_b128 v[44:47], v7 offset:36864\n\t"
    "v_mbcnt_lo_u32_b32 v7, -1, 0\n\t"
    "v_mbcnt_hi_u32_b32 v7, -1, v7\n\t"
    "s_waitcnt lgkmcnt(5)\n\t"
    "v_mfma_f32_16x16x32_bf16 v[56:59], v[48:51], v[16:19], v[56:59]\n\t"
    "v_and_or_b32 v4, v7, 3, 8\n\t"
    "v_lshrrev_b32 v6, 2, v7\n\t"
    "s_waitcnt lgkmcnt(4)\n\t"
    "v_mfma_f32_16x16x32_bf16 v[56:59], v[52:55], v[20:23], v[56:59]\n\t"
    "v_xor_b32 v4, v4, v6\n\t"
    "v_lshl_or_b32 v4, v4, 4, %[pb]\n\t"
    "v_lshl_or_b32 v4, v6, 8, v4\n\t"
    "v_xor_b32 v5, 64, v4\n\t"
    "ds_read_b64_tr_b16 v[48:49], v4 offset:40960\n\t"
    "ds_read_b64_tr_b16 v[50:51], v4 offset:45056\n\t"
    "ds_read_b64_tr_b16 v[52:53], v4 offset:40968\n\t"
    "ds_read_b64_tr_b16 v[54:55], v4 offset:45064\n\t"
    "v_add_f32 v32, v32, v38\n\t"
    "v_add_f32 v33, v33, v39\n\t"
    "s_waitcnt lgkmcnt(7)\n\t"
    "v_mfma_f32_16x16x32_bf16 v[60:63], v[24:27], v[8:11], v[60:63]\n\t"
    "v_and_b32 v6, 12, v7\n\t"
    "v_and_b32 v4, 3, v7\n\t"
    "s_waitcnt lgkmcnt(6)\n\t"
    "v_mfma_f32_16x16x32_bf16 v[60:63], v[28:31], v[12:15], v[60:63]\n\t"
    "v_lshl_add_u32 v4, v6, 1, v4\n\t"
    "v_lshrrev_b32 v6, 4, v7\n\t"
    "s_waitcnt lgkmcnt(5)\n\t"
    "v_mfma_f32_16x16x32_bf16 v[60:63], v[40:43], v[16:19], v[60:63]\n\t"
    "v_lshl_add_u32 v4, v4, 2, v6\n\t"
    "v_lshlrev_b32 v4, 2, v4\n\t"
    "s_waitcnt lgkmcnt(4)\n\t"
    "v_mfma_f32_16x16x32_bf16 v[60:63], v[44:47], v[20:23], v[60:63]\n\t"
    "v_add_u32 v4, 19456, v4\n\t"
    "ds_read_b64_tr_b16 v[40:41], v5 offset:40960\n\t"
    "ds_read_b64_tr_b16 v[42:43], v5 offset:45056\n\t"
    "ds_read_b64_tr_b16 v[44:45], v5 offset:40968\n\t"
    "ds_read_b64_tr_b16 v[46:47], v5 offset:45064\n\t"
    "ds_read2_b32 v[36:37], v4 offset0:0 offset1:16\n\t"
    "ds_read2_b32 v[38:39], v4 offset0:128 offset1:144\n\t"
    "v_and_b32 v4, 7, v7\n\t"
    "v_xor_b32 v6, v6, v4\n\t"
    "v_max3_f32 v34, v32, v33, %[lo]\n\t"
    "v_max3_f32 v34, v34, v56, v57\n\t"
    "v_max3_f32 v34, v34, v58, v59\n\t"
    "v_max3_f32 v34, v34, v60, v61\n\t"
    "v_max3_f32 v34, v34, v62, v63\n\t"
    "v_mov_b32 v35, v34\n\t"
    "v_lshlrev_b32 v6, 4, v6\n\t"
    "v_and_b32 v4, 15, v7\n\t"
    "v_permlane16_swap_b32 v34, v35\n\t"
    "v_max_f32 v34, v34, v35\n\t"
    "v_mov_b32 v35, v34\n\t"
    "v_lshl_or_b32 v6, v4, 7, v6\n\t"
    "v_xor_b32 v4, 64, v6\n\t"
    "v_permlane32_swap_b32 v34, v35\n\t"
    "v_max_f32 v34, v34, v35\n\t"
    "v_mul_f32 v34, %[nl], v34\n\t"
    "v_fma_f32 v32, v32, %[lsc], v34\n\t"
    "v_fma_f32 v33, v33, %[lsc], v34\n\t"
    "v_fma_f32 v56, v56, %[lsc], v34\n\t"
    "v_fma_f32 v57, v57, %[lsc], v34\n\t"
    "v_fma_f32 v58, v58, %[lsc], v34\n\t"
    "v_fma_f32 v59, v59, %[lsc], v34\n\t"
    "v_fma_f32 v60, v60, %[lsc], v34\n\t"
    "v_fma_f32 v61, v61, %[lsc], v34\n\t"
    "v_fma_f32 v62, v62, %[lsc], v34\n\t"
    "v_fma_f32 v63, v63, %[lsc], v34\n\t"
    "v_exp_f32 v56, v56\n\t"
    "v_exp_f32 v57, v57\n\t"
    "v_exp_f32 v58, v58\n\t"
    "v_exp_f32 v59, v59\n\t"
    "v_exp_f32 v60, v60\n\t"
    "v_exp_f32 v61, v61\n\t"
    "v_exp_f32 v62, v62\n\t"
    "v_exp_f32 v63, v63\n\t"
    "v_exp_f32 v32, v32\n\t"
    "v_exp_f32 v33, v33\n\t"
    "v_cvt_pk_bf16_f32 v24, v56, v57\n\t"
    "v_cvt_pk_bf16_f32 v25, v58, v59\n\t"
    "v_cvt_pk_bf16_f32 v26, v60, v61\n\t"
    "v_cvt_pk_bf16_f32 v27, v62, v63\n\t"
    "v_cvt_pk_bf16_f32 v28, v32, v33\n\t"
    "s_waitcnt lgkmcnt(0)\n\t"
    "v_mfma_f32_16x16x32_bf16 v[8:11], v[48:51], v[24:27], 0\n\t"
    "v_mov_b32 v56, %[one]\n\t"
    "v_mov_b32 v57, %[one]\n\t"
    "v_mfma_f32_16x16x32_bf16 v[12:15], v[52:55], v[24:27], 0\n\t"
    "v_mov_b32 v58, %[one]\n\t"
    "v_mov_b32 v59, %[one]\n\t"
    "v_mfma_f32_16x16x32_bf16 v[16:19], v[40:43], v[24:27], 0\n\t"
    "v_mov_b32 v29, 0\n\t"
    "v_mov_b32 v30, 0\n\t"
    "v_mfma_f32_16x16x32_bf16 v[60:63], v[56:59], v[24:27], 0\n\t"
    "v_mov_b32 v31, v28\n\t"
    "v_mfma_f32_16x16x32_bf16 v[20:23], v[44:47], v[24:27], 0\n\t"
    "v_mfma_f32_16x16x16_bf16 v[8:11], v[36:37], v[28:29], v[8:11]\n\t"
    "v_mfma_f32_16x16x16_bf16 v[12:15], v[36:37], v[30:31], v[12:15]\n\t"
    "s_nop 1\n\t"
    "v_mfma_f32_16x16x16_bf16 v[16:19], v[38:39], v[28:29], v[16:19]\n\t"
    "v_mfma_f32_16x16x16_bf16 v[60:63], v[56:57], v[28:29], v[60:63]\n\t"
    "s_nop 1\n\t"
    "v_mfma_f32_16x16x16_bf16 v[20:23], v[38:39], v[30:31], v[20:23]\n\t"
    "s_nop 4\n\t"
    "v_rcp_f32 v60, v60\n\t"
    "s_nop 0\n\t"
    "v_pk_mul_f32 v[8:9], v[8:9], v[60:61] op_sel_hi:[1,0]\n\t"
    "v_pk_mul_f32 v[10:11], v[10:11], v[60:61] op_sel_hi:[1,0]\n\t"
    "v_pk_mul_f32 v[12:13], v[12:13], v[60:61] op_sel_hi:[1,0]\n\t"
    "v_pk_mul_f32 v[14:15], v[14:15], v[60:61] op_sel_hi:[1,0]\n\t"
    "v_cvt_pk_bf16_f32 v24, v8, v9\n\t"
    "v_cvt_pk_bf16_f32 v25, v10, v11\n\t"
    "v_cvt_pk_bf16_f32 v26, v12, v13\n\t"
    "v_cvt_pk_bf16_f32 v27, v14, v15\n\t"
    "global_store_dwordx4 v6, v[24:27], %[od] offset:2048 sc1\n\t"
    "v_pk_mul_f32 v[16:17], v[16:17], v[60:61] op_sel_hi:[1,0]\n\t"
    "v_pk_mul_f32 v[18:19], v[18:19], v[60:61] op_sel_hi:[1,0]\n\t"
    "v_pk_mul_f32 v[20:21], v[20:21], v[60:61] op_sel_hi:[1,0]\n\t"
    "v_pk_mul_f32 v[22:23], v[22:23], v[60:61] op_sel_hi:[1,0]\n\t"
    "v_cvt_pk_bf16_f32 v28, v16, v17\n\t"
    "v_cvt_pk_bf16_f32 v29, v18, v19\n\t"
    "v_cvt_pk_bf16_f32 v30, v20, v21\n\t"
    "v_cvt_pk_bf16_f32 v31, v22, v23\n\t"
    "global_store_dwordx4 v4, v[28:31], %[od] offset:2048 sc1\n\t"
    "s_waitcnt lgkmcnt(0)\n\t"
    "s_nop 0"
    :
    : [pb] "s"(pb), [od] "s"(od), [neg] "s"(-3.0e38f), [lo] "s"(-1.0e38f), [one] "s"(0x3F803F80u), [lsc] "s"(LSC), [nl] "s"(-LSC), "{v[64:95]}"(W.w0), "{v[96:127]}"(W.w1), "{v[128:159]}"(W.w2), "{v[160:191]}"(W.w3), "{v[192:223]}"(W.w4), "{v[224:255]}"(W.w5)
    : "v4", "v5", "v6", "v7", "v8", "v9", "v10", "v11", "v12", "v13", "v14", "v15", "v16", "v17", "v18", "v19", "v20", "v21", "v22", "v23", "v24", "v25", "v26", "v27", "v28", "v29", "v30", "v31", "v32", "v33", "v34", "v35", "v36", "v37", "v38", "v39", "v40", "v41", "v42", "v43", "v44", "v45", "v46", "v47", "v48", "v49", "v50", "v51", "v52", "v53", "v54", "v55", "v56", "v57", "v58", "v59", "v60", "v61", "v62", "v63", "memory");
}
template <int I> __device__ __forceinline__ void qk_wload(u32x32& w, unsigned a0, unsigned a1);
template <> __device__ __forceinline__ void qk_wload<0>(u32x32& w, unsigned a0, unsigned a1) {
  asm volatile("ds_read_b64_tr_b16 v[64:65], %1 offset:0\n\t"
    "ds_read_b64_tr_b16 v[66:67], %1 offset:512\n\t"
    "ds_read_b64_tr_b16 v[68:69], %1 offset:8192\n\t"
    "ds_read_b64_tr_b16 v[70:71], %1 offset:8704\n\t"
    "ds_read_b64_tr_b16 v[72:73], %2 offset:0\n\t"
    "ds_read_b64_tr_b16 v[74:75], %2 offset:512\n\t"
    "ds_read_b64_tr_b16 v[76:77], %2 offset:8192\n\t"
    "ds_read_b64_tr_b16 v[78:79], %2 offset:8704\n\t"
    "s_waitcnt lgkmcnt(0)\n\t"
    "ds_read_b64_tr_b16 v[80:81], %1 offset:16384\n\t"
    "ds_read_b64_tr_b16 v[82:83], %1 offset:16896\n\t"
    "ds_read_b64_tr_b16 v[84:85], %1 offset:24576\n\t"
    "ds_read_b64_tr_b16 v[86:87], %1 offset:25088\n\t"
    "ds_read_b64_tr_b16 v[88:89], %2 offset:16384\n\t"
    "ds_read_b64_tr_b16 v[90:91], %2 offset:16896\n\t"
    "ds_read_b64_tr_b16 v[92:93], %2 offset:24576\n\t"
    "ds_read_b64_tr_b16 v[94:95], %2 offset:25088\n\t"
    "s_waitcnt lgkmcnt(0)"
    : "=&{v[64:95]}"(w) : "v"(a0), "v"(a1) : "memory");
}
template <> __device__ __forceinline__ void qk_wload<1>(u32x32& w, unsigned a0, unsigned a1) {
  asm volatile("ds_read_b64_tr_b16 v[96:97], %1 offset:0\n\t"
    "ds_read_b64_tr_b16 v[98:99], %1 offset:512\n\t"
    "ds_read_b64_tr_b16 v[100:101], %1 offset:8192\n\t"
    "ds_read_b64_tr_b16 v[102:103], %1 offset:8704\n\t"
    "ds_read_b64_tr_b16 v[104:105], %2 offset:0\n\t"
    "ds_read_b64_tr_b16 v[106:107], %2 offset:512\n\t"
    "ds_read_b64_tr_b16 v[108:109], %2 offset:8192\n\t"
    "ds_read_b64_tr_b16 v[110:111], %2 offset:8704\n\t"
    "s_waitcnt lgkmcnt(0)\n\t"
    "ds_read_b64_tr_b16 v[112:113], %1 offset:16384\n\t"
    "ds_read_b64_tr_b16 v[114:115], %1 offset:16896\n\t"
    "ds_read_b64_tr_b16 v[116:117], %1 offset:24576\n\t"
    "ds_read_b64_tr_b16 v[118:119], %1 offset:25088\n\t"
    "ds_read_b64_tr_b16 v[120:121], %2 offset:16384\n\t"
    "ds_read_b64_tr_b16 v[122:123], %2 offset:16896\n\t"
    "ds_read_b64_tr_b16 v[124:125], %2 offset:24576\n\t"
    "ds_read_b64_tr_b16 v[126:127], %2 offset:25088\n\t"
    "s_waitcnt lgkmcnt(0)"
    : "=&{v[96:127]}"(w) : "v"(a0), "v"(a1) : "memory");
}
template <> __device__ __forceinline__ void qk_wload<2>(u32x32& w, unsigned a0, unsigned a1) {
  asm volatile("ds_read_b64_tr_b16 v[128:129], %1 offset:0\n\t"
    "ds_read_b64_tr_b16 v[130:131], %1 offset:512\n\t"
    "ds_read_b64_tr_b16 v[132:133], %1 offset:8192\n\t"
    "ds_read_b64_tr_b16 v[134:135], %1 offset:8704\n\t"
    "ds_read_b64_tr_b16 v[136:137], %2 offset:0\n\t"
    "ds_read_b64_tr_b16 v[138:139], %2 offset:512\n\t"
    "ds_read_b64_tr_b16 v[140:141], %2 offset:8192\n\t"
    "ds_read_b64_tr_b16 v[142:143], %2 offset:8704\n\t"
    "s_waitcnt lgkmcnt(0)\n\t"
    "ds_read_b64_tr_b16 v[144:145], %1 offset:16384\n\t"
    "ds_read_b64_tr_b16 v[146:147], %1 offset:16896\n\t"
    "ds_read_b64_tr_b16 v[148:149], %1 offset:24576\n\t"
    "ds_read_b64_tr_b16 v[150:151], %1 offset:25088\n\t"
    "ds_read_b64_tr_b16 v[152:153], %2 offset:16384\n\t"
    "ds_read_b64_tr_b16 v[154:155], %2 offset:16896\n\t"
    "ds_read_b64_tr_b16 v[156:157], %2 offset:24576\n\t"
    "ds_read_b64_tr_b16 v[158:159], %2 offset:25088\n\t"
    "s_waitcnt lgkmcnt(0)"
    : "=&{v[128:159]}"(w) : "v"(a0), "v"(a1) : "memory");
}
template <> __device__ __forceinline__ void qk_wload<3>(u32x32& w, unsigned a0, unsigned a1) {
  asm volatile("ds_read_b64_tr_b16 v[160:161], %1 offset:0\n\t"
    "ds_read_b64_tr_b16 v[162:163], %1 offset:512\n\t"
    "ds_read_b64_tr_b16 v[164:165], %1 offset:8192\n\t"
    "ds_read_b64_tr_b16 v[166:167], %1 offset:8704\n\t"
    "ds_read_b64_tr_b16 v[168:169], %2 offset:0\n\t"
    "ds_read_b64_tr_b16 v[170:171], %2 offset:512\n\t"
    "ds_read_b64_tr_b16 v[172:173], %2 offset:8192\n\t"
    "ds_read_b64_tr_b16 v[174:175], %2 offset:8704\n\t"
    "s_waitcnt lgkmcnt(0)\n\t"
    "ds_read_b64_tr_b16 v[176:177], %1 offset:16384\n\t"
    "ds_read_b64_tr_b16 v[178:179], %1 offset:16896\n\t"
    "ds_read_b64_tr_b16 v[180:181], %1 offset:24576\n\t"
    "ds_read_b64_tr_b16 v[182:183], %1 offset:25088\n\t"
    "ds_read_b64_tr_b16 v[184:185], %2 offset:16384\n\t"
    "ds_read_b64_tr_b16 v[186:187], %2 offset:16896\n\t"
    "ds_read_b64_tr_b16 v[188:189], %2 offset:24576\n\t"
    "ds_read_b64_tr_b16 v[190:191], %2 offset:25088\n\t"
    "s_waitcnt lgkmcnt(0)"
    : "=&{v[160:191]}"(w) : "v"(a0), "v"(a1) : "memory");
}
template <> __device__ __forceinline__ void qk_wload<4>(u32x32& w, unsigned a0, unsigned a1) {
  asm volatile("ds_read_b64_tr_b16 v[192:193], %1 offset:0\n\t"
    "ds_read_b64_tr_b16 v[194:195], %1 offset:512\n\t"
    "ds_read_b64_tr_b16 v[196:197], %1 offset:8192\n\t"
    "ds_read_b64_tr_b16 v[198:199], %1 offset:8704\n\t"
    "ds_read_b64_tr_b16 v[200:201], %2 offset:0\n\t"
    "ds_read_b64_tr_b16 v[202:203], %2 offset:512\n\t"
    "ds_read_b64_tr_b16 v[204:205], %2 offset:8192\n\t"
    "ds_read_b64_tr_b16 v[206:207], %2 offset:8704\n\t"
    "s_waitcnt lgkmcnt(0)\n\t"
    "ds_read_b64_tr_b16 v[208:209], %1 offset:16384\n\t"
    "ds_read_b64_tr_b16 v[210:211], %1 offset:16896\n\t"
    "ds_read_b64_tr_b16 v[212:213], %1 offset:24576\n\t"
    "ds_read_b64_tr_b16 v[214:215], %1 offset:25088\n\t"
    "ds_read_b64_tr_b16 v[216:217], %2 offset:16384\n\t"
    "ds_read_b64_tr_b16 v[218:219], %2 offset:16896\n\t"
    "ds_read_b64_tr_b16 v[220:221], %2 offset:24576\n\t"
    "ds_read_b64_tr_b16 v[222:223], %2 offset:25088\n\t"
    "s_waitcnt lgkmcnt(0)"
    : "=&{v[192:223]}"(w) : "v"(a0), "v"(a1) : "memory");
}
template <> __device__ __forceinline__ void qk_wload<5>(u32x32& w, unsigned a0, unsigned a1) {
  asm volatile("ds_read_b64_tr_b16 v[224:225], %1 offset:0\n\t"
    "ds_read_b64_tr_b16 v[226:227], %1 offset:512\n\t"
    "ds_read_b64_tr_b16 v[228:229], %1 offset:8192\n\t"
    "ds_read_b64_tr_b16 v[230:231], %1 offset:8704\n\t"
    "ds_read_b64_tr_b16 v[232:233], %2 offset:0\n\t"
    "ds_read_b64_tr_b16 v[234:235], %2 offset:512\n\t"
    "ds_read_b64_tr_b16 v[236:237], %2 offset:8192\n\t"
    "ds_read_b64_tr_b16 v[238:239], %2 offset:8704\n\t"
    "s_waitcnt lgkmcnt(0)\n\t"
    "ds_read_b64_tr_b16 v[240:241], %1 offset:16384\n\t"
    "ds_read_b64_tr_b16 v[242:243], %1 offset:16896\n\t"
    "ds_read_b64_tr_b16 v[244:245], %1 offset:24576\n\t"
    "ds_read_b64_tr_b16 v[246:247], %1 offset:25088\n\t"
    "ds_read_b64_tr_b16 v[248:249], %2 offset:16384\n\t"
    "ds_read_b64_tr_b16 v[250:251], %2 offset:16896\n\t"
    "ds_read_b64_tr_b16 v[252:253], %2 offset:24576\n\t"
    "ds_read_b64_tr_b16 v[254:255], %2 offset:25088\n\t"
    "s_waitcnt lgkmcnt(0)"
    : "=&{v[224:255]}"(w) : "v"(a0), "v"(a1) : "memory");
}
#define OREP 2
#define AREP 2
constexpr int V_P1 = 131072, V_PV = 149504, V_P2 = 157696;
struct VRW { u32x32 k0, k1, o0, o1, o2, o3; };
__device__ __forceinline__ void vr_v0(const VRW& W, const char* ag, const char* vd, unsigned wl, unsigned wa, unsigned wb, unsigned ra, unsigned& cap, unsigned& sp, unsigned long long& tc, unsigned long long* ts) {
  asm volatile("s_mov_b32 %[sp], 0\n\t"
    "v_mbcnt_lo_u32_b32 v20, -1, 0\n\t"
    "v_mbcnt_hi_u32_b32 v20, -1, v20\n\t"
    "v_lshlrev_b32 v21, 4, v20\n\t"
    "global_load_dwordx4 v[24:27], v21, %[ag] sc1\n\t"
    "global_load_dwordx4 v[28:31], v21, %[ag] offset:1024 sc1\n\t"
    "s_memtime %[tc]\n\t"
    "v_lshl_add_u32 v6, v20, 4, %[wl]\n\t"
    "ds_read_b128 v[32:35], v6 offset:0\n\t"
    "ds_read_b128 v[36:39], v6 offset:1024\n\t"
    "ds_read_b128 v[40:43], v6 offset:2048\n\t"
    "ds_read_b128 v[44:47], v6 offset:3072\n\t"
    "ds_read_b128 v[48:51], v6 offset:4096\n\t"
    "ds_read_b128 v[52:55], v6 offset:5120\n\t"
    "ds_read_b128 v[56:59], v6 offset:6144\n\t"
    "ds_read_b128 v[60:63], v6 offset:7168\n\t"
    "s_setprio 2\n\t"
    "v_and_b32 v6, 2, v20\n\t"
    "v_mul_u32_u24 v6, 0x5555, v6\n\t"
    "v_or_b32 v4, %[c44], v6\n\t"
    "v_and_b32 v6, 15, v20\n\t"
    "v_lshl_add_u32 v5, v6, 2, %[wa]\n\t"
    "v_and_b32 v6, 48, v20\n\t"
    "v_mad_u32_u24 v5, v6, 36, v5\n\t"
    "v_mul_u32_u24 v7, 36, v6\n\t"
    "v_and_b32 v6, 4, v20\n\t"
    "v_mul_u32_u24 v6, 72, v6\n\t"
    "v_add_u32 v7, v7, v6\n\t"
    "v_and_b32 v6, 8, v20\n\t"
    "v_lshl_add_u32 v7, v6, 4, v7\n\t"
    "v_and_b32 v6, 3, v20\n\t"
    "v_lshl_add_u32 v7, v6, 3, v7\n\t"
    "v_add_u32 v7, %[ra], v7\n\t"
    "3:\n\t"
    "v_mov_b32 v8, 0\n\t"
    "v_mov_b32 v9, 0\n\t"
    "v_mov_b32 v10, 0\n\t"
    "v_mov_b32 v11, 0\n\t"
    "v_mov_b32 v12, 0\n\t"
    "v_mov_b32 v13, 0\n\t"
    "v_mov_b32 v14, 0\n\t"
    "v_mov_b32 v15, 0\n\t"
    "v_mov_b32 v16, 0\n\t"
    "v_mov_b32 v17, 0\n\t"
    "v_mov_b32 v18, 0\n\t"
    "v_mov_b32 v19, 0\n\t"
    "v_mov_b32 v20, 0\n\t"
    "v_mov_b32 v21, 0\n\t"
    "v_mov_b32 v22, 0\n\t"
    "v_mov_b32 v23, 0\n\t"
    "s_waitcnt vmcnt(0) lgkmcnt(0)\n\t"
    "v_smfmac_f32_16x16x64_bf16 v[8:11], v[24:27], v[64:71], v4\n\t"
    "v_smfmac_f32_16x16x64_bf16 v[8:11], v[28:31], v[72:79], v4\n\t"
    "v_max3_u32 v6, v24, v25, v26\n\t"
    "v_max3_u32 v6, v6, v27, v28\n\t"
    "v_smfmac_f32_16x16x64_bf16 v[12:15], v[24:27], v[80:87], v4\n\t"
    "v_max3_u32 v6, v6, v29, v30\n\t"
    "v_max_u32 v6, v6, v31\n\t"
    "v_smfmac_f32_16x16x64_bf16 v[12:15], v[28:31], v[88:95], v4\n\t"
    "v_cmp_eq_u32 vcc, -1, v6\n\t"
    "s_cbranch_vccnz 1f\n\t"
    "2:\n\t"
    "v_smfmac_f32_16x16x64_bf16 v[16:19], v[24:27], v[96:103], v4\n\t"
    "v_add_f32 v8, v8, v10\n\t"
    "v_add_f32 v9, v9, v11\n\t"
    "ds_write2_b32 v5, v8, v9 offset0:0 offset1:72\n\t"
    "v_smfmac_f32_16x16x64_bf16 v[16:19], v[28:31], v[104:111], v4\n\t"
    "v_mov_b32 v8, 0\n\t"
    "v_mov_b32 v9, 0\n\t"
    "v_smfmac_f32_16x16x64_bf16 v[20:23], v[24:27], v[112:119], v4\n\t"
    "v_add_f32 v12, v12, v14\n\t"
    "v_add_f32 v13, v13, v15\n\t"
    "ds_write2_b32 v5, v12, v13 offset0:16 offset1:88\n\t"
    "v_smfmac_f32_16x16x64_bf16 v[20:23], v[28:31], v[120:127], v4\n\t"
    "v_mov_b32 v10, 0\n\t"
    "v_mov_b32 v11, 0\n\t"
    "v_add_f32 v16, v16, v18\n\t"
    "v_add_f32 v17, v17, v19\n\t"
    "ds_write2_b32 v5, v16, v17 offset0:32 offset1:104\n\t"
    "v_mov_b32 v12, 0\n\t"
    "v_mov_b32 v13, 0\n\t"
    "v_mov_b32 v14, 0\n\t"
    "v_mov_b32 v15, 0\n\t"
    "v_add_f32 v20, v20, v22\n\t"
    "v_add_f32 v21, v21, v23\n\t"
    "ds_write2_b32 v5, v20, v21 offset0:48 offset1:120\n\t"
    "s_setprio 1\n\t"
    "v_smfmac_f32_16x16x64_bf16 v[8:11], v[24:27], v[32:39], v4\n\t"
    "v_smfmac_f32_16x16x64_bf16 v[8:11], v[28:31], v[40:47], v4\n\t"
    "v_smfmac_f32_16x16x64_bf16 v[12:15], v[24:27], v[48:55], v4\n\t"
    "v_smfmac_f32_16x16x64_bf16 v[12:15], v[28:31], v[56:63], v4\n\t"
    "v_mbcnt_lo_u32_b32 v17, -1, 0\n\t"
    "v_mbcnt_hi_u32_b32 v17, -1, v17\n\t"
    "v_lshl_add_u32 v16, v17, 3, %[wb]\n\t"
    "v_lshlrev_b32 v17, 2, v17\n\t"
    "s_nop 1\n\t"
    "v_add_f32 v8, v8, v10\n\t"
    "v_add_f32 v9, v9, v11\n\t"
    "v_add_f32 v12, v12, v14\n\t"
    "v_add_f32 v13, v13, v15\n\t"
    "ds_write2st64_b64 v16, v[8:9], v[12:13] offset1:1\n\t"
    "s_setprio 3\n\t"
    "s_waitcnt lgkmcnt(1)\n\t"
    "s_barrier\n\t"
    "ds_read_b64 v[32:33], v7 offset:0\n\t"
    "ds_read_b64 v[34:35], v7 offset:2304\n\t"
    "ds_read_b64 v[36:37], v7 offset:4608\n\t"
    "ds_read_b64 v[38:39], v7 offset:6912\n\t"
    "ds_read_b64 v[40:41], v7 offset:9216\n\t"
    "ds_read_b64 v[42:43], v7 offset:11520\n\t"
    "ds_read_b64 v[44:45], v7 offset:13824\n\t"
    "ds_read_b64 v[46:47], v7 offset:16128\n\t"
    "s_waitcnt lgkmcnt(6)\n\t"
    "v_pk_add_f32 v[32:33], v[32:33], v[34:35]\n\t"
    "s_waitcnt lgkmcnt(4)\n\t"
    "v_pk_add_f32 v[36:37], v[36:37], v[38:39]\n\t"
    "v_pk_add_f32 v[32:33], v[32:33], v[36:37]\n\t"
    "s_waitcnt lgkmcnt(2)\n\t"
    "v_pk_add_f32 v[40:41], v[40:41], v[42:43]\n\t"
    "s_waitcnt lgkmcnt(0)\n\t"
    "v_pk_add_f32 v[44:45], v[44:45], v[46:47]\n\t"
    "v_pk_add_f32 v[40:41], v[40:41], v[44:45]\n\t"
    "v_pk_add_f32 v[32:33], v[32:33], v[40:41]\n\t"
    "v_cvt_pk_bf16_f32 v6, v32, v33\n\t"
    "v_min_u32 v6, -2, v6\n\t"
    "global_store_dword v17, v6, %[vd] sc1\n\t"
    "s_barrier\n\t"
    "s_setprio 2\n\t"
    "s_waitcnt lgkmcnt(0)\n\t"
    "s_branch 9f\n\t"
    "1:\n\t"
    "s_add_u32 %[sp], %[sp], 1\n\t"
    "s_cmp_ge_u32 %[sp], %[cap]\n\t"
    "s_cbranch_scc1 7f\n\t"
    "v_mbcnt_lo_u32_b32 v20, -1, 0\n\t"
    "v_mbcnt_hi_u32_b32 v20, -1, v20\n\t"
    "v_lshlrev_b32 v21, 4, v20\n\t"
    "global_load_dwordx4 v[24:27], v21, %[ag] sc1\n\t"
    "global_load_dwordx4 v[28:31], v21, %[ag] offset:1024 sc1\n\t"
    "s_waitcnt lgkmcnt(0)\n\t"
    "s_memtime %[tc]\n\t"
    "s_nop 7\n\t"
    "s_branch 3b\n\t"
    "7:\n\t"
    "s_mov_b32 %[cap], 0\n\t"
    "s_branch 2b\n\t"
    "9:"
    : [tc] "=&s"(tc), [sp] "=&s"(sp), [cap] "+s"(cap)
    : [ag] "s"(ag), [vd] "s"(vd), [wl] "s"(wl), [wa] "s"(wa), [wb] "s"(wb), [ra] "s"(ra), [c44] "s"(0x4444u), "{v[64:95]}"(W.k0), "{v[96:127]}"(W.k1), "{v[128:159]}"(W.o0), "{v[160:191]}"(W.o1), "{v[192:223]}"(W.o2), "{v[224:255]}"(W.o3)
    : "v4", "v5", "v6", "v7", "v8", "v9", "v10", "v11", "v12", "v13", "v14", "v15", "v16", "v17", "v18", "v19", "v20", "v21", "v22", "v23", "v24", "v25", "v26", "v27", "v28", "v29", "v30", "v31", "v32", "v33", "v34", "v35", "v36", "v37", "v38", "v39", "v40", "v41", "v42", "v43", "v44", "v45", "v46", "v47", "v48", "v49", "v50", "v51", "v52", "v53", "v54", "v55", "v56", "v57", "v58", "v59", "v60", "v61", "v62", "v63", "vcc", "scc", "memory");
}
__device__ __forceinline__ void vr_v1(const VRW& W, const char* ag, const char* vd, unsigned wl, unsigned wa, unsigned wb, unsigned ra, unsigned& cap, unsigned& sp, unsigned long long& tc, unsigned long long* ts) {
  asm volatile("s_mov_b32 %[sp], 0\n\t"
    "v_mbcnt_lo_u32_b32 v20, -1, 0\n\t"
    "v_mbcnt_hi_u32_b32 v20, -1, v20\n\t"
    "v_lshlrev_b32 v21, 4, v20\n\t"
    "global_load_dwordx4 v[24:27], v21, %[ag] sc1\n\t"
    "global_load_dwordx4 v[28:31], v21, %[ag] offset:1024 sc1\n\t"
    "s_memtime %[tc]\n\t"
    "v_lshl_add_u32 v6, v20, 4, %[wl]\n\t"
    "ds_read_b128 v[32:35], v6 offset:0\n\t"
    "ds_read_b128 v[36:39], v6 offset:1024\n\t"
    "ds_read_b128 v[40:43], v6 offset:2048\n\t"
    "ds_read_b128 v[44:47], v6 offset:3072\n\t"
    "ds_read_b128 v[48:51], v6 offset:4096\n\t"
    "ds_read_b128 v[52:55], v6 offset:5120\n\t"
    "ds_read_b128 v[56:59], v6 offset:6144\n\t"
    "ds_read_b128 v[60:63], v6 offset:7168\n\t"
    "s_setprio 3\n\t"
    "v_and_b32 v6, 2, v20\n\t"
    "v_mul_u32_u24 v6, 0x5555, v6\n\t"
    "v_or_b32 v4, %[c44], v6\n\t"
    "v_and_b32 v6, 15, v20\n\t"
    "v_lshl_add_u32 v5, v6, 2, %[wa]\n\t"
    "v_and_b32 v6, 48, v20\n\t"
    "v_mad_u32_u24 v5, v6, 36, v5\n\t"
    "v_and_b32 v6, 32, v20\n\t"
    "v_mul_u32_u24 v7, 120, v6\n\t"
    "v_lshl_add_u32 v7, v20, 3, v7\n\t"
    "v_add_u32 v7, %[ra], v7\n\t"
    "3:\n\t"
    "v_mov_b32 v8, 0\n\t"
    "v_mov_b32 v9, 0\n\t"
    "v_mov_b32 v10, 0\n\t"
    "v_mov_b32 v11, 0\n\t"
    "v_mov_b32 v12, 0\n\t"
    "v_mov_b32 v13, 0\n\t"
    "v_mov_b32 v14, 0\n\t"
    "v_mov_b32 v15, 0\n\t"
    "v_mov_b32 v16, 0\n\t"
    "v_mov_b32 v17, 0\n\t"
    "v_mov_b32 v18, 0\n\t"
    "v_mov_b32 v19, 0\n\t"
    "v_mov_b32 v20, 0\n\t"
    "v_mov_b32 v21, 0\n\t"
    "v_mov_b32 v22, 0\n\t"
    "v_mov_b32 v23, 0\n\t"
    "s_waitcnt vmcnt(0) lgkmcnt(0)\n\t"
    "v_smfmac_f32_16x16x64_bf16 v[8:11], v[24:27], v[64:71], v4\n\t"
    "v_smfmac_f32_16x16x64_bf16 v[8:11], v[28:31], v[72:79], v4\n\t"
    "v_max3_u32 v6, v24, v25, v26\n\t"
    "v_max3_u32 v6, v6, v27, v28\n\t"
    "v_smfmac_f32_16x16x64_bf16 v[12:15], v[24:27], v[80:87], v4\n\t"
    "v_max3_u32 v6, v6, v29, v30\n\t"
    "v_max_u32 v6, v6, v31\n\t"
    "v_smfmac_f32_16x16x64_bf16 v[12:15], v[28:31], v[88:95], v4\n\t"
    "v_cmp_eq_u32 vcc, -1, v6\n\t"
    "s_cbranch_vccnz 1f\n\t"
    "2:\n\t"
    "v_smfmac_f32_16x16x64_bf16 v[16:19], v[24:27], v[96:103], v4\n\t"
    "v_add_f32 v8, v8, v10\n\t"
    "v_add_f32 v9, v9, v11\n\t"
    "ds_write2_b32 v5, v8, v9 offset0:0 offset1:72\n\t"
    "v_smfmac_f32_16x16x64_bf16 v[16:19], v[28:31], v[104:111], v4\n\t"
    "v_mov_b32 v8, 0\n\t"
    "v_mov_b32 v9, 0\n\t"
    "v_smfmac_f32_16x16x64_bf16 v[20:23], v[24:27], v[112:119], v4\n\t"
    "v_add_f32 v12, v12, v14\n\t"
    "v_add_f32 v13, v13, v15\n\t"
    "ds_write2_b32 v5, v12, v13 offset0:16 offset1:88\n\t"
    "v_smfmac_f32_16x16x64_bf16 v[20:23], v[28:31], v[120:127], v4\n\t"
    "v_mov_b32 v10, 0\n\t"
    "v_mov_b32 v11, 0\n\t"
    "v_add_f32 v16, v16, v18\n\t"
    "v_add_f32 v17, v17, v19\n\t"
    "ds_write2_b32 v5, v16, v17 offset0:32 offset1:104\n\t"
    "v_mov_b32 v12, 0\n\t"
    "v_mov_b32 v13, 0\n\t"
    "v_mov_b32 v14, 0\n\t"
    "v_mov_b32 v15, 0\n\t"
    "v_add_f32 v20, v20, v22\n\t"
    "v_add_f32 v21, v21, v23\n\t"
    "ds_write2_b32 v5, v20, v21 offset0:48 offset1:120\n\t"
    "s_waitcnt lgkmcnt(0)\n\t"
    "s_barrier\n\t"
    "v_smfmac_f32_16x16x64_bf16 v[8:11], v[24:27], v[32:39], v4\n\t"
    "v_smfmac_f32_16x16x64_bf16 v[8:11], v[28:31], v[40:47], v4\n\t"
    "v_smfmac_f32_16x16x64_bf16 v[12:15], v[24:27], v[48:55], v4\n\t"
    "v_smfmac_f32_16x16x64_bf16 v[12:15], v[28:31], v[56:63], v4\n\t"
    "v_mbcnt_lo_u32_b32 v17, -1, 0\n\t"
    "v_mbcnt_hi_u32_b32 v17, -1, v17\n\t"
    "v_lshl_add_u32 v16, v17, 3, %[wb]\n\t"
    "v_lshlrev_b32 v17, 2, v17\n\t"
    "s_nop 1\n\t"
    "v_add_f32 v8, v8, v10\n\t"
    "v_add_f32 v9, v9, v11\n\t"
    "v_add_f32 v12, v12, v14\n\t"
    "v_add_f32 v13, v13, v15\n\t"
    "ds_write2st64_b64 v16, v[8:9], v[12:13] offset1:1\n\t"
    "s_waitcnt lgkmcnt(0)\n\t"
    "s_barrier\n\t"
    "ds_read_b64 v[32:33], v7 offset:0\n\t"
    "ds_read_b64 v[34:35], v7 offset:1024\n\t"
    "ds_read_b64 v[36:37], v7 offset:2048\n\t"
    "ds_read_b64 v[38:39], v7 offset:3072\n\t"
    "s_waitcnt lgkmcnt(2)\n\t"
    "v_pk_add_f32 v[32:33], v[32:33], v[34:35]\n\t"
    "s_waitcnt lgkmcnt(0)\n\t"
    "v_pk_add_f32 v[36:37], v[36:37], v[38:39]\n\t"
    "v_pk_add_f32 v[32:33], v[32:33], v[36:37]\n\t"
    "s_nop 1\n\t"
    "v_permlane32_swap_b32 v32, v34\n\t"
    "v_permlane32_swap_b32 v33, v35\n\t"
    "s_mov_b32 exec_hi, 0\n\t"
    "v_pk_add_f32 v[32:33], v[32:33], v[34:35]\n\t"
    "v_cvt_pk_bf16_f32 v6, v32, v33\n\t"
    "v_min_u32 v6, -2, v6\n\t"
    "global_store_dword v17, v6, %[vd] sc1\n\t"
    "s_mov_b32 exec_hi, -1\n\t"
    "s_setprio 2\n\t"
    "s_waitcnt lgkmcnt(0)\n\t"
    "s_branch 9f\n\t"
    "1:\n\t"
    "s_add_u32 %[sp], %[sp], 1\n\t"
    "s_cmp_ge_u32 %[sp], %[cap]\n\t"
    "s_cbranch_scc1 7f\n\t"
    "v_mbcnt_lo_u32_b32 v20, -1, 0\n\t"
    "v_mbcnt_hi_u32_b32 v20, -1, v20\n\t"
    "v_lshlrev_b32 v21, 4, v20\n\t"
    "global_load_dwordx4 v[24:27], v21, %[ag] sc1\n\t"
    "global_load_dwordx4 v[28:31], v21, %[ag] offset:1024 sc1\n\t"
    "s_waitcnt lgkmcnt(0)\n\t"
    "s_memtime %[tc]\n\t"
    "s_nop 7\n\t"
    "s_branch 3b\n\t"
    "7:\n\t"
    "s_mov_b32 %[cap], 0\n\t"
    "s_branch 2b\n\t"
    "9:"
    : [tc] "=&s"(tc), [sp] "=&s"(sp), [cap] "+s"(cap)
    : [ag] "s"(ag), [vd] "s"(vd), [wl] "s"(wl), [wa] "s"(wa), [wb] "s"(wb), [ra] "s"(ra), [c44] "s"(0x4444u), "{v[64:95]}"(W.k0), "{v[96:127]}"(W.k1), "{v[128:159]}"(W.o0), "{v[160:191]}"(W.o1), "{v[192:223]}"(W.o2), "{v[224:255]}"(W.o3)
    : "v4", "v5", "v6", "v7", "v8", "v9", "v10", "v11", "v12", "v13", "v14", "v15", "v16", "v17", "v18", "v19", "v20", "v21", "v22", "v23", "v24", "v25", "v26", "v27", "v28", "v29", "v30", "v31", "v32", "v33", "v34", "v35", "v36", "v37", "v38", "v39", "v40", "v41", "v42", "v43", "v44", "v45", "v46", "v47", "v48", "v49", "v50", "v51", "v52", "v53", "v54", "v55", "v56", "v57", "v58", "v59", "v60", "v61", "v62", "v63", "vcc", "scc", "memory");
}
__device__ __forceinline__ void vr_r(const VRW& W, const char* os, const char* ad, unsigned wa, unsigned& cap, unsigned& sp, unsigned long long* ts) {
  asm volatile("v_mbcnt_lo_u32_b32 v4, -1, 0\n\t"
    "v_mbcnt_hi_u32_b32 v4, -1, v4\n\t"
    "v_lshlrev_b32 v5, 4, v4\n\t"
    "global_load_dwordx4 v[12:15], v5, %[os] offset:0 sc1\n\t"
    "global_load_dwordx4 v[16:19], v5, %[os] offset:1024 sc1\n\t"
    "global_load_dwordx4 v[20:23], v5, %[os] offset:2048 sc1\n\t"
    "global_load_dwordx4 v[24:27], v5, %[os] offset:3072 sc1\n\t"
    "s_mov_b32 %[sp], 0\n\t"
    "v_and_b32 v10, 1, v4\n\t"
    "v_mul_u32_u24 v10, 0xaaaa, v10\n\t"
    "v_or_b32 v6, %[c44], v10\n\t"
    "v_and_b32 v10, 15, v4\n\t"
    "v_lshl_add_u32 v7, v10, 2, %[wa]\n\t"
    "v_and_b32 v10, 48, v4\n\t"
    "v_mad_u32_u24 v7, v10, 12, v7\n\t"
    "v_and_b32 v10, 16, v4\n\t"
    "v_mul_u32_u24 v8, 24, v10\n\t"
    "v_and_b32 v10, 4, v4\n\t"
    "v_mad_u32_u24 v8, v10, 48, v8\n\t"
    "v_and_b32 v10, 32, v4\n\t"
    "v_lshl_add_u32 v8, v10, 1, v8\n\t"
    "v_and_b32 v10, 8, v4\n\t"
    "v_lshl_add_u32 v8, v10, 2, v8\n\t"
    "v_and_b32 v10, 3, v4\n\t"
    "v_lshl_add_u32 v8, v10, 3, v8\n\t"
    "v_add_u32 v8, 157696, v8\n\t"
    "v_and_b32 v10, 32, v4\n\t"
    "v_add_u32 v9, v10, v4\n\t"
    "v_lshlrev_b32 v9, 2, v9\n\t"
    "3:\n\t"
    "v_mov_b32 v28, 0\n\t"
    "v_mov_b32 v29, 0\n\t"
    "v_mov_b32 v30, 0\n\t"
    "v_mov_b32 v31, 0\n\t"
    "v_mov_b32 v32, 0\n\t"
    "v_mov_b32 v33, 0\n\t"
    "v_mov_b32 v34, 0\n\t"
    "v_mov_b32 v35, 0\n\t"
    "v_mov_b32 v36, 0\n\t"
    "v_mov_b32 v37, 0\n\t"
    "v_mov_b32 v38, 0\n\t"
    "v_mov_b32 v39, 0\n\t"
    "v_mov_b32 v40, 0\n\t"
    "v_mov_b32 v41, 0\n\t"
    "v_mov_b32 v42, 0\n\t"
    "v_mov_b32 v43, 0\n\t"
    "v_mov_b32 v10, 0\n\t"
    "s_waitcnt vmcnt(3)\n\t"
    "v_smfmac_f32_16x16x64_bf16 v[28:31], v[12:15], v[128:135], v6\n\t"
    "v_max3_u32 v10, v10, v12, v13\n\t"
    "v_smfmac_f32_16x16x64_bf16 v[32:35], v[12:15], v[136:143], v6\n\t"
    "v_max3_u32 v10, v10, v14, v15\n\t"
    "v_smfmac_f32_16x16x64_bf16 v[36:39], v[12:15], v[144:151], v6\n\t"
    "v_smfmac_f32_16x16x64_bf16 v[40:43], v[12:15], v[152:159], v6\n\t"
    "s_waitcnt vmcnt(2)\n\t"
    "v_smfmac_f32_16x16x64_bf16 v[28:31], v[16:19], v[160:167], v6\n\t"
    "v_max3_u32 v10, v10, v16, v17\n\t"
    "v_smfmac_f32_16x16x64_bf16 v[32:35], v[16:19], v[168:175], v6\n\t"
    "v_max3_u32 v10, v10, v18, v19\n\t"
    "v_smfmac_f32_16x16x64_bf16 v[36:39], v[16:19], v[176:183], v6\n\t"
    "v_smfmac_f32_16x16x64_bf16 v[40:43], v[16:19], v[184:191], v6\n\t"
    "s_waitcnt vmcnt(1)\n\t"
    "v_smfmac_f32_16x16x64_bf16 v[28:31], v[20:23], v[192:199], v6\n\t"
    "v_max3_u32 v10, v10, v20, v21\n\t"
    "v_smfmac_f32_16x16x64_bf16 v[32:35], v[20:23], v[200:207], v6\n\t"
    "v_max3_u32 v10, v10, v22, v23\n\t"
    "v_smfmac_f32_16x16x64_bf16 v[36:39], v[20:23], v[208:215], v6\n\t"
    "v_smfmac_f32_16x16x64_bf16 v[40:43], v[20:23], v[216:223], v6\n\t"
    "s_waitcnt vmcnt(0)\n\t"
    "v_smfmac_f32_16x16x64_bf16 v[28:31], v[24:27], v[224:231], v6\n\t"
    "v_max3_u32 v10, v10, v24, v25\n\t"
    "v_smfmac_f32_16x16x64_bf16 v[32:35], v[24:27], v[232:239], v6\n\t"
    "v_max3_u32 v10, v10, v26, v27\n\t"
    "v_smfmac_f32_16x16x64_bf16 v[36:39], v[24:27], v[240:247], v6\n\t"
    "v_cmp_eq_u32 vcc, -1, v10\n\t"
    "v_smfmac_f32_16x16x64_bf16 v[40:43], v[24:27], v[248:255], v6\n\t"
    "s_cbranch_vccnz 4f\n\t"
    "5:\n\t"
    "s_cmp_ge_u32 %[wa], 159232\n\t"
    "s_nop 1\n\t"
    "v_add_f32 v28, v28, v29\n\t"
    "v_add_f32 v32, v32, v33\n\t"
    "v_add_f32 v36, v38, v39\n\t"
    "v_add_f32 v28, v28, v36\n\t"
    "v_add_f32 v40, v42, v43\n\t"
    "v_add_f32 v32, v32, v40\n\t"
    "ds_write2_b32 v7, v28, v32 offset1:16\n\t"
    "s_waitcnt lgkmcnt(0)\n\t"
    "s_barrier\n\t"
    "s_cbranch_scc1 9f\n\t"
    "ds_read_b64 v[44:45], v8 offset:0\n\t"
    "ds_read_b64 v[46:47], v8 offset:768\n\t"
    "ds_read_b64 v[48:49], v8 offset:1536\n\t"
    "ds_read_b64 v[50:51], v8 offset:2304\n\t"
    "ds_read_b64 v[52:53], v8 offset:3072\n\t"
    "ds_read_b64 v[54:55], v8 offset:3840\n\t"
    "ds_read_b64 v[56:57], v8 offset:4608\n\t"
    "ds_read_b64 v[58:59], v8 offset:5376\n\t"
    "s_waitcnt lgkmcnt(6)\n\t"
    "v_pk_add_f32 v[44:45], v[44:45], v[46:47]\n\t"
    "s_waitcnt lgkmcnt(4)\n\t"
    "v_pk_add_f32 v[48:49], v[48:49], v[50:51]\n\t"
    "v_pk_add_f32 v[44:45], v[44:45], v[48:49]\n\t"
    "s_waitcnt lgkmcnt(2)\n\t"
    "v_pk_add_f32 v[52:53], v[52:53], v[54:55]\n\t"
    "s_waitcnt lgkmcnt(0)\n\t"
    "v_pk_add_f32 v[56:57], v[56:57], v[58:59]\n\t"
    "v_pk_add_f32 v[52:53], v[52:53], v[56:57]\n\t"
    "v_pk_add_f32 v[44:45], v[44:45], v[52:53]\n\t"
    "v_cvt_pk_bf16_f32 v10, v44, v45\n\t"
    "v_min_u32 v10, -2, v10\n\t"
    "global_store_dword v9, v10, %[ad] sc1\n\t"
    "s_waitcnt lgkmcnt(0)\n\t"
    "s_branch 9f\n\t"
    "4:\n\t"
    "s_add_u32 %[sp], %[sp], 1\n\t"
    "s_cmp_ge_u32 %[sp], %[cap]\n\t"
    "s_cbranch_scc1 7f\n\t"
    "global_load_dwordx4 v[12:15], v5, %[os] offset:0 sc1\n\t"
    "global_load_dwordx4 v[16:19], v5, %[os] offset:1024 sc1\n\t"
    "global_load_dwordx4 v[20:23], v5, %[os] offset:2048 sc1\n\t"
    "global_load_dwordx4 v[24:27], v5, %[os] offset:3072 sc1\n\t"
    "s_nop 3\n\t"
    "s_branch 3b\n\t"
    "7:\n\t"
    "s_mov_b32 %[cap], 0\n\t"
    "s_branch 5b\n\t"
    "9:"
    : [sp] "=&s"(sp), [cap] "+s"(cap)
    : [os] "s"(os), [ad] "s"(ad), [wa] "s"(wa), [c44] "s"(0x4444u), "{v[64:95]}"(W.k0), "{v[96:127]}"(W.k1), "{v[128:159]}"(W.o0), "{v[160:191]}"(W.o1), "{v[192:223]}"(W.o2), "{v[224:255]}"(W.o3)
    : "v4", "v5", "v6", "v7", "v8", "v9", "v10", "v11", "v12", "v13", "v14", "v15", "v16", "v17", "v18", "v19", "v20", "v21", "v22", "v23", "v24", "v25", "v26", "v27", "v28", "v29", "v30", "v31", "v32", "v33", "v34", "v35", "v36", "v37", "v38", "v39", "v40", "v41", "v42", "v43", "v44", "v45", "v46", "v47", "v48", "v49", "v50", "v51", "v52", "v53", "v54", "v55", "v56", "v57", "v58", "v59", "vcc", "scc", "memory");
}
__device__ __forceinline__ void kr_load_k0(VRW& W, unsigned a) {
  asm volatile("ds_read_b64_tr_b16 v[64:65], %[a] offset:0\n\t"
    "ds_read_b64_tr_b16 v[66:67], %[a] offset:256\n\t"
    "ds_read_b64_tr_b16 v[68:69], %[a] offset:4096\n\t"
    "ds_read_b64_tr_b16 v[70:71], %[a] offset:4352\n\t"
    "ds_read_b64_tr_b16 v[80:81], %[a] offset:32\n\t"
    "ds_read_b64_tr_b16 v[82:83], %[a] offset:288\n\t"
    "ds_read_b64_tr_b16 v[84:85], %[a] offset:4128\n\t"
    "ds_read_b64_tr_b16 v[86:87], %[a] offset:4384\n\t"
    "s_waitcnt lgkmcnt(0)\n\t"
    "ds_read_b64_tr_b16 v[96:97], %[a] offset:64\n\t"
    "ds_read_b64_tr_b16 v[98:99], %[a] offset:320\n\t"
    "ds_read_b64_tr_b16 v[100:101], %[a] offset:4160\n\t"
    "ds_read_b64_tr_b16 v[102:103], %[a] offset:4416\n\t"
    "ds_read_b64_tr_b16 v[112:113], %[a] offset:96\n\t"
    "ds_read_b64_tr_b16 v[114:115], %[a] offset:352\n\t"
    "ds_read_b64_tr_b16 v[116:117], %[a] offset:4192\n\t"
    "ds_read_b64_tr_b16 v[118:119], %[a] offset:4448\n\t"
    "s_waitcnt lgkmcnt(0)"
    : "=&{v[64:95]}"(W.k0), "=&{v[96:127]}"(W.k1) : [a] "v"(a) : "memory");
}
__device__ __forceinline__ void kr_load_k1(VRW& W, unsigned a) {
  asm volatile("ds_read_b64_tr_b16 v[72:73], %[a] offset:0\n\t"
    "ds_read_b64_tr_b16 v[74:75], %[a] offset:256\n\t"
    "ds_read_b64_tr_b16 v[76:77], %[a] offset:4096\n\t"
    "ds_read_b64_tr_b16 v[78:79], %[a] offset:4352\n\t"
    "ds_read_b64_tr_b16 v[88:89], %[a] offset:32\n\t"
    "ds_read_b64_tr_b16 v[90:91], %[a] offset:288\n\t"
    "ds_read_b64_tr_b16 v[92:93], %[a] offset:4128\n\t"
    "ds_read_b64_tr_b16 v[94:95], %[a] offset:4384\n\t"
    "s_waitcnt lgkmcnt(0)\n\t"
    "ds_read_b64_tr_b16 v[104:105], %[a] offset:64\n\t"
    "ds_read_b64_tr_b16 v[106:107], %[a] offset:320\n\t"
    "ds_read_b64_tr_b16 v[108:109], %[a] offset:4160\n\t"
    "ds_read_b64_tr_b16 v[110:111], %[a] offset:4416\n\t"
    "ds_read_b64_tr_b16 v[120:121], %[a] offset:96\n\t"
    "ds_read_b64_tr_b16 v[122:123], %[a] offset:352\n\t"
    "ds_read_b64_tr_b16 v[124:125], %[a] offset:4192\n\t"
    "ds_read_b64_tr_b16 v[126:127], %[a] offset:4448\n\t"
    "s_waitcnt lgkmcnt(0)"
    : "+&{v[64:95]}"(W.k0), "+&{v[96:127]}"(W.k1) : [a] "v"(a) : "memory");
}
__device__ __forceinline__ void kr_load_o0(VRW& W, unsigned a) {
  asm volatile("ds_read_b64_tr_b16 v[128:129], %[a] offset:0\n\t"
    "ds_read_b64_tr_b16 v[130:131], %[a] offset:128\n\t"
    "ds_read_b64_tr_b16 v[132:133], %[a] offset:1024\n\t"
    "ds_read_b64_tr_b16 v[134:135], %[a] offset:1152\n\t"
    "ds_read_b64_tr_b16 v[136:137], %[a] offset:32\n\t"
    "ds_read_b64_tr_b16 v[138:139], %[a] offset:160\n\t"
    "ds_read_b64_tr_b16 v[140:141], %[a] offset:1056\n\t"
    "ds_read_b64_tr_b16 v[142:143], %[a] offset:1184\n\t"
    "s_waitcnt lgkmcnt(0)\n\t"
    "ds_read_b64_tr_b16 v[144:145], %[a] offset:4096\n\t"
    "ds_read_b64_tr_b16 v[146:147], %[a] offset:4224\n\t"
    "ds_read_b64_tr_b16 v[148:149], %[a] offset:5120\n\t"
    "ds_read_b64_tr_b16 v[150:151], %[a] offset:5248\n\t"
    "ds_read_b64_tr_b16 v[152:153], %[a] offset:4128\n\t"
    "ds_read_b64_tr_b16 v[154:155], %[a] offset:4256\n\t"
    "ds_read_b64_tr_b16 v[156:157], %[a] offset:5152\n\t"
    "ds_read_b64_tr_b16 v[158:159], %[a] offset:5280\n\t"
    "s_waitcnt lgkmcnt(0)"
    : "=&{v[128:159]}"(W.o0) : [a] "v"(a) : "memory");
}
__device__ __forceinline__ void kr_load_o1(VRW& W, unsigned a) {
  asm volatile("ds_read_b64_tr_b16 v[160:161], %[a] offset:0\n\t"
    "ds_read_b64_tr_b16 v[162:163], %[a] offset:128\n\t"
    "ds_read_b64_tr_b16 v[164:165], %[a] offset:1024\n\t"
    "ds_read_b64_tr_b16 v[166:167], %[a] offset:1152\n\t"
    "ds_read_b64_tr_b16 v[168:169], %[a] offset:32\n\t"
    "ds_read_b64_tr_b16 v[170:171], %[a] offset:160\n\t"
    "ds_read_b64_tr_b16 v[172:173], %[a] offset:1056\n\t"
    "ds_read_b64_tr_b16 v[174:175], %[a] offset:1184\n\t"
    "s_waitcnt lgkmcnt(0)\n\t"
    "ds_read_b64_tr_b16 v[176:177], %[a] offset:4096\n\t"
    "ds_read_b64_tr_b16 v[178:179], %[a] offset:4224\n\t"
    "ds_read_b64_tr_b16 v[180:181], %[a] offset:5120\n\t"
    "ds_read_b64_tr_b16 v[182:183], %[a] offset:5248\n\t"
    "ds_read_b64_tr_b16 v[184:185], %[a] offset:4128\n\t"
    "ds_read_b64_tr_b16 v[186:187], %[a] offset:4256\n\t"
    "ds_read_b64_tr_b16 v[188:189], %[a] offset:5152\n\t"
    "ds_read_b64_tr_b16 v[190:191], %[a] offset:5280\n\t"
    "s_waitcnt lgkmcnt(0)"
    : "=&{v[160:191]}"(W.o1) : [a] "v"(a) : "memory");
}
__device__ __forceinline__ void kr_load_o2(VRW& W, unsigned a) {
  asm volatile("ds_read_b64_tr_b16 v[192:193], %[a] offset:0\n\t"
    "ds_read_b64_tr_b16 v[194:195], %[a] offset:128\n\t"
    "ds_read_b64_tr_b16 v[196:197], %[a] offset:1024\n\t"
    "ds_read_b64_tr_b16 v[198:199], %[a] offset:1152\n\t"
    "ds_read_b64_tr_b16 v[200:201], %[a] offset:32\n\t"
    "ds_read_b64_tr_b16 v[202:203], %[a] offset:160\n\t"
    "ds_read_b64_tr_b16 v[204:205], %[a] offset:1056\n\t"
    "ds_read_b64_tr_b16 v[206:207], %[a] offset:1184\n\t"
    "s_waitcnt lgkmcnt(0)\n\t"
    "ds_read_b64_tr_b16 v[208:209], %[a] offset:4096\n\t"
    "ds_read_b64_tr_b16 v[210:211], %[a] offset:4224\n\t"
    "ds_read_b64_tr_b16 v[212:213], %[a] offset:5120\n\t"
    "ds_read_b64_tr_b16 v[214:215], %[a] offset:5248\n\t"
    "ds_read_b64_tr_b16 v[216:217], %[a] offset:4128\n\t"
    "ds_read_b64_tr_b16 v[218:219], %[a] offset:4256\n\t"
    "ds_read_b64_tr_b16 v[220:221], %[a] offset:5152\n\t"
    "ds_read_b64_tr_b16 v[222:223], %[a] offset:5280\n\t"
    "s_waitcnt lgkmcnt(0)"
    : "=&{v[192:223]}"(W.o2) : [a] "v"(a) : "memory");
}
__device__ __forceinline__ void kr_load_o3(VRW& W, unsigned a) {
  asm volatile("ds_read_b64_tr_b16 v[224:225], %[a] offset:0\n\t"
    "ds_read_b64_tr_b16 v[226:227], %[a] offset:128\n\t"
    "ds_read_b64_tr_b16 v[228:229], %[a] offset:1024\n\t"
    "ds_read_b64_tr_b16 v[230:231], %[a] offset:1152\n\t"
    "ds_read_b64_tr_b16 v[232:233], %[a] offset:32\n\t"
    "ds_read_b64_tr_b16 v[234:235], %[a] offset:160\n\t"
    "ds_read_b64_tr_b16 v[236:237], %[a] offset:1056\n\t"
    "ds_read_b64_tr_b16 v[238:239], %[a] offset:1184\n\t"
    "s_waitcnt lgkmcnt(0)\n\t"
    "ds_read_b64_tr_b16 v[240:241], %[a] offset:4096\n\t"
    "ds_read_b64_tr_b16 v[242:243], %[a] offset:4224\n\t"
    "ds_read_b64_tr_b16 v[244:245], %[a] offset:5120\n\t"
    "ds_read_b64_tr_b16 v[246:247], %[a] offset:5248\n\t"
    "ds_read_b64_tr_b16 v[248:249], %[a] offset:4128\n\t"
    "ds_read_b64_tr_b16 v[250:251], %[a] offset:4256\n\t"
    "ds_read_b64_tr_b16 v[252:253], %[a] offset:5152\n\t"
    "ds_read_b64_tr_b16 v[254:255], %[a] offset:5280\n\t"
    "s_waitcnt lgkmcnt(0)"
    : "=&{v[224:255]}"(W.o3) : [a] "v"(a) : "memory");
}
#define XPTRS(l) const unsigned par_ = ini_resolve(ws, l, tag); char* xch_ = xch_base(ws); \
  const char* xb = uni(xch_ + ((size_t)3 << 21) + (((par_ >> 1) & 1) << 13)); const char* ob = uni(xch_ + ((par_ & 1) << 13)); \
  unsigned* cnt = (unsigned*)(xch_ + ((size_t)4 << 21) + ((par_ >> 2) << 13))
__device__ __forceinline__ void qk_body(char* smem, int wave_id_, int xg, int h, int wg, const u16* __restrict__ Wq, const u16* __restrict__ Wv,
                                        char* ws, unsigned* tmo, unsigned long long tag) {
  const int wave = WAVE_ID;
  const int ty = wave >> 2, w4 = wave & 3;
  const char* wq = uni((const char*)(Wq + h * DH)); const char* wv = uni((const char*)(Wv + h * DH));
  auto stage2 = [&](int i) {
    const int tid = wave * 64 + lane_id();
    if (i < 6) {
      stage_proj<false>(wq + (size_t)i * 128 * HD * 2, Q_RING + ((2 * i) & 3) * 32768, tid, wave);
      if (i < 2) stage_proj<false>(wq + (size_t)(i + 6) * 128 * HD * 2, Q_RING + ((2 * i + 1) & 3) * 32768, tid, wave);
      else stage_proj<true>(wv + (size_t)(i - 2) * 256 * HD * 2, Q_RING + ((2 * i + 1) & 3) * 32768, tid, wave);
    }
  };
  QKW W;
  stage2(0); stage2(1);
#define WLD(i, w) do { if ((i) < 5) WAIT_V(8); else WAIT_V(0); \
    WBAR(); { const int l_ = lane_id(); const unsigned o_ = Q_RING + ty * 32768 + ((i) & 1) * 65536; const bool v_ = ty && (i) >= 2; \
      qk_wload<i>(w, proj_base(v_ ? w4 : 2 * w4, l_) + o_, proj_base(v_ ? w4 + 4 : 2 * w4 + 1, l_) + o_); } \
    WBAR(); stage2((i) + 2); if (wave == 0) STS(ws, wg, 1 + (i)); if ((i) == 4 && wave == 7) ini_request(ws, 30720); } while (0)
  WLD(0, W.w0); WLD(1, W.w1); WLD(2, W.w2); WLD(3, W.w3); WLD(4, W.w4); WLD(5, W.w5);
#undef WLD
  XPTRS(smem + 30720);
  if (opaque_tid() == 64) st4_sc1(cnt + (PQKV + h) * 32 + 3, 1u);
  const char* oh = ob + (size_t)(h >> 2) * 16384 + (h & 3) * 1024;
  auto rearm = [&](int ps) {
    unsigned z = SENT; asm volatile("" : "+v"(z));
    const unsigned l16 = lane_id() * 16;
#pragma unroll
    for (int r = 0; r < 2 * OREP; ++r) st16s(oh + (size_t)(ps * 4 + (r >> 1)) * 131072, (r & 1) * 4096u + l16, u32x4{z, z, z, z});
  };
  const char* dbw = xb + (size_t)48 * 16384 + 4096 + h * 128;
  if (wave == 4) *(volatile LAS u32x4*)(smem + Q_TM) = u32x4{0u, 0u, 0u, 0u};
  if (wave == 6) *(u32x4*)(smem + Q_PQ + 4096 + lane_id() * 16) = u32x4{0u, 0u, 0u, 0u};
  BAR();
  if (wave == 0) STS(ws, wg, 0);
  unsigned avail = 0;
  auto stage_kv = [&](int s) {
    if (avail <= (unsigned)s) {
      for (int spins = 0;; ++spins) {
        avail = qkv_avail<0>(cnt + (PQKV + h) * 32);
        if (avail > (unsigned)s) break;
        __builtin_amdgcn_s_sleep(8);
        if (give_up(spins, tmo, 5)) break;
      }
    }
    head_to_lds(uni(ws + (wave == 5 ? OFF_K : (wave == 6 ? OFF_V : OFF_Q)) + ((size_t)s * NH + h) * 8192), lane_id(), Q_K + (s & 1) * 32768 + (wave - 5) * 8192);
  };
  auto loop = [&](auto w_c) __attribute__((always_inline)) {
    constexpr int WV = decltype(w_c)::value;
    if (WV >= 5) stage_kv(0);
    unsigned t0 = 0, cap = 60000, d0 = 105, d1 = 200;
    int dq = D_Q;
    int sl = 0;
    unsigned fc = 0;
    for (int s = 0; s < NSEG; ++s) {
      TSDECL;
      const char* ag = xb + (size_t)((sl * 4 + (AREP == 4 ? xg : xg >> 1)) * 2 + (WV >> 2)) * 16384 + (WV < 4 ? WV * 2048 : WV * 1024);
      const unsigned pb = (s & 1) * 32768;
      const char* vs = WV == 4 ? dbw : WV == 7 ? (const char*)(cnt + (PROG + h) * 32) : xb + (size_t)(24 + sl * 8 + (h >> 2)) * 16384 + (h & 3) * 2048;
      const char* v2 = xb + (size_t)(52 + sl * 4 + (h >> 3)) * 16384 + (h & 7) * 1024 + (WV & 1) * 512;
      const char* od = oh + (size_t)sl * 524288;
      const unsigned sn = (unsigned)(s + 1) << 24;
      if (s > 0) wait_rt(t0, dq);
      TS(0);
      unsigned long long tc; unsigned sp;
      qk_step<WV>(W, ag, vs, v2, od, pb, sn, cap, sp, tc, nullptr, d0, d1);
      dq = (sp & 0xFFFFu) ? min(dq + DQU, D_Q + DQM) : max(dq - DQD, D_Q);
      if (WV >= 5) {
        if (WV == 7) WAIT_V(0);
        const bool st0 = E_S >= 0 && s + 1 < NSEG && avail > (unsigned)(s + 1);
        if (st0) { delay64<(E_S > 0 ? E_S : 0)>(); stage_kv(s + 1); }
        if (RAE && WV == RAW && s > 0 && s < NSEG - 1) rearm(sl == 0 ? 2 : sl - 1);
        WBAR();
        if (!st0 && s + 1 < NSEG) stage_kv(s + 1);
      }
      if (!RAE && WV == RAW && s > 0 && s < NSEG - 1) rearm(sl == 0 ? 2 : sl - 1);
      unsigned long long* tsp = nullptr;
      seg_attn<WV>(W, pb, ws + OFF_O + ((size_t)s * 64 + 2 * h + (WV >> 1)) * 4096, tsp);
      if (WV == 1) TSFLUSH(0x8000);
      if (WV < 4 && s >= SPUB) {
        WAIT_V(0);
        if (lane_id() == 0 && (__hip_atomic_fetch_add((LAS unsigned*)(smem + Q_TM + 8), 1u, __ATOMIC_RELAXED, __HIP_MEMORY_SCOPE_WORKGROUP) & 3u) == 3u)
          st4_sc1(cnt + (PROG + h) * 32, (unsigned)(s + 1));
      }
      if (s < NSEG - 1) {
        if (WV == 4) {
          delay64<E_B>();
          t0 = bell_time(xb + (size_t)(48 + xg) * 16384 + 4096, (lane_id() & 31) * 128, (unsigned)(s + 1), cap);
          *(volatile LAS u32x2*)(smem + Q_TM) = u32x2{(unsigned)(s + 1), t0};
          TS(14);
          TSFLUSH(0x4000);
        } else {
          for (;;) {
            const u32x2 v = *(volatile LAS u32x2*)(smem + Q_TM);
            t0 = __builtin_amdgcn_readfirstlane(v.y);
            if (__builtin_amdgcn_readfirstlane(v.x) == (unsigned)(s + 1)) break;
            __builtin_amdgcn_s_sleep(1);
          }
        }
      }
      sl = sl == 2 ? 0 : sl + 1;
    }
    if (cap == 0 && lane_id() == 0) st4_sc1(tmo, 1u);
    FCS();
  };
  switch (wave) {
    case 0: loop(std::integral_constant<int, 0>{}); break;
    case 1: loop(std::integral_constant<int, 1>{}); break;
    case 2: loop(std::integral_constant<int, 2>{}); break;
    case 3: loop(std::integral_constant<int, 3>{}); break;
    case 4: loop(std::integral_constant<int, 4>{}); break;
    case 5: loop(std::integral_constant<int, 5>{}); break;
    case 6: loop(std::integral_constant<int, 6>{}); break;
    default: loop(std::integral_constant<int, 7>{}); break;
  }
  __builtin_amdgcn_s_setprio(0);
}

__device__ __forceinline__ void vr_body(char* smem, int wave_id_, int xg, int h, int c, int wg, const u16* __restrict__ Wk, const u16* __restrict__ Wv,
                                        const u16* __restrict__ Wo, char* ws, unsigned* tmo, unsigned long long tag) {
  const int wave = WAVE_ID;
  const unsigned po = (unsigned)(h >> 4) * 16384u + ((h >> 1) & 7) * 1024 + (h & 1) * 512 + c * 128;
  const unsigned ko_ = (unsigned)(24 + (h >> 2)) * 16384u + (h & 3) * 2048 + c * 1024;
  const unsigned vo_ = (unsigned)(52 + (h >> 3)) * 16384u + (h & 7) * 1024 + c * 512;
  const int hg = (wave + ROTW * ((2 * h + c) & 3)) & 7;
  VRW W;
  {
    const int lane = lane_id();
    const int g = lane >> 4, q4 = (lane & 15) >> 2, p4 = lane & 3, fr = 4 * g + (q4 & 1) + 32 * (q4 >> 1);
    const unsigned lw = wave * 16384;
    const char* sk = uni((const char*)(Wk + (size_t)(128 * wave) * HD + h * DH + 64 * c));
    const char* sv_ = uni((const char*)(Wv + (size_t)(128 * wave) * HD + h * DH + 64 + 32 * c));
    const char* so = uni((const char*)(Wo + (size_t)(512 * hg) * DM + 32 * h));
    const int kp = lane >> 2, sr = 32 * (kp >> 3) + 16 * ((kp >> 2) & 1) + (kp & 3);
    auto issue = [&](int p) {
#pragma unroll
      for (int i = 0; i < 8; ++i) {
        const unsigned d = lw + (p & 1) * 8192 + i * 1024;
        if (p < 2) gldsw(sk + (size_t)(8 * p + i) * 8 * HD * 2, (unsigned)((lane >> 3) * HD * 2 + (lane & 7) * 16), d);
        else if (p < 6 && (NTM & 8)) gldsw(so + (size_t)(128 * (p - 2) + 64 * (i & 1) + 4 * (i >> 1)) * DM * 2, (unsigned)(sr * DM * 2 + p4 * 16), d);
        else if (p < 6) glds16s<0>(so + (size_t)(128 * (p - 2) + 64 * (i & 1) + 4 * (i >> 1)) * DM * 2, (unsigned)(sr * DM * 2 + p4 * 16), d);
        else if (NTM & 16) gldsw(sv_ + (size_t)i * 16 * HD * 2, (unsigned)(kp * HD * 2 + p4 * 16), d);
        else glds16s<0>(sv_ + (size_t)i * 16 * HD * 2, (unsigned)(kp * HD * 2 + p4 * 16), d);
      }
    };
#define VTS(p) do { } while (0)
    const int fm = 16 * (g >> 1) + 4 * (g & 1) + (q4 & 1) + 8 * (q4 >> 1);
    const unsigned ak = lw + fm * 128 + p4 * 8, ao = lw + fr * 64 + p4 * 8;
    issue(2); issue(3);
    WAIT_V(8); VTS(1); kr_load_o0(W, ao); issue(4);
    WAIT_V(8); VTS(2); kr_load_o1(W, ao + 8192); issue(5);
    WAIT_V(8); VTS(3); kr_load_o2(W, ao); issue(0);
    WAIT_V(8); VTS(4); kr_load_o3(W, ao + 8192); issue(1);
    WAIT_V(8); VTS(5); kr_load_k0(W, ak); issue(6);
    WAIT_V(8); VTS(6); kr_load_k1(W, ak + 8192);
    if (wave == 0) ini_request(ws, V_P2);
    WAIT_V(0);
    u32x8 x[4];
#pragma unroll
    for (int j = 0; j < 4; ++j) {
      const unsigned a = lw + fm * 64 + p4 * 8 + (j & 1) * 4096 + (j >> 1) * 32;
      const u32x2 t0 = trread(smem, a), t1 = trread(smem, a + 128), t2 = trread(smem, a + 2048), t3 = trread(smem, a + 2176);
      x[j] = u32x8{t0.x, t0.y, t1.x, t1.y, t2.x, t2.y, t3.x, t3.y};
    }
    char* d = smem + lw + 8192 + lane * 16;
#pragma unroll
    for (int j = 0; j < 4; ++j) { *(u32x4*)(d + j * 2048) = u32x4{x[j][0], x[j][1], x[j][2], x[j][3]}; *(u32x4*)(d + j * 2048 + 1024) = u32x4{x[j][4], x[j][5], x[j][6], x[j][7]}; }
    VTS(7);
  }
  BAR();
  XPTRS(smem + V_P2);
  (void)cnt;
  auto rearm = [&](int ps) {
    const int lane = lane_id();
    unsigned z = SENT; asm volatile("" : "+v"(z));
    const u32x4 sv = u32x4{z, z, z, z};
    static_assert(AREP == 2, "");
    st16s(xb, lane < 32 ? (unsigned)(ps * 8 + (lane >> 4) * 2) * 16384u + po + ((lane >> 3) & 1) * 256 + (lane & 7) * 16 : (unsigned)ps * 65536u + vo_ + (lane & 31) * 16, sv);
    st16s(xb, (unsigned)ps * 131072u + ko_ + lane * 16, sv);
  };
  if (wave == 0) STS(ws, wg, 0);
  __builtin_amdgcn_s_setprio(2);
  auto loop = [&](auto cl_c) __attribute__((always_inline)) {
    constexpr int CL = decltype(cl_c)::value;
    unsigned cap = 60000, tr = 0, fc = 0;
    int da = D_A, dd = D_O;
    int sl = 0;
    for (int s = 0; s < NSEG; ++s) {
      TSDECL;
      const int sn = sl == 2 ? 0 : sl + 1;
      const char* ag = xb + (size_t)((sl * 4 + (AREP == 4 ? xg : xg >> 1)) * 2 + (wave >> 2)) * 16384 + (wave & 3) * 2048;
      const char* vd = CL == 0 ? xb + (size_t)sl * 131072 + ko_ + (wave & 3) * 256 : xb + (size_t)sl * 65536 + vo_ + (wave & 3) * 128;
      const char* os = ob + (size_t)((sl * 4 + ORSEL) * 8 + hg) * 16384 + c * 4096;
      const char* ad = xb + (size_t)((sn * 4 + (wave & 3)) * 2) * 16384 + po;
      if (s > 0) { if (wave < AREP) delay64<E_AG - E_W>(); else delay64<E_AG>(); }
      TS(0);
      unsigned long long tc; unsigned sp;
      unsigned long long* tsp = nullptr;
      if (CL == 0) vr_v0(W, ag, vd, wave * 16384 + 8192, V_P1 + wave * 2304, V_PV + wave * 1024, V_P1 + (wave & 3) * 32, cap, sp, tc, tsp);
      else vr_v1(W, ag, vd, wave * 16384 + 8192, V_P1 + wave * 2304, V_PV + wave * 1024, V_PV + (wave & 3) * 256, cap, sp, tc, tsp);
      TN(12, sp + 1);
      if (s > 3 && sp) fc += 1u;
      da = sp ? min(da + DQU, D_A + DQM) : max(da - DQD, D_A);
      if (s == NSEG - 1) break;
      if (wave == 7 && s > 0) rearm(sl == 0 ? 2 : sl - 1);
      wait_clk((unsigned)tc, E_O);
      TS(3);
      const unsigned tb = bell_time(xb + (size_t)(48 + xg) * 16384 + hg * 512, (lane_id() & 3) * 128, (unsigned)(s + 1), cap);
      tr = tb;
      wait_rt(tb, dd);
      TS(4);
      vr_r(W, os, ad, V_P2 + wave * 768, cap, sp, tsp);
      if (s > 3 && sp) fc += 0x10000u;
      dd = sp ? min(dd + DOU, D_O + DOM) : max(dd - DQD, D_O);
      TN(14, (sp & 0xFFFF) + 1);
      if (wave == 1) TSFLUSH(0x6078);
      sl = sn;
    }
    if (cap == 0 && lane_id() == 0) st4_sc1(tmo, 2u);
    FCS();
  };
  if ((wave >> 2) == CL0) loop(std::integral_constant<int, 0>{}); else loop(std::integral_constant<int, 1>{});
  __builtin_amdgcn_s_setprio(0);
}
#define NCS 6
__device__ __forceinline__ int calibrate(char* ws, int i) {
  const int t = lane_id(), ci = (i & 15) % 3, w = (i >> 4) * 4 + (i & 15) / 3, chunk = ci == 0 ? 0 : ci + 2;
  char* base = xch_base(ws) + ((size_t)chunk << 21) + 127 * 16384 + w * 512;
  const char* sb[2] = {uni(base + (w & 1) * 8192), uni(base + (~w & 1) * 8192)};
  if ((t & 31) < 24) st16_sc1(base + (t >> 5) * 8192 + (t & 31) * 16, u32x4{SENT, SENT, SENT, SENT});
  unsigned v, best[2] = {1u << 30, 1u << 30};
  asm volatile("s_load_dword %0, %1, 384 glc\n\ts_waitcnt vmcnt(0) lgkmcnt(0)" : "=&s"(v) : "s"(sb[0]) : "memory");
#pragma unroll
  for (int r = 0; r < NCS; ++r) {
    unsigned long long t0, t1;
    asm volatile("s_memtime %0\n\ts_waitcnt lgkmcnt(0)\n\ts_load_dword %2, %3, %4 glc\n\ts_waitcnt lgkmcnt(0)\n\ts_memtime %1\n\ts_waitcnt lgkmcnt(0)"
                 : "=&s"(t0), "=&s"(t1), "=&s"(v) : "s"(sb[r & 1]), "i"((r >> 1) * 128) : "memory");
    best[r & 1] = min(best[r & 1], (unsigned)(t1 - t0));
  }
  return (w & 1) ? (int)best[0] - (int)best[1] : (int)best[1] - (int)best[0];
}
__device__ __forceinline__ void init_role(char* smem, int wave_id_, int i, const u16* __restrict__ cvec, const u16* __restrict__ wmod, const u16* __restrict__ mem0,
                                          char* ws, unsigned long long tag, unsigned vote) {
  const int t = opaque_tid();
  char* xch = xch_base(ws);
  const int cg = t & 15, r0 = t >> 4, k0 = (i >> 4) * 256 + r0;
  u32x4 w[8]; u16 cv[8];
#pragma unroll
  for (int j = 0; j < 8; ++j) { w[j] = ldnt<1>(wmod + (size_t)(k0 + 32 * j) * (6 * DM) + (i & 15) * 128 + cg * 8); cv[j] = cvec[k0 + 32 * j]; }
  static_assert(OREP == 2 && AREP == 2, "");
  for (int b = i; b < 105; b += 64) {
    if (b < 101) {
      char* p = b < 48 ? xch + (size_t)((b >> 4) * 32 + (b & 15)) * 16384
              : (b < 56 ? xch + ((size_t)3 << 21) + (size_t)(8 + ((b - 48) >> 2) * 8 + (b & 3)) * 16384
              : (b < 96 ? xch + ((size_t)3 << 21) + (size_t)(b - 56 + 24) * 16384 : xch + ((size_t)4 << 21) + (size_t)(b - 96) * 16384));
      const unsigned v = b < 80 || (b >= 84 && b < 96) ? SENT : 0u;
#pragma unroll
      for (int j = 0; j < 2; ++j) st16_sc1(p + j * 8192 + t * 16, u32x4{v, v, v, v});
    } else {
      const int rep = (b - 101) & 1, par = (b - 101) >> 1;
      if (b == 101 && t == 0) st4_sc1(ws + OFF_TMO, 0u);
#pragma unroll
      for (int j = 0; j < 2; ++j) {
        const int u = t + 512 * j, lane = u & 63, g = lane >> 4, hf = (lane >> 1) & 1, row = ((lane >> 1) & 6) + (lane & 1);
        u32x4 v = *(const u32x4*)(mem0 + row * DM + 64 * (u >> 6) + 16 * g + 8 * hf);
        v.x = nosent(v.x); v.y = nosent(v.y); v.z = nosent(v.z); v.w = nosent(v.w);
        st16_sc1(xch + ((size_t)3 << 21) + (size_t)(rep * 2 + j) * 16384 + par * 8192 + t * 16, v);
      }
    }
  }
  f32x2 acc[4] = {f32x2{0, 0}, f32x2{0, 0}, f32x2{0, 0}, f32x2{0, 0}};
#pragma unroll
  for (int j = 0; j < 8; ++j) {
    const float c = bf2f(cv[j]), sv = bfr(c / (1.0f + __expf(-c)));
#pragma unroll
    for (int e = 0; e < 4; ++e) acc[e] = pkfma(f32x2{sv, sv}, f32x2{bflo(w[j][e]), bfhi(w[j][e])}, acc[e]);
  }
  float* red = (float*)smem;
  *(f32x4*)(red + r0 * 128 + cg * 8) = f32x4{acc[0][0], acc[0][1], acc[1][0], acc[1][1]};
  *(f32x4*)(red + r0 * 128 + cg * 8 + 4) = f32x4{acc[2][0], acc[2][1], acc[3][0], acc[3][1]};
  BAR();
  if (t < 128) {
    float s = 0;
#pragma unroll
    for (int r = 0; r < 32; ++r) s += red[r * 128 + t];
    st4_sc1(ws + OFF_MOD + ((i >> 4) * (2 * DM) + (i & 15) * 128 + t) * 4, __builtin_bit_cast(unsigned, s));
  }
  WAIT_V(0);
  BAR();
  if (t == 0) st8_sc1(ws + OFF_INI + i * 128, u32x2{(unsigned)tag, ((unsigned)(tag >> 48) << 16) | vote});
}
#define GEMMX gemm7
#define PFW 0
#define NIMM 3
enum { T_NONE, T_QV, T_KR, T_WO, T_F1, T_F2, T_F2C, T_FIN, T_LN, T_SA };
__device__ __attribute__((aligned(16))) const unsigned ROLES[256][6] = {
  {0x1, 0x0, 0x0, 0x0, 0x0, 0x0},
  {0x11, 0x0, 0x0, 0x0, 0x0, 0x0},
  {0x21, 0x0, 0x0, 0x0, 0x0, 0x0},
  {0x31, 0x0, 0x0, 0x0, 0x0, 0x0},
  {0x41, 0x0, 0x0, 0x0, 0x0, 0x0},
  {0x51, 0x0, 0x0, 0x0, 0x0, 0x0},
  {0x61, 0x0, 0x0, 0x0, 0x0, 0x0},
  {0x71, 0x0, 0x0, 0x0, 0x0, 0x0},
  {0x81, 0x0, 0x0, 0x0, 0x0, 0x0},
  {0x91, 0x0, 0x0, 0x0, 0x0, 0x0},
  {0xa1, 0x0, 0x0, 0x0, 0x0, 0x0},
  {0xb1, 0x0, 0x0, 0x0, 0x0, 0x0},
  {0x2, 0x0, 0x0, 0x0, 0x0, 0x0},
  {0x12, 0x0, 0x0, 0x0, 0x0, 0x0},
  {0x22, 0x0, 0x0, 0x0, 0x0, 0x0},
  {0x32, 0x0, 0x0, 0x0, 0x0, 0x0},
  {0x42, 0x0, 0x0, 0x0, 0x0, 0x0},
  {0x52, 0x0, 0x0, 0x0, 0x0, 0x0},
  {0x62, 0x0, 0x0, 0x0, 0x0, 0x0},
  {0x72, 0x0, 0x0, 0x0, 0x0, 0x0},
  {0x82, 0x0, 0x0, 0x0, 0x0, 0x0},
  {0x92, 0x0, 0x0, 0x0, 0x0, 0x0},
  {0xa2, 0x0, 0x0, 0x0, 0x0, 0x0},
  {0xb2, 0x0, 0x0, 0x0, 0x0, 0x0},
  {0xc2, 0x0, 0x0, 0x0, 0x0, 0x0},
  {0xd2, 0x0, 0x0, 0x0, 0x0, 0x0},
  {0xe2, 0x0, 0x0, 0x0, 0x0, 0x0},
  {0xf2, 0x0, 0x0, 0x0, 0x0, 0x0},
  {0x102, 0x0, 0x0, 0x0, 0x0, 0x0},
  {0x112, 0x0, 0x0, 0x0, 0x0, 0x0},
  {0x122, 0x0, 0x0, 0x0, 0x0, 0x0},
  {0x132, 0x0, 0x0, 0x0, 0x0, 0x0},
  {0xc1, 0x0, 0x0, 0x0, 0x0, 0x0},
  {0xd1, 0x0, 0x0, 0x0, 0x0, 0x0},
  {0xe1, 0x0, 0x0, 0x0, 0x0, 0x0},
  {0xf1, 0x0, 0x0, 0x0, 0x0, 0x0},
  {0x101, 0x0, 0x0, 0x0, 0x0, 0x0},
  {0x111, 0x0, 0x0, 0x0, 0x0, 0x0},
  {0x121, 0x0, 0x0, 0x0, 0x0, 0x0},
  {0x131, 0x0, 0x0, 0x0, 0x0, 0x0},
  {0x142, 0x0, 0x0, 0x0, 0x0, 0x0},
  {0x152, 0x0, 0x0, 0x0, 0x0, 0x0},
  {0x162, 0x0, 0x0, 0x0, 0x0, 0x0},
  {0x172, 0x0, 0x0, 0x0, 0x0, 0x0},
  {0x182, 0x0, 0x0, 0x0, 0x0, 0x0},
  {0x192, 0x0, 0x0, 0x0, 0x0, 0x0},
  {0x1a2, 0x0, 0x0, 0x0, 0x0, 0x0},
  {0x1b2, 0x0, 0x0, 0x0, 0x0, 0x0},
  {0x1c2, 0x0, 0x0, 0x0, 0x0, 0x0},
  {0x1d2, 0x0, 0x0, 0x0, 0x0, 0x0},
  {0x1e2, 0x0, 0x0, 0x0, 0x0, 0x0},
  {0x1f2, 0x0, 0x0, 0x0, 0x0, 0x0},
  {0x202, 0x0, 0x0, 0x0, 0x0, 0x0},
  {0x212, 0x0, 0x0, 0x0, 0x0, 0x0},
  {0x222, 0x0, 0x0, 0x0, 0x0, 0x0},
  {0x232, 0x0, 0x0, 0x0, 0x0, 0x0},
  {0x242, 0x0, 0x0, 0x0, 0x0, 0x0},
  {0x252, 0x0, 0x0, 0x0, 0x0, 0x0},
  {0x262, 0x0, 0x0, 0x0, 0x0, 0x0},
  {0x272, 0x0, 0x0, 0x0, 0x0, 0x0},
  {0x282, 0x0, 0x0, 0x0, 0x0, 0x0},
  {0x292, 0x0, 0x0, 0x0, 0x0, 0x0},
  {0x2a2, 0x0, 0x0, 0x0, 0x0, 0x0},
  {0x2b2, 0x0, 0x0, 0x0, 0x0, 0x0},
  {0x141, 0x0, 0x0, 0x0, 0x0, 0x0},
  {0x151, 0x0, 0x0, 0x0, 0x0, 0x0},
  {0x161, 0x0, 0x0, 0x0, 0x0, 0x0},
  {0x171, 0x0, 0x0, 0x0, 0x0, 0x0},
  {0x181, 0x0, 0x0, 0x0, 0x0, 0x0},
  {0x191, 0x0, 0x0, 0x0, 0x0, 0x0},
  {0x1a1, 0x0, 0x0, 0x0, 0x0, 0x0},
  {0x1b1, 0x0, 0x0, 0x0, 0x0, 0x0},
  {0x1c1, 0x0, 0x0, 0x0, 0x0, 0x0},
  {0x1d1, 0x0, 0x0, 0x0, 0x0, 0x0},
  {0x1e1, 0x0, 0x0, 0x0, 0x0, 0x0},
  {0x1f1, 0x0, 0x0, 0x0, 0x0, 0x0},
  {0x2c2, 0x0, 0x0, 0x0, 0x0, 0x0},
  {0x2d2, 0x0, 0x0, 0x0, 0x0, 0x0},
  {0x2e2, 0x0, 0x0, 0x0, 0x0, 0x0},
  {0x2f2, 0x0, 0x0, 0x0, 0x0, 0x0},
  {0x302, 0x0, 0x0, 0x0, 0x0, 0x0},
  {0x312, 0x0, 0x0, 0x0, 0x0, 0x0},
  {0x322, 0x0, 0x0, 0x0, 0x0, 0x0},
  {0x332, 0x0, 0x0, 0x0, 0x0, 0x0},
  {0x342, 0x0, 0x0, 0x0, 0x0, 0x0},
  {0x352, 0x0, 0x0, 0x0, 0x0, 0x0},
  {0x362, 0x0, 0x0, 0x0, 0x0, 0x0},
  {0x372, 0x0, 0x0, 0x0, 0x0, 0x0},
  {0x382, 0x0, 0x0, 0x0, 0x0, 0x0},
  {0x392, 0x0, 0x0, 0x0, 0x0, 0x0},
  {0x3a2, 0x0, 0x0, 0x0, 0x0, 0x0},
  {0x3b2, 0x0, 0x0, 0x0, 0x0, 0x0},
  {0x3c2, 0x0, 0x0, 0x0, 0x0, 0x0},
  {0x3d2, 0x0, 0x0, 0x0, 0x0, 0x0},
  {0x3e2, 0x0, 0x0, 0x0, 0x0, 0x0},
  {0x3f2, 0x0, 0x0, 0x0, 0x0, 0x0},
  {0x100005, 0x300000, 0x0, 0x0, 0x0, 0x0},
  {0x100015, 0x300004, 0x0, 0x0, 0x0, 0x0},
  {0x100025, 0x300008, 0x0, 0x0, 0x0, 0x0},
  {0x100035, 0x30000c, 0x0, 0x0, 0x0, 0x0},
  {0x100045, 0x300010, 0x0, 0x0, 0x0, 0x0},
  {0x100055, 0x300014, 0x0, 0x0, 0x0, 0x0},
  {0x100065, 0x300018, 0x0, 0x0, 0x0, 0x0},
  {0x100073, 0x16001c, 0x0, 0x0, 0x0, 0x0},
  {0x100075, 0x300020, 0x0, 0x0, 0x0, 0x0},
  {0x100085, 0x300024, 0x0, 0x0, 0x0, 0x0},
  {0x100095, 0x300028, 0x0, 0x0, 0x0, 0x0},
  {0x1000a5, 0x30002c, 0x0, 0x0, 0x0, 0x0},
  {0x1000b5, 0x300030, 0x0, 0x0, 0x0, 0x0},
  {0x1000c5, 0x300034, 0x0, 0x0, 0x0, 0x0},
  {0x1000d5, 0x300038, 0x0, 0x0, 0x0, 0x0},
  {0x1000f3, 0x16003c, 0x0, 0x0, 0x0, 0x0},
  {0x1000e5, 0x300040, 0x0, 0x0, 0x0, 0x0},
  {0x1000f5, 0x300044, 0x0, 0x0, 0x0, 0x0},
  {0x100105, 0x300048, 0x0, 0x0, 0x0, 0x0},
  {0x100115, 0x30004c, 0x0, 0x0, 0x0, 0x0},
  {0x100125, 0x300050, 0x0, 0x0, 0x0, 0x0},
  {0x100135, 0x300054, 0x0, 0x0, 0x0, 0x0},
  {0x100145, 0x300058, 0x0, 0x0, 0x0, 0x0},
  {0x100173, 0x16005c, 0x0, 0x0, 0x0, 0x0},
  {0x100155, 0x300060, 0x0, 0x0, 0x0, 0x0},
  {0x100165, 0x300064, 0x0, 0x0, 0x0, 0x0},
  {0x100175, 0x300068, 0x0, 0x0, 0x0, 0x0},
  {0x100006, 0x30006c, 0x0, 0x0, 0x0, 0x0},
  {0x100016, 0x300070, 0x0, 0x0, 0x0, 0x0},
  {0x100026, 0x300074, 0x0, 0x0, 0x0, 0x0},
  {0x100036, 0x300078, 0x0, 0x0, 0x0, 0x0},
  {0x1001f3, 0x16007c, 0x0, 0x0, 0x0, 0x0},
  {0x100064, 0x5361615, 0x0, 0x0, 0x0, 0x0},
  {0x100074, 0x5361619, 0x0, 0x0, 0x0, 0x0},
  {0x100084, 0x536161d, 0x0, 0x0, 0x0, 0x0},
  {0x100094, 0x536161a, 0x0, 0x0, 0x0, 0x0},
  {0x1000a4, 0x536161e, 0x0, 0x0, 0x0, 0x0},
  {0x1000d4, 0x536161c, 0x0, 0x0, 0x0, 0x0},
  {0x100034, 0x5402812, 0x0, 0x0, 0x0, 0x0},
  {0x1000b4, 0x5402816, 0x0, 0x0, 0x0, 0x0},
  {0x200000, 0x400001, 0x1403000, 0x0, 0x0, 0x0},
  {0x200007, 0x400005, 0x1403004, 0x0, 0x0, 0x0},
  {0x200017, 0x400009, 0x1403008, 0x0, 0x0, 0x0},
  {0x200027, 0x40000d, 0x140300c, 0x0, 0x0, 0x0},
  {0x200037, 0x400011, 0x1403010, 0x0, 0x0, 0x0},
  {0x100003, 0x160015, 0x0, 0x0, 0x0, 0x0},
  {0x100013, 0x160019, 0x0, 0x0, 0x0, 0x0},
  {0x100023, 0x16001d, 0x0, 0x0, 0x0, 0x0},
  {0x100004, 0x280002, 0x0, 0x0, 0x0, 0x0},
  {0x100014, 0x280006, 0x0, 0x0, 0x0, 0x0},
  {0x100024, 0x28000a, 0x0, 0x0, 0x0, 0x0},
  {0x1000c4, 0x28000e, 0x0, 0x0, 0x0, 0x0},
  {0x100044, 0x280012, 0x0, 0x0, 0x0, 0x0},
  {0x100054, 0x280016, 0x0, 0x0, 0x0, 0x0},
  {0x100033, 0x16001a, 0x0, 0x0, 0x0, 0x0},
  {0x100043, 0x16001e, 0x0, 0x0, 0x0, 0x0},
  {0x101053, 0x2403615, 0x0, 0x0, 0x0, 0x0},
  {0x102063, 0x2403619, 0x0, 0x0, 0x0, 0x0},
  {0x303000, 0x1402802, 0x140280a, 0x1403014, 0x0, 0x0},
  {0x304000, 0x1402806, 0x140280e, 0x1403018, 0x0, 0x0},
  {0x105008, 0x240361d, 0x0, 0x0, 0x0, 0x0},
  {0x106018, 0x240361a, 0x0, 0x0, 0x0, 0x0},
  {0x107088, 0x240361e, 0x0, 0x0, 0x0, 0x0},
  {0x108098, 0x240361c, 0x0, 0x0, 0x0, 0x0},
  {0x100144, 0x5361635, 0x0, 0x0, 0x0, 0x0},
  {0x100154, 0x5361639, 0x0, 0x0, 0x0, 0x0},
  {0x100164, 0x536163d, 0x0, 0x0, 0x0, 0x0},
  {0x100174, 0x536163a, 0x0, 0x0, 0x0, 0x0},
  {0x100184, 0x536163e, 0x0, 0x0, 0x0, 0x0},
  {0x100194, 0x536163c, 0x0, 0x0, 0x0, 0x0},
  {0x100134, 0x5402832, 0x0, 0x0, 0x0, 0x0},
  {0x1001b4, 0x5402836, 0x0, 0x0, 0x0, 0x0},
  {0x200000, 0x400021, 0x1403020, 0x0, 0x0, 0x0},
  {0x200047, 0x400025, 0x1403024, 0x0, 0x0, 0x0},
  {0x200057, 0x400029, 0x1403028, 0x0, 0x0, 0x0},
  {0x200067, 0x40002d, 0x140302c, 0x0, 0x0, 0x0},
  {0x200077, 0x400031, 0x1403030, 0x0, 0x0, 0x0},
  {0x100083, 0x160035, 0x0, 0x0, 0x0, 0x0},
  {0x100093, 0x160039, 0x0, 0x0, 0x0, 0x0},
  {0x1000a3, 0x16003d, 0x0, 0x0, 0x0, 0x0},
  {0x1000e4, 0x280022, 0x0, 0x0, 0x0, 0x0},
  {0x1000f4, 0x280026, 0x0, 0x0, 0x0, 0x0},
  {0x100104, 0x28002a, 0x0, 0x0, 0x0, 0x0},
  {0x100114, 0x28002e, 0x0, 0x0, 0x0, 0x0},
  {0x100124, 0x280032, 0x0, 0x0, 0x0, 0x0},
  {0x1001a4, 0x280036, 0x0, 0x0, 0x0, 0x0},
  {0x1000b3, 0x16003a, 0x0, 0x0, 0x0, 0x0},
  {0x1000c3, 0x16003e, 0x0, 0x0, 0x0, 0x0},
  {0x1090d3, 0x2403635, 0x0, 0x0, 0x0, 0x0},
  {0x10a0e3, 0x2403639, 0x0, 0x0, 0x0, 0x0},
  {0x30b000, 0x1402822, 0x140282a, 0x1403034, 0x0, 0x0},
  {0x30c000, 0x1402826, 0x140282e, 0x1403038, 0x0, 0x0},
  {0x10d028, 0x240363d, 0x0, 0x0, 0x0, 0x0},
  {0x10e038, 0x240363a, 0x0, 0x0, 0x0, 0x0},
  {0x10f0a8, 0x240363e, 0x0, 0x0, 0x0, 0x0},
  {0x1100b8, 0x240363c, 0x0, 0x0, 0x0, 0x0},
  {0x100224, 0x5361655, 0x0, 0x0, 0x0, 0x0},
  {0x100284, 0x5361659, 0x0, 0x0, 0x0, 0x0},
  {0x100244, 0x536165d, 0x0, 0x0, 0x0, 0x0},
  {0x100254, 0x536165a, 0x0, 0x0, 0x0, 0x0},
  {0x100264, 0x536165e, 0x0, 0x0, 0x0, 0x0},
  {0x100274, 0x536165c, 0x0, 0x0, 0x0, 0x0},
  {0x100234, 0x5402852, 0x0, 0x0, 0x0, 0x0},
  {0x100294, 0x5402856, 0x0, 0x0, 0x0, 0x0},
  {0x200000, 0x400041, 0x1403040, 0x0, 0x0, 0x0},
  {0x200087, 0x400045, 0x1403044, 0x0, 0x0, 0x0},
  {0x200097, 0x400049, 0x1403048, 0x0, 0x0, 0x0},
  {0x2000a7, 0x40004d, 0x140304c, 0x0, 0x0, 0x0},
  {0x2000b7, 0x400051, 0x1403050, 0x0, 0x0, 0x0},
  {0x100103, 0x160055, 0x0, 0x0, 0x0, 0x0},
  {0x100113, 0x160059, 0x0, 0x0, 0x0, 0x0},
  {0x100123, 0x16005d, 0x0, 0x0, 0x0, 0x0},
  {0x1001c4, 0x280042, 0x0, 0x0, 0x0, 0x0},
  {0x1001d4, 0x280046, 0x0, 0x0, 0x0, 0x0},
  {0x1001e4, 0x28004a, 0x0, 0x0, 0x0, 0x0},
  {0x1001f4, 0x28004e, 0x0, 0x0, 0x0, 0x0},
  {0x100204, 0x280052, 0x0, 0x0, 0x0, 0x0},
  {0x100214, 0x280056, 0x0, 0x0, 0x0, 0x0},
  {0x100133, 0x16005a, 0x0, 0x0, 0x0, 0x0},
  {0x100143, 0x16005e, 0x0, 0x0, 0x0, 0x0},
  {0x111153, 0x2403655, 0x0, 0x0, 0x0, 0x0},
  {0x112163, 0x2403659, 0x0, 0x0, 0x0, 0x0},
  {0x313000, 0x1402842, 0x140284a, 0x1403054, 0x0, 0x0},
  {0x314000, 0x1402846, 0x140284e, 0x1403058, 0x0, 0x0},
  {0x115048, 0x240365d, 0x0, 0x0, 0x0, 0x0},
  {0x116058, 0x240365a, 0x0, 0x0, 0x0, 0x0},
  {0x1170c8, 0x240365e, 0x0, 0x0, 0x0, 0x0},
  {0x1180d8, 0x240365c, 0x0, 0x0, 0x0, 0x0},
  {0x100304, 0x5361675, 0x0, 0x0, 0x0, 0x0},
  {0x100314, 0x5361679, 0x0, 0x0, 0x0, 0x0},
  {0x100324, 0x536167d, 0x0, 0x0, 0x0, 0x0},
  {0x100374, 0x536167a, 0x0, 0x0, 0x0, 0x0},
  {0x100344, 0x536167e, 0x0, 0x0, 0x0, 0x0},
  {0x100354, 0x536167c, 0x0, 0x0, 0x0, 0x0},
  {0x1002b4, 0x5402872, 0x0, 0x0, 0x0, 0x0},
  {0x100334, 0x5402876, 0x0, 0x0, 0x0, 0x0},
  {0x200000, 0x400061, 0x1403060, 0x0, 0x0, 0x0},
  {0x2000c7, 0x400065, 0x1403064, 0x0, 0x0, 0x0},
  {0x2000d7, 0x400069, 0x1403068, 0x0, 0x0, 0x0},
  {0x2000e7, 0x40006d, 0x140306c, 0x0, 0x0, 0x0},
  {0x2000f7, 0x400071, 0x1403070, 0x0, 0x0, 0x0},
  {0x100183, 0x160075, 0x0, 0x0, 0x0, 0x0},
  {0x100193, 0x160079, 0x0, 0x0, 0x0, 0x0},
  {0x1001a3, 0x16007d, 0x0, 0x0, 0x0, 0x0},
  {0x1002a4, 0x280062, 0x0, 0x0, 0x0, 0x0},
  {0x100364, 0x280066, 0x0, 0x0, 0x0, 0x0},
  {0x1002c4, 0x28006a, 0x0, 0x0, 0x0, 0x0},
  {0x1002d4, 0x28006e, 0x0, 0x0, 0x0, 0x0},
  {0x1002e4, 0x280072, 0x0, 0x0, 0x0, 0x0},
  {0x1002f4, 0x280076, 0x0, 0x0, 0x0, 0x0},
  {0x1001b3, 0x16007a, 0x0, 0x0, 0x0, 0x0},
  {0x1001c3, 0x16007e, 0x0, 0x0, 0x0, 0x0},
  {0x1191d3, 0x2403675, 0x0, 0x0, 0x0, 0x0},
  {0x11a1e3, 0x2403679, 0x0, 0x0, 0x0, 0x0},
  {0x31b000, 0x1402862, 0x140286a, 0x1403074, 0x0, 0x0},
  {0x31c000, 0x1402866, 0x140286e, 0x1403078, 0x0, 0x0},
  {0x11d068, 0x240367d, 0x0, 0x0, 0x0, 0x0},
  {0x11e078, 0x240367a, 0x0, 0x0, 0x0, 0x0},
  {0x11f0e8, 0x240367e, 0x0, 0x0, 0x0, 0x0},
  {0x1200f8, 0x240367c, 0x0, 0x0, 0x0, 0x0},
};
constexpr unsigned NQV = 0xc080c;
constexpr int PFN = 32;
__device__ const unsigned PFT[8][PFN + 1] = {
  {0x40000f, 0x41000f, 0x42000f, 0x43000f, 0x44000f, 0x45000f, 0x46000f, 0x47000f, 0x48000f, 0x49000f, 0x4a000f, 0x4b000f, 0x4c000f, 0x4d000f, 0x4e000f, 0x4f000f, 0x40000c, 0x41000c, 0x42000c, 0x43000c, 0x40002c, 0x41002c, 0x42002c, 0x43002c, 0x40004c, 0x41004c, 0x42004c, 0x43004c, 0x40000d, 0x41000d, 0x42000d, 0x43000d, 0x0},
  {0x40006c, 0x41006c, 0x42006c, 0x43006c, 0x40008c, 0x41008c, 0x42008c, 0x43008c, 0x40006d, 0x41006d, 0x42006d, 0x43006d, 0x40006e, 0x41006e, 0x42006e, 0x43006e, 0x40008d, 0x41008d, 0x42008d, 0x43008d, 0x40008e, 0x41008e, 0x42008e, 0x43008e, 0x4000ad, 0x4100ad, 0x4200ad, 0x4300ad, 0x4000ae, 0x4100ae, 0x4200ae, 0x4300ae, 0x0},
  {0x40002f, 0x41002f, 0x42002f, 0x43002f, 0x44002f, 0x45002f, 0x46002f, 0x47002f, 0x48002f, 0x49002f, 0x4a002f, 0x4b002f, 0x4c002f, 0x4d002f, 0x4e002f, 0x4f002f, 0x4000ac, 0x4100ac, 0x4200ac, 0x4300ac, 0x4000cc, 0x4100cc, 0x4200cc, 0x4300cc, 0x4000ec, 0x4100ec, 0x4200ec, 0x4300ec, 0x4000cd, 0x4100cd, 0x4200cd, 0x4300cd, 0x0},
  {0x40000c, 0x41000c, 0x42000c, 0x43000c, 0x40002c, 0x41002c, 0x42002c, 0x43002c, 0x40004c, 0x41004c, 0x42004c, 0x43004c, 0x40006c, 0x41006c, 0x42006c, 0x43006c, 0x40008c, 0x41008c, 0x42008c, 0x43008c, 0x4000ac, 0x4100ac, 0x4200ac, 0x4300ac, 0x4000cc, 0x4100cc, 0x4200cc, 0x4300cc, 0x4000ec, 0x4100ec, 0x4200ec, 0x4300ec, 0x0},
  {0x40000d, 0x41000d, 0x42000d, 0x43000d, 0x40002d, 0x41002d, 0x42002d, 0x43002d, 0x40000e, 0x41000e, 0x42000e, 0x43000e, 0x40002e, 0x41002e, 0x42002e, 0x43002e, 0x600001, 0x600002, 0x620001, 0x620002, 0x601001, 0x601002, 0x621001, 0x621002, 0x602001, 0x602002, 0x622001, 0x622002, 0x603001, 0x603002, 0x623001, 0x623002, 0x0},
  {0x40004d, 0x41004d, 0x42004d, 0x43004d, 0x40006d, 0x41006d, 0x42006d, 0x43006d, 0x40004e, 0x41004e, 0x42004e, 0x43004e, 0x40006e, 0x41006e, 0x42006e, 0x43006e, 0x600041, 0x600042, 0x620041, 0x620042, 0x601041, 0x601042, 0x621041, 0x621042, 0x602041, 0x602042, 0x622041, 0x622042, 0x603041, 0x603042, 0x623041, 0x623042, 0x0},
  {0x40008d, 0x41008d, 0x42008d, 0x43008d, 0x4000ad, 0x4100ad, 0x4200ad, 0x4300ad, 0x40008e, 0x41008e, 0x42008e, 0x43008e, 0x4000ae, 0x4100ae, 0x4200ae, 0x4300ae, 0x600081, 0x600082, 0x620081, 0x620082, 0x601081, 0x601082, 0x621081, 0x621082, 0x602081, 0x602082, 0x622081, 0x622082, 0x603081, 0x603082, 0x623081, 0x623082, 0x0},
  {0x4000cd, 0x4100cd, 0x4200cd, 0x4300cd, 0x4000ed, 0x4100ed, 0x4200ed, 0x4300ed, 0x4000ce, 0x4100ce, 0x4200ce, 0x4300ce, 0x4000ee, 0x4100ee, 0x4200ee, 0x4300ee, 0x6000c1, 0x6000c2, 0x6200c1, 0x6200c2, 0x6010c1, 0x6010c2, 0x6210c1, 0x6210c2, 0x6020c1, 0x6020c2, 0x6220c1, 0x6220c2, 0x6030c1, 0x6030c2, 0x6230c1, 0x6230c2, 0x0},
};
__global__ void __launch_bounds__(512, 2) k_mega(const u16* __restrict__ Wq, const u16* __restrict__ Wk, const u16* __restrict__ Wv,
                                                 const u16* __restrict__ Wo, const u16* __restrict__ w1, const u16* __restrict__ w3,
                                                 const u16* __restrict__ w2, const u16* __restrict__ x, const u16* __restrict__ cvec,
                                                 const u16* __restrict__ wmod, const u16* __restrict__ mem0, char* ws, u16* out) {
  extern __shared__ __attribute__((aligned(16))) char smem[];
  int wave_id_ = __builtin_amdgcn_readfirstlane((int)threadIdx.x >> 6);
  asm volatile("" : "+s"(wave_id_));
  unsigned* tmo = (unsigned*)(ws + OFF_TMO);
  unsigned long long* mts = nullptr;
  const float* mod = (const float*)(ws + OFF_MOD);
  volatile LAS unsigned* tslot = (volatile LAS unsigned*)(smem + L_SLOT + 768);
  unsigned xcc; asm volatile("s_getreg_b32 %0, hwreg(HW_REG_XCC_ID)" : "=s"(xcc)); xcc &= 7;
  auto ini_of = [](int sl) { return sl >= 128 && ((sl + 8) & 31) < 16 ? ((sl >> 5) - 4) * 16 + (sl & 15) : -1; };
  const int guess = xcc * 32 + ((blockIdx.x >> 3) & 31);
  u32x2 rt_ = *(const u32x2*)ROLES[guess];
  if (wave_id_ == 0) {
    if (lane_id() == 0) {
      unsigned long long g = __hip_atomic_load((unsigned long long*)(ws + OFF_EP), __ATOMIC_RELAXED, __HIP_MEMORY_SCOPE_AGENT);
      g = (g ^ (g >> 31) ^ 0x9E3779B97F4A7C15ull) * 0xBF58476D1CE4E5B9ull; g = (g ^ (g >> 29)) * 0x94D049BB133111EBull; g ^= g >> 32;
      unsigned sl = 0xFFFFu;
      for (int i = 0; i < 256; ++i)
        if (__hip_atomic_exchange((unsigned long long*)(ws + OFF_CLM) + ((guess + i) & 255), g, __ATOMIC_RELAXED, __HIP_MEMORY_SCOPE_AGENT) != g) { sl = (guess + i) & 255; break; }
      tslot[0] = sl; tslot[4] = (unsigned)g; tslot[5] = (unsigned)(g >> 32);
    }
  } else if (wave_id_ == 1) {
    const int ig = ini_of(guess);
    tslot[1] = ig >= 0 && (ig & 15) < 12 ? (unsigned)calibrate(ws, ig) : 0u;
  } else if (wave_id_ >= 4) {
#pragma unroll
    for (int i = 0; i < PFN / 32; ++i) {
      const unsigned e = PFT[xcc][i * 32 + ((blockIdx.x >> 3) & 31)];
      const int t = opaque_tid() - 256, m = e & 3, wide = (e >> 21) & 1, row = ((e >> 8) & 8191) + (wide ? t >> 4 : t), cl = ((e >> 2) & 63) + (wide ? t & 15 : 0);
      const u16* p = (m == 0 ? Wq : (m == 1 ? Wk : (m == 2 ? Wv : Wo))) + (size_t)row * (m == 3 ? DM : HD) + cl * 64;
      if (e) asm volatile("s_mov_b32 m0, %1\n\ts_nop 0\n\tglobal_load_lds_dword %0, off" :: "v"(p), "s"((rt_.x & 15) == T_QV ? 31232 : L_GB + 8192) : "memory", "m0");
    }
    if (PFW) WAIT_V(0);
  }
  BAR();
  const int slot = (int)__builtin_amdgcn_readfirstlane(tslot[0]);
  const unsigned long long tag = (unsigned long long)__builtin_amdgcn_readfirstlane(tslot[5]) << 32 | __builtin_amdgcn_readfirstlane(tslot[4]);
  const int cd = (int)__builtin_amdgcn_readfirstlane(tslot[1]);
  if (slot != guess) { rt_ = *(const u32x2*)ROLES[slot & 255]; WAIT_V(0); }
  BAR();
  asm volatile("" : "+s"(wave_id_));
  if (slot >= 256) return;
  const int xg = slot >> 5;
  const unsigned rw = rt_[0];
  const int role = rw & 15, idx = (rw >> 4) & 255;
  if (role == T_QV || role == T_KR) {
    const int wg = role == T_QV ? (idx >> 3) * 32 + (idx & 7) : (idx >> 4) * 32 + 8 + (idx & 15);
    if (role == T_QV) qk_body(smem, wave_id_, xg, idx, wg, Wq, Wv, ws, tmo, tag);
    else vr_body(smem, wave_id_, xg, idx >> 1, idx & 1, wg, Wk, Wv, Wo, ws, tmo, tag);
    return;
  }
  unsigned* cnt = (unsigned*)(xch_base(ws) + ((size_t)4 << 21));
  const int ini = ini_of(slot);
  if (ini >= 0) {
    init_role(smem, wave_id_, ini, cvec, wmod, mem0, ws, tag, slot != guess || cd == 0 ? 0u : ((cd > 0) == !(xcc & 4) ? 1u : 2u));
    cnt += (ini_resolve(ws, nullptr, tag) >> 2) << 11;
  }
  if ((rw >> 12) & 255) l1_role(smem, wave_id_, ((rw >> 12) & 255) - 1, x, mod, cvec, wmod, ws + OFF_H1, cnt, tmo, mts);
  const int ntask = (rw >> 20) & 15;
  for (int it = 0; it < ntask; ++it) {
    const unsigned tk = it == 0 ? rt_[1] : ROLES[slot][1 + it];
    const int m = tk & 3, h = (tk >> 2) & 31, bo = (tk >> 8) & 255, ns = ((tk >> 16) & 255) - bo, part = (tk >> 24) & 3;
    const int lane = lane_id();
    const Dep d{uni((const char*)cnt), (unsigned)(PL1 + (lane & 31)) * 128, 8u, (unsigned)-bo};
    unsigned par = 0;
    if (wave_id_ == 0) {
      if ((tk >> 26) & 1) wait_k0(cnt, h, tmo);
      if (bo) wait_l1(cnt, 8u, tmo);
    }
    BAR();
    gemm7<R_QKV>(smem, wave_id_, (m == 0 ? Wq : (m == 1 ? Wk : Wv)) + h * DH, nullptr, HD, ws + OFF_H1 + (size_t)bo * 65536, 65536, d,
                    ws + (m == 0 ? OFF_Q : (m == 1 ? OFF_K : OFF_V)) + h * 8192 + (size_t)bo * 32 * 8192, 32 * 8192,
                    cnt + (PQKV + h) * 32 + 4 * part + m, tmo, part ? 319 : (h >> 3) * 32 + m * 8 + (h & 7), mts, false,
                    bo ? 0 : NIMM, ns, bo ? ns : 0, bo * 257, tag, &par);
    cnt += (par >> 2) << 11;
  }
  __syncthreads();
  const char* cb = uni((const char*)cnt);
  if (role == T_WO || role == T_F2) {
    const bool wo = role == T_WO;
    const int ks = idx >> 3, cn = idx & 7;
    const int lane = lane_id();
    const Dep d{cb, (unsigned)(wo ? PROG + 8 * ks + (lane & 7) : PF1 + 16 * ks + (lane & 15)) * 128, 1u, 0u};
    gemm7<R_PART>(smem, wave_id_, (wo ? Wo : w2) + (size_t)ks * 1024 * DM + cn * 128, nullptr, DM, ws + (wo ? OFF_O : OFF_G) + ks * 16 * 4096, (wo ? 64 : 56) * 4096, d,
                  ws + (wo ? OFF_P : OFF_P2) + (ks * DM + cn * 128) * (TLP ? 64 : 2), (size_t)32 * PROW, cnt + ((wo ? PWO : PF2) + idx) * 32, tmo,
                  wo ? 128 + ks * 32 + cn : 128 + (idx / 7) * 32 + 22 + idx % 7, mts, !wo);
  } else if (role == T_F2C) {
    const int lane = lane_id();
    const Dep d{cb, (unsigned)(PF1 + 48 + (lane & 7)) * 128, 1u, 0u};
    gemm7<R_PART2>(smem, wave_id_, w2 + (size_t)3072 * DM + idx * 256, nullptr, DM, ws + OFF_G + 48 * 4096, 56 * 4096, d,
                   ws + OFF_P2 + (3 * DM + idx * 256) * (TLP ? 64 : 2), (size_t)32 * PROW, cnt + (PF2 + 24 + idx) * 32, tmo, 128 + 96 + 25 + idx, mts, true);
  } else if (role == T_F1) {
    const int lane = lane_id();
    const Dep d{cb, (unsigned)(PLN + (lane & 15)) * 128, 2u, (unsigned)((lane >> 3) & 1)};
    gemm7<R_F1>(smem, wave_id_, w1 + 64 * idx, w3 + 64 * idx, FF, ws + OFF_H2, 65536, d, ws + OFF_G + idx * 4096, 56 * 4096, cnt + (PF1 + idx) * 32, tmo,
                128 + (idx / 14) * 32 + 8 + idx % 14, mts, false);
  } else if (role == T_FIN) {
    fin_role(smem, wave_id_, idx, mod, ws + OFF_P2, (const u16*)(ws + OFF_X1), out, cnt, tmo, mts);
    if (idx == 15 && opaque_tid() == 0) st8_sc1(ws + OFF_EP, u32x2{(unsigned)tag, (unsigned)(tag >> 32)});
  } else if (role == T_LN) {
    row_role(smem, wave_id_, idx & 7, idx >> 3, mod, ws + OFF_P, x, (u16*)(ws + OFF_X1), ws + OFF_H2, cnt, tmo, 256 + ((idx >> 1) & 3), mts);
  }
}

extern "C" void kernel_launch(void* const* d_in, const int* in_sizes, int n_in, void* d_out, int out_size, void* d_ws, size_t ws_size,
                              hipStream_t stream) {
  const u16* x    = (const u16*)d_in[0];
  const u16* mem0 = (const u16*)d_in[1];
  const u16* Wq   = (const u16*)d_in[2];
  const u16* Wk   = (const u16*)d_in[3];
  const u16* Wv   = (const u16*)d_in[4];
  const u16* Wo   = (const u16*)d_in[5];
  const u16* w1   = (const u16*)d_in[8];
  const u16* w3   = (const u16*)d_in[9];
  const u16* w2   = (const u16*)d_in[10];
  const u16* c    = (const u16*)d_in[11];
  const u16* wmod = (const u16*)d_in[12];
#define WSHIFT 0
  char* ws = (char*)d_ws + ((size_t)WSHIFT << 20);
  hipFuncSetAttribute((const void*)k_mega, hipFuncAttributeMaxDynamicSharedMemorySize, MEGA_LDS);
  k_mega<<<256, 512, MEGA_LDS, stream>>>(Wq, Wk, Wv, Wo, w1, w3, w2, x, c, wmod, mem0, ws, (u16*)d_out);
}
```

```cpp
#include <hip/hip_runtime.h>
#include <hip/hip_bf16.h>
#include <stdint.h>
#include <type_traits>
#pragma clang diagnostic ignored "-Winline-asm"
#pragma clang diagnostic ignored "-Wunused-value"

typedef unsigned short u16;
typedef short s16x4 __attribute__((ext_vector_type(4)));
typedef __bf16 bf16x8 __attribute__((ext_vector_type(8)));
typedef float f32x4 __attribute__((ext_vector_type(4)));
typedef float f32x2 __attribute__((ext_vector_type(2)));
typedef unsigned u32x4 __attribute__((ext_vector_type(4)));
typedef unsigned u32x2 __attribute__((ext_vector_type(2)));
#define LAS __attribute__((address_space(3)))

constexpr int SEQ = 2048, DM = 1024, NH = 32, DH = 128, HD = 4096, NMEM = 8, SEG = 32, NSEG = 64, FF = 3584;
constexpr unsigned SENT = 0xFFFFFFFFu;

__device__ __forceinline__ int lane_id() { int l; asm volatile("v_mbcnt_lo_u32_b32 %0, -1, 0\n\tv_mbcnt_hi_u32_b32 %0, -1, %0" : "=v"(l)); return l; }
#define WAVE_ID wave_id_
#define opaque_tid() (WAVE_ID * 64 + lane_id())
__device__ __forceinline__ float bf2f(u16 v) { return __builtin_bit_cast(float, (unsigned)v << 16); }
__device__ __forceinline__ float bflo(unsigned v) { return __builtin_bit_cast(float, v << 16); }
__device__ __forceinline__ float bfhi(unsigned v) { return __builtin_bit_cast(float, v & 0xffff0000u); }
__device__ __forceinline__ float bfr(float x) { return (float)(__bf16)x; }
__device__ __forceinline__ unsigned pack2(float a, float b) {
  typedef __bf16 bf16x2 __attribute__((ext_vector_type(2)));
  bf16x2 v; v[0] = (__bf16)a; v[1] = (__bf16)b; return __builtin_bit_cast(unsigned, v);
}
__device__ __forceinline__ unsigned nosent(unsigned v) { return v == SENT ? 0x7FC07FC0u : v; }
__device__ __forceinline__ bf16x8 asbf8(u32x4 v) { return __builtin_bit_cast(bf16x8, v); }
__device__ __forceinline__ s16x4 ass4(u32x2 v) { return __builtin_bit_cast(s16x4, v); }
__device__ __forceinline__ u32x2 trread(const char* lds_base, unsigned off) {
  s16x4 v = __builtin_amdgcn_ds_read_tr16_b64_v4i16((LAS s16x4*)(lds_base + off));
  return __builtin_bit_cast(u32x2, v);
}
__device__ __forceinline__ void glds16(const void* gsrc, unsigned lds_dst) {
  unsigned keep;
  asm volatile("s_mov_b32 %0, m0\n\ts_mov_b32 m0, %2\n\ts_nop 0\n\tglobal_load_lds_dwordx4 %1, off\n\ts_mov_b32 m0, %0"
               : "=&s"(keep) : "v"(gsrc), "s"(lds_dst) : "memory");
}
#define CNT_ " nt"
#define WNT_ " nt"
__device__ __forceinline__ void glds16_w(const void* gsrc, unsigned lds_dst) {
  unsigned keep;
  asm volatile("s_mov_b32 %0, m0\n\ts_mov_b32 m0, %2\n\ts_nop 0\n\tglobal_load_lds_dwordx4 %1, off" CNT_ "\n\ts_mov_b32 m0, %0"
               : "=&s"(keep) : "v"(gsrc), "s"(lds_dst) : "memory");
}
__device__ __forceinline__ void glds16_sc1(const void* gsrc, unsigned lds_dst) {
  unsigned keep;
  asm volatile("s_mov_b32 %0, m0\n\ts_mov_b32 m0, %2\n\ts_nop 0\n\tglobal_load_lds_dwordx4 %1, off sc1\n\ts_mov_b32 m0, %0"
               : "=&s"(keep) : "v"(gsrc), "s"(lds_dst) : "memory");
}
#define NTM 1
template <int B> __device__ __forceinline__ u32x4 ldnt(const void* p) { if (NTM & B) return __builtin_nontemporal_load((const u32x4*)p); else return *(const u32x4*)p; }
#define MFMA32(a, b, c) __builtin_amdgcn_mfma_f32_16x16x32_bf16((a), (b), (c), 0, 0, 0)

__device__ __forceinline__ void st16_sc1(void* p, u32x4 v) { asm volatile("global_store_dwordx4 %0, %1, off sc1\n\ts_nop 1" :: "v"(p), "v"(v) : "memory"); }
__device__ __forceinline__ void st8_sc1(void* p, u32x2 v) { asm volatile("global_store_dwordx2 %0, %1, off sc1\n\ts_nop 1" :: "v"(p), "v"(v) : "memory"); }
template <bool WT> __device__ __forceinline__ void st8(void* p, u32x2 v) { if (WT) st8_sc1(p, v); else *(u32x2*)p = v; }
template <bool WT> __device__ __forceinline__ void st16(void* p, u32x4 v) { if (WT) st16_sc1(p, v); else *(u32x4*)p = v; }
__device__ __forceinline__ u32x4 ld16_sc1(const void* p) {
  u32x4 r; asm volatile("global_load_dwordx4 %0, %1, off sc1\n\ts_waitcnt vmcnt(0)" : "=&v"(r) : "v"(p) : "memory"); return r;
}
__device__ __forceinline__ void ld16x4_sc1(const void* p, u32x4& a, u32x4& b, u32x4& c, u32x4& d) {
  asm volatile("global_load_dwordx4 %0, %4, off sc1\n\tglobal_load_dwordx4 %1, %4, off offset:1024 sc1\n\t"
               "global_load_dwordx4 %2, %4, off offset:2048 sc1\n\tglobal_load_dwordx4 %3, %4, off offset:3072 sc1\n\ts_waitcnt vmcnt(0)"
               : "=&v"(a), "=&v"(b), "=&v"(c), "=&v"(d) : "v"(p) : "memory");
}
typedef __amdgpu_buffer_rsrc_t rsrc_t;
__device__ __forceinline__ rsrc_t mkrs(const void* p) { return __builtin_amdgcn_make_buffer_rsrc((void*)p, 0, 0x7FFFFFFF, 0x00020000); }
__device__ __forceinline__ u32x4 bld16(rsrc_t r, unsigned off) { return __builtin_amdgcn_raw_buffer_load_b128(r, off, 0, 16); }
__device__ __forceinline__ u32x2 bld8(rsrc_t r, unsigned off) { return __builtin_amdgcn_raw_buffer_load_b64(r, off, 0, 16); }
__device__ __forceinline__ bool ok4(u32x4 v) { return v.x != SENT && v.y != SENT && v.z != SENT && v.w != SENT; }
constexpr int SPIN_CAP = 2000000;
__device__ __forceinline__ bool give_up(int spins, unsigned* tmo, unsigned code) {
  if ((spins & 4095) != 4095) return false;
  if (spins > SPIN_CAP) { __hip_atomic_store(tmo, code, __ATOMIC_RELAXED, __HIP_MEMORY_SCOPE_AGENT); return true; }
  return __hip_atomic_load(tmo, __ATOMIC_RELAXED, __HIP_MEMORY_SCOPE_AGENT) != 0u;
}
template <bool ACQ = true>
__device__ __forceinline__ void wait_cnt(unsigned* c, unsigned n, unsigned* tmo) {
  for (int spins = 0;; ++spins) {
    const unsigned v = __hip_atomic_load(c, __ATOMIC_RELAXED, __HIP_MEMORY_SCOPE_AGENT);
    if (v >= n) break;
    __builtin_amdgcn_s_sleep(8);
    if (give_up(spins, tmo, 5)) break;
  }
  if (ACQ) {
    __builtin_amdgcn_fence(__ATOMIC_ACQUIRE, "agent");
    asm volatile("s_waitcnt vmcnt(0)" ::: "memory");
  }
}

__device__ __forceinline__ const char* uni(const char* p) {
  const unsigned long long v = (unsigned long long)p;
  const unsigned lo = __builtin_amdgcn_readfirstlane((unsigned)v), hi = __builtin_amdgcn_readfirstlane((unsigned)(v >> 32));
  return (const char*)(((unsigned long long)hi << 32) | lo);
}
#define TSCLK "s_memrealtime"
#define TC(k) do { } while (0)
#define TS(k) do { } while (0)
#define TSDECL do { } while (0)
#define TSFLUSH(mask) do { } while (0)

constexpr size_t OFF_MOD  = 0;
constexpr size_t OFF_MOD2 = 65536;
constexpr size_t OFF_TMO  = 98304;
constexpr size_t OFF_EP   = 98304 + 256;
constexpr size_t OFF_CLM  = 102400;
constexpr size_t OFF_INI  = 131072;
constexpr size_t OFF_H1   = 262144;
constexpr size_t OFF_Q    = OFF_H1 + (size_t)SEQ * DM * 2;
constexpr size_t OFF_K    = OFF_Q + (size_t)SEQ * HD * 2;
constexpr size_t OFF_V    = OFF_K + (size_t)SEQ * HD * 2;
constexpr size_t OFF_O    = OFF_V + (size_t)SEQ * HD * 2;
constexpr size_t OFF_X1   = OFF_O + (size_t)SEQ * HD * 2;
constexpr size_t OFF_H2   = OFF_X1 + (size_t)SEQ * DM * 2;
constexpr size_t OFF_G    = OFF_H2 + (size_t)SEQ * DM * 2;
constexpr size_t OFF_XCH  = (size_t)100 << 20;
constexpr size_t OFF_P    = (size_t)112 << 20;
constexpr size_t OFF_P2   = (size_t)128 << 20;
constexpr size_t OFF_DUMP = (size_t)144 << 20;
constexpr int PROW = 4 * DM * 2;
#define TLQ 1
#define TLP 1
static_assert(OFF_G + (size_t)SEQ * FF * 2 <= OFF_XCH, "layout");
__device__ __forceinline__ char* xch_base(char* ws) { return (char*)(((size_t)(ws + OFF_XCH) + ((2u << 20) - 1)) & ~(size_t)((2u << 20) - 1)); }
constexpr int PROG = 16;
constexpr int PQKV = 64;
constexpr int PL1 = 96;
constexpr int PWO = 192;
constexpr int PLN = 224;
constexpr int PF2 = 320;
constexpr int PF1 = 448;
constexpr int L_RED = 131072;
constexpr int L_SLOT = L_RED + 16384;
constexpr int L_GB = L_SLOT + 1024;
constexpr int MEGA_LDS = 163840;
static_assert(L_GB + 12288 + 2048 <= MEGA_LDS, "lds");
__device__ __forceinline__ int bswz(int row) { return (((row >> 1) & 1) | (((row >> 3) & 1) << 1)) << 1; }
template <int OFF, bool SC1 = false>
__device__ __forceinline__ void glds16s(const char* sbase, unsigned voff, unsigned lds_base) {
  unsigned keep;
  if (SC1)
    asm volatile("s_mov_b32 %0, m0\n\ts_add_u32 m0, %3, %4\n\ts_nop 0\n\tglobal_load_lds_dwordx4 %1, %2 sc1\n\ts_mov_b32 m0, %0"
                 : "=&s"(keep) : "v"(voff), "s"(sbase), "s"(lds_base), "i"(OFF) : "memory", "scc");
  else
    asm volatile("s_mov_b32 %0, m0\n\ts_add_u32 m0, %3, %4\n\ts_nop 0\n\tglobal_load_lds_dwordx4 %1, %2\n\ts_mov_b32 m0, %0"
                 : "=&s"(keep) : "v"(voff), "s"(sbase), "s"(lds_base), "i"(OFF) : "memory", "scc");
}
template <int OFF>
__device__ __forceinline__ void glds16x2s(const char* sbase, unsigned voff, unsigned lds_base) {
  unsigned keep;
  asm volatile("s_mov_b32 %0, m0\n\ts_add_u32 m0, %3, %4\n\ts_nop 0\n\tglobal_load_lds_dwordx4 %1, %2 sc1\n\tglobal_load_lds_dwordx4 %1, %2 offset:1024 sc1\n\ts_mov_b32 m0, %0"
               : "=&s"(keep) : "v"(voff), "s"(sbase), "s"(lds_base), "i"(OFF) : "memory", "scc");
}
__device__ __forceinline__ void st16s(const char* sb, unsigned vo, u32x4 v) { asm volatile("global_store_dwordx4 %0, %1, %2 sc1\n\ts_nop 1" :: "v"(vo), "v"(v), "s"(sb) : "memory"); }
__device__ __forceinline__ void head_to_lds(const char* src, int lane, unsigned lds) {
  if (TLQ) {
    const int c = (lane & 15) ^ (lane >> 4);
    const unsigned v0 = (unsigned)((c >> 2) * 2048 + (lane >> 4) * 64 + (c & 3) * 16);
#pragma unroll
    for (int i = 0; i < 8; ++i) glds16s<0, true>(src + i * 256, v0 ^ ((i & 3) * 2048), lds + i * 1024);
  } else {
#pragma unroll
    for (int i = 0; i < 4; ++i) glds16x2s<0>(src + i * 2048, (unsigned)lane * 16, lds + i * 2048);
  }
}
#define WAIT_V(n) asm volatile("s_waitcnt vmcnt(" #n ")" ::: "memory")
#define BAR() do { asm volatile("s_waitcnt lgkmcnt(0)" ::: "memory"); __builtin_amdgcn_s_barrier(); asm volatile("" ::: "memory"); } while (0)
__device__ __forceinline__ void st4_sc1(void* p, unsigned v) { asm volatile("global_store_dword %0, %1, off sc1" :: "v"(p), "v"(v) : "memory"); }
__device__ __forceinline__ unsigned ld4_sc1(const void* p) {
  unsigned r; asm volatile("global_load_dword %0, %1, off sc1\n\ts_waitcnt vmcnt(0)" : "=&v"(r) : "v"(p) : "memory"); return r;
}
template <int CTRL> __device__ __forceinline__ unsigned dppu(unsigned x) { return (unsigned)__builtin_amdgcn_update_dpp(0, (int)x, CTRL, 0xf, 0xf, false); }
__device__ __forceinline__ unsigned rowmin16(unsigned v) {
  v = min(v, dppu<0x128>(v)); v = min(v, dppu<0x124>(v)); v = min(v, dppu<0x122>(v)); v = min(v, dppu<0x121>(v));
  return v;
}
__device__ __forceinline__ unsigned rdl(unsigned v, int l) { return (unsigned)__builtin_amdgcn_readlane((int)v, l); }
constexpr float SCALE = 0.08838834764831845f;
constexpr float LOG2E = 1.4426950408889634f;
template <int N> __device__ __forceinline__ void delay64() {
  if (N >= 16) { __builtin_amdgcn_s_sleep(15); delay64<(N >= 16 ? N - 15 : 0)>(); } else if (N > 0) __builtin_amdgcn_s_sleep(N);
}
__device__ __forceinline__ void sw16(float x, float& a, float& b) {
  a = x; b = x; asm volatile("s_nop 1\n\tv_permlane16_swap_b32 %0, %1" : "+v"(a), "+v"(b));
}
__device__ __forceinline__ void sw32(float x, float& a, float& b) {
  a = x; b = x; asm volatile("s_nop 1\n\tv_permlane32_swap_b32 %0, %1" : "+v"(a), "+v"(b));
}
__device__ __forceinline__ float red16_32_max(float x) {
  float a, b; sw16(x, a, b); x = fmaxf(a, b); sw32(x, a, b); return fmaxf(a, b);
}
__device__ __forceinline__ float red16_32_add(float x) {
  float a, b; sw16(x, a, b); x = a + b; sw32(x, a, b); return a + b;
}
__device__ __forceinline__ unsigned cvtpk(float a, float b) { unsigned r; asm("v_cvt_pk_bf16_f32 %0, %1, %2" : "=v"(r) : "v"(a), "v"(b)); return r; }
__device__ __forceinline__ float max3(float a, float b, float c) { float r; asm("v_max3_f32 %0, %1, %2, %3" : "=v"(r) : "v"(a), "v"(b), "v"(c)); return r; }
__device__ __forceinline__ float max2(float a, float b) { float r; asm("v_max_f32 %0, %1, %2" : "=v"(r) : "v"(a), "v"(b)); return r; }
__device__ __forceinline__ unsigned ror8u(unsigned x) { return (unsigned)__builtin_amdgcn_update_dpp(0, (int)x, 0x128, 0xf, 0xf, false); }
__device__ __forceinline__ float ror8(float x) { return __builtin_bit_cast(float, ror8u(__builtin_bit_cast(unsigned, x))); }
__device__ __forceinline__ float add_ror8(float x) {
  return x + __builtin_bit_cast(float, __builtin_amdgcn_update_dpp(0, __builtin_bit_cast(int, x), 0x128, 0xf, 0xf, false));
}

#define RSL 4
__device__ __forceinline__ void ini_request(const char* ws, unsigned lds) {
  const char* p = uni(ws + OFF_INI);
  asm volatile("s_mov_b32 m0, %2\n\ts_nop 3\n\tglobal_load_lds_dword %0, %1 sc1\n\ts_add_u32 m0, %2, 252\n\ts_nop 0\n\tglobal_load_lds_dword %0, %1 offset:4 sc1"
               :: "v"(lane_id() * 128), "s"(p), "s"(lds) : "memory", "m0", "scc");
}
__device__ __forceinline__ unsigned ini_resolve(const char* ws, const char* l, unsigned long long tag) {
  const int lane = lane_id();
  u32x2 v = u32x2{~(unsigned)tag, 0u};
  if (l) v = u32x2{*(const volatile LAS unsigned*)(l + lane * 4), *(const volatile LAS unsigned*)(l + 256 + lane * 4)};
  for (int spins = 0; !__all(v.x == (unsigned)tag && (v.y >> 16) == (unsigned)(tag >> 48)) && spins < SPIN_CAP; ++spins) {
    if (spins) __builtin_amdgcn_s_sleep(RSL);
    asm volatile("global_load_dwordx2 %0, %1, off sc1\n\ts_waitcnt vmcnt(0)" : "=&v"(v) : "v"(ws + OFF_INI + lane * 128) : "memory");
  }
  const unsigned long long up = __ballot((v.y & 3u) == 1u), dn = __ballot((v.y & 3u) == 2u);
  unsigned par = 0;
#pragma unroll
  for (int c = 0; c < 3; ++c) { const unsigned long long m = (0x249ull << c) * 0x0001000100010001ull; par |= (__popcll(up & m) > __popcll(dn & m) ? 0u : 1u) << c; }
  par = __builtin_amdgcn_readfirstlane(par);
  asm volatile("" : "+s"(par));
  return par;
}
__device__ __forceinline__ unsigned ini_resolve1(char* smem, int wave_id_, const char* ws, unsigned long long tag) {
  volatile LAS unsigned* w = (volatile LAS unsigned*)(smem + L_SLOT + 800);
  if (WAVE_ID == 1) *w = ini_resolve(ws, nullptr, tag);
  BAR();
  return __builtin_amdgcn_readfirstlane(*w);
}
template <int M0> __device__ __forceinline__ unsigned qkv_avail(const unsigned* line) {
  const unsigned v = ld4_sc1(line + (lane_id() & 15));
  unsigned r = NSEG;
#pragma unroll
  for (int m = M0; m < 3; ++m) {
    unsigned a = rdl(v, m);
#pragma unroll
    for (int p = 1; p < 3; ++p) { const unsigned w = rdl(v, 4 * p + m); if (a >= (w >> 8)) a = max(a, w & 255u); }
    r = min(r, a);
  }
  return r;
}
__device__ __forceinline__ void wait_l1(const unsigned* cnt, unsigned n, unsigned* tmo) {
  const int lane = lane_id();
  for (int spins = 0;; ++spins) {
    const unsigned v = ld4_sc1(cnt + (PL1 + (lane & 31)) * 32);
    if (__all(v >= n)) break;
    __builtin_amdgcn_s_sleep(8);
    if (give_up(spins, tmo, 12)) break;
  }
}
#define GKV 1
__device__ __forceinline__ void wait_k0(const unsigned* cnt, int h, unsigned* tmo, bool kv = true) {
  for (int spins = 0;; ++spins) {
    const int l = lane_id();
    const unsigned v = ld4_sc1(cnt + (PQKV + h) * 32 + (l == 0 || !kv ? 3 : 1 + (l & 1)));
    if (__all(v >= (l == 0 ? 1u : (unsigned)GKV))) break;
    __builtin_amdgcn_s_sleep(32);
    if (give_up(spins, tmo, 13)) break;
  }
}
#define IS(k) do { } while (0)
#define KTON 0
#define WCLK(slot, b) do { } while (0)
#define WTM(slot, b, k) do { } while (0)
#define WTS(slot, b, k) do { } while (0)
#define KTS(k_) do { } while (0)
#define STS(ws_, slot_, k_) do { } while (0)
typedef __bf16 bf16x2 __attribute__((ext_vector_type(2)));
__device__ __forceinline__ f32x2 pkfma(f32x2 a, f32x2 b, f32x2 c) { return __builtin_elementwise_fma(a, b, c); }
__device__ __forceinline__ float wavesum(float v) {
  v += __builtin_bit_cast(float, dppu<0x128>(__builtin_bit_cast(unsigned, v))); v += __builtin_bit_cast(float, dppu<0x124>(__builtin_bit_cast(unsigned, v)));
  v += __builtin_bit_cast(float, dppu<0x122>(__builtin_bit_cast(unsigned, v))); v += __builtin_bit_cast(float, dppu<0x121>(__builtin_bit_cast(unsigned, v)));
  return red16_32_add(v);
}
__device__ __forceinline__ void bst16(rsrc_t r, unsigned off, unsigned soff, u32x4 v) { __builtin_amdgcn_raw_buffer_store_b128(v, r, off + soff, 0, 16); }
__device__ __forceinline__ float adds(unsigned v, unsigned sel, float c) { return __builtin_amdgcn_fdot2_f32_bf16(__builtin_bit_cast(bf16x2, v), __builtin_bit_cast(bf16x2, sel), c, false); }
__device__ __forceinline__ void sum4(const u32x4 (&p)[4], f32x2 (&a)[4]) {
  unsigned slo = 0x00003F80u, shi = 0x3F800000u;
  asm("" : "+s"(slo), "+s"(shi));
#pragma unroll
  for (int e = 0; e < 4; ++e) {
    float lo = bflo(p[0][e]), hi = bfhi(p[0][e]);
#pragma unroll
    for (int q = 1; q < 4; ++q) { lo = adds(p[q][e], slo, lo); hi = adds(p[q][e], shi, hi); }
    a[e] = f32x2{lo, hi};
  }
}
__device__ __forceinline__ u32x4 norm8(const f32x2 (&v)[4], const char* mt, float mb, float rden) {
  const f32x4 h0 = *(const f32x4*)(mt + 4096), h1 = *(const f32x4*)(mt + 4096 + 1024), c0 = *(const f32x4*)(mt + 8192), c1 = *(const f32x4*)(mt + 8192 + 1024);
  const f32x2 m2 = f32x2{mb, mb}, d2 = f32x2{rden, rden};
  const f32x2 r0 = pkfma(v[0] - m2, f32x2{c0[0], c0[1]} * d2, f32x2{h0[0], h0[1]}), r1 = pkfma(v[1] - m2, f32x2{c0[2], c0[3]} * d2, f32x2{h0[2], h0[3]});
  const f32x2 r2 = pkfma(v[2] - m2, f32x2{c1[0], c1[1]} * d2, f32x2{h1[0], h1[1]}), r3 = pkfma(v[3] - m2, f32x2{c1[2], c1[3]} * d2, f32x2{h1[2], h1[3]});
  return u32x4{pack2(r0[0], r0[1]), pack2(r1[0], r1[1]), pack2(r2[0], r2[1]), pack2(r3[0], r3[1])};
}
__device__ __forceinline__ void half_load(const char* P, const u16* res, int row, int hf, int lane, f32x2 (&a)[4], u32x4& rv) {
  const rsrc_t rp = mkrs(P), rr = mkrs(res);
  const unsigned po = TLP ? (unsigned)((row >> 5) * 32 * PROW + (hf * 16 + (lane >> 2)) * 2048 + (row & 31) * 64 + (lane & 3) * 16) : (unsigned)(row * PROW + hf * 1024 + lane * 16), ro = (unsigned)(row * DM * 2 + hf * 1024 + lane * 16);
  u32x4 p[4];
#pragma unroll
  for (int q = 0; q < 4; ++q) p[q] = bld16(rp, po + q * (TLP ? 65536 : 2048));
  if (res) rv = bld16(rr, ro);
  sum4(p, a);
}
__device__ __forceinline__ void fin_half(const char* P, const u16* res, const char* gbt, u16* __restrict__ o1, int row, int hf, int lane) {
  f32x2 a[4]; u32x4 rv;
  half_load(P, res, row, hf, lane, a, rv);
  const u32x4 gv = *(const u32x4*)(gbt + hf * 1024 + lane * 16);
  u32x4 o;
#pragma unroll
  for (int e = 0; e < 4; ++e) { const f32x2 s = pkfma(f32x2{bflo(gv[e]), bfhi(gv[e])}, a[e], f32x2{bflo(rv[e]), bfhi(rv[e])}); o[e] = pack2(s[0], s[1]); }
  st16_sc1(o1 + (size_t)row * DM + hf * 512 + lane * 8, o);
}
__device__ __forceinline__ void ln_half(const u32x4 (&p)[4], u32x4 rv, const f32x4 (&tb)[6], volatile LAS float* xs, const char* x1, unsigned xo, const char* h2, unsigned ho,
                                        int wave, int lane) {
  f32x2 a[4], v[4];
  sum4(p, a);
  u32x4 o;
#pragma unroll
  for (int e = 0; e < 4; ++e) {
    const f32x2 s = pkfma(f32x2{tb[e >> 1][2 * (e & 1)], tb[e >> 1][2 * (e & 1) + 1]}, a[e], f32x2{bflo(rv[e]), bfhi(rv[e])});
    o[e] = pack2(s[0], s[1]);
    v[e] = f32x2{bflo(o[e]), bfhi(o[e])};
  }
  const float c = __builtin_bit_cast(float, __builtin_amdgcn_readfirstlane(__builtin_bit_cast(int, v[0][0])));
  f32x2 s2 = f32x2{0, 0}, q2 = s2;
#pragma unroll
  for (int i = 0; i < 4; ++i) { const f32x2 d = v[i] - f32x2{c, c}; s2 += d; q2 = pkfma(d, d, q2); }
  float s = s2[0] + s2[1], q = q2[0] + q2[1];
#define DA(x, k) x += __builtin_bit_cast(float, dppu<k>(__builtin_bit_cast(unsigned, x)))
  DA(s, 0x128); DA(q, 0x128); DA(s, 0x124); DA(q, 0x124); DA(s, 0x122); DA(q, 0x122); DA(s, 0x121); DA(q, 0x121);
#undef DA
  {
    asm volatile("s_nop 1\n\tv_permlane16_swap_b32 %0, %1" : "+v"(s), "+v"(q));
    s += q; q = s;
    asm volatile("s_nop 1\n\tv_permlane32_swap_b32 %0, %1" : "+v"(s), "+v"(q));
    s += q;
  }
  if (lane < 48 && (lane & 15) == 0) xs[wave * 4 + (lane >> 4)] = lane == 32 ? c : s;
  st16s(x1, xo, o);
  BAR();
  const f32x4 u0 = *(const volatile LAS f32x4*)(xs + (wave & 6) * 4), u1 = *(const volatile LAS f32x4*)(xs + (wave | 1) * 4);
  const float m0 = u0[0] * (2.0f / DM), m1 = u1[0] * (2.0f / DM);
  const float dm = (u0[2] - u1[2]) + (m0 - m1);
  const float mean = ((u0[2] + m0) + (u1[2] + m1)) * 0.5f;
  const float qq = ((u0[1] - u0[0] * m0) + (u1[1] - u1[0] * m1)) + dm * dm * (DM / 4);
  const float mb = bfr(mean), vb = bfr(fmaxf(qq, 0.0f) * (1.0f / DM));
  const float rden = __builtin_amdgcn_rcpf(bfr(__builtin_amdgcn_sqrtf(bfr(vb + 1e-5f))));
  const f32x2 m2 = f32x2{mb, mb}, d2 = f32x2{rden, rden};
  u32x4 r;
#pragma unroll
  for (int e = 0; e < 4; ++e) {
    const f32x2 t = pkfma(v[e] - m2, f32x2{tb[4 + (e >> 1)][2 * (e & 1)], tb[4 + (e >> 1)][2 * (e & 1) + 1]} * d2, f32x2{tb[2 + (e >> 1)][2 * (e & 1)], tb[2 + (e >> 1)][2 * (e & 1) + 1]});
    r[e] = pack2(t[0], t[1]);
  }
  st16s(h2, ho, r);
}
__device__ __forceinline__ void row_table(char* tab, const float* mod, const unsigned* cnt, unsigned* tmo, int tid) {
  wait_l1(cnt, 9u, tmo);
  const rsrc_t rm = mkrs((const char*)mod + OFF_MOD2);
#pragma unroll
  for (int i = 0; i < 2; ++i) {
    const int u = tid + 512 * i, a = u >> 8, c = u & 255;
    f32x4 v = __builtin_bit_cast(f32x4, bld16(rm, (unsigned)u * 16));
    v = f32x4{bfr(v[0]), bfr(v[1]), bfr(v[2]), bfr(v[3])};
    if (a == 2) v = f32x4{bfr(1.0f + v[0]), bfr(1.0f + v[1]), bfr(1.0f + v[2]), bfr(1.0f + v[3])};
    if (a < 3) *(f32x4*)(tab + a * 4096 + ((c >> 7) * 2 + (c & 1)) * 1024 + ((c >> 1) & 63) * 16) = v;
    else *(u32x2*)(tab + 12288 + c * 8) = u32x2{pack2(v[0], v[1]), pack2(v[2], v[3])};
  }
  __syncthreads();
}
#define LS(i) do { } while (0)
#define LNSL 2
__device__ __forceinline__ void row_role(char* smem, int wave_id_, int grp, int set, const float* mod, const char* P, const u16* x, u16* x1, char* h2c,
                                         unsigned* cnt, unsigned* tmo, int slot, unsigned long long* mts) {
  const int tid = opaque_tid(), lane = tid & 63, wave = WAVE_ID;
  if (tid == 0) *(volatile LAS unsigned*)(smem + 16384 + 256) = 0u;
  row_table(smem, mod, cnt, tmo, tid);
  static_assert(TLP, "");
  const int hf = wave & 1, r4 = 4 * grp + (wave >> 1);
  const unsigned pw2 = (unsigned)(PWO + ((lane >> 2) & 3) * 8 + hf * 4 + (lane & 3)) * 128;
  unsigned* myp = cnt + (PLN + 8 * set + grp) * 32;
  unsigned pv = 0;
  u32x4 p[4], rv, rn;
  bool req = false;
  const char* pb = uni(P); const char* cb = uni((const char*)cnt); const char* xs_ = uni((const char*)x);
  auto publish = [&](int n) {
    if (lane == 0 && (__hip_atomic_fetch_add((LAS unsigned*)(smem + 16384 + 256), 1u, __ATOMIC_RELAXED, __HIP_MEMORY_SCOPE_WORKGROUP) & 7u) == 7u) st4_sc1(myp, (unsigned)n);
  };
  f32x4 tb[6];
#pragma unroll
  for (int i = 0; i < 6; ++i) tb[i] = *(const f32x4*)(smem + (i >> 1) * 4096 + hf * 2048 + (i & 1) * 1024 + lane * 16);
  const unsigned po = (unsigned)((hf * 16 + (lane >> 2)) * 2048 + r4 * 64 + (lane & 3) * 16), xo = (unsigned)(r4 * DM * 2 + hf * 1024 + lane * 16);
  const unsigned ho = (unsigned)(r4 * 128 + (((lane & 7) ^ (r4 & 7)) << 4) + (lane >> 3) * 4096 + hf * 32768);
  rn = *(const u32x4*)(xs_ + set * 65536 + xo);
  for (int bb = set; bb < NSEG; bb += 2) {
    if (!req) {
      for (int spins = 0; !__all(pv > (unsigned)bb); ++spins) {
        pv = ld4_sc1((const char*)cnt + pw2);
        if (__all(pv > (unsigned)bb)) break;
        __builtin_amdgcn_s_sleep(LNSL);
        if (give_up(spins, tmo, 11)) break;
      }
    }
    WTM(slot, bb, 0);
    LS(0);
    asm volatile("s_nop 4\n\tglobal_load_dwordx4 %0, %5, %9 sc1\n\tglobal_load_dwordx4 %1, %6, %9 sc1\n\tglobal_load_dwordx4 %2, %7, %9 sc1\n\tglobal_load_dwordx4 %3, %8, %9 sc1\n\t"
                 "global_load_dword %4, %10, %11 sc1\n\ts_waitcnt vmcnt(0)"
                 : "=&v"(p[0]), "=&v"(p[1]), "=&v"(p[2]), "=&v"(p[3]), "=&v"(pv)
                 : "v"(po), "v"(po + 65536), "v"(po + 131072), "v"(po + 196608), "s"(pb + (size_t)bb * 32 * PROW), "v"(pw2), "s"(cb) : "memory");
    asm volatile("v_mov_b32 %0, %4\n\tv_mov_b32 %1, %5\n\tv_mov_b32 %2, %6\n\tv_mov_b32 %3, %7" : "=&v"(rv.x), "=&v"(rv.y), "=&v"(rv.z), "=&v"(rv.w) : "v"(rn.x), "v"(rn.y), "v"(rn.z), "v"(rn.w) : "memory");
    if (bb + 2 < NSEG) rn = *(const u32x4*)(xs_ + (size_t)(bb + 2) * 65536 + xo);
    LS(1);
    if (req) publish(bb >> 1);
    ln_half(p, rv, tb, (volatile LAS float*)(smem + 16384 + ((bb >> 1) & 1) * 128), uni((const char*)x1) + (size_t)bb * 65536, xo, uni(h2c) + (size_t)bb * 65536, ho, wave, lane);
    LS(2);
    req = bb + 2 < NSEG && __all(pv > (unsigned)(bb + 2));
    if (!req) {
      if (bb + 2 < NSEG) pv = ld4_sc1((const char*)cnt + pw2);
      WAIT_V(0);
      LS(3);
      publish((bb >> 1) + 1);
    }
    WTM(slot, bb, 2);
  }
}
#define L1G 0
__device__ __forceinline__ void l1_role(char* smem, int wave_id_, int c, const u16* __restrict__ x, const float* __restrict__ mod, const u16* __restrict__ cvec,
                                        const u16* __restrict__ wmod, char* __restrict__ h1c, unsigned*& cnt, unsigned* tmo, unsigned long long* mts, unsigned long long tag) {
  const int tid = opaque_tid(), lane = tid & 63, wave = WAVE_ID;
  u32x4 xr[8][2];
  const u16* src = x + (size_t)(8 * c + wave) * DM + lane * 8;
  xr[0][0] = ldnt<2>(src); xr[0][1] = ldnt<2>(src + 512);
  {
    const float sva = bf2f(cvec[tid]), svb = bf2f(cvec[tid + 512]);
    cnt += (ini_resolve1(smem, wave_id_, (const char*)mod - OFF_MOD, tag) >> 2) << 11;
    if (wave == 7) IS(6);
    f32x4 m[4];
    const rsrc_t rm = mkrs(mod);
#pragma unroll
    for (int k = 0; k < 4; ++k) m[k] = __builtin_bit_cast(f32x4, bld16(rm, (unsigned)(k * (2 * DM) + tid * 4) * 4));
    f32x4 v = (m[0] + m[1]) + (m[2] + m[3]);
    v = f32x4{bfr(v[0]), bfr(v[1]), bfr(v[2]), bfr(v[3])};
    const int a = tid >> 8, cc = tid & 255;
    if (a) v = f32x4{bfr(1.0f + v[0]), bfr(1.0f + v[1]), bfr(1.0f + v[2]), bfr(1.0f + v[3])};
    *(f32x4*)(smem + 4096 + a * 4096 + ((cc >> 7) * 2 + (cc & 1)) * 1024 + ((cc >> 1) & 63) * 16) = v;
    float* sv = (float*)(smem + 12288);
    sv[tid] = bfr(sva / (1.0f + __expf(-sva))); sv[tid + 512] = bfr(svb / (1.0f + __expf(-svb)));
  }
  __syncthreads();
  if (wave == 7) IS(7);
#pragma unroll
  for (int p = 0; p < 8; ++p) {
    f32x2 v[2][4];
#pragma unroll
    for (int i = 0; i < 2; ++i)
#pragma unroll
      for (int e = 0; e < 4; ++e) v[i][e] = f32x2{bflo(xr[p][i][e]), bfhi(xr[p][i][e])};
    const f32x2 s2 = ((v[0][0] + v[0][1]) + (v[0][2] + v[0][3])) + ((v[1][0] + v[1][1]) + (v[1][2] + v[1][3]));
    const float mean = wavesum(s2[0] + s2[1]) * (1.0f / DM);
    f32x2 q2 = f32x2{0, 0};
#pragma unroll
    for (int i = 0; i < 8; ++i) { const f32x2 d = v[i >> 2][i & 3] - f32x2{mean, mean}; q2 = pkfma(d, d, q2); }
    const float q = wavesum(q2[0] + q2[1]);
    if (p == 0 && wave == 0) STS((char*)tmo - OFF_TMO, 128 + (c >> 3) * 32 + 24 + (c & 7), 0);
    const float mb = bfr(mean), vb = bfr(q * (1.0f / DM));
    const float rden = 1.0f / bfr(sqrtf(bfr(vb + 1e-5f)));
    const int row = 256 * p + 8 * c + wave, rb = row & 31;
    char* dst = h1c + (size_t)(row >> 5) * 65536 + (lane >> 3) * 4096 + rb * 128 + (((lane & 7) ^ (rb & 7)) << 4);
    const char* ml = smem + lane * 16;
#pragma unroll
    for (int i = 0; i < 2; ++i) st16_sc1(dst + i * 32768, norm8(v[i], ml + i * 2048, mb, rden));
    WAIT_V(0);
    __builtin_amdgcn_s_barrier();
    if (tid == 0) st4_sc1(cnt + (PL1 + c) * 32, (unsigned)(p + 1));
    if (p == 0) {
      if (L1G) wait_k0(cnt, c, tmo, L1G == 1);
#pragma unroll
      for (int pp = 1; pp < 8; ++pp) { xr[pp][0] = ldnt<2>(src + (size_t)pp * 256 * DM); xr[pp][1] = ldnt<2>(src + (size_t)pp * 256 * DM + 512); }
    }
    if (wave == 0) WTS(c, p, 0);
    if (p == 0 && wave == 0) STS((char*)tmo - OFF_TMO, 128 + (c >> 3) * 32 + 24 + (c & 7), 1);
  }
  wait_k0(cnt, c, tmo);
  {
    const int cg = tid & 15, r0 = tid >> 4;
    const u16* wp = wmod + (size_t)r0 * (6 * DM) + 2 * DM + 128 * c + cg * 8;
    const float* sv = (const float*)(smem + 12288) + r0;
    f32x2 acc[4] = {f32x2{0, 0}, f32x2{0, 0}, f32x2{0, 0}, f32x2{0, 0}};
#pragma unroll 1
    for (int o = 0; o < 4; ++o) {
      u32x4 w[8];
#pragma unroll
      for (int i = 0; i < 8; ++i) w[i] = ldnt<1>(wp + (size_t)(o * 8 + i) * 32 * (6 * DM));
#pragma unroll
      for (int i = 0; i < 8; ++i) {
        const float s = sv[(o * 8 + i) * 32];
#pragma unroll
        for (int e = 0; e < 4; ++e) acc[e] = pkfma(f32x2{s, s}, f32x2{bflo(w[i][e]), bfhi(w[i][e])}, acc[e]);
      }
    }
    float* red = (float*)(smem + 16384);
    *(f32x4*)(red + r0 * 128 + cg * 8) = f32x4{acc[0][0], acc[0][1], acc[1][0], acc[1][1]};
    *(f32x4*)(red + r0 * 128 + cg * 8 + 4) = f32x4{acc[2][0], acc[2][1], acc[3][0], acc[3][1]};
    __syncthreads();
    if (tid < 128) {
      float s = 0;
#pragma unroll
      for (int r = 0; r < 32; ++r) s += red[r * 128 + tid];
      st4_sc1((char*)mod + OFF_MOD2 + (128 * c + tid) * 4, __builtin_bit_cast(unsigned, s));
    }
    WAIT_V(0);
    __builtin_amdgcn_s_barrier();
    if (tid == 0) st4_sc1(cnt + (PL1 + c) * 32, 9u);
  }
}
enum { R_QKV = 0, R_PART = 1, R_F1 = 2, R_PART2 = 3 };
struct Dep {
  const char* base; unsigned voff; unsigned mul, add;
};
__device__ __forceinline__ unsigned dep_eval(unsigned v, const Dep& d) {
  v = rowmin16(v * d.mul + d.add);
  return min(min(rdl(v, 0), rdl(v, 16)), min(rdl(v, 32), rdl(v, 48)));
}
typedef unsigned u32x32 __attribute__((ext_vector_type(32)));
typedef unsigned u32x16 __attribute__((ext_vector_type(16)));
__device__ __forceinline__ void fast_ld0(const u32x32 (&W)[4], u32x16& pd, unsigned ea, unsigned da, int& b, int& lf, int cp, unsigned sa, unsigned mu, unsigned ad,
    unsigned rw, unsigned ab, const char* p1, const char* p0, unsigned l1, unsigned l0, const unsigned (&av)[8]) {
  asm volatile("s_setprio 0\n\t"
    "s_waitcnt vmcnt(12)\n\t"
    "s_barrier\n\t"
    "ds_read_b128 v[96:99], %[ea] offset:0\n\t"
    "ds_read_b128 v[100:103], %[ea] offset:2048\n\t"
    "ds_read_b128 v[104:107], %[da] offset:0\n\t"
    "ds_read_b128 v[108:111], %[da] offset:2048\n\t"
    "ds_read_b128 v[112:115], %[ea] offset:4096\n\t"
    "ds_read_b128 v[116:119], %[ea] offset:6144\n\t"
    "s_branch 2f\n\t"
    "1:\n\t"
    "s_waitcnt vmcnt(12)\n\t"
    "s_nop 1\n\t"
    "s_barrier\n\t"
    "2:\n\t"
    "s_waitcnt lgkmcnt(4)\n\t"
    "v_mfma_f32_16x16x32_bf16 v[64:67], v[128:131], v[96:99], 0\n\t"
    "ds_read_b128 v[120:123], %[da] offset:4096\n\t"
    "s_add_u32 m0, %[l1], 0\n\t"
    "ds_write_b128 %[rw], v[80:83] offset:0\n\t"
    "v_mfma_f32_16x16x32_bf16 v[68:71], v[132:135], v[96:99], 0\n\t"
    "ds_read_b128 v[124:127], %[da] offset:6144\n\t"
    "global_load_lds_dwordx4 %[av0], s[36:37] sc1\n\t"
    "ds_write_b128 %[rw], v[84:87] offset:1024\n\t"
    "v_mfma_f32_16x16x32_bf16 v[72:75], v[128:131], v[100:103], 0\n\t"
    "s_add_u32 m0, %[l1], 32768\n\t"
    "ds_write_b128 %[rw], v[88:91] offset:2048\n\t"
    "v_mfma_f32_16x16x32_bf16 v[76:79], v[132:135], v[100:103], 0\n\t"
    "global_load_lds_dwordx4 %[av1], s[36:37] sc1\n\t"
    "ds_write_b128 %[rw], v[92:95] offset:3072\n\t"
    "s_waitcnt lgkmcnt(8)\n\t"
    "v_mfma_f32_16x16x32_bf16 v[64:67], v[136:139], v[104:107], v[64:67]\n\t"
    "ds_read_b128 v[96:99], %[ea] offset:8192\n\t"
    "s_add_u32 m0, %[l1], 4096\n\t"
    "v_mfma_f32_16x16x32_bf16 v[68:71], v[140:143], v[104:107], v[68:71]\n\t"
    "ds_read_b128 v[100:103], %[ea] offset:10240\n\t"
    "global_load_lds_dwordx4 %[av2], s[36:37] sc1\n\t"
    "v_mfma_f32_16x16x32_bf16 v[72:75], v[136:139], v[108:111], v[72:75]\n\t"
    "s_add_u32 m0, %[l1], 36864\n\t"
    "v_mfma_f32_16x16x32_bf16 v[76:79], v[140:143], v[108:111], v[76:79]\n\t"
    "global_load_lds_dwordx4 %[av3], s[36:37] sc1\n\t"
    "s_waitcnt lgkmcnt(8)\n\t"
    "v_mfma_f32_16x16x32_bf16 v[64:67], v[144:147], v[112:115], v[64:67]\n\t"
    "ds_read_b128 v[104:107], %[da] offset:8192\n\t"
    "s_add_u32 m0, %[l1], 8192\n\t"
    "v_mfma_f32_16x16x32_bf16 v[68:71], v[148:151], v[112:115], v[68:71]\n\t"
    "ds_read_b128 v[108:111], %[da] offset:10240\n\t"
    "global_load_lds_dwordx4 %[av4], s[36:37] sc1\n\t"
    "v_mfma_f32_16x16x32_bf16 v[72:75], v[144:147], v[116:119], v[72:75]\n\t"
    "s_add_u32 m0, %[l1], 40960\n\t"
    "v_mfma_f32_16x16x32_bf16 v[76:79], v[148:151], v[116:119], v[76:79]\n\t"
    "global_load_lds_dwordx4 %[av5], s[36:37] sc1\n\t"
    "s_waitcnt lgkmcnt(7)\n\t"
    "v_mfma_f32_16x16x32_bf16 v[64:67], v[152:155], v[120:123], v[64:67]\n\t"
    "ds_read_b128 v[112:115], %[ea] offset:12288\n\t"
    "s_add_u32 m0, %[l1], 12288\n\t"
    "v_mfma_f32_16x16x32_bf16 v[68:71], v[156:159], v[120:123], v[68:71]\n\t"
    "ds_read_b128 v[116:119], %[ea] offset:14336\n\t"
    "global_load_lds_dwordx4 %[av6], s[36:37] sc1\n\t"
    "v_mfma_f32_16x16x32_bf16 v[72:75], v[152:155], v[124:127], v[72:75]\n\t"
    "s_add_u32 m0, %[l1], 45056\n\t"
    "v_mfma_f32_16x16x32_bf16 v[76:79], v[156:159], v[124:127], v[76:79]\n\t"
    "global_load_lds_dwordx4 %[av7], s[36:37] sc1\n\t"
    "s_waitcnt lgkmcnt(4)\n\t"
    "v_mfma_f32_16x16x32_bf16 v[64:67], v[160:163], v[96:99], v[64:67]\n\t"
    "ds_read_b128 v[120:123], %[da] offset:12288\n\t"
    "v_mfma_f32_16x16x32_bf16 v[68:71], v[164:167], v[96:99], v[68:71]\n\t"
    "ds_read_b128 v[124:127], %[da] offset:14336\n\t"
    "v_mfma_f32_16x16x32_bf16 v[72:75], v[160:163], v[100:103], v[72:75]\n\t"
    "v_mfma_f32_16x16x32_bf16 v[76:79], v[164:167], v[100:103], v[76:79]\n\t"
    "s_waitcnt lgkmcnt(4)\n\t"
    "v_mfma_f32_16x16x32_bf16 v[64:67], v[168:171], v[104:107], v[64:67]\n\t"
    "ds_read_b128 v[96:99], %[ea] offset:16384\n\t"
    "v_mfma_f32_16x16x32_bf16 v[68:71], v[172:175], v[104:107], v[68:71]\n\t"
    "ds_read_b128 v[100:103], %[ea] offset:18432\n\t"
    "v_mfma_f32_16x16x32_bf16 v[72:75], v[168:171], v[108:111], v[72:75]\n\t"
    "v_mfma_f32_16x16x32_bf16 v[76:79], v[172:175], v[108:111], v[76:79]\n\t"
    "s_waitcnt lgkmcnt(4)\n\t"
    "v_mfma_f32_16x16x32_bf16 v[64:67], v[176:179], v[112:115], v[64:67]\n\t"
    "ds_read_b128 v[104:107], %[da] offset:16384\n\t"
    "v_mfma_f32_16x16x32_bf16 v[68:71], v[180:183], v[112:115], v[68:71]\n\t"
    "ds_read_b128 v[108:111], %[da] offset:18432\n\t"
    "v_mfma_f32_16x16x32_bf16 v[72:75], v[176:179], v[116:119], v[72:75]\n\t"
    "v_mfma_f32_16x16x32_bf16 v[76:79], v[180:183], v[116:119], v[76:79]\n\t"
    "s_waitcnt lgkmcnt(4)\n\t"
    "v_mfma_f32_16x16x32_bf16 v[64:67], v[184:187], v[120:123], v[64:67]\n\t"
    "ds_read_b128 v[112:115], %[ea] offset:20480\n\t"
    "v_mfma_f32_16x16x32_bf16 v[68:71], v[188:191], v[120:123], v[68:71]\n\t"
    "ds_read_b128 v[116:119], %[ea] offset:22528\n\t"
    "v_mfma_f32_16x16x32_bf16 v[72:75], v[184:187], v[124:127], v[72:75]\n\t"
    "v_mfma_f32_16x16x32_bf16 v[76:79], v[188:191], v[124:127], v[76:79]\n\t"
    "s_waitcnt vmcnt(12)\n\t"
    "s_barrier\n\t"
    "s_waitcnt lgkmcnt(4)\n\t"
    "v_mfma_f32_16x16x32_bf16 v[64:67], v[192:195], v[96:99], v[64:67]\n\t"
    "ds_read_b128 v[120:123], %[da] offset:20480\n\t"
    "s_add_u32 m0, %[l0], 0\n\t"
    "v_mfma_f32_16x16x32_bf16 v[68:71], v[196:199], v[96:99], v[68:71]\n\t"
    "ds_read_b128 v[124:127], %[da] offset:22528\n\t"
    "global_load_lds_dwordx4 %[av0], s[38:39] sc1\n\t"
    "v_mfma_f32_16x16x32_bf16 v[72:75], v[192:195], v[100:103], v[72:75]\n\t"
    "s_add_u32 m0, %[l0], 32768\n\t"
    "v_mfma_f32_16x16x32_bf16 v[76:79], v[196:199], v[100:103], v[76:79]\n\t"
    "global_load_lds_dwordx4 %[av1], s[38:39] sc1\n\t"
    "s_waitcnt lgkmcnt(4)\n\t"
    "v_mfma_f32_16x16x32_bf16 v[64:67], v[200:203], v[104:107], v[64:67]\n\t"
    "ds_read_b128 v[96:99], %[ea] offset:24576\n\t"
    "s_add_u32 m0, %[l0], 4096\n\t"
    "v_mfma_f32_16x16x32_bf16 v[68:71], v[204:207], v[104:107], v[68:71]\n\t"
    "ds_read_b128 v[100:103], %[ea] offset:26624\n\t"
    "global_load_lds_dwordx4 %[av2], s[38:39] sc1\n\t"
    "v_mfma_f32_16x16x32_bf16 v[72:75], v[200:203], v[108:111], v[72:75]\n\t"
    "s_add_u32 m0, %[l0], 36864\n\t"
    "v_mfma_f32_16x16x32_bf16 v[76:79], v[204:207], v[108:111], v[76:79]\n\t"
    "global_load_lds_dwordx4 %[av3], s[38:39] sc1\n\t"
    "s_add_u32 s36, s36, %[ab]\n\t"
    "s_addc_u32 s37, s37, 0\n\t"
    "s_waitcnt lgkmcnt(4)\n\t"
    "v_mfma_f32_16x16x32_bf16 v[64:67], v[208:211], v[112:115], v[64:67]\n\t"
    "ds_read_b128 v[104:107], %[da] offset:24576\n\t"
    "s_add_u32 m0, %[l0], 8192\n\t"
    "v_mfma_f32_16x16x32_bf16 v[68:71], v[212:215], v[112:115], v[68:71]\n\t"
    "ds_read_b128 v[108:111], %[da] offset:26624\n\t"
    "global_load_lds_dwordx4 %[av4], s[38:39] sc1\n\t"
    "v_mfma_f32_16x16x32_bf16 v[72:75], v[208:211], v[116:119], v[72:75]\n\t"
    "ds_read_b32 v39, %[sa]\n\t"
    "s_add_u32 m0, %[l0], 40960\n\t"
    "v_mfma_f32_16x16x32_bf16 v[76:79], v[212:215], v[116:119], v[76:79]\n\t"
    "v_xor_b32 %[sa], 0x100, %[sa]\n\t"
    "global_load_lds_dwordx4 %[av5], s[38:39] sc1\n\t"
    "s_xor_b32 %[l1], %[l1], 0x10000\n\t"
    "s_waitcnt lgkmcnt(5)\n\t"
    "v_mfma_f32_16x16x32_bf16 v[64:67], v[216:219], v[120:123], v[64:67]\n\t"
    "ds_read_b128 v[112:115], %[ea] offset:28672\n\t"
    "s_add_u32 m0, %[l0], 12288\n\t"
    "v_mfma_f32_16x16x32_bf16 v[68:71], v[220:223], v[120:123], v[68:71]\n\t"
    "ds_read_b128 v[116:119], %[ea] offset:30720\n\t"
    "global_load_lds_dwordx4 %[av6], s[38:39] sc1\n\t"
    "v_mfma_f32_16x16x32_bf16 v[72:75], v[216:219], v[124:127], v[72:75]\n\t"
    "v_xor_b32 %[ea], 0x10000, %[ea]\n\t"
    "s_add_u32 m0, %[l0], 45056\n\t"
    "v_mfma_f32_16x16x32_bf16 v[76:79], v[220:223], v[124:127], v[76:79]\n\t"
    "global_load_lds_dwordx4 %[av7], s[38:39] sc1\n\t"
    "s_waitcnt lgkmcnt(5)\n\t"
    "v_mfma_f32_16x16x32_bf16 v[64:67], v[224:227], v[96:99], v[64:67]\n\t"
    "ds_read_b128 v[120:123], %[da] offset:28672\n\t"
    "v_mfma_f32_16x16x32_bf16 v[68:71], v[228:231], v[96:99], v[68:71]\n\t"
    "ds_read_b128 v[124:127], %[da] offset:30720\n\t"
    "v_mfma_f32_16x16x32_bf16 v[72:75], v[224:227], v[100:103], v[72:75]\n\t"
    "v_xor_b32 %[da], 0x10000, %[da]\n\t"
    "v_mfma_f32_16x16x32_bf16 v[76:79], v[228:231], v[100:103], v[76:79]\n\t"
    "s_waitcnt lgkmcnt(5)\n\t"
    "v_mfma_f32_16x16x32_bf16 v[64:67], v[232:235], v[104:107], v[64:67]\n\t"
    "ds_read_b128 v[96:99], %[ea] offset:0\n\t"
    "v_mfma_f32_16x16x32_bf16 v[68:71], v[236:239], v[104:107], v[68:71]\n\t"
    "ds_read_b128 v[100:103], %[ea] offset:2048\n\t"
    "v_mfma_f32_16x16x32_bf16 v[72:75], v[232:235], v[108:111], v[72:75]\n\t"
    "v_mfma_f32_16x16x32_bf16 v[76:79], v[236:239], v[108:111], v[76:79]\n\t"
    "s_waitcnt lgkmcnt(4)\n\t"
    "v_mfma_f32_16x16x32_bf16 v[64:67], v[240:243], v[112:115], v[64:67]\n\t"
    "ds_read_b128 v[104:107], %[da] offset:0\n\t"
    "v_mad_u32_u24 v39, v39, %[mu], %[ad]\n\t"
    "v_mfma_f32_16x16x32_bf16 v[68:71], v[244:247], v[112:115], v[68:71]\n\t"
    "ds_read_b128 v[108:111], %[da] offset:2048\n\t"
    "v_cmp_ge_i32 vcc, %[lf], v39\n\t"
    "s_cmp_eq_u64 vcc, 0\n\t"
    "s_addc_u32 %[lf], %[lf], 0\n\t"
    "v_mfma_f32_16x16x32_bf16 v[72:75], v[240:243], v[116:119], v[72:75]\n\t"
    "v_mfma_f32_16x16x32_bf16 v[76:79], v[244:247], v[116:119], v[76:79]\n\t"
    "v_cmp_ge_i32 vcc, %[lf], v39\n\t"
    "s_cmp_eq_u64 vcc, 0\n\t"
    "s_addc_u32 %[lf], %[lf], 0\n\t"
    "s_waitcnt lgkmcnt(4)\n\t"
    "v_mfma_f32_16x16x32_bf16 v[80:83], v[248:251], v[120:123], v[64:67]\n\t"
    "ds_read_b128 v[112:115], %[ea] offset:4096\n\t"
    "s_min_i32 %[lf], %[lf], %[cp]\n\t"
    "s_add_u32 %[b], %[b], 1\n\t"
    "v_mfma_f32_16x16x32_bf16 v[84:87], v[252:255], v[120:123], v[68:71]\n\t"
    "ds_read_b128 v[116:119], %[ea] offset:6144\n\t"
    "v_mfma_f32_16x16x32_bf16 v[88:91], v[248:251], v[124:127], v[72:75]\n\t"
    "v_mfma_f32_16x16x32_bf16 v[92:95], v[252:255], v[124:127], v[76:79]\n\t"
    "s_add_u32 s38, s38, %[ab]\n\t"
    "s_addc_u32 s39, s39, 0\n\t"
    "s_xor_b32 %[l0], %[l0], 0x10000\n\t"
    "s_cmp_lt_i32 %[b], %[lf]\n\t"
    "s_cbranch_scc1 1b\n\t"
    "s_waitcnt lgkmcnt(0)\n\t"
    "s_nop 4"
    : "=&{v[80:95]}"(pd), [ea] "+&v"(ea), [da] "+&v"(da), [b] "+&s"(b), [lf] "+&s"(lf), "+{s[36:37]}"(p1), "+{s[38:39]}"(p0), [sa] "+&v"(sa), [l1] "+&s"(l1), [l0] "+&s"(l0)
    : [rw] "v"(rw), [ab] "s"(ab), [cp] "s"(cp), [mu] "s"(mu), [ad] "v"(ad), "{v[128:159]}"(W[0]), "{v[160:191]}"(W[1]), "{v[192:223]}"(W[2]), "{v[224:255]}"(W[3]), [av0] "v"(av[0]), [av1] "v"(av[1]), [av2] "v"(av[2]), [av3] "v"(av[3]), [av4] "v"(av[4]), [av5] "v"(av[5]), [av6] "v"(av[6]), [av7] "v"(av[7])
    : "v39", "v64", "v65", "v66", "v67", "v68", "v69", "v70", "v71", "v72", "v73", "v74", "v75", "v76", "v77", "v78", "v79", "v96", "v97", "v98", "v99", "v100", "v101", "v102", "v103", "v104", "v105", "v106", "v107", "v108", "v109", "v110", "v111", "v112", "v113", "v114", "v115", "v116", "v117", "v118", "v119", "v120", "v121", "v122", "v123", "v124", "v125", "v126", "v127", "vcc", "scc", "m0", "memory");
}
__device__ __forceinline__ void fast_ld2(const u32x32 (&W)[4], u32x16& pd, unsigned ea, unsigned da, int& b, int& lf, int cp, unsigned sa, unsigned mu, unsigned ad,
    unsigned rw, unsigned ab, const char* p1, const char* p0, unsigned l1, unsigned l0, const unsigned (&av)[8]) {
  asm volatile("s_setprio 0\n\t"
    "s_waitcnt vmcnt(6)\n\t"
    "s_barrier\n\t"
    "ds_read_b128 v[96:99], %[ea] offset:0\n\t"
    "ds_read_b128 v[100:103], %[ea] offset:2048\n\t"
    "ds_read_b128 v[104:107], %[da] offset:0\n\t"
    "ds_read_b128 v[108:111], %[da] offset:2048\n\t"
    "ds_read_b128 v[112:115], %[ea] offset:4096\n\t"
    "ds_read_b128 v[116:119], %[ea] offset:6144\n\t"
    "s_branch 2f\n\t"
    "1:\n\t"
    "s_waitcnt vmcnt(6)\n\t"
    "s_nop 1\n\t"
    "s_barrier\n\t"
    "2:\n\t"
    "s_waitcnt lgkmcnt(4)\n\t"
    "v_mfma_f32_16x16x32_bf16 v[64:67], v[128:131], v[96:99], 0\n\t"
    "ds_read_b128 v[120:123], %[da] offset:4096\n\t"
    "s_add_u32 m0, %[l1], 0\n\t"
    "ds_write_b128 %[rw], v[80:83] offset:0\n\t"
    "v_mfma_f32_16x16x32_bf16 v[68:71], v[132:135], v[96:99], 0\n\t"
    "ds_read_b128 v[124:127], %[da] offset:6144\n\t"
    "global_load_lds_dwordx4 %[av0], s[36:37] sc1\n\t"
    "ds_write_b128 %[rw], v[84:87] offset:1024\n\t"
    "v_mfma_f32_16x16x32_bf16 v[72:75], v[128:131], v[100:103], 0\n\t"
    "ds_write_b128 %[rw], v[88:91] offset:2048\n\t"
    "v_mfma_f32_16x16x32_bf16 v[76:79], v[132:135], v[100:103], 0\n\t"
    "ds_write_b128 %[rw], v[92:95] offset:3072\n\t"
    "s_waitcnt lgkmcnt(8)\n\t"
    "v_mfma_f32_16x16x32_bf16 v[64:67], v[136:139], v[104:107], v[64:67]\n\t"
    "ds_read_b128 v[96:99], %[ea] offset:8192\n\t"
    "s_add_u32 m0, %[l1], 4096\n\t"
    "v_mfma_f32_16x16x32_bf16 v[68:71], v[140:143], v[104:107], v[68:71]\n\t"
    "ds_read_b128 v[100:103], %[ea] offset:10240\n\t"
    "global_load_lds_dwordx4 %[av2], s[36:37] sc1\n\t"
    "v_mfma_f32_16x16x32_bf16 v[72:75], v[136:139], v[108:111], v[72:75]\n\t"
    "v_mfma_f32_16x16x32_bf16 v[76:79], v[140:143], v[108:111], v[76:79]\n\t"
    "s_waitcnt lgkmcnt(8)\n\t"
    "v_mfma_f32_16x16x32_bf16 v[64:67], v[144:147], v[112:115], v[64:67]\n\t"
    "ds_read_b128 v[104:107], %[da] offset:8192\n\t"
    "s_add_u32 m0, %[l1], 8192\n\t"
    "v_mfma_f32_16x16x32_bf16 v[68:71], v[148:151], v[112:115], v[68:71]\n\t"
    "ds_read_b128 v[108:111], %[da] offset:10240\n\t"
    "global_load_lds_dwordx4 %[av4], s[36:37] sc1\n\t"
    "v_mfma_f32_16x16x32_bf16 v[72:75], v[144:147], v[116:119], v[72:75]\n\t"
    "v_mfma_f32_16x16x32_bf16 v[76:79], v[148:151], v[116:119], v[76:79]\n\t"
    "s_waitcnt lgkmcnt(7)\n\t"
    "v_mfma_f32_16x16x32_bf16 v[64:67], v[152:155], v[120:123], v[64:67]\n\t"
    "ds_read_b128 v[112:115], %[ea] offset:12288\n\t"
    "s_add_u32 m0, %[l1], 12288\n\t"
    "v_mfma_f32_16x16x32_bf16 v[68:71], v[156:159], v[120:123], v[68:71]\n\t"
    "ds_read_b128 v[116:119], %[ea] offset:14336\n\t"
    "global_load_lds_dwordx4 %[av6], s[36:37] sc1\n\t"
    "v_mfma_f32_16x16x32_bf16 v[72:75], v[152:155], v[124:127], v[72:75]\n\t"
    "v_mfma_f32_16x16x32_bf16 v[76:79], v[156:159], v[124:127], v[76:79]\n\t"
    "s_waitcnt lgkmcnt(4)\n\t"
    "v_mfma_f32_16x16x32_bf16 v[64:67], v[160:163], v[96:99], v[64:67]\n\t"
    "ds_read_b128 v[120:123], %[da] offset:12288\n\t"
    "v_mfma_f32_16x16x32_bf16 v[68:71], v[164:167], v[96:99], v[68:71]\n\t"
    "ds_read_b128 v[124:127], %[da] offset:14336\n\t"
    "v_mfma_f32_16x16x32_bf16 v[72:75], v[160:163], v[100:103], v[72:75]\n\t"
    "v_mfma_f32_16x16x32_bf16 v[76:79], v[164:167], v[100:103], v[76:79]\n\t"
    "s_waitcnt lgkmcnt(4)\n\t"
    "v_mfma_f32_16x16x32_bf16 v[64:67], v[168:171], v[104:107], v[64:67]\n\t"
    "ds_read_b128 v[96:99], %[ea] offset:16384\n\t"
    "v_mfma_f32_16x16x32_bf16 v[68:71], v[172:175], v[104:107], v[68:71]\n\t"
    "ds_read_b128 v[100:103], %[ea] offset:18432\n\t"
    "v_mfma_f32_16x16x32_bf16 v[72:75], v[168:171], v[108:111], v[72:75]\n\t"
    "v_mfma_f32_16x16x32_bf16 v[76:79], v[172:175], v[108:111], v[76:79]\n\t"
    "s_waitcnt lgkmcnt(4)\n\t"
    "v_mfma_f32_16x16x32_bf16 v[64:67], v[176:179], v[112:115], v[64:67]\n\t"
    "ds_read_b128 v[104:107], %[da] offset:16384\n\t"
    "v_mfma_f32_16x16x32_bf16 v[68:71], v[180:183], v[112:115], v[68:71]\n\t"
    "ds_read_b128 v[108:111], %[da] offset:18432\n\t"
    "v_mfma_f32_16x16x32_bf16 v[72:75], v[176:179], v[116:119], v[72:75]\n\t"
    "v_mfma_f32_16x16x32_bf16 v[76:79], v[180:183], v[116:119], v[76:79]\n\t"
    "s_waitcnt lgkmcnt(4)\n\t"
    "v_mfma_f32_16x16x32_bf16 v[64:67], v[184:187], v[120:123], v[64:67]\n\t"
    "ds_read_b128 v[112:115], %[ea] offset:20480\n\t"
    "v_mfma_f32_16x16x32_bf16 v[68:71], v[188:191], v[120:123], v[68:71]\n\t"
    "ds_read_b128 v[116:119], %[ea] offset:22528\n\t"
    "v_mfma_f32_16x16x32_bf16 v[72:75], v[184:187], v[124:127], v[72:75]\n\t"
    "v_mfma_f32_16x16x32_bf16 v[76:79], v[188:191], v[124:127], v[76:79]\n\t"
    "s_waitcnt vmcnt(6)\n\t"
    "s_barrier\n\t"
    "s_waitcnt lgkmcnt(4)\n\t"
    "v_mfma_f32_16x16x32_bf16 v[64:67], v[192:195], v[96:99], v[64:67]\n\t"
    "ds_read_b128 v[120:123], %[da] offset:20480\n\t"
    "s_add_u32 m0, %[l0], 0\n\t"
    "v_mfma_f32_16x16x32_bf16 v[68:71], v[196:199], v[96:99], v[68:71]\n\t"
    "ds_read_b128 v[124:127], %[da] offset:22528\n\t"
    "global_load_lds_dwordx4 %[av0], s[38:39] sc1\n\t"
    "v_mfma_f32_16x16x32_bf16 v[72:75], v[192:195], v[100:103], v[72:75]\n\t"
    "v_mfma_f32_16x16x32_bf16 v[76:79], v[196:199], v[100:103], v[76:79]\n\t"
    "s_waitcnt lgkmcnt(4)\n\t"
    "v_mfma_f32_16x16x32_bf16 v[64:67], v[200:203], v[104:107], v[64:67]\n\t"
    "ds_read_b128 v[96:99], %[ea] offset:24576\n\t"
    "s_add_u32 m0, %[l0], 4096\n\t"
    "v_mfma_f32_16x16x32_bf16 v[68:71], v[204:207], v[104:107], v[68:71]\n\t"
    "ds_read_b128 v[100:103], %[ea] offset:26624\n\t"
    "global_load_lds_dwordx4 %[av2], s[38:39] sc1\n\t"
    "v_mfma_f32_16x16x32_bf16 v[72:75], v[200:203], v[108:111], v[72:75]\n\t"
    "v_mfma_f32_16x16x32_bf16 v[76:79], v[204:207], v[108:111], v[76:79]\n\t"
    "s_add_u32 s36, s36, %[ab]\n\t"
    "s_addc_u32 s37, s37, 0\n\t"
    "s_waitcnt lgkmcnt(4)\n\t"
    "v_mfma_f32_16x16x32_bf16 v[64:67], v[208:211], v[112:115], v[64:67]\n\t"
    "ds_read_b128 v[104:107], %[da] offset:24576\n\t"
    "s_add_u32 m0, %[l0], 8192\n\t"
    "v_mfma_f32_16x16x32_bf16 v[68:71], v[212:215], v[112:115], v[68:71]\n\t"
    "ds_read_b128 v[108:111], %[da] offset:26624\n\t"
    "global_load_lds_dwordx4 %[av4], s[38:39] sc1\n\t"
    "v_mfma_f32_16x16x32_bf16 v[72:75], v[208:211], v[116:119], v[72:75]\n\t"
    "ds_read_b32 v39, %[sa]\n\t"
    "v_mfma_f32_16x16x32_bf16 v[76:79], v[212:215], v[116:119], v[76:79]\n\t"
    "v_xor_b32 %[sa], 0x100, %[sa]\n\t"
    "s_xor_b32 %[l1], %[l1], 0x10000\n\t"
    "s_waitcnt lgkmcnt(5)\n\t"
    "v_mfma_f32_16x16x32_bf16 v[64:67], v[216:219], v[120:123], v[64:67]\n\t"
    "ds_read_b128 v[112:115], %[ea] offset:28672\n\t"
    "s_add_u32 m0, %[l0], 12288\n\t"
    "v_mfma_f32_16x16x32_bf16 v[68:71], v[220:223], v[120:123], v[68:71]\n\t"
    "ds_read_b128 v[116:119], %[ea] offset:30720\n\t"
    "global_load_lds_dwordx4 %[av6], s[38:39] sc1\n\t"
    "v_mfma_f32_16x16x32_bf16 v[72:75], v[216:219], v[124:127], v[72:75]\n\t"
    "v_xor_b32 %[ea], 0x10000, %[ea]\n\t"
    "v_mfma_f32_16x16x32_bf16 v[76:79], v[220:223], v[124:127], v[76:79]\n\t"
    "s_waitcnt lgkmcnt(5)\n\t"
    "v_mfma_f32_16x16x32_bf16 v[64:67], v[224:227], v[96:99], v[64:67]\n\t"
    "ds_read_b128 v[120:123], %[da] offset:28672\n\t"
    "v_mfma_f32_16x16x32_bf16 v[68:71], v[228:231], v[96:99], v[68:71]\n\t"
    "ds_read_b128 v[124:127], %[da] offset:30720\n\t"
    "v_mfma_f32_16x16x32_bf16 v[72:75], v[224:227], v[100:103], v[72:75]\n\t"
    "v_xor_b32 %[da], 0x10000, %[da]\n\t"
    "v_mfma_f32_16x16x32_bf16 v[76:79], v[228:231], v[100:103], v[76:79]\n\t"
    "s_waitcnt lgkmcnt(5)\n\t"
    "v_mfma_f32_16x16x32_bf16 v[64:67], v[232:235], v[104:107], v[64:67]\n\t"
    "ds_read_b128 v[96:99], %[ea] offset:0\n\t"
    "v_mfma_f32_16x16x32_bf16 v[68:71], v[236:239], v[104:107], v[68:71]\n\t"
    "ds_read_b128 v[100:103], %[ea] offset:2048\n\t"
    "v_mfma_f32_16x16x32_bf16 v[72:75], v[232:235], v[108:111], v[72:75]\n\t"
    "v_mfma_f32_16x16x32_bf16 v[76:79], v[236:239], v[108:111], v[76:79]\n\t"
    "s_waitcnt lgkmcnt(4)\n\t"
    "v_mfma_f32_16x16x32_bf16 v[64:67], v[240:243], v[112:115], v[64:67]\n\t"
    "ds_read_b128 v[104:107], %[da] offset:0\n\t"
    "v_mad_u32_u24 v39, v39, %[mu], %[ad]\n\t"
    "v_mfma_f32_16x16x32_bf16 v[68:71], v[244:247], v[112:115], v[68:71]\n\t"
    "ds_read_b128 v[108:111], %[da] offset:2048\n\t"
    "v_cmp_ge_i32 vcc, %[lf], v39\n\t"
    "s_cmp_eq_u64 vcc, 0\n\t"
    "s_addc_u32 %[lf], %[lf], 0\n\t"
    "v_mfma_f32_16x16x32_bf16 v[72:75], v[240:243], v[116:119], v[72:75]\n\t"
    "v_mfma_f32_16x16x32_bf16 v[76:79], v[244:247], v[116:119], v[76:79]\n\t"
    "v_cmp_ge_i32 vcc, %[lf], v39\n\t"
    "s_cmp_eq_u64 vcc, 0\n\t"
    "s_addc_u32 %[lf], %[lf], 0\n\t"
    "s_waitcnt lgkmcnt(4)\n\t"
    "v_mfma_f32_16x16x32_bf16 v[80:83], v[248:251], v[120:123], v[64:67]\n\t"
    "ds_read_b128 v[112:115], %[ea] offset:4096\n\t"
    "s_min_i32 %[lf], %[lf], %[cp]\n\t"
    "s_add_u32 %[b], %[b], 1\n\t"
    "v_mfma_f32_16x16x32_bf16 v[84:87], v[252:255], v[120:123], v[68:71]\n\t"
    "ds_read_b128 v[116:119], %[ea] offset:6144\n\t"
    "v_mfma_f32_16x16x32_bf16 v[88:91], v[248:251], v[124:127], v[72:75]\n\t"
    "v_mfma_f32_16x16x32_bf16 v[92:95], v[252:255], v[124:127], v[76:79]\n\t"
    "s_add_u32 s38, s38, %[ab]\n\t"
    "s_addc_u32 s39, s39, 0\n\t"
    "s_xor_b32 %[l0], %[l0], 0x10000\n\t"
    "s_cmp_lt_i32 %[b], %[lf]\n\t"
    "s_cbranch_scc1 1b\n\t"
    "s_waitcnt lgkmcnt(0)\n\t"
    "s_nop 4"
    : "=&{v[80:95]}"(pd), [ea] "+&v"(ea), [da] "+&v"(da), [b] "+&s"(b), [lf] "+&s"(lf), "+{s[36:37]}"(p1), "+{s[38:39]}"(p0), [sa] "+&v"(sa), [l1] "+&s"(l1), [l0] "+&s"(l0)
    : [rw] "v"(rw), [ab] "s"(ab), [cp] "s"(cp), [mu] "s"(mu), [ad] "v"(ad), "{v[128:159]}"(W[0]), "{v[160:191]}"(W[1]), "{v[192:223]}"(W[2]), "{v[224:255]}"(W[3]), [av0] "v"(av[0]), [av1] "v"(av[1]), [av2] "v"(av[2]), [av3] "v"(av[3]), [av4] "v"(av[4]), [av5] "v"(av[5]), [av6] "v"(av[6]), [av7] "v"(av[7])
    : "v39", "v64", "v65", "v66", "v67", "v68", "v69", "v70", "v71", "v72", "v73", "v74", "v75", "v76", "v77", "v78", "v79", "v96", "v97", "v98", "v99", "v100", "v101", "v102", "v103", "v104", "v105", "v106", "v107", "v108", "v109", "v110", "v111", "v112", "v113", "v114", "v115", "v116", "v117", "v118", "v119", "v120", "v121", "v122", "v123", "v124", "v125", "v126", "v127", "vcc", "scc", "m0", "memory");
}
__device__ __forceinline__ void fast_st0(const u32x32 (&W)[4], u32x16& pd, unsigned ea, unsigned da, int& b, int& lf, int cp, unsigned sa, unsigned mu, unsigned ad,
    unsigned rw, unsigned ab, const char* p1, const char* p0, unsigned co0, unsigned co1, int po, int pf, unsigned w0, unsigned sl, unsigned dv, const char* db, const char* pg) {
  unsigned st;
  asm volatile("s_barrier\n\t"
    "ds_read_b128 v[96:99], %[ea] offset:0\n\t"
    "ds_read_b128 v[100:103], %[ea] offset:2048\n\t"
    "ds_read_b128 v[104:107], %[da] offset:0\n\t"
    "ds_read_b128 v[108:111], %[da] offset:2048\n\t"
    "ds_read_b128 v[112:115], %[ea] offset:4096\n\t"
    "ds_read_b128 v[116:119], %[ea] offset:6144\n\t"
    "s_branch 2f\n\t"
    "1:\n\t"
    "s_barrier\n\t"
    "2:\n\t"
    "s_waitcnt lgkmcnt(4)\n\t"
    "v_mfma_f32_16x16x32_bf16 v[64:67], v[128:131], v[96:99], 0\n\t"
    "ds_read_b128 v[120:123], %[da] offset:4096\n\t"
    "v_mfma_f32_16x16x32_bf16 v[68:71], v[132:135], v[96:99], 0\n\t"
    "ds_read_b128 v[124:127], %[da] offset:6144\n\t"
    "v_mfma_f32_16x16x32_bf16 v[72:75], v[128:131], v[100:103], 0\n\t"
    "v_mfma_f32_16x16x32_bf16 v[76:79], v[132:135], v[100:103], 0\n\t"
    "s_waitcnt lgkmcnt(4)\n\t"
    "v_mfma_f32_16x16x32_bf16 v[64:67], v[136:139], v[104:107], v[64:67]\n\t"
    "ds_read_b128 v[96:99], %[ea] offset:8192\n\t"
    "v_mfma_f32_16x16x32_bf16 v[68:71], v[140:143], v[104:107], v[68:71]\n\t"
    "ds_read_b128 v[100:103], %[ea] offset:10240\n\t"
    "v_mfma_f32_16x16x32_bf16 v[72:75], v[136:139], v[108:111], v[72:75]\n\t"
    "v_mfma_f32_16x16x32_bf16 v[76:79], v[140:143], v[108:111], v[76:79]\n\t"
    "s_waitcnt lgkmcnt(4)\n\t"
    "v_mfma_f32_16x16x32_bf16 v[64:67], v[144:147], v[112:115], v[64:67]\n\t"
    "ds_read_b128 v[104:107], %[da] offset:8192\n\t"
    "v_mfma_f32_16x16x32_bf16 v[68:71], v[148:151], v[112:115], v[68:71]\n\t"
    "ds_read_b128 v[108:111], %[da] offset:10240\n\t"
    "v_mfma_f32_16x16x32_bf16 v[72:75], v[144:147], v[116:119], v[72:75]\n\t"
    "v_mfma_f32_16x16x32_bf16 v[76:79], v[148:151], v[116:119], v[76:79]\n\t"
    "s_waitcnt lgkmcnt(4)\n\t"
    "v_mfma_f32_16x16x32_bf16 v[64:67], v[152:155], v[120:123], v[64:67]\n\t"
    "ds_read_b128 v[112:115], %[ea] offset:12288\n\t"
    "v_mfma_f32_16x16x32_bf16 v[68:71], v[156:159], v[120:123], v[68:71]\n\t"
    "ds_read_b128 v[116:119], %[ea] offset:14336\n\t"
    "v_mfma_f32_16x16x32_bf16 v[72:75], v[152:155], v[124:127], v[72:75]\n\t"
    "v_mfma_f32_16x16x32_bf16 v[76:79], v[156:159], v[124:127], v[76:79]\n\t"
    "s_waitcnt lgkmcnt(4)\n\t"
    "v_mfma_f32_16x16x32_bf16 v[64:67], v[160:163], v[96:99], v[64:67]\n\t"
    "ds_read_b128 v[120:123], %[da] offset:12288\n\t"
    "v_mfma_f32_16x16x32_bf16 v[68:71], v[164:167], v[96:99], v[68:71]\n\t"
    "ds_read_b128 v[124:127], %[da] offset:14336\n\t"
    "v_mfma_f32_16x16x32_bf16 v[72:75], v[160:163], v[100:103], v[72:75]\n\t"
    "v_mfma_f32_16x16x32_bf16 v[76:79], v[164:167], v[100:103], v[76:79]\n\t"
    "s_waitcnt lgkmcnt(4)\n\t"
    "v_mfma_f32_16x16x32_bf16 v[64:67], v[168:171], v[104:107], v[64:67]\n\t"
    "ds_read_b128 v[96:99], %[ea] offset:16384\n\t"
    "v_mfma_f32_16x16x32_bf16 v[68:71], v[172:175], v[104:107], v[68:71]\n\t"
    "ds_read_b128 v[100:103], %[ea] offset:18432\n\t"
    "v_mfma_f32_16x16x32_bf16 v[72:75], v[168:171], v[108:111], v[72:75]\n\t"
    "v_mfma_f32_16x16x32_bf16 v[76:79], v[172:175], v[108:111], v[76:79]\n\t"
    "s_waitcnt lgkmcnt(4)\n\t"
    "v_mfma_f32_16x16x32_bf16 v[64:67], v[176:179], v[112:115], v[64:67]\n\t"
    "ds_read_b128 v[104:107], %[da] offset:16384\n\t"
    "v_mfma_f32_16x16x32_bf16 v[68:71], v[180:183], v[112:115], v[68:71]\n\t"
    "ds_read_b128 v[108:111], %[da] offset:18432\n\t"
    "v_mfma_f32_16x16x32_bf16 v[72:75], v[176:179], v[116:119], v[72:75]\n\t"
    "v_mfma_f32_16x16x32_bf16 v[76:79], v[180:183], v[116:119], v[76:79]\n\t"
    "s_waitcnt lgkmcnt(4)\n\t"
    "v_mfma_f32_16x16x32_bf16 v[64:67], v[184:187], v[120:123], v[64:67]\n\t"
    "ds_read_b128 v[112:115], %[ea] offset:20480\n\t"
    "v_mfma_f32_16x16x32_bf16 v[68:71], v[188:191], v[120:123], v[68:71]\n\t"
    "ds_read_b128 v[116:119], %[ea] offset:22528\n\t"
    "v_mfma_f32_16x16x32_bf16 v[72:75], v[184:187], v[124:127], v[72:75]\n\t"
    "v_mfma_f32_16x16x32_bf16 v[76:79], v[188:191], v[124:127], v[76:79]\n\t"
    "s_waitcnt vmcnt(0)\n\t"
    "s_barrier\n\t"
    "s_waitcnt lgkmcnt(4)\n\t"
    "v_mfma_f32_16x16x32_bf16 v[64:67], v[192:195], v[96:99], v[64:67]\n\t"
    "ds_read_b128 v[120:123], %[da] offset:20480\n\t"
    "v_mfma_f32_16x16x32_bf16 v[68:71], v[196:199], v[96:99], v[68:71]\n\t"
    "ds_read_b128 v[124:127], %[da] offset:22528\n\t"
    "v_mfma_f32_16x16x32_bf16 v[72:75], v[192:195], v[100:103], v[72:75]\n\t"
    "ds_read_b128 v[48:51], %[rw] offset:0\n\t"
    "v_mfma_f32_16x16x32_bf16 v[76:79], v[196:199], v[100:103], v[76:79]\n\t"
    "ds_read_b128 v[52:55], %[rw] offset:1024\n\t"
    "s_cmp_eq_u32 %[w0], 0\n\t"
    "s_cbranch_scc1 5f\n\t"
    "6:\n\t"
    "s_waitcnt lgkmcnt(6)\n\t"
    "v_mfma_f32_16x16x32_bf16 v[64:67], v[200:203], v[104:107], v[64:67]\n\t"
    "ds_read_b128 v[96:99], %[ea] offset:24576\n\t"
    "v_mfma_f32_16x16x32_bf16 v[68:71], v[204:207], v[104:107], v[68:71]\n\t"
    "ds_read_b128 v[100:103], %[ea] offset:26624\n\t"
    "v_mfma_f32_16x16x32_bf16 v[72:75], v[200:203], v[108:111], v[72:75]\n\t"
    "ds_read_b128 v[56:59], %[rw] offset:2048\n\t"
    "v_mfma_f32_16x16x32_bf16 v[76:79], v[204:207], v[108:111], v[76:79]\n\t"
    "ds_read_b128 v[60:63], %[rw] offset:3072\n\t"
    "s_waitcnt lgkmcnt(0)\n\t"
    "v_mfma_f32_16x16x32_bf16 v[64:67], v[208:211], v[112:115], v[64:67]\n\t"
    "ds_read_b128 v[104:107], %[da] offset:24576\n\t"
    "v_add_f32 v48, v48, v80\n\t"
    "v_add_f32 v49, v49, v81\n\t"
    "v_mfma_f32_16x16x32_bf16 v[68:71], v[212:215], v[112:115], v[68:71]\n\t"
    "ds_read_b128 v[108:111], %[da] offset:26624\n\t"
    "v_add_f32 v50, v50, v82\n\t"
    "v_mfma_f32_16x16x32_bf16 v[72:75], v[208:211], v[116:119], v[72:75]\n\t"
    "ds_read_b32 v39, %[sa]\n\t"
    "v_add_f32 v51, v51, v83\n\t"
    "v_mfma_f32_16x16x32_bf16 v[76:79], v[212:215], v[116:119], v[76:79]\n\t"
    "v_xor_b32 %[sa], 0x100, %[sa]\n\t"
    "v_add_f32 v52, v52, v84\n\t"
    "v_add_f32 v53, v53, v85\n\t"
    "v_mfma_f32_16x16x32_bf16 v[64:67], v[216:219], v[120:123], v[64:67]\n\t"
    "ds_read_b128 v[112:115], %[ea] offset:28672\n\t"
    "v_add_f32 v54, v54, v86\n\t"
    "v_mfma_f32_16x16x32_bf16 v[68:71], v[220:223], v[120:123], v[68:71]\n\t"
    "ds_read_b128 v[116:119], %[ea] offset:30720\n\t"
    "v_add_f32 v55, v55, v87\n\t"
    "v_mfma_f32_16x16x32_bf16 v[72:75], v[216:219], v[124:127], v[72:75]\n\t"
    "v_xor_b32 %[ea], 0x10000, %[ea]\n\t"
    "v_add_f32 v56, v56, v88\n\t"
    "v_add_f32 v57, v57, v89\n\t"
    "v_mfma_f32_16x16x32_bf16 v[76:79], v[220:223], v[124:127], v[76:79]\n\t"
    "v_add_f32 v58, v58, v90\n\t"
    "v_mfma_f32_16x16x32_bf16 v[64:67], v[224:227], v[96:99], v[64:67]\n\t"
    "ds_read_b128 v[120:123], %[da] offset:28672\n\t"
    "v_add_f32 v59, v59, v91\n\t"
    "v_mfma_f32_16x16x32_bf16 v[68:71], v[228:231], v[96:99], v[68:71]\n\t"
    "ds_read_b128 v[124:127], %[da] offset:30720\n\t"
    "v_add_f32 v60, v60, v92\n\t"
    "v_mfma_f32_16x16x32_bf16 v[72:75], v[224:227], v[100:103], v[72:75]\n\t"
    "v_xor_b32 %[da], 0x10000, %[da]\n\t"
    "v_add_f32 v61, v61, v93\n\t"
    "v_add_f32 v62, v62, v94\n\t"
    "v_mfma_f32_16x16x32_bf16 v[76:79], v[228:231], v[100:103], v[76:79]\n\t"
    "v_add_f32 v63, v63, v95\n\t"
    "s_waitcnt lgkmcnt(5)\n\t"
    "v_mfma_f32_16x16x32_bf16 v[64:67], v[232:235], v[104:107], v[64:67]\n\t"
    "ds_read_b128 v[96:99], %[ea] offset:0\n\t"
    "v_cvt_pk_bf16_f32 v40, v48, v49\n\t"
    "v_mfma_f32_16x16x32_bf16 v[68:71], v[236:239], v[104:107], v[68:71]\n\t"
    "ds_read_b128 v[100:103], %[ea] offset:2048\n\t"
    "v_cvt_pk_bf16_f32 v41, v50, v51\n\t"
    "v_cvt_pk_bf16_f32 v42, v52, v53\n\t"
    "v_mfma_f32_16x16x32_bf16 v[72:75], v[232:235], v[108:111], v[72:75]\n\t"
    "v_cvt_pk_bf16_f32 v43, v54, v55\n\t"
    "v_mfma_f32_16x16x32_bf16 v[76:79], v[236:239], v[108:111], v[76:79]\n\t"
    "global_store_dwordx4 %[co0], v[40:43], s[36:37] sc1\n\t"
    "s_waitcnt lgkmcnt(4)\n\t"
    "v_mfma_f32_16x16x32_bf16 v[64:67], v[240:243], v[112:115], v[64:67]\n\t"
    "ds_read_b128 v[104:107], %[da] offset:0\n\t"
    "v_mad_u32_u24 v39, v39, %[mu], %[ad]\n\t"
    "v_cvt_pk_bf16_f32 v44, v56, v57\n\t"
    "v_cvt_pk_bf16_f32 v45, v58, v59\n\t"
    "v_mfma_f32_16x16x32_bf16 v[68:71], v[244:247], v[112:115], v[68:71]\n\t"
    "ds_read_b128 v[108:111], %[da] offset:2048\n\t"
    "v_cmp_ge_i32 vcc, %[lf], v39\n\t"
    "s_cmp_eq_u64 vcc, 0\n\t"
    "s_addc_u32 %[lf], %[lf], 0\n\t"
    "v_cvt_pk_bf16_f32 v46, v60, v61\n\t"
    "v_mfma_f32_16x16x32_bf16 v[72:75], v[240:243], v[116:119], v[72:75]\n\t"
    "v_cvt_pk_bf16_f32 v47, v62, v63\n\t"
    "v_mfma_f32_16x16x32_bf16 v[76:79], v[244:247], v[116:119], v[76:79]\n\t"
    "v_cmp_ge_i32 vcc, %[lf], v39\n\t"
    "s_cmp_eq_u64 vcc, 0\n\t"
    "s_addc_u32 %[lf], %[lf], 0\n\t"
    "global_store_dwordx4 %[co1], v[44:47], s[36:37] sc1\n\t"
    "s_waitcnt lgkmcnt(4)\n\t"
    "v_mfma_f32_16x16x32_bf16 v[80:83], v[248:251], v[120:123], v[64:67]\n\t"
    "ds_read_b128 v[112:115], %[ea] offset:4096\n\t"
    "s_min_i32 %[lf], %[lf], %[cp]\n\t"
    "s_add_u32 %[b], %[b], 1\n\t"
    "v_mfma_f32_16x16x32_bf16 v[84:87], v[252:255], v[120:123], v[68:71]\n\t"
    "ds_read_b128 v[116:119], %[ea] offset:6144\n\t"
    "v_mfma_f32_16x16x32_bf16 v[88:91], v[248:251], v[124:127], v[72:75]\n\t"
    "v_mfma_f32_16x16x32_bf16 v[92:95], v[252:255], v[124:127], v[76:79]\n\t"
    "s_mov_b64 s[36:37], s[38:39]\n\t"
    "s_add_u32 s38, s38, %[ab]\n\t"
    "s_addc_u32 s39, s39, 0\n\t"
    "s_cmp_lt_i32 %[b], %[lf]\n\t"
    "s_cbranch_scc1 1b\n\t"
    "s_waitcnt lgkmcnt(0)\n\t"
    "s_nop 4\n\t"
    "s_branch 9f\n\t"
    "5:\n\t"
    "s_add_u32 %[st], %[b], %[po]\n\t"
    "s_max_i32 %[st], %[st], %[p0]\n\t"
    "v_mov_b32 v39, %[st]\n\t"
    "v_mov_b32 v38, 0\n\t"
    "s_mov_b64 exec, 1\n\t"
    "global_store_dword v38, v39, %[pg] sc1\n\t"
    "s_mov_b64 exec, -1\n\t"
    "s_mov_b32 %[p0], %[p1]\n\t"
    "s_xor_b32 %[sl], %[sl], 0x100\n\t"
    "s_cmp_ge_i32 %[lf], %[cp]\n\t"
    "s_cbranch_scc1 6b\n\t"
    "s_mov_b32 m0, %[sl]\n\t"
    "s_nop 0\n\t"
    "global_load_lds_dword %[dv], %[db] sc1\n\t"
    "s_branch 6b\n\t"
    "9:"
    : "=&{v[80:95]}"(pd), [ea] "+&v"(ea), [da] "+&v"(da), [b] "+&s"(b), [lf] "+&s"(lf), "+{s[36:37]}"(p1), "+{s[38:39]}"(p0), [sa] "+&v"(sa), [st] "=&s"(st), [sl] "+&s"(sl), [p0] "+&s"(pf)
    : [rw] "v"(rw), [ab] "s"(ab), [cp] "s"(cp), [mu] "s"(mu), [ad] "v"(ad), "{v[128:159]}"(W[0]), "{v[160:191]}"(W[1]), "{v[192:223]}"(W[2]), "{v[224:255]}"(W[3]), [co0] "v"(co0), [w0] "s"(w0), [dv] "v"(dv), [db] "s"(db), [pg] "s"(pg), [po] "s"(po - 1), [p1] "s"(b + po), [co1] "v"(co1)
    : "v38", "v39", "v40", "v41", "v42", "v43", "v44", "v45", "v46", "v47", "v48", "v49", "v50", "v51", "v52", "v53", "v54", "v55", "v56", "v57", "v58", "v59", "v60", "v61", "v62", "v63", "v64", "v65", "v66", "v67", "v68", "v69", "v70", "v71", "v72", "v73", "v74", "v75", "v76", "v77", "v78", "v79", "v96", "v97", "v98", "v99", "v100", "v101", "v102", "v103", "v104", "v105", "v106", "v107", "v108", "v109", "v110", "v111", "v112", "v113", "v114", "v115", "v116", "v117", "v118", "v119", "v120", "v121", "v122", "v123", "v124", "v125", "v126", "v127", "vcc", "scc", "m0", "memory");
}
__device__ __forceinline__ void fast_st1(const u32x32 (&W)[4], u32x16& pd, unsigned ea, unsigned da, int& b, int& lf, int cp, unsigned sa, unsigned mu, unsigned ad,
    unsigned rw, unsigned ab, const char* p1, const char* p0, unsigned co0, unsigned co1, int po, int pf, unsigned w0, unsigned sl, unsigned dv, const char* db, const char* pg) {
  unsigned st;
  asm volatile("s_barrier\n\t"
    "ds_read_b128 v[96:99], %[ea] offset:0\n\t"
    "ds_read_b128 v[100:103], %[ea] offset:2048\n\t"
    "ds_read_b128 v[104:107], %[da] offset:0\n\t"
    "ds_read_b128 v[108:111], %[da] offset:2048\n\t"
    "ds_read_b128 v[112:115], %[ea] offset:4096\n\t"
    "ds_read_b128 v[116:119], %[ea] offset:6144\n\t"
    "s_branch 2f\n\t"
    "1:\n\t"
    "s_barrier\n\t"
    "2:\n\t"
    "s_waitcnt lgkmcnt(4)\n\t"
    "v_mfma_f32_16x16x32_bf16 v[64:67], v[128:131], v[96:99], 0\n\t"
    "ds_read_b128 v[120:123], %[da] offset:4096\n\t"
    "v_mfma_f32_16x16x32_bf16 v[68:71], v[132:135], v[96:99], 0\n\t"
    "ds_read_b128 v[124:127], %[da] offset:6144\n\t"
    "v_mfma_f32_16x16x32_bf16 v[72:75], v[128:131], v[100:103], 0\n\t"
    "v_mfma_f32_16x16x32_bf16 v[76:79], v[132:135], v[100:103], 0\n\t"
    "s_waitcnt lgkmcnt(4)\n\t"
    "v_mfma_f32_16x16x32_bf16 v[64:67], v[136:139], v[104:107], v[64:67]\n\t"
    "ds_read_b128 v[96:99], %[ea] offset:8192\n\t"
    "v_mfma_f32_16x16x32_bf16 v[68:71], v[140:143], v[104:107], v[68:71]\n\t"
    "ds_read_b128 v[100:103], %[ea] offset:10240\n\t"
    "v_mfma_f32_16x16x32_bf16 v[72:75], v[136:139], v[108:111], v[72:75]\n\t"
    "v_mfma_f32_16x16x32_bf16 v[76:79], v[140:143], v[108:111], v[76:79]\n\t"
    "s_waitcnt lgkmcnt(4)\n\t"
    "v_mfma_f32_16x16x32_bf16 v[64:67], v[144:147], v[112:115], v[64:67]\n\t"
    "ds_read_b128 v[104:107], %[da] offset:8192\n\t"
    "v_mfma_f32_16x16x32_bf16 v[68:71], v[148:151], v[112:115], v[68:71]\n\t"
    "ds_read_b128 v[108:111], %[da] offset:10240\n\t"
    "v_mfma_f32_16x16x32_bf16 v[72:75], v[144:147], v[116:119], v[72:75]\n\t"
    "v_mfma_f32_16x16x32_bf16 v[76:79], v[148:151], v[116:119], v[76:79]\n\t"
    "s_waitcnt lgkmcnt(4)\n\t"
    "v_mfma_f32_16x16x32_bf16 v[64:67], v[152:155], v[120:123], v[64:67]\n\t"
    "ds_read_b128 v[112:115], %[ea] offset:12288\n\t"
    "v_mfma_f32_16x16x32_bf16 v[68:71], v[156:159], v[120:123], v[68:71]\n\t"
    "ds_read_b128 v[116:119], %[ea] offset:14336\n\t"
    "v_mfma_f32_16x16x32_bf16 v[72:75], v[152:155], v[124:127], v[72:75]\n\t"
    "v_mfma_f32_16x16x32_bf16 v[76:79], v[156:159], v[124:127], v[76:79]\n\t"
    "s_waitcnt lgkmcnt(4)\n\t"
    "v_mfma_f32_16x16x32_bf16 v[64:67], v[160:163], v[96:99], v[64:67]\n\t"
    "ds_read_b128 v[120:123], %[da] offset:12288\n\t"
    "v_mfma_f32_16x16x32_bf16 v[68:71], v[164:167], v[96:99], v[68:71]\n\t"
    "ds_read_b128 v[124:127], %[da] offset:14336\n\t"
    "v_mfma_f32_16x16x32_bf16 v[72:75], v[160:163], v[100:103], v[72:75]\n\t"
    "v_mfma_f32_16x16x32_bf16 v[76:79], v[164:167], v[100:103], v[76:79]\n\t"
    "s_waitcnt lgkmcnt(4)\n\t"
    "v_mfma_f32_16x16x32_bf16 v[64:67], v[168:171], v[104:107], v[64:67]\n\t"
    "ds_read_b128 v[96:99], %[ea] offset:16384\n\t"
    "v_mfma_f32_16x16x32_bf16 v[68:71], v[172:175], v[104:107], v[68:71]\n\t"
    "ds_read_b128 v[100:103], %[ea] offset:18432\n\t"
    "v_mfma_f32_16x16x32_bf16 v[72:75], v[168:171], v[108:111], v[72:75]\n\t"
    "v_mfma_f32_16x16x32_bf16 v[76:79], v[172:175], v[108:111], v[76:79]\n\t"
    "s_waitcnt lgkmcnt(4)\n\t"
    "v_mfma_f32_16x16x32_bf16 v[64:67], v[176:179], v[112:115], v[64:67]\n\t"
    "ds_read_b128 v[104:107], %[da] offset:16384\n\t"
    "v_mfma_f32_16x16x32_bf16 v[68:71], v[180:183], v[112:115], v[68:71]\n\t"
    "ds_read_b128 v[108:111], %[da] offset:18432\n\t"
    "v_mfma_f32_16x16x32_bf16 v[72:75], v[176:179], v[116:119], v[72:75]\n\t"
    "v_mfma_f32_16x16x32_bf16 v[76:79], v[180:183], v[116:119], v[76:79]\n\t"
    "s_waitcnt lgkmcnt(4)\n\t"
    "v_mfma_f32_16x16x32_bf16 v[64:67], v[184:187], v[120:123], v[64:67]\n\t"
    "ds_read_b128 v[112:115], %[ea] offset:20480\n\t"
    "v_mfma_f32_16x16x32_bf16 v[68:71], v[188:191], v[120:123], v[68:71]\n\t"
    "ds_read_b128 v[116:119], %[ea] offset:22528\n\t"
    "v_mfma_f32_16x16x32_bf16 v[72:75], v[184:187], v[124:127], v[72:75]\n\t"
    "v_mfma_f32_16x16x32_bf16 v[76:79], v[188:191], v[124:127], v[76:79]\n\t"
    "s_waitcnt vmcnt(0)\n\t"
    "s_barrier\n\t"
    "s_waitcnt lgkmcnt(4)\n\t"
    "v_mfma_f32_16x16x32_bf16 v[64:67], v[192:195], v[96:99], v[64:67]\n\t"
    "ds_read_b128 v[120:123], %[da] offset:20480\n\t"
    "v_mfma_f32_16x16x32_bf16 v[68:71], v[196:199], v[96:99], v[68:71]\n\t"
    "ds_read_b128 v[124:127], %[da] offset:22528\n\t"
    "v_mfma_f32_16x16x32_bf16 v[72:75], v[192:195], v[100:103], v[72:75]\n\t"
    "ds_read_b128 v[48:51], %[rw] offset:0\n\t"
    "v_mfma_f32_16x16x32_bf16 v[76:79], v[196:199], v[100:103], v[76:79]\n\t"
    "ds_read_b128 v[52:55], %[rw] offset:1024\n\t"
    "s_cmp_eq_u32 %[w0], 0\n\t"
    "s_cbranch_scc1 5f\n\t"
    "6:\n\t"
    "s_waitcnt lgkmcnt(6)\n\t"
    "v_mfma_f32_16x16x32_bf16 v[64:67], v[200:203], v[104:107], v[64:67]\n\t"
    "ds_read_b128 v[96:99], %[ea] offset:24576\n\t"
    "v_mfma_f32_16x16x32_bf16 v[68:71], v[204:207], v[104:107], v[68:71]\n\t"
    "ds_read_b128 v[100:103], %[ea] offset:26624\n\t"
    "v_mfma_f32_16x16x32_bf16 v[72:75], v[200:203], v[108:111], v[72:75]\n\t"
    "ds_read_b128 v[56:59], %[rw] offset:2048\n\t"
    "v_mfma_f32_16x16x32_bf16 v[76:79], v[204:207], v[108:111], v[76:79]\n\t"
    "ds_read_b128 v[60:63], %[rw] offset:3072\n\t"
    "s_waitcnt lgkmcnt(0)\n\t"
    "v_mfma_f32_16x16x32_bf16 v[64:67], v[208:211], v[112:115], v[64:67]\n\t"
    "ds_read_b128 v[104:107], %[da] offset:24576\n\t"
    "v_add_f32 v48, v48, v80\n\t"
    "v_add_f32 v49, v49, v81\n\t"
    "v_add_f32 v50, v50, v82\n\t"
    "v_mfma_f32_16x16x32_bf16 v[68:71], v[212:215], v[112:115], v[68:71]\n\t"
    "ds_read_b128 v[108:111], %[da] offset:26624\n\t"
    "v_add_f32 v51, v51, v83\n\t"
    "v_add_f32 v52, v52, v84\n\t"
    "v_add_f32 v53, v53, v85\n\t"
    "v_mfma_f32_16x16x32_bf16 v[72:75], v[208:211], v[116:119], v[72:75]\n\t"
    "ds_read_b32 v39, %[sa]\n\t"
    "v_add_f32 v54, v54, v86\n\t"
    "v_add_f32 v55, v55, v87\n\t"
    "v_add_f32 v56, v56, v88\n\t"
    "v_mfma_f32_16x16x32_bf16 v[76:79], v[212:215], v[116:119], v[76:79]\n\t"
    "v_xor_b32 %[sa], 0x100, %[sa]\n\t"
    "v_add_f32 v57, v57, v89\n\t"
    "v_add_f32 v58, v58, v90\n\t"
    "v_add_f32 v59, v59, v91\n\t"
    "v_mfma_f32_16x16x32_bf16 v[64:67], v[216:219], v[120:123], v[64:67]\n\t"
    "ds_read_b128 v[112:115], %[ea] offset:28672\n\t"
    "v_add_f32 v60, v60, v92\n\t"
    "v_add_f32 v61, v61, v93\n\t"
    "v_add_f32 v62, v62, v94\n\t"
    "v_mfma_f32_16x16x32_bf16 v[68:71], v[220:223], v[120:123], v[68:71]\n\t"
    "ds_read_b128 v[116:119], %[ea] offset:30720\n\t"
    "v_add_f32 v63, v63, v95\n\t"
    "v_mul_f32 v40, %[nl], v48\n\t"
    "v_mul_f32 v41, %[nl], v49\n\t"
    "v_mfma_f32_16x16x32_bf16 v[72:75], v[216:219], v[124:127], v[72:75]\n\t"
    "v_xor_b32 %[ea], 0x10000, %[ea]\n\t"
    "v_mul_f32 v42, %[nl], v50\n\t"
    "v_mul_f32 v43, %[nl], v51\n\t"
    "v_mul_f32 v44, %[nl], v56\n\t"
    "v_mfma_f32_16x16x32_bf16 v[76:79], v[220:223], v[124:127], v[76:79]\n\t"
    "v_mul_f32 v45, %[nl], v57\n\t"
    "v_mul_f32 v46, %[nl], v58\n\t"
    "v_mul_f32 v47, %[nl], v59\n\t"
    "v_mfma_f32_16x16x32_bf16 v[64:67], v[224:227], v[96:99], v[64:67]\n\t"
    "ds_read_b128 v[120:123], %[da] offset:28672\n\t"
    "v_exp_f32 v40, v40\n\t"
    "v_exp_f32 v41, v41\n\t"
    "v_exp_f32 v42, v42\n\t"
    "v_mfma_f32_16x16x32_bf16 v[68:71], v[228:231], v[96:99], v[68:71]\n\t"
    "ds_read_b128 v[124:127], %[da] offset:30720\n\t"
    "v_exp_f32 v43, v43\n\t"
    "v_exp_f32 v44, v44\n\t"
    "v_exp_f32 v45, v45\n\t"
    "v_mfma_f32_16x16x32_bf16 v[72:75], v[224:227], v[100:103], v[72:75]\n\t"
    "v_xor_b32 %[da], 0x10000, %[da]\n\t"
    "v_exp_f32 v46, v46\n\t"
    "v_exp_f32 v47, v47\n\t"
    "v_add_f32 v40, 1.0, v40\n\t"
    "v_mfma_f32_16x16x32_bf16 v[76:79], v[228:231], v[100:103], v[76:79]\n\t"
    "v_add_f32 v41, 1.0, v41\n\t"
    "v_add_f32 v42, 1.0, v42\n\t"
    "v_add_f32 v43, 1.0, v43\n\t"
    "s_waitcnt lgkmcnt(5)\n\t"
    "v_mfma_f32_16x16x32_bf16 v[64:67], v[232:235], v[104:107], v[64:67]\n\t"
    "ds_read_b128 v[96:99], %[ea] offset:0\n\t"
    "v_add_f32 v44, 1.0, v44\n\t"
    "v_add_f32 v45, 1.0, v45\n\t"
    "v_add_f32 v46, 1.0, v46\n\t"
    "v_mfma_f32_16x16x32_bf16 v[68:71], v[236:239], v[104:107], v[68:71]\n\t"
    "ds_read_b128 v[100:103], %[ea] offset:2048\n\t"
    "v_add_f32 v47, 1.0, v47\n\t"
    "v_rcp_f32 v40, v40\n\t"
    "v_rcp_f32 v41, v41\n\t"
    "v_mfma_f32_16x16x32_bf16 v[72:75], v[232:235], v[108:111], v[72:75]\n\t"
    "v_rcp_f32 v42, v42\n\t"
    "v_rcp_f32 v43, v43\n\t"
    "v_rcp_f32 v44, v44\n\t"
    "v_mfma_f32_16x16x32_bf16 v[76:79], v[236:239], v[108:111], v[76:79]\n\t"
    "v_rcp_f32 v45, v45\n\t"
    "v_rcp_f32 v46, v46\n\t"
    "v_rcp_f32 v47, v47\n\t"
    "s_waitcnt lgkmcnt(4)\n\t"
    "v_mfma_f32_16x16x32_bf16 v[64:67], v[240:243], v[112:115], v[64:67]\n\t"
    "ds_read_b128 v[104:107], %[da] offset:0\n\t"
    "v_mad_u32_u24 v39, v39, %[mu], %[ad]\n\t"
    "v_mul_f32 v40, v48, v40\n\t"
    "v_mul_f32 v41, v49, v41\n\t"
    "v_mul_f32 v42, v50, v42\n\t"
    "v_mfma_f32_16x16x32_bf16 v[68:71], v[244:247], v[112:115], v[68:71]\n\t"
    "ds_read_b128 v[108:111], %[da] offset:2048\n\t"
    "v_cmp_ge_i32 vcc, %[lf], v39\n\t"
    "s_cmp_eq_u64 vcc, 0\n\t"
    "s_addc_u32 %[lf], %[lf], 0\n\t"
    "v_mul_f32 v43, v51, v43\n\t"
    "v_mul_f32 v44, v56, v44\n\t"
    "v_mul_f32 v45, v57, v45\n\t"
    "v_mfma_f32_16x16x32_bf16 v[72:75], v[240:243], v[116:119], v[72:75]\n\t"
    "v_mul_f32 v46, v58, v46\n\t"
    "v_mul_f32 v47, v59, v47\n\t"
    "v_mul_f32 v40, v40, v52\n\t"
    "v_mfma_f32_16x16x32_bf16 v[76:79], v[244:247], v[116:119], v[76:79]\n\t"
    "v_cmp_ge_i32 vcc, %[lf], v39\n\t"
    "s_cmp_eq_u64 vcc, 0\n\t"
    "s_addc_u32 %[lf], %[lf], 0\n\t"
    "v_mul_f32 v41, v41, v53\n\t"
    "v_mul_f32 v42, v42, v54\n\t"
    "v_mul_f32 v43, v43, v55\n\t"
    "s_waitcnt lgkmcnt(4)\n\t"
    "v_mfma_f32_16x16x32_bf16 v[80:83], v[248:251], v[120:123], v[64:67]\n\t"
    "ds_read_b128 v[112:115], %[ea] offset:4096\n\t"
    "s_min_i32 %[lf], %[lf], %[cp]\n\t"
    "s_add_u32 %[b], %[b], 1\n\t"
    "v_mul_f32 v44, v44, v60\n\t"
    "v_mul_f32 v45, v45, v61\n\t"
    "v_mul_f32 v46, v46, v62\n\t"
    "v_mfma_f32_16x16x32_bf16 v[84:87], v[252:255], v[120:123], v[68:71]\n\t"
    "ds_read_b128 v[116:119], %[ea] offset:6144\n\t"
    "v_mul_f32 v47, v47, v63\n\t"
    "v_cvt_pk_bf16_f32 v48, v40, v41\n\t"
    "v_cvt_pk_bf16_f32 v49, v42, v43\n\t"
    "v_mfma_f32_16x16x32_bf16 v[88:91], v[248:251], v[124:127], v[72:75]\n\t"
    "v_cvt_pk_bf16_f32 v50, v44, v45\n\t"
    "v_cvt_pk_bf16_f32 v51, v46, v47\n\t"
    "s_nop 1\n\t"
    "v_mfma_f32_16x16x32_bf16 v[92:95], v[252:255], v[124:127], v[76:79]\n\t"
    "v_permlane16_swap_b32 v48, v50\n\t"
    "v_permlane16_swap_b32 v49, v51\n\t"
    "global_store_dwordx4 %[co0], v[48:51], s[36:37] sc1\n\t"
    "s_mov_b64 s[36:37], s[38:39]\n\t"
    "s_add_u32 s38, s38, %[ab]\n\t"
    "s_addc_u32 s39, s39, 0\n\t"
    "s_cmp_lt_i32 %[b], %[lf]\n\t"
    "s_cbranch_scc1 1b\n\t"
    "s_waitcnt lgkmcnt(0)\n\t"
    "s_nop 4\n\t"
    "s_branch 9f\n\t"
    "5:\n\t"
    "s_add_u32 %[st], %[b], %[po]\n\t"
    "s_max_i32 %[st], %[st], %[p0]\n\t"
    "v_mov_b32 v39, %[st]\n\t"
    "v_mov_b32 v38, 0\n\t"
    "s_mov_b64 exec, 1\n\t"
    "global_store_dword v38, v39, %[pg] sc1\n\t"
    "s_mov_b64 exec, -1\n\t"
    "s_mov_b32 %[p0], %[p1]\n\t"
    "s_xor_b32 %[sl], %[sl], 0x100\n\t"
    "s_cmp_ge_i32 %[lf], %[cp]\n\t"
    "s_cbranch_scc1 6b\n\t"
    "s_mov_b32 m0, %[sl]\n\t"
    "s_nop 0\n\t"
    "global_load_lds_dword %[dv], %[db] sc1\n\t"
    "s_branch 6b\n\t"
    "9:"
    : "=&{v[80:95]}"(pd), [ea] "+&v"(ea), [da] "+&v"(da), [b] "+&s"(b), [lf] "+&s"(lf), "+{s[36:37]}"(p1), "+{s[38:39]}"(p0), [sa] "+&v"(sa), [st] "=&s"(st), [sl] "+&s"(sl), [p0] "+&s"(pf)
    : [rw] "v"(rw), [ab] "s"(ab), [cp] "s"(cp), [mu] "s"(mu), [ad] "v"(ad), "{v[128:159]}"(W[0]), "{v[160:191]}"(W[1]), "{v[192:223]}"(W[2]), "{v[224:255]}"(W[3]), [co0] "v"(co0), [w0] "s"(w0), [dv] "v"(dv), [db] "s"(db), [pg] "s"(pg), [po] "s"(po - 1), [p1] "s"(b + po), [nl] "s"(-LOG2E)
    : "v38", "v39", "v40", "v41", "v42", "v43", "v44", "v45", "v46", "v47", "v48", "v49", "v50", "v51", "v52", "v53", "v54", "v55", "v56", "v57", "v58", "v59", "v60", "v61", "v62", "v63", "v64", "v65", "v66", "v67", "v68", "v69", "v70", "v71", "v72", "v73", "v74", "v75", "v76", "v77", "v78", "v79", "v96", "v97", "v98", "v99", "v100", "v101", "v102", "v103", "v104", "v105", "v106", "v107", "v108", "v109", "v110", "v111", "v112", "v113", "v114", "v115", "v116", "v117", "v118", "v119", "v120", "v121", "v122", "v123", "v124", "v125", "v126", "v127", "vcc", "scc", "m0", "memory");
}
__device__ __forceinline__ void fast_st2(const u32x32 (&W)[4], u32x16& pd, unsigned ea, unsigned da, int& b, int& lf, int cp, unsigned sa, unsigned mu, unsigned ad,
    unsigned rw, unsigned ab, const char* p1, const char* p0, unsigned co0, unsigned co1, int po, int pf, unsigned w0, unsigned sl, unsigned dv, const char* db, const char* pg) {
  unsigned st;
  asm volatile("s_barrier\n\t"
    "ds_read_b128 v[96:99], %[ea] offset:0\n\t"
    "ds_read_b128 v[100:103], %[ea] offset:2048\n\t"
    "ds_read_b128 v[104:107], %[da] offset:0\n\t"
    "ds_read_b128 v[108:111], %[da] offset:2048\n\t"
    "ds_read_b128 v[112:115], %[ea] offset:4096\n\t"
    "ds_read_b128 v[116:119], %[ea] offset:6144\n\t"
    "s_branch 2f\n\t"
    "1:\n\t"
    "s_barrier\n\t"
    "2:\n\t"
    "s_waitcnt lgkmcnt(4)\n\t"
    "v_mfma_f32_16x16x32_bf16 v[64:67], v[128:131], v[96:99], 0\n\t"
    "ds_read_b128 v[120:123], %[da] offset:4096\n\t"
    "v_mfma_f32_16x16x32_bf16 v[68:71], v[132:135], v[96:99], 0\n\t"
    "ds_read_b128 v[124:127], %[da] offset:6144\n\t"
    "v_mfma_f32_16x16x32_bf16 v[72:75], v[128:131], v[100:103], 0\n\t"
    "v_mfma_f32_16x16x32_bf16 v[76:79], v[132:135], v[100:103], 0\n\t"
    "s_waitcnt lgkmcnt(4)\n\t"
    "v_mfma_f32_16x16x32_bf16 v[64:67], v[136:139], v[104:107], v[64:67]\n\t"
    "ds_read_b128 v[96:99], %[ea] offset:8192\n\t"
    "v_mfma_f32_16x16x32_bf16 v[68:71], v[140:143], v[104:107], v[68:71]\n\t"
    "ds_read_b128 v[100:103], %[ea] offset:10240\n\t"
    "v_mfma_f32_16x16x32_bf16 v[72:75], v[136:139], v[108:111], v[72:75]\n\t"
    "v_mfma_f32_16x16x32_bf16 v[76:79], v[140:143], v[108:111], v[76:79]\n\t"
    "s_waitcnt lgkmcnt(4)\n\t"
    "v_mfma_f32_16x16x32_bf16 v[64:67], v[144:147], v[112:115], v[64:67]\n\t"
    "ds_read_b128 v[104:107], %[da] offset:8192\n\t"
    "v_mfma_f32_16x16x32_bf16 v[68:71], v[148:151], v[112:115], v[68:71]\n\t"
    "ds_read_b128 v[108:111], %[da] offset:10240\n\t"
    "v_mfma_f32_16x16x32_bf16 v[72:75], v[144:147], v[116:119], v[72:75]\n\t"
    "v_mfma_f32_16x16x32_bf16 v[76:79], v[148:151], v[116:119], v[76:79]\n\t"
    "s_waitcnt lgkmcnt(4)\n\t"
    "v_mfma_f32_16x16x32_bf16 v[64:67], v[152:155], v[120:123], v[64:67]\n\t"
    "ds_read_b128 v[112:115], %[ea] offset:12288\n\t"
    "v_mfma_f32_16x16x32_bf16 v[68:71], v[156:159], v[120:123], v[68:71]\n\t"
    "ds_read_b128 v[116:119], %[ea] offset:14336\n\t"
    "v_mfma_f32_16x16x32_bf16 v[72:75], v[152:155], v[124:127], v[72:75]\n\t"
    "v_mfma_f32_16x16x32_bf16 v[76:79], v[156:159], v[124:127], v[76:79]\n\t"
    "s_waitcnt lgkmcnt(4)\n\t"
    "v_mfma_f32_16x16x32_bf16 v[64:67], v[160:163], v[96:99], v[64:67]\n\t"
    "ds_read_b128 v[120:123], %[da] offset:12288\n\t"
    "v_mfma_f32_16x16x32_bf16 v[68:71], v[164:167], v[96:99], v[68:71]\n\t"
    "ds_read_b128 v[124:127], %[da] offset:14336\n\t"
    "v_mfma_f32_16x16x32_bf16 v[72:75], v[160:163], v[100:103], v[72:75]\n\t"
    "v_mfma_f32_16x16x32_bf16 v[76:79], v[164:167], v[100:103], v[76:79]\n\t"
    "s_waitcnt lgkmcnt(4)\n\t"
    "v_mfma_f32_16x16x32_bf16 v[64:67], v[168:171], v[104:107], v[64:67]\n\t"
    "ds_read_b128 v[96:99], %[ea] offset:16384\n\t"
    "v_mfma_f32_16x16x32_bf16 v[68:71], v[172:175], v[104:107], v[68:71]\n\t"
    "ds_read_b128 v[100:103], %[ea] offset:18432\n\t"
    "v_mfma_f32_16x16x32_bf16 v[72:75], v[168:171], v[108:111], v[72:75]\n\t"
    "v_mfma_f32_16x16x32_bf16 v[76:79], v[172:175], v[108:111], v[76:79]\n\t"
    "s_waitcnt lgkmcnt(4)\n\t"
    "v_mfma_f32_16x16x32_bf16 v[64:67], v[176:179], v[112:115], v[64:67]\n\t"
    "ds_read_b128 v[104:107], %[da] offset:16384\n\t"
    "v_mfma_f32_16x16x32_bf16 v[68:71], v[180:183], v[112:115], v[68:71]\n\t"
    "ds_read_b128 v[108:111], %[da] offset:18432\n\t"
    "v_mfma_f32_16x16x32_bf16 v[72:75], v[176:179], v[116:119], v[72:75]\n\t"
    "v_mfma_f32_16x16x32_bf16 v[76:79], v[180:183], v[116:119], v[76:79]\n\t"
    "s_waitcnt lgkmcnt(4)\n\t"
    "v_mfma_f32_16x16x32_bf16 v[64:67], v[184:187], v[120:123], v[64:67]\n\t"
    "ds_read_b128 v[112:115], %[ea] offset:20480\n\t"
    "v_mfma_f32_16x16x32_bf16 v[68:71], v[188:191], v[120:123], v[68:71]\n\t"
    "ds_read_b128 v[116:119], %[ea] offset:22528\n\t"
    "v_mfma_f32_16x16x32_bf16 v[72:75], v[184:187], v[124:127], v[72:75]\n\t"
    "v_mfma_f32_16x16x32_bf16 v[76:79], v[188:191], v[124:127], v[76:79]\n\t"
    "s_waitcnt vmcnt(0)\n\t"
    "s_barrier\n\t"
    "s_waitcnt lgkmcnt(4)\n\t"
    "v_mfma_f32_16x16x32_bf16 v[64:67], v[192:195], v[96:99], v[64:67]\n\t"
    "ds_read_b128 v[120:123], %[da] offset:20480\n\t"
    "v_mfma_f32_16x16x32_bf16 v[68:71], v[196:199], v[96:99], v[68:71]\n\t"
    "ds_read_b128 v[124:127], %[da] offset:22528\n\t"
    "v_mfma_f32_16x16x32_bf16 v[72:75], v[192:195], v[100:103], v[72:75]\n\t"
    "ds_read_b128 v[48:51], %[rw] offset:0\n\t"
    "v_mfma_f32_16x16x32_bf16 v[76:79], v[196:199], v[100:103], v[76:79]\n\t"
    "ds_read_b128 v[52:55], %[rw] offset:1024\n\t"
    "s_cmp_eq_u32 %[w0], 0\n\t"
    "s_cbranch_scc1 5f\n\t"
    "6:\n\t"
    "s_waitcnt lgkmcnt(6)\n\t"
    "v_mfma_f32_16x16x32_bf16 v[64:67], v[200:203], v[104:107], v[64:67]\n\t"
    "ds_read_b128 v[96:99], %[ea] offset:24576\n\t"
    "v_mfma_f32_16x16x32_bf16 v[68:71], v[204:207], v[104:107], v[68:71]\n\t"
    "ds_read_b128 v[100:103], %[ea] offset:26624\n\t"
    "v_mfma_f32_16x16x32_bf16 v[72:75], v[200:203], v[108:111], v[72:75]\n\t"
    "ds_read_b128 v[56:59], %[rw] offset:2048\n\t"
    "v_mfma_f32_16x16x32_bf16 v[76:79], v[204:207], v[108:111], v[76:79]\n\t"
    "ds_read_b128 v[60:63], %[rw] offset:3072\n\t"
    "s_waitcnt lgkmcnt(0)\n\t"
    "v_mfma_f32_16x16x32_bf16 v[64:67], v[208:211], v[112:115], v[64:67]\n\t"
    "ds_read_b128 v[104:107], %[da] offset:24576\n\t"
    "v_cvt_pk_bf16_f32 v40, v80, v81\n\t"
    "v_mfma_f32_16x16x32_bf16 v[68:71], v[212:215], v[112:115], v[68:71]\n\t"
    "ds_read_b128 v[108:111], %[da] offset:26624\n\t"
    "v_cvt_pk_bf16_f32 v41, v82, v83\n\t"
    "v_mfma_f32_16x16x32_bf16 v[72:75], v[208:211], v[116:119], v[72:75]\n\t"
    "ds_read_b32 v39, %[sa]\n\t"
    "v_cvt_pk_bf16_f32 v42, v84, v85\n\t"
    "v_mfma_f32_16x16x32_bf16 v[76:79], v[212:215], v[116:119], v[76:79]\n\t"
    "v_xor_b32 %[sa], 0x100, %[sa]\n\t"
    "v_cvt_pk_bf16_f32 v43, v86, v87\n\t"
    "v_mfma_f32_16x16x32_bf16 v[64:67], v[216:219], v[120:123], v[64:67]\n\t"
    "ds_read_b128 v[112:115], %[ea] offset:28672\n\t"
    "global_store_dwordx4 %[co0], v[40:43], s[36:37] sc1\n\t"
    "v_mfma_f32_16x16x32_bf16 v[68:71], v[220:223], v[120:123], v[68:71]\n\t"
    "ds_read_b128 v[116:119], %[ea] offset:30720\n\t"
    "v_cvt_pk_bf16_f32 v44, v88, v89\n\t"
    "v_mfma_f32_16x16x32_bf16 v[72:75], v[216:219], v[124:127], v[72:75]\n\t"
    "v_xor_b32 %[ea], 0x10000, %[ea]\n\t"
    "v_cvt_pk_bf16_f32 v45, v90, v91\n\t"
    "v_mfma_f32_16x16x32_bf16 v[76:79], v[220:223], v[124:127], v[76:79]\n\t"
    "v_cvt_pk_bf16_f32 v46, v92, v93\n\t"
    "v_mfma_f32_16x16x32_bf16 v[64:67], v[224:227], v[96:99], v[64:67]\n\t"
    "ds_read_b128 v[120:123], %[da] offset:28672\n\t"
    "v_cvt_pk_bf16_f32 v47, v94, v95\n\t"
    "v_mfma_f32_16x16x32_bf16 v[68:71], v[228:231], v[96:99], v[68:71]\n\t"
    "ds_read_b128 v[124:127], %[da] offset:30720\n\t"
    "global_store_dwordx4 %[co1], v[44:47], s[36:37] sc1\n\t"
    "v_mfma_f32_16x16x32_bf16 v[72:75], v[224:227], v[100:103], v[72:75]\n\t"
    "v_xor_b32 %[da], 0x10000, %[da]\n\t"
    "v_cvt_pk_bf16_f32 v48, v48, v49\n\t"
    "v_mfma_f32_16x16x32_bf16 v[76:79], v[228:231], v[100:103], v[76:79]\n\t"
    "v_cvt_pk_bf16_f32 v49, v50, v51\n\t"
    "s_waitcnt lgkmcnt(5)\n\t"
    "v_mfma_f32_16x16x32_bf16 v[64:67], v[232:235], v[104:107], v[64:67]\n\t"
    "ds_read_b128 v[96:99], %[ea] offset:0\n\t"
    "v_cvt_pk_bf16_f32 v50, v52, v53\n\t"
    "v_mfma_f32_16x16x32_bf16 v[68:71], v[236:239], v[104:107], v[68:71]\n\t"
    "ds_read_b128 v[100:103], %[ea] offset:2048\n\t"
    "v_cvt_pk_bf16_f32 v51, v54, v55\n\t"
    "v_mfma_f32_16x16x32_bf16 v[72:75], v[232:235], v[108:111], v[72:75]\n\t"
    "global_store_dwordx4 %[co0], v[48:51], s[36:37] offset:2048 sc1\n\t"
    "v_mfma_f32_16x16x32_bf16 v[76:79], v[236:239], v[108:111], v[76:79]\n\t"
    "v_cvt_pk_bf16_f32 v56, v56, v57\n\t"
    "s_waitcnt lgkmcnt(4)\n\t"
    "v_mfma_f32_16x16x32_bf16 v[64:67], v[240:243], v[112:115], v[64:67]\n\t"
    "ds_read_b128 v[104:107], %[da] offset:0\n\t"
    "v_mad_u32_u24 v39, v39, %[mu], %[ad]\n\t"
    "v_cvt_pk_bf16_f32 v57, v58, v59\n\t"
    "v_mfma_f32_16x16x32_bf16 v[68:71], v[244:247], v[112:115], v[68:71]\n\t"
    "ds_read_b128 v[108:111], %[da] offset:2048\n\t"
    "v_cmp_ge_i32 vcc, %[lf], v39\n\t"
    "s_cmp_eq_u64 vcc, 0\n\t"
    "s_addc_u32 %[lf], %[lf], 0\n\t"
    "v_cvt_pk_bf16_f32 v58, v60, v61\n\t"
    "v_mfma_f32_16x16x32_bf16 v[72:75], v[240:243], v[116:119], v[72:75]\n\t"
    "v_cvt_pk_bf16_f32 v59, v62, v63\n\t"
    "v_mfma_f32_16x16x32_bf16 v[76:79], v[244:247], v[116:119], v[76:79]\n\t"
    "v_cmp_ge_i32 vcc, %[lf], v39\n\t"
    "s_cmp_eq_u64 vcc, 0\n\t"
    "s_addc_u32 %[lf], %[lf], 0\n\t"
    "global_store_dwordx4 %[co1], v[56:59], s[36:37] offset:2048 sc1\n\t"
    "s_waitcnt lgkmcnt(4)\n\t"
    "v_mfma_f32_16x16x32_bf16 v[80:83], v[248:251], v[120:123], v[64:67]\n\t"
    "ds_read_b128 v[112:115], %[ea] offset:4096\n\t"
    "s_min_i32 %[lf], %[lf], %[cp]\n\t"
    "s_add_u32 %[b], %[b], 1\n\t"
    "v_mfma_f32_16x16x32_bf16 v[84:87], v[252:255], v[120:123], v[68:71]\n\t"
    "ds_read_b128 v[116:119], %[ea] offset:6144\n\t"
    "v_mfma_f32_16x16x32_bf16 v[88:91], v[248:251], v[124:127], v[72:75]\n\t"
    "v_mfma_f32_16x16x32_bf16 v[92:95], v[252:255], v[124:127], v[76:79]\n\t"
    "s_mov_b64 s[36:37], s[38:39]\n\t"
    "s_add_u32 s38, s38, %[ab]\n\t"
    "s_addc_u32 s39, s39, 0\n\t"
    "s_cmp_lt_i32 %[b], %[lf]\n\t"
    "s_cbranch_scc1 1b\n\t"
    "s_waitcnt lgkmcnt(0)\n\t"
    "s_nop 4\n\t"
    "s_branch 9f\n\t"
    "5:\n\t"
    "s_add_u32 %[st], %[b], %[po]\n\t"
    "s_max_i32 %[st], %[st], %[p0]\n\t"
    "v_mov_b32 v39, %[st]\n\t"
    "v_mov_b32 v38, 0\n\t"
    "s_mov_b64 exec, 1\n\t"
    "global_store_dword v38, v39, %[pg] sc1\n\t"
    "s_mov_b64 exec, -1\n\t"
    "s_mov_b32 %[p0], %[p1]\n\t"
    "s_xor_b32 %[sl], %[sl], 0x100\n\t"
    "s_cmp_ge_i32 %[lf], %[cp]\n\t"
    "s_cbranch_scc1 6b\n\t"
    "s_mov_b32 m0, %[sl]\n\t"
    "s_nop 0\n\t"
    "global_load_lds_dword %[dv], %[db] sc1\n\t"
    "s_branch 6b\n\t"
    "9:"
    : "=&{v[80:95]}"(pd), [ea] "+&v"(ea), [da] "+&v"(da), [b] "+&s"(b), [lf] "+&s"(lf), "+{s[36:37]}"(p1), "+{s[38:39]}"(p0), [sa] "+&v"(sa), [st] "=&s"(st), [sl] "+&s"(sl), [p0] "+&s"(pf)
    : [rw] "v"(rw), [ab] "s"(ab), [cp] "s"(cp), [mu] "s"(mu), [ad] "v"(ad), "{v[128:159]}"(W[0]), "{v[160:191]}"(W[1]), "{v[192:223]}"(W[2]), "{v[224:255]}"(W[3]), [co0] "v"(co0), [w0] "s"(w0), [dv] "v"(dv), [db] "s"(db), [pg] "s"(pg), [po] "s"(po - 1), [p1] "s"(b + po), [co1] "v"(co1)
    : "v38", "v39", "v40", "v41", "v42", "v43", "v44", "v45", "v46", "v47", "v48", "v49", "v50", "v51", "v52", "v53", "v54", "v55", "v56", "v57", "v58", "v59", "v60", "v61", "v62", "v63", "v64", "v65", "v66", "v67", "v68", "v69", "v70", "v71", "v72", "v73", "v74", "v75", "v76", "v77", "v78", "v79", "v96", "v97", "v98", "v99", "v100", "v101", "v102", "v103", "v104", "v105", "v106", "v107", "v108", "v109", "v110", "v111", "v112", "v113", "v114", "v115", "v116", "v117", "v118", "v119", "v120", "v121", "v122", "v123", "v124", "v125", "v126", "v127", "vcc", "scc", "m0", "memory");
}
__device__ __forceinline__ void near_ld0(const u32x32 (&W)[4], u32x16& pd, unsigned ea, unsigned da, int& b, int& lf, int cp, unsigned sa, unsigned mu, unsigned ad,
    unsigned rw, unsigned ab, const char* p1, const char* p0, unsigned l1, unsigned l0, const unsigned (&av)[8]) {
  asm volatile("s_setprio 0\n\t"
    "s_waitcnt vmcnt(4)\n\t"
    "s_barrier\n\t"
    "ds_read_b128 v[96:99], %[ea] offset:0\n\t"
    "ds_read_b128 v[100:103], %[ea] offset:2048\n\t"
    "ds_read_b128 v[104:107], %[da] offset:0\n\t"
    "ds_read_b128 v[108:111], %[da] offset:2048\n\t"
    "ds_read_b128 v[112:115], %[ea] offset:4096\n\t"
    "ds_read_b128 v[116:119], %[ea] offset:6144\n\t"
    "s_branch 2f\n\t"
    "1:\n\t"
    "s_waitcnt vmcnt(4)\n\t"
    "s_nop 1\n\t"
    "s_barrier\n\t"
    "2:\n\t"
    "s_waitcnt lgkmcnt(4)\n\t"
    "v_mfma_f32_16x16x32_bf16 v[64:67], v[128:131], v[96:99], 0\n\t"
    "ds_read_b128 v[120:123], %[da] offset:4096\n\t"
    "s_add_u32 m0, %[l1], 0\n\t"
    "ds_write_b128 %[rw], v[80:83] offset:0\n\t"
    "v_mfma_f32_16x16x32_bf16 v[68:71], v[132:135], v[96:99], 0\n\t"
    "ds_read_b128 v[124:127], %[da] offset:6144\n\t"
    "global_load_lds_dwordx4 %[av0], s[36:37] sc1\n\t"
    "ds_write_b128 %[rw], v[84:87] offset:1024\n\t"
    "v_mfma_f32_16x16x32_bf16 v[72:75], v[128:131], v[100:103], 0\n\t"
    "s_add_u32 m0, %[l1], 32768\n\t"
    "ds_write_b128 %[rw], v[88:91] offset:2048\n\t"
    "v_mfma_f32_16x16x32_bf16 v[76:79], v[132:135], v[100:103], 0\n\t"
    "global_load_lds_dwordx4 %[av1], s[36:37] sc1\n\t"
    "ds_write_b128 %[rw], v[92:95] offset:3072\n\t"
    "s_waitcnt lgkmcnt(8)\n\t"
    "v_mfma_f32_16x16x32_bf16 v[64:67], v[136:139], v[104:107], v[64:67]\n\t"
    "ds_read_b128 v[96:99], %[ea] offset:8192\n\t"
    "s_add_u32 m0, %[l1], 4096\n\t"
    "v_mfma_f32_16x16x32_bf16 v[68:71], v[140:143], v[104:107], v[68:71]\n\t"
    "ds_read_b128 v[100:103], %[ea] offset:10240\n\t"
    "global_load_lds_dwordx4 %[av2], s[36:37] sc1\n\t"
    "v_mfma_f32_16x16x32_bf16 v[72:75], v[136:139], v[108:111], v[72:75]\n\t"
    "s_add_u32 m0, %[l1], 36864\n\t"
    "v_mfma_f32_16x16x32_bf16 v[76:79], v[140:143], v[108:111], v[76:79]\n\t"
    "global_load_lds_dwordx4 %[av3], s[36:37] sc1\n\t"
    "s_waitcnt lgkmcnt(2)\n\t"
    "v_mfma_f32_16x16x32_bf16 v[64:67], v[144:147], v[112:115], v[64:67]\n\t"
    "ds_read_b128 v[104:107], %[da] offset:8192\n\t"
    "s_add_u32 m0, %[l1], 8192\n\t"
    "v_mfma_f32_16x16x32_bf16 v[68:71], v[148:151], v[112:115], v[68:71]\n\t"
    "ds_read_b128 v[108:111], %[da] offset:10240\n\t"
    "global_load_lds_dwordx4 %[av4], s[36:37] sc1\n\t"
    "v_mfma_f32_16x16x32_bf16 v[72:75], v[144:147], v[116:119], v[72:75]\n\t"
    "s_add_u32 m0, %[l1], 40960\n\t"
    "v_mfma_f32_16x16x32_bf16 v[76:79], v[148:151], v[116:119], v[76:79]\n\t"
    "global_load_lds_dwordx4 %[av5], s[36:37] sc1\n\t"
    "s_waitcnt vmcnt(6)\n\t"
    "s_barrier\n\t"
    "v_mfma_f32_16x16x32_bf16 v[64:67], v[152:155], v[120:123], v[64:67]\n\t"
    "ds_read_b128 v[112:115], %[ea] offset:12288\n\t"
    "s_add_u32 m0, %[l1], 12288\n\t"
    "v_mfma_f32_16x16x32_bf16 v[68:71], v[156:159], v[120:123], v[68:71]\n\t"
    "ds_read_b128 v[116:119], %[ea] offset:14336\n\t"
    "global_load_lds_dwordx4 %[av6], s[36:37] sc1\n\t"
    "v_mfma_f32_16x16x32_bf16 v[72:75], v[152:155], v[124:127], v[72:75]\n\t"
    "s_add_u32 m0, %[l1], 45056\n\t"
    "v_mfma_f32_16x16x32_bf16 v[76:79], v[156:159], v[124:127], v[76:79]\n\t"
    "global_load_lds_dwordx4 %[av7], s[36:37] sc1\n\t"
    "s_waitcnt lgkmcnt(4)\n\t"
    "v_mfma_f32_16x16x32_bf16 v[64:67], v[160:163], v[96:99], v[64:67]\n\t"
    "ds_read_b128 v[120:123], %[da] offset:12288\n\t"
    "s_add_u32 m0, %[l0], 0\n\t"
    "v_mfma_f32_16x16x32_bf16 v[68:71], v[164:167], v[96:99], v[68:71]\n\t"
    "ds_read_b128 v[124:127], %[da] offset:14336\n\t"
    "global_load_lds_dwordx4 %[av0], s[38:39] sc1\n\t"
    "v_mfma_f32_16x16x32_bf16 v[72:75], v[160:163], v[100:103], v[72:75]\n\t"
    "s_add_u32 m0, %[l0], 32768\n\t"
    "v_mfma_f32_16x16x32_bf16 v[76:79], v[164:167], v[100:103], v[76:79]\n\t"
    "global_load_lds_dwordx4 %[av1], s[38:39] sc1\n\t"
    "s_waitcnt lgkmcnt(4)\n\t"
    "v_mfma_f32_16x16x32_bf16 v[64:67], v[168:171], v[104:107], v[64:67]\n\t"
    "ds_read_b128 v[96:99], %[ea] offset:16384\n\t"
    "s_add_u32 m0, %[l0], 4096\n\t"
    "v_mfma_f32_16x16x32_bf16 v[68:71], v[172:175], v[104:107], v[68:71]\n\t"
    "ds_read_b128 v[100:103], %[ea] offset:18432\n\t"
    "global_load_lds_dwordx4 %[av2], s[38:39] sc1\n\t"
    "v_mfma_f32_16x16x32_bf16 v[72:75], v[168:171], v[108:111], v[72:75]\n\t"
    "s_add_u32 m0, %[l0], 36864\n\t"
    "v_mfma_f32_16x16x32_bf16 v[76:79], v[172:175], v[108:111], v[76:79]\n\t"
    "global_load_lds_dwordx4 %[av3], s[38:39] sc1\n\t"
    "s_waitcnt lgkmcnt(4)\n\t"
    "v_mfma_f32_16x16x32_bf16 v[64:67], v[176:179], v[112:115], v[64:67]\n\t"
    "ds_read_b128 v[104:107], %[da] offset:16384\n\t"
    "s_add_u32 m0, %[l0], 8192\n\t"
    "v_mfma_f32_16x16x32_bf16 v[68:71], v[180:183], v[112:115], v[68:71]\n\t"
    "ds_read_b128 v[108:111], %[da] offset:18432\n\t"
    "global_load_lds_dwordx4 %[av4], s[38:39] sc1\n\t"
    "v_mfma_f32_16x16x32_bf16 v[72:75], v[176:179], v[116:119], v[72:75]\n\t"
    "s_add_u32 m0, %[l0], 40960\n\t"
    "v_mfma_f32_16x16x32_bf16 v[76:79], v[180:183], v[116:119], v[76:79]\n\t"
    "global_load_lds_dwordx4 %[av5], s[38:39] sc1\n\t"
    "s_waitcnt lgkmcnt(4)\n\t"
    "v_mfma_f32_16x16x32_bf16 v[64:67], v[184:187], v[120:123], v[64:67]\n\t"
    "ds_read_b128 v[112:115], %[ea] offset:20480\n\t"
    "s_add_u32 m0, %[l0], 12288\n\t"
    "v_mfma_f32_16x16x32_bf16 v[68:71], v[188:191], v[120:123], v[68:71]\n\t"
    "ds_read_b128 v[116:119], %[ea] offset:22528\n\t"
    "global_load_lds_dwordx4 %[av6], s[38:39] sc1\n\t"
    "v_mfma_f32_16x16x32_bf16 v[72:75], v[184:187], v[124:127], v[72:75]\n\t"
    "s_add_u32 m0, %[l0], 45056\n\t"
    "v_mfma_f32_16x16x32_bf16 v[76:79], v[188:191], v[124:127], v[76:79]\n\t"
    "global_load_lds_dwordx4 %[av7], s[38:39] sc1\n\t"
    "s_waitcnt lgkmcnt(4)\n\t"
    "v_mfma_f32_16x16x32_bf16 v[64:67], v[192:195], v[96:99], v[64:67]\n\t"
    "ds_read_b128 v[120:123], %[da] offset:20480\n\t"
    "v_mfma_f32_16x16x32_bf16 v[68:71], v[196:199], v[96:99], v[68:71]\n\t"
    "ds_read_b128 v[124:127], %[da] offset:22528\n\t"
    "v_mfma_f32_16x16x32_bf16 v[72:75], v[192:195], v[100:103], v[72:75]\n\t"
    "v_mfma_f32_16x16x32_bf16 v[76:79], v[196:199], v[100:103], v[76:79]\n\t"
    "s_waitcnt lgkmcnt(4)\n\t"
    "v_mfma_f32_16x16x32_bf16 v[64:67], v[200:203], v[104:107], v[64:67]\n\t"
    "ds_read_b128 v[96:99], %[ea] offset:24576\n\t"
    "v_mfma_f32_16x16x32_bf16 v[68:71], v[204:207], v[104:107], v[68:71]\n\t"
    "ds_read_b128 v[100:103], %[ea] offset:26624\n\t"
    "v_mfma_f32_16x16x32_bf16 v[72:75], v[200:203], v[108:111], v[72:75]\n\t"
    "v_mfma_f32_16x16x32_bf16 v[76:79], v[204:207], v[108:111], v[76:79]\n\t"
    "s_add_u32 s36, s36, %[ab]\n\t"
    "s_addc_u32 s37, s37, 0\n\t"
    "s_waitcnt lgkmcnt(4)\n\t"
    "v_mfma_f32_16x16x32_bf16 v[64:67], v[208:211], v[112:115], v[64:67]\n\t"
    "ds_read_b128 v[104:107], %[da] offset:24576\n\t"
    "v_mfma_f32_16x16x32_bf16 v[68:71], v[212:215], v[112:115], v[68:71]\n\t"
    "ds_read_b128 v[108:111], %[da] offset:26624\n\t"
    "v_mfma_f32_16x16x32_bf16 v[72:75], v[208:211], v[116:119], v[72:75]\n\t"
    "v_mfma_f32_16x16x32_bf16 v[76:79], v[212:215], v[116:119], v[76:79]\n\t"
    "s_xor_b32 %[l1], %[l1], 0x10000\n\t"
    "s_waitcnt lgkmcnt(4)\n\t"
    "v_mfma_f32_16x16x32_bf16 v[64:67], v[216:219], v[120:123], v[64:67]\n\t"
    "ds_read_b128 v[112:115], %[ea] offset:28672\n\t"
    "v_mfma_f32_16x16x32_bf16 v[68:71], v[220:223], v[120:123], v[68:71]\n\t"
    "ds_read_b128 v[116:119], %[ea] offset:30720\n\t"
    "v_mfma_f32_16x16x32_bf16 v[72:75], v[216:219], v[124:127], v[72:75]\n\t"
    "v_xor_b32 %[ea], 0x10000, %[ea]\n\t"
    "v_mfma_f32_16x16x32_bf16 v[76:79], v[220:223], v[124:127], v[76:79]\n\t"
    "s_waitcnt vmcnt(12)\n\t"
    "s_barrier\n\t"
    "s_waitcnt lgkmcnt(4)\n\t"
    "v_mfma_f32_16x16x32_bf16 v[64:67], v[224:227], v[96:99], v[64:67]\n\t"
    "ds_read_b32 v39, %[sa]\n\t"
    "ds_read_b128 v[120:123], %[da] offset:28672\n\t"
    "v_mfma_f32_16x16x32_bf16 v[68:71], v[228:231], v[96:99], v[68:71]\n\t"
    "ds_read_b128 v[124:127], %[da] offset:30720\n\t"
    "v_mfma_f32_16x16x32_bf16 v[72:75], v[224:227], v[100:103], v[72:75]\n\t"
    "v_xor_b32 %[da], 0x10000, %[da]\n\t"
    "v_mfma_f32_16x16x32_bf16 v[76:79], v[228:231], v[100:103], v[76:79]\n\t"
    "s_waitcnt lgkmcnt(5)\n\t"
    "v_mfma_f32_16x16x32_bf16 v[64:67], v[232:235], v[104:107], v[64:67]\n\t"
    "ds_read_b128 v[96:99], %[ea] offset:0\n\t"
    "v_mfma_f32_16x16x32_bf16 v[68:71], v[236:239], v[104:107], v[68:71]\n\t"
    "ds_read_b128 v[100:103], %[ea] offset:2048\n\t"
    "v_mfma_f32_16x16x32_bf16 v[72:75], v[232:235], v[108:111], v[72:75]\n\t"
    "v_mfma_f32_16x16x32_bf16 v[76:79], v[236:239], v[108:111], v[76:79]\n\t"
    "s_waitcnt lgkmcnt(4)\n\t"
    "v_mfma_f32_16x16x32_bf16 v[64:67], v[240:243], v[112:115], v[64:67]\n\t"
    "ds_read_b128 v[104:107], %[da] offset:0\n\t"
    "v_mad_u32_u24 v39, v39, %[mu], %[ad]\n\t"
    "v_mfma_f32_16x16x32_bf16 v[68:71], v[244:247], v[112:115], v[68:71]\n\t"
    "ds_read_b128 v[108:111], %[da] offset:2048\n\t"
    "v_cmp_ge_i32 vcc, %[lf], v39\n\t"
    "s_cmp_eq_u64 vcc, 0\n\t"
    "s_addc_u32 %[lf], %[lf], 0\n\t"
    "v_mfma_f32_16x16x32_bf16 v[72:75], v[240:243], v[116:119], v[72:75]\n\t"
    "v_mfma_f32_16x16x32_bf16 v[76:79], v[244:247], v[116:119], v[76:79]\n\t"
    "v_cmp_ge_i32 vcc, %[lf], v39\n\t"
    "s_cmp_eq_u64 vcc, 0\n\t"
    "s_addc_u32 %[lf], %[lf], 0\n\t"
    "s_waitcnt lgkmcnt(4)\n\t"
    "v_mfma_f32_16x16x32_bf16 v[80:83], v[248:251], v[120:123], v[64:67]\n\t"
    "ds_read_b128 v[112:115], %[ea] offset:4096\n\t"
    "s_min_i32 %[lf], %[lf], %[cp]\n\t"
    "s_add_u32 %[b], %[b], 1\n\t"
    "v_mfma_f32_16x16x32_bf16 v[84:87], v[252:255], v[120:123], v[68:71]\n\t"
    "ds_read_b128 v[116:119], %[ea] offset:6144\n\t"
    "v_mfma_f32_16x16x32_bf16 v[88:91], v[248:251], v[124:127], v[72:75]\n\t"
    "v_mfma_f32_16x16x32_bf16 v[92:95], v[252:255], v[124:127], v[76:79]\n\t"
    "s_add_u32 s38, s38, %[ab]\n\t"
    "s_addc_u32 s39, s39, 0\n\t"
    "s_xor_b32 %[l0], %[l0], 0x10000\n\t"
    "s_cmp_lt_i32 %[b], %[lf]\n\t"
    "s_cbranch_scc1 1b\n\t"
    "s_waitcnt lgkmcnt(0)\n\t"
    "s_nop 4"
    : "=&{v[80:95]}"(pd), [ea] "+&v"(ea), [da] "+&v"(da), [b] "+&s"(b), [lf] "+&s"(lf), "+{s[36:37]}"(p1), "+{s[38:39]}"(p0), [sa] "+&v"(sa), [l1] "+&s"(l1), [l0] "+&s"(l0)
    : [rw] "v"(rw), [ab] "s"(ab), [cp] "s"(cp), [mu] "s"(mu), [ad] "v"(ad), "{v[128:159]}"(W[0]), "{v[160:191]}"(W[1]), "{v[192:223]}"(W[2]), "{v[224:255]}"(W[3]), [av0] "v"(av[0]), [av1] "v"(av[1]), [av2] "v"(av[2]), [av3] "v"(av[3]), [av4] "v"(av[4]), [av5] "v"(av[5]), [av6] "v"(av[6]), [av7] "v"(av[7])
    : "v39", "v64", "v65", "v66", "v67", "v68", "v69", "v70", "v71", "v72", "v73", "v74", "v75", "v76", "v77", "v78", "v79", "v96", "v97", "v98", "v99", "v100", "v101", "v102", "v103", "v104", "v105", "v106", "v107", "v108", "v109", "v110", "v111", "v112", "v113", "v114", "v115", "v116", "v117", "v118", "v119", "v120", "v121", "v122", "v123", "v124", "v125", "v126", "v127", "vcc", "scc", "m0", "memory");
}
__device__ __forceinline__ void near_ld2(const u32x32 (&W)[4], u32x16& pd, unsigned ea, unsigned da, int& b, int& lf, int cp, unsigned sa, unsigned mu, unsigned ad,
    unsigned rw, unsigned ab, const char* p1, const char* p0, unsigned l1, unsigned l0, const unsigned (&av)[8]) {
  asm volatile("s_setprio 0\n\t"
    "s_waitcnt vmcnt(2)\n\t"
    "s_barrier\n\t"
    "ds_read_b128 v[96:99], %[ea] offset:0\n\t"
    "ds_read_b128 v[100:103], %[ea] offset:2048\n\t"
    "ds_read_b128 v[104:107], %[da] offset:0\n\t"
    "ds_read_b128 v[108:111], %[da] offset:2048\n\t"
    "ds_read_b128 v[112:115], %[ea] offset:4096\n\t"
    "ds_read_b128 v[116:119], %[ea] offset:6144\n\t"
    "s_branch 2f\n\t"
    "1:\n\t"
    "s_waitcnt vmcnt(2)\n\t"
    "s_nop 1\n\t"
    "s_barrier\n\t"
    "2:\n\t"
    "s_waitcnt lgkmcnt(4)\n\t"
    "v_mfma_f32_16x16x32_bf16 v[64:67], v[128:131], v[96:99], 0\n\t"
    "ds_read_b128 v[120:123], %[da] offset:4096\n\t"
    "s_add_u32 m0, %[l1], 0\n\t"
    "ds_write_b128 %[rw], v[80:83] offset:0\n\t"
    "v_mfma_f32_16x16x32_bf16 v[68:71], v[132:135], v[96:99], 0\n\t"
    "ds_read_b128 v[124:127], %[da] offset:6144\n\t"
    "global_load_lds_dwordx4 %[av0], s[36:37] sc1\n\t"
    "ds_write_b128 %[rw], v[84:87] offset:1024\n\t"
    "v_mfma_f32_16x16x32_bf16 v[72:75], v[128:131], v[100:103], 0\n\t"
    "ds_write_b128 %[rw], v[88:91] offset:2048\n\t"
    "v_mfma_f32_16x16x32_bf16 v[76:79], v[132:135], v[100:103], 0\n\t"
    "ds_write_b128 %[rw], v[92:95] offset:3072\n\t"
    "s_waitcnt lgkmcnt(8)\n\t"
    "v_mfma_f32_16x16x32_bf16 v[64:67], v[136:139], v[104:107], v[64:67]\n\t"
    "ds_read_b128 v[96:99], %[ea] offset:8192\n\t"
    "s_add_u32 m0, %[l1], 4096\n\t"
    "v_mfma_f32_16x16x32_bf16 v[68:71], v[140:143], v[104:107], v[68:71]\n\t"
    "ds_read_b128 v[100:103], %[ea] offset:10240\n\t"
    "global_load_lds_dwordx4 %[av2], s[36:37] sc1\n\t"
    "v_mfma_f32_16x16x32_bf16 v[72:75], v[136:139], v[108:111], v[72:75]\n\t"
    "v_mfma_f32_16x16x32_bf16 v[76:79], v[140:143], v[108:111], v[76:79]\n\t"
    "s_waitcnt lgkmcnt(2)\n\t"
    "v_mfma_f32_16x16x32_bf16 v[64:67], v[144:147], v[112:115], v[64:67]\n\t"
    "ds_read_b128 v[104:107], %[da] offset:8192\n\t"
    "s_add_u32 m0, %[l1], 8192\n\t"
    "v_mfma_f32_16x16x32_bf16 v[68:71], v[148:151], v[112:115], v[68:71]\n\t"
    "ds_read_b128 v[108:111], %[da] offset:10240\n\t"
    "global_load_lds_dwordx4 %[av4], s[36:37] sc1\n\t"
    "v_mfma_f32_16x16x32_bf16 v[72:75], v[144:147], v[116:119], v[72:75]\n\t"
    "v_mfma_f32_16x16x32_bf16 v[76:79], v[148:151], v[116:119], v[76:79]\n\t"
    "s_waitcnt vmcnt(3)\n\t"
    "s_barrier\n\t"
    "v_mfma_f32_16x16x32_bf16 v[64:67], v[152:155], v[120:123], v[64:67]\n\t"
    "ds_read_b128 v[112:115], %[ea] offset:12288\n\t"
    "s_add_u32 m0, %[l1], 12288\n\t"
    "v_mfma_f32_16x16x32_bf16 v[68:71], v[156:159], v[120:123], v[68:71]\n\t"
    "ds_read_b128 v[116:119], %[ea] offset:14336\n\t"
    "global_load_lds_dwordx4 %[av6], s[36:37] sc1\n\t"
    "v_mfma_f32_16x16x32_bf16 v[72:75], v[152:155], v[124:127], v[72:75]\n\t"
    "v_mfma_f32_16x16x32_bf16 v[76:79], v[156:159], v[124:127], v[76:79]\n\t"
    "s_waitcnt lgkmcnt(4)\n\t"
    "v_mfma_f32_16x16x32_bf16 v[64:67], v[160:163], v[96:99], v[64:67]\n\t"
    "ds_read_b128 v[120:123], %[da] offset:12288\n\t"
    "s_add_u32 m0, %[l0], 0\n\t"
    "v_mfma_f32_16x16x32_bf16 v[68:71], v[164:167], v[96:99], v[68:71]\n\t"
    "ds_read_b128 v[124:127], %[da] offset:14336\n\t"
    "global_load_lds_dwordx4 %[av0], s[38:39] sc1\n\t"
    "v_mfma_f32_16x16x32_bf16 v[72:75], v[160:163], v[100:103], v[72:75]\n\t"
    "v_mfma_f32_16x16x32_bf16 v[76:79], v[164:167], v[100:103], v[76:79]\n\t"
    "s_waitcnt lgkmcnt(4)\n\t"
    "v_mfma_f32_16x16x32_bf16 v[64:67], v[168:171], v[104:107], v[64:67]\n\t"
    "ds_read_b128 v[96:99], %[ea] offset:16384\n\t"
    "s_add_u32 m0, %[l0], 4096\n\t"
    "v_mfma_f32_16x16x32_bf16 v[68:71], v[172:175], v[104:107], v[68:71]\n\t"
    "ds_read_b128 v[100:103], %[ea] offset:18432\n\t"
    "global_load_lds_dwordx4 %[av2], s[38:39] sc1\n\t"
    "v_mfma_f32_16x16x32_bf16 v[72:75], v[168:171], v[108:111], v[72:75]\n\t"
    "v_mfma_f32_16x16x32_bf16 v[76:79], v[172:175], v[108:111], v[76:79]\n\t"
    "s_waitcnt lgkmcnt(4)\n\t"
    "v_mfma_f32_16x16x32_bf16 v[64:67], v[176:179], v[112:115], v[64:67]\n\t"
    "ds_read_b128 v[104:107], %[da] offset:16384\n\t"
    "s_add_u32 m0, %[l0], 8192\n\t"
    "v_mfma_f32_16x16x32_bf16 v[68:71], v[180:183], v[112:115], v[68:71]\n\t"
    "ds_read_b128 v[108:111], %[da] offset:18432\n\t"
    "global_load_lds_dwordx4 %[av4], s[38:39] sc1\n\t"
    "v_mfma_f32_16x16x32_bf16 v[72:75], v[176:179], v[116:119], v[72:75]\n\t"
    "v_mfma_f32_16x16x32_bf16 v[76:79], v[180:183], v[116:119], v[76:79]\n\t"
    "s_waitcnt lgkmcnt(4)\n\t"
    "v_mfma_f32_16x16x32_bf16 v[64:67], v[184:187], v[120:123], v[64:67]\n\t"
    "ds_read_b128 v[112:115], %[ea] offset:20480\n\t"
    "s_add_u32 m0, %[l0], 12288\n\t"
    "v_mfma_f32_16x16x32_bf16 v[68:71], v[188:191], v[120:123], v[68:71]\n\t"
    "ds_read_b128 v[116:119], %[ea] offset:22528\n\t"
    "global_load_lds_dwordx4 %[av6], s[38:39] sc1\n\t"
    "v_mfma_f32_16x16x32_bf16 v[72:75], v[184:187], v[124:127], v[72:75]\n\t"
    "v_mfma_f32_16x16x32_bf16 v[76:79], v[188:191], v[124:127], v[76:79]\n\t"
    "s_waitcnt lgkmcnt(4)\n\t"
    "v_mfma_f32_16x16x32_bf16 v[64:67], v[192:195], v[96:99], v[64:67]\n\t"
    "ds_read_b128 v[120:123], %[da] offset:20480\n\t"
    "v_mfma_f32_16x16x32_bf16 v[68:71], v[196:199], v[96:99], v[68:71]\n\t"
    "ds_read_b128 v[124:127], %[da] offset:22528\n\t"
    "v_mfma_f32_16x16x32_bf16 v[72:75], v[192:195], v[100:103], v[72:75]\n\t"
    "v_mfma_f32_16x16x32_bf16 v[76:79], v[196:199], v[100:103], v[76:79]\n\t"
    "s_waitcnt lgkmcnt(4)\n\t"
    "v_mfma_f32_16x16x32_bf16 v[64:67], v[200:203], v[104:107], v[64:67]\n\t"
    "ds_read_b128 v[96:99], %[ea] offset:24576\n\t"
    "v_mfma_f32_16x16x32_bf16 v[68:71], v[204:207], v[104:107], v[68:71]\n\t"
    "ds_read_b128 v[100:103], %[ea] offset:26624\n\t"
    "v_mfma_f32_16x16x32_bf16 v[72:75], v[200:203], v[108:111], v[72:75]\n\t"
    "v_mfma_f32_16x16x32_bf16 v[76:79], v[204:207], v[108:111], v[76:79]\n\t"
    "s_add_u32 s36, s36, %[ab]\n\t"
    "s_addc_u32 s37, s37, 0\n\t"
    "s_waitcnt lgkmcnt(4)\n\t"
    "v_mfma_f32_16x16x32_bf16 v[64:67], v[208:211], v[112:115], v[64:67]\n\t"
    "ds_read_b128 v[104:107], %[da] offset:24576\n\t"
    "v_mfma_f32_16x16x32_bf16 v[68:71], v[212:215], v[112:115], v[68:71]\n\t"
    "ds_read_b128 v[108:111], %[da] offset:26624\n\t"
    "v_mfma_f32_16x16x32_bf16 v[72:75], v[208:211], v[116:119], v[72:75]\n\t"
    "v_mfma_f32_16x16x32_bf16 v[76:79], v[212:215], v[116:119], v[76:79]\n\t"
    "s_xor_b32 %[l1], %[l1], 0x10000\n\t"
    "s_waitcnt lgkmcnt(4)\n\t"
    "v_mfma_f32_16x16x32_bf16 v[64:67], v[216:219], v[120:123], v[64:67]\n\t"
    "ds_read_b128 v[112:115], %[ea] offset:28672\n\t"
    "v_mfma_f32_16x16x32_bf16 v[68:71], v[220:223], v[120:123], v[68:71]\n\t"
    "ds_read_b128 v[116:119], %[ea] offset:30720\n\t"
    "v_mfma_f32_16x16x32_bf16 v[72:75], v[216:219], v[124:127], v[72:75]\n\t"
    "v_xor_b32 %[ea], 0x10000, %[ea]\n\t"
    "v_mfma_f32_16x16x32_bf16 v[76:79], v[220:223], v[124:127], v[76:79]\n\t"
    "s_waitcnt vmcnt(6)\n\t"
    "s_barrier\n\t"
    "s_waitcnt lgkmcnt(4)\n\t"
    "v_mfma_f32_16x16x32_bf16 v[64:67], v[224:227], v[96:99], v[64:67]\n\t"
    "ds_read_b32 v39, %[sa]\n\t"
    "ds_read_b128 v[120:123], %[da] offset:28672\n\t"
    "v_mfma_f32_16x16x32_bf16 v[68:71], v[228:231], v[96:99], v[68:71]\n\t"
    "ds_read_b128 v[124:127], %[da] offset:30720\n\t"
    "v_mfma_f32_16x16x32_bf16 v[72:75], v[224:227], v[100:103], v[72:75]\n\t"
    "v_xor_b32 %[da], 0x10000, %[da]\n\t"
    "v_mfma_f32_16x16x32_bf16 v[76:79], v[228:231], v[100:103], v[76:79]\n\t"
    "s_waitcnt lgkmcnt(5)\n\t"
    "v_mfma_f32_16x16x32_bf16 v[64:67], v[232:235], v[104:107], v[64:67]\n\t"
    "ds_read_b128 v[96:99], %[ea] offset:0\n\t"
    "v_mfma_f32_16x16x32_bf16 v[68:71], v[236:239], v[104:107], v[68:71]\n\t"
    "ds_read_b128 v[100:103], %[ea] offset:2048\n\t"
    "v_mfma_f32_16x16x32_bf16 v[72:75], v[232:235], v[108:111], v[72:75]\n\t"
    "v_mfma_f32_16x16x32_bf16 v[76:79], v[236:239], v[108:111], v[76:79]\n\t"
    "s_waitcnt lgkmcnt(4)\n\t"
    "v_mfma_f32_16x16x32_bf16 v[64:67], v[240:243], v[112:115], v[64:67]\n\t"
    "ds_read_b128 v[104:107], %[da] offset:0\n\t"
    "v_mad_u32_u24 v39, v39, %[mu], %[ad]\n\t"
    "v_mfma_f32_16x16x32_bf16 v[68:71], v[244:247], v[112:115], v[68:71]\n\t"
    "ds_read_b128 v[108:111], %[da] offset:2048\n\t"
    "v_cmp_ge_i32 vcc, %[lf], v39\n\t"
    "s_cmp_eq_u64 vcc, 0\n\t"
    "s_addc_u32 %[lf], %[lf], 0\n\t"
    "v_mfma_f32_16x16x32_bf16 v[72:75], v[240:243], v[116:119], v[72:75]\n\t"
    "v_mfma_f32_16x16x32_bf16 v[76:79], v[244:247], v[116:119], v[76:79]\n\t"
    "v_cmp_ge_i32 vcc, %[lf], v39\n\t"
    "s_cmp_eq_u64 vcc, 0\n\t"
    "s_addc_u32 %[lf], %[lf], 0\n\t"
    "s_waitcnt lgkmcnt(4)\n\t"
    "v_mfma_f32_16x16x32_bf16 v[80:83], v[248:251], v[120:123], v[64:67]\n\t"
    "ds_read_b128 v[112:115], %[ea] offset:4096\n\t"
    "s_min_i32 %[lf], %[lf], %[cp]\n\t"
    "s_add_u32 %[b], %[b], 1\n\t"
    "v_mfma_f32_16x16x32_bf16 v[84:87], v[252:255], v[120:123], v[68:71]\n\t"
    "ds_read_b128 v[116:119], %[ea] offset:6144\n\t"
    "v_mfma_f32_16x16x32_bf16 v[88:91], v[248:251], v[124:127], v[72:75]\n\t"
    "v_mfma_f32_16x16x32_bf16 v[92:95], v[252:255], v[124:127], v[76:79]\n\t"
    "s_add_u32 s38, s38, %[ab]\n\t"
    "s_addc_u32 s39, s39, 0\n\t"
    "s_xor_b32 %[l0], %[l0], 0x10000\n\t"
    "s_cmp_lt_i32 %[b], %[lf]\n\t"
    "s_cbranch_scc1 1b\n\t"
    "s_waitcnt lgkmcnt(0)\n\t"
    "s_nop 4"
    : "=&{v[80:95]}"(pd), [ea] "+&v"(ea), [da] "+&v"(da), [b] "+&s"(b), [lf] "+&s"(lf), "+{s[36:37]}"(p1), "+{s[38:39]}"(p0), [sa] "+&v"(sa), [l1] "+&s"(l1), [l0] "+&s"(l0)
    : [rw] "v"(rw), [ab] "s"(ab), [cp] "s"(cp), [mu] "s"(mu), [ad] "v"(ad), "{v[128:159]}"(W[0]), "{v[160:191]}"(W[1]), "{v[192:223]}"(W[2]), "{v[224:255]}"(W[3]), [av0] "v"(av[0]), [av1] "v"(av[1]), [av2] "v"(av[2]), [av3] "v"(av[3]), [av4] "v"(av[4]), [av5] "v"(av[5]), [av6] "v"(av[6]), [av7] "v"(av[7])
    : "v39", "v64", "v65", "v66", "v67", "v68", "v69", "v70", "v71", "v72", "v73", "v74", "v75", "v76", "v77", "v78", "v79", "v96", "v97", "v98", "v99", "v100", "v101", "v102", "v103", "v104", "v105", "v106", "v107", "v108", "v109", "v110", "v111", "v112", "v113", "v114", "v115", "v116", "v117", "v118", "v119", "v120", "v121", "v122", "v123", "v124", "v125", "v126", "v127", "vcc", "scc", "m0", "memory");
}
__device__ __forceinline__ void near_st0(const u32x32 (&W)[4], u32x16& pd, unsigned ea, unsigned da, int& b, int& lf, int cp, unsigned sa, unsigned mu, unsigned ad,
    unsigned rw, unsigned ab, const char* p1, const char* p0, unsigned co0, unsigned co1, int po, int pf, unsigned w0, unsigned sl, unsigned dv, const char* db, const char* pg) {
  unsigned st;
  asm volatile("s_waitcnt vmcnt(0)\n\t"
    "s_barrier\n\t"
    "ds_read_b128 v[96:99], %[ea] offset:0\n\t"
    "ds_read_b128 v[100:103], %[ea] offset:2048\n\t"
    "ds_read_b128 v[104:107], %[da] offset:0\n\t"
    "ds_read_b128 v[108:111], %[da] offset:2048\n\t"
    "ds_read_b128 v[112:115], %[ea] offset:4096\n\t"
    "ds_read_b128 v[116:119], %[ea] offset:6144\n\t"
    "s_branch 2f\n\t"
    "1:\n\t"
    "s_waitcnt vmcnt(0)\n\t"
    "s_barrier\n\t"
    "2:\n\t"
    "s_waitcnt lgkmcnt(4)\n\t"
    "v_mfma_f32_16x16x32_bf16 v[64:67], v[128:131], v[96:99], 0\n\t"
    "ds_read_b128 v[120:123], %[da] offset:4096\n\t"
    "v_mfma_f32_16x16x32_bf16 v[68:71], v[132:135], v[96:99], 0\n\t"
    "ds_read_b128 v[124:127], %[da] offset:6144\n\t"
    "v_mfma_f32_16x16x32_bf16 v[72:75], v[128:131], v[100:103], 0\n\t"
    "v_mfma_f32_16x16x32_bf16 v[76:79], v[132:135], v[100:103], 0\n\t"
    "s_cmp_eq_u32 %[w0], 0\n\t"
    "s_cbranch_scc1 5f\n\t"
    "6:\n\t"
    "s_waitcnt lgkmcnt(4)\n\t"
    "v_mfma_f32_16x16x32_bf16 v[64:67], v[136:139], v[104:107], v[64:67]\n\t"
    "ds_read_b128 v[96:99], %[ea] offset:8192\n\t"
    "v_mfma_f32_16x16x32_bf16 v[68:71], v[140:143], v[104:107], v[68:71]\n\t"
    "ds_read_b128 v[100:103], %[ea] offset:10240\n\t"
    "v_mfma_f32_16x16x32_bf16 v[72:75], v[136:139], v[108:111], v[72:75]\n\t"
    "v_mfma_f32_16x16x32_bf16 v[76:79], v[140:143], v[108:111], v[76:79]\n\t"
    "s_waitcnt lgkmcnt(4)\n\t"
    "v_mfma_f32_16x16x32_bf16 v[64:67], v[144:147], v[112:115], v[64:67]\n\t"
    "ds_read_b128 v[104:107], %[da] offset:8192\n\t"
    "v_mfma_f32_16x16x32_bf16 v[68:71], v[148:151], v[112:115], v[68:71]\n\t"
    "ds_read_b128 v[108:111], %[da] offset:10240\n\t"
    "v_mfma_f32_16x16x32_bf16 v[72:75], v[144:147], v[116:119], v[72:75]\n\t"
    "v_mfma_f32_16x16x32_bf16 v[76:79], v[148:151], v[116:119], v[76:79]\n\t"
    "s_barrier\n\t"
    "s_waitcnt lgkmcnt(4)\n\t"
    "v_mfma_f32_16x16x32_bf16 v[64:67], v[152:155], v[120:123], v[64:67]\n\t"
    "ds_read_b128 v[112:115], %[ea] offset:12288\n\t"
    "v_mfma_f32_16x16x32_bf16 v[68:71], v[156:159], v[120:123], v[68:71]\n\t"
    "ds_read_b128 v[116:119], %[ea] offset:14336\n\t"
    "v_mfma_f32_16x16x32_bf16 v[72:75], v[152:155], v[124:127], v[72:75]\n\t"
    "ds_read_b128 v[48:51], %[rw] offset:0\n\t"
    "v_mfma_f32_16x16x32_bf16 v[76:79], v[156:159], v[124:127], v[76:79]\n\t"
    "ds_read_b128 v[52:55], %[rw] offset:1024\n\t"
    "s_waitcnt lgkmcnt(6)\n\t"
    "v_mfma_f32_16x16x32_bf16 v[64:67], v[160:163], v[96:99], v[64:67]\n\t"
    "ds_read_b128 v[120:123], %[da] offset:12288\n\t"
    "v_mfma_f32_16x16x32_bf16 v[68:71], v[164:167], v[96:99], v[68:71]\n\t"
    "ds_read_b128 v[124:127], %[da] offset:14336\n\t"
    "v_mfma_f32_16x16x32_bf16 v[72:75], v[160:163], v[100:103], v[72:75]\n\t"
    "ds_read_b128 v[56:59], %[rw] offset:2048\n\t"
    "v_mfma_f32_16x16x32_bf16 v[76:79], v[164:167], v[100:103], v[76:79]\n\t"
    "ds_read_b128 v[60:63], %[rw] offset:3072\n\t"
    "s_waitcnt lgkmcnt(0)\n\t"
    "v_mfma_f32_16x16x32_bf16 v[64:67], v[168:171], v[104:107], v[64:67]\n\t"
    "ds_read_b128 v[96:99], %[ea] offset:16384\n\t"
    "v_add_f32 v48, v48, v80\n\t"
    "v_add_f32 v49, v49, v81\n\t"
    "v_mfma_f32_16x16x32_bf16 v[68:71], v[172:175], v[104:107], v[68:71]\n\t"
    "ds_read_b128 v[100:103], %[ea] offset:18432\n\t"
    "v_add_f32 v50, v50, v82\n\t"
    "v_mfma_f32_16x16x32_bf16 v[72:75], v[168:171], v[108:111], v[72:75]\n\t"
    "v_add_f32 v51, v51, v83\n\t"
    "v_mfma_f32_16x16x32_bf16 v[76:79], v[172:175], v[108:111], v[76:79]\n\t"
    "v_add_f32 v52, v52, v84\n\t"
    "v_add_f32 v53, v53, v85\n\t"
    "v_mfma_f32_16x16x32_bf16 v[64:67], v[176:179], v[112:115], v[64:67]\n\t"
    "ds_read_b128 v[104:107], %[da] offset:16384\n\t"
    "v_add_f32 v54, v54, v86\n\t"
    "v_mfma_f32_16x16x32_bf16 v[68:71], v[180:183], v[112:115], v[68:71]\n\t"
    "ds_read_b128 v[108:111], %[da] offset:18432\n\t"
    "v_add_f32 v55, v55, v87\n\t"
    "v_mfma_f32_16x16x32_bf16 v[72:75], v[176:179], v[116:119], v[72:75]\n\t"
    "v_add_f32 v56, v56, v88\n\t"
    "v_add_f32 v57, v57, v89\n\t"
    "v_mfma_f32_16x16x32_bf16 v[76:79], v[180:183], v[116:119], v[76:79]\n\t"
    "v_add_f32 v58, v58, v90\n\t"
    "v_mfma_f32_16x16x32_bf16 v[64:67], v[184:187], v[120:123], v[64:67]\n\t"
    "ds_read_b128 v[112:115], %[ea] offset:20480\n\t"
    "v_add_f32 v59, v59, v91\n\t"
    "v_mfma_f32_16x16x32_bf16 v[68:71], v[188:191], v[120:123], v[68:71]\n\t"
    "ds_read_b128 v[116:119], %[ea] offset:22528\n\t"
    "v_add_f32 v60, v60, v92\n\t"
    "v_mfma_f32_16x16x32_bf16 v[72:75], v[184:187], v[124:127], v[72:75]\n\t"
    "v_add_f32 v61, v61, v93\n\t"
    "v_add_f32 v62, v62, v94\n\t"
    "v_mfma_f32_16x16x32_bf16 v[76:79], v[188:191], v[124:127], v[76:79]\n\t"
    "v_add_f32 v63, v63, v95\n\t"
    "s_waitcnt lgkmcnt(4)\n\t"
    "v_mfma_f32_16x16x32_bf16 v[64:67], v[192:195], v[96:99], v[64:67]\n\t"
    "ds_read_b128 v[120:123], %[da] offset:20480\n\t"
    "v_cvt_pk_bf16_f32 v40, v48, v49\n\t"
    "v_mfma_f32_16x16x32_bf16 v[68:71], v[196:199], v[96:99], v[68:71]\n\t"
    "ds_read_b128 v[124:127], %[da] offset:22528\n\t"
    "v_cvt_pk_bf16_f32 v41, v50, v51\n\t"
    "v_cvt_pk_bf16_f32 v42, v52, v53\n\t"
    "v_mfma_f32_16x16x32_bf16 v[72:75], v[192:195], v[100:103], v[72:75]\n\t"
    "v_cvt_pk_bf16_f32 v43, v54, v55\n\t"
    "v_mfma_f32_16x16x32_bf16 v[76:79], v[196:199], v[100:103], v[76:79]\n\t"
    "global_store_dwordx4 %[co0], v[40:43], s[36:37] sc1\n\t"
    "s_waitcnt lgkmcnt(4)\n\t"
    "v_mfma_f32_16x16x32_bf16 v[64:67], v[200:203], v[104:107], v[64:67]\n\t"
    "ds_read_b128 v[96:99], %[ea] offset:24576\n\t"
    "v_cvt_pk_bf16_f32 v44, v56, v57\n\t"
    "v_cvt_pk_bf16_f32 v45, v58, v59\n\t"
    "v_mfma_f32_16x16x32_bf16 v[68:71], v[204:207], v[104:107], v[68:71]\n\t"
    "ds_read_b128 v[100:103], %[ea] offset:26624\n\t"
    "v_cvt_pk_bf16_f32 v46, v60, v61\n\t"
    "v_mfma_f32_16x16x32_bf16 v[72:75], v[200:203], v[108:111], v[72:75]\n\t"
    "v_cvt_pk_bf16_f32 v47, v62, v63\n\t"
    "v_mfma_f32_16x16x32_bf16 v[76:79], v[204:207], v[108:111], v[76:79]\n\t"
    "global_store_dwordx4 %[co1], v[44:47], s[36:37] sc1\n\t"
    "s_waitcnt lgkmcnt(4)\n\t"
    "v_mfma_f32_16x16x32_bf16 v[64:67], v[208:211], v[112:115], v[64:67]\n\t"
    "ds_read_b128 v[104:107], %[da] offset:24576\n\t"
    "v_mfma_f32_16x16x32_bf16 v[68:71], v[212:215], v[112:115], v[68:71]\n\t"
    "ds_read_b128 v[108:111], %[da] offset:26624\n\t"
    "v_mfma_f32_16x16x32_bf16 v[72:75], v[208:211], v[116:119], v[72:75]\n\t"
    "v_mfma_f32_16x16x32_bf16 v[76:79], v[212:215], v[116:119], v[76:79]\n\t"
    "s_waitcnt lgkmcnt(4)\n\t"
    "v_mfma_f32_16x16x32_bf16 v[64:67], v[216:219], v[120:123], v[64:67]\n\t"
    "ds_read_b128 v[112:115], %[ea] offset:28672\n\t"
    "v_mfma_f32_16x16x32_bf16 v[68:71], v[220:223], v[120:123], v[68:71]\n\t"
    "ds_read_b128 v[116:119], %[ea] offset:30720\n\t"
    "v_mfma_f32_16x16x32_bf16 v[72:75], v[216:219], v[124:127], v[72:75]\n\t"
    "v_xor_b32 %[ea], 0x10000, %[ea]\n\t"
    "v_mfma_f32_16x16x32_bf16 v[76:79], v[220:223], v[124:127], v[76:79]\n\t"
    "s_waitcnt vmcnt(2)\n\t"
    "s_barrier\n\t"
    "s_waitcnt lgkmcnt(4)\n\t"
    "v_mfma_f32_16x16x32_bf16 v[64:67], v[224:227], v[96:99], v[64:67]\n\t"
    "ds_read_b32 v39, %[sa]\n\t"
    "ds_read_b128 v[120:123], %[da] offset:28672\n\t"
    "v_mfma_f32_16x16x32_bf16 v[68:71], v[228:231], v[96:99], v[68:71]\n\t"
    "ds_read_b128 v[124:127], %[da] offset:30720\n\t"
    "v_mfma_f32_16x16x32_bf16 v[72:75], v[224:227], v[100:103], v[72:75]\n\t"
    "v_xor_b32 %[da], 0x10000, %[da]\n\t"
    "v_mfma_f32_16x16x32_bf16 v[76:79], v[228:231], v[100:103], v[76:79]\n\t"
    "s_waitcnt lgkmcnt(5)\n\t"
    "v_mfma_f32_16x16x32_bf16 v[64:67], v[232:235], v[104:107], v[64:67]\n\t"
    "ds_read_b128 v[96:99], %[ea] offset:0\n\t"
    "v_mfma_f32_16x16x32_bf16 v[68:71], v[236:239], v[104:107], v[68:71]\n\t"
    "ds_read_b128 v[100:103], %[ea] offset:2048\n\t"
    "v_mfma_f32_16x16x32_bf16 v[72:75], v[232:235], v[108:111], v[72:75]\n\t"
    "v_mfma_f32_16x16x32_bf16 v[76:79], v[236:239], v[108:111], v[76:79]\n\t"
    "s_waitcnt lgkmcnt(4)\n\t"
    "v_mfma_f32_16x16x32_bf16 v[64:67], v[240:243], v[112:115], v[64:67]\n\t"
    "ds_read_b128 v[104:107], %[da] offset:0\n\t"
    "v_mad_u32_u24 v39, v39, %[mu], %[ad]\n\t"
    "v_mfma_f32_16x16x32_bf16 v[68:71], v[244:247], v[112:115], v[68:71]\n\t"
    "ds_read_b128 v[108:111], %[da] offset:2048\n\t"
    "v_cmp_ge_i32 vcc, %[lf], v39\n\t"
    "s_cmp_eq_u64 vcc, 0\n\t"
    "s_addc_u32 %[lf], %[lf], 0\n\t"
    "v_mfma_f32_16x16x32_bf16 v[72:75], v[240:243], v[116:119], v[72:75]\n\t"
    "v_mfma_f32_16x16x32_bf16 v[76:79], v[244:247], v[116:119], v[76:79]\n\t"
    "v_cmp_ge_i32 vcc, %[lf], v39\n\t"
    "s_cmp_eq_u64 vcc, 0\n\t"
    "s_addc_u32 %[lf], %[lf], 0\n\t"
    "s_waitcnt lgkmcnt(4)\n\t"
    "v_mfma_f32_16x16x32_bf16 v[80:83], v[248:251], v[120:123], v[64:67]\n\t"
    "ds_read_b128 v[112:115], %[ea] offset:4096\n\t"
    "s_min_i32 %[lf], %[lf], %[cp]\n\t"
    "s_add_u32 %[b], %[b], 1\n\t"
    "v_mfma_f32_16x16x32_bf16 v[84:87], v[252:255], v[120:123], v[68:71]\n\t"
    "ds_read_b128 v[116:119], %[ea] offset:6144\n\t"
    "v_mfma_f32_16x16x32_bf16 v[88:91], v[248:251], v[124:127], v[72:75]\n\t"
    "v_mfma_f32_16x16x32_bf16 v[92:95], v[252:255], v[124:127], v[76:79]\n\t"
    "s_mov_b64 s[36:37], s[38:39]\n\t"
    "s_add_u32 s38, s38, %[ab]\n\t"
    "s_addc_u32 s39, s39, 0\n\t"
    "s_cmp_lt_i32 %[b], %[lf]\n\t"
    "s_cbranch_scc1 1b\n\t"
    "s_waitcnt lgkmcnt(0)\n\t"
    "s_nop 4\n\t"
    "s_branch 9f\n\t"
    "5:\n\t"
    "s_add_u32 %[st], %[b], %[po]\n\t"
    "s_max_i32 %[st], %[st], %[p0]\n\t"
    "v_mov_b32 v39, %[st]\n\t"
    "v_mov_b32 v38, 0\n\t"
    "s_mov_b64 exec, 1\n\t"
    "global_store_dword v38, v39, %[pg] sc1\n\t"
    "s_mov_b64 exec, -1\n\t"
    "s_mov_b32 %[p0], %[p1]\n\t"
    "s_cmp_ge_i32 %[lf], %[cp]\n\t"
    "s_cbranch_scc1 6b\n\t"
    "s_mov_b32 m0, %[sl]\n\t"
    "s_nop 0\n\t"
    "global_load_lds_dword %[dv], %[db] sc1\n\t"
    "s_branch 6b\n\t"
    "9:"
    : "=&{v[80:95]}"(pd), [ea] "+&v"(ea), [da] "+&v"(da), [b] "+&s"(b), [lf] "+&s"(lf), "+{s[36:37]}"(p1), "+{s[38:39]}"(p0), [sa] "+&v"(sa), [st] "=&s"(st), [sl] "+&s"(sl), [p0] "+&s"(pf)
    : [rw] "v"(rw), [ab] "s"(ab), [cp] "s"(cp), [mu] "s"(mu), [ad] "v"(ad), "{v[128:159]}"(W[0]), "{v[160:191]}"(W[1]), "{v[192:223]}"(W[2]), "{v[224:255]}"(W[3]), [co0] "v"(co0), [w0] "s"(w0), [dv] "v"(dv), [db] "s"(db), [pg] "s"(pg), [po] "s"(po - 1), [p1] "s"(b + po), [co1] "v"(co1)
    : "v38", "v39", "v40", "v41", "v42", "v43", "v44", "v45", "v46", "v47", "v48", "v49", "v50", "v51", "v52", "v53", "v54", "v55", "v56", "v57", "v58", "v59", "v60", "v61", "v62", "v63", "v64", "v65", "v66", "v67", "v68", "v69", "v70", "v71", "v72", "v73", "v74", "v75", "v76", "v77", "v78", "v79", "v96", "v97", "v98", "v99", "v100", "v101", "v102", "v103", "v104", "v105", "v106", "v107", "v108", "v109", "v110", "v111", "v112", "v113", "v114", "v115", "v116", "v117", "v118", "v119", "v120", "v121", "v122", "v123", "v124", "v125", "v126", "v127", "vcc", "scc", "m0", "memory");
}
__device__ __forceinline__ void near_st1(const u32x32 (&W)[4], u32x16& pd, unsigned ea, unsigned da, int& b, int& lf, int cp, unsigned sa, unsigned mu, unsigned ad,
    unsigned rw, unsigned ab, const char* p1, const char* p0, unsigned co0, unsigned co1, int po, int pf, unsigned w0, unsigned sl, unsigned dv, const char* db, const char* pg) {
  unsigned st;
  asm volatile("s_waitcnt vmcnt(0)\n\t"
    "s_barrier\n\t"
    "ds_read_b128 v[96:99], %[ea] offset:0\n\t"
    "ds_read_b128 v[100:103], %[ea] offset:2048\n\t"
    "ds_read_b128 v[104:107], %[da] offset:0\n\t"
    "ds_read_b128 v[108:111], %[da] offset:2048\n\t"
    "ds_read_b128 v[112:115], %[ea] offset:4096\n\t"
    "ds_read_b128 v[116:119], %[ea] offset:6144\n\t"
    "s_branch 2f\n\t"
    "1:\n\t"
    "s_waitcnt vmcnt(0)\n\t"
    "s_barrier\n\t"
    "2:\n\t"
    "s_waitcnt lgkmcnt(4)\n\t"
    "v_mfma_f32_16x16x32_bf16 v[64:67], v[128:131], v[96:99], 0\n\t"
    "ds_read_b128 v[120:123], %[da] offset:4096\n\t"
    "v_mfma_f32_16x16x32_bf16 v[68:71], v[132:135], v[96:99], 0\n\t"
    "ds_read_b128 v[124:127], %[da] offset:6144\n\t"
    "v_mfma_f32_16x16x32_bf16 v[72:75], v[128:131], v[100:103], 0\n\t"
    "v_mfma_f32_16x16x32_bf16 v[76:79], v[132:135], v[100:103], 0\n\t"
    "s_cmp_eq_u32 %[w0], 0\n\t"
    "s_cbranch_scc1 5f\n\t"
    "6:\n\t"
    "s_waitcnt lgkmcnt(4)\n\t"
    "v_mfma_f32_16x16x32_bf16 v[64:67], v[136:139], v[104:107], v[64:67]\n\t"
    "ds_read_b128 v[96:99], %[ea] offset:8192\n\t"
    "v_mfma_f32_16x16x32_bf16 v[68:71], v[140:143], v[104:107], v[68:71]\n\t"
    "ds_read_b128 v[100:103], %[ea] offset:10240\n\t"
    "v_mfma_f32_16x16x32_bf16 v[72:75], v[136:139], v[108:111], v[72:75]\n\t"
    "v_mfma_f32_16x16x32_bf16 v[76:79], v[140:143], v[108:111], v[76:79]\n\t"
    "s_waitcnt lgkmcnt(4)\n\t"
    "v_mfma_f32_16x16x32_bf16 v[64:67], v[144:147], v[112:115], v[64:67]\n\t"
    "ds_read_b128 v[104:107], %[da] offset:8192\n\t"
    "v_mfma_f32_16x16x32_bf16 v[68:71], v[148:151], v[112:115], v[68:71]\n\t"
    "ds_read_b128 v[108:111], %[da] offset:10240\n\t"
    "v_mfma_f32_16x16x32_bf16 v[72:75], v[144:147], v[116:119], v[72:75]\n\t"
    "v_mfma_f32_16x16x32_bf16 v[76:79], v[148:151], v[116:119], v[76:79]\n\t"
    "s_barrier\n\t"
    "s_waitcnt lgkmcnt(4)\n\t"
    "v_mfma_f32_16x16x32_bf16 v[64:67], v[152:155], v[120:123], v[64:67]\n\t"
    "ds_read_b128 v[112:115], %[ea] offset:12288\n\t"
    "v_mfma_f32_16x16x32_bf16 v[68:71], v[156:159], v[120:123], v[68:71]\n\t"
    "ds_read_b128 v[116:119], %[ea] offset:14336\n\t"
    "v_mfma_f32_16x16x32_bf16 v[72:75], v[152:155], v[124:127], v[72:75]\n\t"
    "ds_read_b128 v[48:51], %[rw] offset:0\n\t"
    "v_mfma_f32_16x16x32_bf16 v[76:79], v[156:159], v[124:127], v[76:79]\n\t"
    "ds_read_b128 v[52:55], %[rw] offset:1024\n\t"
    "s_waitcnt lgkmcnt(6)\n\t"
    "v_mfma_f32_16x16x32_bf16 v[64:67], v[160:163], v[96:99], v[64:67]\n\t"
    "ds_read_b128 v[120:123], %[da] offset:12288\n\t"
    "v_mfma_f32_16x16x32_bf16 v[68:71], v[164:167], v[96:99], v[68:71]\n\t"
    "ds_read_b128 v[124:127], %[da] offset:14336\n\t"
    "v_mfma_f32_16x16x32_bf16 v[72:75], v[160:163], v[100:103], v[72:75]\n\t"
    "ds_read_b128 v[56:59], %[rw] offset:2048\n\t"
    "v_mfma_f32_16x16x32_bf16 v[76:79], v[164:167], v[100:103], v[76:79]\n\t"
    "ds_read_b128 v[60:63], %[rw] offset:3072\n\t"
    "s_waitcnt lgkmcnt(0)\n\t"
    "v_mfma_f32_16x16x32_bf16 v[64:67], v[168:171], v[104:107], v[64:67]\n\t"
    "ds_read_b128 v[96:99], %[ea] offset:16384\n\t"
    "v_add_f32 v48, v48, v80\n\t"
    "v_add_f32 v49, v49, v81\n\t"
    "v_add_f32 v50, v50, v82\n\t"
    "v_mfma_f32_16x16x32_bf16 v[68:71], v[172:175], v[104:107], v[68:71]\n\t"
    "ds_read_b128 v[100:103], %[ea] offset:18432\n\t"
    "v_add_f32 v51, v51, v83\n\t"
    "v_add_f32 v52, v52, v84\n\t"
    "v_add_f32 v53, v53, v85\n\t"
    "v_mfma_f32_16x16x32_bf16 v[72:75], v[168:171], v[108:111], v[72:75]\n\t"
    "v_add_f32 v54, v54, v86\n\t"
    "v_add_f32 v55, v55, v87\n\t"
    "v_add_f32 v56, v56, v88\n\t"
    "v_mfma_f32_16x16x32_bf16 v[76:79], v[172:175], v[108:111], v[76:79]\n\t"
    "v_add_f32 v57, v57, v89\n\t"
    "v_add_f32 v58, v58, v90\n\t"
    "v_add_f32 v59, v59, v91\n\t"
    "v_mfma_f32_16x16x32_bf16 v[64:67], v[176:179], v[112:115], v[64:67]\n\t"
    "ds_read_b128 v[104:107], %[da] offset:16384\n\t"
    "v_add_f32 v60, v60, v92\n\t"
    "v_add_f32 v61, v61, v93\n\t"
    "v_add_f32 v62, v62, v94\n\t"
    "v_mfma_f32_16x16x32_bf16 v[68:71], v[180:183], v[112:115], v[68:71]\n\t"
    "ds_read_b128 v[108:111], %[da] offset:18432\n\t"
    "v_add_f32 v63, v63, v95\n\t"
    "v_mul_f32 v40, %[nl], v48\n\t"
    "v_mul_f32 v41, %[nl], v49\n\t"
    "v_mfma_f32_16x16x32_bf16 v[72:75], v[176:179], v[116:119], v[72:75]\n\t"
    "v_mul_f32 v42, %[nl], v50\n\t"
    "v_mul_f32 v43, %[nl], v51\n\t"
    "v_mul_f32 v44, %[nl], v56\n\t"
    "v_mfma_f32_16x16x32_bf16 v[76:79], v[180:183], v[116:119], v[76:79]\n\t"
    "v_mul_f32 v45, %[nl], v57\n\t"
    "v_mul_f32 v46, %[nl], v58\n\t"
    "v_mul_f32 v47, %[nl], v59\n\t"
    "v_mfma_f32_16x16x32_bf16 v[64:67], v[184:187], v[120:123], v[64:67]\n\t"
    "ds_read_b128 v[112:115], %[ea] offset:20480\n\t"
    "v_exp_f32 v40, v40\n\t"
    "v_exp_f32 v41, v41\n\t"
    "v_exp_f32 v42, v42\n\t"
    "v_mfma_f32_16x16x32_bf16 v[68:71], v[188:191], v[120:123], v[68:71]\n\t"
    "ds_read_b128 v[116:119], %[ea] offset:22528\n\t"
    "v_exp_f32 v43, v43\n\t"
    "v_exp_f32 v44, v44\n\t"
    "v_exp_f32 v45, v45\n\t"
    "v_mfma_f32_16x16x32_bf16 v[72:75], v[184:187], v[124:127], v[72:75]\n\t"
    "v_exp_f32 v46, v46\n\t"
    "v_exp_f32 v47, v47\n\t"
    "v_add_f32 v40, 1.0, v40\n\t"
    "v_mfma_f32_16x16x32_bf16 v[76:79], v[188:191], v[124:127], v[76:79]\n\t"
    "v_add_f32 v41, 1.0, v41\n\t"
    "v_add_f32 v42, 1.0, v42\n\t"
    "v_add_f32 v43, 1.0, v43\n\t"
    "s_waitcnt lgkmcnt(4)\n\t"
    "v_mfma_f32_16x16x32_bf16 v[64:67], v[192:195], v[96:99], v[64:67]\n\t"
    "ds_read_b128 v[120:123], %[da] offset:20480\n\t"
    "v_add_f32 v44, 1.0, v44\n\t"
    "v_add_f32 v45, 1.0, v45\n\t"
    "v_add_f32 v46, 1.0, v46\n\t"
    "v_mfma_f32_16x16x32_bf16 v[68:71], v[196:199], v[96:99], v[68:71]\n\t"
    "ds_read_b128 v[124:127], %[da] offset:22528\n\t"
    "v_add_f32 v47, 1.0, v47\n\t"
    "v_rcp_f32 v40, v40\n\t"
    "v_rcp_f32 v41, v41\n\t"
    "v_mfma_f32_16x16x32_bf16 v[72:75], v[192:195], v[100:103], v[72:75]\n\t"
    "v_rcp_f32 v42, v42\n\t"
    "v_rcp_f32 v43, v43\n\t"
    "v_rcp_f32 v44, v44\n\t"
    "v_mfma_f32_16x16x32_bf16 v[76:79], v[196:199], v[100:103], v[76:79]\n\t"
    "v_rcp_f32 v45, v45\n\t"
    "v_rcp_f32 v46, v46\n\t"
    "v_rcp_f32 v47, v47\n\t"
    "s_waitcnt lgkmcnt(4)\n\t"
    "v_mfma_f32_16x16x32_bf16 v[64:67], v[200:203], v[104:107], v[64:67]\n\t"
    "ds_read_b128 v[96:99], %[ea] offset:24576\n\t"
    "v_mul_f32 v40, v48, v40\n\t"
    "v_mul_f32 v41, v49, v41\n\t"
    "v_mul_f32 v42, v50, v42\n\t"
    "v_mfma_f32_16x16x32_bf16 v[68:71], v[204:207], v[104:107], v[68:71]\n\t"
    "ds_read_b128 v[100:103], %[ea] offset:26624\n\t"
    "v_mul_f32 v43, v51, v43\n\t"
    "v_mul_f32 v44, v56, v44\n\t"
    "v_mul_f32 v45, v57, v45\n\t"
    "v_mfma_f32_16x16x32_bf16 v[72:75], v[200:203], v[108:111], v[72:75]\n\t"
    "v_mul_f32 v46, v58, v46\n\t"
    "v_mul_f32 v47, v59, v47\n\t"
    "v_mul_f32 v40, v40, v52\n\t"
    "v_mfma_f32_16x16x32_bf16 v[76:79], v[204:207], v[108:111], v[76:79]\n\t"
    "v_mul_f32 v41, v41, v53\n\t"
    "v_mul_f32 v42, v42, v54\n\t"
    "v_mul_f32 v43, v43, v55\n\t"
    "s_waitcnt lgkmcnt(4)\n\t"
    "v_mfma_f32_16x16x32_bf16 v[64:67], v[208:211], v[112:115], v[64:67]\n\t"
    "ds_read_b128 v[104:107], %[da] offset:24576\n\t"
    "v_mul_f32 v44, v44, v60\n\t"
    "v_mul_f32 v45, v45, v61\n\t"
    "v_mul_f32 v46, v46, v62\n\t"
    "v_mfma_f32_16x16x32_bf16 v[68:71], v[212:215], v[112:115], v[68:71]\n\t"
    "ds_read_b128 v[108:111], %[da] offset:26624\n\t"
    "v_mul_f32 v47, v47, v63\n\t"
    "v_cvt_pk_bf16_f32 v48, v40, v41\n\t"
    "v_cvt_pk_bf16_f32 v49, v42, v43\n\t"
    "v_mfma_f32_16x16x32_bf16 v[72:75], v[208:211], v[116:119], v[72:75]\n\t"
    "v_cvt_pk_bf16_f32 v50, v44, v45\n\t"
    "v_cvt_pk_bf16_f32 v51, v46, v47\n\t"
    "s_nop 1\n\t"
    "v_mfma_f32_16x16x32_bf16 v[76:79], v[212:215], v[116:119], v[76:79]\n\t"
    "v_permlane16_swap_b32 v48, v50\n\t"
    "v_permlane16_swap_b32 v49, v51\n\t"
    "global_store_dwordx4 %[co0], v[48:51], s[36:37] sc1\n\t"
    "s_waitcnt lgkmcnt(4)\n\t"
    "v_mfma_f32_16x16x32_bf16 v[64:67], v[216:219], v[120:123], v[64:67]\n\t"
    "ds_read_b128 v[112:115], %[ea] offset:28672\n\t"
    "v_mfma_f32_16x16x32_bf16 v[68:71], v[220:223], v[120:123], v[68:71]\n\t"
    "ds_read_b128 v[116:119], %[ea] offset:30720\n\t"
    "v_mfma_f32_16x16x32_bf16 v[72:75], v[216:219], v[124:127], v[72:75]\n\t"
    "v_xor_b32 %[ea], 0x10000, %[ea]\n\t"
    "v_mfma_f32_16x16x32_bf16 v[76:79], v[220:223], v[124:127], v[76:79]\n\t"
    "s_waitcnt vmcnt(1)\n\t"
    "s_barrier\n\t"
    "s_waitcnt lgkmcnt(4)\n\t"
    "v_mfma_f32_16x16x32_bf16 v[64:67], v[224:227], v[96:99], v[64:67]\n\t"
    "ds_read_b32 v39, %[sa]\n\t"
    "ds_read_b128 v[120:123], %[da] offset:28672\n\t"
    "v_mfma_f32_16x16x32_bf16 v[68:71], v[228:231], v[96:99], v[68:71]\n\t"
    "ds_read_b128 v[124:127], %[da] offset:30720\n\t"
    "v_mfma_f32_16x16x32_bf16 v[72:75], v[224:227], v[100:103], v[72:75]\n\t"
    "v_xor_b32 %[da], 0x10000, %[da]\n\t"
    "v_mfma_f32_16x16x32_bf16 v[76:79], v[228:231], v[100:103], v[76:79]\n\t"
    "s_waitcnt lgkmcnt(5)\n\t"
    "v_mfma_f32_16x16x32_bf16 v[64:67], v[232:235], v[104:107], v[64:67]\n\t"
    "ds_read_b128 v[96:99], %[ea] offset:0\n\t"
    "v_mfma_f32_16x16x32_bf16 v[68:71], v[236:239], v[104:107], v[68:71]\n\t"
    "ds_read_b128 v[100:103], %[ea] offset:2048\n\t"
    "v_mfma_f32_16x16x32_bf16 v[72:75], v[232:235], v[108:111], v[72:75]\n\t"
    "v_mfma_f32_16x16x32_bf16 v[76:79], v[236:239], v[108:111], v[76:79]\n\t"
    "s_waitcnt lgkmcnt(4)\n\t"
    "v_mfma_f32_16x16x32_bf16 v[64:67], v[240:243], v[112:115], v[64:67]\n\t"
    "ds_read_b128 v[104:107], %[da] offset:0\n\t"
    "v_mad_u32_u24 v39, v39, %[mu], %[ad]\n\t"
    "v_mfma_f32_16x16x32_bf16 v[68:71], v[244:247], v[112:115], v[68:71]\n\t"
    "ds_read_b128 v[108:111], %[da] offset:2048\n\t"
    "v_cmp_ge_i32 vcc, %[lf], v39\n\t"
    "s_cmp_eq_u64 vcc, 0\n\t"
    "s_addc_u32 %[lf], %[lf], 0\n\t"
    "v_mfma_f32_16x16x32_bf16 v[72:75], v[240:243], v[116:119], v[72:75]\n\t"
    "v_mfma_f32_16x16x32_bf16 v[76:79], v[244:247], v[116:119], v[76:79]\n\t"
    "v_cmp_ge_i32 vcc, %[lf], v39\n\t"
    "s_cmp_eq_u64 vcc, 0\n\t"
    "s_addc_u32 %[lf], %[lf], 0\n\t"
    "s_waitcnt lgkmcnt(4)\n\t"
    "v_mfma_f32_16x16x32_bf16 v[80:83], v[248:251], v[120:123], v[64:67]\n\t"
    "ds_read_b128 v[112:115], %[ea] offset:4096\n\t"
    "s_min_i32 %[lf], %[lf], %[cp]\n\t"
    "s_add_u32 %[b], %[b], 1\n\t"
    "v_mfma_f32_16x16x32_bf16 v[84:87], v[252:255], v[120:123], v[68:71]\n\t"
    "ds_read_b128 v[116:119], %[ea] offset:6144\n\t"
    "v_mfma_f32_16x16x32_bf16 v[88:91], v[248:251], v[124:127], v[72:75]\n\t"
    "v_mfma_f32_16x16x32_bf16 v[92:95], v[252:255], v[124:127], v[76:79]\n\t"
    "s_mov_b64 s[36:37], s[38:39]\n\t"
    "s_add_u32 s38, s38, %[ab]\n\t"
    "s_addc_u32 s39, s39, 0\n\t"
    "s_cmp_lt_i32 %[b], %[lf]\n\t"
    "s_cbranch_scc1 1b\n\t"
    "s_waitcnt lgkmcnt(0)\n\t"
    "s_nop 4\n\t"
    "s_branch 9f\n\t"
    "5:\n\t"
    "s_add_u32 %[st], %[b], %[po]\n\t"
    "s_max_i32 %[st], %[st], %[p0]\n\t"
    "v_mov_b32 v39, %[st]\n\t"
    "v_mov_b32 v38, 0\n\t"
    "s_mov_b64 exec, 1\n\t"
    "global_store_dword v38, v39, %[pg] sc1\n\t"
    "s_mov_b64 exec, -1\n\t"
    "s_mov_b32 %[p0], %[p1]\n\t"
    "s_cmp_ge_i32 %[lf], %[cp]\n\t"
    "s_cbranch_scc1 6b\n\t"
    "s_mov_b32 m0, %[sl]\n\t"
    "s_nop 0\n\t"
    "global_load_lds_dword %[dv], %[db] sc1\n\t"
    "s_branch 6b\n\t"
    "9:"
    : "=&{v[80:95]}"(pd), [ea] "+&v"(ea), [da] "+&v"(da), [b] "+&s"(b), [lf] "+&s"(lf), "+{s[36:37]}"(p1), "+{s[38:39]}"(p0), [sa] "+&v"(sa), [st] "=&s"(st), [sl] "+&s"(sl), [p0] "+&s"(pf)
    : [rw] "v"(rw), [ab] "s"(ab), [cp] "s"(cp), [mu] "s"(mu), [ad] "v"(ad), "{v[128:159]}"(W[0]), "{v[160:191]}"(W[1]), "{v[192:223]}"(W[2]), "{v[224:255]}"(W[3]), [co0] "v"(co0), [w0] "s"(w0), [dv] "v"(dv), [db] "s"(db), [pg] "s"(pg), [po] "s"(po - 1), [p1] "s"(b + po), [nl] "s"(-LOG2E)
    : "v38", "v39", "v40", "v41", "v42", "v43", "v44", "v45", "v46", "v47", "v48", "v49", "v50", "v51", "v52", "v53", "v54", "v55", "v56", "v57", "v58", "v59", "v60", "v61", "v62", "v63", "v64", "v65", "v66", "v67", "v68", "v69", "v70", "v71", "v72", "v73", "v74", "v75", "v76", "v77", "v78", "v79", "v96", "v97", "v98", "v99", "v100", "v101", "v102", "v103", "v104", "v105", "v106", "v107", "v108", "v109", "v110", "v111", "v112", "v113", "v114", "v115", "v116", "v117", "v118", "v119", "v120", "v121", "v122", "v123", "v124", "v125", "v126", "v127", "vcc", "scc", "m0", "memory");
}
__device__ __forceinline__ void near_st2(const u32x32 (&W)[4], u32x16& pd, unsigned ea, unsigned da, int& b, int& lf, int cp, unsigned sa, unsigned mu, unsigned ad,
    unsigned rw, unsigned ab, const char* p1, const char* p0, unsigned co0, unsigned co1, int po, int pf, unsigned w0, unsigned sl, unsigned dv, const char* db, const char* pg) {
  unsigned st;
  asm volatile("s_waitcnt vmcnt(0)\n\t"
    "s_barrier\n\t"
    "ds_read_b128 v[96:99], %[ea] offset:0\n\t"
    "ds_read_b128 v[100:103], %[ea] offset:2048\n\t"
    "ds_read_b128 v[104:107], %[da] offset:0\n\t"
    "ds_read_b128 v[108:111], %[da] offset:2048\n\t"
    "ds_read_b128 v[112:115], %[ea] offset:4096\n\t"
    "ds_read_b128 v[116:119], %[ea] offset:6144\n\t"
    "s_branch 2f\n\t"
    "1:\n\t"
    "s_waitcnt vmcnt(0)\n\t"
    "s_barrier\n\t"
    "2:\n\t"
    "s_waitcnt lgkmcnt(4)\n\t"
    "v_mfma_f32_16x16x32_bf16 v[64:67], v[128:131], v[96:99], 0\n\t"
    "ds_read_b128 v[120:123], %[da] offset:4096\n\t"
    "v_mfma_f32_16x16x32_bf16 v[68:71], v[132:135], v[96:99], 0\n\t"
    "ds_read_b128 v[124:127], %[da] offset:6144\n\t"
    "v_mfma_f32_16x16x32_bf16 v[72:75], v[128:131], v[100:103], 0\n\t"
    "v_mfma_f32_16x16x32_bf16 v[76:79], v[132:135], v[100:103], 0\n\t"
    "s_cmp_eq_u32 %[w0], 0\n\t"
    "s_cbranch_scc1 5f\n\t"
    "6:\n\t"
    "s_waitcnt lgkmcnt(4)\n\t"
    "v_mfma_f32_16x16x32_bf16 v[64:67], v[136:139], v[104:107], v[64:67]\n\t"
    "ds_read_b128 v[96:99], %[ea] offset:8192\n\t"
    "v_mfma_f32_16x16x32_bf16 v[68:71], v[140:143], v[104:107], v[68:71]\n\t"
    "ds_read_b128 v[100:103], %[ea] offset:10240\n\t"
    "v_mfma_f32_16x16x32_bf16 v[72:75], v[136:139], v[108:111], v[72:75]\n\t"
    "v_mfma_f32_16x16x32_bf16 v[76:79], v[140:143], v[108:111], v[76:79]\n\t"
    "s_waitcnt lgkmcnt(4)\n\t"
    "v_mfma_f32_16x16x32_bf16 v[64:67], v[144:147], v[112:115], v[64:67]\n\t"
    "ds_read_b128 v[104:107], %[da] offset:8192\n\t"
    "v_mfma_f32_16x16x32_bf16 v[68:71], v[148:151], v[112:115], v[68:71]\n\t"
    "ds_read_b128 v[108:111], %[da] offset:10240\n\t"
    "v_mfma_f32_16x16x32_bf16 v[72:75], v[144:147], v[116:119], v[72:75]\n\t"
    "v_mfma_f32_16x16x32_bf16 v[76:79], v[148:151], v[116:119], v[76:79]\n\t"
    "s_barrier\n\t"
    "s_waitcnt lgkmcnt(4)\n\t"
    "v_mfma_f32_16x16x32_bf16 v[64:67], v[152:155], v[120:123], v[64:67]\n\t"
    "ds_read_b128 v[112:115], %[ea] offset:12288\n\t"
    "v_mfma_f32_16x16x32_bf16 v[68:71], v[156:159], v[120:123], v[68:71]\n\t"
    "ds_read_b128 v[116:119], %[ea] offset:14336\n\t"
    "v_mfma_f32_16x16x32_bf16 v[72:75], v[152:155], v[124:127], v[72:75]\n\t"
    "ds_read_b128 v[48:51], %[rw] offset:0\n\t"
    "v_mfma_f32_16x16x32_bf16 v[76:79], v[156:159], v[124:127], v[76:79]\n\t"
    "ds_read_b128 v[52:55], %[rw] offset:1024\n\t"
    "s_waitcnt lgkmcnt(6)\n\t"
    "v_mfma_f32_16x16x32_bf16 v[64:67], v[160:163], v[96:99], v[64:67]\n\t"
    "ds_read_b128 v[120:123], %[da] offset:12288\n\t"
    "v_mfma_f32_16x16x32_bf16 v[68:71], v[164:167], v[96:99], v[68:71]\n\t"
    "ds_read_b128 v[124:127], %[da] offset:14336\n\t"
    "v_mfma_f32_16x16x32_bf16 v[72:75], v[160:163], v[100:103], v[72:75]\n\t"
    "ds_read_b128 v[56:59], %[rw] offset:2048\n\t"
    "v_mfma_f32_16x16x32_bf16 v[76:79], v[164:167], v[100:103], v[76:79]\n\t"
    "ds_read_b128 v[60:63], %[rw] offset:3072\n\t"
    "s_waitcnt lgkmcnt(0)\n\t"
    "v_mfma_f32_16x16x32_bf16 v[64:67], v[168:171], v[104:107], v[64:67]\n\t"
    "ds_read_b128 v[96:99], %[ea] offset:16384\n\t"
    "v_cvt_pk_bf16_f32 v40, v80, v81\n\t"
    "v_mfma_f32_16x16x32_bf16 v[68:71], v[172:175], v[104:107], v[68:71]\n\t"
    "ds_read_b128 v[100:103], %[ea] offset:18432\n\t"
    "v_cvt_pk_bf16_f32 v41, v82, v83\n\t"
    "v_mfma_f32_16x16x32_bf16 v[72:75], v[168:171], v[108:111], v[72:75]\n\t"
    "v_cvt_pk_bf16_f32 v42, v84, v85\n\t"
    "v_mfma_f32_16x16x32_bf16 v[76:79], v[172:175], v[108:111], v[76:79]\n\t"
    "v_cvt_pk_bf16_f32 v43, v86, v87\n\t"
    "v_mfma_f32_16x16x32_bf16 v[64:67], v[176:179], v[112:115], v[64:67]\n\t"
    "ds_read_b128 v[104:107], %[da] offset:16384\n\t"
    "global_store_dwordx4 %[co0], v[40:43], s[36:37] sc1\n\t"
    "v_mfma_f32_16x16x32_bf16 v[68:71], v[180:183], v[112:115], v[68:71]\n\t"
    "ds_read_b128 v[108:111], %[da] offset:18432\n\t"
    "v_cvt_pk_bf16_f32 v44, v88, v89\n\t"
    "v_mfma_f32_16x16x32_bf16 v[72:75], v[176:179], v[116:119], v[72:75]\n\t"
    "v_cvt_pk_bf16_f32 v45, v90, v91\n\t"
    "v_mfma_f32_16x16x32_bf16 v[76:79], v[180:183], v[116:119], v[76:79]\n\t"
    "v_cvt_pk_bf16_f32 v46, v92, v93\n\t"
    "v_mfma_f32_16x16x32_bf16 v[64:67], v[184:187], v[120:123], v[64:67]\n\t"
    "ds_read_b128 v[112:115], %[ea] offset:20480\n\t"
    "v_cvt_pk_bf16_f32 v47, v94, v95\n\t"
    "v_mfma_f32_16x16x32_bf16 v[68:71], v[188:191], v[120:123], v[68:71]\n\t"
    "ds_read_b128 v[116:119], %[ea] offset:22528\n\t"
    "global_store_dwordx4 %[co1], v[44:47], s[36:37] sc1\n\t"
    "v_mfma_f32_16x16x32_bf16 v[72:75], v[184:187], v[124:127], v[72:75]\n\t"
    "v_cvt_pk_bf16_f32 v48, v48, v49\n\t"
    "v_mfma_f32_16x16x32_bf16 v[76:79], v[188:191], v[124:127], v[76:79]\n\t"
    "v_cvt_pk_bf16_f32 v49, v50, v51\n\t"
    "s_waitcnt lgkmcnt(4)\n\t"
    "v_mfma_f32_16x16x32_bf16 v[64:67], v[192:195], v[96:99], v[64:67]\n\t"
    "ds_read_b128 v[120:123], %[da] offset:20480\n\t"
    "v_cvt_pk_bf16_f32 v50, v52, v53\n\t"
    "v_mfma_f32_16x16x32_bf16 v[68:71], v[196:199], v[96:99], v[68:71]\n\t"
    "ds_read_b128 v[124:127], %[da] offset:22528\n\t"
    "v_cvt_pk_bf16_f32 v51, v54, v55\n\t"
    "v_mfma_f32_16x16x32_bf16 v[72:75], v[192:195], v[100:103], v[72:75]\n\t"
    "global_store_dwordx4 %[co0], v[48:51], s[36:37] offset:2048 sc1\n\t"
    "v_mfma_f32_16x16x32_bf16 v[76:79], v[196:199], v[100:103], v[76:79]\n\t"
    "v_cvt_pk_bf16_f32 v56, v56, v57\n\t"
    "s_waitcnt lgkmcnt(4)\n\t"
    "v_mfma_f32_16x16x32_bf16 v[64:67], v[200:203], v[104:107], v[64:67]\n\t"
    "ds_read_b128 v[96:99], %[ea] offset:24576\n\t"
    "v_cvt_pk_bf16_f32 v57, v58, v59\n\t"
    "v_mfma_f32_16x16x32_bf16 v[68:71], v[204:207], v[104:107], v[68:71]\n\t"
    "ds_read_b128 v[100:103], %[ea] offset:26624\n\t"
    "v_cvt_pk_bf16_f32 v58, v60, v61\n\t"
    "v_mfma_f32_16x16x32_bf16 v[72:75], v[200:203], v[108:111], v[72:75]\n\t"
    "v_cvt_pk_bf16_f32 v59, v62, v63\n\t"
    "v_mfma_f32_16x16x32_bf16 v[76:79], v[204:207], v[108:111], v[76:79]\n\t"
    "global_store_dwordx4 %[co1], v[56:59], s[36:37] offset:2048 sc1\n\t"
    "s_waitcnt lgkmcnt(4)\n\t"
    "v_mfma_f32_16x16x32_bf16 v[64:67], v[208:211], v[112:115], v[64:67]\n\t"
    "ds_read_b128 v[104:107], %[da] offset:24576\n\t"
    "v_mfma_f32_16x16x32_bf16 v[68:71], v[212:215], v[112:115], v[68:71]\n\t"
    "ds_read_b128 v[108:111], %[da] offset:26624\n\t"
    "v_mfma_f32_16x16x32_bf16 v[72:75], v[208:211], v[116:119], v[72:75]\n\t"
    "v_mfma_f32_16x16x32_bf16 v[76:79], v[212:215], v[116:119], v[76:79]\n\t"
    "s_waitcnt lgkmcnt(4)\n\t"
    "v_mfma_f32_16x16x32_bf16 v[64:67], v[216:219], v[120:123], v[64:67]\n\t"
    "ds_read_b128 v[112:115], %[ea] offset:28672\n\t"
    "v_mfma_f32_16x16x32_bf16 v[68:71], v[220:223], v[120:123], v[68:71]\n\t"
    "ds_read_b128 v[116:119], %[ea] offset:30720\n\t"
    "v_mfma_f32_16x16x32_bf16 v[72:75], v[216:219], v[124:127], v[72:75]\n\t"
    "v_xor_b32 %[ea], 0x10000, %[ea]\n\t"
    "v_mfma_f32_16x16x32_bf16 v[76:79], v[220:223], v[124:127], v[76:79]\n\t"
    "s_waitcnt vmcnt(4)\n\t"
    "s_barrier\n\t"
    "s_waitcnt lgkmcnt(4)\n\t"
    "v_mfma_f32_16x16x32_bf16 v[64:67], v[224:227], v[96:99], v[64:67]\n\t"
    "ds_read_b32 v39, %[sa]\n\t"
    "ds_read_b128 v[120:123], %[da] offset:28672\n\t"
    "v_mfma_f32_16x16x32_bf16 v[68:71], v[228:231], v[96:99], v[68:71]\n\t"
    "ds_read_b128 v[124:127], %[da] offset:30720\n\t"
    "v_mfma_f32_16x16x32_bf16 v[72:75], v[224:227], v[100:103], v[72:75]\n\t"
    "v_xor_b32 %[da], 0x10000, %[da]\n\t"
    "v_mfma_f32_16x16x32_bf16 v[76:79], v[228:231], v[100:103], v[76:79]\n\t"
    "s_waitcnt lgkmcnt(5)\n\t"
    "v_mfma_f32_16x16x32_bf16 v[64:67], v[232:235], v[104:107], v[64:67]\n\t"
    "ds_read_b128 v[96:99], %[ea] offset:0\n\t"
    "v_mfma_f32_16x16x32_bf16 v[68:71], v[236:239], v[104:107], v[68:71]\n\t"
    "ds_read_b128 v[100:103], %[ea] offset:2048\n\t"
    "v_mfma_f32_16x16x32_bf16 v[72:75], v[232:235], v[108:111], v[72:75]\n\t"
    "v_mfma_f32_16x16x32_bf16 v[76:79], v[236:239], v[108:111], v[76:79]\n\t"
    "s_waitcnt lgkmcnt(4)\n\t"
    "v_mfma_f32_16x16x32_bf16 v[64:67], v[240:243], v[112:115], v[64:67]\n\t"
    "ds_read_b128 v[104:107], %[da] offset:0\n\t"
    "v_mad_u32_u24 v39, v39, %[mu], %[ad]\n\t"
    "v_mfma_f32_16x16x32_bf16 v[68:71], v[244:247], v[112:115], v[68:71]\n\t"
    "ds_read_b128 v[108:111], %[da] offset:2048\n\t"
    "v_cmp_ge_i32 vcc, %[lf], v39\n\t"
    "s_cmp_eq_u64 vcc, 0\n\t"
    "s_addc_u32 %[lf], %[lf], 0\n\t"
    "v_mfma_f32_16x16x32_bf16 v[72:75], v[240:243], v[116:119], v[72:75]\n\t"
    "v_mfma_f32_16x16x32_bf16 v[76:79], v[244:247], v[116:119], v[76:79]\n\t"
    "v_cmp_ge_i32 vcc, %[lf], v39\n\t"
    "s_cmp_eq_u64 vcc, 0\n\t"
    "s_addc_u32 %[lf], %[lf], 0\n\t"
    "s_waitcnt lgkmcnt(4)\n\t"
    "v_mfma_f32_16x16x32_bf16 v[80:83], v[248:251], v[120:123], v[64:67]\n\t"
    "ds_read_b128 v[112:115], %[ea] offset:4096\n\t"
    "s_min_i32 %[lf], %[lf], %[cp]\n\t"
    "s_add_u32 %[b], %[b], 1\n\t"
    "v_mfma_f32_16x16x32_bf16 v[84:87], v[252:255], v[120:123], v[68:71]\n\t"
    "ds_read_b128 v[116:119], %[ea] offset:6144\n\t"
    "v_mfma_f32_16x16x32_bf16 v[88:91], v[248:251], v[124:127], v[72:75]\n\t"
    "v_mfma_f32_16x16x32_bf16 v[92:95], v[252:255], v[124:127], v[76:79]\n\t"
    "s_mov_b64 s[36:37], s[38:39]\n\t"
    "s_add_u32 s38, s38, %[ab]\n\t"
    "s_addc_u32 s39, s39, 0\n\t"
    "s_cmp_lt_i32 %[b], %[lf]\n\t"
    "s_cbranch_scc1 1b\n\t"
    "s_waitcnt lgkmcnt(0)\n\t"
    "s_nop 4\n\t"
    "s_branch 9f\n\t"
    "5:\n\t"
    "s_add_u32 %[st], %[b], %[po]\n\t"
    "s_max_i32 %[st], %[st], %[p0]\n\t"
    "v_mov_b32 v39, %[st]\n\t"
    "v_mov_b32 v38, 0\n\t"
    "s_mov_b64 exec, 1\n\t"
    "global_store_dword v38, v39, %[pg] sc1\n\t"
    "s_mov_b64 exec, -1\n\t"
    "s_mov_b32 %[p0], %[p1]\n\t"
    "s_cmp_ge_i32 %[lf], %[cp]\n\t"
    "s_cbranch_scc1 6b\n\t"
    "s_mov_b32 m0, %[sl]\n\t"
    "s_nop 0\n\t"
    "global_load_lds_dword %[dv], %[db] sc1\n\t"
    "s_branch 6b\n\t"
    "9:"
    : "=&{v[80:95]}"(pd), [ea] "+&v"(ea), [da] "+&v"(da), [b] "+&s"(b), [lf] "+&s"(lf), "+{s[36:37]}"(p1), "+{s[38:39]}"(p0), [sa] "+&v"(sa), [st] "=&s"(st), [sl] "+&s"(sl), [p0] "+&s"(pf)
    : [rw] "v"(rw), [ab] "s"(ab), [cp] "s"(cp), [mu] "s"(mu), [ad] "v"(ad), "{v[128:159]}"(W[0]), "{v[160:191]}"(W[1]), "{v[192:223]}"(W[2]), "{v[224:255]}"(W[3]), [co0] "v"(co0), [w0] "s"(w0), [dv] "v"(dv), [db] "s"(db), [pg] "s"(pg), [po] "s"(po - 1), [p1] "s"(b + po), [co1] "v"(co1)
    : "v38", "v39", "v40", "v41", "v42", "v43", "v44", "v45", "v46", "v47", "v48", "v49", "v50", "v51", "v52", "v53", "v54", "v55", "v56", "v57", "v58", "v59", "v60", "v61", "v62", "v63", "v64", "v65", "v66", "v67", "v68", "v69", "v70", "v71", "v72", "v73", "v74", "v75", "v76", "v77", "v78", "v79", "v96", "v97", "v98", "v99", "v100", "v101", "v102", "v103", "v104", "v105", "v106", "v107", "v108", "v109", "v110", "v111", "v112", "v113", "v114", "v115", "v116", "v117", "v118", "v119", "v120", "v121", "v122", "v123", "v124", "v125", "v126", "v127", "vcc", "scc", "m0", "memory");
}
__device__ __forceinline__ void lean_ld0(const u32x32 (&W)[4], u32x16& pd, unsigned ea, unsigned da, unsigned x, unsigned lb, const char* s0, const char* s1, const unsigned (&av)[8], unsigned sa, unsigned mu, unsigned ad, int th, unsigned s2, unsigned dv, const char* db) {
  asm volatile("s_setprio 2\n\t"
    "ds_read_b128 v[96:99], %[ea] offset:0\n\t"
    "ds_read_b128 v[100:103], %[ea] offset:2048\n\t"
    "ds_read_b128 v[104:107], %[da] offset:0\n\t"
    "ds_read_b128 v[108:111], %[da] offset:2048\n\t"
    "ds_read_b128 v[112:115], %[ea] offset:4096\n\t"
    "ds_read_b128 v[116:119], %[ea] offset:6144\n\t"
    "s_waitcnt lgkmcnt(4)\n\t"
    "v_mfma_f32_16x16x32_bf16 v[80:83], v[128:131], v[96:99], 0\n\t"
    "ds_read_b128 v[120:123], %[da] offset:4096\n\t"
    "v_mfma_f32_16x16x32_bf16 v[84:87], v[132:135], v[96:99], 0\n\t"
    "ds_read_b128 v[124:127], %[da] offset:6144\n\t"
    "v_mfma_f32_16x16x32_bf16 v[88:91], v[128:131], v[100:103], 0\n\t"
    "v_mfma_f32_16x16x32_bf16 v[92:95], v[132:135], v[100:103], 0\n\t"
    "s_waitcnt vmcnt(24)\n\t"
    "s_cmp_lt_u32 %[x], 2\n\t"
    "s_cbranch_scc0 41f\n\t"
    "s_waitcnt vmcnt(8)\n\t"
    "41:\n\t"
    "s_barrier\n\t"
    "s_waitcnt lgkmcnt(4)\n\t"
    "v_mfma_f32_16x16x32_bf16 v[80:83], v[136:139], v[104:107], v[80:83]\n\t"
    "ds_read_b128 v[96:99], %[ea] offset:8192\n\t"
    "v_mfma_f32_16x16x32_bf16 v[84:87], v[140:143], v[104:107], v[84:87]\n\t"
    "ds_read_b128 v[100:103], %[ea] offset:10240\n\t"
    "v_mfma_f32_16x16x32_bf16 v[88:91], v[136:139], v[108:111], v[88:91]\n\t"
    "v_mfma_f32_16x16x32_bf16 v[92:95], v[140:143], v[108:111], v[92:95]\n\t"
    "s_waitcnt lgkmcnt(4)\n\t"
    "v_mfma_f32_16x16x32_bf16 v[80:83], v[144:147], v[112:115], v[80:83]\n\t"
    "ds_read_b128 v[104:107], %[da] offset:8192\n\t"
    "v_mfma_f32_16x16x32_bf16 v[84:87], v[148:151], v[112:115], v[84:87]\n\t"
    "ds_read_b128 v[108:111], %[da] offset:10240\n\t"
    "v_mfma_f32_16x16x32_bf16 v[88:91], v[144:147], v[116:119], v[88:91]\n\t"
    "v_mfma_f32_16x16x32_bf16 v[92:95], v[148:151], v[116:119], v[92:95]\n\t"
    "s_waitcnt lgkmcnt(4)\n\t"
    "v_mfma_f32_16x16x32_bf16 v[80:83], v[152:155], v[120:123], v[80:83]\n\t"
    "ds_read_b128 v[112:115], %[ea] offset:12288\n\t"
    "v_mfma_f32_16x16x32_bf16 v[84:87], v[156:159], v[120:123], v[84:87]\n\t"
    "ds_read_b128 v[116:119], %[ea] offset:14336\n\t"
    "v_mfma_f32_16x16x32_bf16 v[88:91], v[152:155], v[124:127], v[88:91]\n\t"
    "v_mfma_f32_16x16x32_bf16 v[92:95], v[156:159], v[124:127], v[92:95]\n\t"
    "s_waitcnt lgkmcnt(4)\n\t"
    "v_mfma_f32_16x16x32_bf16 v[80:83], v[160:163], v[96:99], v[80:83]\n\t"
    "ds_read_b128 v[120:123], %[da] offset:12288\n\t"
    "v_mfma_f32_16x16x32_bf16 v[84:87], v[164:167], v[96:99], v[84:87]\n\t"
    "ds_read_b128 v[124:127], %[da] offset:14336\n\t"
    "v_mfma_f32_16x16x32_bf16 v[88:91], v[160:163], v[100:103], v[88:91]\n\t"
    "v_mfma_f32_16x16x32_bf16 v[92:95], v[164:167], v[100:103], v[92:95]\n\t"
    "s_waitcnt vmcnt(20)\n\t"
    "s_cmp_lt_u32 %[x], 2\n\t"
    "s_cbranch_scc0 45f\n\t"
    "s_waitcnt vmcnt(4)\n\t"
    "45:\n\t"
    "s_barrier\n\t"
    "s_waitcnt lgkmcnt(4)\n\t"
    "v_mfma_f32_16x16x32_bf16 v[80:83], v[168:171], v[104:107], v[80:83]\n\t"
    "ds_read_b128 v[96:99], %[ea] offset:16384\n\t"
    "v_mfma_f32_16x16x32_bf16 v[84:87], v[172:175], v[104:107], v[84:87]\n\t"
    "ds_read_b128 v[100:103], %[ea] offset:18432\n\t"
    "v_mfma_f32_16x16x32_bf16 v[88:91], v[168:171], v[108:111], v[88:91]\n\t"
    "v_mfma_f32_16x16x32_bf16 v[92:95], v[172:175], v[108:111], v[92:95]\n\t"
    "s_waitcnt lgkmcnt(4)\n\t"
    "v_mfma_f32_16x16x32_bf16 v[80:83], v[176:179], v[112:115], v[80:83]\n\t"
    "ds_read_b128 v[104:107], %[da] offset:16384\n\t"
    "v_mfma_f32_16x16x32_bf16 v[84:87], v[180:183], v[112:115], v[84:87]\n\t"
    "ds_read_b128 v[108:111], %[da] offset:18432\n\t"
    "v_mfma_f32_16x16x32_bf16 v[88:91], v[176:179], v[116:119], v[88:91]\n\t"
    "v_mfma_f32_16x16x32_bf16 v[92:95], v[180:183], v[116:119], v[92:95]\n\t"
    "s_waitcnt lgkmcnt(4)\n\t"
    "v_mfma_f32_16x16x32_bf16 v[80:83], v[184:187], v[120:123], v[80:83]\n\t"
    "ds_read_b128 v[112:115], %[ea] offset:20480\n\t"
    "v_mfma_f32_16x16x32_bf16 v[84:87], v[188:191], v[120:123], v[84:87]\n\t"
    "ds_read_b128 v[116:119], %[ea] offset:22528\n\t"
    "v_mfma_f32_16x16x32_bf16 v[88:91], v[184:187], v[124:127], v[88:91]\n\t"
    "v_mfma_f32_16x16x32_bf16 v[92:95], v[188:191], v[124:127], v[92:95]\n\t"
    "s_waitcnt lgkmcnt(4)\n\t"
    "v_mfma_f32_16x16x32_bf16 v[80:83], v[192:195], v[96:99], v[80:83]\n\t"
    "ds_read_b128 v[120:123], %[da] offset:20480\n\t"
    "v_mfma_f32_16x16x32_bf16 v[84:87], v[196:199], v[96:99], v[84:87]\n\t"
    "ds_read_b128 v[124:127], %[da] offset:22528\n\t"
    "v_mfma_f32_16x16x32_bf16 v[88:91], v[192:195], v[100:103], v[88:91]\n\t"
    "v_mfma_f32_16x16x32_bf16 v[92:95], v[196:199], v[100:103], v[92:95]\n\t"
    "s_waitcnt vmcnt(16)\n\t"
    "s_cmp_lt_u32 %[x], 2\n\t"
    "s_cbranch_scc0 49f\n\t"
    "s_waitcnt vmcnt(0)\n\t"
    "49:\n\t"
    "s_barrier\n\t"
    "ds_read_b32 v79, %[sa]\n\t"
    "s_waitcnt lgkmcnt(5)\n\t"
    "v_mfma_f32_16x16x32_bf16 v[80:83], v[200:203], v[104:107], v[80:83]\n\t"
    "ds_read_b128 v[96:99], %[ea] offset:24576\n\t"
    "v_mfma_f32_16x16x32_bf16 v[84:87], v[204:207], v[104:107], v[84:87]\n\t"
    "ds_read_b128 v[100:103], %[ea] offset:26624\n\t"
    "v_mfma_f32_16x16x32_bf16 v[88:91], v[200:203], v[108:111], v[88:91]\n\t"
    "v_mfma_f32_16x16x32_bf16 v[92:95], v[204:207], v[108:111], v[92:95]\n\t"
    "s_waitcnt lgkmcnt(5)\n\t"
    "v_mfma_f32_16x16x32_bf16 v[80:83], v[208:211], v[112:115], v[80:83]\n\t"
    "ds_read_b128 v[104:107], %[da] offset:24576\n\t"
    "v_mfma_f32_16x16x32_bf16 v[84:87], v[212:215], v[112:115], v[84:87]\n\t"
    "ds_read_b128 v[108:111], %[da] offset:26624\n\t"
    "v_mfma_f32_16x16x32_bf16 v[88:91], v[208:211], v[116:119], v[88:91]\n\t"
    "v_mfma_f32_16x16x32_bf16 v[92:95], v[212:215], v[116:119], v[92:95]\n\t"
    "s_cmp_eq_u32 %[s2], 0\n\t"
    "s_cbranch_scc1 7f\n\t"
    "s_mov_b32 m0, %[s2]\n\t"
    "s_nop 0\n\t"
    "global_load_lds_dword %[dv], %[db] sc1\n\t"
    "7:\n\t"
    "s_waitcnt lgkmcnt(4)\n\t"
    "v_mfma_f32_16x16x32_bf16 v[80:83], v[216:219], v[120:123], v[80:83]\n\t"
    "ds_read_b128 v[112:115], %[ea] offset:28672\n\t"
    "v_mfma_f32_16x16x32_bf16 v[84:87], v[220:223], v[120:123], v[84:87]\n\t"
    "ds_read_b128 v[116:119], %[ea] offset:30720\n\t"
    "v_mfma_f32_16x16x32_bf16 v[88:91], v[216:219], v[124:127], v[88:91]\n\t"
    "v_mad_u32_u24 v79, v79, %[mu], %[ad]\n\t"
    "v_cmp_ge_i32 vcc, %[th], v79\n\t"
    "v_mfma_f32_16x16x32_bf16 v[92:95], v[220:223], v[124:127], v[92:95]\n\t"
    "s_cbranch_vccz 5f\n\t"
    "s_waitcnt lgkmcnt(4)\n\t"
    "v_mfma_f32_16x16x32_bf16 v[80:83], v[224:227], v[96:99], v[80:83]\n\t"
    "ds_read_b128 v[120:123], %[da] offset:28672\n\t"
    "v_mfma_f32_16x16x32_bf16 v[84:87], v[228:231], v[96:99], v[84:87]\n\t"
    "ds_read_b128 v[124:127], %[da] offset:30720\n\t"
    "v_mfma_f32_16x16x32_bf16 v[88:91], v[224:227], v[100:103], v[88:91]\n\t"
    "v_mfma_f32_16x16x32_bf16 v[92:95], v[228:231], v[100:103], v[92:95]\n\t"
    "s_waitcnt lgkmcnt(4)\n\t"
    "v_mfma_f32_16x16x32_bf16 v[80:83], v[232:235], v[104:107], v[80:83]\n\t"
    "v_mfma_f32_16x16x32_bf16 v[84:87], v[236:239], v[104:107], v[84:87]\n\t"
    "v_mfma_f32_16x16x32_bf16 v[88:91], v[232:235], v[108:111], v[88:91]\n\t"
    "v_mfma_f32_16x16x32_bf16 v[92:95], v[236:239], v[108:111], v[92:95]\n\t"
    "s_waitcnt lgkmcnt(2)\n\t"
    "v_mfma_f32_16x16x32_bf16 v[80:83], v[240:243], v[112:115], v[80:83]\n\t"
    "v_mfma_f32_16x16x32_bf16 v[84:87], v[244:247], v[112:115], v[84:87]\n\t"
    "v_mfma_f32_16x16x32_bf16 v[88:91], v[240:243], v[116:119], v[88:91]\n\t"
    "v_mfma_f32_16x16x32_bf16 v[92:95], v[244:247], v[116:119], v[92:95]\n\t"
    "s_waitcnt lgkmcnt(0)\n\t"
    "v_mfma_f32_16x16x32_bf16 v[80:83], v[248:251], v[120:123], v[80:83]\n\t"
    "v_mfma_f32_16x16x32_bf16 v[84:87], v[252:255], v[120:123], v[84:87]\n\t"
    "v_mfma_f32_16x16x32_bf16 v[88:91], v[248:251], v[124:127], v[88:91]\n\t"
    "v_mfma_f32_16x16x32_bf16 v[92:95], v[252:255], v[124:127], v[92:95]\n\t"
    "s_branch 6f\n\t"
    "5:\n\t"
    "s_waitcnt lgkmcnt(4)\n\t"
    "v_mfma_f32_16x16x32_bf16 v[80:83], v[224:227], v[96:99], v[80:83]\n\t"
    "ds_read_b128 v[120:123], %[da] offset:28672\n\t"
    "s_add_u32 m0, %[lb], 0\n\t"
    "s_nop 0\n\t"
    "global_load_lds_dwordx4 %[av0], %[s0] sc1\n\t"
    "v_mfma_f32_16x16x32_bf16 v[84:87], v[228:231], v[96:99], v[84:87]\n\t"
    "ds_read_b128 v[124:127], %[da] offset:30720\n\t"
    "s_add_u32 m0, %[lb], 32768\n\t"
    "s_nop 0\n\t"
    "global_load_lds_dwordx4 %[av1], %[s0] sc1\n\t"
    "v_mfma_f32_16x16x32_bf16 v[88:91], v[224:227], v[100:103], v[88:91]\n\t"
    "s_add_u32 m0, %[lb], 4096\n\t"
    "s_nop 0\n\t"
    "global_load_lds_dwordx4 %[av2], %[s0] sc1\n\t"
    "v_mfma_f32_16x16x32_bf16 v[92:95], v[228:231], v[100:103], v[92:95]\n\t"
    "s_add_u32 m0, %[lb], 36864\n\t"
    "s_nop 0\n\t"
    "global_load_lds_dwordx4 %[av3], %[s0] sc1\n\t"
    "s_waitcnt lgkmcnt(4)\n\t"
    "v_mfma_f32_16x16x32_bf16 v[80:83], v[232:235], v[104:107], v[80:83]\n\t"
    "s_add_u32 m0, %[lb], 8192\n\t"
    "s_nop 0\n\t"
    "global_load_lds_dwordx4 %[av4], %[s0] sc1\n\t"
    "v_mfma_f32_16x16x32_bf16 v[84:87], v[236:239], v[104:107], v[84:87]\n\t"
    "s_add_u32 m0, %[lb], 40960\n\t"
    "s_nop 0\n\t"
    "global_load_lds_dwordx4 %[av5], %[s0] sc1\n\t"
    "v_mfma_f32_16x16x32_bf16 v[88:91], v[232:235], v[108:111], v[88:91]\n\t"
    "s_add_u32 m0, %[lb], 12288\n\t"
    "s_nop 0\n\t"
    "global_load_lds_dwordx4 %[av6], %[s0] sc1\n\t"
    "v_mfma_f32_16x16x32_bf16 v[92:95], v[236:239], v[108:111], v[92:95]\n\t"
    "s_add_u32 m0, %[lb], 45056\n\t"
    "s_nop 0\n\t"
    "global_load_lds_dwordx4 %[av7], %[s0] sc1\n\t"
    "s_waitcnt lgkmcnt(2)\n\t"
    "v_mfma_f32_16x16x32_bf16 v[80:83], v[240:243], v[112:115], v[80:83]\n\t"
    "s_add_u32 m0, %[lb], 16384\n\t"
    "s_nop 0\n\t"
    "global_load_lds_dwordx4 %[av0], %[s1] sc1\n\t"
    "v_mfma_f32_16x16x32_bf16 v[84:87], v[244:247], v[112:115], v[84:87]\n\t"
    "s_add_u32 m0, %[lb], 49152\n\t"
    "s_nop 0\n\t"
    "global_load_lds_dwordx4 %[av1], %[s1] sc1\n\t"
    "v_mfma_f32_16x16x32_bf16 v[88:91], v[240:243], v[116:119], v[88:91]\n\t"
    "s_add_u32 m0, %[lb], 20480\n\t"
    "s_nop 0\n\t"
    "global_load_lds_dwordx4 %[av2], %[s1] sc1\n\t"
    "v_mfma_f32_16x16x32_bf16 v[92:95], v[244:247], v[116:119], v[92:95]\n\t"
    "s_add_u32 m0, %[lb], 53248\n\t"
    "s_nop 0\n\t"
    "global_load_lds_dwordx4 %[av3], %[s1] sc1\n\t"
    "s_waitcnt lgkmcnt(0)\n\t"
    "v_mfma_f32_16x16x32_bf16 v[80:83], v[248:251], v[120:123], v[80:83]\n\t"
    "s_add_u32 m0, %[lb], 24576\n\t"
    "s_nop 0\n\t"
    "global_load_lds_dwordx4 %[av4], %[s1] sc1\n\t"
    "v_mfma_f32_16x16x32_bf16 v[84:87], v[252:255], v[120:123], v[84:87]\n\t"
    "s_add_u32 m0, %[lb], 57344\n\t"
    "s_nop 0\n\t"
    "global_load_lds_dwordx4 %[av5], %[s1] sc1\n\t"
    "v_mfma_f32_16x16x32_bf16 v[88:91], v[248:251], v[124:127], v[88:91]\n\t"
    "s_add_u32 m0, %[lb], 28672\n\t"
    "s_nop 0\n\t"
    "global_load_lds_dwordx4 %[av6], %[s1] sc1\n\t"
    "v_mfma_f32_16x16x32_bf16 v[92:95], v[252:255], v[124:127], v[92:95]\n\t"
    "s_add_u32 m0, %[lb], 61440\n\t"
    "s_nop 0\n\t"
    "global_load_lds_dwordx4 %[av7], %[s1] sc1\n\t"
    "6:\n\t"
    "s_nop 6\n\t"
    "s_branch 9f\n\t"
    "3:\n\t"
    "9:"
    : "=&{v[80:95]}"(pd)
    : [ea] "v"(ea), [da] "v"(da), [x] "s"(x), "{v[128:159]}"(W[0]), "{v[160:191]}"(W[1]), "{v[192:223]}"(W[2]), "{v[224:255]}"(W[3]), [lb] "s"(lb), [s0] "s"(s0), [s1] "s"(s1), [av0] "v"(av[0]), [av1] "v"(av[1]), [av2] "v"(av[2]), [av3] "v"(av[3]), [av4] "v"(av[4]), [av5] "v"(av[5]), [av6] "v"(av[6]), [av7] "v"(av[7]), [sa] "v"(sa), [mu] "s"(mu), [ad] "v"(ad), [th] "s"(th), [s2] "s"(s2), [dv] "v"(dv), [db] "s"(db)
    : "v96", "v97", "v98", "v99", "v100", "v101", "v102", "v103", "v104", "v105", "v106", "v107", "v108", "v109", "v110", "v111", "v112", "v113", "v114", "v115", "v116", "v117", "v118", "v119", "v120", "v121", "v122", "v123", "v124", "v125", "v126", "v127", "v79", "vcc", "scc", "m0", "memory");
}
__device__ __forceinline__ void lean_dm0(const u32x32 (&W)[4], u32x16& pd, unsigned ea, unsigned da, unsigned x, unsigned lb, const char* s0, const char* s1, const unsigned (&av)[8]) {
  asm volatile("s_setprio 2\n\t"
    "ds_read_b128 v[96:99], %[ea] offset:0\n\t"
    "ds_read_b128 v[100:103], %[ea] offset:2048\n\t"
    "ds_read_b128 v[104:107], %[da] offset:0\n\t"
    "ds_read_b128 v[108:111], %[da] offset:2048\n\t"
    "ds_read_b128 v[112:115], %[ea] offset:4096\n\t"
    "ds_read_b128 v[116:119], %[ea] offset:6144\n\t"
    "s_waitcnt lgkmcnt(4)\n\t"
    "v_mfma_f32_16x16x32_bf16 v[80:83], v[128:131], v[96:99], 0\n\t"
    "ds_read_b128 v[120:123], %[da] offset:4096\n\t"
    "s_add_u32 m0, %[lb], 0\n\t"
    "v_mfma_f32_16x16x32_bf16 v[84:87], v[132:135], v[96:99], 0\n\t"
    "ds_read_b128 v[124:127], %[da] offset:6144\n\t"
    "global_load_lds_dwordx4 %[av0], %[s0] sc1\n\t"
    "v_mfma_f32_16x16x32_bf16 v[88:91], v[128:131], v[100:103], 0\n\t"
    "s_add_u32 m0, %[lb], 32768\n\t"
    "v_mfma_f32_16x16x32_bf16 v[92:95], v[132:135], v[100:103], 0\n\t"
    "global_load_lds_dwordx4 %[av1], %[s0] sc1\n\t"
    "s_waitcnt vmcnt(10)\n\t"
    "s_barrier\n\t"
    "s_waitcnt lgkmcnt(4)\n\t"
    "v_mfma_f32_16x16x32_bf16 v[80:83], v[136:139], v[104:107], v[80:83]\n\t"
    "ds_read_b128 v[96:99], %[ea] offset:8192\n\t"
    "s_add_u32 m0, %[lb], 4096\n\t"
    "v_mfma_f32_16x16x32_bf16 v[84:87], v[140:143], v[104:107], v[84:87]\n\t"
    "ds_read_b128 v[100:103], %[ea] offset:10240\n\t"
    "global_load_lds_dwordx4 %[av2], %[s0] sc1\n\t"
    "v_mfma_f32_16x16x32_bf16 v[88:91], v[136:139], v[108:111], v[88:91]\n\t"
    "s_add_u32 m0, %[lb], 36864\n\t"
    "v_mfma_f32_16x16x32_bf16 v[92:95], v[140:143], v[108:111], v[92:95]\n\t"
    "global_load_lds_dwordx4 %[av3], %[s0] sc1\n\t"
    "s_waitcnt lgkmcnt(4)\n\t"
    "v_mfma_f32_16x16x32_bf16 v[80:83], v[144:147], v[112:115], v[80:83]\n\t"
    "ds_read_b128 v[104:107], %[da] offset:8192\n\t"
    "s_add_u32 m0, %[lb], 8192\n\t"
    "v_mfma_f32_16x16x32_bf16 v[84:87], v[148:151], v[112:115], v[84:87]\n\t"
    "ds_read_b128 v[108:111], %[da] offset:10240\n\t"
    "global_load_lds_dwordx4 %[av4], %[s0] sc1\n\t"
    "v_mfma_f32_16x16x32_bf16 v[88:91], v[144:147], v[116:119], v[88:91]\n\t"
    "s_add_u32 m0, %[lb], 40960\n\t"
    "v_mfma_f32_16x16x32_bf16 v[92:95], v[148:151], v[116:119], v[92:95]\n\t"
    "global_load_lds_dwordx4 %[av5], %[s0] sc1\n\t"
    "s_waitcnt lgkmcnt(4)\n\t"
    "v_mfma_f32_16x16x32_bf16 v[80:83], v[152:155], v[120:123], v[80:83]\n\t"
    "ds_read_b128 v[112:115], %[ea] offset:12288\n\t"
    "s_add_u32 m0, %[lb], 12288\n\t"
    "v_mfma_f32_16x16x32_bf16 v[84:87], v[156:159], v[120:123], v[84:87]\n\t"
    "ds_read_b128 v[116:119], %[ea] offset:14336\n\t"
    "global_load_lds_dwordx4 %[av6], %[s0] sc1\n\t"
    "v_mfma_f32_16x16x32_bf16 v[88:91], v[152:155], v[124:127], v[88:91]\n\t"
    "s_add_u32 m0, %[lb], 45056\n\t"
    "v_mfma_f32_16x16x32_bf16 v[92:95], v[156:159], v[124:127], v[92:95]\n\t"
    "global_load_lds_dwordx4 %[av7], %[s0] sc1\n\t"
    "s_waitcnt lgkmcnt(4)\n\t"
    "v_mfma_f32_16x16x32_bf16 v[80:83], v[160:163], v[96:99], v[80:83]\n\t"
    "ds_read_b128 v[120:123], %[da] offset:12288\n\t"
    "s_add_u32 m0, %[lb], 16384\n\t"
    "v_mfma_f32_16x16x32_bf16 v[84:87], v[164:167], v[96:99], v[84:87]\n\t"
    "ds_read_b128 v[124:127], %[da] offset:14336\n\t"
    "global_load_lds_dwordx4 %[av0], %[s1] sc1\n\t"
    "v_mfma_f32_16x16x32_bf16 v[88:91], v[160:163], v[100:103], v[88:91]\n\t"
    "s_add_u32 m0, %[lb], 49152\n\t"
    "v_mfma_f32_16x16x32_bf16 v[92:95], v[164:167], v[100:103], v[92:95]\n\t"
    "global_load_lds_dwordx4 %[av1], %[s1] sc1\n\t"
    "s_waitcnt vmcnt(14)\n\t"
    "s_barrier\n\t"
    "s_waitcnt lgkmcnt(4)\n\t"
    "v_mfma_f32_16x16x32_bf16 v[80:83], v[168:171], v[104:107], v[80:83]\n\t"
    "ds_read_b128 v[96:99], %[ea] offset:16384\n\t"
    "s_add_u32 m0, %[lb], 20480\n\t"
    "v_mfma_f32_16x16x32_bf16 v[84:87], v[172:175], v[104:107], v[84:87]\n\t"
    "ds_read_b128 v[100:103], %[ea] offset:18432\n\t"
    "global_load_lds_dwordx4 %[av2], %[s1] sc1\n\t"
    "v_mfma_f32_16x16x32_bf16 v[88:91], v[168:171], v[108:111], v[88:91]\n\t"
    "s_add_u32 m0, %[lb], 53248\n\t"
    "v_mfma_f32_16x16x32_bf16 v[92:95], v[172:175], v[108:111], v[92:95]\n\t"
    "global_load_lds_dwordx4 %[av3], %[s1] sc1\n\t"
    "s_waitcnt lgkmcnt(4)\n\t"
    "v_mfma_f32_16x16x32_bf16 v[80:83], v[176:179], v[112:115], v[80:83]\n\t"
    "ds_read_b128 v[104:107], %[da] offset:16384\n\t"
    "s_add_u32 m0, %[lb], 24576\n\t"
    "v_mfma_f32_16x16x32_bf16 v[84:87], v[180:183], v[112:115], v[84:87]\n\t"
    "ds_read_b128 v[108:111], %[da] offset:18432\n\t"
    "global_load_lds_dwordx4 %[av4], %[s1] sc1\n\t"
    "v_mfma_f32_16x16x32_bf16 v[88:91], v[176:179], v[116:119], v[88:91]\n\t"
    "s_add_u32 m0, %[lb], 57344\n\t"
    "v_mfma_f32_16x16x32_bf16 v[92:95], v[180:183], v[116:119], v[92:95]\n\t"
    "global_load_lds_dwordx4 %[av5], %[s1] sc1\n\t"
    "s_waitcnt lgkmcnt(4)\n\t"
    "v_mfma_f32_16x16x32_bf16 v[80:83], v[184:187], v[120:123], v[80:83]\n\t"
    "ds_read_b128 v[112:115], %[ea] offset:20480\n\t"
    "s_add_u32 m0, %[lb], 28672\n\t"
    "v_mfma_f32_16x16x32_bf16 v[84:87], v[188:191], v[120:123], v[84:87]\n\t"
    "ds_read_b128 v[116:119], %[ea] offset:22528\n\t"
    "global_load_lds_dwordx4 %[av6], %[s1] sc1\n\t"
    "v_mfma_f32_16x16x32_bf16 v[88:91], v[184:187], v[124:127], v[88:91]\n\t"
    "s_add_u32 m0, %[lb], 61440\n\t"
    "v_mfma_f32_16x16x32_bf16 v[92:95], v[188:191], v[124:127], v[92:95]\n\t"
    "global_load_lds_dwordx4 %[av7], %[s1] sc1\n\t"
    "s_waitcnt lgkmcnt(4)\n\t"
    "v_mfma_f32_16x16x32_bf16 v[80:83], v[192:195], v[96:99], v[80:83]\n\t"
    "ds_read_b128 v[120:123], %[da] offset:20480\n\t"
    "v_mfma_f32_16x16x32_bf16 v[84:87], v[196:199], v[96:99], v[84:87]\n\t"
    "ds_read_b128 v[124:127], %[da] offset:22528\n\t"
    "v_mfma_f32_16x16x32_bf16 v[88:91], v[192:195], v[100:103], v[88:91]\n\t"
    "v_mfma_f32_16x16x32_bf16 v[92:95], v[196:199], v[100:103], v[92:95]\n\t"
    "s_waitcnt vmcnt(16)\n\t"
    "s_barrier\n\t"
    "s_waitcnt lgkmcnt(4)\n\t"
    "v_mfma_f32_16x16x32_bf16 v[80:83], v[200:203], v[104:107], v[80:83]\n\t"
    "ds_read_b128 v[96:99], %[ea] offset:24576\n\t"
    "v_mfma_f32_16x16x32_bf16 v[84:87], v[204:207], v[104:107], v[84:87]\n\t"
    "ds_read_b128 v[100:103], %[ea] offset:26624\n\t"
    "v_mfma_f32_16x16x32_bf16 v[88:91], v[200:203], v[108:111], v[88:91]\n\t"
    "v_mfma_f32_16x16x32_bf16 v[92:95], v[204:207], v[108:111], v[92:95]\n\t"
    "s_waitcnt lgkmcnt(4)\n\t"
    "v_mfma_f32_16x16x32_bf16 v[80:83], v[208:211], v[112:115], v[80:83]\n\t"
    "ds_read_b128 v[104:107], %[da] offset:24576\n\t"
    "v_mfma_f32_16x16x32_bf16 v[84:87], v[212:215], v[112:115], v[84:87]\n\t"
    "ds_read_b128 v[108:111], %[da] offset:26624\n\t"
    "v_mfma_f32_16x16x32_bf16 v[88:91], v[208:211], v[116:119], v[88:91]\n\t"
    "v_mfma_f32_16x16x32_bf16 v[92:95], v[212:215], v[116:119], v[92:95]\n\t"
    "s_waitcnt lgkmcnt(4)\n\t"
    "v_mfma_f32_16x16x32_bf16 v[80:83], v[216:219], v[120:123], v[80:83]\n\t"
    "ds_read_b128 v[112:115], %[ea] offset:28672\n\t"
    "v_mfma_f32_16x16x32_bf16 v[84:87], v[220:223], v[120:123], v[84:87]\n\t"
    "ds_read_b128 v[116:119], %[ea] offset:30720\n\t"
    "v_mfma_f32_16x16x32_bf16 v[88:91], v[216:219], v[124:127], v[88:91]\n\t"
    "v_mfma_f32_16x16x32_bf16 v[92:95], v[220:223], v[124:127], v[92:95]\n\t"
    "s_waitcnt lgkmcnt(4)\n\t"
    "v_mfma_f32_16x16x32_bf16 v[80:83], v[224:227], v[96:99], v[80:83]\n\t"
    "ds_read_b128 v[120:123], %[da] offset:28672\n\t"
    "v_mfma_f32_16x16x32_bf16 v[84:87], v[228:231], v[96:99], v[84:87]\n\t"
    "ds_read_b128 v[124:127], %[da] offset:30720\n\t"
    "v_mfma_f32_16x16x32_bf16 v[88:91], v[224:227], v[100:103], v[88:91]\n\t"
    "v_mfma_f32_16x16x32_bf16 v[92:95], v[228:231], v[100:103], v[92:95]\n\t"
    "s_waitcnt lgkmcnt(4)\n\t"
    "v_mfma_f32_16x16x32_bf16 v[80:83], v[232:235], v[104:107], v[80:83]\n\t"
    "v_mfma_f32_16x16x32_bf16 v[84:87], v[236:239], v[104:107], v[84:87]\n\t"
    "v_mfma_f32_16x16x32_bf16 v[88:91], v[232:235], v[108:111], v[88:91]\n\t"
    "v_mfma_f32_16x16x32_bf16 v[92:95], v[236:239], v[108:111], v[92:95]\n\t"
    "s_waitcnt lgkmcnt(2)\n\t"
    "v_mfma_f32_16x16x32_bf16 v[80:83], v[240:243], v[112:115], v[80:83]\n\t"
    "v_mfma_f32_16x16x32_bf16 v[84:87], v[244:247], v[112:115], v[84:87]\n\t"
    "v_mfma_f32_16x16x32_bf16 v[88:91], v[240:243], v[116:119], v[88:91]\n\t"
    "v_mfma_f32_16x16x32_bf16 v[92:95], v[244:247], v[116:119], v[92:95]\n\t"
    "s_waitcnt lgkmcnt(0)\n\t"
    "v_mfma_f32_16x16x32_bf16 v[80:83], v[248:251], v[120:123], v[80:83]\n\t"
    "v_mfma_f32_16x16x32_bf16 v[84:87], v[252:255], v[120:123], v[84:87]\n\t"
    "v_mfma_f32_16x16x32_bf16 v[88:91], v[248:251], v[124:127], v[88:91]\n\t"
    "v_mfma_f32_16x16x32_bf16 v[92:95], v[252:255], v[124:127], v[92:95]\n\t"
    "s_nop 6\n\t"
    "s_branch 9f\n\t"
    "3:\n\t"
    "9:"
    : "=&{v[80:95]}"(pd)
    : [ea] "v"(ea), [da] "v"(da), [x] "s"(x), "{v[128:159]}"(W[0]), "{v[160:191]}"(W[1]), "{v[192:223]}"(W[2]), "{v[224:255]}"(W[3]), [lb] "s"(lb), [s0] "s"(s0), [s1] "s"(s1), [av0] "v"(av[0]), [av1] "v"(av[1]), [av2] "v"(av[2]), [av3] "v"(av[3]), [av4] "v"(av[4]), [av5] "v"(av[5]), [av6] "v"(av[6]), [av7] "v"(av[7])
    : "v96", "v97", "v98", "v99", "v100", "v101", "v102", "v103", "v104", "v105", "v106", "v107", "v108", "v109", "v110", "v111", "v112", "v113", "v114", "v115", "v116", "v117", "v118", "v119", "v120", "v121", "v122", "v123", "v124", "v125", "v126", "v127", "vcc", "scc", "m0", "memory");
}
__device__ __forceinline__ void lean_ld1(const u32x32 (&W)[4], u32x16& pd, unsigned ea, unsigned da, unsigned x, unsigned lb, const char* s0, const char* s1, const unsigned (&av)[8], unsigned sa, unsigned mu, unsigned ad, int th, unsigned s2, unsigned dv, const char* db) {
  asm volatile("s_setprio 2\n\t"
    "ds_read_b128 v[96:99], %[ea] offset:0\n\t"
    "ds_read_b128 v[100:103], %[ea] offset:2048\n\t"
    "ds_read_b128 v[104:107], %[da] offset:0\n\t"
    "ds_read_b128 v[108:111], %[da] offset:2048\n\t"
    "ds_read_b128 v[112:115], %[ea] offset:4096\n\t"
    "ds_read_b128 v[116:119], %[ea] offset:6144\n\t"
    "s_waitcnt lgkmcnt(4)\n\t"
    "v_mfma_f32_16x16x32_bf16 v[80:83], v[128:131], v[96:99], 0\n\t"
    "ds_read_b128 v[120:123], %[da] offset:4096\n\t"
    "v_mfma_f32_16x16x32_bf16 v[84:87], v[132:135], v[96:99], 0\n\t"
    "ds_read_b128 v[124:127], %[da] offset:6144\n\t"
    "v_mfma_f32_16x16x32_bf16 v[88:91], v[128:131], v[100:103], 0\n\t"
    "v_mfma_f32_16x16x32_bf16 v[92:95], v[132:135], v[100:103], 0\n\t"
    "s_waitcnt vmcnt(12)\n\t"
    "s_cmp_lt_u32 %[x], 2\n\t"
    "s_cbranch_scc0 41f\n\t"
    "s_waitcnt vmcnt(4)\n\t"
    "41:\n\t"
    "s_barrier\n\t"
    "s_waitcnt lgkmcnt(4)\n\t"
    "v_mfma_f32_16x16x32_bf16 v[80:83], v[136:139], v[104:107], v[80:83]\n\t"
    "ds_read_b128 v[96:99], %[ea] offset:8192\n\t"
    "v_mfma_f32_16x16x32_bf16 v[84:87], v[140:143], v[104:107], v[84:87]\n\t"
    "ds_read_b128 v[100:103], %[ea] offset:10240\n\t"
    "v_mfma_f32_16x16x32_bf16 v[88:91], v[136:139], v[108:111], v[88:91]\n\t"
    "v_mfma_f32_16x16x32_bf16 v[92:95], v[140:143], v[108:111], v[92:95]\n\t"
    "s_waitcnt lgkmcnt(4)\n\t"
    "v_mfma_f32_16x16x32_bf16 v[80:83], v[144:147], v[112:115], v[80:83]\n\t"
    "ds_read_b128 v[104:107], %[da] offset:8192\n\t"
    "v_mfma_f32_16x16x32_bf16 v[84:87], v[148:151], v[112:115], v[84:87]\n\t"
    "ds_read_b128 v[108:111], %[da] offset:10240\n\t"
    "v_mfma_f32_16x16x32_bf16 v[88:91], v[144:147], v[116:119], v[88:91]\n\t"
    "v_mfma_f32_16x16x32_bf16 v[92:95], v[148:151], v[116:119], v[92:95]\n\t"
    "s_waitcnt lgkmcnt(4)\n\t"
    "v_mfma_f32_16x16x32_bf16 v[80:83], v[152:155], v[120:123], v[80:83]\n\t"
    "ds_read_b128 v[112:115], %[ea] offset:12288\n\t"
    "v_mfma_f32_16x16x32_bf16 v[84:87], v[156:159], v[120:123], v[84:87]\n\t"
    "ds_read_b128 v[116:119], %[ea] offset:14336\n\t"
    "v_mfma_f32_16x16x32_bf16 v[88:91], v[152:155], v[124:127], v[88:91]\n\t"
    "v_mfma_f32_16x16x32_bf16 v[92:95], v[156:159], v[124:127], v[92:95]\n\t"
    "s_waitcnt lgkmcnt(4)\n\t"
    "v_mfma_f32_16x16x32_bf16 v[80:83], v[160:163], v[96:99], v[80:83]\n\t"
    "ds_read_b128 v[120:123], %[da] offset:12288\n\t"
    "v_mfma_f32_16x16x32_bf16 v[84:87], v[164:167], v[96:99], v[84:87]\n\t"
    "ds_read_b128 v[124:127], %[da] offset:14336\n\t"
    "v_mfma_f32_16x16x32_bf16 v[88:91], v[160:163], v[100:103], v[88:91]\n\t"
    "v_mfma_f32_16x16x32_bf16 v[92:95], v[164:167], v[100:103], v[92:95]\n\t"
    "s_waitcnt vmcnt(10)\n\t"
    "s_cmp_lt_u32 %[x], 2\n\t"
    "s_cbranch_scc0 45f\n\t"
    "s_waitcnt vmcnt(2)\n\t"
    "45:\n\t"
    "s_barrier\n\t"
    "s_waitcnt lgkmcnt(4)\n\t"
    "v_mfma_f32_16x16x32_bf16 v[80:83], v[168:171], v[104:107], v[80:83]\n\t"
    "ds_read_b128 v[96:99], %[ea] offset:16384\n\t"
    "v_mfma_f32_16x16x32_bf16 v[84:87], v[172:175], v[104:107], v[84:87]\n\t"
    "ds_read_b128 v[100:103], %[ea] offset:18432\n\t"
    "v_mfma_f32_16x16x32_bf16 v[88:91], v[168:171], v[108:111], v[88:91]\n\t"
    "v_mfma_f32_16x16x32_bf16 v[92:95], v[172:175], v[108:111], v[92:95]\n\t"
    "s_waitcnt lgkmcnt(4)\n\t"
    "v_mfma_f32_16x16x32_bf16 v[80:83], v[176:179], v[112:115], v[80:83]\n\t"
    "ds_read_b128 v[104:107], %[da] offset:16384\n\t"
    "v_mfma_f32_16x16x32_bf16 v[84:87], v[180:183], v[112:115], v[84:87]\n\t"
    "ds_read_b128 v[108:111], %[da] offset:18432\n\t"
    "v_mfma_f32_16x16x32_bf16 v[88:91], v[176:179], v[116:119], v[88:91]\n\t"
    "v_mfma_f32_16x16x32_bf16 v[92:95], v[180:183], v[116:119], v[92:95]\n\t"
    "s_waitcnt lgkmcnt(4)\n\t"
    "v_mfma_f32_16x16x32_bf16 v[80:83], v[184:187], v[120:123], v[80:83]\n\t"
    "ds_read_b128 v[112:115], %[ea] offset:20480\n\t"
    "v_mfma_f32_16x16x32_bf16 v[84:87], v[188:191], v[120:123], v[84:87]\n\t"
    "ds_read_b128 v[116:119], %[ea] offset:22528\n\t"
    "v_mfma_f32_16x16x32_bf16 v[88:91], v[184:187], v[124:127], v[88:91]\n\t"
    "v_mfma_f32_16x16x32_bf16 v[92:95], v[188:191], v[124:127], v[92:95]\n\t"
    "s_waitcnt lgkmcnt(4)\n\t"
    "v_mfma_f32_16x16x32_bf16 v[80:83], v[192:195], v[96:99], v[80:83]\n\t"
    "ds_read_b128 v[120:123], %[da] offset:20480\n\t"
    "v_mfma_f32_16x16x32_bf16 v[84:87], v[196:199], v[96:99], v[84:87]\n\t"
    "ds_read_b128 v[124:127], %[da] offset:22528\n\t"
    "v_mfma_f32_16x16x32_bf16 v[88:91], v[192:195], v[100:103], v[88:91]\n\t"
    "v_mfma_f32_16x16x32_bf16 v[92:95], v[196:199], v[100:103], v[92:95]\n\t"
    "s_waitcnt vmcnt(8)\n\t"
    "s_cmp_lt_u32 %[x], 2\n\t"
    "s_cbranch_scc0 49f\n\t"
    "s_waitcnt vmcnt(0)\n\t"
    "49:\n\t"
    "s_barrier\n\t"
    "ds_read_b32 v79, %[sa]\n\t"
    "s_waitcnt lgkmcnt(5)\n\t"
    "v_mfma_f32_16x16x32_bf16 v[80:83], v[200:203], v[104:107], v[80:83]\n\t"
    "ds_read_b128 v[96:99], %[ea] offset:24576\n\t"
    "v_mfma_f32_16x16x32_bf16 v[84:87], v[204:207], v[104:107], v[84:87]\n\t"
    "ds_read_b128 v[100:103], %[ea] offset:26624\n\t"
    "v_mfma_f32_16x16x32_bf16 v[88:91], v[200:203], v[108:111], v[88:91]\n\t"
    "v_mfma_f32_16x16x32_bf16 v[92:95], v[204:207], v[108:111], v[92:95]\n\t"
    "s_waitcnt lgkmcnt(5)\n\t"
    "v_mfma_f32_16x16x32_bf16 v[80:83], v[208:211], v[112:115], v[80:83]\n\t"
    "ds_read_b128 v[104:107], %[da] offset:24576\n\t"
    "v_mfma_f32_16x16x32_bf16 v[84:87], v[212:215], v[112:115], v[84:87]\n\t"
    "ds_read_b128 v[108:111], %[da] offset:26624\n\t"
    "v_mfma_f32_16x16x32_bf16 v[88:91], v[208:211], v[116:119], v[88:91]\n\t"
    "v_mfma_f32_16x16x32_bf16 v[92:95], v[212:215], v[116:119], v[92:95]\n\t"
    "s_cmp_eq_u32 %[s2], 0\n\t"
    "s_cbranch_scc1 7f\n\t"
    "s_mov_b32 m0, %[s2]\n\t"
    "s_nop 0\n\t"
    "global_load_lds_dword %[dv], %[db] sc1\n\t"
    "7:\n\t"
    "s_waitcnt lgkmcnt(4)\n\t"
    "v_mfma_f32_16x16x32_bf16 v[80:83], v[216:219], v[120:123], v[80:83]\n\t"
    "ds_read_b128 v[112:115], %[ea] offset:28672\n\t"
    "v_mfma_f32_16x16x32_bf16 v[84:87], v[220:223], v[120:123], v[84:87]\n\t"
    "ds_read_b128 v[116:119], %[ea] offset:30720\n\t"
    "v_mfma_f32_16x16x32_bf16 v[88:91], v[216:219], v[124:127], v[88:91]\n\t"
    "v_mad_u32_u24 v79, v79, %[mu], %[ad]\n\t"
    "v_cmp_ge_i32 vcc, %[th], v79\n\t"
    "v_mfma_f32_16x16x32_bf16 v[92:95], v[220:223], v[124:127], v[92:95]\n\t"
    "s_cbranch_vccz 5f\n\t"
    "s_waitcnt lgkmcnt(4)\n\t"
    "v_mfma_f32_16x16x32_bf16 v[80:83], v[224:227], v[96:99], v[80:83]\n\t"
    "ds_read_b128 v[120:123], %[da] offset:28672\n\t"
    "v_mfma_f32_16x16x32_bf16 v[84:87], v[228:231], v[96:99], v[84:87]\n\t"
    "ds_read_b128 v[124:127], %[da] offset:30720\n\t"
    "v_mfma_f32_16x16x32_bf16 v[88:91], v[224:227], v[100:103], v[88:91]\n\t"
    "v_mfma_f32_16x16x32_bf16 v[92:95], v[228:231], v[100:103], v[92:95]\n\t"
    "s_waitcnt lgkmcnt(4)\n\t"
    "v_mfma_f32_16x16x32_bf16 v[80:83], v[232:235], v[104:107], v[80:83]\n\t"
    "v_mfma_f32_16x16x32_bf16 v[84:87], v[236:239], v[104:107], v[84:87]\n\t"
    "v_mfma_f32_16x16x32_bf16 v[88:91], v[232:235], v[108:111], v[88:91]\n\t"
    "v_mfma_f32_16x16x32_bf16 v[92:95], v[236:239], v[108:111], v[92:95]\n\t"
    "s_waitcnt lgkmcnt(2)\n\t"
    "v_mfma_f32_16x16x32_bf16 v[80:83], v[240:243], v[112:115], v[80:83]\n\t"
    "v_mfma_f32_16x16x32_bf16 v[84:87], v[244:247], v[112:115], v[84:87]\n\t"
    "v_mfma_f32_16x16x32_bf16 v[88:91], v[240:243], v[116:119], v[88:91]\n\t"
    "v_mfma_f32_16x16x32_bf16 v[92:95], v[244:247], v[116:119], v[92:95]\n\t"
    "s_waitcnt lgkmcnt(0)\n\t"
    "v_mfma_f32_16x16x32_bf16 v[80:83], v[248:251], v[120:123], v[80:83]\n\t"
    "v_mfma_f32_16x16x32_bf16 v[84:87], v[252:255], v[120:123], v[84:87]\n\t"
    "v_mfma_f32_16x16x32_bf16 v[88:91], v[248:251], v[124:127], v[88:91]\n\t"
    "v_mfma_f32_16x16x32_bf16 v[92:95], v[252:255], v[124:127], v[92:95]\n\t"
    "s_branch 6f\n\t"
    "5:\n\t"
    "s_waitcnt lgkmcnt(4)\n\t"
    "v_mfma_f32_16x16x32_bf16 v[80:83], v[224:227], v[96:99], v[80:83]\n\t"
    "ds_read_b128 v[120:123], %[da] offset:28672\n\t"
    "s_add_u32 m0, %[lb], 0\n\t"
    "s_nop 0\n\t"
    "global_load_lds_dwordx4 %[av0], %[s0] sc1\n\t"
    "v_mfma_f32_16x16x32_bf16 v[84:87], v[228:231], v[96:99], v[84:87]\n\t"
    "ds_read_b128 v[124:127], %[da] offset:30720\n\t"
    "v_mfma_f32_16x16x32_bf16 v[88:91], v[224:227], v[100:103], v[88:91]\n\t"
    "s_add_u32 m0, %[lb], 4096\n\t"
    "s_nop 0\n\t"
    "global_load_lds_dwordx4 %[av2], %[s0] sc1\n\t"
    "v_mfma_f32_16x16x32_bf16 v[92:95], v[228:231], v[100:103], v[92:95]\n\t"
    "s_waitcnt lgkmcnt(4)\n\t"
    "v_mfma_f32_16x16x32_bf16 v[80:83], v[232:235], v[104:107], v[80:83]\n\t"
    "s_add_u32 m0, %[lb], 8192\n\t"
    "s_nop 0\n\t"
    "global_load_lds_dwordx4 %[av4], %[s0] sc1\n\t"
    "v_mfma_f32_16x16x32_bf16 v[84:87], v[236:239], v[104:107], v[84:87]\n\t"
    "v_mfma_f32_16x16x32_bf16 v[88:91], v[232:235], v[108:111], v[88:91]\n\t"
    "s_add_u32 m0, %[lb], 12288\n\t"
    "s_nop 0\n\t"
    "global_load_lds_dwordx4 %[av6], %[s0] sc1\n\t"
    "v_mfma_f32_16x16x32_bf16 v[92:95], v[236:239], v[108:111], v[92:95]\n\t"
    "s_waitcnt lgkmcnt(2)\n\t"
    "v_mfma_f32_16x16x32_bf16 v[80:83], v[240:243], v[112:115], v[80:83]\n\t"
    "s_add_u32 m0, %[lb], 16384\n\t"
    "s_nop 0\n\t"
    "global_load_lds_dwordx4 %[av0], %[s1] sc1\n\t"
    "v_mfma_f32_16x16x32_bf16 v[84:87], v[244:247], v[112:115], v[84:87]\n\t"
    "v_mfma_f32_16x16x32_bf16 v[88:91], v[240:243], v[116:119], v[88:91]\n\t"
    "s_add_u32 m0, %[lb], 20480\n\t"
    "s_nop 0\n\t"
    "global_load_lds_dwordx4 %[av2], %[s1] sc1\n\t"
    "v_mfma_f32_16x16x32_bf16 v[92:95], v[244:247], v[116:119], v[92:95]\n\t"
    "s_waitcnt lgkmcnt(0)\n\t"
    "v_mfma_f32_16x16x32_bf16 v[80:83], v[248:251], v[120:123], v[80:83]\n\t"
    "s_add_u32 m0, %[lb], 24576\n\t"
    "s_nop 0\n\t"
    "global_load_lds_dwordx4 %[av4], %[s1] sc1\n\t"
    "v_mfma_f32_16x16x32_bf16 v[84:87], v[252:255], v[120:123], v[84:87]\n\t"
    "v_mfma_f32_16x16x32_bf16 v[88:91], v[248:251], v[124:127], v[88:91]\n\t"
    "s_add_u32 m0, %[lb], 28672\n\t"
    "s_nop 0\n\t"
    "global_load_lds_dwordx4 %[av6], %[s1] sc1\n\t"
    "v_mfma_f32_16x16x32_bf16 v[92:95], v[252:255], v[124:127], v[92:95]\n\t"
    "6:\n\t"
    "s_nop 6\n\t"
    "s_branch 9f\n\t"
    "3:\n\t"
    "9:"
    : "=&{v[80:95]}"(pd)
    : [ea] "v"(ea), [da] "v"(da), [x] "s"(x), "{v[128:159]}"(W[0]), "{v[160:191]}"(W[1]), "{v[192:223]}"(W[2]), "{v[224:255]}"(W[3]), [lb] "s"(lb), [s0] "s"(s0), [s1] "s"(s1), [av0] "v"(av[0]), [av1] "v"(av[1]), [av2] "v"(av[2]), [av3] "v"(av[3]), [av4] "v"(av[4]), [av5] "v"(av[5]), [av6] "v"(av[6]), [av7] "v"(av[7]), [sa] "v"(sa), [mu] "s"(mu), [ad] "v"(ad), [th] "s"(th), [s2] "s"(s2), [dv] "v"(dv), [db] "s"(db)
    : "v96", "v97", "v98", "v99", "v100", "v101", "v102", "v103", "v104", "v105", "v106", "v107", "v108", "v109", "v110", "v111", "v112", "v113", "v114", "v115", "v116", "v117", "v118", "v119", "v120", "v121", "v122", "v123", "v124", "v125", "v126", "v127", "v79", "vcc", "scc", "m0", "memory");
}
__device__ __forceinline__ void lean_dm1(const u32x32 (&W)[4], u32x16& pd, unsigned ea, unsigned da, unsigned x, unsigned lb, const char* s0, const char* s1, const unsigned (&av)[8]) {
  asm volatile("s_setprio 2\n\t"
    "ds_read_b128 v[96:99], %[ea] offset:0\n\t"
    "ds_read_b128 v[100:103], %[ea] offset:2048\n\t"
    "ds_read_b128 v[104:107], %[da] offset:0\n\t"
    "ds_read_b128 v[108:111], %[da] offset:2048\n\t"
    "ds_read_b128 v[112:115], %[ea] offset:4096\n\t"
    "ds_read_b128 v[116:119], %[ea] offset:6144\n\t"
    "s_waitcnt lgkmcnt(4)\n\t"
    "v_mfma_f32_16x16x32_bf16 v[80:83], v[128:131], v[96:99], 0\n\t"
    "ds_read_b128 v[120:123], %[da] offset:4096\n\t"
    "s_add_u32 m0, %[lb], 0\n\t"
    "v_mfma_f32_16x16x32_bf16 v[84:87], v[132:135], v[96:99], 0\n\t"
    "ds_read_b128 v[124:127], %[da] offset:6144\n\t"
    "global_load_lds_dwordx4 %[av0], %[s0] sc1\n\t"
    "v_mfma_f32_16x16x32_bf16 v[88:91], v[128:131], v[100:103], 0\n\t"
    "v_mfma_f32_16x16x32_bf16 v[92:95], v[132:135], v[100:103], 0\n\t"
    "s_waitcnt vmcnt(5)\n\t"
    "s_barrier\n\t"
    "s_waitcnt lgkmcnt(4)\n\t"
    "v_mfma_f32_16x16x32_bf16 v[80:83], v[136:139], v[104:107], v[80:83]\n\t"
    "ds_read_b128 v[96:99], %[ea] offset:8192\n\t"
    "s_add_u32 m0, %[lb], 4096\n\t"
    "v_mfma_f32_16x16x32_bf16 v[84:87], v[140:143], v[104:107], v[84:87]\n\t"
    "ds_read_b128 v[100:103], %[ea] offset:10240\n\t"
    "global_load_lds_dwordx4 %[av2], %[s0] sc1\n\t"
    "v_mfma_f32_16x16x32_bf16 v[88:91], v[136:139], v[108:111], v[88:91]\n\t"
    "v_mfma_f32_16x16x32_bf16 v[92:95], v[140:143], v[108:111], v[92:95]\n\t"
    "s_waitcnt lgkmcnt(4)\n\t"
    "v_mfma_f32_16x16x32_bf16 v[80:83], v[144:147], v[112:115], v[80:83]\n\t"
    "ds_read_b128 v[104:107], %[da] offset:8192\n\t"
    "s_add_u32 m0, %[lb], 8192\n\t"
    "v_mfma_f32_16x16x32_bf16 v[84:87], v[148:151], v[112:115], v[84:87]\n\t"
    "ds_read_b128 v[108:111], %[da] offset:10240\n\t"
    "global_load_lds_dwordx4 %[av4], %[s0] sc1\n\t"
    "v_mfma_f32_16x16x32_bf16 v[88:91], v[144:147], v[116:119], v[88:91]\n\t"
    "v_mfma_f32_16x16x32_bf16 v[92:95], v[148:151], v[116:119], v[92:95]\n\t"
    "s_waitcnt lgkmcnt(4)\n\t"
    "v_mfma_f32_16x16x32_bf16 v[80:83], v[152:155], v[120:123], v[80:83]\n\t"
    "ds_read_b128 v[112:115], %[ea] offset:12288\n\t"
    "s_add_u32 m0, %[lb], 12288\n\t"
    "v_mfma_f32_16x16x32_bf16 v[84:87], v[156:159], v[120:123], v[84:87]\n\t"
    "ds_read_b128 v[116:119], %[ea] offset:14336\n\t"
    "global_load_lds_dwordx4 %[av6], %[s0] sc1\n\t"
    "v_mfma_f32_16x16x32_bf16 v[88:91], v[152:155], v[124:127], v[88:91]\n\t"
    "v_mfma_f32_16x16x32_bf16 v[92:95], v[156:159], v[124:127], v[92:95]\n\t"
    "s_waitcnt lgkmcnt(4)\n\t"
    "v_mfma_f32_16x16x32_bf16 v[80:83], v[160:163], v[96:99], v[80:83]\n\t"
    "ds_read_b128 v[120:123], %[da] offset:12288\n\t"
    "s_add_u32 m0, %[lb], 16384\n\t"
    "v_mfma_f32_16x16x32_bf16 v[84:87], v[164:167], v[96:99], v[84:87]\n\t"
    "ds_read_b128 v[124:127], %[da] offset:14336\n\t"
    "global_load_lds_dwordx4 %[av0], %[s1] sc1\n\t"
    "v_mfma_f32_16x16x32_bf16 v[88:91], v[160:163], v[100:103], v[88:91]\n\t"
    "v_mfma_f32_16x16x32_bf16 v[92:95], v[164:167], v[100:103], v[92:95]\n\t"
    "s_waitcnt vmcnt(7)\n\t"
    "s_barrier\n\t"
    "s_waitcnt lgkmcnt(4)\n\t"
    "v_mfma_f32_16x16x32_bf16 v[80:83], v[168:171], v[104:107], v[80:83]\n\t"
    "ds_read_b128 v[96:99], %[ea] offset:16384\n\t"
    "s_add_u32 m0, %[lb], 20480\n\t"
    "v_mfma_f32_16x16x32_bf16 v[84:87], v[172:175], v[104:107], v[84:87]\n\t"
    "ds_read_b128 v[100:103], %[ea] offset:18432\n\t"
    "global_load_lds_dwordx4 %[av2], %[s1] sc1\n\t"
    "v_mfma_f32_16x16x32_bf16 v[88:91], v[168:171], v[108:111], v[88:91]\n\t"
    "v_mfma_f32_16x16x32_bf16 v[92:95], v[172:175], v[108:111], v[92:95]\n\t"
    "s_waitcnt lgkmcnt(4)\n\t"
    "v_mfma_f32_16x16x32_bf16 v[80:83], v[176:179], v[112:115], v[80:83]\n\t"
    "ds_read_b128 v[104:107], %[da] offset:16384\n\t"
    "s_add_u32 m0, %[lb], 24576\n\t"
    "v_mfma_f32_16x16x32_bf16 v[84:87], v[180:183], v[112:115], v[84:87]\n\t"
    "ds_read_b128 v[108:111], %[da] offset:18432\n\t"
    "global_load_lds_dwordx4 %[av4], %[s1] sc1\n\t"
    "v_mfma_f32_16x16x32_bf16 v[88:91], v[176:179], v[116:119], v[88:91]\n\t"
    "v_mfma_f32_16x16x32_bf16 v[92:95], v[180:183], v[116:119], v[92:95]\n\t"
    "s_waitcnt lgkmcnt(4)\n\t"
    "v_mfma_f32_16x16x32_bf16 v[80:83], v[184:187], v[120:123], v[80:83]\n\t"
    "ds_read_b128 v[112:115], %[ea] offset:20480\n\t"
    "s_add_u32 m0, %[lb], 28672\n\t"
    "v_mfma_f32_16x16x32_bf16 v[84:87], v[188:191], v[120:123], v[84:87]\n\t"
    "ds_read_b128 v[116:119], %[ea] offset:22528\n\t"
    "global_load_lds_dwordx4 %[av6], %[s1] sc1\n\t"
    "v_mfma_f32_16x16x32_bf16 v[88:91], v[184:187], v[124:127], v[88:91]\n\t"
    "v_mfma_f32_16x16x32_bf16 v[92:95], v[188:191], v[124:127], v[92:95]\n\t"
    "s_waitcnt lgkmcnt(4)\n\t"
    "v_mfma_f32_16x16x32_bf16 v[80:83], v[192:195], v[96:99], v[80:83]\n\t"
    "ds_read_b128 v[120:123], %[da] offset:20480\n\t"
    "v_mfma_f32_16x16x32_bf16 v[84:87], v[196:199], v[96:99], v[84:87]\n\t"
    "ds_read_b128 v[124:127], %[da] offset:22528\n\t"
    "v_mfma_f32_16x16x32_bf16 v[88:91], v[192:195], v[100:103], v[88:91]\n\t"
    "v_mfma_f32_16x16x32_bf16 v[92:95], v[196:199], v[100:103], v[92:95]\n\t"
    "s_waitcnt vmcnt(8)\n\t"
    "s_barrier\n\t"
    "s_waitcnt lgkmcnt(4)\n\t"
    "v_mfma_f32_16x16x32_bf16 v[80:83], v[200:203], v[104:107], v[80:83]\n\t"
    "ds_read_b128 v[96:99], %[ea] offset:24576\n\t"
    "v_mfma_f32_16x16x32_bf16 v[84:87], v[204:207], v[104:107], v[84:87]\n\t"
    "ds_read_b128 v[100:103], %[ea] offset:26624\n\t"
    "v_mfma_f32_16x16x32_bf16 v[88:91], v[200:203], v[108:111], v[88:91]\n\t"
    "v_mfma_f32_16x16x32_bf16 v[92:95], v[204:207], v[108:111], v[92:95]\n\t"
    "s_waitcnt lgkmcnt(4)\n\t"
    "v_mfma_f32_16x16x32_bf16 v[80:83], v[208:211], v[112:115], v[80:83]\n\t"
    "ds_read_b128 v[104:107], %[da] offset:24576\n\t"
    "v_mfma_f32_16x16x32_bf16 v[84:87], v[212:215], v[112:115], v[84:87]\n\t"
    "ds_read_b128 v[108:111], %[da] offset:26624\n\t"
    "v_mfma_f32_16x16x32_bf16 v[88:91], v[208:211], v[116:119], v[88:91]\n\t"
    "v_mfma_f32_16x16x32_bf16 v[92:95], v[212:215], v[116:119], v[92:95]\n\t"
    "s_waitcnt lgkmcnt(4)\n\t"
    "v_mfma_f32_16x16x32_bf16 v[80:83], v[216:219], v[120:123], v[80:83]\n\t"
    "ds_read_b128 v[112:115], %[ea] offset:28672\n\t"
    "v_mfma_f32_16x16x32_bf16 v[84:87], v[220:223], v[120:123], v[84:87]\n\t"
    "ds_read_b128 v[116:119], %[ea] offset:30720\n\t"
    "v_mfma_f32_16x16x32_bf16 v[88:91], v[216:219], v[124:127], v[88:91]\n\t"
    "v_mfma_f32_16x16x32_bf16 v[92:95], v[220:223], v[124:127], v[92:95]\n\t"
    "s_waitcnt lgkmcnt(4)\n\t"
    "v_mfma_f32_16x16x32_bf16 v[80:83], v[224:227], v[96:99], v[80:83]\n\t"
    "ds_read_b128 v[120:123], %[da] offset:28672\n\t"
    "v_mfma_f32_16x16x32_bf16 v[84:87], v[228:231], v[96:99], v[84:87]\n\t"
    "ds_read_b128 v[124:127], %[da] offset:30720\n\t"
    "v_mfma_f32_16x16x32_bf16 v[88:91], v[224:227], v[100:103], v[88:91]\n\t"
    "v_mfma_f32_16x16x32_bf16 v[92:95], v[228:231], v[100:103], v[92:95]\n\t"
    "s_waitcnt lgkmcnt(4)\n\t"
    "v_mfma_f32_16x16x32_bf16 v[80:83], v[232:235], v[104:107], v[80:83]\n\t"
    "v_mfma_f32_16x16x32_bf16 v[84:87], v[236:239], v[104:107], v[84:87]\n\t"
    "v_mfma_f32_16x16x32_bf16 v[88:91], v[232:235], v[108:111], v[88:91]\n\t"
    "v_mfma_f32_16x16x32_bf16 v[92:95], v[236:239], v[108:111], v[92:95]\n\t"
    "s_waitcnt lgkmcnt(2)\n\t"
    "v_mfma_f32_16x16x32_bf16 v[80:83], v[240:243], v[112:115], v[80:83]\n\t"
    "v_mfma_f32_16x16x32_bf16 v[84:87], v[244:247], v[112:115], v[84:87]\n\t"
    "v_mfma_f32_16x16x32_bf16 v[88:91], v[240:243], v[116:119], v[88:91]\n\t"
    "v_mfma_f32_16x16x32_bf16 v[92:95], v[244:247], v[116:119], v[92:95]\n\t"
    "s_waitcnt lgkmcnt(0)\n\t"
    "v_mfma_f32_16x16x32_bf16 v[80:83], v[248:251], v[120:123], v[80:83]\n\t"
    "v_mfma_f32_16x16x32_bf16 v[84:87], v[252:255], v[120:123], v[84:87]\n\t"
    "v_mfma_f32_16x16x32_bf16 v[88:91], v[248:251], v[124:127], v[88:91]\n\t"
    "v_mfma_f32_16x16x32_bf16 v[92:95], v[252:255], v[124:127], v[92:95]\n\t"
    "s_nop 6\n\t"
    "s_branch 9f\n\t"
    "3:\n\t"
    "9:"
    : "=&{v[80:95]}"(pd)
    : [ea] "v"(ea), [da] "v"(da), [x] "s"(x), "{v[128:159]}"(W[0]), "{v[160:191]}"(W[1]), "{v[192:223]}"(W[2]), "{v[224:255]}"(W[3]), [lb] "s"(lb), [s0] "s"(s0), [s1] "s"(s1), [av0] "v"(av[0]), [av1] "v"(av[1]), [av2] "v"(av[2]), [av3] "v"(av[3]), [av4] "v"(av[4]), [av5] "v"(av[5]), [av6] "v"(av[6]), [av7] "v"(av[7])
    : "v96", "v97", "v98", "v99", "v100", "v101", "v102", "v103", "v104", "v105", "v106", "v107", "v108", "v109", "v110", "v111", "v112", "v113", "v114", "v115", "v116", "v117", "v118", "v119", "v120", "v121", "v122", "v123", "v124", "v125", "v126", "v127", "vcc", "scc", "m0", "memory");
}
__device__ __forceinline__ void lean_st0(const u32x32 (&W)[4], u32x16& pd, unsigned ea, unsigned da, unsigned x, unsigned pv, unsigned cn, unsigned sl, unsigned dv, const char* db, const char* pg) {
  unsigned st;
  asm volatile("ds_read_b128 v[96:99], %[ea] offset:0\n\t"
    "ds_read_b128 v[100:103], %[ea] offset:2048\n\t"
    "ds_read_b128 v[104:107], %[da] offset:0\n\t"
    "ds_read_b128 v[108:111], %[da] offset:2048\n\t"
    "ds_read_b128 v[112:115], %[ea] offset:4096\n\t"
    "ds_read_b128 v[116:119], %[ea] offset:6144\n\t"
    "s_waitcnt lgkmcnt(4)\n\t"
    "v_mfma_f32_16x16x32_bf16 v[80:83], v[128:131], v[96:99], 0\n\t"
    "ds_read_b128 v[120:123], %[da] offset:4096\n\t"
    "v_mfma_f32_16x16x32_bf16 v[84:87], v[132:135], v[96:99], 0\n\t"
    "ds_read_b128 v[124:127], %[da] offset:6144\n\t"
    "v_mfma_f32_16x16x32_bf16 v[88:91], v[128:131], v[100:103], 0\n\t"
    "v_mfma_f32_16x16x32_bf16 v[92:95], v[132:135], v[100:103], 0\n\t"
    "s_barrier\n\t"
    "s_waitcnt lgkmcnt(4)\n\t"
    "v_mfma_f32_16x16x32_bf16 v[80:83], v[136:139], v[104:107], v[80:83]\n\t"
    "ds_read_b128 v[96:99], %[ea] offset:8192\n\t"
    "v_mfma_f32_16x16x32_bf16 v[84:87], v[140:143], v[104:107], v[84:87]\n\t"
    "ds_read_b128 v[100:103], %[ea] offset:10240\n\t"
    "v_mfma_f32_16x16x32_bf16 v[88:91], v[136:139], v[108:111], v[88:91]\n\t"
    "v_mfma_f32_16x16x32_bf16 v[92:95], v[140:143], v[108:111], v[92:95]\n\t"
    "s_waitcnt lgkmcnt(4)\n\t"
    "v_mfma_f32_16x16x32_bf16 v[80:83], v[144:147], v[112:115], v[80:83]\n\t"
    "ds_read_b128 v[104:107], %[da] offset:8192\n\t"
    "v_mfma_f32_16x16x32_bf16 v[84:87], v[148:151], v[112:115], v[84:87]\n\t"
    "ds_read_b128 v[108:111], %[da] offset:10240\n\t"
    "v_mfma_f32_16x16x32_bf16 v[88:91], v[144:147], v[116:119], v[88:91]\n\t"
    "v_mfma_f32_16x16x32_bf16 v[92:95], v[148:151], v[116:119], v[92:95]\n\t"
    "s_waitcnt lgkmcnt(4)\n\t"
    "v_mfma_f32_16x16x32_bf16 v[80:83], v[152:155], v[120:123], v[80:83]\n\t"
    "ds_read_b128 v[112:115], %[ea] offset:12288\n\t"
    "v_mfma_f32_16x16x32_bf16 v[84:87], v[156:159], v[120:123], v[84:87]\n\t"
    "ds_read_b128 v[116:119], %[ea] offset:14336\n\t"
    "v_mfma_f32_16x16x32_bf16 v[88:91], v[152:155], v[124:127], v[88:91]\n\t"
    "v_mfma_f32_16x16x32_bf16 v[92:95], v[156:159], v[124:127], v[92:95]\n\t"
    "s_bitcmp1_b32 %[x], 2\n\t"
    "s_cbranch_scc1 7f\n\t"
    "8:\n\t"
    "s_waitcnt lgkmcnt(4)\n\t"
    "v_mfma_f32_16x16x32_bf16 v[80:83], v[160:163], v[96:99], v[80:83]\n\t"
    "ds_read_b128 v[120:123], %[da] offset:12288\n\t"
    "v_mfma_f32_16x16x32_bf16 v[84:87], v[164:167], v[96:99], v[84:87]\n\t"
    "ds_read_b128 v[124:127], %[da] offset:14336\n\t"
    "v_mfma_f32_16x16x32_bf16 v[88:91], v[160:163], v[100:103], v[88:91]\n\t"
    "v_mfma_f32_16x16x32_bf16 v[92:95], v[164:167], v[100:103], v[92:95]\n\t"
    "s_barrier\n\t"
    "s_waitcnt lgkmcnt(4)\n\t"
    "v_mfma_f32_16x16x32_bf16 v[80:83], v[168:171], v[104:107], v[80:83]\n\t"
    "ds_read_b128 v[96:99], %[ea] offset:16384\n\t"
    "v_mfma_f32_16x16x32_bf16 v[84:87], v[172:175], v[104:107], v[84:87]\n\t"
    "ds_read_b128 v[100:103], %[ea] offset:18432\n\t"
    "v_mfma_f32_16x16x32_bf16 v[88:91], v[168:171], v[108:111], v[88:91]\n\t"
    "v_mfma_f32_16x16x32_bf16 v[92:95], v[172:175], v[108:111], v[92:95]\n\t"
    "s_waitcnt lgkmcnt(4)\n\t"
    "v_mfma_f32_16x16x32_bf16 v[80:83], v[176:179], v[112:115], v[80:83]\n\t"
    "ds_read_b128 v[104:107], %[da] offset:16384\n\t"
    "v_mfma_f32_16x16x32_bf16 v[84:87], v[180:183], v[112:115], v[84:87]\n\t"
    "ds_read_b128 v[108:111], %[da] offset:18432\n\t"
    "v_mfma_f32_16x16x32_bf16 v[88:91], v[176:179], v[116:119], v[88:91]\n\t"
    "v_mfma_f32_16x16x32_bf16 v[92:95], v[180:183], v[116:119], v[92:95]\n\t"
    "s_waitcnt lgkmcnt(4)\n\t"
    "v_mfma_f32_16x16x32_bf16 v[80:83], v[184:187], v[120:123], v[80:83]\n\t"
    "ds_read_b128 v[112:115], %[ea] offset:20480\n\t"
    "v_mfma_f32_16x16x32_bf16 v[84:87], v[188:191], v[120:123], v[84:87]\n\t"
    "ds_read_b128 v[116:119], %[ea] offset:22528\n\t"
    "v_mfma_f32_16x16x32_bf16 v[88:91], v[184:187], v[124:127], v[88:91]\n\t"
    "v_mfma_f32_16x16x32_bf16 v[92:95], v[188:191], v[124:127], v[92:95]\n\t"
    "s_waitcnt lgkmcnt(4)\n\t"
    "v_mfma_f32_16x16x32_bf16 v[80:83], v[192:195], v[96:99], v[80:83]\n\t"
    "ds_read_b128 v[120:123], %[da] offset:20480\n\t"
    "v_mfma_f32_16x16x32_bf16 v[84:87], v[196:199], v[96:99], v[84:87]\n\t"
    "ds_read_b128 v[124:127], %[da] offset:22528\n\t"
    "v_mfma_f32_16x16x32_bf16 v[88:91], v[192:195], v[100:103], v[88:91]\n\t"
    "v_mfma_f32_16x16x32_bf16 v[92:95], v[196:199], v[100:103], v[92:95]\n\t"
    "s_barrier\n\t"
    "s_and_b32 %[st], %[x], 3\n\t"
    "s_cbranch_scc1 3f\n\t"
    "4:\n\t"
    "s_waitcnt lgkmcnt(4)\n\t"
    "v_mfma_f32_16x16x32_bf16 v[80:83], v[200:203], v[104:107], v[80:83]\n\t"
    "ds_read_b128 v[96:99], %[ea] offset:24576\n\t"
    "v_mfma_f32_16x16x32_bf16 v[84:87], v[204:207], v[104:107], v[84:87]\n\t"
    "ds_read_b128 v[100:103], %[ea] offset:26624\n\t"
    "v_mfma_f32_16x16x32_bf16 v[88:91], v[200:203], v[108:111], v[88:91]\n\t"
    "v_mfma_f32_16x16x32_bf16 v[92:95], v[204:207], v[108:111], v[92:95]\n\t"
    "s_waitcnt lgkmcnt(4)\n\t"
    "v_mfma_f32_16x16x32_bf16 v[80:83], v[208:211], v[112:115], v[80:83]\n\t"
    "ds_read_b128 v[104:107], %[da] offset:24576\n\t"
    "v_mfma_f32_16x16x32_bf16 v[84:87], v[212:215], v[112:115], v[84:87]\n\t"
    "ds_read_b128 v[108:111], %[da] offset:26624\n\t"
    "v_mfma_f32_16x16x32_bf16 v[88:91], v[208:211], v[116:119], v[88:91]\n\t"
    "v_mfma_f32_16x16x32_bf16 v[92:95], v[212:215], v[116:119], v[92:95]\n\t"
    "s_waitcnt lgkmcnt(4)\n\t"
    "v_mfma_f32_16x16x32_bf16 v[80:83], v[216:219], v[120:123], v[80:83]\n\t"
    "ds_read_b128 v[112:115], %[ea] offset:28672\n\t"
    "v_mfma_f32_16x16x32_bf16 v[84:87], v[220:223], v[120:123], v[84:87]\n\t"
    "ds_read_b128 v[116:119], %[ea] offset:30720\n\t"
    "v_mfma_f32_16x16x32_bf16 v[88:91], v[216:219], v[124:127], v[88:91]\n\t"
    "v_mfma_f32_16x16x32_bf16 v[92:95], v[220:223], v[124:127], v[92:95]\n\t"
    "s_waitcnt lgkmcnt(4)\n\t"
    "v_mfma_f32_16x16x32_bf16 v[80:83], v[224:227], v[96:99], v[80:83]\n\t"
    "ds_read_b128 v[120:123], %[da] offset:28672\n\t"
    "v_mfma_f32_16x16x32_bf16 v[84:87], v[228:231], v[96:99], v[84:87]\n\t"
    "ds_read_b128 v[124:127], %[da] offset:30720\n\t"
    "v_mfma_f32_16x16x32_bf16 v[88:91], v[224:227], v[100:103], v[88:91]\n\t"
    "v_mfma_f32_16x16x32_bf16 v[92:95], v[228:231], v[100:103], v[92:95]\n\t"
    "s_waitcnt lgkmcnt(4)\n\t"
    "v_mfma_f32_16x16x32_bf16 v[80:83], v[232:235], v[104:107], v[80:83]\n\t"
    "v_mfma_f32_16x16x32_bf16 v[84:87], v[236:239], v[104:107], v[84:87]\n\t"
    "v_mfma_f32_16x16x32_bf16 v[88:91], v[232:235], v[108:111], v[88:91]\n\t"
    "v_mfma_f32_16x16x32_bf16 v[92:95], v[236:239], v[108:111], v[92:95]\n\t"
    "s_waitcnt lgkmcnt(2)\n\t"
    "v_mfma_f32_16x16x32_bf16 v[80:83], v[240:243], v[112:115], v[80:83]\n\t"
    "v_mfma_f32_16x16x32_bf16 v[84:87], v[244:247], v[112:115], v[84:87]\n\t"
    "v_mfma_f32_16x16x32_bf16 v[88:91], v[240:243], v[116:119], v[88:91]\n\t"
    "v_mfma_f32_16x16x32_bf16 v[92:95], v[244:247], v[116:119], v[92:95]\n\t"
    "s_waitcnt lgkmcnt(0)\n\t"
    "v_mfma_f32_16x16x32_bf16 v[80:83], v[248:251], v[120:123], v[80:83]\n\t"
    "v_mfma_f32_16x16x32_bf16 v[84:87], v[252:255], v[120:123], v[84:87]\n\t"
    "v_mfma_f32_16x16x32_bf16 v[88:91], v[248:251], v[124:127], v[88:91]\n\t"
    "v_mfma_f32_16x16x32_bf16 v[92:95], v[252:255], v[124:127], v[92:95]\n\t"
    "s_nop 6\n\t"
    "s_branch 9f\n\t"
    "3:\n\t"
    "s_bitcmp0_b32 %[x], 0\n\t"
    "s_cbranch_scc1 5f\n\t"
    "7:\n\t"
    "v_mov_b32 v76, %[cn]\n\t"
    "v_mov_b32 v77, 1\n\t"
    "v_mov_b32 v78, %[pv]\n\t"
    "v_mov_b32 v79, 0\n\t"
    "s_waitcnt vmcnt(0)\n\t"
    "s_mov_b64 exec, 1\n\t"
    "ds_add_rtn_u32 v77, v76, v77\n\t"
    "s_waitcnt lgkmcnt(0)\n\t"
    "v_readfirstlane_b32 %[st], v77\n\t"
    "s_and_b32 %[st], %[st], 3\n\t"
    "s_cmp_lg_u32 %[st], 3\n\t"
    "s_cbranch_scc1 6f\n\t"
    "global_store_dword v79, v78, %[pg] sc1\n\t"
    "6:\n\t"
    "s_mov_b64 exec, -1\n\t"
    "s_bitcmp1_b32 %[x], 2\n\t"
    "s_cbranch_scc1 8b\n\t"
    "5:\n\t"
    "s_bitcmp0_b32 %[x], 1\n\t"
    "s_cbranch_scc1 4b\n\t"
    "s_mov_b32 m0, %[sl]\n\t"
    "s_nop 0\n\t"
    "global_load_lds_dword %[dv], %[db] sc1\n\t"
    "s_branch 4b\n\t"
    "9:"
    : "=&{v[80:95]}"(pd), [st] "=&s"(st)
    : [ea] "v"(ea), [da] "v"(da), [x] "s"(x), "{v[128:159]}"(W[0]), "{v[160:191]}"(W[1]), "{v[192:223]}"(W[2]), "{v[224:255]}"(W[3]), [pv] "s"(pv), [cn] "s"(cn), [sl] "s"(sl), [dv] "v"(dv), [db] "s"(db), [pg] "s"(pg)
    : "v96", "v97", "v98", "v99", "v100", "v101", "v102", "v103", "v104", "v105", "v106", "v107", "v108", "v109", "v110", "v111", "v112", "v113", "v114", "v115", "v116", "v117", "v118", "v119", "v120", "v121", "v122", "v123", "v124", "v125", "v126", "v127", "v76", "v77", "v78", "v79", "vcc", "scc", "m0", "memory");
}
#define LEAN_QB 1
__device__ __forceinline__ void paced_ld0(const u32x32 (&W)[4], unsigned ea, unsigned da, int b, int ns, unsigned ab, unsigned rw, unsigned fa,
    const char* p0, const char* p1, unsigned lb, int& cap, int wm, unsigned rq, unsigned mu, unsigned ad, unsigned dv, const char* db, unsigned sl, unsigned sa, const unsigned (&av)[8]) {
  unsigned st;
  asm volatile("s_setprio 2\n\t"
    "v_mov_b32 v78, %[wm]\n\t"
    "s_cmp_lg_u32 %[rq], 0\n\t"
    "s_cbranch_scc1 3f\n\t"
    "2:\n\t"
    "v_cmp_ge_i32 vcc, %[b], v78\n\t"
    "s_cbranch_vccz 4f\n\t"
    "21:\n\t"
    "s_cmp_eq_u32 %[sl], 0\n\t"
    "s_cbranch_scc1 71f\n\t"
    "s_mov_b32 m0, %[sl]\n\t"
    "s_nop 0\n\t"
    "global_load_lds_dword %[dv], %[db] sc1\n\t"
    "71:\n\t"
    "s_sub_u32 %[cap], %[cap], 1\n\t"
    "s_max_i32 %[cap], %[cap], 0\n\t"
    "s_waitcnt vmcnt(0)\n\t"
    "ds_read_b32 v79, %[sa]\n\t"
    "s_waitcnt lgkmcnt(0)\n\t"
    "v_mad_u32_u24 v79, v79, %[mu], %[ad]\n\t"
    "v_max_i32 v78, v78, v79\n\t"
    "v_cmp_ge_i32 vcc, %[b], v78\n\t"
    "s_cbranch_vccz 4f\n\t"
    "s_sleep 1\n\t"
    "s_cmp_lg_u32 %[cap], 0\n\t"
    "s_cbranch_scc1 21b\n\t"
    "4:\n\t"
    "s_add_u32 m0, %[lb], 0\n\t"
    "s_nop 0\n\t"
    "global_load_lds_dwordx4 %[av0], s[36:37] sc1\n\t"
    "s_add_u32 m0, %[lb], 32768\n\t"
    "s_nop 0\n\t"
    "global_load_lds_dwordx4 %[av1], s[36:37] sc1\n\t"
    "s_add_u32 m0, %[lb], 4096\n\t"
    "s_nop 0\n\t"
    "global_load_lds_dwordx4 %[av2], s[36:37] sc1\n\t"
    "s_add_u32 m0, %[lb], 36864\n\t"
    "s_nop 0\n\t"
    "global_load_lds_dwordx4 %[av3], s[36:37] sc1\n\t"
    "s_add_u32 m0, %[lb], 8192\n\t"
    "s_nop 0\n\t"
    "global_load_lds_dwordx4 %[av4], s[36:37] sc1\n\t"
    "s_add_u32 m0, %[lb], 40960\n\t"
    "s_nop 0\n\t"
    "global_load_lds_dwordx4 %[av5], s[36:37] sc1\n\t"
    "s_add_u32 m0, %[lb], 12288\n\t"
    "s_nop 0\n\t"
    "global_load_lds_dwordx4 %[av6], s[36:37] sc1\n\t"
    "s_add_u32 m0, %[lb], 45056\n\t"
    "s_nop 0\n\t"
    "global_load_lds_dwordx4 %[av7], s[36:37] sc1\n\t"
    "s_add_u32 m0, %[lb], 16384\n\t"
    "s_nop 0\n\t"
    "global_load_lds_dwordx4 %[av0], s[38:39] sc1\n\t"
    "s_add_u32 m0, %[lb], 49152\n\t"
    "s_nop 0\n\t"
    "global_load_lds_dwordx4 %[av1], s[38:39] sc1\n\t"
    "s_add_u32 m0, %[lb], 20480\n\t"
    "s_nop 0\n\t"
    "global_load_lds_dwordx4 %[av2], s[38:39] sc1\n\t"
    "s_add_u32 m0, %[lb], 53248\n\t"
    "s_nop 0\n\t"
    "global_load_lds_dwordx4 %[av3], s[38:39] sc1\n\t"
    "s_add_u32 m0, %[lb], 24576\n\t"
    "s_nop 0\n\t"
    "global_load_lds_dwordx4 %[av4], s[38:39] sc1\n\t"
    "s_add_u32 m0, %[lb], 57344\n\t"
    "s_nop 0\n\t"
    "global_load_lds_dwordx4 %[av5], s[38:39] sc1\n\t"
    "s_add_u32 m0, %[lb], 28672\n\t"
    "s_nop 0\n\t"
    "global_load_lds_dwordx4 %[av6], s[38:39] sc1\n\t"
    "s_add_u32 m0, %[lb], 61440\n\t"
    "s_nop 0\n\t"
    "global_load_lds_dwordx4 %[av7], s[38:39] sc1\n\t"
    "s_add_u32 s36, s36, %[ab]\n\t"
    "s_addc_u32 s37, s37, 0\n\t"
    "s_add_u32 s38, s38, %[ab]\n\t"
    "s_addc_u32 s39, s39, 0\n\t"
    "s_xor_b32 %[lb], %[lb], 0x10000\n\t"
    "3:\n\t"
    "s_add_u32 %[st], %[b], 1\n\t"
    "v_min_i32 v78, %[ns], v78\n\t"
    "v_cmp_ge_i32 vcc, %[st], v78\n\t"
    "s_waitcnt vmcnt(12) lgkmcnt(0)\n\t"
    "s_barrier\n\t"
    "ds_read_b128 v[96:99], %[ea] offset:0\n\t"
    "ds_read_b128 v[100:103], %[ea] offset:2048\n\t"
    "ds_read_b128 v[104:107], %[da] offset:0\n\t"
    "ds_read_b128 v[108:111], %[da] offset:2048\n\t"
    "ds_read_b128 v[112:115], %[ea] offset:4096\n\t"
    "ds_read_b128 v[116:119], %[ea] offset:6144\n\t"
    "s_cbranch_vccz 5f\n\t"
    "s_waitcnt lgkmcnt(4)\n\t"
    "v_mfma_f32_16x16x32_bf16 v[80:83], v[128:131], v[96:99], 0\n\t"
    "ds_read_b128 v[120:123], %[da] offset:4096\n\t"
    "v_mfma_f32_16x16x32_bf16 v[84:87], v[132:135], v[96:99], 0\n\t"
    "ds_read_b128 v[124:127], %[da] offset:6144\n\t"
    "v_mfma_f32_16x16x32_bf16 v[88:91], v[128:131], v[100:103], 0\n\t"
    "v_mfma_f32_16x16x32_bf16 v[92:95], v[132:135], v[100:103], 0\n\t"
    "s_waitcnt vmcnt(8)\n\t"
    "s_barrier\n\t"
    "s_waitcnt lgkmcnt(4)\n\t"
    "v_mfma_f32_16x16x32_bf16 v[80:83], v[136:139], v[104:107], v[80:83]\n\t"
    "ds_read_b128 v[96:99], %[ea] offset:8192\n\t"
    "v_mfma_f32_16x16x32_bf16 v[84:87], v[140:143], v[104:107], v[84:87]\n\t"
    "ds_read_b128 v[100:103], %[ea] offset:10240\n\t"
    "v_mfma_f32_16x16x32_bf16 v[88:91], v[136:139], v[108:111], v[88:91]\n\t"
    "v_mfma_f32_16x16x32_bf16 v[92:95], v[140:143], v[108:111], v[92:95]\n\t"
    "s_waitcnt lgkmcnt(4)\n\t"
    "v_mfma_f32_16x16x32_bf16 v[80:83], v[144:147], v[112:115], v[80:83]\n\t"
    "ds_read_b128 v[104:107], %[da] offset:8192\n\t"
    "v_mfma_f32_16x16x32_bf16 v[84:87], v[148:151], v[112:115], v[84:87]\n\t"
    "ds_read_b128 v[108:111], %[da] offset:10240\n\t"
    "v_mfma_f32_16x16x32_bf16 v[88:91], v[144:147], v[116:119], v[88:91]\n\t"
    "v_mfma_f32_16x16x32_bf16 v[92:95], v[148:151], v[116:119], v[92:95]\n\t"
    "s_waitcnt lgkmcnt(4)\n\t"
    "v_mfma_f32_16x16x32_bf16 v[80:83], v[152:155], v[120:123], v[80:83]\n\t"
    "ds_read_b128 v[112:115], %[ea] offset:12288\n\t"
    "v_mfma_f32_16x16x32_bf16 v[84:87], v[156:159], v[120:123], v[84:87]\n\t"
    "ds_read_b128 v[116:119], %[ea] offset:14336\n\t"
    "v_mfma_f32_16x16x32_bf16 v[88:91], v[152:155], v[124:127], v[88:91]\n\t"
    "v_mfma_f32_16x16x32_bf16 v[92:95], v[156:159], v[124:127], v[92:95]\n\t"
    "s_waitcnt lgkmcnt(4)\n\t"
    "v_mfma_f32_16x16x32_bf16 v[80:83], v[160:163], v[96:99], v[80:83]\n\t"
    "ds_read_b128 v[120:123], %[da] offset:12288\n\t"
    "v_mfma_f32_16x16x32_bf16 v[84:87], v[164:167], v[96:99], v[84:87]\n\t"
    "ds_read_b128 v[124:127], %[da] offset:14336\n\t"
    "v_mfma_f32_16x16x32_bf16 v[88:91], v[160:163], v[100:103], v[88:91]\n\t"
    "v_mfma_f32_16x16x32_bf16 v[92:95], v[164:167], v[100:103], v[92:95]\n\t"
    "s_waitcnt vmcnt(4)\n\t"
    "s_barrier\n\t"
    "s_waitcnt lgkmcnt(4)\n\t"
    "v_mfma_f32_16x16x32_bf16 v[80:83], v[168:171], v[104:107], v[80:83]\n\t"
    "ds_read_b128 v[96:99], %[ea] offset:16384\n\t"
    "v_mfma_f32_16x16x32_bf16 v[84:87], v[172:175], v[104:107], v[84:87]\n\t"
    "ds_read_b128 v[100:103], %[ea] offset:18432\n\t"
    "v_mfma_f32_16x16x32_bf16 v[88:91], v[168:171], v[108:111], v[88:91]\n\t"
    "v_mfma_f32_16x16x32_bf16 v[92:95], v[172:175], v[108:111], v[92:95]\n\t"
    "s_waitcnt lgkmcnt(4)\n\t"
    "v_mfma_f32_16x16x32_bf16 v[80:83], v[176:179], v[112:115], v[80:83]\n\t"
    "ds_read_b128 v[104:107], %[da] offset:16384\n\t"
    "v_mfma_f32_16x16x32_bf16 v[84:87], v[180:183], v[112:115], v[84:87]\n\t"
    "ds_read_b128 v[108:111], %[da] offset:18432\n\t"
    "v_mfma_f32_16x16x32_bf16 v[88:91], v[176:179], v[116:119], v[88:91]\n\t"
    "v_mfma_f32_16x16x32_bf16 v[92:95], v[180:183], v[116:119], v[92:95]\n\t"
    "s_waitcnt lgkmcnt(4)\n\t"
    "v_mfma_f32_16x16x32_bf16 v[80:83], v[184:187], v[120:123], v[80:83]\n\t"
    "ds_read_b128 v[112:115], %[ea] offset:20480\n\t"
    "v_mfma_f32_16x16x32_bf16 v[84:87], v[188:191], v[120:123], v[84:87]\n\t"
    "ds_read_b128 v[116:119], %[ea] offset:22528\n\t"
    "v_mfma_f32_16x16x32_bf16 v[88:91], v[184:187], v[124:127], v[88:91]\n\t"
    "v_mfma_f32_16x16x32_bf16 v[92:95], v[188:191], v[124:127], v[92:95]\n\t"
    "s_waitcnt lgkmcnt(4)\n\t"
    "v_mfma_f32_16x16x32_bf16 v[80:83], v[192:195], v[96:99], v[80:83]\n\t"
    "ds_read_b128 v[120:123], %[da] offset:20480\n\t"
    "v_mfma_f32_16x16x32_bf16 v[84:87], v[196:199], v[96:99], v[84:87]\n\t"
    "ds_read_b128 v[124:127], %[da] offset:22528\n\t"
    "v_mfma_f32_16x16x32_bf16 v[88:91], v[192:195], v[100:103], v[88:91]\n\t"
    "v_mfma_f32_16x16x32_bf16 v[92:95], v[196:199], v[100:103], v[92:95]\n\t"
    "s_waitcnt vmcnt(0)\n\t"
    "s_barrier\n\t"
    "s_waitcnt lgkmcnt(4)\n\t"
    "v_mfma_f32_16x16x32_bf16 v[80:83], v[200:203], v[104:107], v[80:83]\n\t"
    "ds_read_b128 v[96:99], %[ea] offset:24576\n\t"
    "v_mfma_f32_16x16x32_bf16 v[84:87], v[204:207], v[104:107], v[84:87]\n\t"
    "ds_read_b128 v[100:103], %[ea] offset:26624\n\t"
    "v_mfma_f32_16x16x32_bf16 v[88:91], v[200:203], v[108:111], v[88:91]\n\t"
    "v_mfma_f32_16x16x32_bf16 v[92:95], v[204:207], v[108:111], v[92:95]\n\t"
    "s_waitcnt lgkmcnt(4)\n\t"
    "v_mfma_f32_16x16x32_bf16 v[80:83], v[208:211], v[112:115], v[80:83]\n\t"
    "ds_read_b128 v[104:107], %[da] offset:24576\n\t"
    "v_mfma_f32_16x16x32_bf16 v[84:87], v[212:215], v[112:115], v[84:87]\n\t"
    "ds_read_b128 v[108:111], %[da] offset:26624\n\t"
    "v_mfma_f32_16x16x32_bf16 v[88:91], v[208:211], v[116:119], v[88:91]\n\t"
    "v_mfma_f32_16x16x32_bf16 v[92:95], v[212:215], v[116:119], v[92:95]\n\t"
    "s_cmp_eq_u32 %[sl], 0\n\t"
    "s_cbranch_scc1 72f\n\t"
    "s_mov_b32 m0, %[sl]\n\t"
    "s_nop 0\n\t"
    "global_load_lds_dword %[dv], %[db] sc1\n\t"
    "72:\n\t"
    "s_waitcnt lgkmcnt(4)\n\t"
    "v_mfma_f32_16x16x32_bf16 v[80:83], v[216:219], v[120:123], v[80:83]\n\t"
    "ds_read_b128 v[112:115], %[ea] offset:28672\n\t"
    "v_mfma_f32_16x16x32_bf16 v[84:87], v[220:223], v[120:123], v[84:87]\n\t"
    "ds_read_b128 v[116:119], %[ea] offset:30720\n\t"
    "v_mfma_f32_16x16x32_bf16 v[88:91], v[216:219], v[124:127], v[88:91]\n\t"
    "v_mfma_f32_16x16x32_bf16 v[92:95], v[220:223], v[124:127], v[92:95]\n\t"
    "s_waitcnt lgkmcnt(4)\n\t"
    "v_mfma_f32_16x16x32_bf16 v[80:83], v[224:227], v[96:99], v[80:83]\n\t"
    "ds_read_b128 v[120:123], %[da] offset:28672\n\t"
    "v_mfma_f32_16x16x32_bf16 v[84:87], v[228:231], v[96:99], v[84:87]\n\t"
    "ds_read_b128 v[124:127], %[da] offset:30720\n\t"
    "v_mfma_f32_16x16x32_bf16 v[88:91], v[224:227], v[100:103], v[88:91]\n\t"
    "v_mfma_f32_16x16x32_bf16 v[92:95], v[228:231], v[100:103], v[92:95]\n\t"
    "s_waitcnt lgkmcnt(4)\n\t"
    "v_mfma_f32_16x16x32_bf16 v[80:83], v[232:235], v[104:107], v[80:83]\n\t"
    "v_mfma_f32_16x16x32_bf16 v[84:87], v[236:239], v[104:107], v[84:87]\n\t"
    "v_mfma_f32_16x16x32_bf16 v[88:91], v[232:235], v[108:111], v[88:91]\n\t"
    "v_xor_b32 %[ea], 0x10000, %[ea]\n\t"
    "v_mfma_f32_16x16x32_bf16 v[92:95], v[236:239], v[108:111], v[92:95]\n\t"
    "v_xor_b32 %[da], 0x10000, %[da]\n\t"
    "s_waitcnt lgkmcnt(2)\n\t"
    "v_mfma_f32_16x16x32_bf16 v[80:83], v[240:243], v[112:115], v[80:83]\n\t"
    "v_mfma_f32_16x16x32_bf16 v[84:87], v[244:247], v[112:115], v[84:87]\n\t"
    "v_mfma_f32_16x16x32_bf16 v[88:91], v[240:243], v[116:119], v[88:91]\n\t"
    "s_add_u32 %[b], %[b], 1\n\t"
    "v_mfma_f32_16x16x32_bf16 v[92:95], v[244:247], v[116:119], v[92:95]\n\t"
    "s_waitcnt lgkmcnt(0)\n\t"
    "v_mfma_f32_16x16x32_bf16 v[80:83], v[248:251], v[120:123], v[80:83]\n\t"
    "v_mfma_f32_16x16x32_bf16 v[84:87], v[252:255], v[120:123], v[84:87]\n\t"
    "v_mfma_f32_16x16x32_bf16 v[88:91], v[248:251], v[124:127], v[88:91]\n\t"
    "v_mfma_f32_16x16x32_bf16 v[92:95], v[252:255], v[124:127], v[92:95]\n\t"
    "s_nop 4\n\t"
    "ds_write_b128 %[rw], v[80:83] offset:0\n\t"
    "ds_write_b128 %[rw], v[84:87] offset:1024\n\t"
    "ds_write_b128 %[rw], v[88:91] offset:2048\n\t"
    "ds_write_b128 %[rw], v[92:95] offset:3072\n\t"
    "s_waitcnt vmcnt(0)\n\t"
    "ds_read_b32 v79, %[sa]\n\t"
    "s_waitcnt lgkmcnt(0)\n\t"
    "v_mad_u32_u24 v79, v79, %[mu], %[ad]\n\t"
    "v_max_i32 v78, v78, v79\n\t"
    "v_mov_b32 v77, %[b]\n\t"
    "v_cmp_ge_i32 vcc, %[b], v78\n\t"
    "s_cmp_ge_i32 %[b], %[ns]\n\t"
    "s_cbranch_scc1 6f\n\t"
    "s_cbranch_vccnz 6f\n\t"
    "v_or_b32 v77, 0x100, v77\n\t"
    "ds_write_b32 %[fa], v77\n\t"
    "s_branch 4b\n\t"
    "6:\n\t"
    "ds_write_b32 %[fa], v77\n\t"
    "s_cbranch_scc0 21b\n\t"
    "s_branch 9f\n\t"
    "5:\n\t"
    "s_waitcnt lgkmcnt(4)\n\t"
    "v_mfma_f32_16x16x32_bf16 v[80:83], v[128:131], v[96:99], 0\n\t"
    "ds_read_b128 v[120:123], %[da] offset:4096\n\t"
    "s_add_u32 m0, %[lb], 0\n\t"
    "v_mfma_f32_16x16x32_bf16 v[84:87], v[132:135], v[96:99], 0\n\t"
    "ds_read_b128 v[124:127], %[da] offset:6144\n\t"
    "global_load_lds_dwordx4 %[av0], s[36:37] sc1\n\t"
    "v_mfma_f32_16x16x32_bf16 v[88:91], v[128:131], v[100:103], 0\n\t"
    "s_add_u32 m0, %[lb], 32768\n\t"
    "v_mfma_f32_16x16x32_bf16 v[92:95], v[132:135], v[100:103], 0\n\t"
    "global_load_lds_dwordx4 %[av1], s[36:37] sc1\n\t"
    "s_waitcnt vmcnt(10)\n\t"
    "s_barrier\n\t"
    "s_waitcnt lgkmcnt(4)\n\t"
    "v_mfma_f32_16x16x32_bf16 v[80:83], v[136:139], v[104:107], v[80:83]\n\t"
    "ds_read_b128 v[96:99], %[ea] offset:8192\n\t"
    "s_add_u32 m0, %[lb], 4096\n\t"
    "v_mfma_f32_16x16x32_bf16 v[84:87], v[140:143], v[104:107], v[84:87]\n\t"
    "ds_read_b128 v[100:103], %[ea] offset:10240\n\t"
    "global_load_lds_dwordx4 %[av2], s[36:37] sc1\n\t"
    "v_mfma_f32_16x16x32_bf16 v[88:91], v[136:139], v[108:111], v[88:91]\n\t"
    "s_add_u32 m0, %[lb], 36864\n\t"
    "v_mfma_f32_16x16x32_bf16 v[92:95], v[140:143], v[108:111], v[92:95]\n\t"
    "global_load_lds_dwordx4 %[av3], s[36:37] sc1\n\t"
    "s_waitcnt lgkmcnt(4)\n\t"
    "v_mfma_f32_16x16x32_bf16 v[80:83], v[144:147], v[112:115], v[80:83]\n\t"
    "ds_read_b128 v[104:107], %[da] offset:8192\n\t"
    "s_add_u32 m0, %[lb], 8192\n\t"
    "v_mfma_f32_16x16x32_bf16 v[84:87], v[148:151], v[112:115], v[84:87]\n\t"
    "ds_read_b128 v[108:111], %[da] offset:10240\n\t"
    "global_load_lds_dwordx4 %[av4], s[36:37] sc1\n\t"
    "v_mfma_f32_16x16x32_bf16 v[88:91], v[144:147], v[116:119], v[88:91]\n\t"
    "s_add_u32 m0, %[lb], 40960\n\t"
    "v_mfma_f32_16x16x32_bf16 v[92:95], v[148:151], v[116:119], v[92:95]\n\t"
    "global_load_lds_dwordx4 %[av5], s[36:37] sc1\n\t"
    "s_waitcnt lgkmcnt(4)\n\t"
    "v_mfma_f32_16x16x32_bf16 v[80:83], v[152:155], v[120:123], v[80:83]\n\t"
    "ds_read_b128 v[112:115], %[ea] offset:12288\n\t"
    "s_add_u32 m0, %[lb], 12288\n\t"
    "v_mfma_f32_16x16x32_bf16 v[84:87], v[156:159], v[120:123], v[84:87]\n\t"
    "ds_read_b128 v[116:119], %[ea] offset:14336\n\t"
    "global_load_lds_dwordx4 %[av6], s[36:37] sc1\n\t"
    "v_mfma_f32_16x16x32_bf16 v[88:91], v[152:155], v[124:127], v[88:91]\n\t"
    "s_add_u32 m0, %[lb], 45056\n\t"
    "v_mfma_f32_16x16x32_bf16 v[92:95], v[156:159], v[124:127], v[92:95]\n\t"
    "global_load_lds_dwordx4 %[av7], s[36:37] sc1\n\t"
    "s_waitcnt lgkmcnt(4)\n\t"
    "v_mfma_f32_16x16x32_bf16 v[80:83], v[160:163], v[96:99], v[80:83]\n\t"
    "ds_read_b128 v[120:123], %[da] offset:12288\n\t"
    "s_add_u32 m0, %[lb], 16384\n\t"
    "v_mfma_f32_16x16x32_bf16 v[84:87], v[164:167], v[96:99], v[84:87]\n\t"
    "ds_read_b128 v[124:127], %[da] offset:14336\n\t"
    "global_load_lds_dwordx4 %[av0], s[38:39] sc1\n\t"
    "v_mfma_f32_16x16x32_bf16 v[88:91], v[160:163], v[100:103], v[88:91]\n\t"
    "s_add_u32 m0, %[lb], 49152\n\t"
    "v_mfma_f32_16x16x32_bf16 v[92:95], v[164:167], v[100:103], v[92:95]\n\t"
    "global_load_lds_dwordx4 %[av1], s[38:39] sc1\n\t"
    "s_waitcnt vmcnt(14)\n\t"
    "s_barrier\n\t"
    "s_waitcnt lgkmcnt(4)\n\t"
    "v_mfma_f32_16x16x32_bf16 v[80:83], v[168:171], v[104:107], v[80:83]\n\t"
    "ds_read_b128 v[96:99], %[ea] offset:16384\n\t"
    "s_add_u32 m0, %[lb], 20480\n\t"
    "v_mfma_f32_16x16x32_bf16 v[84:87], v[172:175], v[104:107], v[84:87]\n\t"
    "ds_read_b128 v[100:103], %[ea] offset:18432\n\t"
    "global_load_lds_dwordx4 %[av2], s[38:39] sc1\n\t"
    "v_mfma_f32_16x16x32_bf16 v[88:91], v[168:171], v[108:111], v[88:91]\n\t"
    "s_add_u32 m0, %[lb], 53248\n\t"
    "v_mfma_f32_16x16x32_bf16 v[92:95], v[172:175], v[108:111], v[92:95]\n\t"
    "global_load_lds_dwordx4 %[av3], s[38:39] sc1\n\t"
    "s_waitcnt lgkmcnt(4)\n\t"
    "v_mfma_f32_16x16x32_bf16 v[80:83], v[176:179], v[112:115], v[80:83]\n\t"
    "ds_read_b128 v[104:107], %[da] offset:16384\n\t"
    "s_add_u32 m0, %[lb], 24576\n\t"
    "v_mfma_f32_16x16x32_bf16 v[84:87], v[180:183], v[112:115], v[84:87]\n\t"
    "ds_read_b128 v[108:111], %[da] offset:18432\n\t"
    "global_load_lds_dwordx4 %[av4], s[38:39] sc1\n\t"
    "v_mfma_f32_16x16x32_bf16 v[88:91], v[176:179], v[116:119], v[88:91]\n\t"
    "s_add_u32 m0, %[lb], 57344\n\t"
    "v_mfma_f32_16x16x32_bf16 v[92:95], v[180:183], v[116:119], v[92:95]\n\t"
    "global_load_lds_dwordx4 %[av5], s[38:39] sc1\n\t"
    "s_waitcnt lgkmcnt(4)\n\t"
    "v_mfma_f32_16x16x32_bf16 v[80:83], v[184:187], v[120:123], v[80:83]\n\t"
    "ds_read_b128 v[112:115], %[ea] offset:20480\n\t"
    "s_add_u32 m0, %[lb], 28672\n\t"
    "v_mfma_f32_16x16x32_bf16 v[84:87], v[188:191], v[120:123], v[84:87]\n\t"
    "ds_read_b128 v[116:119], %[ea] offset:22528\n\t"
    "global_load_lds_dwordx4 %[av6], s[38:39] sc1\n\t"
    "v_mfma_f32_16x16x32_bf16 v[88:91], v[184:187], v[124:127], v[88:91]\n\t"
    "s_add_u32 m0, %[lb], 61440\n\t"
    "v_mfma_f32_16x16x32_bf16 v[92:95], v[188:191], v[124:127], v[92:95]\n\t"
    "global_load_lds_dwordx4 %[av7], s[38:39] sc1\n\t"
    "s_waitcnt lgkmcnt(4)\n\t"
    "v_mfma_f32_16x16x32_bf16 v[80:83], v[192:195], v[96:99], v[80:83]\n\t"
    "ds_read_b128 v[120:123], %[da] offset:20480\n\t"
    "v_mfma_f32_16x16x32_bf16 v[84:87], v[196:199], v[96:99], v[84:87]\n\t"
    "ds_read_b128 v[124:127], %[da] offset:22528\n\t"
    "v_mfma_f32_16x16x32_bf16 v[88:91], v[192:195], v[100:103], v[88:91]\n\t"
    "v_mfma_f32_16x16x32_bf16 v[92:95], v[196:199], v[100:103], v[92:95]\n\t"
    "s_add_u32 s36, s36, %[ab]\n\t"
    "s_addc_u32 s37, s37, 0\n\t"
    "s_add_u32 s38, s38, %[ab]\n\t"
    "s_addc_u32 s39, s39, 0\n\t"
    "s_xor_b32 %[lb], %[lb], 0x10000\n\t"
    "s_waitcnt vmcnt(16)\n\t"
    "s_barrier\n\t"
    "s_waitcnt lgkmcnt(4)\n\t"
    "v_mfma_f32_16x16x32_bf16 v[80:83], v[200:203], v[104:107], v[80:83]\n\t"
    "ds_read_b128 v[96:99], %[ea] offset:24576\n\t"
    "v_mfma_f32_16x16x32_bf16 v[84:87], v[204:207], v[104:107], v[84:87]\n\t"
    "ds_read_b128 v[100:103], %[ea] offset:26624\n\t"
    "v_mfma_f32_16x16x32_bf16 v[88:91], v[200:203], v[108:111], v[88:91]\n\t"
    "v_mfma_f32_16x16x32_bf16 v[92:95], v[204:207], v[108:111], v[92:95]\n\t"
    "s_waitcnt lgkmcnt(4)\n\t"
    "v_mfma_f32_16x16x32_bf16 v[80:83], v[208:211], v[112:115], v[80:83]\n\t"
    "ds_read_b128 v[104:107], %[da] offset:24576\n\t"
    "v_mfma_f32_16x16x32_bf16 v[84:87], v[212:215], v[112:115], v[84:87]\n\t"
    "ds_read_b128 v[108:111], %[da] offset:26624\n\t"
    "v_mfma_f32_16x16x32_bf16 v[88:91], v[208:211], v[116:119], v[88:91]\n\t"
    "v_mfma_f32_16x16x32_bf16 v[92:95], v[212:215], v[116:119], v[92:95]\n\t"
    "s_cmp_eq_u32 %[sl], 0\n\t"
    "s_cbranch_scc1 73f\n\t"
    "s_mov_b32 m0, %[sl]\n\t"
    "s_nop 0\n\t"
    "global_load_lds_dword %[dv], %[db] sc1\n\t"
    "73:\n\t"
    "s_waitcnt lgkmcnt(4)\n\t"
    "v_mfma_f32_16x16x32_bf16 v[80:83], v[216:219], v[120:123], v[80:83]\n\t"
    "ds_read_b128 v[112:115], %[ea] offset:28672\n\t"
    "v_mfma_f32_16x16x32_bf16 v[84:87], v[220:223], v[120:123], v[84:87]\n\t"
    "ds_read_b128 v[116:119], %[ea] offset:30720\n\t"
    "v_mfma_f32_16x16x32_bf16 v[88:91], v[216:219], v[124:127], v[88:91]\n\t"
    "v_mfma_f32_16x16x32_bf16 v[92:95], v[220:223], v[124:127], v[92:95]\n\t"
    "s_waitcnt lgkmcnt(4)\n\t"
    "v_mfma_f32_16x16x32_bf16 v[80:83], v[224:227], v[96:99], v[80:83]\n\t"
    "ds_read_b128 v[120:123], %[da] offset:28672\n\t"
    "v_mfma_f32_16x16x32_bf16 v[84:87], v[228:231], v[96:99], v[84:87]\n\t"
    "ds_read_b128 v[124:127], %[da] offset:30720\n\t"
    "v_mfma_f32_16x16x32_bf16 v[88:91], v[224:227], v[100:103], v[88:91]\n\t"
    "v_mfma_f32_16x16x32_bf16 v[92:95], v[228:231], v[100:103], v[92:95]\n\t"
    "s_waitcnt lgkmcnt(4)\n\t"
    "v_mfma_f32_16x16x32_bf16 v[80:83], v[232:235], v[104:107], v[80:83]\n\t"
    "v_mfma_f32_16x16x32_bf16 v[84:87], v[236:239], v[104:107], v[84:87]\n\t"
    "v_mfma_f32_16x16x32_bf16 v[88:91], v[232:235], v[108:111], v[88:91]\n\t"
    "v_xor_b32 %[ea], 0x10000, %[ea]\n\t"
    "v_mfma_f32_16x16x32_bf16 v[92:95], v[236:239], v[108:111], v[92:95]\n\t"
    "v_xor_b32 %[da], 0x10000, %[da]\n\t"
    "s_waitcnt lgkmcnt(2)\n\t"
    "v_mfma_f32_16x16x32_bf16 v[80:83], v[240:243], v[112:115], v[80:83]\n\t"
    "v_mfma_f32_16x16x32_bf16 v[84:87], v[244:247], v[112:115], v[84:87]\n\t"
    "v_mfma_f32_16x16x32_bf16 v[88:91], v[240:243], v[116:119], v[88:91]\n\t"
    "s_add_u32 %[b], %[b], 1\n\t"
    "v_mfma_f32_16x16x32_bf16 v[92:95], v[244:247], v[116:119], v[92:95]\n\t"
    "s_waitcnt lgkmcnt(0)\n\t"
    "v_mfma_f32_16x16x32_bf16 v[80:83], v[248:251], v[120:123], v[80:83]\n\t"
    "v_mfma_f32_16x16x32_bf16 v[84:87], v[252:255], v[120:123], v[84:87]\n\t"
    "v_mfma_f32_16x16x32_bf16 v[88:91], v[248:251], v[124:127], v[88:91]\n\t"
    "v_mfma_f32_16x16x32_bf16 v[92:95], v[252:255], v[124:127], v[92:95]\n\t"
    "v_mov_b32 v77, %[b]\n\t"
    "v_or_b32 v77, 0x100, v77\n\t"
    "s_nop 2\n\t"
    "ds_write_b128 %[rw], v[80:83] offset:0\n\t"
    "ds_write_b128 %[rw], v[84:87] offset:1024\n\t"
    "ds_write_b128 %[rw], v[88:91] offset:2048\n\t"
    "ds_write_b128 %[rw], v[92:95] offset:3072\n\t"
    "ds_write_b32 %[fa], v77\n\t"
    "s_waitcnt vmcnt(0)\n\t"
    "ds_read_b32 v79, %[sa]\n\t"
    "s_waitcnt lgkmcnt(0)\n\t"
    "v_mad_u32_u24 v79, v79, %[mu], %[ad]\n\t"
    "v_max_i32 v78, v78, v79\n\t"
    "s_branch 3b\n\t"
    "9:\n\t"
    "s_waitcnt lgkmcnt(0)"
    : [ea] "+&v"(ea), [da] "+&v"(da), [b] "+&s"(b), [st] "=&s"(st), "+{s[36:37]}"(p0), "+{s[38:39]}"(p1), [lb] "+&s"(lb), [cap] "+&s"(cap)
    : [ns] "s"(ns), [ab] "s"(ab), [rw] "v"(rw), [fa] "v"(fa), "{v[128:159]}"(W[0]), "{v[160:191]}"(W[1]), "{v[192:223]}"(W[2]), "{v[224:255]}"(W[3]), [wm] "s"(wm), [rq] "s"(rq), [mu] "s"(mu), [ad] "v"(ad), [dv] "v"(dv), [db] "s"(db), [sl] "s"(sl), [sa] "v"(sa), [av0] "v"(av[0]), [av1] "v"(av[1]), [av2] "v"(av[2]), [av3] "v"(av[3]), [av4] "v"(av[4]), [av5] "v"(av[5]), [av6] "v"(av[6]), [av7] "v"(av[7])
    : "v77", "v78", "v79", "v80", "v81", "v82", "v83", "v84", "v85", "v86", "v87", "v88", "v89", "v90", "v91", "v92", "v93", "v94", "v95", "v96", "v97", "v98", "v99", "v100", "v101", "v102", "v103", "v104", "v105", "v106", "v107", "v108", "v109", "v110", "v111", "v112", "v113", "v114", "v115", "v116", "v117", "v118", "v119", "v120", "v121", "v122", "v123", "v124", "v125", "v126", "v127", "vcc", "scc", "m0", "memory");
}
__device__ __forceinline__ void paced_ld2(const u32x32 (&W)[4], unsigned ea, unsigned da, int b, int ns, unsigned ab, unsigned rw, unsigned fa,
    const char* p0, const char* p1, unsigned lb, int& cap, int wm, unsigned rq, unsigned mu, unsigned ad, unsigned dv, const char* db, unsigned sl, unsigned sa, const unsigned (&av)[8]) {
  unsigned st;
  asm volatile("s_setprio 2\n\t"
    "v_mov_b32 v78, %[wm]\n\t"
    "s_cmp_lg_u32 %[rq], 0\n\t"
    "s_cbranch_scc1 3f\n\t"
    "2:\n\t"
    "v_cmp_ge_i32 vcc, %[b], v78\n\t"
    "s_cbranch_vccz 4f\n\t"
    "21:\n\t"
    "s_cmp_eq_u32 %[sl], 0\n\t"
    "s_cbranch_scc1 71f\n\t"
    "s_mov_b32 m0, %[sl]\n\t"
    "s_nop 0\n\t"
    "global_load_lds_dword %[dv], %[db] sc1\n\t"
    "71:\n\t"
    "s_sub_u32 %[cap], %[cap], 1\n\t"
    "s_max_i32 %[cap], %[cap], 0\n\t"
    "s_waitcnt vmcnt(0)\n\t"
    "ds_read_b32 v79, %[sa]\n\t"
    "s_waitcnt lgkmcnt(0)\n\t"
    "v_mad_u32_u24 v79, v79, %[mu], %[ad]\n\t"
    "v_max_i32 v78, v78, v79\n\t"
    "v_cmp_ge_i32 vcc, %[b], v78\n\t"
    "s_cbranch_vccz 4f\n\t"
    "s_sleep 1\n\t"
    "s_cmp_lg_u32 %[cap], 0\n\t"
    "s_cbranch_scc1 21b\n\t"
    "4:\n\t"
    "s_add_u32 m0, %[lb], 0\n\t"
    "s_nop 0\n\t"
    "global_load_lds_dwordx4 %[av0], s[36:37] sc1\n\t"
    "s_add_u32 m0, %[lb], 4096\n\t"
    "s_nop 0\n\t"
    "global_load_lds_dwordx4 %[av2], s[36:37] sc1\n\t"
    "s_add_u32 m0, %[lb], 8192\n\t"
    "s_nop 0\n\t"
    "global_load_lds_dwordx4 %[av4], s[36:37] sc1\n\t"
    "s_add_u32 m0, %[lb], 12288\n\t"
    "s_nop 0\n\t"
    "global_load_lds_dwordx4 %[av6], s[36:37] sc1\n\t"
    "s_add_u32 m0, %[lb], 16384\n\t"
    "s_nop 0\n\t"
    "global_load_lds_dwordx4 %[av0], s[38:39] sc1\n\t"
    "s_add_u32 m0, %[lb], 20480\n\t"
    "s_nop 0\n\t"
    "global_load_lds_dwordx4 %[av2], s[38:39] sc1\n\t"
    "s_add_u32 m0, %[lb], 24576\n\t"
    "s_nop 0\n\t"
    "global_load_lds_dwordx4 %[av4], s[38:39] sc1\n\t"
    "s_add_u32 m0, %[lb], 28672\n\t"
    "s_nop 0\n\t"
    "global_load_lds_dwordx4 %[av6], s[38:39] sc1\n\t"
    "s_add_u32 s36, s36, %[ab]\n\t"
    "s_addc_u32 s37, s37, 0\n\t"
    "s_add_u32 s38, s38, %[ab]\n\t"
    "s_addc_u32 s39, s39, 0\n\t"
    "s_xor_b32 %[lb], %[lb], 0x10000\n\t"
    "3:\n\t"
    "s_add_u32 %[st], %[b], 1\n\t"
    "v_min_i32 v78, %[ns], v78\n\t"
    "v_cmp_ge_i32 vcc, %[st], v78\n\t"
    "s_waitcnt vmcnt(6) lgkmcnt(0)\n\t"
    "s_barrier\n\t"
    "ds_read_b128 v[96:99], %[ea] offset:0\n\t"
    "ds_read_b128 v[100:103], %[ea] offset:2048\n\t"
    "ds_read_b128 v[104:107], %[da] offset:0\n\t"
    "ds_read_b128 v[108:111], %[da] offset:2048\n\t"
    "ds_read_b128 v[112:115], %[ea] offset:4096\n\t"
    "ds_read_b128 v[116:119], %[ea] offset:6144\n\t"
    "s_cbranch_vccz 5f\n\t"
    "s_waitcnt lgkmcnt(4)\n\t"
    "v_mfma_f32_16x16x32_bf16 v[80:83], v[128:131], v[96:99], 0\n\t"
    "ds_read_b128 v[120:123], %[da] offset:4096\n\t"
    "v_mfma_f32_16x16x32_bf16 v[84:87], v[132:135], v[96:99], 0\n\t"
    "ds_read_b128 v[124:127], %[da] offset:6144\n\t"
    "v_mfma_f32_16x16x32_bf16 v[88:91], v[128:131], v[100:103], 0\n\t"
    "v_mfma_f32_16x16x32_bf16 v[92:95], v[132:135], v[100:103], 0\n\t"
    "s_waitcnt vmcnt(4)\n\t"
    "s_barrier\n\t"
    "s_waitcnt lgkmcnt(4)\n\t"
    "v_mfma_f32_16x16x32_bf16 v[80:83], v[136:139], v[104:107], v[80:83]\n\t"
    "ds_read_b128 v[96:99], %[ea] offset:8192\n\t"
    "v_mfma_f32_16x16x32_bf16 v[84:87], v[140:143], v[104:107], v[84:87]\n\t"
    "ds_read_b128 v[100:103], %[ea] offset:10240\n\t"
    "v_mfma_f32_16x16x32_bf16 v[88:91], v[136:139], v[108:111], v[88:91]\n\t"
    "v_mfma_f32_16x16x32_bf16 v[92:95], v[140:143], v[108:111], v[92:95]\n\t"
    "s_waitcnt lgkmcnt(4)\n\t"
    "v_mfma_f32_16x16x32_bf16 v[80:83], v[144:147], v[112:115], v[80:83]\n\t"
    "ds_read_b128 v[104:107], %[da] offset:8192\n\t"
    "v_mfma_f32_16x16x32_bf16 v[84:87], v[148:151], v[112:115], v[84:87]\n\t"
    "ds_read_b128 v[108:111], %[da] offset:10240\n\t"
    "v_mfma_f32_16x16x32_bf16 v[88:91], v[144:147], v[116:119], v[88:91]\n\t"
    "v_mfma_f32_16x16x32_bf16 v[92:95], v[148:151], v[116:119], v[92:95]\n\t"
    "s_waitcnt lgkmcnt(4)\n\t"
    "v_mfma_f32_16x16x32_bf16 v[80:83], v[152:155], v[120:123], v[80:83]\n\t"
    "ds_read_b128 v[112:115], %[ea] offset:12288\n\t"
    "v_mfma_f32_16x16x32_bf16 v[84:87], v[156:159], v[120:123], v[84:87]\n\t"
    "ds_read_b128 v[116:119], %[ea] offset:14336\n\t"
    "v_mfma_f32_16x16x32_bf16 v[88:91], v[152:155], v[124:127], v[88:91]\n\t"
    "v_mfma_f32_16x16x32_bf16 v[92:95], v[156:159], v[124:127], v[92:95]\n\t"
    "s_waitcnt lgkmcnt(4)\n\t"
    "v_mfma_f32_16x16x32_bf16 v[80:83], v[160:163], v[96:99], v[80:83]\n\t"
    "ds_read_b128 v[120:123], %[da] offset:12288\n\t"
    "v_mfma_f32_16x16x32_bf16 v[84:87], v[164:167], v[96:99], v[84:87]\n\t"
    "ds_read_b128 v[124:127], %[da] offset:14336\n\t"
    "v_mfma_f32_16x16x32_bf16 v[88:91], v[160:163], v[100:103], v[88:91]\n\t"
    "v_mfma_f32_16x16x32_bf16 v[92:95], v[164:167], v[100:103], v[92:95]\n\t"
    "s_waitcnt vmcnt(2)\n\t"
    "s_barrier\n\t"
    "s_waitcnt lgkmcnt(4)\n\t"
    "v_mfma_f32_16x16x32_bf16 v[80:83], v[168:171], v[104:107], v[80:83]\n\t"
    "ds_read_b128 v[96:99], %[ea] offset:16384\n\t"
    "v_mfma_f32_16x16x32_bf16 v[84:87], v[172:175], v[104:107], v[84:87]\n\t"
    "ds_read_b128 v[100:103], %[ea] offset:18432\n\t"
    "v_mfma_f32_16x16x32_bf16 v[88:91], v[168:171], v[108:111], v[88:91]\n\t"
    "v_mfma_f32_16x16x32_bf16 v[92:95], v[172:175], v[108:111], v[92:95]\n\t"
    "s_waitcnt lgkmcnt(4)\n\t"
    "v_mfma_f32_16x16x32_bf16 v[80:83], v[176:179], v[112:115], v[80:83]\n\t"
    "ds_read_b128 v[104:107], %[da] offset:16384\n\t"
    "v_mfma_f32_16x16x32_bf16 v[84:87], v[180:183], v[112:115], v[84:87]\n\t"
    "ds_read_b128 v[108:111], %[da] offset:18432\n\t"
    "v_mfma_f32_16x16x32_bf16 v[88:91], v[176:179], v[116:119], v[88:91]\n\t"
    "v_mfma_f32_16x16x32_bf16 v[92:95], v[180:183], v[116:119], v[92:95]\n\t"
    "s_waitcnt lgkmcnt(4)\n\t"
    "v_mfma_f32_16x16x32_bf16 v[80:83], v[184:187], v[120:123], v[80:83]\n\t"
    "ds_read_b128 v[112:115], %[ea] offset:20480\n\t"
    "v_mfma_f32_16x16x32_bf16 v[84:87], v[188:191], v[120:123], v[84:87]\n\t"
    "ds_read_b128 v[116:119], %[ea] offset:22528\n\t"
    "v_mfma_f32_16x16x32_bf16 v[88:91], v[184:187], v[124:127], v[88:91]\n\t"
    "v_mfma_f32_16x16x32_bf16 v[92:95], v[188:191], v[124:127], v[92:95]\n\t"
    "s_waitcnt lgkmcnt(4)\n\t"
    "v_mfma_f32_16x16x32_bf16 v[80:83], v[192:195], v[96:99], v[80:83]\n\t"
    "ds_read_b128 v[120:123], %[da] offset:20480\n\t"
    "v_mfma_f32_16x16x32_bf16 v[84:87], v[196:199], v[96:99], v[84:87]\n\t"
    "ds_read_b128 v[124:127], %[da] offset:22528\n\t"
    "v_mfma_f32_16x16x32_bf16 v[88:91], v[192:195], v[100:103], v[88:91]\n\t"
    "v_mfma_f32_16x16x32_bf16 v[92:95], v[196:199], v[100:103], v[92:95]\n\t"
    "s_waitcnt vmcnt(0)\n\t"
    "s_barrier\n\t"
    "s_waitcnt lgkmcnt(4)\n\t"
    "v_mfma_f32_16x16x32_bf16 v[80:83], v[200:203], v[104:107], v[80:83]\n\t"
    "ds_read_b128 v[96:99], %[ea] offset:24576\n\t"
    "v_mfma_f32_16x16x32_bf16 v[84:87], v[204:207], v[104:107], v[84:87]\n\t"
    "ds_read_b128 v[100:103], %[ea] offset:26624\n\t"
    "v_mfma_f32_16x16x32_bf16 v[88:91], v[200:203], v[108:111], v[88:91]\n\t"
    "v_mfma_f32_16x16x32_bf16 v[92:95], v[204:207], v[108:111], v[92:95]\n\t"
    "s_waitcnt lgkmcnt(4)\n\t"
    "v_mfma_f32_16x16x32_bf16 v[80:83], v[208:211], v[112:115], v[80:83]\n\t"
    "ds_read_b128 v[104:107], %[da] offset:24576\n\t"
    "v_mfma_f32_16x16x32_bf16 v[84:87], v[212:215], v[112:115], v[84:87]\n\t"
    "ds_read_b128 v[108:111], %[da] offset:26624\n\t"
    "v_mfma_f32_16x16x32_bf16 v[88:91], v[208:211], v[116:119], v[88:91]\n\t"
    "v_mfma_f32_16x16x32_bf16 v[92:95], v[212:215], v[116:119], v[92:95]\n\t"
    "s_cmp_eq_u32 %[sl], 0\n\t"
    "s_cbranch_scc1 72f\n\t"
    "s_mov_b32 m0, %[sl]\n\t"
    "s_nop 0\n\t"
    "global_load_lds_dword %[dv], %[db] sc1\n\t"
    "72:\n\t"
    "s_waitcnt lgkmcnt(4)\n\t"
    "v_mfma_f32_16x16x32_bf16 v[80:83], v[216:219], v[120:123], v[80:83]\n\t"
    "ds_read_b128 v[112:115], %[ea] offset:28672\n\t"
    "v_mfma_f32_16x16x32_bf16 v[84:87], v[220:223], v[120:123], v[84:87]\n\t"
    "ds_read_b128 v[116:119], %[ea] offset:30720\n\t"
    "v_mfma_f32_16x16x32_bf16 v[88:91], v[216:219], v[124:127], v[88:91]\n\t"
    "v_mfma_f32_16x16x32_bf16 v[92:95], v[220:223], v[124:127], v[92:95]\n\t"
    "s_waitcnt lgkmcnt(4)\n\t"
    "v_mfma_f32_16x16x32_bf16 v[80:83], v[224:227], v[96:99], v[80:83]\n\t"
    "ds_read_b128 v[120:123], %[da] offset:28672\n\t"
    "v_mfma_f32_16x16x32_bf16 v[84:87], v[228:231], v[96:99], v[84:87]\n\t"
    "ds_read_b128 v[124:127], %[da] offset:30720\n\t"
    "v_mfma_f32_16x16x32_bf16 v[88:91], v[224:227], v[100:103], v[88:91]\n\t"
    "v_mfma_f32_16x16x32_bf16 v[92:95], v[228:231], v[100:103], v[92:95]\n\t"
    "s_waitcnt lgkmcnt(4)\n\t"
    "v_mfma_f32_16x16x32_bf16 v[80:83], v[232:235], v[104:107], v[80:83]\n\t"
    "v_mfma_f32_16x16x32_bf16 v[84:87], v[236:239], v[104:107], v[84:87]\n\t"
    "v_mfma_f32_16x16x32_bf16 v[88:91], v[232:235], v[108:111], v[88:91]\n\t"
    "v_xor_b32 %[ea], 0x10000, %[ea]\n\t"
    "v_mfma_f32_16x16x32_bf16 v[92:95], v[236:239], v[108:111], v[92:95]\n\t"
    "v_xor_b32 %[da], 0x10000, %[da]\n\t"
    "s_waitcnt lgkmcnt(2)\n\t"
    "v_mfma_f32_16x16x32_bf16 v[80:83], v[240:243], v[112:115], v[80:83]\n\t"
    "v_mfma_f32_16x16x32_bf16 v[84:87], v[244:247], v[112:115], v[84:87]\n\t"
    "v_mfma_f32_16x16x32_bf16 v[88:91], v[240:243], v[116:119], v[88:91]\n\t"
    "s_add_u32 %[b], %[b], 1\n\t"
    "v_mfma_f32_16x16x32_bf16 v[92:95], v[244:247], v[116:119], v[92:95]\n\t"
    "s_waitcnt lgkmcnt(0)\n\t"
    "v_mfma_f32_16x16x32_bf16 v[80:83], v[248:251], v[120:123], v[80:83]\n\t"
    "v_mfma_f32_16x16x32_bf16 v[84:87], v[252:255], v[120:123], v[84:87]\n\t"
    "v_mfma_f32_16x16x32_bf16 v[88:91], v[248:251], v[124:127], v[88:91]\n\t"
    "v_mfma_f32_16x16x32_bf16 v[92:95], v[252:255], v[124:127], v[92:95]\n\t"
    "s_nop 4\n\t"
    "ds_write_b128 %[rw], v[80:83] offset:0\n\t"
    "ds_write_b128 %[rw], v[84:87] offset:1024\n\t"
    "ds_write_b128 %[rw], v[88:91] offset:2048\n\t"
    "ds_write_b128 %[rw], v[92:95] offset:3072\n\t"
    "s_waitcnt vmcnt(0)\n\t"
    "ds_read_b32 v79, %[sa]\n\t"
    "s_waitcnt lgkmcnt(0)\n\t"
    "v_mad_u32_u24 v79, v79, %[mu], %[ad]\n\t"
    "v_max_i32 v78, v78, v79\n\t"
    "v_mov_b32 v77, %[b]\n\t"
    "v_cmp_ge_i32 vcc, %[b], v78\n\t"
    "s_cmp_ge_i32 %[b], %[ns]\n\t"
    "s_cbranch_scc1 6f\n\t"
    "s_cbranch_vccnz 6f\n\t"
    "v_or_b32 v77, 0x100, v77\n\t"
    "ds_write_b32 %[fa], v77\n\t"
    "s_branch 4b\n\t"
    "6:\n\t"
    "ds_write_b32 %[fa], v77\n\t"
    "s_cbranch_scc0 21b\n\t"
    "s_branch 9f\n\t"
    "5:\n\t"
    "s_waitcnt lgkmcnt(4)\n\t"
    "v_mfma_f32_16x16x32_bf16 v[80:83], v[128:131], v[96:99], 0\n\t"
    "ds_read_b128 v[120:123], %[da] offset:4096\n\t"
    "s_add_u32 m0, %[lb], 0\n\t"
    "v_mfma_f32_16x16x32_bf16 v[84:87], v[132:135], v[96:99], 0\n\t"
    "ds_read_b128 v[124:127], %[da] offset:6144\n\t"
    "global_load_lds_dwordx4 %[av0], s[36:37] sc1\n\t"
    "v_mfma_f32_16x16x32_bf16 v[88:91], v[128:131], v[100:103], 0\n\t"
    "v_mfma_f32_16x16x32_bf16 v[92:95], v[132:135], v[100:103], 0\n\t"
    "s_waitcnt vmcnt(5)\n\t"
    "s_barrier\n\t"
    "s_waitcnt lgkmcnt(4)\n\t"
    "v_mfma_f32_16x16x32_bf16 v[80:83], v[136:139], v[104:107], v[80:83]\n\t"
    "ds_read_b128 v[96:99], %[ea] offset:8192\n\t"
    "s_add_u32 m0, %[lb], 4096\n\t"
    "v_mfma_f32_16x16x32_bf16 v[84:87], v[140:143], v[104:107], v[84:87]\n\t"
    "ds_read_b128 v[100:103], %[ea] offset:10240\n\t"
    "global_load_lds_dwordx4 %[av2], s[36:37] sc1\n\t"
    "v_mfma_f32_16x16x32_bf16 v[88:91], v[136:139], v[108:111], v[88:91]\n\t"
    "v_mfma_f32_16x16x32_bf16 v[92:95], v[140:143], v[108:111], v[92:95]\n\t"
    "s_waitcnt lgkmcnt(4)\n\t"
    "v_mfma_f32_16x16x32_bf16 v[80:83], v[144:147], v[112:115], v[80:83]\n\t"
    "ds_read_b128 v[104:107], %[da] offset:8192\n\t"
    "s_add_u32 m0, %[lb], 8192\n\t"
    "v_mfma_f32_16x16x32_bf16 v[84:87], v[148:151], v[112:115], v[84:87]\n\t"
    "ds_read_b128 v[108:111], %[da] offset:10240\n\t"
    "global_load_lds_dwordx4 %[av4], s[36:37] sc1\n\t"
    "v_mfma_f32_16x16x32_bf16 v[88:91], v[144:147], v[116:119], v[88:91]\n\t"
    "v_mfma_f32_16x16x32_bf16 v[92:95], v[148:151], v[116:119], v[92:95]\n\t"
    "s_waitcnt lgkmcnt(4)\n\t"
    "v_mfma_f32_16x16x32_bf16 v[80:83], v[152:155], v[120:123], v[80:83]\n\t"
    "ds_read_b128 v[112:115], %[ea] offset:12288\n\t"
    "s_add_u32 m0, %[lb], 12288\n\t"
    "v_mfma_f32_16x16x32_bf16 v[84:87], v[156:159], v[120:123], v[84:87]\n\t"
    "ds_read_b128 v[116:119], %[ea] offset:14336\n\t"
    "global_load_lds_dwordx4 %[av6], s[36:37] sc1\n\t"
    "v_mfma_f32_16x16x32_bf16 v[88:91], v[152:155], v[124:127], v[88:91]\n\t"
    "v_mfma_f32_16x16x32_bf16 v[92:95], v[156:159], v[124:127], v[92:95]\n\t"
    "s_waitcnt lgkmcnt(4)\n\t"
    "v_mfma_f32_16x16x32_bf16 v[80:83], v[160:163], v[96:99], v[80:83]\n\t"
    "ds_read_b128 v[120:123], %[da] offset:12288\n\t"
    "s_add_u32 m0, %[lb], 16384\n\t"
    "v_mfma_f32_16x16x32_bf16 v[84:87], v[164:167], v[96:99], v[84:87]\n\t"
    "ds_read_b128 v[124:127], %[da] offset:14336\n\t"
    "global_load_lds_dwordx4 %[av0], s[38:39] sc1\n\t"
    "v_mfma_f32_16x16x32_bf16 v[88:91], v[160:163], v[100:103], v[88:91]\n\t"
    "v_mfma_f32_16x16x32_bf16 v[92:95], v[164:167], v[100:103], v[92:95]\n\t"
    "s_waitcnt vmcnt(7)\n\t"
    "s_barrier\n\t"
    "s_waitcnt lgkmcnt(4)\n\t"
    "v_mfma_f32_16x16x32_bf16 v[80:83], v[168:171], v[104:107], v[80:83]\n\t"
    "ds_read_b128 v[96:99], %[ea] offset:16384\n\t"
    "s_add_u32 m0, %[lb], 20480\n\t"
    "v_mfma_f32_16x16x32_bf16 v[84:87], v[172:175], v[104:107], v[84:87]\n\t"
    "ds_read_b128 v[100:103], %[ea] offset:18432\n\t"
    "global_load_lds_dwordx4 %[av2], s[38:39] sc1\n\t"
    "v_mfma_f32_16x16x32_bf16 v[88:91], v[168:171], v[108:111], v[88:91]\n\t"
    "v_mfma_f32_16x16x32_bf16 v[92:95], v[172:175], v[108:111], v[92:95]\n\t"
    "s_waitcnt lgkmcnt(4)\n\t"
    "v_mfma_f32_16x16x32_bf16 v[80:83], v[176:179], v[112:115], v[80:83]\n\t"
    "ds_read_b128 v[104:107], %[da] offset:16384\n\t"
    "s_add_u32 m0, %[lb], 24576\n\t"
    "v_mfma_f32_16x16x32_bf16 v[84:87], v[180:183], v[112:115], v[84:87]\n\t"
    "ds_read_b128 v[108:111], %[da] offset:18432\n\t"
    "global_load_lds_dwordx4 %[av4], s[38:39] sc1\n\t"
    "v_mfma_f32_16x16x32_bf16 v[88:91], v[176:179], v[116:119], v[88:91]\n\t"
    "v_mfma_f32_16x16x32_bf16 v[92:95], v[180:183], v[116:119], v[92:95]\n\t"
    "s_waitcnt lgkmcnt(4)\n\t"
    "v_mfma_f32_16x16x32_bf16 v[80:83], v[184:187], v[120:123], v[80:83]\n\t"
    "ds_read_b128 v[112:115], %[ea] offset:20480\n\t"
    "s_add_u32 m0, %[lb], 28672\n\t"
    "v_mfma_f32_16x16x32_bf16 v[84:87], v[188:191], v[120:123], v[84:87]\n\t"
    "ds_read_b128 v[116:119], %[ea] offset:22528\n\t"
    "global_load_lds_dwordx4 %[av6], s[38:39] sc1\n\t"
    "v_mfma_f32_16x16x32_bf16 v[88:91], v[184:187], v[124:127], v[88:91]\n\t"
    "v_mfma_f32_16x16x32_bf16 v[92:95], v[188:191], v[124:127], v[92:95]\n\t"
    "s_waitcnt lgkmcnt(4)\n\t"
    "v_mfma_f32_16x16x32_bf16 v[80:83], v[192:195], v[96:99], v[80:83]\n\t"
    "ds_read_b128 v[120:123], %[da] offset:20480\n\t"
    "v_mfma_f32_16x16x32_bf16 v[84:87], v[196:199], v[96:99], v[84:87]\n\t"
    "ds_read_b128 v[124:127], %[da] offset:22528\n\t"
    "v_mfma_f32_16x16x32_bf16 v[88:91], v[192:195], v[100:103], v[88:91]\n\t"
    "v_mfma_f32_16x16x32_bf16 v[92:95], v[196:199], v[100:103], v[92:95]\n\t"
    "s_add_u32 s36, s36, %[ab]\n\t"
    "s_addc_u32 s37, s37, 0\n\t"
    "s_add_u32 s38, s38, %[ab]\n\t"
    "s_addc_u32 s39, s39, 0\n\t"
    "s_xor_b32 %[lb], %[lb], 0x10000\n\t"
    "s_waitcnt vmcnt(8)\n\t"
    "s_barrier\n\t"
    "s_waitcnt lgkmcnt(4)\n\t"
    "v_mfma_f32_16x16x32_bf16 v[80:83], v[200:203], v[104:107], v[80:83]\n\t"
    "ds_read_b128 v[96:99], %[ea] offset:24576\n\t"
    "v_mfma_f32_16x16x32_bf16 v[84:87], v[204:207], v[104:107], v[84:87]\n\t"
    "ds_read_b128 v[100:103], %[ea] offset:26624\n\t"
    "v_mfma_f32_16x16x32_bf16 v[88:91], v[200:203], v[108:111], v[88:91]\n\t"
    "v_mfma_f32_16x16x32_bf16 v[92:95], v[204:207], v[108:111], v[92:95]\n\t"
    "s_waitcnt lgkmcnt(4)\n\t"
    "v_mfma_f32_16x16x32_bf16 v[80:83], v[208:211], v[112:115], v[80:83]\n\t"
    "ds_read_b128 v[104:107], %[da] offset:24576\n\t"
    "v_mfma_f32_16x16x32_bf16 v[84:87], v[212:215], v[112:115], v[84:87]\n\t"
    "ds_read_b128 v[108:111], %[da] offset:26624\n\t"
    "v_mfma_f32_16x16x32_bf16 v[88:91], v[208:211], v[116:119], v[88:91]\n\t"
    "v_mfma_f32_16x16x32_bf16 v[92:95], v[212:215], v[116:119], v[92:95]\n\t"
    "s_cmp_eq_u32 %[sl], 0\n\t"
    "s_cbranch_scc1 73f\n\t"
    "s_mov_b32 m0, %[sl]\n\t"
    "s_nop 0\n\t"
    "global_load_lds_dword %[dv], %[db] sc1\n\t"
    "73:\n\t"
    "s_waitcnt lgkmcnt(4)\n\t"
    "v_mfma_f32_16x16x32_bf16 v[80:83], v[216:219], v[120:123], v[80:83]\n\t"
    "ds_read_b128 v[112:115], %[ea] offset:28672\n\t"
    "v_mfma_f32_16x16x32_bf16 v[84:87], v[220:223], v[120:123], v[84:87]\n\t"
    "ds_read_b128 v[116:119], %[ea] offset:30720\n\t"
    "v_mfma_f32_16x16x32_bf16 v[88:91], v[216:219], v[124:127], v[88:91]\n\t"
    "v_mfma_f32_16x16x32_bf16 v[92:95], v[220:223], v[124:127], v[92:95]\n\t"
    "s_waitcnt lgkmcnt(4)\n\t"
    "v_mfma_f32_16x16x32_bf16 v[80:83], v[224:227], v[96:99], v[80:83]\n\t"
    "ds_read_b128 v[120:123], %[da] offset:28672\n\t"
    "v_mfma_f32_16x16x32_bf16 v[84:87], v[228:231], v[96:99], v[84:87]\n\t"
    "ds_read_b128 v[124:127], %[da] offset:30720\n\t"
    "v_mfma_f32_16x16x32_bf16 v[88:91], v[224:227], v[100:103], v[88:91]\n\t"
    "v_mfma_f32_16x16x32_bf16 v[92:95], v[228:231], v[100:103], v[92:95]\n\t"
    "s_waitcnt lgkmcnt(4)\n\t"
    "v_mfma_f32_16x16x32_bf16 v[80:83], v[232:235], v[104:107], v[80:83]\n\t"
    "v_mfma_f32_16x16x32_bf16 v[84:87], v[236:239], v[104:107], v[84:87]\n\t"
    "v_mfma_f32_16x16x32_bf16 v[88:91], v[232:235], v[108:111], v[88:91]\n\t"
    "v_xor_b32 %[ea], 0x10000, %[ea]\n\t"
    "v_mfma_f32_16x16x32_bf16 v[92:95], v[236:239], v[108:111], v[92:95]\n\t"
    "v_xor_b32 %[da], 0x10000, %[da]\n\t"
    "s_waitcnt lgkmcnt(2)\n\t"
    "v_mfma_f32_16x16x32_bf16 v[80:83], v[240:243], v[112:115], v[80:83]\n\t"
    "v_mfma_f32_16x16x32_bf16 v[84:87], v[244:247], v[112:115], v[84:87]\n\t"
    "v_mfma_f32_16x16x32_bf16 v[88:91], v[240:243], v[116:119], v[88:91]\n\t"
    "s_add_u32 %[b], %[b], 1\n\t"
    "v_mfma_f32_16x16x32_bf16 v[92:95], v[244:247], v[116:119], v[92:95]\n\t"
    "s_waitcnt lgkmcnt(0)\n\t"
    "v_mfma_f32_16x16x32_bf16 v[80:83], v[248:251], v[120:123], v[80:83]\n\t"
    "v_mfma_f32_16x16x32_bf16 v[84:87], v[252:255], v[120:123], v[84:87]\n\t"
    "v_mfma_f32_16x16x32_bf16 v[88:91], v[248:251], v[124:127], v[88:91]\n\t"
    "v_mfma_f32_16x16x32_bf16 v[92:95], v[252:255], v[124:127], v[92:95]\n\t"
    "v_mov_b32 v77, %[b]\n\t"
    "v_or_b32 v77, 0x100, v77\n\t"
    "s_nop 2\n\t"
    "ds_write_b128 %[rw], v[80:83] offset:0\n\t"
    "ds_write_b128 %[rw], v[84:87] offset:1024\n\t"
    "ds_write_b128 %[rw], v[88:91] offset:2048\n\t"
    "ds_write_b128 %[rw], v[92:95] offset:3072\n\t"
    "ds_write_b32 %[fa], v77\n\t"
    "s_waitcnt vmcnt(0)\n\t"
    "ds_read_b32 v79, %[sa]\n\t"
    "s_waitcnt lgkmcnt(0)\n\t"
    "v_mad_u32_u24 v79, v79, %[mu], %[ad]\n\t"
    "v_max_i32 v78, v78, v79\n\t"
    "s_branch 3b\n\t"
    "9:\n\t"
    "s_waitcnt lgkmcnt(0)"
    : [ea] "+&v"(ea), [da] "+&v"(da), [b] "+&s"(b), [st] "=&s"(st), "+{s[36:37]}"(p0), "+{s[38:39]}"(p1), [lb] "+&s"(lb), [cap] "+&s"(cap)
    : [ns] "s"(ns), [ab] "s"(ab), [rw] "v"(rw), [fa] "v"(fa), "{v[128:159]}"(W[0]), "{v[160:191]}"(W[1]), "{v[192:223]}"(W[2]), "{v[224:255]}"(W[3]), [wm] "s"(wm), [rq] "s"(rq), [mu] "s"(mu), [ad] "v"(ad), [dv] "v"(dv), [db] "s"(db), [sl] "s"(sl), [sa] "v"(sa), [av0] "v"(av[0]), [av1] "v"(av[1]), [av2] "v"(av[2]), [av3] "v"(av[3]), [av4] "v"(av[4]), [av5] "v"(av[5]), [av6] "v"(av[6]), [av7] "v"(av[7])
    : "v77", "v78", "v79", "v80", "v81", "v82", "v83", "v84", "v85", "v86", "v87", "v88", "v89", "v90", "v91", "v92", "v93", "v94", "v95", "v96", "v97", "v98", "v99", "v100", "v101", "v102", "v103", "v104", "v105", "v106", "v107", "v108", "v109", "v110", "v111", "v112", "v113", "v114", "v115", "v116", "v117", "v118", "v119", "v120", "v121", "v122", "v123", "v124", "v125", "v126", "v127", "vcc", "scc", "m0", "memory");
}
__device__ __forceinline__ void paced_st0(const u32x32 (&W)[4], unsigned ea, unsigned da, int b, int ns, unsigned ab, unsigned rw, unsigned fa,
    const char* p0, unsigned pp, int po, unsigned cn, const char* pg, unsigned co0, unsigned co1) {
  unsigned st;
  asm volatile("1:\n\t"
    "s_barrier\n\t"
    "ds_read_b128 v[96:99], %[ea] offset:0\n\t"
    "ds_read_b128 v[100:103], %[ea] offset:2048\n\t"
    "ds_read_b128 v[104:107], %[da] offset:0\n\t"
    "ds_read_b128 v[108:111], %[da] offset:2048\n\t"
    "ds_read_b128 v[112:115], %[ea] offset:4096\n\t"
    "ds_read_b128 v[116:119], %[ea] offset:6144\n\t"
    "s_waitcnt lgkmcnt(4)\n\t"
    "v_mfma_f32_16x16x32_bf16 v[80:83], v[128:131], v[96:99], 0\n\t"
    "ds_read_b128 v[120:123], %[da] offset:4096\n\t"
    "v_mfma_f32_16x16x32_bf16 v[84:87], v[132:135], v[96:99], 0\n\t"
    "ds_read_b128 v[124:127], %[da] offset:6144\n\t"
    "v_mfma_f32_16x16x32_bf16 v[88:91], v[128:131], v[100:103], 0\n\t"
    "v_mfma_f32_16x16x32_bf16 v[92:95], v[132:135], v[100:103], 0\n\t"
    "s_barrier\n\t"
    "s_waitcnt lgkmcnt(4)\n\t"
    "v_mfma_f32_16x16x32_bf16 v[80:83], v[136:139], v[104:107], v[80:83]\n\t"
    "ds_read_b128 v[96:99], %[ea] offset:8192\n\t"
    "v_mfma_f32_16x16x32_bf16 v[84:87], v[140:143], v[104:107], v[84:87]\n\t"
    "ds_read_b128 v[100:103], %[ea] offset:10240\n\t"
    "v_mfma_f32_16x16x32_bf16 v[88:91], v[136:139], v[108:111], v[88:91]\n\t"
    "v_mfma_f32_16x16x32_bf16 v[92:95], v[140:143], v[108:111], v[92:95]\n\t"
    "s_waitcnt lgkmcnt(4)\n\t"
    "v_mfma_f32_16x16x32_bf16 v[80:83], v[144:147], v[112:115], v[80:83]\n\t"
    "ds_read_b128 v[104:107], %[da] offset:8192\n\t"
    "v_mfma_f32_16x16x32_bf16 v[84:87], v[148:151], v[112:115], v[84:87]\n\t"
    "ds_read_b128 v[108:111], %[da] offset:10240\n\t"
    "v_mfma_f32_16x16x32_bf16 v[88:91], v[144:147], v[116:119], v[88:91]\n\t"
    "v_mfma_f32_16x16x32_bf16 v[92:95], v[148:151], v[116:119], v[92:95]\n\t"
    "s_waitcnt lgkmcnt(4)\n\t"
    "v_mfma_f32_16x16x32_bf16 v[80:83], v[152:155], v[120:123], v[80:83]\n\t"
    "ds_read_b128 v[112:115], %[ea] offset:12288\n\t"
    "v_mfma_f32_16x16x32_bf16 v[84:87], v[156:159], v[120:123], v[84:87]\n\t"
    "ds_read_b128 v[116:119], %[ea] offset:14336\n\t"
    "v_mfma_f32_16x16x32_bf16 v[88:91], v[152:155], v[124:127], v[88:91]\n\t"
    "v_mfma_f32_16x16x32_bf16 v[92:95], v[156:159], v[124:127], v[92:95]\n\t"
    "s_cmp_lg_u32 %[pp], 0\n\t"
    "s_cbranch_scc1 7f\n\t"
    "8:\n\t"
    "s_waitcnt lgkmcnt(4)\n\t"
    "v_mfma_f32_16x16x32_bf16 v[80:83], v[160:163], v[96:99], v[80:83]\n\t"
    "ds_read_b128 v[120:123], %[da] offset:12288\n\t"
    "v_mfma_f32_16x16x32_bf16 v[84:87], v[164:167], v[96:99], v[84:87]\n\t"
    "ds_read_b128 v[124:127], %[da] offset:14336\n\t"
    "v_mfma_f32_16x16x32_bf16 v[88:91], v[160:163], v[100:103], v[88:91]\n\t"
    "v_mfma_f32_16x16x32_bf16 v[92:95], v[164:167], v[100:103], v[92:95]\n\t"
    "s_barrier\n\t"
    "s_waitcnt lgkmcnt(4)\n\t"
    "v_mfma_f32_16x16x32_bf16 v[80:83], v[168:171], v[104:107], v[80:83]\n\t"
    "ds_read_b128 v[96:99], %[ea] offset:16384\n\t"
    "v_mfma_f32_16x16x32_bf16 v[84:87], v[172:175], v[104:107], v[84:87]\n\t"
    "ds_read_b128 v[100:103], %[ea] offset:18432\n\t"
    "v_mfma_f32_16x16x32_bf16 v[88:91], v[168:171], v[108:111], v[88:91]\n\t"
    "v_mfma_f32_16x16x32_bf16 v[92:95], v[172:175], v[108:111], v[92:95]\n\t"
    "s_waitcnt lgkmcnt(4)\n\t"
    "v_mfma_f32_16x16x32_bf16 v[80:83], v[176:179], v[112:115], v[80:83]\n\t"
    "ds_read_b128 v[104:107], %[da] offset:16384\n\t"
    "v_mfma_f32_16x16x32_bf16 v[84:87], v[180:183], v[112:115], v[84:87]\n\t"
    "ds_read_b128 v[108:111], %[da] offset:18432\n\t"
    "v_mfma_f32_16x16x32_bf16 v[88:91], v[176:179], v[116:119], v[88:91]\n\t"
    "v_mfma_f32_16x16x32_bf16 v[92:95], v[180:183], v[116:119], v[92:95]\n\t"
    "s_waitcnt lgkmcnt(4)\n\t"
    "v_mfma_f32_16x16x32_bf16 v[80:83], v[184:187], v[120:123], v[80:83]\n\t"
    "ds_read_b128 v[112:115], %[ea] offset:20480\n\t"
    "v_mfma_f32_16x16x32_bf16 v[84:87], v[188:191], v[120:123], v[84:87]\n\t"
    "ds_read_b128 v[116:119], %[ea] offset:22528\n\t"
    "v_mfma_f32_16x16x32_bf16 v[88:91], v[184:187], v[124:127], v[88:91]\n\t"
    "v_mfma_f32_16x16x32_bf16 v[92:95], v[188:191], v[124:127], v[92:95]\n\t"
    "s_waitcnt lgkmcnt(4)\n\t"
    "v_mfma_f32_16x16x32_bf16 v[80:83], v[192:195], v[96:99], v[80:83]\n\t"
    "ds_read_b128 v[120:123], %[da] offset:20480\n\t"
    "v_mfma_f32_16x16x32_bf16 v[84:87], v[196:199], v[96:99], v[84:87]\n\t"
    "ds_read_b128 v[124:127], %[da] offset:22528\n\t"
    "v_mfma_f32_16x16x32_bf16 v[88:91], v[192:195], v[100:103], v[88:91]\n\t"
    "v_mfma_f32_16x16x32_bf16 v[92:95], v[196:199], v[100:103], v[92:95]\n\t"
    "s_barrier\n\t"
    "s_waitcnt lgkmcnt(4)\n\t"
    "v_mfma_f32_16x16x32_bf16 v[80:83], v[200:203], v[104:107], v[80:83]\n\t"
    "ds_read_b128 v[96:99], %[ea] offset:24576\n\t"
    "v_mfma_f32_16x16x32_bf16 v[84:87], v[204:207], v[104:107], v[84:87]\n\t"
    "ds_read_b128 v[100:103], %[ea] offset:26624\n\t"
    "v_mfma_f32_16x16x32_bf16 v[88:91], v[200:203], v[108:111], v[88:91]\n\t"
    "v_mfma_f32_16x16x32_bf16 v[92:95], v[204:207], v[108:111], v[92:95]\n\t"
    "s_waitcnt lgkmcnt(4)\n\t"
    "v_mfma_f32_16x16x32_bf16 v[80:83], v[208:211], v[112:115], v[80:83]\n\t"
    "ds_read_b128 v[104:107], %[da] offset:24576\n\t"
    "v_mfma_f32_16x16x32_bf16 v[84:87], v[212:215], v[112:115], v[84:87]\n\t"
    "ds_read_b128 v[108:111], %[da] offset:26624\n\t"
    "v_mfma_f32_16x16x32_bf16 v[88:91], v[208:211], v[116:119], v[88:91]\n\t"
    "v_mfma_f32_16x16x32_bf16 v[92:95], v[212:215], v[116:119], v[92:95]\n\t"
    "s_waitcnt lgkmcnt(4)\n\t"
    "v_mfma_f32_16x16x32_bf16 v[80:83], v[216:219], v[120:123], v[80:83]\n\t"
    "ds_read_b128 v[112:115], %[ea] offset:28672\n\t"
    "v_mfma_f32_16x16x32_bf16 v[84:87], v[220:223], v[120:123], v[84:87]\n\t"
    "ds_read_b128 v[116:119], %[ea] offset:30720\n\t"
    "v_mfma_f32_16x16x32_bf16 v[88:91], v[216:219], v[124:127], v[88:91]\n\t"
    "v_mfma_f32_16x16x32_bf16 v[92:95], v[220:223], v[124:127], v[92:95]\n\t"
    "s_waitcnt lgkmcnt(4)\n\t"
    "v_mfma_f32_16x16x32_bf16 v[80:83], v[224:227], v[96:99], v[80:83]\n\t"
    "ds_read_b128 v[120:123], %[da] offset:28672\n\t"
    "v_mfma_f32_16x16x32_bf16 v[84:87], v[228:231], v[96:99], v[84:87]\n\t"
    "ds_read_b128 v[124:127], %[da] offset:30720\n\t"
    "v_mfma_f32_16x16x32_bf16 v[88:91], v[224:227], v[100:103], v[88:91]\n\t"
    "v_mfma_f32_16x16x32_bf16 v[92:95], v[228:231], v[100:103], v[92:95]\n\t"
    "s_waitcnt lgkmcnt(4)\n\t"
    "v_mfma_f32_16x16x32_bf16 v[80:83], v[232:235], v[104:107], v[80:83]\n\t"
    "v_mfma_f32_16x16x32_bf16 v[84:87], v[236:239], v[104:107], v[84:87]\n\t"
    "v_mfma_f32_16x16x32_bf16 v[88:91], v[232:235], v[108:111], v[88:91]\n\t"
    "v_xor_b32 %[ea], 0x10000, %[ea]\n\t"
    "v_mfma_f32_16x16x32_bf16 v[92:95], v[236:239], v[108:111], v[92:95]\n\t"
    "v_xor_b32 %[da], 0x10000, %[da]\n\t"
    "s_waitcnt lgkmcnt(2)\n\t"
    "v_mfma_f32_16x16x32_bf16 v[80:83], v[240:243], v[112:115], v[80:83]\n\t"
    "v_mfma_f32_16x16x32_bf16 v[84:87], v[244:247], v[112:115], v[84:87]\n\t"
    "v_mfma_f32_16x16x32_bf16 v[88:91], v[240:243], v[116:119], v[88:91]\n\t"
    "s_add_u32 %[b], %[b], 1\n\t"
    "v_mfma_f32_16x16x32_bf16 v[92:95], v[244:247], v[116:119], v[92:95]\n\t"
    "s_waitcnt lgkmcnt(0)\n\t"
    "v_mfma_f32_16x16x32_bf16 v[80:83], v[248:251], v[120:123], v[80:83]\n\t"
    "v_mfma_f32_16x16x32_bf16 v[84:87], v[252:255], v[120:123], v[84:87]\n\t"
    "v_mfma_f32_16x16x32_bf16 v[88:91], v[248:251], v[124:127], v[88:91]\n\t"
    "v_mfma_f32_16x16x32_bf16 v[92:95], v[252:255], v[124:127], v[92:95]\n\t"
    "2:\n\t"
    "ds_read_b32 v39, %[fa]\n\t"
    "ds_read_b128 v[48:51], %[rw] offset:0\n\t"
    "ds_read_b128 v[52:55], %[rw] offset:1024\n\t"
    "ds_read_b128 v[56:59], %[rw] offset:2048\n\t"
    "ds_read_b128 v[60:63], %[rw] offset:3072\n\t"
    "s_waitcnt lgkmcnt(2)\n\t"
    "v_readfirstlane_b32 %[pp], v39\n\t"
    "s_and_b32 %[st], %[pp], 0xff\n\t"
    "s_cmp_eq_u32 %[st], %[b]\n\t"
    "s_cbranch_scc1 3f\n\t"
    "s_waitcnt lgkmcnt(0)\n\t"
    "s_branch 2b\n\t"
    "3:\n\t"
    "v_add_f32 v48, v48, v80\n\t"
    "v_add_f32 v49, v49, v81\n\t"
    "v_add_f32 v50, v50, v82\n\t"
    "v_add_f32 v51, v51, v83\n\t"
    "v_add_f32 v52, v52, v84\n\t"
    "v_add_f32 v53, v53, v85\n\t"
    "v_add_f32 v54, v54, v86\n\t"
    "v_add_f32 v55, v55, v87\n\t"
    "v_cvt_pk_bf16_f32 v40, v48, v49\n\t"
    "v_cvt_pk_bf16_f32 v41, v50, v51\n\t"
    "v_cvt_pk_bf16_f32 v42, v52, v53\n\t"
    "v_cvt_pk_bf16_f32 v43, v54, v55\n\t"
    "global_store_dwordx4 %[co0], v[40:43], s[36:37] sc1\n\t"
    "s_waitcnt lgkmcnt(0)\n\t"
    "v_add_f32 v56, v56, v88\n\t"
    "v_add_f32 v57, v57, v89\n\t"
    "v_add_f32 v58, v58, v90\n\t"
    "v_add_f32 v59, v59, v91\n\t"
    "v_add_f32 v60, v60, v92\n\t"
    "v_add_f32 v61, v61, v93\n\t"
    "v_add_f32 v62, v62, v94\n\t"
    "v_add_f32 v63, v63, v95\n\t"
    "v_cvt_pk_bf16_f32 v44, v56, v57\n\t"
    "v_cvt_pk_bf16_f32 v45, v58, v59\n\t"
    "v_cvt_pk_bf16_f32 v46, v60, v61\n\t"
    "v_cvt_pk_bf16_f32 v47, v62, v63\n\t"
    "global_store_dwordx4 %[co1], v[44:47], s[36:37] sc1\n\t"
    "s_add_u32 s36, s36, %[ab]\n\t"
    "s_addc_u32 s37, s37, 0\n\t"
    "s_lshr_b32 %[pp], %[pp], 8\n\t"
    "s_cbranch_scc1 1b\n\t"
    "v_mov_b32 v72, %[cn]\n\t"
    "v_mov_b32 v73, 1\n\t"
    "s_add_u32 %[st], %[b], %[po]\n\t"
    "v_mov_b32 v74, %[st]\n\t"
    "v_mov_b32 v75, 0\n\t"
    "s_waitcnt vmcnt(0)\n\t"
    "s_mov_b64 exec, 1\n\t"
    "ds_add_rtn_u32 v73, v72, v73\n\t"
    "s_waitcnt lgkmcnt(0)\n\t"
    "v_readfirstlane_b32 %[st], v73\n\t"
    "s_and_b32 %[st], %[st], 3\n\t"
    "s_cmp_lg_u32 %[st], 3\n\t"
    "s_cbranch_scc1 61f\n\t"
    "global_store_dword v75, v74, %[pg] sc1\n\t"
    "61:\n\t"
    "s_mov_b64 exec, -1\n\t"
    "s_mov_b32 %[pp], 0\n\t"
    "s_cmp_lt_i32 %[b], %[ns]\n\t"
    "s_cbranch_scc1 1b\n\t"
    "s_branch 9f\n\t"
    "7:\n\t"
    "v_mov_b32 v72, %[cn]\n\t"
    "v_mov_b32 v73, 1\n\t"
    "s_add_u32 %[st], %[b], %[po]\n\t"
    "v_mov_b32 v74, %[st]\n\t"
    "v_mov_b32 v75, 0\n\t"
    "s_waitcnt vmcnt(0)\n\t"
    "s_mov_b64 exec, 1\n\t"
    "ds_add_rtn_u32 v73, v72, v73\n\t"
    "s_waitcnt lgkmcnt(0)\n\t"
    "v_readfirstlane_b32 %[st], v73\n\t"
    "s_and_b32 %[st], %[st], 3\n\t"
    "s_cmp_lg_u32 %[st], 3\n\t"
    "s_cbranch_scc1 61f\n\t"
    "global_store_dword v75, v74, %[pg] sc1\n\t"
    "61:\n\t"
    "s_mov_b64 exec, -1\n\t"
    "s_mov_b32 %[pp], 0\n\t"
    "s_branch 8b\n\t"
    "9:"
    : [ea] "+&v"(ea), [da] "+&v"(da), [b] "+&s"(b), [st] "=&s"(st), "+{s[36:37]}"(p0), [pp] "+&s"(pp)
    : [ns] "s"(ns), [ab] "s"(ab), [rw] "v"(rw), [fa] "v"(fa), "{v[128:159]}"(W[0]), "{v[160:191]}"(W[1]), "{v[192:223]}"(W[2]), "{v[224:255]}"(W[3]), [po] "s"(po), [cn] "s"(cn), [pg] "s"(pg), [co0] "v"(co0), [co1] "v"(co1)
    : "v39", "v40", "v41", "v42", "v43", "v44", "v45", "v46", "v47", "v48", "v49", "v50", "v51", "v52", "v53", "v54", "v55", "v56", "v57", "v58", "v59", "v60", "v61", "v62", "v63", "v72", "v73", "v74", "v75", "v80", "v81", "v82", "v83", "v84", "v85", "v86", "v87", "v88", "v89", "v90", "v91", "v92", "v93", "v94", "v95", "v96", "v97", "v98", "v99", "v100", "v101", "v102", "v103", "v104", "v105", "v106", "v107", "v108", "v109", "v110", "v111", "v112", "v113", "v114", "v115", "v116", "v117", "v118", "v119", "v120", "v121", "v122", "v123", "v124", "v125", "v126", "v127", "vcc", "scc", "m0", "memory");
}
__device__ __forceinline__ void paced_st1(const u32x32 (&W)[4], unsigned ea, unsigned da, int b, int ns, unsigned ab, unsigned rw, unsigned fa,
    const char* p0, unsigned pp, int po, unsigned cn, const char* pg, unsigned co0, unsigned co1) {
  unsigned st;
  asm volatile("1:\n\t"
    "s_barrier\n\t"
    "ds_read_b128 v[96:99], %[ea] offset:0\n\t"
    "ds_read_b128 v[100:103], %[ea] offset:2048\n\t"
    "ds_read_b128 v[104:107], %[da] offset:0\n\t"
    "ds_read_b128 v[108:111], %[da] offset:2048\n\t"
    "ds_read_b128 v[112:115], %[ea] offset:4096\n\t"
    "ds_read_b128 v[116:119], %[ea] offset:6144\n\t"
    "s_waitcnt lgkmcnt(4)\n\t"
    "v_mfma_f32_16x16x32_bf16 v[80:83], v[128:131], v[96:99], 0\n\t"
    "ds_read_b128 v[120:123], %[da] offset:4096\n\t"
    "v_mfma_f32_16x16x32_bf16 v[84:87], v[132:135], v[96:99], 0\n\t"
    "ds_read_b128 v[124:127], %[da] offset:6144\n\t"
    "v_mfma_f32_16x16x32_bf16 v[88:91], v[128:131], v[100:103], 0\n\t"
    "v_mfma_f32_16x16x32_bf16 v[92:95], v[132:135], v[100:103], 0\n\t"
    "s_barrier\n\t"
    "s_waitcnt lgkmcnt(4)\n\t"
    "v_mfma_f32_16x16x32_bf16 v[80:83], v[136:139], v[104:107], v[80:83]\n\t"
    "ds_read_b128 v[96:99], %[ea] offset:8192\n\t"
    "v_mfma_f32_16x16x32_bf16 v[84:87], v[140:143], v[104:107], v[84:87]\n\t"
    "ds_read_b128 v[100:103], %[ea] offset:10240\n\t"
    "v_mfma_f32_16x16x32_bf16 v[88:91], v[136:139], v[108:111], v[88:91]\n\t"
    "v_mfma_f32_16x16x32_bf16 v[92:95], v[140:143], v[108:111], v[92:95]\n\t"
    "s_waitcnt lgkmcnt(4)\n\t"
    "v_mfma_f32_16x16x32_bf16 v[80:83], v[144:147], v[112:115], v[80:83]\n\t"
    "ds_read_b128 v[104:107], %[da] offset:8192\n\t"
    "v_mfma_f32_16x16x32_bf16 v[84:87], v[148:151], v[112:115], v[84:87]\n\t"
    "ds_read_b128 v[108:111], %[da] offset:10240\n\t"
    "v_mfma_f32_16x16x32_bf16 v[88:91], v[144:147], v[116:119], v[88:91]\n\t"
    "v_mfma_f32_16x16x32_bf16 v[92:95], v[148:151], v[116:119], v[92:95]\n\t"
    "s_waitcnt lgkmcnt(4)\n\t"
    "v_mfma_f32_16x16x32_bf16 v[80:83], v[152:155], v[120:123], v[80:83]\n\t"
    "ds_read_b128 v[112:115], %[ea] offset:12288\n\t"
    "v_mfma_f32_16x16x32_bf16 v[84:87], v[156:159], v[120:123], v[84:87]\n\t"
    "ds_read_b128 v[116:119], %[ea] offset:14336\n\t"
    "v_mfma_f32_16x16x32_bf16 v[88:91], v[152:155], v[124:127], v[88:91]\n\t"
    "v_mfma_f32_16x16x32_bf16 v[92:95], v[156:159], v[124:127], v[92:95]\n\t"
    "s_cmp_lg_u32 %[pp], 0\n\t"
    "s_cbranch_scc1 7f\n\t"
    "8:\n\t"
    "s_waitcnt lgkmcnt(4)\n\t"
    "v_mfma_f32_16x16x32_bf16 v[80:83], v[160:163], v[96:99], v[80:83]\n\t"
    "ds_read_b128 v[120:123], %[da] offset:12288\n\t"
    "v_mfma_f32_16x16x32_bf16 v[84:87], v[164:167], v[96:99], v[84:87]\n\t"
    "ds_read_b128 v[124:127], %[da] offset:14336\n\t"
    "v_mfma_f32_16x16x32_bf16 v[88:91], v[160:163], v[100:103], v[88:91]\n\t"
    "v_mfma_f32_16x16x32_bf16 v[92:95], v[164:167], v[100:103], v[92:95]\n\t"
    "s_barrier\n\t"
    "s_waitcnt lgkmcnt(4)\n\t"
    "v_mfma_f32_16x16x32_bf16 v[80:83], v[168:171], v[104:107], v[80:83]\n\t"
    "ds_read_b128 v[96:99], %[ea] offset:16384\n\t"
    "v_mfma_f32_16x16x32_bf16 v[84:87], v[172:175], v[104:107], v[84:87]\n\t"
    "ds_read_b128 v[100:103], %[ea] offset:18432\n\t"
    "v_mfma_f32_16x16x32_bf16 v[88:91], v[168:171], v[108:111], v[88:91]\n\t"
    "v_mfma_f32_16x16x32_bf16 v[92:95], v[172:175], v[108:111], v[92:95]\n\t"
    "s_waitcnt lgkmcnt(4)\n\t"
    "v_mfma_f32_16x16x32_bf16 v[80:83], v[176:179], v[112:115], v[80:83]\n\t"
    "ds_read_b128 v[104:107], %[da] offset:16384\n\t"
    "v_mfma_f32_16x16x32_bf16 v[84:87], v[180:183], v[112:115], v[84:87]\n\t"
    "ds_read_b128 v[108:111], %[da] offset:18432\n\t"
    "v_mfma_f32_16x16x32_bf16 v[88:91], v[176:179], v[116:119], v[88:91]\n\t"
    "v_mfma_f32_16x16x32_bf16 v[92:95], v[180:183], v[116:119], v[92:95]\n\t"
    "s_waitcnt lgkmcnt(4)\n\t"
    "v_mfma_f32_16x16x32_bf16 v[80:83], v[184:187], v[120:123], v[80:83]\n\t"
    "ds_read_b128 v[112:115], %[ea] offset:20480\n\t"
    "v_mfma_f32_16x16x32_bf16 v[84:87], v[188:191], v[120:123], v[84:87]\n\t"
    "ds_read_b128 v[116:119], %[ea] offset:22528\n\t"
    "v_mfma_f32_16x16x32_bf16 v[88:91], v[184:187], v[124:127], v[88:91]\n\t"
    "v_mfma_f32_16x16x32_bf16 v[92:95], v[188:191], v[124:127], v[92:95]\n\t"
    "s_waitcnt lgkmcnt(4)\n\t"
    "v_mfma_f32_16x16x32_bf16 v[80:83], v[192:195], v[96:99], v[80:83]\n\t"
    "ds_read_b128 v[120:123], %[da] offset:20480\n\t"
    "v_mfma_f32_16x16x32_bf16 v[84:87], v[196:199], v[96:99], v[84:87]\n\t"
    "ds_read_b128 v[124:127], %[da] offset:22528\n\t"
    "v_mfma_f32_16x16x32_bf16 v[88:91], v[192:195], v[100:103], v[88:91]\n\t"
    "v_mfma_f32_16x16x32_bf16 v[92:95], v[196:199], v[100:103], v[92:95]\n\t"
    "s_barrier\n\t"
    "s_waitcnt lgkmcnt(4)\n\t"
    "v_mfma_f32_16x16x32_bf16 v[80:83], v[200:203], v[104:107], v[80:83]\n\t"
    "ds_read_b128 v[96:99], %[ea] offset:24576\n\t"
    "v_mfma_f32_16x16x32_bf16 v[84:87], v[204:207], v[104:107], v[84:87]\n\t"
    "ds_read_b128 v[100:103], %[ea] offset:26624\n\t"
    "v_mfma_f32_16x16x32_bf16 v[88:91], v[200:203], v[108:111], v[88:91]\n\t"
    "v_mfma_f32_16x16x32_bf16 v[92:95], v[204:207], v[108:111], v[92:95]\n\t"
    "s_waitcnt lgkmcnt(4)\n\t"
    "v_mfma_f32_16x16x32_bf16 v[80:83], v[208:211], v[112:115], v[80:83]\n\t"
    "ds_read_b128 v[104:107], %[da] offset:24576\n\t"
    "v_mfma_f32_16x16x32_bf16 v[84:87], v[212:215], v[112:115], v[84:87]\n\t"
    "ds_read_b128 v[108:111], %[da] offset:26624\n\t"
    "v_mfma_f32_16x16x32_bf16 v[88:91], v[208:211], v[116:119], v[88:91]\n\t"
    "v_mfma_f32_16x16x32_bf16 v[92:95], v[212:215], v[116:119], v[92:95]\n\t"
    "s_waitcnt lgkmcnt(4)\n\t"
    "v_mfma_f32_16x16x32_bf16 v[80:83], v[216:219], v[120:123], v[80:83]\n\t"
    "ds_read_b128 v[112:115], %[ea] offset:28672\n\t"
    "v_mfma_f32_16x16x32_bf16 v[84:87], v[220:223], v[120:123], v[84:87]\n\t"
    "ds_read_b128 v[116:119], %[ea] offset:30720\n\t"
    "v_mfma_f32_16x16x32_bf16 v[88:91], v[216:219], v[124:127], v[88:91]\n\t"
    "v_mfma_f32_16x16x32_bf16 v[92:95], v[220:223], v[124:127], v[92:95]\n\t"
    "s_waitcnt lgkmcnt(4)\n\t"
    "v_mfma_f32_16x16x32_bf16 v[80:83], v[224:227], v[96:99], v[80:83]\n\t"
    "ds_read_b128 v[120:123], %[da] offset:28672\n\t"
    "v_mfma_f32_16x16x32_bf16 v[84:87], v[228:231], v[96:99], v[84:87]\n\t"
    "ds_read_b128 v[124:127], %[da] offset:30720\n\t"
    "v_mfma_f32_16x16x32_bf16 v[88:91], v[224:227], v[100:103], v[88:91]\n\t"
    "v_mfma_f32_16x16x32_bf16 v[92:95], v[228:231], v[100:103], v[92:95]\n\t"
    "s_waitcnt lgkmcnt(4)\n\t"
    "v_mfma_f32_16x16x32_bf16 v[80:83], v[232:235], v[104:107], v[80:83]\n\t"
    "v_mfma_f32_16x16x32_bf16 v[84:87], v[236:239], v[104:107], v[84:87]\n\t"
    "v_mfma_f32_16x16x32_bf16 v[88:91], v[232:235], v[108:111], v[88:91]\n\t"
    "v_xor_b32 %[ea], 0x10000, %[ea]\n\t"
    "v_mfma_f32_16x16x32_bf16 v[92:95], v[236:239], v[108:111], v[92:95]\n\t"
    "v_xor_b32 %[da], 0x10000, %[da]\n\t"
    "s_waitcnt lgkmcnt(2)\n\t"
    "v_mfma_f32_16x16x32_bf16 v[80:83], v[240:243], v[112:115], v[80:83]\n\t"
    "v_mfma_f32_16x16x32_bf16 v[84:87], v[244:247], v[112:115], v[84:87]\n\t"
    "v_mfma_f32_16x16x32_bf16 v[88:91], v[240:243], v[116:119], v[88:91]\n\t"
    "s_add_u32 %[b], %[b], 1\n\t"
    "v_mfma_f32_16x16x32_bf16 v[92:95], v[244:247], v[116:119], v[92:95]\n\t"
    "s_waitcnt lgkmcnt(0)\n\t"
    "v_mfma_f32_16x16x32_bf16 v[80:83], v[248:251], v[120:123], v[80:83]\n\t"
    "v_mfma_f32_16x16x32_bf16 v[84:87], v[252:255], v[120:123], v[84:87]\n\t"
    "v_mfma_f32_16x16x32_bf16 v[88:91], v[248:251], v[124:127], v[88:91]\n\t"
    "v_mfma_f32_16x16x32_bf16 v[92:95], v[252:255], v[124:127], v[92:95]\n\t"
    "2:\n\t"
    "ds_read_b32 v39, %[fa]\n\t"
    "ds_read_b128 v[48:51], %[rw] offset:0\n\t"
    "ds_read_b128 v[52:55], %[rw] offset:1024\n\t"
    "ds_read_b128 v[56:59], %[rw] offset:2048\n\t"
    "ds_read_b128 v[60:63], %[rw] offset:3072\n\t"
    "s_waitcnt lgkmcnt(2)\n\t"
    "v_readfirstlane_b32 %[pp], v39\n\t"
    "s_and_b32 %[st], %[pp], 0xff\n\t"
    "s_cmp_eq_u32 %[st], %[b]\n\t"
    "s_cbranch_scc1 3f\n\t"
    "s_waitcnt lgkmcnt(0)\n\t"
    "s_branch 2b\n\t"
    "3:\n\t"
    "v_add_f32 v48, v48, v80\n\t"
    "v_add_f32 v49, v49, v81\n\t"
    "v_add_f32 v50, v50, v82\n\t"
    "v_add_f32 v51, v51, v83\n\t"
    "v_add_f32 v52, v52, v84\n\t"
    "v_add_f32 v53, v53, v85\n\t"
    "v_add_f32 v54, v54, v86\n\t"
    "v_add_f32 v55, v55, v87\n\t"
    "s_waitcnt lgkmcnt(0)\n\t"
    "v_add_f32 v56, v56, v88\n\t"
    "v_add_f32 v57, v57, v89\n\t"
    "v_add_f32 v58, v58, v90\n\t"
    "v_add_f32 v59, v59, v91\n\t"
    "v_add_f32 v60, v60, v92\n\t"
    "v_add_f32 v61, v61, v93\n\t"
    "v_add_f32 v62, v62, v94\n\t"
    "v_add_f32 v63, v63, v95\n\t"
    "v_mul_f32 v40, %[nl], v48\n\t"
    "v_mul_f32 v41, %[nl], v49\n\t"
    "v_mul_f32 v42, %[nl], v50\n\t"
    "v_mul_f32 v43, %[nl], v51\n\t"
    "v_mul_f32 v44, %[nl], v56\n\t"
    "v_mul_f32 v45, %[nl], v57\n\t"
    "v_mul_f32 v46, %[nl], v58\n\t"
    "v_mul_f32 v47, %[nl], v59\n\t"
    "v_exp_f32 v40, v40\n\t"
    "v_exp_f32 v41, v41\n\t"
    "v_exp_f32 v42, v42\n\t"
    "v_exp_f32 v43, v43\n\t"
    "v_exp_f32 v44, v44\n\t"
    "v_exp_f32 v45, v45\n\t"
    "v_exp_f32 v46, v46\n\t"
    "v_exp_f32 v47, v47\n\t"
    "v_add_f32 v40, 1.0, v40\n\t"
    "v_add_f32 v41, 1.0, v41\n\t"
    "v_add_f32 v42, 1.0, v42\n\t"
    "v_add_f32 v43, 1.0, v43\n\t"
    "v_add_f32 v44, 1.0, v44\n\t"
    "v_add_f32 v45, 1.0, v45\n\t"
    "v_add_f32 v46, 1.0, v46\n\t"
    "v_add_f32 v47, 1.0, v47\n\t"
    "v_rcp_f32 v40, v40\n\t"
    "v_rcp_f32 v41, v41\n\t"
    "v_rcp_f32 v42, v42\n\t"
    "v_rcp_f32 v43, v43\n\t"
    "v_rcp_f32 v44, v44\n\t"
    "v_rcp_f32 v45, v45\n\t"
    "v_rcp_f32 v46, v46\n\t"
    "v_rcp_f32 v47, v47\n\t"
    "v_mul_f32 v40, v48, v40\n\t"
    "v_mul_f32 v41, v49, v41\n\t"
    "v_mul_f32 v42, v50, v42\n\t"
    "v_mul_f32 v43, v51, v43\n\t"
    "v_mul_f32 v44, v56, v44\n\t"
    "v_mul_f32 v45, v57, v45\n\t"
    "v_mul_f32 v46, v58, v46\n\t"
    "v_mul_f32 v47, v59, v47\n\t"
    "v_mul_f32 v40, v40, v52\n\t"
    "v_mul_f32 v41, v41, v53\n\t"
    "v_mul_f32 v42, v42, v54\n\t"
    "v_mul_f32 v43, v43, v55\n\t"
    "v_mul_f32 v44, v44, v60\n\t"
    "v_mul_f32 v45, v45, v61\n\t"
    "v_mul_f32 v46, v46, v62\n\t"
    "v_mul_f32 v47, v47, v63\n\t"
    "v_cvt_pk_bf16_f32 v48, v40, v41\n\t"
    "v_cvt_pk_bf16_f32 v49, v42, v43\n\t"
    "v_cvt_pk_bf16_f32 v50, v44, v45\n\t"
    "v_cvt_pk_bf16_f32 v51, v46, v47\n\t"
    "s_nop 1\n\t"
    "v_permlane16_swap_b32 v48, v50\n\t"
    "v_permlane16_swap_b32 v49, v51\n\t"
    "global_store_dwordx4 %[co0], v[48:51], s[36:37] sc1\n\t"
    "s_add_u32 s36, s36, %[ab]\n\t"
    "s_addc_u32 s37, s37, 0\n\t"
    "s_lshr_b32 %[pp], %[pp], 8\n\t"
    "s_cbranch_scc1 1b\n\t"
    "v_mov_b32 v72, %[cn]\n\t"
    "v_mov_b32 v73, 1\n\t"
    "s_add_u32 %[st], %[b], %[po]\n\t"
    "v_mov_b32 v74, %[st]\n\t"
    "v_mov_b32 v75, 0\n\t"
    "s_waitcnt vmcnt(0)\n\t"
    "s_mov_b64 exec, 1\n\t"
    "ds_add_rtn_u32 v73, v72, v73\n\t"
    "s_waitcnt lgkmcnt(0)\n\t"
    "v_readfirstlane_b32 %[st], v73\n\t"
    "s_and_b32 %[st], %[st], 3\n\t"
    "s_cmp_lg_u32 %[st], 3\n\t"
    "s_cbranch_scc1 61f\n\t"
    "global_store_dword v75, v74, %[pg] sc1\n\t"
    "61:\n\t"
    "s_mov_b64 exec, -1\n\t"
    "s_mov_b32 %[pp], 0\n\t"
    "s_cmp_lt_i32 %[b], %[ns]\n\t"
    "s_cbranch_scc1 1b\n\t"
    "s_branch 9f\n\t"
    "7:\n\t"
    "v_mov_b32 v72, %[cn]\n\t"
    "v_mov_b32 v73, 1\n\t"
    "s_add_u32 %[st], %[b], %[po]\n\t"
    "v_mov_b32 v74, %[st]\n\t"
    "v_mov_b32 v75, 0\n\t"
    "s_waitcnt vmcnt(0)\n\t"
    "s_mov_b64 exec, 1\n\t"
    "ds_add_rtn_u32 v73, v72, v73\n\t"
    "s_waitcnt lgkmcnt(0)\n\t"
    "v_readfirstlane_b32 %[st], v73\n\t"
    "s_and_b32 %[st], %[st], 3\n\t"
    "s_cmp_lg_u32 %[st], 3\n\t"
    "s_cbranch_scc1 61f\n\t"
    "global_store_dword v75, v74, %[pg] sc1\n\t"
    "61:\n\t"
    "s_mov_b64 exec, -1\n\t"
    "s_mov_b32 %[pp], 0\n\t"
    "s_branch 8b\n\t"
    "9:"
    : [ea] "+&v"(ea), [da] "+&v"(da), [b] "+&s"(b), [st] "=&s"(st), "+{s[36:37]}"(p0), [pp] "+&s"(pp)
    : [ns] "s"(ns), [ab] "s"(ab), [rw] "v"(rw), [fa] "v"(fa), "{v[128:159]}"(W[0]), "{v[160:191]}"(W[1]), "{v[192:223]}"(W[2]), "{v[224:255]}"(W[3]), [po] "s"(po), [cn] "s"(cn), [pg] "s"(pg), [co0] "v"(co0), [nl] "s"(-LOG2E)
    : "v39", "v40", "v41", "v42", "v43", "v44", "v45", "v46", "v47", "v48", "v49", "v50", "v51", "v52", "v53", "v54", "v55", "v56", "v57", "v58", "v59", "v60", "v61", "v62", "v63", "v72", "v73", "v74", "v75", "v80", "v81", "v82", "v83", "v84", "v85", "v86", "v87", "v88", "v89", "v90", "v91", "v92", "v93", "v94", "v95", "v96", "v97", "v98", "v99", "v100", "v101", "v102", "v103", "v104", "v105", "v106", "v107", "v108", "v109", "v110", "v111", "v112", "v113", "v114", "v115", "v116", "v117", "v118", "v119", "v120", "v121", "v122", "v123", "v124", "v125", "v126", "v127", "vcc", "scc", "m0", "memory");
}
__device__ __forceinline__ void paced_st2(const u32x32 (&W)[4], unsigned ea, unsigned da, int b, int ns, unsigned ab, unsigned rw, unsigned fa,
    const char* p0, unsigned pp, int po, unsigned cn, const char* pg, unsigned co0, unsigned co1) {
  unsigned st;
  asm volatile("1:\n\t"
    "s_barrier\n\t"
    "ds_read_b128 v[96:99], %[ea] offset:0\n\t"
    "ds_read_b128 v[100:103], %[ea] offset:2048\n\t"
    "ds_read_b128 v[104:107], %[da] offset:0\n\t"
    "ds_read_b128 v[108:111], %[da] offset:2048\n\t"
    "ds_read_b128 v[112:115], %[ea] offset:4096\n\t"
    "ds_read_b128 v[116:119], %[ea] offset:6144\n\t"
    "s_waitcnt lgkmcnt(4)\n\t"
    "v_mfma_f32_16x16x32_bf16 v[80:83], v[128:131], v[96:99], 0\n\t"
    "ds_read_b128 v[120:123], %[da] offset:4096\n\t"
    "v_mfma_f32_16x16x32_bf16 v[84:87], v[132:135], v[96:99], 0\n\t"
    "ds_read_b128 v[124:127], %[da] offset:6144\n\t"
    "v_mfma_f32_16x16x32_bf16 v[88:91], v[128:131], v[100:103], 0\n\t"
    "v_mfma_f32_16x16x32_bf16 v[92:95], v[132:135], v[100:103], 0\n\t"
    "s_barrier\n\t"
    "s_waitcnt lgkmcnt(4)\n\t"
    "v_mfma_f32_16x16x32_bf16 v[80:83], v[136:139], v[104:107], v[80:83]\n\t"
    "ds_read_b128 v[96:99], %[ea] offset:8192\n\t"
    "v_mfma_f32_16x16x32_bf16 v[84:87], v[140:143], v[104:107], v[84:87]\n\t"
    "ds_read_b128 v[100:103], %[ea] offset:10240\n\t"
    "v_mfma_f32_16x16x32_bf16 v[88:91], v[136:139], v[108:111], v[88:91]\n\t"
    "v_mfma_f32_16x16x32_bf16 v[92:95], v[140:143], v[108:111], v[92:95]\n\t"
    "s_waitcnt lgkmcnt(4)\n\t"
    "v_mfma_f32_16x16x32_bf16 v[80:83], v[144:147], v[112:115], v[80:83]\n\t"
    "ds_read_b128 v[104:107], %[da] offset:8192\n\t"
    "v_mfma_f32_16x16x32_bf16 v[84:87], v[148:151], v[112:115], v[84:87]\n\t"
    "ds_read_b128 v[108:111], %[da] offset:10240\n\t"
    "v_mfma_f32_16x16x32_bf16 v[88:91], v[144:147], v[116:119], v[88:91]\n\t"
    "v_mfma_f32_16x16x32_bf16 v[92:95], v[148:151], v[116:119], v[92:95]\n\t"
    "s_waitcnt lgkmcnt(4)\n\t"
    "v_mfma_f32_16x16x32_bf16 v[80:83], v[152:155], v[120:123], v[80:83]\n\t"
    "ds_read_b128 v[112:115], %[ea] offset:12288\n\t"
    "v_mfma_f32_16x16x32_bf16 v[84:87], v[156:159], v[120:123], v[84:87]\n\t"
    "ds_read_b128 v[116:119], %[ea] offset:14336\n\t"
    "v_mfma_f32_16x16x32_bf16 v[88:91], v[152:155], v[124:127], v[88:91]\n\t"
    "v_mfma_f32_16x16x32_bf16 v[92:95], v[156:159], v[124:127], v[92:95]\n\t"
    "s_cmp_lg_u32 %[pp], 0\n\t"
    "s_cbranch_scc1 7f\n\t"
    "8:\n\t"
    "s_waitcnt lgkmcnt(4)\n\t"
    "v_mfma_f32_16x16x32_bf16 v[80:83], v[160:163], v[96:99], v[80:83]\n\t"
    "ds_read_b128 v[120:123], %[da] offset:12288\n\t"
    "v_mfma_f32_16x16x32_bf16 v[84:87], v[164:167], v[96:99], v[84:87]\n\t"
    "ds_read_b128 v[124:127], %[da] offset:14336\n\t"
    "v_mfma_f32_16x16x32_bf16 v[88:91], v[160:163], v[100:103], v[88:91]\n\t"
    "v_mfma_f32_16x16x32_bf16 v[92:95], v[164:167], v[100:103], v[92:95]\n\t"
    "s_barrier\n\t"
    "s_waitcnt lgkmcnt(4)\n\t"
    "v_mfma_f32_16x16x32_bf16 v[80:83], v[168:171], v[104:107], v[80:83]\n\t"
    "ds_read_b128 v[96:99], %[ea] offset:16384\n\t"
    "v_mfma_f32_16x16x32_bf16 v[84:87], v[172:175], v[104:107], v[84:87]\n\t"
    "ds_read_b128 v[100:103], %[ea] offset:18432\n\t"
    "v_mfma_f32_16x16x32_bf16 v[88:91], v[168:171], v[108:111], v[88:91]\n\t"
    "v_mfma_f32_16x16x32_bf16 v[92:95], v[172:175], v[108:111], v[92:95]\n\t"
    "s_waitcnt lgkmcnt(4)\n\t"
    "v_mfma_f32_16x16x32_bf16 v[80:83], v[176:179], v[112:115], v[80:83]\n\t"
    "ds_read_b128 v[104:107], %[da] offset:16384\n\t"
    "v_mfma_f32_16x16x32_bf16 v[84:87], v[180:183], v[112:115], v[84:87]\n\t"
    "ds_read_b128 v[108:111], %[da] offset:18432\n\t"
    "v_mfma_f32_16x16x32_bf16 v[88:91], v[176:179], v[116:119], v[88:91]\n\t"
    "v_mfma_f32_16x16x32_bf16 v[92:95], v[180:183], v[116:119], v[92:95]\n\t"
    "s_waitcnt lgkmcnt(4)\n\t"
    "v_mfma_f32_16x16x32_bf16 v[80:83], v[184:187], v[120:123], v[80:83]\n\t"
    "ds_read_b128 v[112:115], %[ea] offset:20480\n\t"
    "v_mfma_f32_16x16x32_bf16 v[84:87], v[188:191], v[120:123], v[84:87]\n\t"
    "ds_read_b128 v[116:119], %[ea] offset:22528\n\t"
    "v_mfma_f32_16x16x32_bf16 v[88:91], v[184:187], v[124:127], v[88:91]\n\t"
    "v_mfma_f32_16x16x32_bf16 v[92:95], v[188:191], v[124:127], v[92:95]\n\t"
    "s_waitcnt lgkmcnt(4)\n\t"
    "v_mfma_f32_16x16x32_bf16 v[80:83], v[192:195], v[96:99], v[80:83]\n\t"
    "ds_read_b128 v[120:123], %[da] offset:20480\n\t"
    "v_mfma_f32_16x16x32_bf16 v[84:87], v[196:199], v[96:99], v[84:87]\n\t"
    "ds_read_b128 v[124:127], %[da] offset:22528\n\t"
    "v_mfma_f32_16x16x32_bf16 v[88:91], v[192:195], v[100:103], v[88:91]\n\t"
    "v_mfma_f32_16x16x32_bf16 v[92:95], v[196:199], v[100:103], v[92:95]\n\t"
    "s_barrier\n\t"
    "s_waitcnt lgkmcnt(4)\n\t"
    "v_mfma_f32_16x16x32_bf16 v[80:83], v[200:203], v[104:107], v[80:83]\n\t"
    "ds_read_b128 v[96:99], %[ea] offset:24576\n\t"
    "v_mfma_f32_16x16x32_bf16 v[84:87], v[204:207], v[104:107], v[84:87]\n\t"
    "ds_read_b128 v[100:103], %[ea] offset:26624\n\t"
    "v_mfma_f32_16x16x32_bf16 v[88:91], v[200:203], v[108:111], v[88:91]\n\t"
    "v_mfma_f32_16x16x32_bf16 v[92:95], v[204:207], v[108:111], v[92:95]\n\t"
    "s_waitcnt lgkmcnt(4)\n\t"
    "v_mfma_f32_16x16x32_bf16 v[80:83], v[208:211], v[112:115], v[80:83]\n\t"
    "ds_read_b128 v[104:107], %[da] offset:24576\n\t"
    "v_mfma_f32_16x16x32_bf16 v[84:87], v[212:215], v[112:115], v[84:87]\n\t"
    "ds_read_b128 v[108:111], %[da] offset:26624\n\t"
    "v_mfma_f32_16x16x32_bf16 v[88:91], v[208:211], v[116:119], v[88:91]\n\t"
    "v_mfma_f32_16x16x32_bf16 v[92:95], v[212:215], v[116:119], v[92:95]\n\t"
    "s_waitcnt lgkmcnt(4)\n\t"
    "v_mfma_f32_16x16x32_bf16 v[80:83], v[216:219], v[120:123], v[80:83]\n\t"
    "ds_read_b128 v[112:115], %[ea] offset:28672\n\t"
    "v_mfma_f32_16x16x32_bf16 v[84:87], v[220:223], v[120:123], v[84:87]\n\t"
    "ds_read_b128 v[116:119], %[ea] offset:30720\n\t"
    "v_mfma_f32_16x16x32_bf16 v[88:91], v[216:219], v[124:127], v[88:91]\n\t"
    "v_mfma_f32_16x16x32_bf16 v[92:95], v[220:223], v[124:127], v[92:95]\n\t"
    "s_waitcnt lgkmcnt(4)\n\t"
    "v_mfma_f32_16x16x32_bf16 v[80:83], v[224:227], v[96:99], v[80:83]\n\t"
    "ds_read_b128 v[120:123], %[da] offset:28672\n\t"
    "v_mfma_f32_16x16x32_bf16 v[84:87], v[228:231], v[96:99], v[84:87]\n\t"
    "ds_read_b128 v[124:127], %[da] offset:30720\n\t"
    "v_mfma_f32_16x16x32_bf16 v[88:91], v[224:227], v[100:103], v[88:91]\n\t"
    "v_mfma_f32_16x16x32_bf16 v[92:95], v[228:231], v[100:103], v[92:95]\n\t"
    "s_waitcnt lgkmcnt(4)\n\t"
    "v_mfma_f32_16x16x32_bf16 v[80:83], v[232:235], v[104:107], v[80:83]\n\t"
    "v_mfma_f32_16x16x32_bf16 v[84:87], v[236:239], v[104:107], v[84:87]\n\t"
    "v_mfma_f32_16x16x32_bf16 v[88:91], v[232:235], v[108:111], v[88:91]\n\t"
    "v_xor_b32 %[ea], 0x10000, %[ea]\n\t"
    "v_mfma_f32_16x16x32_bf16 v[92:95], v[236:239], v[108:111], v[92:95]\n\t"
    "v_xor_b32 %[da], 0x10000, %[da]\n\t"
    "s_waitcnt lgkmcnt(2)\n\t"
    "v_mfma_f32_16x16x32_bf16 v[80:83], v[240:243], v[112:115], v[80:83]\n\t"
    "v_mfma_f32_16x16x32_bf16 v[84:87], v[244:247], v[112:115], v[84:87]\n\t"
    "v_mfma_f32_16x16x32_bf16 v[88:91], v[240:243], v[116:119], v[88:91]\n\t"
    "s_add_u32 %[b], %[b], 1\n\t"
    "v_mfma_f32_16x16x32_bf16 v[92:95], v[244:247], v[116:119], v[92:95]\n\t"
    "s_waitcnt lgkmcnt(0)\n\t"
    "v_mfma_f32_16x16x32_bf16 v[80:83], v[248:251], v[120:123], v[80:83]\n\t"
    "v_mfma_f32_16x16x32_bf16 v[84:87], v[252:255], v[120:123], v[84:87]\n\t"
    "v_mfma_f32_16x16x32_bf16 v[88:91], v[248:251], v[124:127], v[88:91]\n\t"
    "v_mfma_f32_16x16x32_bf16 v[92:95], v[252:255], v[124:127], v[92:95]\n\t"
    "s_nop 6\n\t"
    "v_cvt_pk_bf16_f32 v40, v80, v81\n\t"
    "v_cvt_pk_bf16_f32 v41, v82, v83\n\t"
    "v_cvt_pk_bf16_f32 v42, v84, v85\n\t"
    "v_cvt_pk_bf16_f32 v43, v86, v87\n\t"
    "global_store_dwordx4 %[co0], v[40:43], s[36:37] sc1\n\t"
    "v_cvt_pk_bf16_f32 v44, v88, v89\n\t"
    "v_cvt_pk_bf16_f32 v45, v90, v91\n\t"
    "v_cvt_pk_bf16_f32 v46, v92, v93\n\t"
    "v_cvt_pk_bf16_f32 v47, v94, v95\n\t"
    "global_store_dwordx4 %[co1], v[44:47], s[36:37] sc1\n\t"
    "2:\n\t"
    "ds_read_b32 v39, %[fa]\n\t"
    "ds_read_b128 v[48:51], %[rw] offset:0\n\t"
    "ds_read_b128 v[52:55], %[rw] offset:1024\n\t"
    "ds_read_b128 v[56:59], %[rw] offset:2048\n\t"
    "ds_read_b128 v[60:63], %[rw] offset:3072\n\t"
    "s_waitcnt lgkmcnt(2)\n\t"
    "v_readfirstlane_b32 %[pp], v39\n\t"
    "s_and_b32 %[st], %[pp], 0xff\n\t"
    "s_cmp_eq_u32 %[st], %[b]\n\t"
    "s_cbranch_scc1 3f\n\t"
    "s_waitcnt lgkmcnt(0)\n\t"
    "s_branch 2b\n\t"
    "3:\n\t"
    "v_cvt_pk_bf16_f32 v48, v48, v49\n\t"
    "v_cvt_pk_bf16_f32 v49, v50, v51\n\t"
    "v_cvt_pk_bf16_f32 v50, v52, v53\n\t"
    "v_cvt_pk_bf16_f32 v51, v54, v55\n\t"
    "global_store_dwordx4 %[co0], v[48:51], s[36:37] offset:2048 sc1\n\t"
    "s_waitcnt lgkmcnt(0)\n\t"
    "v_cvt_pk_bf16_f32 v56, v56, v57\n\t"
    "v_cvt_pk_bf16_f32 v57, v58, v59\n\t"
    "v_cvt_pk_bf16_f32 v58, v60, v61\n\t"
    "v_cvt_pk_bf16_f32 v59, v62, v63\n\t"
    "global_store_dwordx4 %[co1], v[56:59], s[36:37] offset:2048 sc1\n\t"
    "s_add_u32 s36, s36, %[ab]\n\t"
    "s_addc_u32 s37, s37, 0\n\t"
    "s_lshr_b32 %[pp], %[pp], 8\n\t"
    "s_cbranch_scc1 1b\n\t"
    "v_mov_b32 v72, %[cn]\n\t"
    "v_mov_b32 v73, 1\n\t"
    "s_add_u32 %[st], %[b], %[po]\n\t"
    "v_mov_b32 v74, %[st]\n\t"
    "v_mov_b32 v75, 0\n\t"
    "s_waitcnt vmcnt(0)\n\t"
    "s_mov_b64 exec, 1\n\t"
    "ds_add_rtn_u32 v73, v72, v73\n\t"
    "s_waitcnt lgkmcnt(0)\n\t"
    "v_readfirstlane_b32 %[st], v73\n\t"
    "s_and_b32 %[st], %[st], 3\n\t"
    "s_cmp_lg_u32 %[st], 3\n\t"
    "s_cbranch_scc1 61f\n\t"
    "global_store_dword v75, v74, %[pg] sc1\n\t"
    "61:\n\t"
    "s_mov_b64 exec, -1\n\t"
    "s_mov_b32 %[pp], 0\n\t"
    "s_cmp_lt_i32 %[b], %[ns]\n\t"
    "s_cbranch_scc1 1b\n\t"
    "s_branch 9f\n\t"
    "7:\n\t"
    "v_mov_b32 v72, %[cn]\n\t"
    "v_mov_b32 v73, 1\n\t"
    "s_add_u32 %[st], %[b], %[po]\n\t"
    "v_mov_b32 v74, %[st]\n\t"
    "v_mov_b32 v75, 0\n\t"
    "s_waitcnt vmcnt(0)\n\t"
    "s_mov_b64 exec, 1\n\t"
    "ds_add_rtn_u32 v73, v72, v73\n\t"
    "s_waitcnt lgkmcnt(0)\n\t"
    "v_readfirstlane_b32 %[st], v73\n\t"
    "s_and_b32 %[st], %[st], 3\n\t"
    "s_cmp_lg_u32 %[st], 3\n\t"
    "s_cbranch_scc1 61f\n\t"
    "global_store_dword v75, v74, %[pg] sc1\n\t"
    "61:\n\t"
    "s_mov_b64 exec, -1\n\t"
    "s_mov_b32 %[pp], 0\n\t"
    "s_branch 8b\n\t"
    "9:"
    : [ea] "+&v"(ea), [da] "+&v"(da), [b] "+&s"(b), [st] "=&s"(st), "+{s[36:37]}"(p0), [pp] "+&s"(pp)
    : [ns] "s"(ns), [ab] "s"(ab), [rw] "v"(rw), [fa] "v"(fa), "{v[128:159]}"(W[0]), "{v[160:191]}"(W[1]), "{v[192:223]}"(W[2]), "{v[224:255]}"(W[3]), [po] "s"(po), [cn] "s"(cn), [pg] "s"(pg), [co0] "v"(co0), [co1] "v"(co1)
    : "v39", "v40", "v41", "v42", "v43", "v44", "v45", "v46", "v47", "v48", "v49", "v50", "v51", "v52", "v53", "v54", "v55", "v56", "v57", "v58", "v59", "v60", "v61", "v62", "v63", "v72", "v73", "v74", "v75", "v80", "v81", "v82", "v83", "v84", "v85", "v86", "v87", "v88", "v89", "v90", "v91", "v92", "v93", "v94", "v95", "v96", "v97", "v98", "v99", "v100", "v101", "v102", "v103", "v104", "v105", "v106", "v107", "v108", "v109", "v110", "v111", "v112", "v113", "v114", "v115", "v116", "v117", "v118", "v119", "v120", "v121", "v122", "v123", "v124", "v125", "v126", "v127", "vcc", "scc", "m0", "memory");
}
__device__ __forceinline__ void fin_ld(const u32x16& pd, unsigned rw, unsigned fa, unsigned fv) {
  asm volatile("ds_write_b128 %[rw], v[80:83] offset:0\n\t"
    "ds_write_b128 %[rw], v[84:87] offset:1024\n\t"
    "ds_write_b128 %[rw], v[88:91] offset:2048\n\t"
    "ds_write_b128 %[rw], v[92:95] offset:3072\n\t"
    "ds_write_b32 %[fa], %[fv]"
    :
    : [rw] "v"(rw), "{v[80:95]}"(pd), [fa] "v"(fa), [fv] "v"(fv)
    : "memory");
}
__device__ __forceinline__ void fin_st0(const u32x16& pd, unsigned rw, unsigned fa, unsigned fv, const char* cb, unsigned co0, unsigned co1) {
  asm volatile("1:\n\t"
    "ds_read_b32 v39, %[fa]\n\t"
    "ds_read_b128 v[48:51], %[rw] offset:0\n\t"
    "ds_read_b128 v[52:55], %[rw] offset:1024\n\t"
    "ds_read_b128 v[56:59], %[rw] offset:2048\n\t"
    "ds_read_b128 v[60:63], %[rw] offset:3072\n\t"
    "s_waitcnt lgkmcnt(2)\n\t"
    "v_cmp_ne_u32 vcc, v39, %[fv]\n\t"
    "s_cbranch_vccz 2f\n\t"
    "s_waitcnt lgkmcnt(0)\n\t"
    "s_branch 1b\n\t"
    "2:\n\t"
    "v_add_f32 v48, v48, v80\n\t"
    "v_add_f32 v49, v49, v81\n\t"
    "v_add_f32 v50, v50, v82\n\t"
    "v_add_f32 v51, v51, v83\n\t"
    "v_add_f32 v52, v52, v84\n\t"
    "v_add_f32 v53, v53, v85\n\t"
    "v_add_f32 v54, v54, v86\n\t"
    "v_add_f32 v55, v55, v87\n\t"
    "v_cvt_pk_bf16_f32 v40, v48, v49\n\t"
    "v_cvt_pk_bf16_f32 v41, v50, v51\n\t"
    "v_cvt_pk_bf16_f32 v42, v52, v53\n\t"
    "v_cvt_pk_bf16_f32 v43, v54, v55\n\t"
    "global_store_dwordx4 %[co0], v[40:43], %[cb] sc1\n\t"
    "s_waitcnt lgkmcnt(0)\n\t"
    "v_add_f32 v56, v56, v88\n\t"
    "v_add_f32 v57, v57, v89\n\t"
    "v_add_f32 v58, v58, v90\n\t"
    "v_add_f32 v59, v59, v91\n\t"
    "v_add_f32 v60, v60, v92\n\t"
    "v_add_f32 v61, v61, v93\n\t"
    "v_add_f32 v62, v62, v94\n\t"
    "v_add_f32 v63, v63, v95\n\t"
    "v_cvt_pk_bf16_f32 v44, v56, v57\n\t"
    "v_cvt_pk_bf16_f32 v45, v58, v59\n\t"
    "v_cvt_pk_bf16_f32 v46, v60, v61\n\t"
    "v_cvt_pk_bf16_f32 v47, v62, v63\n\t"
    "global_store_dwordx4 %[co1], v[44:47], %[cb] sc1\n\t"
    "s_nop 1"
    :
    : [rw] "v"(rw), "{v[80:95]}"(pd), [fa] "v"(fa), [fv] "v"(fv), [cb] "s"(cb), [co0] "v"(co0), [co1] "v"(co1)
    : "v39", "v40", "v41", "v42", "v43", "v44", "v45", "v46", "v47", "v48", "v49", "v50", "v51", "v52", "v53", "v54", "v55", "v56", "v57", "v58", "v59", "v60", "v61", "v62", "v63", "vcc", "memory");
}
__device__ __forceinline__ void fin_st1(const u32x16& pd, unsigned rw, unsigned fa, unsigned fv, const char* cb, unsigned co0, unsigned co1) {
  asm volatile("1:\n\t"
    "ds_read_b32 v39, %[fa]\n\t"
    "ds_read_b128 v[48:51], %[rw] offset:0\n\t"
    "ds_read_b128 v[52:55], %[rw] offset:1024\n\t"
    "ds_read_b128 v[56:59], %[rw] offset:2048\n\t"
    "ds_read_b128 v[60:63], %[rw] offset:3072\n\t"
    "s_waitcnt lgkmcnt(2)\n\t"
    "v_cmp_ne_u32 vcc, v39, %[fv]\n\t"
    "s_cbranch_vccz 2f\n\t"
    "s_waitcnt lgkmcnt(0)\n\t"
    "s_branch 1b\n\t"
    "2:\n\t"
    "v_add_f32 v48, v48, v80\n\t"
    "v_add_f32 v49, v49, v81\n\t"
    "v_add_f32 v50, v50, v82\n\t"
    "v_add_f32 v51, v51, v83\n\t"
    "v_add_f32 v52, v52, v84\n\t"
    "v_add_f32 v53, v53, v85\n\t"
    "v_add_f32 v54, v54, v86\n\t"
    "v_add_f32 v55, v55, v87\n\t"
    "s_waitcnt lgkmcnt(0)\n\t"
    "v_add_f32 v56, v56, v88\n\t"
    "v_add_f32 v57, v57, v89\n\t"
    "v_add_f32 v58, v58, v90\n\t"
    "v_add_f32 v59, v59, v91\n\t"
    "v_add_f32 v60, v60, v92\n\t"
    "v_add_f32 v61, v61, v93\n\t"
    "v_add_f32 v62, v62, v94\n\t"
    "v_add_f32 v63, v63, v95\n\t"
    "v_mul_f32 v40, %[nl], v48\n\t"
    "v_mul_f32 v41, %[nl], v49\n\t"
    "v_mul_f32 v42, %[nl], v50\n\t"
    "v_mul_f32 v43, %[nl], v51\n\t"
    "v_mul_f32 v44, %[nl], v56\n\t"
    "v_mul_f32 v45, %[nl], v57\n\t"
    "v_mul_f32 v46, %[nl], v58\n\t"
    "v_mul_f32 v47, %[nl], v59\n\t"
    "v_exp_f32 v40, v40\n\t"
    "v_exp_f32 v41, v41\n\t"
    "v_exp_f32 v42, v42\n\t"
    "v_exp_f32 v43, v43\n\t"
    "v_exp_f32 v44, v44\n\t"
    "v_exp_f32 v45, v45\n\t"
    "v_exp_f32 v46, v46\n\t"
    "v_exp_f32 v47, v47\n\t"
    "v_add_f32 v40, 1.0, v40\n\t"
    "v_add_f32 v41, 1.0, v41\n\t"
    "v_add_f32 v42, 1.0, v42\n\t"
    "v_add_f32 v43, 1.0, v43\n\t"
    "v_add_f32 v44, 1.0, v44\n\t"
    "v_add_f32 v45, 1.0, v45\n\t"
    "v_add_f32 v46, 1.0, v46\n\t"
    "v_add_f32 v47, 1.0, v47\n\t"
    "v_rcp_f32 v40, v40\n\t"
    "v_rcp_f32 v41, v41\n\t"
    "v_rcp_f32 v42, v42\n\t"
    "v_rcp_f32 v43, v43\n\t"
    "v_rcp_f32 v44, v44\n\t"
    "v_rcp_f32 v45, v45\n\t"
    "v_rcp_f32 v46, v46\n\t"
    "v_rcp_f32 v47, v47\n\t"
    "v_mul_f32 v40, v48, v40\n\t"
    "v_mul_f32 v41, v49, v41\n\t"
    "v_mul_f32 v42, v50, v42\n\t"
    "v_mul_f32 v43, v51, v43\n\t"
    "v_mul_f32 v44, v56, v44\n\t"
    "v_mul_f32 v45, v57, v45\n\t"
    "v_mul_f32 v46, v58, v46\n\t"
    "v_mul_f32 v47, v59, v47\n\t"
    "v_mul_f32 v40, v40, v52\n\t"
    "v_mul_f32 v41, v41, v53\n\t"
    "v_mul_f32 v42, v42, v54\n\t"
    "v_mul_f32 v43, v43, v55\n\t"
    "v_mul_f32 v44, v44, v60\n\t"
    "v_mul_f32 v45, v45, v61\n\t"
    "v_mul_f32 v46, v46, v62\n\t"
    "v_mul_f32 v47, v47, v63\n\t"
    "v_cvt_pk_bf16_f32 v48, v40, v41\n\t"
    "v_cvt_pk_bf16_f32 v49, v42, v43\n\t"
    "v_cvt_pk_bf16_f32 v50, v44, v45\n\t"
    "v_cvt_pk_bf16_f32 v51, v46, v47\n\t"
    "s_nop 1\n\t"
    "v_permlane16_swap_b32 v48, v50\n\t"
    "v_permlane16_swap_b32 v49, v51\n\t"
    "global_store_dwordx4 %[co0], v[48:51], %[cb] sc1\n\t"
    "s_nop 1"
    :
    : [rw] "v"(rw), "{v[80:95]}"(pd), [fa] "v"(fa), [fv] "v"(fv), [cb] "s"(cb), [co0] "v"(co0), [nl] "s"(-LOG2E)
    : "v39", "v40", "v41", "v42", "v43", "v44", "v45", "v46", "v47", "v48", "v49", "v50", "v51", "v52", "v53", "v54", "v55", "v56", "v57", "v58", "v59", "v60", "v61", "v62", "v63", "vcc", "memory");
}
__device__ __forceinline__ void fin_st2(const u32x16& pd, unsigned rw, unsigned fa, unsigned fv, const char* cb, unsigned co0, unsigned co1) {
  asm volatile("s_nop 1\n\t"
    "v_cvt_pk_bf16_f32 v40, v80, v81\n\t"
    "v_cvt_pk_bf16_f32 v41, v82, v83\n\t"
    "v_cvt_pk_bf16_f32 v42, v84, v85\n\t"
    "v_cvt_pk_bf16_f32 v43, v86, v87\n\t"
    "global_store_dwordx4 %[co0], v[40:43], %[cb] sc1\n\t"
    "v_cvt_pk_bf16_f32 v44, v88, v89\n\t"
    "v_cvt_pk_bf16_f32 v45, v90, v91\n\t"
    "v_cvt_pk_bf16_f32 v46, v92, v93\n\t"
    "v_cvt_pk_bf16_f32 v47, v94, v95\n\t"
    "global_store_dwordx4 %[co1], v[44:47], %[cb] sc1\n\t"
    "1:\n\t"
    "ds_read_b32 v39, %[fa]\n\t"
    "ds_read_b128 v[48:51], %[rw] offset:0\n\t"
    "ds_read_b128 v[52:55], %[rw] offset:1024\n\t"
    "ds_read_b128 v[56:59], %[rw] offset:2048\n\t"
    "ds_read_b128 v[60:63], %[rw] offset:3072\n\t"
    "s_waitcnt lgkmcnt(2)\n\t"
    "v_cmp_ne_u32 vcc, v39, %[fv]\n\t"
    "s_cbranch_vccz 2f\n\t"
    "s_waitcnt lgkmcnt(0)\n\t"
    "s_branch 1b\n\t"
    "2:\n\t"
    "v_cvt_pk_bf16_f32 v48, v48, v49\n\t"
    "v_cvt_pk_bf16_f32 v49, v50, v51\n\t"
    "v_cvt_pk_bf16_f32 v50, v52, v53\n\t"
    "v_cvt_pk_bf16_f32 v51, v54, v55\n\t"
    "global_store_dwordx4 %[co0], v[48:51], %[cb] offset:2048 sc1\n\t"
    "s_waitcnt lgkmcnt(0)\n\t"
    "v_cvt_pk_bf16_f32 v56, v56, v57\n\t"
    "v_cvt_pk_bf16_f32 v57, v58, v59\n\t"
    "v_cvt_pk_bf16_f32 v58, v60, v61\n\t"
    "v_cvt_pk_bf16_f32 v59, v62, v63\n\t"
    "global_store_dwordx4 %[co1], v[56:59], %[cb] offset:2048 sc1\n\t"
    "s_nop 1"
    :
    : [rw] "v"(rw), "{v[80:95]}"(pd), [fa] "v"(fa), [fv] "v"(fv), [cb] "s"(cb), [co0] "v"(co0), [co1] "v"(co1)
    : "v39", "v40", "v41", "v42", "v43", "v44", "v45", "v46", "v47", "v48", "v49", "v50", "v51", "v52", "v53", "v54", "v55", "v56", "v57", "v58", "v59", "v60", "v61", "v62", "v63", "vcc", "memory");
}
template <int R> __device__ __forceinline__ void gw_load(u32x32& w, unsigned a0, unsigned a1);
template <> __device__ __forceinline__ void gw_load<0>(u32x32& w, unsigned a0, unsigned a1) {
  asm volatile("ds_read_b64_tr_b16 v[128:129], %1 offset:0\n\t"
    "ds_read_b64_tr_b16 v[130:131], %1 offset:512\n\t"
    "ds_read_b64_tr_b16 v[132:133], %2 offset:0\n\t"
    "ds_read_b64_tr_b16 v[134:135], %2 offset:512\n\t"
    "ds_read_b64_tr_b16 v[136:137], %1 offset:4096\n\t"
    "ds_read_b64_tr_b16 v[138:139], %1 offset:4608\n\t"
    "ds_read_b64_tr_b16 v[140:141], %2 offset:4096\n\t"
    "ds_read_b64_tr_b16 v[142:143], %2 offset:4608\n\t"
    "s_waitcnt lgkmcnt(0)\n\t"
    "ds_read_b64_tr_b16 v[144:145], %1 offset:16384\n\t"
    "ds_read_b64_tr_b16 v[146:147], %1 offset:16896\n\t"
    "ds_read_b64_tr_b16 v[148:149], %2 offset:16384\n\t"
    "ds_read_b64_tr_b16 v[150:151], %2 offset:16896\n\t"
    "ds_read_b64_tr_b16 v[152:153], %1 offset:20480\n\t"
    "ds_read_b64_tr_b16 v[154:155], %1 offset:20992\n\t"
    "ds_read_b64_tr_b16 v[156:157], %2 offset:20480\n\t"
    "ds_read_b64_tr_b16 v[158:159], %2 offset:20992\n\t"
    "s_waitcnt lgkmcnt(0)"
    : "=&{v[128:159]}"(w) : "v"(a0), "v"(a1) : "memory");
}
template <> __device__ __forceinline__ void gw_load<1>(u32x32& w, unsigned a0, unsigned a1) {
  asm volatile("ds_read_b64_tr_b16 v[160:161], %1 offset:0\n\t"
    "ds_read_b64_tr_b16 v[162:163], %1 offset:512\n\t"
    "ds_read_b64_tr_b16 v[164:165], %2 offset:0\n\t"
    "ds_read_b64_tr_b16 v[166:167], %2 offset:512\n\t"
    "ds_read_b64_tr_b16 v[168:169], %1 offset:4096\n\t"
    "ds_read_b64_tr_b16 v[170:171], %1 offset:4608\n\t"
    "ds_read_b64_tr_b16 v[172:173], %2 offset:4096\n\t"
    "ds_read_b64_tr_b16 v[174:175], %2 offset:4608\n\t"
    "s_waitcnt lgkmcnt(0)\n\t"
    "ds_read_b64_tr_b16 v[176:177], %1 offset:16384\n\t"
    "ds_read_b64_tr_b16 v[178:179], %1 offset:16896\n\t"
    "ds_read_b64_tr_b16 v[180:181], %2 offset:16384\n\t"
    "ds_read_b64_tr_b16 v[182:183], %2 offset:16896\n\t"
    "ds_read_b64_tr_b16 v[184:185], %1 offset:20480\n\t"
    "ds_read_b64_tr_b16 v[186:187], %1 offset:20992\n\t"
    "ds_read_b64_tr_b16 v[188:189], %2 offset:20480\n\t"
    "ds_read_b64_tr_b16 v[190:191], %2 offset:20992\n\t"
    "s_waitcnt lgkmcnt(0)"
    : "=&{v[160:191]}"(w) : "v"(a0), "v"(a1) : "memory");
}
template <> __device__ __forceinline__ void gw_load<2>(u32x32& w, unsigned a0, unsigned a1) {
  asm volatile("ds_read_b64_tr_b16 v[192:193], %1 offset:0\n\t"
    "ds_read_b64_tr_b16 v[194:195], %1 offset:512\n\t"
    "ds_read_b64_tr_b16 v[196:197], %2 offset:0\n\t"
    "ds_read_b64_tr_b16 v[198:199], %2 offset:512\n\t"
    "ds_read_b64_tr_b16 v[200:201], %1 offset:4096\n\t"
    "ds_read_b64_tr_b16 v[202:203], %1 offset:4608\n\t"
    "ds_read_b64_tr_b16 v[204:205], %2 offset:4096\n\t"
    "ds_read_b64_tr_b16 v[206:207], %2 offset:4608\n\t"
    "s_waitcnt lgkmcnt(0)\n\t"
    "ds_read_b64_tr_b16 v[208:209], %1 offset:16384\n\t"
    "ds_read_b64_tr_b16 v[210:211], %1 offset:16896\n\t"
    "ds_read_b64_tr_b16 v[212:213], %2 offset:16384\n\t"
    "ds_read_b64_tr_b16 v[214:215], %2 offset:16896\n\t"
    "ds_read_b64_tr_b16 v[216:217], %1 offset:20480\n\t"
    "ds_read_b64_tr_b16 v[218:219], %1 offset:20992\n\t"
    "ds_read_b64_tr_b16 v[220:221], %2 offset:20480\n\t"
    "ds_read_b64_tr_b16 v[222:223], %2 offset:20992\n\t"
    "s_waitcnt lgkmcnt(0)"
    : "=&{v[192:223]}"(w) : "v"(a0), "v"(a1) : "memory");
}
template <> __device__ __forceinline__ void gw_load<3>(u32x32& w, unsigned a0, unsigned a1) {
  asm volatile("ds_read_b64_tr_b16 v[224:225], %1 offset:0\n\t"
    "ds_read_b64_tr_b16 v[226:227], %1 offset:512\n\t"
    "ds_read_b64_tr_b16 v[228:229], %2 offset:0\n\t"
    "ds_read_b64_tr_b16 v[230:231], %2 offset:512\n\t"
    "ds_read_b64_tr_b16 v[232:233], %1 offset:4096\n\t"
    "ds_read_b64_tr_b16 v[234:235], %1 offset:4608\n\t"
    "ds_read_b64_tr_b16 v[236:237], %2 offset:4096\n\t"
    "ds_read_b64_tr_b16 v[238:239], %2 offset:4608\n\t"
    "s_waitcnt lgkmcnt(0)\n\t"
    "ds_read_b64_tr_b16 v[240:241], %1 offset:16384\n\t"
    "ds_read_b64_tr_b16 v[242:243], %1 offset:16896\n\t"
    "ds_read_b64_tr_b16 v[244:245], %2 offset:16384\n\t"
    "ds_read_b64_tr_b16 v[246:247], %2 offset:16896\n\t"
    "ds_read_b64_tr_b16 v[248:249], %1 offset:20480\n\t"
    "ds_read_b64_tr_b16 v[250:251], %1 offset:20992\n\t"
    "ds_read_b64_tr_b16 v[252:253], %2 offset:20480\n\t"
    "ds_read_b64_tr_b16 v[254:255], %2 offset:20992\n\t"
    "s_waitcnt lgkmcnt(0)"
    : "=&{v[224:255]}"(w) : "v"(a0), "v"(a1) : "memory");
}
template <int I, int E, class F> __device__ __forceinline__ void sfor(F&& f) { if constexpr (I < E) { f(std::integral_constant<int, I>{}); sfor<I + 1, E>(f); } }
template <int ROLE, int KQ, class F0, class F>
__device__ __forceinline__ void wload(char* smem, int wave_id_, const u16* W0, const u16* W1, int ldb, u32x32 (&bw)[4], int rot, F0&& hook0, F&& hook, unsigned long long* wts = nullptr) {
  constexpr int ABUF = 65536;
  const int tid = opaque_tid(), lane = tid & 63, wave = WAVE_ID;
  const int kq = KQ == 2 ? wave >> 2 : 0, nq = KQ == 2 ? wave & 3 : wave;
  const int g = lane >> 4, r16 = lane & 15, q4 = r16 >> 2, p4 = lane & 3;
  const unsigned ldsb = __builtin_amdgcn_readfirstlane((unsigned)(size_t)(LAS char*)smem);
  {
    const int srow = 8 * wave + (lane >> 3), ch = (lane & 7) ^ bswz(srow);
    const unsigned voff = (unsigned)(srow * ldb + ch * 8) * 2;
    const char* w0 = uni((const char*)W0); const char* w1 = uni((const char*)(ROLE == R_F1 ? W1 : W0 + 64));
    const size_t kb = (size_t)64 * ldb * 2;
    auto wsrc = [&](int r, int i) {
      const int kbi = KQ == 2 ? (ROLE == R_PART2 ? 0 : (i >> 2) * 8) + ((2 * r + ((i >> 1) & 1)) ^ rot) : 2 * r + (i >> 2);
      return KQ == 2 ? ((i & 1) ? w1 : w0) + kbi * kb + (ROLE == R_PART2 ? (i >> 2) * 256 : 0) : w0 + kbi * kb + (i & 3) * 128;
    };
    auto wstage = [&](int r) {
      const unsigned ld = ldsb + (r & 1) * ABUF + wave * 1024;
#pragma unroll
      for (int i = 0; i < 8; ++i) {
        unsigned keep;
        asm volatile("s_mov_b32 %0, m0\n\ts_mov_b32 m0, %3\n\ts_nop 0\n\tglobal_load_lds_dwordx4 %1, %2" WNT_ "\n\ts_mov_b32 m0, %0"
                     : "=&s"(keep) : "v"(voff), "s"(wsrc(r, i)), "s"(ld + i * 8192) : "memory");
      }
    };
    const int brow = 8 * g + q4, bs = bswz(brow);
    unsigned la[2];
#pragma unroll
    for (int j = 0; j < 2; ++j) {
      int blk, c16, sub;
      if (ROLE == R_F1) { blk = j; c16 = nq * 2 + (p4 >> 1); sub = (p4 & 1) * 8; }
      else { blk = nq >> 1; c16 = (nq & 1) * 4 + p4; sub = j * 8; }
      int set = kq;
      if (ROLE == R_PART2 && TLP) { set = nq >> 1; blk = nq & 1; c16 = kq * 4 + p4; }
      la[j] = (unsigned)((KQ == 2 ? set * 4 + blk : blk) * 8192 + brow * 128 + ((c16 ^ bs) << 4) + sub);
    }
    wstage(0); wstage(1);
    u32x4 raw[2][8];
#pragma unroll
    for (int r = 0; r < 2; ++r)
#pragma unroll
      for (int i = 0; i < 8; ++i) asm volatile("global_load_dwordx4 %0, %1, %2" WNT_ : "=&v"(raw[r][i]) : "v"(voff), "s"(wsrc(r + 2, i)) : "memory");
    sfor<0, 4>([&](auto r_c) __attribute__((always_inline)) {
      constexpr int r = decltype(r_c)::value;
      if (r == 0) WAIT_V(24); else if (r == 1) WAIT_V(16);
      if (r == 3) BAR(); else if (r < 2) { __builtin_amdgcn_s_barrier(); asm volatile("" ::: "memory"); }
      gw_load<r>(bw[r], ldsb + (r & 1) * ABUF + la[0], ldsb + (r & 1) * ABUF + la[1]);
      if (r == 1 || r == 2) {
        u32x4 (&q)[8] = raw[(r + 1) & 1];
        if (r == 1) hook0();
        if (r == 1) asm volatile("s_waitcnt vmcnt(8)" : "+v"(q[0]), "+v"(q[1]), "+v"(q[2]), "+v"(q[3]), "+v"(q[4]), "+v"(q[5]), "+v"(q[6]), "+v"(q[7]) :: "memory");
        else asm volatile("s_waitcnt vmcnt(0)" : "+v"(q[0]), "+v"(q[1]), "+v"(q[2]), "+v"(q[3]), "+v"(q[4]), "+v"(q[5]), "+v"(q[6]), "+v"(q[7]) :: "memory");
#pragma unroll
        for (int i = 0; i < 8; ++i) *(u32x4*)(smem + ((r + 1) & 1) * ABUF + wave * 1024 + i * 8192 + lane * 16) = q[i];
        if (r == 1) BAR();
      }
      if (r == 3) { hook(); BAR(); }
    });
  }
}
#define DSR2(d0, d1, a0_, a1_, off) asm volatile("ds_read_b128 %0, %2 offset:%4\n\tds_read_b128 %1, %3 offset:%4" : "=&v"(d0), "=&v"(d1) : "v"(a0_), "v"(a1_), "i"(off))
template <int N> __device__ __forceinline__ void waitl(u32x4& a, u32x4& b) { asm volatile("s_waitcnt lgkmcnt(%2)" : "+v"(a), "+v"(b) : "i"(N)); }
__device__ __forceinline__ void glds1(const char* sbase, unsigned voff, unsigned lds) {
  unsigned keep;
  asm volatile("s_mov_b32 %0, m0\n\ts_mov_b32 m0, %3\n\ts_nop 0\n\tglobal_load_lds_dwordx4 %1, %2 sc1\n\ts_mov_b32 m0, %0"
               : "=&s"(keep) : "v"(voff), "s"(sbase), "s"(lds) : "memory");
}
#define XS(i) do { } while (0)
#define XFL(b_) do { } while (0)
#define XA_ 0
#define XF_ 0
#define XE_ 0
#define XP_ 0
#define PUBK 9
#define LATE 0
#define NOWP 2
#define NEARM 15
#define PNEAR 2
#define LATEQ 0
#define DEFP 1
#define ROTM 0
#define ROTK 7
#define BPACE 40
#define W2 1
#define PDEEP 4
template <int ROLE>
__device__ __forceinline__ void gemm7(char* smem, int wave_id_, const u16* W0, const u16* W1, int ldb, const char* A_, size_t a_blk, Dep dep,
                                      char* C_, size_t c_blk, unsigned* myprog, unsigned* tmo, int slot, unsigned long long* mts,
                                      bool gl, int nimm = 0, int nseg = NSEG, int wm0 = 0, int pofs = 0, unsigned long long tag = 0, unsigned* par = nullptr) {
  volatile LAS unsigned* wsl = (volatile LAS unsigned*)(smem + L_SLOT + 512);
  if (opaque_tid() < 144) *(volatile LAS unsigned*)(smem + L_SLOT + opaque_tid() * 4) = 0u;
  const int wave = WAVE_ID;
  const int kq = wave >> 2, nq = wave & 3;
  const unsigned ldsb = __builtin_amdgcn_readfirstlane((unsigned)(size_t)(LAS char*)smem);
  const int rot = (ROTM >> ROLE) & 1 ? slot & ROTK : 0;
  const char* A = uni(A_) + nq * 1024;
  constexpr bool NEAR = (NEARM >> ROLE) & 1;
  constexpr int AH = NEAR ? 2 : 3, PD = NEAR ? PNEAR : PDEEP;
  constexpr bool P2C = ROLE == R_PART2;
  int pw = 0;
  int wm = wm0, nh = 0;
  auto lpoll = [&](int b, bool hs = false) {
    unsigned w;
    if (wave == 4) {
      for (int spins = 0;; ++spins) {
        unsigned v;
        if (hs) { WAIT_V(0); v = *(volatile LAS unsigned*)(smem + L_SLOT + 768 + lane_id() * 4); }
        else asm volatile("s_nop 4\n\tglobal_load_dword %0, %1, %2 sc1\n\ts_waitcnt vmcnt(0)" : "=&v"(v) : "v"(dep.voff), "s"(dep.base) : "memory");
        w = dep_eval(v, dep);
        if ((int)w > b) break;
        if (!hs) __builtin_amdgcn_s_sleep(2);
        hs = false;
        if (give_up(spins, tmo, 9)) { w = NSEG; break; }
      }
      w = w < 255u ? w : 255u;
      if (lane_id() == 0) wsl[0] = ((unsigned)(b + 1) << 8) | w;
    } else {
      for (;;) {
        w = __builtin_amdgcn_readfirstlane(wsl[0]);
        if ((w >> 8) == (unsigned)(b + 1)) break;
        __builtin_amdgcn_s_sleep(1);
      }
      w &= 255u;
    }
    return (int)w;
  };
  auto pubw = [&](int n) {
    WAIT_V(0);
    if (lane_id() == 0) {
      const unsigned o = __hip_atomic_fetch_add((LAS unsigned*)(smem + L_SLOT + 516), 1u, __ATOMIC_RELAXED, __HIP_MEMORY_SCOPE_WORKGROUP);
      if ((o & 3u) == 3u) {
        st4_sc1(myprog, (unsigned)(n + pofs));
      }
    }
    asm volatile("s_waitcnt lgkmcnt(0)" ::: "memory");
  };
  auto hsrc = [&](int hh) { return A + (size_t)(hh >> 1) * a_blk + ((hh ^ (rot >> 2)) & 1) * 16384; };
  auto hlds = [&](int hh) { return ldsb + (unsigned)(((hh >> 1) & 1) * 65536 + (hh & 1) * 16384 + nq * 1024); };
#define POFF(i_) (((i_) & 1) * 32768 + ((i_) >> 1) * 4096)
#define PIECE(s_, l_, i_) asm volatile("s_add_u32 m0, %2, %3\n\ts_nop 0\n\tglobal_load_lds_dwordx4 %0, %1 sc1" :: "v"(av[i_]), "s"(s_), "s"(l_), "i"(POFF(i_)) : "memory", "scc", "m0")
  u32x32 BW[4];
  KTS(1);
  wload<ROLE, 2>(smem, wave_id_, W0, W1, ldb, BW, rot, [&]() {
    if (ROLE == R_QKV && nimm > 0 && wave == 7) ini_request((const char*)tmo - OFF_TMO, ldsb + L_GB);
  }, [&]() {
    if (ROLE == R_QKV && nimm > 0) {
      *par = ini_resolve((const char*)tmo - OFF_TMO, smem + L_GB, tag);
      dep.base += (*par >> 2) << 13; myprog += (*par >> 2) << 11;
      if (kq) {
        wm = lpoll(0);
        const unsigned l16 = lane_id() * 16;
#pragma unroll
        for (int i = 0; i < 16; ++i) glds1(hsrc(i >> 3) + (i & 1) * 32768 + ((((i & 7) >> 1) ^ rot) & 3) * 4096, l16, hlds(i >> 3) + POFF(i & 7));
      }
      nh = 2;
    }
  }, ROLE == R_QKV ? (unsigned long long*)((char*)tmo - OFF_TMO + ((size_t)246 << 20)) + (128 + slot) * 8 : nullptr);
  KTS(2);
  if (ROLE == R_QKV && nimm > 0 && !kq) wm = (int)(__builtin_amdgcn_readfirstlane(wsl[0]) & 255u);
  if (ROLE == R_QKV && opaque_tid() == 0) STS((char*)tmo - OFF_TMO, 128 + slot, 0);
  auto body = [&](auto ldr_c) __attribute__((always_inline)) {
    constexpr bool LDR = decltype(ldr_c)::value;
    if (XP_ == 0 ? !LDR : (XP_ == 1 ? LDR : false)) __builtin_amdgcn_s_setprio(1);
    unsigned av[8];
    const int lane = lane_id();
#pragma unroll
    for (int i = 0; i < 8; ++i) av[i] = lane * 16 + (P2C ? 0 : (i & 1) * 32768) + (((i >> 1) ^ rot) & 3) * 4096;
    auto issue_all = [&](int hh) {
      if (LDR) {
        const char* s_ = hsrc(hh); const unsigned l_ = hlds(hh);
#pragma unroll
        for (int i = 0; i < 8; ++i) if (!(P2C && (i & 1))) PIECE(s_, l_, i);
      }
    };
    int b = 0;
    bool hs = false;
    while (b < nseg) {
      const int ln = lane_id(), g = ln >> 4, r16 = ln & 15;
      const unsigned e0 = ldsb + (unsigned)((P2C ? 0 : kq * 32768) + (b & 1) * 65536 + (gl ? (g >> 1) * 512 + r16 * 32 + (g & 1) * 16 : r16 * 128 + ((g ^ (r16 & 7)) << 4)));
      const unsigned d0 = e0 ^ (gl ? 1024u : 64u);
      const unsigned rw = ldsb + L_RED + nq * 4096 + ln * 16;
      const unsigned co0 = ROLE == R_F1 ? (unsigned)((g & 1) * 2048 + nq * 512 + r16 * 32 + (g >> 1) * 16)
                         : ((ROLE == R_QKV ? TLQ : TLP) ? (unsigned)(nq * (P2C ? 4096 : 2048) + r16 * 64 + g * 16)
                         : (ROLE == R_QKV ? (unsigned)(r16 * 256 + (((4 * nq + g) ^ r16) << 4)) : (unsigned)(r16 * PROW + (32 * nq + 8 * g) * 2)));
      const unsigned co1 = co0 + ((ROLE == R_QKV ? TLQ : TLP) ? 1024u : (ROLE == R_QKV ? 4096u : 16u * PROW));
      u32x16 pv_;
      bool smp = false, lt0 = false;
      if ((wm < nseg ? wm : nseg) >= b + PD && b >= nimm && nh <= 2 * b + AH) {
        __builtin_amdgcn_s_barrier(); asm volatile("" ::: "memory");
        for (; nh < 2 * b + AH; ++nh) issue_all(nh);
        int lf = (wm < nseg ? wm : nseg) - (AH - 1), cp = nseg - (AH - 1);
        if (!LDR) KTS(128 + b);
        const unsigned sa = ldsb + L_SLOT + ln * 4, ad = dep.add - (AH - 1);
        if constexpr (LDR) (NEAR ? (P2C ? near_ld2 : near_ld0) : (P2C ? fast_ld2 : fast_ld0))(BW, pv_, e0, d0, b, lf, cp, sa, dep.mul, ad, rw, (unsigned)a_blk, hsrc(2 * b + AH), hsrc(2 * b + AH + 1), hlds(2 * b + AH), hlds(2 * b + AH + 1), av);
        else {
          const char* c0_ = uni((const char*)tmo - OFF_TMO + OFF_DUMP); const char* c1_ = uni(C_ + (size_t)b * c_blk);
          const int sf = b + pofs - (pw > 0 ? 1 : 0);
          const char* pg = uni((const char*)myprog);
          (NEAR ? (ROLE == R_F1 ? near_st1 : (P2C ? near_st2 : near_st0)) : (ROLE == R_F1 ? fast_st1 : (P2C ? fast_st2 : fast_st0)))(BW, pv_, e0, d0, b, lf, cp, sa, dep.mul, ad, rw, (unsigned)c_blk, c0_, c1_, co0, co1, pofs, sf, (unsigned)wave,
                                                                   ldsb + L_SLOT, dep.voff, dep.base, pg);
        }
        if (!LDR) KTS(64 + b);
        wm = lf + (AH - 1) > wm ? lf + (AH - 1) : wm;
        nh = 2 * b + AH;
        --b;
        __builtin_amdgcn_s_barrier(); asm volatile("" ::: "memory");
      } else if (NEAR && b >= BPACE) {
        if constexpr (LDR) {
          int cap = wave == 4 ? SPIN_CAP : 8 * SPIN_CAP;
          if (hs) WAIT_V(0);
          (P2C ? paced_ld2 : paced_ld0)(BW, e0, d0, b, nseg, (unsigned)a_blk, rw, ldsb + L_SLOT + 528 + nq * 4, hsrc(nh), hsrc(nh + 1), hlds(nh), cap, wm, (unsigned)(nh - 2 * b),
                                        dep.mul, dep.add, dep.voff, dep.base, wave == 4 ? ldsb + L_SLOT : 0u, ldsb + L_SLOT + ln * 4, av);
          if (cap == 0 && ln == 0) st4_sc1(tmo, 15u);
        } else (ROLE == R_F1 ? paced_st1 : (P2C ? paced_st2 : paced_st0))(BW, e0, d0, b, nseg, (unsigned)c_blk, rw, ldsb + L_SLOT + 528 + nq * 4, uni(C_ + (size_t)b * c_blk),
                                                                          __builtin_amdgcn_readfirstlane(pw > 0 ? 1u : 0u), pofs, ldsb + L_SLOT + 516, uni((const char*)myprog), co0, co1);
        break;
      } else {
        const bool pl = wm <= b;
        XS(0);
        if (pl && LDR) wm = lpoll(b, hs);
        XS(1);
        const bool cold = nh < 2 * b + 2;
        { const int upto = nh == 2 * b + 3 ? 2 * b + 4 : 2 * b + 2; for (; nh < upto; ++nh) issue_all(nh); }
        smp = nh == 2 * b + 2 && b + 1 < nseg && wm0 < nseg;
        if (LDR && wave == 4 && smp)
          asm volatile("s_mov_b32 m0, %0\n\ts_nop 3\n\tglobal_load_lds_dword %1, %2 sc1" :: "s"(ldsb + L_SLOT + (b & 1) * 256), "v"(dep.voff), "s"(dep.base) : "memory", "m0");
        XS(2);
        if (LDR && LEAN_QB) { if (nh >= 2 * b + 4) { if (P2C) WAIT_V(14); else WAIT_V(28); } else { if (P2C) WAIT_V(6); else WAIT_V(12); } }
        else if (LDR) { if (nh >= 2 * b + 4) { if (P2C) WAIT_V(12); else WAIT_V(24); } else { if (P2C) WAIT_V(4); else WAIT_V(8); } }
        BAR();
        XS(3);
        if (pl && !LDR) wm = (int)(__builtin_amdgcn_readfirstlane(wsl[0]) & 255u);
        const bool dm = nh == 2 * b + 2 && wm > b + 1 && b + 1 < nseg;
        if (ROLE == R_QKV && b < 2 && opaque_tid() == 0) STS((char*)tmo - OFF_TMO, 128 + slot, 1 + b);
        lt0 = smp && !dm;
        if constexpr (LDR) {
          if (dm) (P2C ? lean_dm1 : lean_dm0)(BW, pv_, e0, d0, 0u, hlds(nh), hsrc(nh), hsrc(nh + 1), av);
          else (P2C ? lean_ld1 : lean_ld0)(BW, pv_, e0, d0, (unsigned)(nh - 2 * b - 2), hlds(nh), hsrc(nh), hsrc(nh + 1), av, ldsb + L_SLOT + (b & 1) * 256 + ln * 4, dep.mul, dep.add, LATEQ && lt0 ? b + 1 : 0x7FFFFFFF,
                                           __builtin_amdgcn_readfirstlane(lt0 && wave == 4 ? ldsb + L_SLOT + 768 : 0u), dep.voff, dep.base);
        }
        else lean_st0(BW, pv_, e0, d0, __builtin_amdgcn_readfirstlane(pw > 0 ? (cold ? 4u : 1u) : 0u), (unsigned)(pw + pofs), ldsb + L_SLOT + 516, ldsb + L_SLOT + 256,
                     dep.voff, dep.base, uni((const char*)myprog));
        if (dm) nh += 2;
        XS(5);
      }
      if constexpr (LDR) fin_ld(pv_, rw, ldsb + L_SLOT + 528 + nq * 4, (unsigned)(b + 1));
      else (ROLE == R_F1 ? fin_st1 : (P2C ? fin_st2 : fin_st0))(pv_, rw, ldsb + L_SLOT + 528 + nq * 4, (unsigned)(b + 1), uni(C_ + (size_t)b * c_blk), co0, co1);
      XS(6);
      if (smp) {
        const int w = (int)dep_eval(*(volatile LAS unsigned*)(smem + L_SLOT + (b & 1) * 256 + lane_id() * 4), dep);
        if (LATEQ && lt0 && w > b + 1) nh += 2;
        wm = w > wm ? w : wm;
      }
      bool now = (NOWP == 2 ? nh < 2 * b + 4 && wm < b + 1 + PD : wm <= b + 1) || b + 1 >= nseg || b < nimm;
      if (!LDR && now) {
        if (DEFP && b >= nimm && b + 1 < nseg && (wm > b + 1 || (__builtin_amdgcn_readfirstlane(wsl[0]) >> 8) == (unsigned)(b + 2))) now = false;
        else pubw(b + 1);
      }
      XS(7);
      XFL(b);
      pw = now ? 0 : b + 1;
      hs = W2 && lt0;
      ++b;
    }
    __builtin_amdgcn_s_setprio(0);
  };
  if (kq) body(std::true_type{}); else body(std::false_type{});
#undef PIECE
#undef POFF
  WAIT_V(0);
  BAR();
  KTS(3);
}
__device__ __forceinline__ void fin_role(char* smem, int wave_id_, int fidx, const float* mod, const char* P2, const u16* x1, u16* out, unsigned* cnt, unsigned* tmo,
                                         unsigned long long* mts) {
  const int tid = opaque_tid(), lane = tid & 63, wave = WAVE_ID;
  row_table(smem + L_GB, mod, cnt, tmo, tid);
  volatile LAS unsigned* wsl = (volatile LAS unsigned*)(smem + L_SLOT + 512);
  int wf = 0, par = 0;
  for (int nf = fidx >> 3; nf < NSEG; nf += 2) {
    if (wf <= nf) {
      if (wave == 0) {
        unsigned w;
        for (int spins = 0;; ++spins) {
          const unsigned v = rowmin16(ld4_sc1(cnt + (PF2 + ((lane & 31) < 28 ? (lane & 31) : 0)) * 32));
          w = min(rdl(v, 0), rdl(v, 16));
          if ((int)w > nf) break;
          __builtin_amdgcn_s_sleep(2);
          if (give_up(spins, tmo, 9)) { w = NSEG; break; }
        }
        if (lane == 0) wsl[par] = w;
      }
      BAR();
      wf = (int)__builtin_amdgcn_readfirstlane(wsl[par]);
      par ^= 1;
    }
    int ln = lane; asm volatile("" : "+v"(ln));
    if (wave == 0) WTS(256 + 8 + fidx, nf / 2, 0);
    fin_half(P2, x1, smem + L_GB + 12288, out, nf * 32 + 4 * (fidx & 7) + (wave >> 1), wave & 1, ln);
    if (wave == 0) WTS(256 + 8 + fidx, nf / 2, 2);
  }
}
constexpr int Q_MEM = 0;
constexpr int Q_QK  = 16384;
constexpr int Q_TM  = 22528;
constexpr int Q_PQ  = 24576;
constexpr int Q_K   = 32768;
constexpr int Q_RING = 32768;
constexpr int NCHAIN = 96;
#define NQQ 6
#define NQK 4
#define E_AG 8
#define RDYI -1
#define ROTW 2
#define DQU 6
#define DQM 18
#define DQD 1
#define ORSEL (xg >> 1)
#define D_Q 85
#define D_O 5
#define D_A 0
#define WOF 1
#define E_S -1
#define RAE 0
#define SPUB 62
#define QDL 0
#define KFB 1
#define RAW 5
#define E_MIN 3
#define E_W 3
#define DOU 0
#define DOM 12
#define CL0 1
#define TSW 0
#define E_B 8
#define E_O 1900
#define KDEL 5
#define VPOS 0

typedef __bf16 bf16x16 __attribute__((ext_vector_type(16)));
typedef unsigned u32x8 __attribute__((ext_vector_type(8)));
__device__ __forceinline__ f32x4 SMF(u32x4 a, u32x8 b, f32x4 c, int idx) {
  return __builtin_amdgcn_smfmac_f32_16x16x64_bf16(__builtin_bit_cast(bf16x8, a), __builtin_bit_cast(bf16x16, b), c, idx, 0, 0);
}
__device__ __forceinline__ float fold32(float a, float b) {
  const auto p = __builtin_amdgcn_permlane32_swap(__float_as_uint(a), __float_as_uint(b), false, false);
  return __uint_as_float(p[0]) + __uint_as_float(p[1]);
}
__device__ __forceinline__ f32x2 rowsum(f32x4 x) { return f32x2{fold32(x[0], x[2]), fold32(x[1], x[3])}; }
__device__ __forceinline__ unsigned nosent1(unsigned v) { return v < 0xFFFFFFFEu ? v : 0xFFFFFFFEu; }
__device__ __forceinline__ void st4s(const char* sb, unsigned vo, unsigned v) { asm volatile("global_store_dword %0, %1, %2 sc1\n\ts_nop 0" :: "v"(vo), "v"(v), "s"(sb) : "memory"); }
__device__ __forceinline__ void st8s(const char* sb, unsigned vo, u32x2 v) { asm volatile("global_store_dwordx2 %0, %1, %2 sc1\n\ts_nop 1" :: "v"(vo), "v"(v), "s"(sb) : "memory"); }
__device__ __forceinline__ void gldsw(const char* sbase, unsigned voff, unsigned lds) {
  unsigned keep;
  asm volatile("s_mov_b32 %0, m0\n\ts_mov_b32 m0, %3\n\ts_nop 0\n\tglobal_load_lds_dwordx4 %1, %2" CNT_ "\n\ts_mov_b32 m0, %0"
               : "=&s"(keep) : "v"(voff), "s"(sbase), "s"(lds) : "memory");
}
#define PWC 1
#define PWL 0
__device__ __forceinline__ void wait_clk(unsigned t0, unsigned d) {
  while ((unsigned)__builtin_amdgcn_s_memtime() - t0 < d) __builtin_amdgcn_s_sleep(PWC);
}
__device__ __forceinline__ unsigned bell_time(const char* a, unsigned vo, unsigned tag, unsigned cap, unsigned v = 0) {
  for (unsigned spins = 0; !__all((v >> 24) >= tag); ++spins) {
    asm volatile("s_nop 4\n\tglobal_load_dword %0, %1, %2 sc1\n\ts_waitcnt vmcnt(0)" : "=&v"(v) : "v"(vo), "s"(a) : "memory");
    if (__all((v >> 24) >= tag) || spins >= cap) break;
  }
  const unsigned now = (unsigned)__builtin_amdgcn_s_memrealtime() + 250u;
  const unsigned age = rowmin16((now - v) & 0xFFFFFFu);
  unsigned r = __builtin_amdgcn_readfirstlane(now - min(min(rdl(age, 0), rdl(age, 16)), 500u));
  asm volatile("" : "+s"(r));
  return r;
}
#define PWS 0
__device__ __forceinline__ void wait_rt(unsigned t0, int d) {
  for (;;) {
    const int r = d - (int)((unsigned)__builtin_amdgcn_s_memrealtime() - t0);
    if (r <= 0) break;
    if (PWS && r > 24) __builtin_amdgcn_s_sleep(6); else if (PWS && r > 9) __builtin_amdgcn_s_sleep(2);
  }
}
#define WBAR() do { __builtin_amdgcn_s_barrier(); asm volatile("" ::: "memory"); } while (0)
#define SB() __builtin_amdgcn_sched_barrier(0)
#define TN(k, v) do { } while (0)
template <bool HALF>
__device__ __forceinline__ void stage_proj(const char* src, unsigned lds, int tid, int wave) {
  const int row = tid >> 4, f = 2 * ((row & 1) | (((row >> 2) & 3) << 1));
#pragma unroll
  for (int j = 0; j < 4; ++j) {
    const int pc = (tid & 15) ^ f;
    gldsw(src + (size_t)j * 32 * HD * 2, (unsigned)(HALF ? (row + (pc >> 3) * 128) * HD * 2 + ((pc & 7) << 4) : row * HD * 2 + (pc << 4)), lds + (wave * 64 + 512 * j) * 16);
  }
}
__device__ __forceinline__ unsigned proj_base(int T, int lane) {
  const int g = lane >> 4, q4 = (lane & 15) >> 2, p4 = lane & 3, fs = 2 * ((q4 & 1) | ((g & 1) << 1) | ((q4 >> 1) << 2));
  return (unsigned)((16 * (g >> 1) + 4 * (g & 1) + (q4 & 1) + 8 * (q4 >> 1)) * 256 + (((2 * T) ^ fs) << 4) + p4 * 8);
}
__device__ __forceinline__ f32x4 zero4() { float z = 0.0f; asm volatile("" : "+v"(z)); return f32x4{z, z, z, z}; }
__device__ __forceinline__ unsigned lds_word(const char* p) { return __builtin_amdgcn_readfirstlane(*(volatile LAS unsigned*)p); }

#define MFMA16(a, b, c) __builtin_amdgcn_mfma_f32_16x16x16bf16_1k((a), (b), (c), 0, 0, 0)
#define UF(x) __uint_as_float(x)
__device__ __forceinline__ unsigned umax3(unsigned a, unsigned b, unsigned c) { return max(max(a, b), c); }
__device__ __forceinline__ f32x2 logit2(float a, float b) {
  return f32x2{a, b};
}
constexpr float LSC = LOG2E * SCALE;

#define FCS() do { } while (0)
struct QKW { u32x32 w0, w1, w2, w3, w4, w5; };
template <int W_> __device__ __forceinline__ void qk_step(const QKW& W, const char* ag, const char* vs, const char* v2, const char* od, unsigned pb, unsigned sn, unsigned& cap, unsigned& sp,
    unsigned long long& tc, unsigned long long* ts, unsigned& d0, unsigned& d1);
template <> __device__ __forceinline__ void qk_step<0>(const QKW& W, const char* ag, const char* vs, const char* v2, const char* od, unsigned pb, unsigned sn, unsigned& cap, unsigned& sp,
    unsigned long long& tc, unsigned long long* ts, unsigned& d0, unsigned& d1) {
  unsigned st, bg;
  asm volatile("v_mbcnt_lo_u32_b32 v60, -1, 0\n\t"
    "v_mbcnt_hi_u32_b32 v60, -1, v60\n\t"
    "v_lshlrev_b32 v5, 4, v60\n\t"
    "s_mov_b32 %[sp], 0\n\t"
    "s_setprio 2\n\t"
    "global_load_dwordx4 v[24:27], v5, %[ag] sc1\n\t"
    "global_load_dwordx4 v[28:31], v5, %[ag] offset:1024 sc1\n\t"
    "s_memtime %[tc]\n\t"
    "v_lshrrev_b32 v32, 4, v60\n\t"
    "v_and_b32 v33, 15, v60\n\t"
    "v_lshrrev_b32 v34, 2, v33\n\t"
    "v_and_b32 v35, 3, v60\n\t"
    "v_and_b32 v61, 1, v60\n\t"
    "v_lshlrev_b32 v61, 3, v61\n\t"
    "v_lshl_or_b32 v7, v34, 4, v61\n\t"
    "v_lshl_or_b32 v7, v32, 7, v7\n\t"
    "v_xor_b32 v10, v32, v33\n\t"
    "v_lshlrev_b32 v10, 4, v10\n\t"
    "v_lshl_or_b32 v10, v33, 8, v10\n\t"
    "v_or_b32 v10, %[pb], v10\n\t"
    "v_xor_b32 v11, 0x40, v10\n\t"
    "v_xor_b32 v12, 0x80, v10\n\t"
    "v_xor_b32 v13, 0xc0, v10\n\t"
    "v_lshl_or_b32 v34, v32, 2, v34\n\t"
    "v_lshrrev_b32 v35, 1, v35\n\t"
    "v_or_b32 v35, 0, v35\n\t"
    "v_xor_b32 v35, v35, v34\n\t"
    "v_lshl_or_b32 v14, v35, 4, v61\n\t"
    "v_lshl_or_b32 v14, v34, 8, v14\n\t"
    "v_or_b32 v14, %[pb], v14\n\t"
    "v_and_b32 v61, 3, v60\n\t"
    "v_lshlrev_b32 v61, 6, v61\n\t"
    "v_lshl_or_b32 v4, v32, 4, v61\n\t"
    "v_and_b32 v61, 4, v60\n\t"
    "v_lshl_or_b32 v4, v61, 10, v4\n\t"
    "v_and_b32 v61, 8, v60\n\t"
    "v_lshl_or_b32 v4, v61, 14, v4\n\t"
    "v_or_b32 v4, 0, v4\n\t"
    "v_and_b32 v61, 2, v60\n\t"
    "v_mul_u32_u24 v61, 0x5555, v61\n\t"
    "v_or_b32 v6, %[c44], v61\n\t"
    "v_lshlrev_b32 v61, 2, v32\n\t"
    "v_lshl_or_b32 v15, v33, 4, v61\n\t"
    "3:\n\t"
    "v_mov_b32 v16, 0\n\t"
    "v_mov_b32 v17, 0\n\t"
    "v_mov_b32 v18, 0\n\t"
    "v_mov_b32 v19, 0\n\t"
    "v_mov_b32 v20, 0\n\t"
    "v_mov_b32 v21, 0\n\t"
    "v_mov_b32 v22, 0\n\t"
    "v_mov_b32 v23, 0\n\t"
    "s_waitcnt vmcnt(0) lgkmcnt(0)\n\t"
    "ds_write_b128 v5, v[24:27] offset:0\n\t"
    "ds_write_b128 v5, v[28:31] offset:1024\n\t"
    "v_smfmac_f32_16x16x64_bf16 v[16:19], v[24:27], v[64:71], v6\n\t"
    "v_max3_u32 v60, v24, v25, v26\n\t"
    "v_max3_u32 v61, v27, v28, v29\n\t"
    "v_smfmac_f32_16x16x64_bf16 v[20:23], v[24:27], v[72:79], v6\n\t"
    "v_max3_u32 v60, v60, v30, v31\n\t"
    "v_max_u32 v60, v60, v61\n\t"
    "v_cmp_eq_u32 vcc, -1, v60\n\t"
    "s_cbranch_vccnz 1f\n\t"
    "2:\n\t"
    "s_waitcnt lgkmcnt(0)\n\t"
    "s_barrier\n\t"
    "ds_read_b128 v[32:35], v5 offset:2048\n\t"
    "ds_read_b128 v[36:39], v5 offset:3072\n\t"
    "v_smfmac_f32_16x16x64_bf16 v[16:19], v[28:31], v[80:87], v6\n\t"
    "ds_read_b128 v[40:43], v5 offset:4096\n\t"
    "ds_read_b128 v[44:47], v5 offset:5120\n\t"
    "v_smfmac_f32_16x16x64_bf16 v[20:23], v[28:31], v[88:95], v6\n\t"
    "s_waitcnt lgkmcnt(3)\n\t"
    "v_smfmac_f32_16x16x64_bf16 v[16:19], v[32:35], v[96:103], v6\n\t"
    "ds_read_b128 v[48:51], v5 offset:6144\n\t"
    "v_smfmac_f32_16x16x64_bf16 v[20:23], v[32:35], v[104:111], v6\n\t"
    "ds_read_b128 v[52:55], v5 offset:7168\n\t"
    "s_waitcnt lgkmcnt(4)\n\t"
    "v_smfmac_f32_16x16x64_bf16 v[16:19], v[36:39], v[112:119], v6\n\t"
    "ds_read_b128 v[24:27], v5 offset:8192\n\t"
    "v_smfmac_f32_16x16x64_bf16 v[20:23], v[36:39], v[120:127], v6\n\t"
    "ds_read_b128 v[28:31], v5 offset:9216\n\t"
    "s_waitcnt lgkmcnt(5)\n\t"
    "v_smfmac_f32_16x16x64_bf16 v[16:19], v[40:43], v[128:135], v6\n\t"
    "ds_read_b128 v[56:59], v5 offset:10240\n\t"
    "v_smfmac_f32_16x16x64_bf16 v[20:23], v[40:43], v[136:143], v6\n\t"
    "ds_read_b128 v[60:63], v5 offset:11264\n\t"
    "ds_read_b128 v[36:39], v11 offset:36864\n\t"
    "s_waitcnt lgkmcnt(7)\n\t"
    "v_smfmac_f32_16x16x64_bf16 v[16:19], v[44:47], v[144:151], v6\n\t"
    "ds_read_b128 v[40:43], v12 offset:32768\n\t"
    "v_smfmac_f32_16x16x64_bf16 v[20:23], v[44:47], v[152:159], v6\n\t"
    "ds_read_b128 v[44:47], v12 offset:36864\n\t"
    "s_waitcnt lgkmcnt(8)\n\t"
    "v_smfmac_f32_16x16x64_bf16 v[16:19], v[48:51], v[160:167], v6\n\t"
    "v_smfmac_f32_16x16x64_bf16 v[20:23], v[48:51], v[168:175], v6\n\t"
    "ds_read_b128 v[48:51], v13 offset:32768\n\t"
    "s_waitcnt lgkmcnt(8)\n\t"
    "v_smfmac_f32_16x16x64_bf16 v[16:19], v[52:55], v[176:183], v6\n\t"
    "v_smfmac_f32_16x16x64_bf16 v[20:23], v[52:55], v[184:191], v6\n\t"
    "ds_read_b128 v[52:55], v13 offset:36864\n\t"
    "s_waitcnt lgkmcnt(8)\n\t"
    "v_smfmac_f32_16x16x64_bf16 v[16:19], v[24:27], v[192:199], v6\n\t"
    "v_smfmac_f32_16x16x64_bf16 v[20:23], v[24:27], v[200:207], v6\n\t"
    "ds_read_b128 v[24:27], v10 offset:32768\n\t"
    "s_waitcnt lgkmcnt(8)\n\t"
    "v_smfmac_f32_16x16x64_bf16 v[16:19], v[28:31], v[208:215], v6\n\t"
    "ds_read_b32 v8, v15 offset:28672\n\t"
    "ds_read_b128 v[32:35], v5 offset:24576\n\t"
    "s_setprio 3\n\t"
    "s_waitcnt lgkmcnt(9)\n\t"
    "v_smfmac_f32_16x16x64_bf16 v[16:19], v[56:59], v[224:231], v6\n\t"
    "s_waitcnt lgkmcnt(8)\n\t"
    "v_smfmac_f32_16x16x64_bf16 v[16:19], v[60:63], v[240:247], v6\n\t"
    "v_smfmac_f32_16x16x64_bf16 v[20:23], v[28:31], v[216:223], v6\n\t"
    "s_waitcnt lgkmcnt(0)\n\t"
    "21:\n\t"
    "v_cmp_ne_u32 vcc, %[sn], v8\n\t"
    "s_cbranch_vccnz 22f\n\t"
    "v_mul_f32 v32, %[lsc], v32\n\t"
    "v_mul_f32 v33, %[lsc], v33\n\t"
    "v_smfmac_f32_16x16x64_bf16 v[20:23], v[56:59], v[232:239], v6\n\t"
    "v_mul_f32 v34, %[lsc], v34\n\t"
    "v_mul_f32 v35, %[lsc], v35\n\t"
    "v_add_f32 v16, v16, v18\n\t"
    "v_add_f32 v17, v17, v19\n\t"
    "v_smfmac_f32_16x16x64_bf16 v[20:23], v[60:63], v[248:255], v6\n\t"
    "v_fma_f32 v16, v16, %[lsc], v32\n\t"
    "v_fma_f32 v17, v17, %[lsc], v33\n\t"
    "v_cvt_pk_bf16_f32 v16, v16, v17\n\t"
    "ds_write_b32 v15, v16 offset:16384\n\t"
    "s_mov_b32 %[bg], %[big]\n\t"
    "s_nop 2\n\t"
    "v_add_f32 v20, v20, v22\n\t"
    "v_add_f32 v21, v21, v23\n\t"
    "v_fma_f32 v20, v20, %[lsc], v34\n\t"
    "v_fma_f32 v21, v21, %[lsc], v35\n\t"
    "v_cvt_pk_bf16_f32 v20, v20, v21\n\t"
    "ds_write_b32 v15, v20 offset:16640\n\t"
    "ds_read_b128 v[28:31], v10 offset:36864\n\t"
    "ds_read_b128 v[32:35], v11 offset:32768\n\t"
    "s_waitcnt lgkmcnt(2)\n\t"
    "s_barrier\n\t"
    "ds_read_b64_tr_b16 v[16:17], v7 offset:16384\n\t"
    "ds_read_b64_tr_b16 v[18:19], v7 offset:16448\n\t"
    "ds_read_b64_tr_b16 v[20:21], v7 offset:16896\n\t"
    "ds_read_b64_tr_b16 v[22:23], v7 offset:16960\n\t"
    "ds_read_b64_tr_b16 v[56:57], v7 offset:17408\n\t"
    "ds_read_b64_tr_b16 v[58:59], v7 offset:17472\n\t"
    "ds_read_b64_tr_b16 v[60:61], v7 offset:17920\n\t"
    "ds_read_b64_tr_b16 v[62:63], v7 offset:17984\n\t"
    "global_load_dwordx4 v[6:9], v5, %[vs] sc1\n\t"
    "global_load_dwordx4 v[10:13], v5, %[vs] offset:1024 sc1\n\t"
    "s_waitcnt lgkmcnt(6)\n\t"
    "v_mfma_f32_16x16x32_bf16 v[24:27], v[24:27], v[16:19], 0\n\t"
    "v_mfma_f32_16x16x32_bf16 v[28:31], v[28:31], v[16:19], 0\n\t"
    "s_waitcnt lgkmcnt(4)\n\t"
    "v_mfma_f32_16x16x32_bf16 v[24:27], v[32:35], v[20:23], v[24:27]\n\t"
    "v_mfma_f32_16x16x32_bf16 v[28:31], v[36:39], v[20:23], v[28:31]\n\t"
    "v_xor_b32 v39, 32, v14\n\t"
    "ds_read_b64_tr_b16 v[32:33], v14 offset:40960\n\t"
    "ds_read_b64_tr_b16 v[34:35], v14 offset:45056\n\t"
    "ds_read_b64_tr_b16 v[36:37], v39 offset:40960\n\t"
    "ds_read_b64_tr_b16 v[38:39], v39 offset:45056\n\t"
    "s_waitcnt lgkmcnt(6)\n\t"
    "v_mfma_f32_16x16x32_bf16 v[24:27], v[40:43], v[56:59], v[24:27]\n\t"
    "v_mfma_f32_16x16x32_bf16 v[28:31], v[44:47], v[56:59], v[28:31]\n\t"
    "s_waitcnt lgkmcnt(4)\n\t"
    "v_mfma_f32_16x16x32_bf16 v[24:27], v[48:51], v[60:63], v[24:27]\n\t"
    "v_mfma_f32_16x16x32_bf16 v[28:31], v[52:55], v[60:63], v[28:31]\n\t"
    "v_mov_b32 v52, %[one]\n\t"
    "v_mov_b32 v53, %[one]\n\t"
    "v_mov_b32 v54, %[one]\n\t"
    "v_mov_b32 v55, %[one]\n\t"
    "v_mov_b32 v45, 0\n\t"
    "v_mov_b32 v47, 0\n\t"
    "v_mov_b32 v48, %[one]\n\t"
    "v_max3_f32 v14, |v24|, |v25|, |v26|\n\t"
    "v_max3_f32 v40, |v27|, |v28|, |v29|\n\t"
    "v_max3_f32 v14, v14, |v30|, |v31|\n\t"
    "v_max_f32 v14, v14, v40\n\t"
    "v_cmp_nge_f32 vcc, %[big], v14\n\t"
    "v_mov_b32 v14, 0\n\t"
    "s_cbranch_vccnz 12f\n\t"
    "13:\n\t"
    "v_exp_f32 v24, v24\n\t"
    "v_exp_f32 v25, v25\n\t"
    "v_exp_f32 v26, v26\n\t"
    "v_exp_f32 v27, v27\n\t"
    "v_exp_f32 v28, v28\n\t"
    "v_exp_f32 v29, v29\n\t"
    "v_exp_f32 v30, v30\n\t"
    "v_exp_f32 v31, v31\n\t"
    "v_cvt_pk_bf16_f32 v40, v24, v25\n\t"
    "v_cvt_pk_bf16_f32 v41, v26, v27\n\t"
    "v_cvt_pk_bf16_f32 v42, v28, v29\n\t"
    "v_cvt_pk_bf16_f32 v43, v30, v31\n\t"
    "v_mov_b32 v49, 0\n\t"
    "v_mov_b32 v51, 0\n\t"
    "s_waitcnt lgkmcnt(0)\n\t"
    "v_mfma_f32_16x16x32_bf16 v[52:55], v[52:55], v[40:43], 0\n\t"
    "v_mfma_f32_16x16x32_bf16 v[24:27], v[32:35], v[40:43], 0\n\t"
    "v_mfma_f32_16x16x32_bf16 v[28:31], v[36:39], v[40:43], 0\n\t"
    "s_waitcnt vmcnt(1)\n\t"
    "5:\n\t"
    "v_mfma_f32_16x16x32_bf16 v[32:35], v[6:9], v[16:19], 0\n\t"
    "v_mfma_f32_16x16x32_bf16 v[36:39], v[6:9], v[20:23], 0\n\t"
    "s_waitcnt vmcnt(0)\n\t"
    "v_mfma_f32_16x16x32_bf16 v[32:35], v[10:13], v[56:59], v[32:35]\n\t"
    "v_mfma_f32_16x16x32_bf16 v[36:39], v[10:13], v[60:63], v[36:39]\n\t"
    "ds_read_b32 v44, v15 offset:18432\n\t"
    "ds_read_b32 v46, v15 offset:18688\n\t"
    "v_max3_u32 v40, v6, v7, v8\n\t"
    "v_max3_u32 v41, v9, v10, v11\n\t"
    "v_max3_u32 v40, v40, v12, v13\n\t"
    "v_max_u32 v40, v40, v41\n\t"
    "v_cmp_eq_u32 vcc, -1, v40\n\t"
    "s_cbranch_vccnz 4f\n\t"
    "6:\n\t"
    "s_nop 0\n\t"
    "v_add_f32 v32, v32, v38\n\t"
    "v_add_f32 v33, v33, v39\n\t"
    "v_exp_f32 v34, v32\n\t"
    "v_max_f32 v40, v32, v33\n\t"
    "v_exp_f32 v35, v33\n\t"
    "v_cmp_nge_f32 vcc, %[bg], v40\n\t"
    "s_cbranch_vccnz 10f\n\t"
    "11:\n\t"
    "v_cvt_pk_bf16_f32 v50, v34, v35\n\t"
    "17:\n\t"
    "s_waitcnt vmcnt(0) lgkmcnt(0)\n\t"
    "v_max_u32 v40, v44, v46\n\t"
    "v_cmp_eq_u32 vcc, -1, v40\n\t"
    "s_cbranch_vccnz 18f\n\t"
    "19:\n\t"
    "v_mfma_f32_16x16x16_bf16 v[52:55], v[48:49], v[50:51], v[52:55]\n\t"
    "v_mfma_f32_16x16x16_bf16 v[24:27], v[44:45], v[50:51], v[24:27]\n\t"
    "v_mfma_f32_16x16x16_bf16 v[28:31], v[46:47], v[50:51], v[28:31]\n\t"
    "s_nop 5\n\t"
    "v_rcp_f32 v42, v52\n\t"
    "s_nop 0\n\t"
    "v_pk_mul_f32 v[24:25], v[24:25], v[42:43] op_sel_hi:[1,0]\n\t"
    "v_pk_mul_f32 v[26:27], v[26:27], v[42:43] op_sel_hi:[1,0]\n\t"
    "v_pk_mul_f32 v[28:29], v[28:29], v[42:43] op_sel_hi:[1,0]\n\t"
    "v_pk_mul_f32 v[30:31], v[30:31], v[42:43] op_sel_hi:[1,0]\n\t"
    "v_cvt_pk_bf16_f32 v36, v24, v25\n\t"
    "v_cvt_pk_bf16_f32 v37, v26, v27\n\t"
    "v_cvt_pk_bf16_f32 v38, v28, v29\n\t"
    "v_cvt_pk_bf16_f32 v39, v30, v31\n\t"
    "v_min_u32 v36, -2, v36\n\t"
    "v_min_u32 v37, -2, v37\n\t"
    "v_min_u32 v38, -2, v38\n\t"
    "v_min_u32 v39, -2, v39\n\t"
    "global_store_dwordx4 v4, v[36:39], %[od] sc1\n\t"
    "ds_write_b128 v5, v[6:9] offset:20480\n\t"
    "s_setprio 1\n\t"
    "s_waitcnt lgkmcnt(0)\n\t"
    "s_barrier\n\t"
    "s_waitcnt lgkmcnt(0)\n\t"
    "s_branch 9f\n\t"
    "1:\n\t"
    "s_add_u32 %[sp], %[sp], 1\n\t"
    "s_cmp_ge_u32 %[sp], %[cap]\n\t"
    "s_cbranch_scc1 7f\n\t"
    "global_load_dwordx4 v[24:27], v5, %[ag] sc1\n\t"
    "global_load_dwordx4 v[28:31], v5, %[ag] offset:1024 sc1\n\t"
    "s_waitcnt lgkmcnt(0)\n\t"
    "s_memtime %[tc]\n\t"
    "s_nop 7\n\t"
    "s_branch 3b\n\t"
    "7:\n\t"
    "s_mov_b32 %[cap], 0\n\t"
    "s_branch 2b\n\t"
    "4:\n\t"
    "s_add_u32 %[sp], %[sp], 0x10000\n\t"
    "s_lshr_b32 %[st], %[sp], 16\n\t"
    "s_cmp_ge_u32 %[st], %[cap]\n\t"
    "s_cbranch_scc1 8f\n\t"
    "global_load_dwordx4 v[6:9], v5, %[vs] sc1\n\t"
    "global_load_dwordx4 v[10:13], v5, %[vs] offset:1024 sc1\n\t"
    "s_waitcnt vmcnt(0) lgkmcnt(0)\n\t"
    "s_branch 5b\n\t"
    "8:\n\t"
    "s_mov_b32 %[cap], 0\n\t"
    "s_branch 6b\n\t"
    "18:\n\t"
    "s_add_u32 %[sp], %[sp], 0x10000\n\t"
    "s_lshr_b32 %[st], %[sp], 16\n\t"
    "s_cmp_ge_u32 %[st], %[cap]\n\t"
    "s_cbranch_scc1 20f\n\t"
    "ds_read_b32 v44, v15 offset:18432\n\t"
    "ds_read_b32 v46, v15 offset:18688\n\t"
    "s_waitcnt vmcnt(0) lgkmcnt(0)\n\t"
    "s_branch 17b\n\t"
    "20:\n\t"
    "s_mov_b32 %[cap], 0\n\t"
    "s_branch 19b\n\t"
    "22:\n\t"
    "ds_read_b32 v8, v15 offset:28672\n\t"
    "ds_read_b128 v[32:35], v5 offset:24576\n\t"
    "s_waitcnt lgkmcnt(0)\n\t"
    "s_branch 21b\n\t"
    "10:\n\t"
    "v_max_f32 v40, v40, v14\n\t"
    "v_mov_b32 v42, v40\n\t"
    "s_nop 1\n\t"
    "v_permlane16_swap_b32 v40, v42\n\t"
    "v_max_f32 v40, v40, v42\n\t"
    "v_mov_b32 v42, v40\n\t"
    "s_nop 1\n\t"
    "v_permlane32_swap_b32 v40, v42\n\t"
    "v_max_f32 v40, v40, v42\n\t"
    "v_sub_f32 v34, v32, v40\n\t"
    "v_sub_f32 v35, v33, v40\n\t"
    "v_sub_f32 v40, v14, v40\n\t"
    "v_exp_f32 v40, v40\n\t"
    "v_exp_f32 v34, v34\n\t"
    "v_exp_f32 v35, v35\n\t"
    "v_mul_f32 v24, v24, v40\n\t"
    "v_mul_f32 v25, v25, v40\n\t"
    "v_mul_f32 v26, v26, v40\n\t"
    "v_mul_f32 v27, v27, v40\n\t"
    "v_mul_f32 v28, v28, v40\n\t"
    "v_mul_f32 v29, v29, v40\n\t"
    "v_mul_f32 v30, v30, v40\n\t"
    "v_mul_f32 v31, v31, v40\n\t"
    "v_mul_f32 v52, v52, v40\n\t"
    "v_mul_f32 v53, v53, v40\n\t"
    "v_mul_f32 v54, v54, v40\n\t"
    "v_mul_f32 v55, v55, v40\n\t"
    "s_branch 11b\n\t"
    "12:\n\t"
    "v_max3_f32 v14, v24, v25, v26\n\t"
    "v_max3_f32 v40, v27, v28, v29\n\t"
    "v_max3_f32 v14, v14, v30, v31\n\t"
    "v_max3_f32 v14, v14, v40, %[neg]\n\t"
    "v_mov_b32 v40, v14\n\t"
    "s_nop 1\n\t"
    "v_permlane16_swap_b32 v14, v40\n\t"
    "v_max_f32 v14, v14, v40\n\t"
    "v_mov_b32 v40, v14\n\t"
    "s_nop 1\n\t"
    "v_permlane32_swap_b32 v14, v40\n\t"
    "v_max_f32 v14, v14, v40\n\t"
    "v_sub_f32 v24, v24, v14\n\t"
    "v_sub_f32 v25, v25, v14\n\t"
    "v_sub_f32 v26, v26, v14\n\t"
    "v_sub_f32 v27, v27, v14\n\t"
    "v_sub_f32 v28, v28, v14\n\t"
    "v_sub_f32 v29, v29, v14\n\t"
    "v_sub_f32 v30, v30, v14\n\t"
    "v_sub_f32 v31, v31, v14\n\t"
    "s_mov_b32 %[bg], 0x7fc00000\n\t"
    "s_branch 13b\n\t"
    "9:"
    : [tc] "=&s"(tc), [sp] "=&s"(sp), [cap] "+s"(cap), [st] "=&s"(st), [bg] "=&s"(bg)
    : [ag] "s"(ag), [c44] "s"(0x4444u), [lsc] "s"(LSC), [sn] "s"(sn), [pb] "s"(pb), [vs] "s"(vs), [od] "s"(od), [neg] "s"(-1.0e38f), [one] "s"(0x3F803F80u), [big] "s"(24.0f), "{v[64:95]}"(W.w0), "{v[96:127]}"(W.w1), "{v[128:159]}"(W.w2), "{v[160:191]}"(W.w3), "{v[192:223]}"(W.w4), "{v[224:255]}"(W.w5)
    : "v4", "v5", "v6", "v7", "v8", "v9", "v10", "v11", "v12", "v13", "v14", "v15", "v16", "v17", "v18", "v19", "v20", "v21", "v22", "v23", "v24", "v25", "v26", "v27", "v28", "v29", "v30", "v31", "v32", "v33", "v34", "v35", "v36", "v37", "v38", "v39", "v40", "v41", "v42", "v43", "v44", "v45", "v46", "v47", "v48", "v49", "v50", "v51", "v52", "v53", "v54", "v55", "v56", "v57", "v58", "v59", "v60", "v61", "v62", "v63", "vcc", "scc", "memory");
}
template <> __device__ __forceinline__ void qk_step<1>(const QKW& W, const char* ag, const char* vs, const char* v2, const char* od, unsigned pb, unsigned sn, unsigned& cap, unsigned& sp,
    unsigned long long& tc, unsigned long long* ts, unsigned& d0, unsigned& d1) {
  unsigned st, bg;
  asm volatile("v_mbcnt_lo_u32_b32 v60, -1, 0\n\t"
    "v_mbcnt_hi_u32_b32 v60, -1, v60\n\t"
    "v_lshlrev_b32 v5, 4, v60\n\t"
    "s_mov_b32 %[sp], 0\n\t"
    "s_setprio 2\n\t"
    "global_load_dwordx4 v[24:27], v5, %[ag] sc1\n\t"
    "global_load_dwordx4 v[28:31], v5, %[ag] offset:1024 sc1\n\t"
    "s_memtime %[tc]\n\t"
    "v_lshrrev_b32 v32, 4, v60\n\t"
    "v_and_b32 v33, 15, v60\n\t"
    "v_lshrrev_b32 v34, 2, v33\n\t"
    "v_and_b32 v35, 3, v60\n\t"
    "v_and_b32 v61, 1, v60\n\t"
    "v_lshlrev_b32 v61, 3, v61\n\t"
    "v_lshl_or_b32 v7, v34, 4, v61\n\t"
    "v_lshl_or_b32 v7, v32, 7, v7\n\t"
    "v_xor_b32 v10, v32, v33\n\t"
    "v_lshlrev_b32 v10, 4, v10\n\t"
    "v_lshl_or_b32 v10, v33, 8, v10\n\t"
    "v_or_b32 v10, %[pb], v10\n\t"
    "v_xor_b32 v11, 0x40, v10\n\t"
    "v_xor_b32 v12, 0x80, v10\n\t"
    "v_xor_b32 v13, 0xc0, v10\n\t"
    "v_lshl_or_b32 v34, v32, 2, v34\n\t"
    "v_lshrrev_b32 v35, 1, v35\n\t"
    "v_or_b32 v35, 4, v35\n\t"
    "v_xor_b32 v35, v35, v34\n\t"
    "v_lshl_or_b32 v14, v35, 4, v61\n\t"
    "v_lshl_or_b32 v14, v34, 8, v14\n\t"
    "v_or_b32 v14, %[pb], v14\n\t"
    "v_and_b32 v61, 3, v60\n\t"
    "v_lshlrev_b32 v61, 6, v61\n\t"
    "v_lshl_or_b32 v4, v32, 4, v61\n\t"
    "v_and_b32 v61, 4, v60\n\t"
    "v_lshl_or_b32 v4, v61, 10, v4\n\t"
    "v_and_b32 v61, 8, v60\n\t"
    "v_lshl_or_b32 v4, v61, 14, v4\n\t"
    "v_or_b32 v4, 256, v4\n\t"
    "v_and_b32 v61, 2, v60\n\t"
    "v_mul_u32_u24 v61, 0x5555, v61\n\t"
    "v_or_b32 v6, %[c44], v61\n\t"
    "v_lshlrev_b32 v61, 2, v32\n\t"
    "v_lshl_or_b32 v15, v33, 4, v61\n\t"
    "3:\n\t"
    "v_mov_b32 v16, 0\n\t"
    "v_mov_b32 v17, 0\n\t"
    "v_mov_b32 v18, 0\n\t"
    "v_mov_b32 v19, 0\n\t"
    "v_mov_b32 v20, 0\n\t"
    "v_mov_b32 v21, 0\n\t"
    "v_mov_b32 v22, 0\n\t"
    "v_mov_b32 v23, 0\n\t"
    "s_waitcnt vmcnt(0) lgkmcnt(0)\n\t"
    "ds_write_b128 v5, v[24:27] offset:2048\n\t"
    "ds_write_b128 v5, v[28:31] offset:3072\n\t"
    "v_smfmac_f32_16x16x64_bf16 v[16:19], v[24:27], v[96:103], v6\n\t"
    "v_max3_u32 v60, v24, v25, v26\n\t"
    "v_max3_u32 v61, v27, v28, v29\n\t"
    "v_smfmac_f32_16x16x64_bf16 v[20:23], v[24:27], v[104:111], v6\n\t"
    "v_max3_u32 v60, v60, v30, v31\n\t"
    "v_max_u32 v60, v60, v61\n\t"
    "v_cmp_eq_u32 vcc, -1, v60\n\t"
    "s_cbranch_vccnz 1f\n\t"
    "2:\n\t"
    "s_waitcnt lgkmcnt(0)\n\t"
    "s_barrier\n\t"
    "ds_read_b128 v[32:35], v5 offset:0\n\t"
    "ds_read_b128 v[36:39], v5 offset:1024\n\t"
    "v_smfmac_f32_16x16x64_bf16 v[16:19], v[28:31], v[112:119], v6\n\t"
    "ds_read_b128 v[40:43], v5 offset:4096\n\t"
    "ds_read_b128 v[44:47], v5 offset:5120\n\t"
    "v_smfmac_f32_16x16x64_bf16 v[20:23], v[28:31], v[120:127], v6\n\t"
    "s_waitcnt lgkmcnt(3)\n\t"
    "v_smfmac_f32_16x16x64_bf16 v[16:19], v[32:35], v[64:71], v6\n\t"
    "ds_read_b128 v[48:51], v5 offset:6144\n\t"
    "v_smfmac_f32_16x16x64_bf16 v[20:23], v[32:35], v[72:79], v6\n\t"
    "ds_read_b128 v[52:55], v5 offset:7168\n\t"
    "s_waitcnt lgkmcnt(4)\n\t"
    "v_smfmac_f32_16x16x64_bf16 v[16:19], v[36:39], v[80:87], v6\n\t"
    "ds_read_b128 v[24:27], v5 offset:8192\n\t"
    "v_smfmac_f32_16x16x64_bf16 v[20:23], v[36:39], v[88:95], v6\n\t"
    "ds_read_b128 v[28:31], v5 offset:9216\n\t"
    "s_waitcnt lgkmcnt(5)\n\t"
    "v_smfmac_f32_16x16x64_bf16 v[16:19], v[40:43], v[128:135], v6\n\t"
    "ds_read_b128 v[56:59], v5 offset:10240\n\t"
    "v_smfmac_f32_16x16x64_bf16 v[20:23], v[40:43], v[136:143], v6\n\t"
    "ds_read_b128 v[60:63], v5 offset:11264\n\t"
    "ds_read_b128 v[36:39], v11 offset:36864\n\t"
    "s_waitcnt lgkmcnt(7)\n\t"
    "v_smfmac_f32_16x16x64_bf16 v[16:19], v[44:47], v[144:151], v6\n\t"
    "ds_read_b128 v[40:43], v12 offset:32768\n\t"
    "v_smfmac_f32_16x16x64_bf16 v[20:23], v[44:47], v[152:159], v6\n\t"
    "ds_read_b128 v[44:47], v12 offset:36864\n\t"
    "s_waitcnt lgkmcnt(8)\n\t"
    "v_smfmac_f32_16x16x64_bf16 v[16:19], v[48:51], v[160:167], v6\n\t"
    "v_smfmac_f32_16x16x64_bf16 v[20:23], v[48:51], v[168:175], v6\n\t"
    "ds_read_b128 v[48:51], v13 offset:32768\n\t"
    "s_waitcnt lgkmcnt(8)\n\t"
    "v_smfmac_f32_16x16x64_bf16 v[16:19], v[52:55], v[176:183], v6\n\t"
    "v_smfmac_f32_16x16x64_bf16 v[20:23], v[52:55], v[184:191], v6\n\t"
    "ds_read_b128 v[52:55], v13 offset:36864\n\t"
    "s_waitcnt lgkmcnt(8)\n\t"
    "v_smfmac_f32_16x16x64_bf16 v[16:19], v[24:27], v[192:199], v6\n\t"
    "v_smfmac_f32_16x16x64_bf16 v[20:23], v[24:27], v[200:207], v6\n\t"
    "ds_read_b128 v[24:27], v10 offset:32768\n\t"
    "s_waitcnt lgkmcnt(8)\n\t"
    "v_smfmac_f32_16x16x64_bf16 v[16:19], v[28:31], v[208:215], v6\n\t"
    "ds_read_b32 v8, v15 offset:28928\n\t"
    "ds_read_b128 v[32:35], v5 offset:25600\n\t"
    "s_setprio 3\n\t"
    "s_waitcnt lgkmcnt(9)\n\t"
    "v_smfmac_f32_16x16x64_bf16 v[16:19], v[56:59], v[224:231], v6\n\t"
    "s_waitcnt lgkmcnt(8)\n\t"
    "v_smfmac_f32_16x16x64_bf16 v[16:19], v[60:63], v[240:247], v6\n\t"
    "v_smfmac_f32_16x16x64_bf16 v[20:23], v[28:31], v[216:223], v6\n\t"
    "s_waitcnt lgkmcnt(0)\n\t"
    "21:\n\t"
    "v_cmp_ne_u32 vcc, %[sn], v8\n\t"
    "s_cbranch_vccnz 22f\n\t"
    "v_mul_f32 v32, %[lsc], v32\n\t"
    "v_mul_f32 v33, %[lsc], v33\n\t"
    "v_smfmac_f32_16x16x64_bf16 v[20:23], v[56:59], v[232:239], v6\n\t"
    "v_mul_f32 v34, %[lsc], v34\n\t"
    "v_mul_f32 v35, %[lsc], v35\n\t"
    "v_add_f32 v16, v16, v18\n\t"
    "v_add_f32 v17, v17, v19\n\t"
    "v_smfmac_f32_16x16x64_bf16 v[20:23], v[60:63], v[248:255], v6\n\t"
    "v_fma_f32 v16, v16, %[lsc], v32\n\t"
    "v_fma_f32 v17, v17, %[lsc], v33\n\t"
    "v_cvt_pk_bf16_f32 v16, v16, v17\n\t"
    "ds_write_b32 v15, v16 offset:16896\n\t"
    "s_mov_b32 %[bg], %[big]\n\t"
    "s_nop 2\n\t"
    "v_add_f32 v20, v20, v22\n\t"
    "v_add_f32 v21, v21, v23\n\t"
    "v_fma_f32 v20, v20, %[lsc], v34\n\t"
    "v_fma_f32 v21, v21, %[lsc], v35\n\t"
    "v_cvt_pk_bf16_f32 v20, v20, v21\n\t"
    "ds_write_b32 v15, v20 offset:17152\n\t"
    "ds_read_b128 v[28:31], v10 offset:36864\n\t"
    "ds_read_b128 v[32:35], v11 offset:32768\n\t"
    "s_waitcnt lgkmcnt(2)\n\t"
    "s_barrier\n\t"
    "ds_read_b64_tr_b16 v[16:17], v7 offset:16384\n\t"
    "ds_read_b64_tr_b16 v[18:19], v7 offset:16448\n\t"
    "ds_read_b64_tr_b16 v[20:21], v7 offset:16896\n\t"
    "ds_read_b64_tr_b16 v[22:23], v7 offset:16960\n\t"
    "ds_read_b64_tr_b16 v[56:57], v7 offset:17408\n\t"
    "ds_read_b64_tr_b16 v[58:59], v7 offset:17472\n\t"
    "ds_read_b64_tr_b16 v[60:61], v7 offset:17920\n\t"
    "ds_read_b64_tr_b16 v[62:63], v7 offset:17984\n\t"
    "global_load_dwordx4 v[6:9], v5, %[vs] sc1\n\t"
    "global_load_dwordx4 v[10:13], v5, %[vs] offset:1024 sc1\n\t"
    "s_waitcnt lgkmcnt(6)\n\t"
    "v_mfma_f32_16x16x32_bf16 v[24:27], v[24:27], v[16:19], 0\n\t"
    "v_mfma_f32_16x16x32_bf16 v[28:31], v[28:31], v[16:19], 0\n\t"
    "s_waitcnt lgkmcnt(4)\n\t"
    "v_mfma_f32_16x16x32_bf16 v[24:27], v[32:35], v[20:23], v[24:27]\n\t"
    "v_mfma_f32_16x16x32_bf16 v[28:31], v[36:39], v[20:23], v[28:31]\n\t"
    "v_xor_b32 v39, 32, v14\n\t"
    "ds_read_b64_tr_b16 v[32:33], v14 offset:40960\n\t"
    "ds_read_b64_tr_b16 v[34:35], v14 offset:45056\n\t"
    "ds_read_b64_tr_b16 v[36:37], v39 offset:40960\n\t"
    "ds_read_b64_tr_b16 v[38:39], v39 offset:45056\n\t"
    "s_waitcnt lgkmcnt(6)\n\t"
    "v_mfma_f32_16x16x32_bf16 v[24:27], v[40:43], v[56:59], v[24:27]\n\t"
    "v_mfma_f32_16x16x32_bf16 v[28:31], v[44:47], v[56:59], v[28:31]\n\t"
    "s_waitcnt lgkmcnt(4)\n\t"
    "v_mfma_f32_16x16x32_bf16 v[24:27], v[48:51], v[60:63], v[24:27]\n\t"
    "v_mfma_f32_16x16x32_bf16 v[28:31], v[52:55], v[60:63], v[28:31]\n\t"
    "v_mov_b32 v52, %[one]\n\t"
    "v_mov_b32 v53, %[one]\n\t"
    "v_mov_b32 v54, %[one]\n\t"
    "v_mov_b32 v55, %[one]\n\t"
    "v_mov_b32 v45, 0\n\t"
    "v_mov_b32 v47, 0\n\t"
    "v_mov_b32 v48, %[one]\n\t"
    "v_max3_f32 v14, |v24|, |v25|, |v26|\n\t"
    "v_max3_f32 v40, |v27|, |v28|, |v29|\n\t"
    "v_max3_f32 v14, v14, |v30|, |v31|\n\t"
    "v_max_f32 v14, v14, v40\n\t"
    "v_cmp_nge_f32 vcc, %[big], v14\n\t"
    "v_mov_b32 v14, 0\n\t"
    "s_cbranch_vccnz 12f\n\t"
    "13:\n\t"
    "v_exp_f32 v24, v24\n\t"
    "v_exp_f32 v25, v25\n\t"
    "v_exp_f32 v26, v26\n\t"
    "v_exp_f32 v27, v27\n\t"
    "v_exp_f32 v28, v28\n\t"
    "v_exp_f32 v29, v29\n\t"
    "v_exp_f32 v30, v30\n\t"
    "v_exp_f32 v31, v31\n\t"
    "v_cvt_pk_bf16_f32 v40, v24, v25\n\t"
    "v_cvt_pk_bf16_f32 v41, v26, v27\n\t"
    "v_cvt_pk_bf16_f32 v42, v28, v29\n\t"
    "v_cvt_pk_bf16_f32 v43, v30, v31\n\t"
    "v_mov_b32 v49, 0\n\t"
    "v_mov_b32 v51, 0\n\t"
    "s_waitcnt lgkmcnt(0)\n\t"
    "v_mfma_f32_16x16x32_bf16 v[52:55], v[52:55], v[40:43], 0\n\t"
    "v_mfma_f32_16x16x32_bf16 v[24:27], v[32:35], v[40:43], 0\n\t"
    "v_mfma_f32_16x16x32_bf16 v[28:31], v[36:39], v[40:43], 0\n\t"
    "s_waitcnt vmcnt(1)\n\t"
    "5:\n\t"
    "v_mfma_f32_16x16x32_bf16 v[32:35], v[6:9], v[16:19], 0\n\t"
    "v_mfma_f32_16x16x32_bf16 v[36:39], v[6:9], v[20:23], 0\n\t"
    "s_waitcnt vmcnt(0)\n\t"
    "v_mfma_f32_16x16x32_bf16 v[32:35], v[10:13], v[56:59], v[32:35]\n\t"
    "v_mfma_f32_16x16x32_bf16 v[36:39], v[10:13], v[60:63], v[36:39]\n\t"
    "ds_read_b32 v44, v15 offset:18944\n\t"
    "ds_read_b32 v46, v15 offset:19200\n\t"
    "v_max3_u32 v40, v6, v7, v8\n\t"
    "v_max3_u32 v41, v9, v10, v11\n\t"
    "v_max3_u32 v40, v40, v12, v13\n\t"
    "v_max_u32 v40, v40, v41\n\t"
    "v_cmp_eq_u32 vcc, -1, v40\n\t"
    "s_cbranch_vccnz 4f\n\t"
    "6:\n\t"
    "s_nop 0\n\t"
    "v_add_f32 v32, v32, v38\n\t"
    "v_add_f32 v33, v33, v39\n\t"
    "v_exp_f32 v34, v32\n\t"
    "v_max_f32 v40, v32, v33\n\t"
    "v_exp_f32 v35, v33\n\t"
    "v_cmp_nge_f32 vcc, %[bg], v40\n\t"
    "s_cbranch_vccnz 10f\n\t"
    "11:\n\t"
    "v_cvt_pk_bf16_f32 v50, v34, v35\n\t"
    "17:\n\t"
    "s_waitcnt vmcnt(0) lgkmcnt(0)\n\t"
    "v_max_u32 v40, v44, v46\n\t"
    "v_cmp_eq_u32 vcc, -1, v40\n\t"
    "s_cbranch_vccnz 18f\n\t"
    "19:\n\t"
    "v_mfma_f32_16x16x16_bf16 v[52:55], v[48:49], v[50:51], v[52:55]\n\t"
    "v_mfma_f32_16x16x16_bf16 v[24:27], v[44:45], v[50:51], v[24:27]\n\t"
    "v_mfma_f32_16x16x16_bf16 v[28:31], v[46:47], v[50:51], v[28:31]\n\t"
    "s_nop 5\n\t"
    "v_rcp_f32 v42, v52\n\t"
    "s_nop 0\n\t"
    "v_pk_mul_f32 v[24:25], v[24:25], v[42:43] op_sel_hi:[1,0]\n\t"
    "v_pk_mul_f32 v[26:27], v[26:27], v[42:43] op_sel_hi:[1,0]\n\t"
    "v_pk_mul_f32 v[28:29], v[28:29], v[42:43] op_sel_hi:[1,0]\n\t"
    "v_pk_mul_f32 v[30:31], v[30:31], v[42:43] op_sel_hi:[1,0]\n\t"
    "v_cvt_pk_bf16_f32 v36, v24, v25\n\t"
    "v_cvt_pk_bf16_f32 v37, v26, v27\n\t"
    "v_cvt_pk_bf16_f32 v38, v28, v29\n\t"
    "v_cvt_pk_bf16_f32 v39, v30, v31\n\t"
    "v_min_u32 v36, -2, v36\n\t"
    "v_min_u32 v37, -2, v37\n\t"
    "v_min_u32 v38, -2, v38\n\t"
    "v_min_u32 v39, -2, v39\n\t"
    "global_store_dwordx4 v4, v[36:39], %[od] sc1\n\t"
    "ds_write_b128 v5, v[10:13] offset:21504\n\t"
    "s_setprio 1\n\t"
    "s_waitcnt lgkmcnt(0)\n\t"
    "s_barrier\n\t"
    "s_waitcnt lgkmcnt(0)\n\t"
    "s_branch 9f\n\t"
    "1:\n\t"
    "s_add_u32 %[sp], %[sp], 1\n\t"
    "s_cmp_ge_u32 %[sp], %[cap]\n\t"
    "s_cbranch_scc1 7f\n\t"
    "global_load_dwordx4 v[24:27], v5, %[ag] sc1\n\t"
    "global_load_dwordx4 v[28:31], v5, %[ag] offset:1024 sc1\n\t"
    "s_waitcnt lgkmcnt(0)\n\t"
    "s_memtime %[tc]\n\t"
    "s_nop 7\n\t"
    "s_branch 3b\n\t"
    "7:\n\t"
    "s_mov_b32 %[cap], 0\n\t"
    "s_branch 2b\n\t"
    "4:\n\t"
    "s_add_u32 %[sp], %[sp], 0x10000\n\t"
    "s_lshr_b32 %[st], %[sp], 16\n\t"
    "s_cmp_ge_u32 %[st], %[cap]\n\t"
    "s_cbranch_scc1 8f\n\t"
    "global_load_dwordx4 v[6:9], v5, %[vs] sc1\n\t"
    "global_load_dwordx4 v[10:13], v5, %[vs] offset:1024 sc1\n\t"
    "s_waitcnt vmcnt(0) lgkmcnt(0)\n\t"
    "s_branch 5b\n\t"
    "8:\n\t"
    "s_mov_b32 %[cap], 0\n\t"
    "s_branch 6b\n\t"
    "18:\n\t"
    "s_add_u32 %[sp], %[sp], 0x10000\n\t"
    "s_lshr_b32 %[st], %[sp], 16\n\t"
    "s_cmp_ge_u32 %[st], %[cap]\n\t"
    "s_cbranch_scc1 20f\n\t"
    "ds_read_b32 v44, v15 offset:18944\n\t"
    "ds_read_b32 v46, v15 offset:19200\n\t"
    "s_waitcnt vmcnt(0) lgkmcnt(0)\n\t"
    "s_branch 17b\n\t"
    "20:\n\t"
    "s_mov_b32 %[cap], 0\n\t"
    "s_branch 19b\n\t"
    "22:\n\t"
    "ds_read_b32 v8, v15 offset:28928\n\t"
    "ds_read_b128 v[32:35], v5 offset:25600\n\t"
    "s_waitcnt lgkmcnt(0)\n\t"
    "s_branch 21b\n\t"
    "10:\n\t"
    "v_max_f32 v40, v40, v14\n\t"
    "v_mov_b32 v42, v40\n\t"
    "s_nop 1\n\t"
    "v_permlane16_swap_b32 v40, v42\n\t"
    "v_max_f32 v40, v40, v42\n\t"
    "v_mov_b32 v42, v40\n\t"
    "s_nop 1\n\t"
    "v_permlane32_swap_b32 v40, v42\n\t"
    "v_max_f32 v40, v40, v42\n\t"
    "v_sub_f32 v34, v32, v40\n\t"
    "v_sub_f32 v35, v33, v40\n\t"
    "v_sub_f32 v40, v14, v40\n\t"
    "v_exp_f32 v40, v40\n\t"
    "v_exp_f32 v34, v34\n\t"
    "v_exp_f32 v35, v35\n\t"
    "v_mul_f32 v24, v24, v40\n\t"
    "v_mul_f32 v25, v25, v40\n\t"
    "v_mul_f32 v26, v26, v40\n\t"
    "v_mul_f32 v27, v27, v40\n\t"
    "v_mul_f32 v28, v28, v40\n\t"
    "v_mul_f32 v29, v29, v40\n\t"
    "v_mul_f32 v30, v30, v40\n\t"
    "v_mul_f32 v31, v31, v40\n\t"
    "v_mul_f32 v52, v52, v40\n\t"
    "v_mul_f32 v53, v53, v40\n\t"
    "v_mul_f32 v54, v54, v40\n\t"
    "v_mul_f32 v55, v55, v40\n\t"
    "s_branch 11b\n\t"
    "12:\n\t"
    "v_max3_f32 v14, v24, v25, v26\n\t"
    "v_max3_f32 v40, v27, v28, v29\n\t"
    "v_max3_f32 v14, v14, v30, v31\n\t"
    "v_max3_f32 v14, v14, v40, %[neg]\n\t"
    "v_mov_b32 v40, v14\n\t"
    "s_nop 1\n\t"
    "v_permlane16_swap_b32 v14, v40\n\t"
    "v_max_f32 v14, v14, v40\n\t"
    "v_mov_b32 v40, v14\n\t"
    "s_nop 1\n\t"
    "v_permlane32_swap_b32 v14, v40\n\t"
    "v_max_f32 v14, v14, v40\n\t"
    "v_sub_f32 v24, v24, v14\n\t"
    "v_sub_f32 v25, v25, v14\n\t"
    "v_sub_f32 v26, v26, v14\n\t"
    "v_sub_f32 v27, v27, v14\n\t"
    "v_sub_f32 v28, v28, v14\n\t"
    "v_sub_f32 v29, v29, v14\n\t"
    "v_sub_f32 v30, v30, v14\n\t"
    "v_sub_f32 v31, v31, v14\n\t"
    "s_mov_b32 %[bg], 0x7fc00000\n\t"
    "s_branch 13b\n\t"
    "9:"
    : [tc] "=&s"(tc), [sp] "=&s"(sp), [cap] "+s"(cap), [st] "=&s"(st), [bg] "=&s"(bg)
    : [ag] "s"(ag), [c44] "s"(0x4444u), [lsc] "s"(LSC), [sn] "s"(sn), [pb] "s"(pb), [vs] "s"(vs), [od] "s"(od), [neg] "s"(-1.0e38f), [one] "s"(0x3F803F80u), [big] "s"(24.0f), "{v[64:95]}"(W.w0), "{v[96:127]}"(W.w1), "{v[128:159]}"(W.w2), "{v[160:191]}"(W.w3), "{v[192:223]}"(W.w4), "{v[224:255]}"(W.w5)
    : "v4", "v5", "v6", "v7", "v8", "v9", "v10", "v11", "v12", "v13", "v14", "v15", "v16", "v17", "v18", "v19", "v20", "v21", "v22", "v23", "v24", "v25", "v26", "v27", "v28", "v29", "v30", "v31", "v32", "v33", "v34", "v35", "v36", "v37", "v38", "v39", "v40", "v41", "v42", "v43", "v44", "v45", "v46", "v47", "v48", "v49", "v50", "v51", "v52", "v53", "v54", "v55", "v56", "v57", "v58", "v59", "v60", "v61", "v62", "v63", "vcc", "scc", "memory");
}
template <> __device__ __forceinline__ void qk_step<2>(const QKW& W, const char* ag, const char* vs, const char* v2, const char* od, unsigned pb, unsigned sn, unsigned& cap, unsigned& sp,
    unsigned long long& tc, unsigned long long* ts, unsigned& d0, unsigned& d1) {
  unsigned st, bg;
  asm volatile("v_mbcnt_lo_u32_b32 v60, -1, 0\n\t"
    "v_mbcnt_hi_u32_b32 v60, -1, v60\n\t"
    "v_lshlrev_b32 v5, 4, v60\n\t"
    "s_mov_b32 %[sp], 0\n\t"
    "s_setprio 2\n\t"
    "global_load_dwordx4 v[24:27], v5, %[ag] sc1\n\t"
    "global_load_dwordx4 v[28:31], v5, %[ag] offset:1024 sc1\n\t"
    "s_memtime %[tc]\n\t"
    "v_lshrrev_b32 v32, 4, v60\n\t"
    "v_and_b32 v33, 15, v60\n\t"
    "v_lshrrev_b32 v34, 2, v33\n\t"
    "v_and_b32 v35, 3, v60\n\t"
    "v_and_b32 v61, 1, v60\n\t"
    "v_lshlrev_b32 v61, 3, v61\n\t"
    "v_lshl_or_b32 v7, v34, 4, v61\n\t"
    "v_lshl_or_b32 v7, v32, 7, v7\n\t"
    "v_xor_b32 v10, v32, v33\n\t"
    "v_lshlrev_b32 v10, 4, v10\n\t"
    "v_lshl_or_b32 v10, v33, 8, v10\n\t"
    "v_or_b32 v10, %[pb], v10\n\t"
    "v_xor_b32 v11, 0x40, v10\n\t"
    "v_xor_b32 v12, 0x80, v10\n\t"
    "v_xor_b32 v13, 0xc0, v10\n\t"
    "v_lshl_or_b32 v34, v32, 2, v34\n\t"
    "v_lshrrev_b32 v35, 1, v35\n\t"
    "v_or_b32 v35, 8, v35\n\t"
    "v_xor_b32 v35, v35, v34\n\t"
    "v_lshl_or_b32 v14, v35, 4, v61\n\t"
    "v_lshl_or_b32 v14, v34, 8, v14\n\t"
    "v_or_b32 v14, %[pb], v14\n\t"
    "v_and_b32 v61, 3, v60\n\t"
    "v_lshlrev_b32 v61, 6, v61\n\t"
    "v_lshl_or_b32 v4, v32, 4, v61\n\t"
    "v_and_b32 v61, 4, v60\n\t"
    "v_lshl_or_b32 v4, v61, 10, v4\n\t"
    "v_and_b32 v61, 8, v60\n\t"
    "v_lshl_or_b32 v4, v61, 14, v4\n\t"
    "v_or_b32 v4, 512, v4\n\t"
    "v_and_b32 v61, 2, v60\n\t"
    "v_mul_u32_u24 v61, 0x5555, v61\n\t"
    "v_or_b32 v6, %[c44], v61\n\t"
    "v_lshlrev_b32 v61, 2, v32\n\t"
    "v_lshl_or_b32 v15, v33, 4, v61\n\t"
    "3:\n\t"
    "v_mov_b32 v16, 0\n\t"
    "v_mov_b32 v17, 0\n\t"
    "v_mov_b32 v18, 0\n\t"
    "v_mov_b32 v19, 0\n\t"
    "v_mov_b32 v20, 0\n\t"
    "v_mov_b32 v21, 0\n\t"
    "v_mov_b32 v22, 0\n\t"
    "v_mov_b32 v23, 0\n\t"
    "s_waitcnt vmcnt(0) lgkmcnt(0)\n\t"
    "ds_write_b128 v5, v[24:27] offset:4096\n\t"
    "ds_write_b128 v5, v[28:31] offset:5120\n\t"
    "v_smfmac_f32_16x16x64_bf16 v[16:19], v[24:27], v[128:135], v6\n\t"
    "v_max3_u32 v60, v24, v25, v26\n\t"
    "v_max3_u32 v61, v27, v28, v29\n\t"
    "v_smfmac_f32_16x16x64_bf16 v[20:23], v[24:27], v[136:143], v6\n\t"
    "v_max3_u32 v60, v60, v30, v31\n\t"
    "v_max_u32 v60, v60, v61\n\t"
    "v_cmp_eq_u32 vcc, -1, v60\n\t"
    "s_cbranch_vccnz 1f\n\t"
    "2:\n\t"
    "s_waitcnt lgkmcnt(0)\n\t"
    "s_barrier\n\t"
    "ds_read_b128 v[32:35], v5 offset:0\n\t"
    "ds_read_b128 v[36:39], v5 offset:1024\n\t"
    "v_smfmac_f32_16x16x64_bf16 v[16:19], v[28:31], v[144:151], v6\n\t"
    "ds_read_b128 v[40:43], v5 offset:2048\n\t"
    "ds_read_b128 v[44:47], v5 offset:3072\n\t"
    "v_smfmac_f32_16x16x64_bf16 v[20:23], v[28:31], v[152:159], v6\n\t"
    "s_waitcnt lgkmcnt(3)\n\t"
    "v_smfmac_f32_16x16x64_bf16 v[16:19], v[32:35], v[64:71], v6\n\t"
    "ds_read_b128 v[48:51], v5 offset:6144\n\t"
    "v_smfmac_f32_16x16x64_bf16 v[20:23], v[32:35], v[72:79], v6\n\t"
    "ds_read_b128 v[52:55], v5 offset:7168\n\t"
    "s_waitcnt lgkmcnt(4)\n\t"
    "v_smfmac_f32_16x16x64_bf16 v[16:19], v[36:39], v[80:87], v6\n\t"
    "ds_read_b128 v[24:27], v5 offset:8192\n\t"
    "v_smfmac_f32_16x16x64_bf16 v[20:23], v[36:39], v[88:95], v6\n\t"
    "ds_read_b128 v[28:31], v5 offset:9216\n\t"
    "s_waitcnt lgkmcnt(5)\n\t"
    "v_smfmac_f32_16x16x64_bf16 v[16:19], v[40:43], v[96:103], v6\n\t"
    "ds_read_b128 v[56:59], v5 offset:10240\n\t"
    "v_smfmac_f32_16x16x64_bf16 v[20:23], v[40:43], v[104:111], v6\n\t"
    "ds_read_b128 v[60:63], v5 offset:11264\n\t"
    "ds_read_b128 v[36:39], v11 offset:36864\n\t"
    "s_waitcnt lgkmcnt(7)\n\t"
    "v_smfmac_f32_16x16x64_bf16 v[16:19], v[44:47], v[112:119], v6\n\t"
    "ds_read_b128 v[40:43], v12 offset:32768\n\t"
    "v_smfmac_f32_16x16x64_bf16 v[20:23], v[44:47], v[120:127], v6\n\t"
    "ds_read_b128 v[44:47], v12 offset:36864\n\t"
    "s_waitcnt lgkmcnt(8)\n\t"
    "v_smfmac_f32_16x16x64_bf16 v[16:19], v[48:51], v[160:167], v6\n\t"
    "v_smfmac_f32_16x16x64_bf16 v[20:23], v[48:51], v[168:175], v6\n\t"
    "ds_read_b128 v[48:51], v13 offset:32768\n\t"
    "s_waitcnt lgkmcnt(8)\n\t"
    "v_smfmac_f32_16x16x64_bf16 v[16:19], v[52:55], v[176:183], v6\n\t"
    "v_smfmac_f32_16x16x64_bf16 v[20:23], v[52:55], v[184:191], v6\n\t"
    "ds_read_b128 v[52:55], v13 offset:36864\n\t"
    "s_waitcnt lgkmcnt(8)\n\t"
    "v_smfmac_f32_16x16x64_bf16 v[16:19], v[24:27], v[192:199], v6\n\t"
    "v_smfmac_f32_16x16x64_bf16 v[20:23], v[24:27], v[200:207], v6\n\t"
    "ds_read_b128 v[24:27], v10 offset:32768\n\t"
    "s_waitcnt lgkmcnt(8)\n\t"
    "v_smfmac_f32_16x16x64_bf16 v[16:19], v[28:31], v[208:215], v6\n\t"
    "ds_read_b32 v8, v15 offset:29184\n\t"
    "ds_read_b128 v[32:35], v5 offset:26624\n\t"
    "s_setprio 3\n\t"
    "s_waitcnt lgkmcnt(9)\n\t"
    "v_smfmac_f32_16x16x64_bf16 v[16:19], v[56:59], v[224:231], v6\n\t"
    "s_waitcnt lgkmcnt(8)\n\t"
    "v_smfmac_f32_16x16x64_bf16 v[16:19], v[60:63], v[240:247], v6\n\t"
    "v_smfmac_f32_16x16x64_bf16 v[20:23], v[28:31], v[216:223], v6\n\t"
    "s_waitcnt lgkmcnt(0)\n\t"
    "21:\n\t"
    "v_cmp_ne_u32 vcc, %[sn], v8\n\t"
    "s_cbranch_vccnz 22f\n\t"
    "v_mul_f32 v32, %[lsc], v32\n\t"
    "v_mul_f32 v33, %[lsc], v33\n\t"
    "v_smfmac_f32_16x16x64_bf16 v[20:23], v[56:59], v[232:239], v6\n\t"
    "v_mul_f32 v34, %[lsc], v34\n\t"
    "v_mul_f32 v35, %[lsc], v35\n\t"
    "v_add_f32 v16, v16, v18\n\t"
    "v_add_f32 v17, v17, v19\n\t"
    "v_smfmac_f32_16x16x64_bf16 v[20:23], v[60:63], v[248:255], v6\n\t"
    "v_fma_f32 v16, v16, %[lsc], v32\n\t"
    "v_fma_f32 v17, v17, %[lsc], v33\n\t"
    "v_cvt_pk_bf16_f32 v16, v16, v17\n\t"
    "ds_write_b32 v15, v16 offset:17408\n\t"
    "s_mov_b32 %[bg], %[big]\n\t"
    "s_nop 2\n\t"
    "v_add_f32 v20, v20, v22\n\t"
    "v_add_f32 v21, v21, v23\n\t"
    "v_fma_f32 v20, v20, %[lsc], v34\n\t"
    "v_fma_f32 v21, v21, %[lsc], v35\n\t"
    "v_cvt_pk_bf16_f32 v20, v20, v21\n\t"
    "ds_write_b32 v15, v20 offset:17664\n\t"
    "ds_read_b128 v[28:31], v10 offset:36864\n\t"
    "ds_read_b128 v[32:35], v11 offset:32768\n\t"
    "s_waitcnt lgkmcnt(2)\n\t"
    "s_barrier\n\t"
    "ds_read_b64_tr_b16 v[16:17], v7 offset:16384\n\t"
    "ds_read_b64_tr_b16 v[18:19], v7 offset:16448\n\t"
    "ds_read_b64_tr_b16 v[20:21], v7 offset:16896\n\t"
    "ds_read_b64_tr_b16 v[22:23], v7 offset:16960\n\t"
    "ds_read_b64_tr_b16 v[56:57], v7 offset:17408\n\t"
    "ds_read_b64_tr_b16 v[58:59], v7 offset:17472\n\t"
    "ds_read_b64_tr_b16 v[60:61], v7 offset:17920\n\t"
    "ds_read_b64_tr_b16 v[62:63], v7 offset:17984\n\t"
    "global_load_dwordx4 v[6:9], v5, %[vs] sc1\n\t"
    "global_load_dwordx4 v[10:13], v5, %[vs] offset:1024 sc1\n\t"
    "s_waitcnt lgkmcnt(6)\n\t"
    "v_mfma_f32_16x16x32_bf16 v[24:27], v[24:27], v[16:19], 0\n\t"
    "v_mfma_f32_16x16x32_bf16 v[28:31], v[28:31], v[16:19], 0\n\t"
    "s_waitcnt lgkmcnt(4)\n\t"
    "v_mfma_f32_16x16x32_bf16 v[24:27], v[32:35], v[20:23], v[24:27]\n\t"
    "v_mfma_f32_16x16x32_bf16 v[28:31], v[36:39], v[20:23], v[28:31]\n\t"
    "v_xor_b32 v39, 32, v14\n\t"
    "ds_read_b64_tr_b16 v[32:33], v14 offset:40960\n\t"
    "ds_read_b64_tr_b16 v[34:35], v14 offset:45056\n\t"
    "ds_read_b64_tr_b16 v[36:37], v39 offset:40960\n\t"
    "ds_read_b64_tr_b16 v[38:39], v39 offset:45056\n\t"
    "s_waitcnt lgkmcnt(6)\n\t"
    "v_mfma_f32_16x16x32_bf16 v[24:27], v[40:43], v[56:59], v[24:27]\n\t"
    "v_mfma_f32_16x16x32_bf16 v[28:31], v[44:47], v[56:59], v[28:31]\n\t"
    "s_waitcnt lgkmcnt(4)\n\t"
    "v_mfma_f32_16x16x32_bf16 v[24:27], v[48:51], v[60:63], v[24:27]\n\t"
    "v_mfma_f32_16x16x32_bf16 v[28:31], v[52:55], v[60:63], v[28:31]\n\t"
    "v_mov_b32 v52, %[one]\n\t"
    "v_mov_b32 v53, %[one]\n\t"
    "v_mov_b32 v54, %[one]\n\t"
    "v_mov_b32 v55, %[one]\n\t"
    "v_mov_b32 v45, 0\n\t"
    "v_mov_b32 v47, 0\n\t"
    "v_mov_b32 v48, %[one]\n\t"
    "v_max3_f32 v14, |v24|, |v25|, |v26|\n\t"
    "v_max3_f32 v40, |v27|, |v28|, |v29|\n\t"
    "v_max3_f32 v14, v14, |v30|, |v31|\n\t"
    "v_max_f32 v14, v14, v40\n\t"
    "v_cmp_nge_f32 vcc, %[big], v14\n\t"
    "v_mov_b32 v14, 0\n\t"
    "s_cbranch_vccnz 12f\n\t"
    "13:\n\t"
    "v_exp_f32 v24, v24\n\t"
    "v_exp_f32 v25, v25\n\t"
    "v_exp_f32 v26, v26\n\t"
    "v_exp_f32 v27, v27\n\t"
    "v_exp_f32 v28, v28\n\t"
    "v_exp_f32 v29, v29\n\t"
    "v_exp_f32 v30, v30\n\t"
    "v_exp_f32 v31, v31\n\t"
    "v_lshrrev_b32 v46, 2, v5\n\t"
    "global_load_dword v44, v46, %[v2] sc1\n\t"
    "global_load_dword v46, v46, %[v2] offset:256 sc1\n\t"
    "v_cvt_pk_bf16_f32 v40, v24, v25\n\t"
    "v_cvt_pk_bf16_f32 v41, v26, v27\n\t"
    "v_cvt_pk_bf16_f32 v42, v28, v29\n\t"
    "v_cvt_pk_bf16_f32 v43, v30, v31\n\t"
    "v_mov_b32 v49, 0\n\t"
    "v_mov_b32 v51, 0\n\t"
    "s_waitcnt lgkmcnt(0)\n\t"
    "v_mfma_f32_16x16x32_bf16 v[52:55], v[52:55], v[40:43], 0\n\t"
    "v_mfma_f32_16x16x32_bf16 v[24:27], v[32:35], v[40:43], 0\n\t"
    "v_mfma_f32_16x16x32_bf16 v[28:31], v[36:39], v[40:43], 0\n\t"
    "s_waitcnt vmcnt(3)\n\t"
    "5:\n\t"
    "v_mfma_f32_16x16x32_bf16 v[32:35], v[6:9], v[16:19], 0\n\t"
    "v_mfma_f32_16x16x32_bf16 v[36:39], v[6:9], v[20:23], 0\n\t"
    "s_waitcnt vmcnt(2)\n\t"
    "v_mfma_f32_16x16x32_bf16 v[32:35], v[10:13], v[56:59], v[32:35]\n\t"
    "v_mfma_f32_16x16x32_bf16 v[36:39], v[10:13], v[60:63], v[36:39]\n\t"
    "v_max3_u32 v40, v6, v7, v8\n\t"
    "v_max3_u32 v41, v9, v10, v11\n\t"
    "v_max3_u32 v40, v40, v12, v13\n\t"
    "v_max_u32 v40, v40, v41\n\t"
    "v_cmp_eq_u32 vcc, -1, v40\n\t"
    "s_cbranch_vccnz 4f\n\t"
    "6:\n\t"
    "s_nop 0\n\t"
    "s_nop 0\n\t"
    "v_add_f32 v32, v32, v38\n\t"
    "v_add_f32 v33, v33, v39\n\t"
    "v_exp_f32 v34, v32\n\t"
    "v_max_f32 v40, v32, v33\n\t"
    "v_exp_f32 v35, v33\n\t"
    "v_cmp_nge_f32 vcc, %[bg], v40\n\t"
    "s_cbranch_vccnz 10f\n\t"
    "11:\n\t"
    "v_cvt_pk_bf16_f32 v50, v34, v35\n\t"
    "17:\n\t"
    "s_waitcnt vmcnt(0) lgkmcnt(0)\n\t"
    "v_max_u32 v40, v44, v46\n\t"
    "v_cmp_eq_u32 vcc, -1, v40\n\t"
    "s_cbranch_vccnz 18f\n\t"
    "19:\n\t"
    "v_mfma_f32_16x16x16_bf16 v[52:55], v[48:49], v[50:51], v[52:55]\n\t"
    "v_mfma_f32_16x16x16_bf16 v[24:27], v[44:45], v[50:51], v[24:27]\n\t"
    "v_mfma_f32_16x16x16_bf16 v[28:31], v[46:47], v[50:51], v[28:31]\n\t"
    "s_nop 5\n\t"
    "v_rcp_f32 v42, v52\n\t"
    "s_nop 0\n\t"
    "v_pk_mul_f32 v[24:25], v[24:25], v[42:43] op_sel_hi:[1,0]\n\t"
    "v_pk_mul_f32 v[26:27], v[26:27], v[42:43] op_sel_hi:[1,0]\n\t"
    "v_pk_mul_f32 v[28:29], v[28:29], v[42:43] op_sel_hi:[1,0]\n\t"
    "v_pk_mul_f32 v[30:31], v[30:31], v[42:43] op_sel_hi:[1,0]\n\t"
    "v_cvt_pk_bf16_f32 v36, v24, v25\n\t"
    "v_cvt_pk_bf16_f32 v37, v26, v27\n\t"
    "v_cvt_pk_bf16_f32 v38, v28, v29\n\t"
    "v_cvt_pk_bf16_f32 v39, v30, v31\n\t"
    "v_min_u32 v36, -2, v36\n\t"
    "v_min_u32 v37, -2, v37\n\t"
    "v_min_u32 v38, -2, v38\n\t"
    "v_min_u32 v39, -2, v39\n\t"
    "global_store_dwordx4 v4, v[36:39], %[od] sc1\n\t"
    "ds_write_b32 v15, v44 offset:19456\n\t"
    "ds_write_b32 v15, v46 offset:19712\n\t"
    "s_setprio 1\n\t"
    "s_waitcnt lgkmcnt(0)\n\t"
    "s_barrier\n\t"
    "s_waitcnt lgkmcnt(0)\n\t"
    "s_branch 9f\n\t"
    "1:\n\t"
    "s_add_u32 %[sp], %[sp], 1\n\t"
    "s_cmp_ge_u32 %[sp], %[cap]\n\t"
    "s_cbranch_scc1 7f\n\t"
    "global_load_dwordx4 v[24:27], v5, %[ag] sc1\n\t"
    "global_load_dwordx4 v[28:31], v5, %[ag] offset:1024 sc1\n\t"
    "s_waitcnt lgkmcnt(0)\n\t"
    "s_memtime %[tc]\n\t"
    "s_nop 7\n\t"
    "s_branch 3b\n\t"
    "7:\n\t"
    "s_mov_b32 %[cap], 0\n\t"
    "s_branch 2b\n\t"
    "4:\n\t"
    "s_add_u32 %[sp], %[sp], 0x10000\n\t"
    "s_lshr_b32 %[st], %[sp], 16\n\t"
    "s_cmp_ge_u32 %[st], %[cap]\n\t"
    "s_cbranch_scc1 8f\n\t"
    "global_load_dwordx4 v[6:9], v5, %[vs] sc1\n\t"
    "global_load_dwordx4 v[10:13], v5, %[vs] offset:1024 sc1\n\t"
    "s_waitcnt vmcnt(0) lgkmcnt(0)\n\t"
    "s_branch 5b\n\t"
    "8:\n\t"
    "s_mov_b32 %[cap], 0\n\t"
    "s_branch 6b\n\t"
    "18:\n\t"
    "s_add_u32 %[sp], %[sp], 0x10000\n\t"
    "s_lshr_b32 %[st], %[sp], 16\n\t"
    "s_cmp_ge_u32 %[st], %[cap]\n\t"
    "s_cbranch_scc1 20f\n\t"
    "v_lshrrev_b32 v46, 2, v5\n\t"
    "global_load_dword v44, v46, %[v2] sc1\n\t"
    "global_load_dword v46, v46, %[v2] offset:256 sc1\n\t"
    "s_waitcnt vmcnt(0) lgkmcnt(0)\n\t"
    "s_branch 17b\n\t"
    "20:\n\t"
    "s_mov_b32 %[cap], 0\n\t"
    "s_branch 19b\n\t"
    "22:\n\t"
    "ds_read_b32 v8, v15 offset:29184\n\t"
    "ds_read_b128 v[32:35], v5 offset:26624\n\t"
    "s_waitcnt lgkmcnt(0)\n\t"
    "s_branch 21b\n\t"
    "10:\n\t"
    "v_max_f32 v40, v40, v14\n\t"
    "v_mov_b32 v42, v40\n\t"
    "s_nop 1\n\t"
    "v_permlane16_swap_b32 v40, v42\n\t"
    "v_max_f32 v40, v40, v42\n\t"
    "v_mov_b32 v42, v40\n\t"
    "s_nop 1\n\t"
    "v_permlane32_swap_b32 v40, v42\n\t"
    "v_max_f32 v40, v40, v42\n\t"
    "v_sub_f32 v34, v32, v40\n\t"
    "v_sub_f32 v35, v33, v40\n\t"
    "v_sub_f32 v40, v14, v40\n\t"
    "v_exp_f32 v40, v40\n\t"
    "v_exp_f32 v34, v34\n\t"
    "v_exp_f32 v35, v35\n\t"
    "v_mul_f32 v24, v24, v40\n\t"
    "v_mul_f32 v25, v25, v40\n\t"
    "v_mul_f32 v26, v26, v40\n\t"
    "v_mul_f32 v27, v27, v40\n\t"
    "v_mul_f32 v28, v28, v40\n\t"
    "v_mul_f32 v29, v29, v40\n\t"
    "v_mul_f32 v30, v30, v40\n\t"
    "v_mul_f32 v31, v31, v40\n\t"
    "v_mul_f32 v52, v52, v40\n\t"
    "v_mul_f32 v53, v53, v40\n\t"
    "v_mul_f32 v54, v54, v40\n\t"
    "v_mul_f32 v55, v55, v40\n\t"
    "s_branch 11b\n\t"
    "12:\n\t"
    "v_max3_f32 v14, v24, v25, v26\n\t"
    "v_max3_f32 v40, v27, v28, v29\n\t"
    "v_max3_f32 v14, v14, v30, v31\n\t"
    "v_max3_f32 v14, v14, v40, %[neg]\n\t"
    "v_mov_b32 v40, v14\n\t"
    "s_nop 1\n\t"
    "v_permlane16_swap_b32 v14, v40\n\t"
    "v_max_f32 v14, v14, v40\n\t"
    "v_mov_b32 v40, v14\n\t"
    "s_nop 1\n\t"
    "v_permlane32_swap_b32 v14, v40\n\t"
    "v_max_f32 v14, v14, v40\n\t"
    "v_sub_f32 v24, v24, v14\n\t"
    "v_sub_f32 v25, v25, v14\n\t"
    "v_sub_f32 v26, v26, v14\n\t"
    "v_sub_f32 v27, v27, v14\n\t"
    "v_sub_f32 v28, v28, v14\n\t"
    "v_sub_f32 v29, v29, v14\n\t"
    "v_sub_f32 v30, v30, v14\n\t"
    "v_sub_f32 v31, v31, v14\n\t"
    "s_mov_b32 %[bg], 0x7fc00000\n\t"
    "s_branch 13b\n\t"
    "9:"
    : [tc] "=&s"(tc), [sp] "=&s"(sp), [cap] "+s"(cap), [st] "=&s"(st), [bg] "=&s"(bg)
    : [ag] "s"(ag), [c44] "s"(0x4444u), [lsc] "s"(LSC), [sn] "s"(sn), [pb] "s"(pb), [vs] "s"(vs), [od] "s"(od), [neg] "s"(-1.0e38f), [one] "s"(0x3F803F80u), [big] "s"(24.0f), [v2] "s"(v2), "{v[64:95]}"(W.w0), "{v[96:127]}"(W.w1), "{v[128:159]}"(W.w2), "{v[160:191]}"(W.w3), "{v[192:223]}"(W.w4), "{v[224:255]}"(W.w5)
    : "v4", "v5", "v6", "v7", "v8", "v9", "v10", "v11", "v12", "v13", "v14", "v15", "v16", "v17", "v18", "v19", "v20", "v21", "v22", "v23", "v24", "v25", "v26", "v27", "v28", "v29", "v30", "v31", "v32", "v33", "v34", "v35", "v36", "v37", "v38", "v39", "v40", "v41", "v42", "v43", "v44", "v45", "v46", "v47", "v48", "v49", "v50", "v51", "v52", "v53", "v54", "v55", "v56", "v57", "v58", "v59", "v60", "v61", "v62", "v63", "vcc", "scc", "memory");
}
template <> __device__ __forceinline__ void qk_step<3>(const QKW& W, const char* ag, const char* vs, const char* v2, const char* od, unsigned pb, unsigned sn, unsigned& cap, unsigned& sp,
    unsigned long long& tc, unsigned long long* ts, unsigned& d0, unsigned& d1) {
  unsigned st, bg;
  asm volatile("v_mbcnt_lo_u32_b32 v60, -1, 0\n\t"
    "v_mbcnt_hi_u32_b32 v60, -1, v60\n\t"
    "v_lshlrev_b32 v5, 4, v60\n\t"
    "s_mov_b32 %[sp], 0\n\t"
    "s_setprio 2\n\t"
    "global_load_dwordx4 v[24:27], v5, %[ag] sc1\n\t"
    "global_load_dwordx4 v[28:31], v5, %[ag] offset:1024 sc1\n\t"
    "s_memtime %[tc]\n\t"
    "v_lshrrev_b32 v32, 4, v60\n\t"
    "v_and_b32 v33, 15, v60\n\t"
    "v_lshrrev_b32 v34, 2, v33\n\t"
    "v_and_b32 v35, 3, v60\n\t"
    "v_and_b32 v61, 1, v60\n\t"
    "v_lshlrev_b32 v61, 3, v61\n\t"
    "v_lshl_or_b32 v7, v34, 4, v61\n\t"
    "v_lshl_or_b32 v7, v32, 7, v7\n\t"
    "v_xor_b32 v10, v32, v33\n\t"
    "v_lshlrev_b32 v10, 4, v10\n\t"
    "v_lshl_or_b32 v10, v33, 8, v10\n\t"
    "v_or_b32 v10, %[pb], v10\n\t"
    "v_xor_b32 v11, 0x40, v10\n\t"
    "v_xor_b32 v12, 0x80, v10\n\t"
    "v_xor_b32 v13, 0xc0, v10\n\t"
    "v_lshl_or_b32 v34, v32, 2, v34\n\t"
    "v_lshrrev_b32 v35, 1, v35\n\t"
    "v_or_b32 v35, 12, v35\n\t"
    "v_xor_b32 v35, v35, v34\n\t"
    "v_lshl_or_b32 v14, v35, 4, v61\n\t"
    "v_lshl_or_b32 v14, v34, 8, v14\n\t"
    "v_or_b32 v14, %[pb], v14\n\t"
    "v_and_b32 v61, 3, v60\n\t"
    "v_lshlrev_b32 v61, 6, v61\n\t"
    "v_lshl_or_b32 v4, v32, 4, v61\n\t"
    "v_and_b32 v61, 4, v60\n\t"
    "v_lshl_or_b32 v4, v61, 10, v4\n\t"
    "v_and_b32 v61, 8, v60\n\t"
    "v_lshl_or_b32 v4, v61, 14, v4\n\t"
    "v_or_b32 v4, 768, v4\n\t"
    "v_and_b32 v61, 2, v60\n\t"
    "v_mul_u32_u24 v61, 0x5555, v61\n\t"
    "v_or_b32 v6, %[c44], v61\n\t"
    "v_lshlrev_b32 v61, 2, v32\n\t"
    "v_lshl_or_b32 v15, v33, 4, v61\n\t"
    "3:\n\t"
    "v_mov_b32 v16, 0\n\t"
    "v_mov_b32 v17, 0\n\t"
    "v_mov_b32 v18, 0\n\t"
    "v_mov_b32 v19, 0\n\t"
    "v_mov_b32 v20, 0\n\t"
    "v_mov_b32 v21, 0\n\t"
    "v_mov_b32 v22, 0\n\t"
    "v_mov_b32 v23, 0\n\t"
    "s_waitcnt vmcnt(0) lgkmcnt(0)\n\t"
    "ds_write_b128 v5, v[24:27] offset:6144\n\t"
    "ds_write_b128 v5, v[28:31] offset:7168\n\t"
    "v_smfmac_f32_16x16x64_bf16 v[16:19], v[24:27], v[160:167], v6\n\t"
    "v_max3_u32 v60, v24, v25, v26\n\t"
    "v_max3_u32 v61, v27, v28, v29\n\t"
    "v_smfmac_f32_16x16x64_bf16 v[20:23], v[24:27], v[168:175], v6\n\t"
    "v_max3_u32 v60, v60, v30, v31\n\t"
    "v_max_u32 v60, v60, v61\n\t"
    "v_cmp_eq_u32 vcc, -1, v60\n\t"
    "s_cbranch_vccnz 1f\n\t"
    "2:\n\t"
    "s_waitcnt lgkmcnt(0)\n\t"
    "s_barrier\n\t"
    "ds_read_b128 v[32:35], v5 offset:0\n\t"
    "ds_read_b128 v[36:39], v5 offset:1024\n\t"
    "v_smfmac_f32_16x16x64_bf16 v[16:19], v[28:31], v[176:183], v6\n\t"
    "ds_read_b128 v[40:43], v5 offset:2048\n\t"
    "ds_read_b128 v[44:47], v5 offset:3072\n\t"
    "v_smfmac_f32_16x16x64_bf16 v[20:23], v[28:31], v[184:191], v6\n\t"
    "s_waitcnt lgkmcnt(3)\n\t"
    "v_smfmac_f32_16x16x64_bf16 v[16:19], v[32:35], v[64:71], v6\n\t"
    "ds_read_b128 v[48:51], v5 offset:4096\n\t"
    "v_smfmac_f32_16x16x64_bf16 v[20:23], v[32:35], v[72:79], v6\n\t"
    "ds_read_b128 v[52:55], v5 offset:5120\n\t"
    "s_waitcnt lgkmcnt(4)\n\t"
    "v_smfmac_f32_16x16x64_bf16 v[16:19], v[36:39], v[80:87], v6\n\t"
    "ds_read_b128 v[24:27], v5 offset:8192\n\t"
    "v_smfmac_f32_16x16x64_bf16 v[20:23], v[36:39], v[88:95], v6\n\t"
    "ds_read_b128 v[28:31], v5 offset:9216\n\t"
    "s_waitcnt lgkmcnt(5)\n\t"
    "v_smfmac_f32_16x16x64_bf16 v[16:19], v[40:43], v[96:103], v6\n\t"
    "ds_read_b128 v[56:59], v5 offset:10240\n\t"
    "v_smfmac_f32_16x16x64_bf16 v[20:23], v[40:43], v[104:111], v6\n\t"
    "ds_read_b128 v[60:63], v5 offset:11264\n\t"
    "ds_read_b128 v[36:39], v11 offset:36864\n\t"
    "s_waitcnt lgkmcnt(7)\n\t"
    "v_smfmac_f32_16x16x64_bf16 v[16:19], v[44:47], v[112:119], v6\n\t"
    "ds_read_b128 v[40:43], v12 offset:32768\n\t"
    "v_smfmac_f32_16x16x64_bf16 v[20:23], v[44:47], v[120:127], v6\n\t"
    "ds_read_b128 v[44:47], v12 offset:36864\n\t"
    "s_waitcnt lgkmcnt(8)\n\t"
    "v_smfmac_f32_16x16x64_bf16 v[16:19], v[48:51], v[128:135], v6\n\t"
    "v_smfmac_f32_16x16x64_bf16 v[20:23], v[48:51], v[136:143], v6\n\t"
    "ds_read_b128 v[48:51], v13 offset:32768\n\t"
    "s_waitcnt lgkmcnt(8)\n\t"
    "v_smfmac_f32_16x16x64_bf16 v[16:19], v[52:55], v[144:151], v6\n\t"
    "v_smfmac_f32_16x16x64_bf16 v[20:23], v[52:55], v[152:159], v6\n\t"
    "ds_read_b128 v[52:55], v13 offset:36864\n\t"
    "s_waitcnt lgkmcnt(8)\n\t"
    "v_smfmac_f32_16x16x64_bf16 v[16:19], v[24:27], v[192:199], v6\n\t"
    "v_smfmac_f32_16x16x64_bf16 v[20:23], v[24:27], v[200:207], v6\n\t"
    "ds_read_b128 v[24:27], v10 offset:32768\n\t"
    "s_waitcnt lgkmcnt(8)\n\t"
    "v_smfmac_f32_16x16x64_bf16 v[16:19], v[28:31], v[208:215], v6\n\t"
    "ds_read_b32 v8, v15 offset:29440\n\t"
    "ds_read_b128 v[32:35], v5 offset:27648\n\t"
    "s_setprio 3\n\t"
    "s_waitcnt lgkmcnt(9)\n\t"
    "v_smfmac_f32_16x16x64_bf16 v[16:19], v[56:59], v[224:231], v6\n\t"
    "s_waitcnt lgkmcnt(8)\n\t"
    "v_smfmac_f32_16x16x64_bf16 v[16:19], v[60:63], v[240:247], v6\n\t"
    "v_smfmac_f32_16x16x64_bf16 v[20:23], v[28:31], v[216:223], v6\n\t"
    "s_waitcnt lgkmcnt(0)\n\t"
    "21:\n\t"
    "v_cmp_ne_u32 vcc, %[sn], v8\n\t"
    "s_cbranch_vccnz 22f\n\t"
    "v_mul_f32 v32, %[lsc], v32\n\t"
    "v_mul_f32 v33, %[lsc], v33\n\t"
    "v_smfmac_f32_16x16x64_bf16 v[20:23], v[56:59], v[232:239], v6\n\t"
    "v_mul_f32 v34, %[lsc], v34\n\t"
    "v_mul_f32 v35, %[lsc], v35\n\t"
    "v_add_f32 v16, v16, v18\n\t"
    "v_add_f32 v17, v17, v19\n\t"
    "v_smfmac_f32_16x16x64_bf16 v[20:23], v[60:63], v[248:255], v6\n\t"
    "v_fma_f32 v16, v16, %[lsc], v32\n\t"
    "v_fma_f32 v17, v17, %[lsc], v33\n\t"
    "v_cvt_pk_bf16_f32 v16, v16, v17\n\t"
    "ds_write_b32 v15, v16 offset:17920\n\t"
    "s_mov_b32 %[bg], %[big]\n\t"
    "s_nop 2\n\t"
    "v_add_f32 v20, v20, v22\n\t"
    "v_add_f32 v21, v21, v23\n\t"
    "v_fma_f32 v20, v20, %[lsc], v34\n\t"
    "v_fma_f32 v21, v21, %[lsc], v35\n\t"
    "v_cvt_pk_bf16_f32 v20, v20, v21\n\t"
    "ds_write_b32 v15, v20 offset:18176\n\t"
    "ds_read_b128 v[28:31], v10 offset:36864\n\t"
    "ds_read_b128 v[32:35], v11 offset:32768\n\t"
    "s_waitcnt lgkmcnt(2)\n\t"
    "s_barrier\n\t"
    "ds_read_b64_tr_b16 v[16:17], v7 offset:16384\n\t"
    "ds_read_b64_tr_b16 v[18:19], v7 offset:16448\n\t"
    "ds_read_b64_tr_b16 v[20:21], v7 offset:16896\n\t"
    "ds_read_b64_tr_b16 v[22:23], v7 offset:16960\n\t"
    "ds_read_b64_tr_b16 v[56:57], v7 offset:17408\n\t"
    "ds_read_b64_tr_b16 v[58:59], v7 offset:17472\n\t"
    "ds_read_b64_tr_b16 v[60:61], v7 offset:17920\n\t"
    "ds_read_b64_tr_b16 v[62:63], v7 offset:17984\n\t"
    "global_load_dwordx4 v[6:9], v5, %[vs] sc1\n\t"
    "global_load_dwordx4 v[10:13], v5, %[vs] offset:1024 sc1\n\t"
    "s_waitcnt lgkmcnt(6)\n\t"
    "v_mfma_f32_16x16x32_bf16 v[24:27], v[24:27], v[16:19], 0\n\t"
    "v_mfma_f32_16x16x32_bf16 v[28:31], v[28:31], v[16:19], 0\n\t"
    "s_waitcnt lgkmcnt(4)\n\t"
    "v_mfma_f32_16x16x32_bf16 v[24:27], v[32:35], v[20:23], v[24:27]\n\t"
    "v_mfma_f32_16x16x32_bf16 v[28:31], v[36:39], v[20:23], v[28:31]\n\t"
    "v_xor_b32 v39, 32, v14\n\t"
    "ds_read_b64_tr_b16 v[32:33], v14 offset:40960\n\t"
    "ds_read_b64_tr_b16 v[34:35], v14 offset:45056\n\t"
    "ds_read_b64_tr_b16 v[36:37], v39 offset:40960\n\t"
    "ds_read_b64_tr_b16 v[38:39], v39 offset:45056\n\t"
    "s_waitcnt lgkmcnt(6)\n\t"
    "v_mfma_f32_16x16x32_bf16 v[24:27], v[40:43], v[56:59], v[24:27]\n\t"
    "v_mfma_f32_16x16x32_bf16 v[28:31], v[44:47], v[56:59], v[28:31]\n\t"
    "s_waitcnt lgkmcnt(4)\n\t"
    "v_mfma_f32_16x16x32_bf16 v[24:27], v[48:51], v[60:63], v[24:27]\n\t"
    "v_mfma_f32_16x16x32_bf16 v[28:31], v[52:55], v[60:63], v[28:31]\n\t"
    "v_mov_b32 v52, %[one]\n\t"
    "v_mov_b32 v53, %[one]\n\t"
    "v_mov_b32 v54, %[one]\n\t"
    "v_mov_b32 v55, %[one]\n\t"
    "v_mov_b32 v45, 0\n\t"
    "v_mov_b32 v47, 0\n\t"
    "v_mov_b32 v48, %[one]\n\t"
    "v_max3_f32 v14, |v24|, |v25|, |v26|\n\t"
    "v_max3_f32 v40, |v27|, |v28|, |v29|\n\t"
    "v_max3_f32 v14, v14, |v30|, |v31|\n\t"
    "v_max_f32 v14, v14, v40\n\t"
    "v_cmp_nge_f32 vcc, %[big], v14\n\t"
    "v_mov_b32 v14, 0\n\t"
    "s_cbranch_vccnz 12f\n\t"
    "13:\n\t"
    "v_exp_f32 v24, v24\n\t"
    "v_exp_f32 v25, v25\n\t"
    "v_exp_f32 v26, v26\n\t"
    "v_exp_f32 v27, v27\n\t"
    "v_exp_f32 v28, v28\n\t"
    "v_exp_f32 v29, v29\n\t"
    "v_exp_f32 v30, v30\n\t"
    "v_exp_f32 v31, v31\n\t"
    "v_lshrrev_b32 v46, 2, v5\n\t"
    "global_load_dword v44, v46, %[v2] sc1\n\t"
    "global_load_dword v46, v46, %[v2] offset:256 sc1\n\t"
    "v_cvt_pk_bf16_f32 v40, v24, v25\n\t"
    "v_cvt_pk_bf16_f32 v41, v26, v27\n\t"
    "v_cvt_pk_bf16_f32 v42, v28, v29\n\t"
    "v_cvt_pk_bf16_f32 v43, v30, v31\n\t"
    "v_mov_b32 v49, 0\n\t"
    "v_mov_b32 v51, 0\n\t"
    "s_waitcnt lgkmcnt(0)\n\t"
    "v_mfma_f32_16x16x32_bf16 v[52:55], v[52:55], v[40:43], 0\n\t"
    "v_mfma_f32_16x16x32_bf16 v[24:27], v[32:35], v[40:43], 0\n\t"
    "v_mfma_f32_16x16x32_bf16 v[28:31], v[36:39], v[40:43], 0\n\t"
    "s_waitcnt vmcnt(3)\n\t"
    "5:\n\t"
    "v_mfma_f32_16x16x32_bf16 v[32:35], v[6:9], v[16:19], 0\n\t"
    "v_mfma_f32_16x16x32_bf16 v[36:39], v[6:9], v[20:23], 0\n\t"
    "s_waitcnt vmcnt(2)\n\t"
    "v_mfma_f32_16x16x32_bf16 v[32:35], v[10:13], v[56:59], v[32:35]\n\t"
    "v_mfma_f32_16x16x32_bf16 v[36:39], v[10:13], v[60:63], v[36:39]\n\t"
    "v_max3_u32 v40, v6, v7, v8\n\t"
    "v_max3_u32 v41, v9, v10, v11\n\t"
    "v_max3_u32 v40, v40, v12, v13\n\t"
    "v_max_u32 v40, v40, v41\n\t"
    "v_cmp_eq_u32 vcc, -1, v40\n\t"
    "s_cbranch_vccnz 4f\n\t"
    "6:\n\t"
    "s_nop 0\n\t"
    "s_nop 0\n\t"
    "v_add_f32 v32, v32, v38\n\t"
    "v_add_f32 v33, v33, v39\n\t"
    "v_exp_f32 v34, v32\n\t"
    "v_max_f32 v40, v32, v33\n\t"
    "v_exp_f32 v35, v33\n\t"
    "v_cmp_nge_f32 vcc, %[bg], v40\n\t"
    "s_cbranch_vccnz 10f\n\t"
    "11:\n\t"
    "v_cvt_pk_bf16_f32 v50, v34, v35\n\t"
    "17:\n\t"
    "s_waitcnt vmcnt(0) lgkmcnt(0)\n\t"
    "v_max_u32 v40, v44, v46\n\t"
    "v_cmp_eq_u32 vcc, -1, v40\n\t"
    "s_cbranch_vccnz 18f\n\t"
    "19:\n\t"
    "v_mfma_f32_16x16x16_bf16 v[52:55], v[48:49], v[50:51], v[52:55]\n\t"
    "v_mfma_f32_16x16x16_bf16 v[24:27], v[44:45], v[50:51], v[24:27]\n\t"
    "v_mfma_f32_16x16x16_bf16 v[28:31], v[46:47], v[50:51], v[28:31]\n\t"
    "s_nop 5\n\t"
    "v_rcp_f32 v42, v52\n\t"
    "s_nop 0\n\t"
    "v_pk_mul_f32 v[24:25], v[24:25], v[42:43] op_sel_hi:[1,0]\n\t"
    "v_pk_mul_f32 v[26:27], v[26:27], v[42:43] op_sel_hi:[1,0]\n\t"
    "v_pk_mul_f32 v[28:29], v[28:29], v[42:43] op_sel_hi:[1,0]\n\t"
    "v_pk_mul_f32 v[30:31], v[30:31], v[42:43] op_sel_hi:[1,0]\n\t"
    "v_cvt_pk_bf16_f32 v36, v24, v25\n\t"
    "v_cvt_pk_bf16_f32 v37, v26, v27\n\t"
    "v_cvt_pk_bf16_f32 v38, v28, v29\n\t"
    "v_cvt_pk_bf16_f32 v39, v30, v31\n\t"
    "v_min_u32 v36, -2, v36\n\t"
    "v_min_u32 v37, -2, v37\n\t"
    "v_min_u32 v38, -2, v38\n\t"
    "v_min_u32 v39, -2, v39\n\t"
    "global_store_dwordx4 v4, v[36:39], %[od] sc1\n\t"
    "ds_write_b32 v15, v44 offset:19968\n\t"
    "ds_write_b32 v15, v46 offset:20224\n\t"
    "s_setprio 1\n\t"
    "s_waitcnt lgkmcnt(0)\n\t"
    "s_barrier\n\t"
    "s_waitcnt lgkmcnt(0)\n\t"
    "s_branch 9f\n\t"
    "1:\n\t"
    "s_add_u32 %[sp], %[sp], 1\n\t"
    "s_cmp_ge_u32 %[sp], %[cap]\n\t"
    "s_cbranch_scc1 7f\n\t"
    "global_load_dwordx4 v[24:27], v5, %[ag] sc1\n\t"
    "global_load_dwordx4 v[28:31], v5, %[ag] offset:1024 sc1\n\t"
    "s_waitcnt lgkmcnt(0)\n\t"
    "s_memtime %[tc]\n\t"
    "s_nop 7\n\t"
    "s_branch 3b\n\t"
    "7:\n\t"
    "s_mov_b32 %[cap], 0\n\t"
    "s_branch 2b\n\t"
    "4:\n\t"
    "s_add_u32 %[sp], %[sp], 0x10000\n\t"
    "s_lshr_b32 %[st], %[sp], 16\n\t"
    "s_cmp_ge_u32 %[st], %[cap]\n\t"
    "s_cbranch_scc1 8f\n\t"
    "global_load_dwordx4 v[6:9], v5, %[vs] sc1\n\t"
    "global_load_dwordx4 v[10:13], v5, %[vs] offset:1024 sc1\n\t"
    "s_waitcnt vmcnt(0) lgkmcnt(0)\n\t"
    "s_branch 5b\n\t"
    "8:\n\t"
    "s_mov_b32 %[cap], 0\n\t"
    "s_branch 6b\n\t"
    "18:\n\t"
    "s_add_u32 %[sp], %[sp], 0x10000\n\t"
    "s_lshr_b32 %[st], %[sp], 16\n\t"
    "s_cmp_ge_u32 %[st], %[cap]\n\t"
    "s_cbranch_scc1 20f\n\t"
    "v_lshrrev_b32 v46, 2, v5\n\t"
    "global_load_dword v44, v46, %[v2] sc1\n\t"
    "global_load_dword v46, v46, %[v2] offset:256 sc1\n\t"
    "s_waitcnt vmcnt(0) lgkmcnt(0)\n\t"
    "s_branch 17b\n\t"
    "20:\n\t"
    "s_mov_b32 %[cap], 0\n\t"
    "s_branch 19b\n\t"
    "22:\n\t"
    "ds_read_b32 v8, v15 offset:29440\n\t"
    "ds_read_b128 v[32:35], v5 offset:27648\n\t"
    "s_waitcnt lgkmcnt(0)\n\t"
    "s_branch 21b\n\t"
    "10:\n\t"
    "v_max_f32 v40, v40, v14\n\t"
    "v_mov_b32 v42, v40\n\t"
    "s_nop 1\n\t"
    "v_permlane16_swap_b32 v40, v42\n\t"
    "v_max_f32 v40, v40, v42\n\t"
    "v_mov_b32 v42, v40\n\t"
    "s_nop 1\n\t"
    "v_permlane32_swap_b32 v40, v42\n\t"
    "v_max_f32 v40, v40, v42\n\t"
    "v_sub_f32 v34, v32, v40\n\t"
    "v_sub_f32 v35, v33, v40\n\t"
    "v_sub_f32 v40, v14, v40\n\t"
    "v_exp_f32 v40, v40\n\t"
    "v_exp_f32 v34, v34\n\t"
    "v_exp_f32 v35, v35\n\t"
    "v_mul_f32 v24, v24, v40\n\t"
    "v_mul_f32 v25, v25, v40\n\t"
    "v_mul_f32 v26, v26, v40\n\t"
    "v_mul_f32 v27, v27, v40\n\t"
    "v_mul_f32 v28, v28, v40\n\t"
    "v_mul_f32 v29, v29, v40\n\t"
    "v_mul_f32 v30, v30, v40\n\t"
    "v_mul_f32 v31, v31, v40\n\t"
    "v_mul_f32 v52, v52, v40\n\t"
    "v_mul_f32 v53, v53, v40\n\t"
    "v_mul_f32 v54, v54, v40\n\t"
    "v_mul_f32 v55, v55, v40\n\t"
    "s_branch 11b\n\t"
    "12:\n\t"
    "v_max3_f32 v14, v24, v25, v26\n\t"
    "v_max3_f32 v40, v27, v28, v29\n\t"
    "v_max3_f32 v14, v14, v30, v31\n\t"
    "v_max3_f32 v14, v14, v40, %[neg]\n\t"
    "v_mov_b32 v40, v14\n\t"
    "s_nop 1\n\t"
    "v_permlane16_swap_b32 v14, v40\n\t"
    "v_max_f32 v14, v14, v40\n\t"
    "v_mov_b32 v40, v14\n\t"
    "s_nop 1\n\t"
    "v_permlane32_swap_b32 v14, v40\n\t"
    "v_max_f32 v14, v14, v40\n\t"
    "v_sub_f32 v24, v24, v14\n\t"
    "v_sub_f32 v25, v25, v14\n\t"
    "v_sub_f32 v26, v26, v14\n\t"
    "v_sub_f32 v27, v27, v14\n\t"
    "v_sub_f32 v28, v28, v14\n\t"
    "v_sub_f32 v29, v29, v14\n\t"
    "v_sub_f32 v30, v30, v14\n\t"
    "v_sub_f32 v31, v31, v14\n\t"
    "s_mov_b32 %[bg], 0x7fc00000\n\t"
    "s_branch 13b\n\t"
    "9:"
    : [tc] "=&s"(tc), [sp] "=&s"(sp), [cap] "+s"(cap), [st] "=&s"(st), [bg] "=&s"(bg)
    : [ag] "s"(ag), [c44] "s"(0x4444u), [lsc] "s"(LSC), [sn] "s"(sn), [pb] "s"(pb), [vs] "s"(vs), [od] "s"(od), [neg] "s"(-1.0e38f), [one] "s"(0x3F803F80u), [big] "s"(24.0f), [v2] "s"(v2), "{v[64:95]}"(W.w0), "{v[96:127]}"(W.w1), "{v[128:159]}"(W.w2), "{v[160:191]}"(W.w3), "{v[192:223]}"(W.w4), "{v[224:255]}"(W.w5)
    : "v4", "v5", "v6", "v7", "v8", "v9", "v10", "v11", "v12", "v13", "v14", "v15", "v16", "v17", "v18", "v19", "v20", "v21", "v22", "v23", "v24", "v25", "v26", "v27", "v28", "v29", "v30", "v31", "v32", "v33", "v34", "v35", "v36", "v37", "v38", "v39", "v40", "v41", "v42", "v43", "v44", "v45", "v46", "v47", "v48", "v49", "v50", "v51", "v52", "v53", "v54", "v55", "v56", "v57", "v58", "v59", "v60", "v61", "v62", "v63", "vcc", "scc", "memory");
}
template <> __device__ __forceinline__ void qk_step<4>(const QKW& W, const char* ag, const char* vs, const char* v2, const char* od, unsigned pb, unsigned sn, unsigned& cap, unsigned& sp,
    unsigned long long& tc, unsigned long long* ts, unsigned& d0, unsigned& d1) {
  unsigned st, bg;
  asm volatile("v_mbcnt_lo_u32_b32 v60, -1, 0\n\t"
    "v_mbcnt_hi_u32_b32 v60, -1, v60\n\t"
    "v_lshlrev_b32 v6, 4, v60\n\t"
    "s_mov_b32 %[sp], 0\n\t"
    "s_setprio 3\n\t"
    "global_load_dwordx4 v[24:27], v6, %[ag] sc1\n\t"
    "global_load_dwordx4 v[28:31], v6, %[ag] offset:-4096 sc1\n\t"
    "s_memtime %[tc]\n\t"
    "v_lshrrev_b32 v56, 4, v60\n\t"
    "v_and_b32 v57, 15, v60\n\t"
    "v_mov_b32 v20, 0\n\t"
    "v_mov_b32 v21, 0\n\t"
    "v_mov_b32 v22, 0\n\t"
    "v_mov_b32 v23, 0\n\t"
    "v_mov_b32 v11, %[sn]\n\t"
    "v_mov_b32 v10, -1\n\t"
    "v_and_b32 v61, 2, v60\n\t"
    "v_mul_u32_u24 v61, 0x5555, v61\n\t"
    "v_or_b32 v8, %[c44], v61\n\t"
    "v_lshlrev_b32 v61, 2, v56\n\t"
    "v_lshl_or_b32 v9, v57, 4, v61\n\t"
    "3:\n\t"
    "v_mov_b32 v12, 0\n\t"
    "v_mov_b32 v13, 0\n\t"
    "v_mov_b32 v14, 0\n\t"
    "v_mov_b32 v15, 0\n\t"
    "v_mov_b32 v16, 0\n\t"
    "v_mov_b32 v17, 0\n\t"
    "v_mov_b32 v18, 0\n\t"
    "v_mov_b32 v19, 0\n\t"
    "s_waitcnt vmcnt(0) lgkmcnt(0)\n\t"
    "ds_write_b128 v6, v[24:27] offset:12288\n\t"
    "ds_write_b128 v6, v[28:31] offset:8192\n\t"
    "v_smfmac_f32_16x16x64_bf16 v[12:15], v[24:27], v[64:71], v8\n\t"
    "v_max3_u32 v60, v24, v25, v26\n\t"
    "v_max3_u32 v61, v27, v28, v29\n\t"
    "v_smfmac_f32_16x16x64_bf16 v[16:19], v[24:27], v[72:79], v8\n\t"
    "v_max3_u32 v60, v60, v30, v31\n\t"
    "v_max_u32 v60, v60, v61\n\t"
    "v_cmp_eq_u32 vcc, -1, v60\n\t"
    "s_cbranch_vccnz 1f\n\t"
    "2:\n\t"
    "s_waitcnt lgkmcnt(0)\n\t"
    "s_barrier\n\t"
    "ds_read_b128 v[32:35], v6 offset:13312\n\t"
    "ds_read_b128 v[36:39], v6 offset:14336\n\t"
    "ds_read_b128 v[40:43], v6 offset:15360\n\t"
    "ds_write_b32 v9, v10 offset:18432\n\t"
    "s_waitcnt lgkmcnt(3)\n\t"
    "v_smfmac_f32_16x16x64_bf16 v[12:15], v[32:35], v[80:87], v8\n\t"
    "v_smfmac_f32_16x16x64_bf16 v[16:19], v[32:35], v[88:95], v8\n\t"
    "s_waitcnt lgkmcnt(2)\n\t"
    "v_smfmac_f32_16x16x64_bf16 v[12:15], v[36:39], v[96:103], v8\n\t"
    "v_smfmac_f32_16x16x64_bf16 v[16:19], v[36:39], v[104:111], v8\n\t"
    "s_waitcnt lgkmcnt(1)\n\t"
    "v_smfmac_f32_16x16x64_bf16 v[12:15], v[40:43], v[112:119], v8\n\t"
    "v_smfmac_f32_16x16x64_bf16 v[16:19], v[40:43], v[120:127], v8\n\t"
    "s_setprio 0\n\t"
    "s_sleep 1\n\t"
    "s_nop 6\n\t"
    "v_add_f32 v12, v12, v14\n\t"
    "v_add_f32 v13, v13, v15\n\t"
    "v_add_f32 v14, v16, v18\n\t"
    "v_add_f32 v15, v17, v19\n\t"
    "ds_write_b128 v6, v[12:15] offset:24576\n\t"
    "ds_write_b32 v9, v11 offset:28672\n\t"
    "s_setprio 0\n\t"
    "s_memrealtime s[36:37]\n\t"
    "s_waitcnt lgkmcnt(0)\n\t"
    "s_mov_b32 s38, s36\n\t"
    "s_min_u32 s37, %[d0], %[d1]\n\t"
    "s_add_u32 s36, s36, s37\n\t"
    "s_and_b32 s36, s36, 0xffffff\n\t"
    "s_or_b32 s36, s36, %[sn]\n\t"
    "v_lshlrev_b32 v10, 10, v6\n\t"
    "v_mov_b32 v5, s36\n\t"
    "s_mov_b64 exec, 15\n\t"
    "global_store_dword v10, v5, %[vs] offset:-4096 sc1\n\t"
    "s_mov_b64 exec, -1\n\t"
    "ds_read_b128 v[44:47], v6 offset:0\n\t"
    "ds_read_b128 v[48:51], v6 offset:1024\n\t"
    "ds_read_b128 v[52:55], v6 offset:2048\n\t"
    "ds_read_b128 v[56:59], v6 offset:3072\n\t"
    "ds_read_b128 v[60:63], v6 offset:4096\n\t"
    "s_sleep 1\n\t"
    "v_smfmac_f32_16x16x64_bf16 v[20:23], v[28:31], v[192:199], v8\n\t"
    "ds_read_b128 v[28:31], v6 offset:5120\n\t"
    "v_smfmac_f32_16x16x64_bf16 v[20:23], v[24:27], v[224:231], v8\n\t"
    "ds_read_b128 v[24:27], v6 offset:6144\n\t"
    "v_smfmac_f32_16x16x64_bf16 v[20:23], v[32:35], v[240:247], v8\n\t"
    "ds_read_b128 v[32:35], v6 offset:7168\n\t"
    "v_smfmac_f32_16x16x64_bf16 v[20:23], v[36:39], v[232:239], v8\n\t"
    "ds_read_b128 v[36:39], v6 offset:9216\n\t"
    "v_smfmac_f32_16x16x64_bf16 v[20:23], v[40:43], v[248:255], v8\n\t"
    "ds_read_b128 v[40:43], v6 offset:10240\n\t"
    "s_waitcnt lgkmcnt(9)\n\t"
    "v_smfmac_f32_16x16x64_bf16 v[20:23], v[44:47], v[128:135], v8\n\t"
    "ds_read_b128 v[44:47], v6 offset:11264\n\t"
    "s_barrier\n\t"
    "s_setprio 0\n\t"
    "s_waitcnt lgkmcnt(9)\n\t"
    "v_smfmac_f32_16x16x64_bf16 v[20:23], v[48:51], v[144:151], v8\n\t"
    "s_waitcnt lgkmcnt(8)\n\t"
    "v_smfmac_f32_16x16x64_bf16 v[20:23], v[52:55], v[136:143], v8\n\t"
    "s_waitcnt lgkmcnt(7)\n\t"
    "v_smfmac_f32_16x16x64_bf16 v[20:23], v[56:59], v[152:159], v8\n\t"
    "s_waitcnt lgkmcnt(6)\n\t"
    "v_smfmac_f32_16x16x64_bf16 v[20:23], v[60:63], v[160:167], v8\n\t"
    "s_waitcnt lgkmcnt(5)\n\t"
    "v_smfmac_f32_16x16x64_bf16 v[20:23], v[28:31], v[176:183], v8\n\t"
    "s_waitcnt lgkmcnt(4)\n\t"
    "v_smfmac_f32_16x16x64_bf16 v[20:23], v[24:27], v[168:175], v8\n\t"
    "s_waitcnt lgkmcnt(3)\n\t"
    "v_smfmac_f32_16x16x64_bf16 v[20:23], v[32:35], v[184:191], v8\n\t"
    "s_waitcnt lgkmcnt(2)\n\t"
    "v_smfmac_f32_16x16x64_bf16 v[20:23], v[36:39], v[208:215], v8\n\t"
    "s_waitcnt lgkmcnt(1)\n\t"
    "v_smfmac_f32_16x16x64_bf16 v[20:23], v[40:43], v[200:207], v8\n\t"
    "s_waitcnt lgkmcnt(0)\n\t"
    "v_smfmac_f32_16x16x64_bf16 v[20:23], v[44:47], v[216:223], v8\n\t"
    "s_nop 7\n\t"
    "v_add_f32 v20, v20, v22\n\t"
    "v_add_f32 v21, v21, v23\n\t"
    "v_cvt_pk_bf16_f32 v20, v20, v21\n\t"
    "v_min_u32 v20, -2, v20\n\t"
    "ds_write_b32 v9, v20 offset:18432\n\t"
    "s_waitcnt lgkmcnt(0)\n\t"
    "s_setprio 3\n\t"
    "s_barrier\n\t"
    "s_memrealtime s[36:37]\n\t"
    "s_mov_b32 %[d1], %[d0]\n\t"
    "s_waitcnt lgkmcnt(0)\n\t"
    "s_sub_u32 %[d0], s36, s38\n\t"
    "s_min_u32 %[d0], %[d0], 200\n\t"
    "s_and_b32 s36, s36, 0xffffff\n\t"
    "s_or_b32 s36, s36, %[sn]\n\t"
    "v_lshlrev_b32 v10, 10, v6\n\t"
    "v_mov_b32 v5, s36\n\t"
    "s_mov_b64 exec, 15\n\t"
    "global_store_dword v10, v5, %[vs] offset:0 sc1\n\t"
    "s_mov_b64 exec, -1\n\t"
    "s_waitcnt lgkmcnt(0)\n\t"
    "s_branch 9f\n\t"
    "1:\n\t"
    "s_add_u32 %[sp], %[sp], 1\n\t"
    "s_cmp_ge_u32 %[sp], %[cap]\n\t"
    "s_cbranch_scc1 7f\n\t"
    "global_load_dwordx4 v[24:27], v6, %[ag] sc1\n\t"
    "global_load_dwordx4 v[28:31], v6, %[ag] offset:-4096 sc1\n\t"
    "s_waitcnt lgkmcnt(0)\n\t"
    "s_memtime %[tc]\n\t"
    "s_nop 7\n\t"
    "s_branch 3b\n\t"
    "7:\n\t"
    "s_mov_b32 %[cap], 0\n\t"
    "s_branch 2b\n\t"
    "9:"
    : [tc] "=&s"(tc), [sp] "=&s"(sp), [cap] "+s"(cap), [st] "=&s"(st), [d0] "+s"(d0), [d1] "+s"(d1)
    : [ag] "s"(ag), [c44] "s"(0x4444u), [lsc] "s"(LSC), [sn] "s"(sn), [vs] "s"(vs), "{v[64:95]}"(W.w0), "{v[96:127]}"(W.w1), "{v[128:159]}"(W.w2), "{v[160:191]}"(W.w3), "{v[192:223]}"(W.w4), "{v[224:255]}"(W.w5)
    : "v5", "v6", "v7", "v8", "v9", "v10", "v11", "v12", "v13", "v14", "v15", "v16", "v17", "v18", "v19", "v20", "v21", "v22", "v23", "v24", "v25", "v26", "v27", "v28", "v29", "v30", "v31", "v32", "v33", "v34", "v35", "v36", "v37", "v38", "v39", "v40", "v41", "v42", "v43", "v44", "v45", "v46", "v47", "v48", "v49", "v50", "v51", "v52", "v53", "v54", "v55", "v56", "v57", "v58", "v59", "v60", "v61", "v62", "v63", "vcc", "scc", "memory", "s36", "s37", "s38");
}
template <> __device__ __forceinline__ void qk_step<5>(const QKW& W, const char* ag, const char* vs, const char* v2, const char* od, unsigned pb, unsigned sn, unsigned& cap, unsigned& sp,
    unsigned long long& tc, unsigned long long* ts, unsigned& d0, unsigned& d1) {
  unsigned st, bg;
  asm volatile("v_mbcnt_lo_u32_b32 v60, -1, 0\n\t"
    "v_mbcnt_hi_u32_b32 v60, -1, v60\n\t"
    "v_lshlrev_b32 v6, 4, v60\n\t"
    "s_mov_b32 %[sp], 0\n\t"
    "s_setprio 3\n\t"
    "global_load_dwordx4 v[24:27], v6, %[ag] sc1\n\t"
    "global_load_dwordx4 v[28:31], v6, %[ag] offset:-4096 sc1\n\t"
    "s_memtime %[tc]\n\t"
    "v_lshrrev_b32 v56, 4, v60\n\t"
    "v_and_b32 v57, 15, v60\n\t"
    "v_mov_b32 v20, 0\n\t"
    "v_mov_b32 v21, 0\n\t"
    "v_mov_b32 v22, 0\n\t"
    "v_mov_b32 v23, 0\n\t"
    "v_mov_b32 v11, %[sn]\n\t"
    "v_mov_b32 v10, -1\n\t"
    "v_and_b32 v61, 2, v60\n\t"
    "v_mul_u32_u24 v61, 0x5555, v61\n\t"
    "v_or_b32 v8, %[c44], v61\n\t"
    "v_lshlrev_b32 v61, 2, v56\n\t"
    "v_lshl_or_b32 v9, v57, 4, v61\n\t"
    "3:\n\t"
    "v_mov_b32 v12, 0\n\t"
    "v_mov_b32 v13, 0\n\t"
    "v_mov_b32 v14, 0\n\t"
    "v_mov_b32 v15, 0\n\t"
    "v_mov_b32 v16, 0\n\t"
    "v_mov_b32 v17, 0\n\t"
    "v_mov_b32 v18, 0\n\t"
    "v_mov_b32 v19, 0\n\t"
    "s_waitcnt vmcnt(0) lgkmcnt(0)\n\t"
    "ds_write_b128 v6, v[24:27] offset:13312\n\t"
    "ds_write_b128 v6, v[28:31] offset:9216\n\t"
    "v_smfmac_f32_16x16x64_bf16 v[12:15], v[24:27], v[80:87], v8\n\t"
    "v_max3_u32 v60, v24, v25, v26\n\t"
    "v_max3_u32 v61, v27, v28, v29\n\t"
    "v_smfmac_f32_16x16x64_bf16 v[16:19], v[24:27], v[88:95], v8\n\t"
    "v_max3_u32 v60, v60, v30, v31\n\t"
    "v_max_u32 v60, v60, v61\n\t"
    "v_cmp_eq_u32 vcc, -1, v60\n\t"
    "s_cbranch_vccnz 1f\n\t"
    "2:\n\t"
    "s_waitcnt lgkmcnt(0)\n\t"
    "s_barrier\n\t"
    "ds_read_b128 v[32:35], v6 offset:12288\n\t"
    "ds_read_b128 v[36:39], v6 offset:14336\n\t"
    "ds_read_b128 v[40:43], v6 offset:15360\n\t"
    "ds_write_b32 v9, v10 offset:18688\n\t"
    "s_waitcnt lgkmcnt(3)\n\t"
    "v_smfmac_f32_16x16x64_bf16 v[12:15], v[32:35], v[64:71], v8\n\t"
    "v_smfmac_f32_16x16x64_bf16 v[16:19], v[32:35], v[72:79], v8\n\t"
    "s_waitcnt lgkmcnt(2)\n\t"
    "v_smfmac_f32_16x16x64_bf16 v[12:15], v[36:39], v[96:103], v8\n\t"
    "v_smfmac_f32_16x16x64_bf16 v[16:19], v[36:39], v[104:111], v8\n\t"
    "s_waitcnt lgkmcnt(1)\n\t"
    "v_smfmac_f32_16x16x64_bf16 v[12:15], v[40:43], v[112:119], v8\n\t"
    "v_smfmac_f32_16x16x64_bf16 v[16:19], v[40:43], v[120:127], v8\n\t"
    "s_setprio 0\n\t"
    "s_sleep 1\n\t"
    "s_nop 6\n\t"
    "v_add_f32 v12, v12, v14\n\t"
    "v_add_f32 v13, v13, v15\n\t"
    "v_add_f32 v14, v16, v18\n\t"
    "v_add_f32 v15, v17, v19\n\t"
    "ds_write_b128 v6, v[12:15] offset:25600\n\t"
    "ds_write_b32 v9, v11 offset:28928\n\t"
    "s_setprio 0\n\t"
    "ds_read_b128 v[44:47], v6 offset:0\n\t"
    "ds_read_b128 v[48:51], v6 offset:1024\n\t"
    "ds_read_b128 v[52:55], v6 offset:2048\n\t"
    "ds_read_b128 v[56:59], v6 offset:3072\n\t"
    "ds_read_b128 v[60:63], v6 offset:4096\n\t"
    "s_sleep 1\n\t"
    "v_smfmac_f32_16x16x64_bf16 v[20:23], v[28:31], v[208:215], v8\n\t"
    "ds_read_b128 v[28:31], v6 offset:5120\n\t"
    "v_smfmac_f32_16x16x64_bf16 v[20:23], v[24:27], v[240:247], v8\n\t"
    "ds_read_b128 v[24:27], v6 offset:6144\n\t"
    "v_smfmac_f32_16x16x64_bf16 v[20:23], v[32:35], v[224:231], v8\n\t"
    "ds_read_b128 v[32:35], v6 offset:7168\n\t"
    "v_smfmac_f32_16x16x64_bf16 v[20:23], v[36:39], v[232:239], v8\n\t"
    "ds_read_b128 v[36:39], v6 offset:8192\n\t"
    "v_smfmac_f32_16x16x64_bf16 v[20:23], v[40:43], v[248:255], v8\n\t"
    "ds_read_b128 v[40:43], v6 offset:10240\n\t"
    "s_waitcnt lgkmcnt(9)\n\t"
    "v_smfmac_f32_16x16x64_bf16 v[20:23], v[44:47], v[128:135], v8\n\t"
    "ds_read_b128 v[44:47], v6 offset:11264\n\t"
    "s_barrier\n\t"
    "s_setprio 0\n\t"
    "s_waitcnt lgkmcnt(9)\n\t"
    "v_smfmac_f32_16x16x64_bf16 v[20:23], v[48:51], v[144:151], v8\n\t"
    "s_waitcnt lgkmcnt(8)\n\t"
    "v_smfmac_f32_16x16x64_bf16 v[20:23], v[52:55], v[136:143], v8\n\t"
    "s_waitcnt lgkmcnt(7)\n\t"
    "v_smfmac_f32_16x16x64_bf16 v[20:23], v[56:59], v[152:159], v8\n\t"
    "s_waitcnt lgkmcnt(6)\n\t"
    "v_smfmac_f32_16x16x64_bf16 v[20:23], v[60:63], v[160:167], v8\n\t"
    "s_waitcnt lgkmcnt(5)\n\t"
    "v_smfmac_f32_16x16x64_bf16 v[20:23], v[28:31], v[176:183], v8\n\t"
    "s_waitcnt lgkmcnt(4)\n\t"
    "v_smfmac_f32_16x16x64_bf16 v[20:23], v[24:27], v[168:175], v8\n\t"
    "s_waitcnt lgkmcnt(3)\n\t"
    "v_smfmac_f32_16x16x64_bf16 v[20:23], v[32:35], v[184:191], v8\n\t"
    "s_waitcnt lgkmcnt(2)\n\t"
    "v_smfmac_f32_16x16x64_bf16 v[20:23], v[36:39], v[192:199], v8\n\t"
    "s_waitcnt lgkmcnt(1)\n\t"
    "v_smfmac_f32_16x16x64_bf16 v[20:23], v[40:43], v[200:207], v8\n\t"
    "s_waitcnt lgkmcnt(0)\n\t"
    "v_smfmac_f32_16x16x64_bf16 v[20:23], v[44:47], v[216:223], v8\n\t"
    "s_nop 7\n\t"
    "v_add_f32 v20, v20, v22\n\t"
    "v_add_f32 v21, v21, v23\n\t"
    "v_cvt_pk_bf16_f32 v20, v20, v21\n\t"
    "v_min_u32 v20, -2, v20\n\t"
    "ds_write_b32 v9, v20 offset:18688\n\t"
    "s_waitcnt lgkmcnt(0)\n\t"
    "s_setprio 0\n\t"
    "s_setprio 0\n\t"
    "s_waitcnt lgkmcnt(0)\n\t"
    "s_branch 9f\n\t"
    "1:\n\t"
    "s_add_u32 %[sp], %[sp], 1\n\t"
    "s_cmp_ge_u32 %[sp], %[cap]\n\t"
    "s_cbranch_scc1 7f\n\t"
    "global_load_dwordx4 v[24:27], v6, %[ag] sc1\n\t"
    "global_load_dwordx4 v[28:31], v6, %[ag] offset:-4096 sc1\n\t"
    "s_waitcnt lgkmcnt(0)\n\t"
    "s_memtime %[tc]\n\t"
    "s_nop 7\n\t"
    "s_branch 3b\n\t"
    "7:\n\t"
    "s_mov_b32 %[cap], 0\n\t"
    "s_branch 2b\n\t"
    "9:"
    : [tc] "=&s"(tc), [sp] "=&s"(sp), [cap] "+s"(cap), [st] "=&s"(st)
    : [ag] "s"(ag), [c44] "s"(0x4444u), [lsc] "s"(LSC), [sn] "s"(sn), "{v[64:95]}"(W.w0), "{v[96:127]}"(W.w1), "{v[128:159]}"(W.w2), "{v[160:191]}"(W.w3), "{v[192:223]}"(W.w4), "{v[224:255]}"(W.w5)
    : "v6", "v7", "v8", "v9", "v10", "v11", "v12", "v13", "v14", "v15", "v16", "v17", "v18", "v19", "v20", "v21", "v22", "v23", "v24", "v25", "v26", "v27", "v28", "v29", "v30", "v31", "v32", "v33", "v34", "v35", "v36", "v37", "v38", "v39", "v40", "v41", "v42", "v43", "v44", "v45", "v46", "v47", "v48", "v49", "v50", "v51", "v52", "v53", "v54", "v55", "v56", "v57", "v58", "v59", "v60", "v61", "v62", "v63", "vcc", "scc", "memory");
}
template <> __device__ __forceinline__ void qk_step<6>(const QKW& W, const char* ag, const char* vs, const char* v2, const char* od, unsigned pb, unsigned sn, unsigned& cap, unsigned& sp,
    unsigned long long& tc, unsigned long long* ts, unsigned& d0, unsigned& d1) {
  unsigned st, bg;
  asm volatile("v_mbcnt_lo_u32_b32 v60, -1, 0\n\t"
    "v_mbcnt_hi_u32_b32 v60, -1, v60\n\t"
    "v_lshlrev_b32 v6, 4, v60\n\t"
    "s_mov_b32 %[sp], 0\n\t"
    "s_setprio 3\n\t"
    "global_load_dwordx4 v[24:27], v6, %[ag] sc1\n\t"
    "global_load_dwordx4 v[28:31], v6, %[ag] offset:-4096 sc1\n\t"
    "s_memtime %[tc]\n\t"
    "v_lshrrev_b32 v56, 4, v60\n\t"
    "v_and_b32 v57, 15, v60\n\t"
    "v_mov_b32 v20, 0\n\t"
    "v_mov_b32 v21, 0\n\t"
    "v_mov_b32 v22, 0\n\t"
    "v_mov_b32 v23, 0\n\t"
    "v_mov_b32 v11, %[sn]\n\t"
    "v_mov_b32 v10, -1\n\t"
    "v_and_b32 v61, 2, v60\n\t"
    "v_mul_u32_u24 v61, 0x5555, v61\n\t"
    "v_or_b32 v8, %[c44], v61\n\t"
    "v_lshlrev_b32 v61, 2, v56\n\t"
    "v_lshl_or_b32 v9, v57, 4, v61\n\t"
    "s_setprio 0\n\t"
    "s_setprio 3\n\t"
    "3:\n\t"
    "v_mov_b32 v12, 0\n\t"
    "v_mov_b32 v13, 0\n\t"
    "v_mov_b32 v14, 0\n\t"
    "v_mov_b32 v15, 0\n\t"
    "v_mov_b32 v16, 0\n\t"
    "v_mov_b32 v17, 0\n\t"
    "v_mov_b32 v18, 0\n\t"
    "v_mov_b32 v19, 0\n\t"
    "s_waitcnt vmcnt(0) lgkmcnt(0)\n\t"
    "ds_write_b128 v6, v[24:27] offset:14336\n\t"
    "ds_write_b128 v6, v[28:31] offset:10240\n\t"
    "v_smfmac_f32_16x16x64_bf16 v[12:15], v[24:27], v[96:103], v8\n\t"
    "v_max3_u32 v60, v24, v25, v26\n\t"
    "v_max3_u32 v61, v27, v28, v29\n\t"
    "v_smfmac_f32_16x16x64_bf16 v[16:19], v[24:27], v[104:111], v8\n\t"
    "v_max3_u32 v60, v60, v30, v31\n\t"
    "v_max_u32 v60, v60, v61\n\t"
    "v_cmp_eq_u32 vcc, -1, v60\n\t"
    "s_cbranch_vccnz 1f\n\t"
    "2:\n\t"
    "s_waitcnt lgkmcnt(0)\n\t"
    "s_barrier\n\t"
    "ds_read_b128 v[32:35], v6 offset:12288\n\t"
    "ds_read_b128 v[36:39], v6 offset:13312\n\t"
    "ds_read_b128 v[40:43], v6 offset:15360\n\t"
    "ds_write_b32 v9, v10 offset:18944\n\t"
    "s_waitcnt lgkmcnt(3)\n\t"
    "v_smfmac_f32_16x16x64_bf16 v[12:15], v[32:35], v[64:71], v8\n\t"
    "v_smfmac_f32_16x16x64_bf16 v[16:19], v[32:35], v[72:79], v8\n\t"
    "s_waitcnt lgkmcnt(2)\n\t"
    "v_smfmac_f32_16x16x64_bf16 v[12:15], v[36:39], v[80:87], v8\n\t"
    "v_smfmac_f32_16x16x64_bf16 v[16:19], v[36:39], v[88:95], v8\n\t"
    "s_waitcnt lgkmcnt(1)\n\t"
    "v_smfmac_f32_16x16x64_bf16 v[12:15], v[40:43], v[112:119], v8\n\t"
    "v_smfmac_f32_16x16x64_bf16 v[16:19], v[40:43], v[120:127], v8\n\t"
    "s_setprio 0\n\t"
    "s_sleep 1\n\t"
    "s_nop 6\n\t"
    "v_add_f32 v12, v12, v14\n\t"
    "v_add_f32 v13, v13, v15\n\t"
    "v_add_f32 v14, v16, v18\n\t"
    "v_add_f32 v15, v17, v19\n\t"
    "ds_write_b128 v6, v[12:15] offset:26624\n\t"
    "ds_write_b32 v9, v11 offset:29184\n\t"
    "s_setprio 0\n\t"
    "ds_read_b128 v[44:47], v6 offset:0\n\t"
    "ds_read_b128 v[48:51], v6 offset:1024\n\t"
    "ds_read_b128 v[52:55], v6 offset:2048\n\t"
    "ds_read_b128 v[56:59], v6 offset:3072\n\t"
    "ds_read_b128 v[60:63], v6 offset:4096\n\t"
    "s_sleep 1\n\t"
    "v_smfmac_f32_16x16x64_bf16 v[20:23], v[28:31], v[200:207], v8\n\t"
    "ds_read_b128 v[28:31], v6 offset:5120\n\t"
    "v_smfmac_f32_16x16x64_bf16 v[20:23], v[24:27], v[232:239], v8\n\t"
    "ds_read_b128 v[24:27], v6 offset:6144\n\t"
    "v_smfmac_f32_16x16x64_bf16 v[20:23], v[32:35], v[224:231], v8\n\t"
    "ds_read_b128 v[32:35], v6 offset:7168\n\t"
    "v_smfmac_f32_16x16x64_bf16 v[20:23], v[36:39], v[240:247], v8\n\t"
    "ds_read_b128 v[36:39], v6 offset:8192\n\t"
    "v_smfmac_f32_16x16x64_bf16 v[20:23], v[40:43], v[248:255], v8\n\t"
    "ds_read_b128 v[40:43], v6 offset:9216\n\t"
    "s_waitcnt lgkmcnt(9)\n\t"
    "v_smfmac_f32_16x16x64_bf16 v[20:23], v[44:47], v[128:135], v8\n\t"
    "ds_read_b128 v[44:47], v6 offset:11264\n\t"
    "s_barrier\n\t"
    "s_setprio 0\n\t"
    "s_waitcnt lgkmcnt(9)\n\t"
    "v_smfmac_f32_16x16x64_bf16 v[20:23], v[48:51], v[144:151], v8\n\t"
    "s_waitcnt lgkmcnt(8)\n\t"
    "v_smfmac_f32_16x16x64_bf16 v[20:23], v[52:55], v[136:143], v8\n\t"
    "s_waitcnt lgkmcnt(7)\n\t"
    "v_smfmac_f32_16x16x64_bf16 v[20:23], v[56:59], v[152:159], v8\n\t"
    "s_waitcnt lgkmcnt(6)\n\t"
    "v_smfmac_f32_16x16x64_bf16 v[20:23], v[60:63], v[160:167], v8\n\t"
    "s_waitcnt lgkmcnt(5)\n\t"
    "v_smfmac_f32_16x16x64_bf16 v[20:23], v[28:31], v[176:183], v8\n\t"
    "s_waitcnt lgkmcnt(4)\n\t"
    "v_smfmac_f32_16x16x64_bf16 v[20:23], v[24:27], v[168:175], v8\n\t"
    "s_waitcnt lgkmcnt(3)\n\t"
    "v_smfmac_f32_16x16x64_bf16 v[20:23], v[32:35], v[184:191], v8\n\t"
    "s_waitcnt lgkmcnt(2)\n\t"
    "v_smfmac_f32_16x16x64_bf16 v[20:23], v[36:39], v[192:199], v8\n\t"
    "s_waitcnt lgkmcnt(1)\n\t"
    "v_smfmac_f32_16x16x64_bf16 v[20:23], v[40:43], v[208:215], v8\n\t"
    "s_waitcnt lgkmcnt(0)\n\t"
    "v_smfmac_f32_16x16x64_bf16 v[20:23], v[44:47], v[216:223], v8\n\t"
    "s_nop 7\n\t"
    "v_add_f32 v20, v20, v22\n\t"
    "v_add_f32 v21, v21, v23\n\t"
    "v_cvt_pk_bf16_f32 v20, v20, v21\n\t"
    "v_min_u32 v20, -2, v20\n\t"
    "ds_write_b32 v9, v20 offset:18944\n\t"
    "s_waitcnt lgkmcnt(0)\n\t"
    "s_setprio 0\n\t"
    "s_setprio 0\n\t"
    "s_waitcnt lgkmcnt(0)\n\t"
    "s_branch 9f\n\t"
    "1:\n\t"
    "s_add_u32 %[sp], %[sp], 1\n\t"
    "s_cmp_ge_u32 %[sp], %[cap]\n\t"
    "s_cbranch_scc1 7f\n\t"
    "global_load_dwordx4 v[24:27], v6, %[ag] sc1\n\t"
    "global_load_dwordx4 v[28:31], v6, %[ag] offset:-4096 sc1\n\t"
    "s_waitcnt lgkmcnt(0)\n\t"
    "s_memtime %[tc]\n\t"
    "s_nop 7\n\t"
    "s_branch 3b\n\t"
    "7:\n\t"
    "s_mov_b32 %[cap], 0\n\t"
    "s_branch 2b\n\t"
    "9:"
    : [tc] "=&s"(tc), [sp] "=&s"(sp), [cap] "+s"(cap), [st] "=&s"(st)
    : [ag] "s"(ag), [c44] "s"(0x4444u), [lsc] "s"(LSC), [sn] "s"(sn), "{v[64:95]}"(W.w0), "{v[96:127]}"(W.w1), "{v[128:159]}"(W.w2), "{v[160:191]}"(W.w3), "{v[192:223]}"(W.w4), "{v[224:255]}"(W.w5)
    : "v6", "v7", "v8", "v9", "v10", "v11", "v12", "v13", "v14", "v15", "v16", "v17", "v18", "v19", "v20", "v21", "v22", "v23", "v24", "v25", "v26", "v27", "v28", "v29", "v30", "v31", "v32", "v33", "v34", "v35", "v36", "v37", "v38", "v39", "v40", "v41", "v42", "v43", "v44", "v45", "v46", "v47", "v48", "v49", "v50", "v51", "v52", "v53", "v54", "v55", "v56", "v57", "v58", "v59", "v60", "v61", "v62", "v63", "vcc", "scc", "memory");
}
template <> __device__ __forceinline__ void qk_step<7>(const QKW& W, const char* ag, const char* vs, const char* v2, const char* od, unsigned pb, unsigned sn, unsigned& cap, unsigned& sp,
    unsigned long long& tc, unsigned long long* ts, unsigned& d0, unsigned& d1) {
  unsigned st, bg;
  asm volatile("v_mbcnt_lo_u32_b32 v60, -1, 0\n\t"
    "v_mbcnt_hi_u32_b32 v60, -1, v60\n\t"
    "v_lshlrev_b32 v6, 4, v60\n\t"
    "s_mov_b32 %[sp], 0\n\t"
    "s_setprio 3\n\t"
    "global_load_dwordx4 v[24:27], v6, %[ag] sc1\n\t"
    "global_load_dwordx4 v[28:31], v6, %[ag] offset:-4096 sc1\n\t"
    "s_memtime %[tc]\n\t"
    "v_lshrrev_b32 v56, 4, v60\n\t"
    "v_and_b32 v57, 15, v60\n\t"
    "v_mov_b32 v20, 0\n\t"
    "v_mov_b32 v21, 0\n\t"
    "v_mov_b32 v22, 0\n\t"
    "v_mov_b32 v23, 0\n\t"
    "v_mov_b32 v11, %[sn]\n\t"
    "v_mov_b32 v10, -1\n\t"
    "v_and_b32 v61, 2, v60\n\t"
    "v_mul_u32_u24 v61, 0x5555, v61\n\t"
    "v_or_b32 v8, %[c44], v61\n\t"
    "v_lshlrev_b32 v61, 2, v56\n\t"
    "v_lshl_or_b32 v9, v57, 4, v61\n\t"
    "s_setprio 0\n\t"
    "s_setprio 3\n\t"
    "3:\n\t"
    "v_mov_b32 v12, 0\n\t"
    "v_mov_b32 v13, 0\n\t"
    "v_mov_b32 v14, 0\n\t"
    "v_mov_b32 v15, 0\n\t"
    "v_mov_b32 v16, 0\n\t"
    "v_mov_b32 v17, 0\n\t"
    "v_mov_b32 v18, 0\n\t"
    "v_mov_b32 v19, 0\n\t"
    "s_waitcnt vmcnt(0) lgkmcnt(0)\n\t"
    "ds_write_b128 v6, v[24:27] offset:15360\n\t"
    "ds_write_b128 v6, v[28:31] offset:11264\n\t"
    "v_smfmac_f32_16x16x64_bf16 v[12:15], v[24:27], v[112:119], v8\n\t"
    "v_max3_u32 v60, v24, v25, v26\n\t"
    "v_max3_u32 v61, v27, v28, v29\n\t"
    "v_smfmac_f32_16x16x64_bf16 v[16:19], v[24:27], v[120:127], v8\n\t"
    "v_max3_u32 v60, v60, v30, v31\n\t"
    "v_max_u32 v60, v60, v61\n\t"
    "v_cmp_eq_u32 vcc, -1, v60\n\t"
    "s_cbranch_vccnz 1f\n\t"
    "2:\n\t"
    "s_waitcnt lgkmcnt(0)\n\t"
    "s_barrier\n\t"
    "ds_read_b128 v[32:35], v6 offset:12288\n\t"
    "ds_read_b128 v[36:39], v6 offset:13312\n\t"
    "ds_read_b128 v[40:43], v6 offset:14336\n\t"
    "ds_write_b32 v9, v10 offset:19200\n\t"
    "s_waitcnt lgkmcnt(3)\n\t"
    "v_smfmac_f32_16x16x64_bf16 v[12:15], v[32:35], v[64:71], v8\n\t"
    "v_smfmac_f32_16x16x64_bf16 v[16:19], v[32:35], v[72:79], v8\n\t"
    "s_waitcnt lgkmcnt(2)\n\t"
    "v_smfmac_f32_16x16x64_bf16 v[12:15], v[36:39], v[80:87], v8\n\t"
    "v_smfmac_f32_16x16x64_bf16 v[16:19], v[36:39], v[88:95], v8\n\t"
    "s_waitcnt lgkmcnt(1)\n\t"
    "v_smfmac_f32_16x16x64_bf16 v[12:15], v[40:43], v[96:103], v8\n\t"
    "v_smfmac_f32_16x16x64_bf16 v[16:19], v[40:43], v[104:111], v8\n\t"
    "s_setprio 0\n\t"
    "s_sleep 1\n\t"
    "s_nop 6\n\t"
    "v_add_f32 v12, v12, v14\n\t"
    "v_add_f32 v13, v13, v15\n\t"
    "v_add_f32 v14, v16, v18\n\t"
    "v_add_f32 v15, v17, v19\n\t"
    "ds_write_b128 v6, v[12:15] offset:27648\n\t"
    "ds_write_b32 v9, v11 offset:29440\n\t"
    "s_setprio 0\n\t"
    "v_lshrrev_b32 v10, 24, v11\n\t"
    "v_add_u32 v10, -1, v10\n\t"
    "s_mov_b64 exec, 1\n\t"
    "global_store_dword v6, v10, %[vs] sc1\n\t"
    "s_mov_b64 exec, -1\n\t"
    "ds_read_b128 v[44:47], v6 offset:0\n\t"
    "ds_read_b128 v[48:51], v6 offset:1024\n\t"
    "ds_read_b128 v[52:55], v6 offset:2048\n\t"
    "ds_read_b128 v[56:59], v6 offset:3072\n\t"
    "ds_read_b128 v[60:63], v6 offset:4096\n\t"
    "s_sleep 1\n\t"
    "v_smfmac_f32_16x16x64_bf16 v[20:23], v[28:31], v[216:223], v8\n\t"
    "ds_read_b128 v[28:31], v6 offset:5120\n\t"
    "v_smfmac_f32_16x16x64_bf16 v[20:23], v[24:27], v[248:255], v8\n\t"
    "ds_read_b128 v[24:27], v6 offset:6144\n\t"
    "v_smfmac_f32_16x16x64_bf16 v[20:23], v[32:35], v[224:231], v8\n\t"
    "ds_read_b128 v[32:35], v6 offset:7168\n\t"
    "v_smfmac_f32_16x16x64_bf16 v[20:23], v[36:39], v[240:247], v8\n\t"
    "ds_read_b128 v[36:39], v6 offset:8192\n\t"
    "v_smfmac_f32_16x16x64_bf16 v[20:23], v[40:43], v[232:239], v8\n\t"
    "ds_read_b128 v[40:43], v6 offset:9216\n\t"
    "s_waitcnt lgkmcnt(9)\n\t"
    "v_smfmac_f32_16x16x64_bf16 v[20:23], v[44:47], v[128:135], v8\n\t"
    "ds_read_b128 v[44:47], v6 offset:10240\n\t"
    "s_barrier\n\t"
    "s_setprio 0\n\t"
    "s_waitcnt lgkmcnt(9)\n\t"
    "v_smfmac_f32_16x16x64_bf16 v[20:23], v[48:51], v[144:151], v8\n\t"
    "s_waitcnt lgkmcnt(8)\n\t"
    "v_smfmac_f32_16x16x64_bf16 v[20:23], v[52:55], v[136:143], v8\n\t"
    "s_waitcnt lgkmcnt(7)\n\t"
    "v_smfmac_f32_16x16x64_bf16 v[20:23], v[56:59], v[152:159], v8\n\t"
    "s_waitcnt lgkmcnt(6)\n\t"
    "v_smfmac_f32_16x16x64_bf16 v[20:23], v[60:63], v[160:167], v8\n\t"
    "s_waitcnt lgkmcnt(5)\n\t"
    "v_smfmac_f32_16x16x64_bf16 v[20:23], v[28:31], v[176:183], v8\n\t"
    "s_waitcnt lgkmcnt(4)\n\t"
    "v_smfmac_f32_16x16x64_bf16 v[20:23], v[24:27], v[168:175], v8\n\t"
    "s_waitcnt lgkmcnt(3)\n\t"
    "v_smfmac_f32_16x16x64_bf16 v[20:23], v[32:35], v[184:191], v8\n\t"
    "s_waitcnt lgkmcnt(2)\n\t"
    "v_smfmac_f32_16x16x64_bf16 v[20:23], v[36:39], v[192:199], v8\n\t"
    "s_waitcnt lgkmcnt(1)\n\t"
    "v_smfmac_f32_16x16x64_bf16 v[20:23], v[40:43], v[208:215], v8\n\t"
    "s_waitcnt lgkmcnt(0)\n\t"
    "v_smfmac_f32_16x16x64_bf16 v[20:23], v[44:47], v[200:207], v8\n\t"
    "s_nop 7\n\t"
    "v_add_f32 v20, v20, v22\n\t"
    "v_add_f32 v21, v21, v23\n\t"
    "v_cvt_pk_bf16_f32 v20, v20, v21\n\t"
    "v_min_u32 v20, -2, v20\n\t"
    "ds_write_b32 v9, v20 offset:19200\n\t"
    "s_waitcnt lgkmcnt(0)\n\t"
    "s_setprio 0\n\t"
    "s_waitcnt lgkmcnt(0)\n\t"
    "s_branch 9f\n\t"
    "1:\n\t"
    "s_add_u32 %[sp], %[sp], 1\n\t"
    "s_cmp_ge_u32 %[sp], %[cap]\n\t"
    "s_cbranch_scc1 7f\n\t"
    "global_load_dwordx4 v[24:27], v6, %[ag] sc1\n\t"
    "global_load_dwordx4 v[28:31], v6, %[ag] offset:-4096 sc1\n\t"
    "s_waitcnt lgkmcnt(0)\n\t"
    "s_memtime %[tc]\n\t"
    "s_nop 7\n\t"
    "s_branch 3b\n\t"
    "7:\n\t"
    "s_mov_b32 %[cap], 0\n\t"
    "s_branch 2b\n\t"
    "9:"
    : [tc] "=&s"(tc), [sp] "=&s"(sp), [cap] "+s"(cap), [st] "=&s"(st)
    : [ag] "s"(ag), [c44] "s"(0x4444u), [lsc] "s"(LSC), [sn] "s"(sn), [vs] "s"(vs), "{v[64:95]}"(W.w0), "{v[96:127]}"(W.w1), "{v[128:159]}"(W.w2), "{v[160:191]}"(W.w3), "{v[192:223]}"(W.w4), "{v[224:255]}"(W.w5)
    : "v6", "v7", "v8", "v9", "v10", "v11", "v12", "v13", "v14", "v15", "v16", "v17", "v18", "v19", "v20", "v21", "v22", "v23", "v24", "v25", "v26", "v27", "v28", "v29", "v30", "v31", "v32", "v33", "v34", "v35", "v36", "v37", "v38", "v39", "v40", "v41", "v42", "v43", "v44", "v45", "v46", "v47", "v48", "v49", "v50", "v51", "v52", "v53", "v54", "v55", "v56", "v57", "v58", "v59", "v60", "v61", "v62", "v63", "vcc", "scc", "memory");
}
template <int W_> __device__ __forceinline__ void seg_attn(const QKW& W, unsigned pb, const char* od, unsigned long long* ts) { }
template <> __device__ __forceinline__ void seg_attn<0>(const QKW& W, unsigned pb, const char* od, unsigned long long* ts) {
  asm volatile("v_mbcnt_lo_u32_b32 v32, -1, 0\n\t"
    "v_mbcnt_hi_u32_b32 v32, -1, v32\n\t"
    "v_lshlrev_b32 v35, 4, v32\n\t"
    "ds_read_b128 v[24:27], v35 offset:20480\n\t"
    "ds_read_b128 v[28:31], v35 offset:21504\n\t"
    "v_lshrrev_b32 v33, 4, v32\n\t"
    "v_and_b32 v34, 15, v32\n\t"
    "v_xor_b32 v4, v33, v34\n\t"
    "v_lshl_or_b32 v4, v4, 4, %[pb]\n\t"
    "v_lshl_or_b32 v4, v34, 8, v4\n\t"
    "ds_read_b128 v[8:11], v4 offset:49152\n\t"
    "v_xor_b32 v5, 0x40, v4\n\t"
    "ds_read_b128 v[12:15], v5 offset:49152\n\t"
    "v_xor_b32 v6, 0x80, v4\n\t"
    "ds_read_b128 v[16:19], v6 offset:49152\n\t"
    "v_xor_b32 v7, 0xc0, v4\n\t"
    "ds_read_b128 v[20:23], v7 offset:49152\n\t"
    "ds_read_b128 v[40:43], v4 offset:32768\n\t"
    "ds_read_b128 v[44:47], v5 offset:32768\n\t"
    "ds_read_b128 v[48:51], v6 offset:32768\n\t"
    "ds_read_b128 v[52:55], v7 offset:32768\n\t"
    "v_lshlrev_b32 v35, 2, v33\n\t"
    "v_sub_u32 v35, v34, v35\n\t"
    "v_ashrrev_i32 v60, 31, v35\n\t"
    "v_and_b32 v60, %[neg], v60\n\t"
    "v_add_u32 v61, -1, v35\n\t"
    "v_ashrrev_i32 v61, 31, v61\n\t"
    "v_and_b32 v61, %[neg], v61\n\t"
    "v_add_u32 v62, -2, v35\n\t"
    "v_ashrrev_i32 v62, 31, v62\n\t"
    "v_and_b32 v62, %[neg], v62\n\t"
    "v_add_u32 v63, -3, v35\n\t"
    "v_ashrrev_i32 v63, 31, v63\n\t"
    "v_and_b32 v63, %[neg], v63\n\t"
    "s_waitcnt lgkmcnt(6)\n\t"
    "v_mfma_f32_16x16x32_bf16 v[32:35], v[24:27], v[8:11], 0\n\t"
    "v_mfma_f32_16x16x32_bf16 v[36:39], v[24:27], v[12:15], 0\n\t"
    "s_waitcnt lgkmcnt(4)\n\t"
    "v_mfma_f32_16x16x32_bf16 v[32:35], v[28:31], v[16:19], v[32:35]\n\t"
    "v_mfma_f32_16x16x32_bf16 v[36:39], v[28:31], v[20:23], v[36:39]\n\t"
    "s_waitcnt lgkmcnt(3)\n\t"
    "v_mfma_f32_16x16x32_bf16 v[60:63], v[40:43], v[8:11], v[60:63]\n\t"
    "s_waitcnt lgkmcnt(2)\n\t"
    "v_mfma_f32_16x16x32_bf16 v[60:63], v[44:47], v[12:15], v[60:63]\n\t"
    "v_mbcnt_lo_u32_b32 v7, -1, 0\n\t"
    "v_mbcnt_hi_u32_b32 v7, -1, v7\n\t"
    "s_waitcnt lgkmcnt(1)\n\t"
    "v_mfma_f32_16x16x32_bf16 v[60:63], v[48:51], v[16:19], v[60:63]\n\t"
    "v_and_or_b32 v4, v7, 3, 0\n\t"
    "v_lshrrev_b32 v6, 2, v7\n\t"
    "s_waitcnt lgkmcnt(0)\n\t"
    "v_mfma_f32_16x16x32_bf16 v[60:63], v[52:55], v[20:23], v[60:63]\n\t"
    "v_xor_b32 v4, v4, v6\n\t"
    "v_lshl_or_b32 v4, v4, 4, %[pb]\n\t"
    "v_lshl_or_b32 v4, v6, 8, v4\n\t"
    "v_xor_b32 v5, 64, v4\n\t"
    "ds_read_b64_tr_b16 v[48:49], v4 offset:40960\n\t"
    "ds_read_b64_tr_b16 v[52:53], v4 offset:40968\n\t"
    "v_add_f32 v32, v32, v38\n\t"
    "v_add_f32 v33, v33, v39\n\t"
    "v_and_b32 v6, 12, v7\n\t"
    "v_and_b32 v4, 3, v7\n\t"
    "v_lshl_add_u32 v4, v6, 1, v4\n\t"
    "v_lshrrev_b32 v6, 4, v7\n\t"
    "v_lshl_add_u32 v4, v4, 2, v6\n\t"
    "v_lshlrev_b32 v4, 2, v4\n\t"
    "v_add_u32 v4, 18432, v4\n\t"
    "ds_read_b64_tr_b16 v[40:41], v5 offset:40960\n\t"
    "ds_read_b64_tr_b16 v[44:45], v5 offset:40968\n\t"
    "ds_read2_b32 v[36:37], v4 offset0:0 offset1:16\n\t"
    "ds_read2_b32 v[38:39], v4 offset0:128 offset1:144\n\t"
    "v_and_b32 v4, 7, v7\n\t"
    "v_xor_b32 v6, v6, v4\n\t"
    "v_max3_f32 v34, v32, v33, %[lo]\n\t"
    "v_max3_f32 v34, v34, v60, v61\n\t"
    "v_max3_f32 v34, v34, v62, v63\n\t"
    "v_mov_b32 v35, v34\n\t"
    "v_lshlrev_b32 v6, 4, v6\n\t"
    "v_and_b32 v4, 15, v7\n\t"
    "v_permlane16_swap_b32 v34, v35\n\t"
    "v_max_f32 v34, v34, v35\n\t"
    "v_mov_b32 v35, v34\n\t"
    "v_lshl_or_b32 v6, v4, 7, v6\n\t"
    "v_xor_b32 v4, 64, v6\n\t"
    "v_permlane32_swap_b32 v34, v35\n\t"
    "v_max_f32 v34, v34, v35\n\t"
    "v_mul_f32 v34, %[nl], v34\n\t"
    "v_fma_f32 v32, v32, %[lsc], v34\n\t"
    "v_fma_f32 v33, v33, %[lsc], v34\n\t"
    "v_fma_f32 v60, v60, %[lsc], v34\n\t"
    "v_fma_f32 v61, v61, %[lsc], v34\n\t"
    "v_fma_f32 v62, v62, %[lsc], v34\n\t"
    "v_fma_f32 v63, v63, %[lsc], v34\n\t"
    "v_exp_f32 v60, v60\n\t"
    "v_exp_f32 v61, v61\n\t"
    "v_exp_f32 v62, v62\n\t"
    "v_exp_f32 v63, v63\n\t"
    "v_exp_f32 v32, v32\n\t"
    "v_exp_f32 v33, v33\n\t"
    "v_cvt_pk_bf16_f32 v24, v60, v61\n\t"
    "v_cvt_pk_bf16_f32 v25, v62, v63\n\t"
    "v_cvt_pk_bf16_f32 v28, v32, v33\n\t"
    "s_waitcnt lgkmcnt(0)\n\t"
    "v_mfma_f32_16x16x16_bf16 v[8:11], v[48:49], v[24:25], 0\n\t"
    "v_mov_b32 v56, %[one]\n\t"
    "v_mov_b32 v57, %[one]\n\t"
    "v_mfma_f32_16x16x16_bf16 v[12:15], v[52:53], v[24:25], 0\n\t"
    "v_mov_b32 v58, %[one]\n\t"
    "v_mov_b32 v59, %[one]\n\t"
    "v_mfma_f32_16x16x16_bf16 v[16:19], v[40:41], v[24:25], 0\n\t"
    "v_mov_b32 v29, 0\n\t"
    "v_mov_b32 v30, 0\n\t"
    "v_mfma_f32_16x16x16_bf16 v[60:63], v[56:57], v[24:25], 0\n\t"
    "v_mov_b32 v31, v28\n\t"
    "v_mfma_f32_16x16x16_bf16 v[20:23], v[44:45], v[24:25], 0\n\t"
    "v_mfma_f32_16x16x16_bf16 v[8:11], v[36:37], v[28:29], v[8:11]\n\t"
    "v_mfma_f32_16x16x16_bf16 v[12:15], v[36:37], v[30:31], v[12:15]\n\t"
    "s_nop 1\n\t"
    "v_mfma_f32_16x16x16_bf16 v[16:19], v[38:39], v[28:29], v[16:19]\n\t"
    "v_mfma_f32_16x16x16_bf16 v[60:63], v[56:57], v[28:29], v[60:63]\n\t"
    "s_nop 1\n\t"
    "v_mfma_f32_16x16x16_bf16 v[20:23], v[38:39], v[30:31], v[20:23]\n\t"
    "s_nop 4\n\t"
    "v_rcp_f32 v60, v60\n\t"
    "s_nop 0\n\t"
    "v_pk_mul_f32 v[8:9], v[8:9], v[60:61] op_sel_hi:[1,0]\n\t"
    "v_pk_mul_f32 v[10:11], v[10:11], v[60:61] op_sel_hi:[1,0]\n\t"
    "v_pk_mul_f32 v[12:13], v[12:13], v[60:61] op_sel_hi:[1,0]\n\t"
    "v_pk_mul_f32 v[14:15], v[14:15], v[60:61] op_sel_hi:[1,0]\n\t"
    "v_cvt_pk_bf16_f32 v24, v8, v9\n\t"
    "v_cvt_pk_bf16_f32 v25, v10, v11\n\t"
    "v_cvt_pk_bf16_f32 v26, v12, v13\n\t"
    "v_cvt_pk_bf16_f32 v27, v14, v15\n\t"
    "global_store_dwordx4 v6, v[24:27], %[od] offset:0 sc1\n\t"
    "v_pk_mul_f32 v[16:17], v[16:17], v[60:61] op_sel_hi:[1,0]\n\t"
    "v_pk_mul_f32 v[18:19], v[18:19], v[60:61] op_sel_hi:[1,0]\n\t"
    "v_pk_mul_f32 v[20:21], v[20:21], v[60:61] op_sel_hi:[1,0]\n\t"
    "v_pk_mul_f32 v[22:23], v[22:23], v[60:61] op_sel_hi:[1,0]\n\t"
    "v_cvt_pk_bf16_f32 v28, v16, v17\n\t"
    "v_cvt_pk_bf16_f32 v29, v18, v19\n\t"
    "v_cvt_pk_bf16_f32 v30, v20, v21\n\t"
    "v_cvt_pk_bf16_f32 v31, v22, v23\n\t"
    "global_store_dwordx4 v4, v[28:31], %[od] offset:0 sc1\n\t"
    "s_waitcnt lgkmcnt(0)\n\t"
    "s_nop 0"
    :
    : [pb] "s"(pb), [od] "s"(od), [neg] "s"(-3.0e38f), [lo] "s"(-1.0e38f), [one] "s"(0x3F803F80u), [lsc] "s"(LSC), [nl] "s"(-LSC), "{v[64:95]}"(W.w0), "{v[96:127]}"(W.w1), "{v[128:159]}"(W.w2), "{v[160:191]}"(W.w3), "{v[192:223]}"(W.w4), "{v[224:255]}"(W.w5)
    : "v4", "v5", "v6", "v7", "v8", "v9", "v10", "v11", "v12", "v13", "v14", "v15", "v16", "v17", "v18", "v19", "v20", "v21", "v22", "v23", "v24", "v25", "v26", "v27", "v28", "v29", "v30", "v31", "v32", "v33", "v34", "v35", "v36", "v37", "v38", "v39", "v40", "v41", "v42", "v43", "v44", "v45", "v46", "v47", "v48", "v49", "v50", "v51", "v52", "v53", "v54", "v55", "v56", "v57", "v58", "v59", "v60", "v61", "v62", "v63", "memory");
}
template <> __device__ __forceinline__ void seg_attn<1>(const QKW& W, unsigned pb, const char* od, unsigned long long* ts) {
  asm volatile("v_mbcnt_lo_u32_b32 v32, -1, 0\n\t"
    "v_mbcnt_hi_u32_b32 v32, -1, v32\n\t"
    "v_lshlrev_b32 v35, 4, v32\n\t"
    "ds_read_b128 v[24:27], v35 offset:20480\n\t"
    "ds_read_b128 v[28:31], v35 offset:21504\n\t"
    "v_lshrrev_b32 v33, 4, v32\n\t"
    "v_and_b32 v34, 15, v32\n\t"
    "v_xor_b32 v4, v33, v34\n\t"
    "v_lshl_or_b32 v4, v4, 4, %[pb]\n\t"
    "v_lshl_or_b32 v4, v34, 8, v4\n\t"
    "ds_read_b128 v[8:11], v4 offset:53248\n\t"
    "v_xor_b32 v5, 0x40, v4\n\t"
    "ds_read_b128 v[12:15], v5 offset:53248\n\t"
    "v_xor_b32 v6, 0x80, v4\n\t"
    "ds_read_b128 v[16:19], v6 offset:53248\n\t"
    "v_xor_b32 v7, 0xc0, v4\n\t"
    "ds_read_b128 v[20:23], v7 offset:53248\n\t"
    "ds_read_b128 v[40:43], v4 offset:32768\n\t"
    "ds_read_b128 v[44:47], v5 offset:32768\n\t"
    "ds_read_b128 v[48:51], v6 offset:32768\n\t"
    "ds_read_b128 v[52:55], v7 offset:32768\n\t"
    "v_lshlrev_b32 v35, 2, v33\n\t"
    "v_sub_u32 v35, v34, v35\n\t"
    "v_ashrrev_i32 v60, 31, v35\n\t"
    "v_and_b32 v60, %[neg], v60\n\t"
    "v_add_u32 v61, -1, v35\n\t"
    "v_ashrrev_i32 v61, 31, v61\n\t"
    "v_and_b32 v61, %[neg], v61\n\t"
    "v_add_u32 v62, -2, v35\n\t"
    "v_ashrrev_i32 v62, 31, v62\n\t"
    "v_and_b32 v62, %[neg], v62\n\t"
    "v_add_u32 v63, -3, v35\n\t"
    "v_ashrrev_i32 v63, 31, v63\n\t"
    "v_and_b32 v63, %[neg], v63\n\t"
    "s_waitcnt lgkmcnt(6)\n\t"
    "v_mfma_f32_16x16x32_bf16 v[32:35], v[24:27], v[8:11], 0\n\t"
    "v_mfma_f32_16x16x32_bf16 v[36:39], v[24:27], v[12:15], 0\n\t"
    "s_waitcnt lgkmcnt(4)\n\t"
    "v_mfma_f32_16x16x32_bf16 v[32:35], v[28:31], v[16:19], v[32:35]\n\t"
    "v_mfma_f32_16x16x32_bf16 v[36:39], v[28:31], v[20:23], v[36:39]\n\t"
    "ds_read_b128 v[24:27], v4 offset:36864\n\t"
    "ds_read_b128 v[28:31], v5 offset:36864\n\t"
    "s_waitcnt lgkmcnt(5)\n\t"
    "v_mfma_f32_16x16x32_bf16 v[56:59], v[40:43], v[8:11], 0\n\t"
    "s_waitcnt lgkmcnt(4)\n\t"
    "v_mfma_f32_16x16x32_bf16 v[56:59], v[44:47], v[12:15], v[56:59]\n\t"
    "ds_read_b128 v[40:43], v6 offset:36864\n\t"
    "ds_read_b128 v[44:47], v7 offset:36864\n\t"
    "v_mbcnt_lo_u32_b32 v7, -1, 0\n\t"
    "v_mbcnt_hi_u32_b32 v7, -1, v7\n\t"
    "s_waitcnt lgkmcnt(5)\n\t"
    "v_mfma_f32_16x16x32_bf16 v[56:59], v[48:51], v[16:19], v[56:59]\n\t"
    "v_and_or_b32 v4, v7, 3, 0\n\t"
    "v_lshrrev_b32 v6, 2, v7\n\t"
    "s_waitcnt lgkmcnt(4)\n\t"
    "v_mfma_f32_16x16x32_bf16 v[56:59], v[52:55], v[20:23], v[56:59]\n\t"
    "v_xor_b32 v4, v4, v6\n\t"
    "v_lshl_or_b32 v4, v4, 4, %[pb]\n\t"
    "v_lshl_or_b32 v4, v6, 8, v4\n\t"
    "v_xor_b32 v5, 64, v4\n\t"
    "ds_read_b64_tr_b16 v[48:49], v4 offset:40960\n\t"
    "ds_read_b64_tr_b16 v[50:51], v4 offset:45056\n\t"
    "ds_read_b64_tr_b16 v[52:53], v4 offset:40968\n\t"
    "ds_read_b64_tr_b16 v[54:55], v4 offset:45064\n\t"
    "v_add_f32 v32, v32, v38\n\t"
    "v_add_f32 v33, v33, v39\n\t"
    "s_waitcnt lgkmcnt(7)\n\t"
    "v_mfma_f32_16x16x32_bf16 v[60:63], v[24:27], v[8:11], v[60:63]\n\t"
    "v_and_b32 v6, 12, v7\n\t"
    "v_and_b32 v4, 3, v7\n\t"
    "s_waitcnt lgkmcnt(6)\n\t"
    "v_mfma_f32_16x16x32_bf16 v[60:63], v[28:31], v[12:15], v[60:63]\n\t"
    "v_lshl_add_u32 v4, v6, 1, v4\n\t"
    "v_lshrrev_b32 v6, 4, v7\n\t"
    "s_waitcnt lgkmcnt(5)\n\t"
    "v_mfma_f32_16x16x32_bf16 v[60:63], v[40:43], v[16:19], v[60:63]\n\t"
    "v_lshl_add_u32 v4, v4, 2, v6\n\t"
    "v_lshlrev_b32 v4, 2, v4\n\t"
    "s_waitcnt lgkmcnt(4)\n\t"
    "v_mfma_f32_16x16x32_bf16 v[60:63], v[44:47], v[20:23], v[60:63]\n\t"
    "v_add_u32 v4, 18432, v4\n\t"
    "ds_read_b64_tr_b16 v[40:41], v5 offset:40960\n\t"
    "ds_read_b64_tr_b16 v[42:43], v5 offset:45056\n\t"
    "ds_read_b64_tr_b16 v[44:45], v5 offset:40968\n\t"
    "ds_read_b64_tr_b16 v[46:47], v5 offset:45064\n\t"
    "ds_read2_b32 v[36:37], v4 offset0:0 offset1:16\n\t"
    "ds_read2_b32 v[38:39], v4 offset0:128 offset1:144\n\t"
    "v_and_b32 v4, 7, v7\n\t"
    "v_xor_b32 v6, v6, v4\n\t"
    "v_max3_f32 v34, v32, v33, %[lo]\n\t"
    "v_max3_f32 v34, v34, v56, v57\n\t"
    "v_max3_f32 v34, v34, v58, v59\n\t"
    "v_max3_f32 v34, v34, v60, v61\n\t"
    "v_max3_f32 v34, v34, v62, v63\n\t"
    "v_mov_b32 v35, v34\n\t"
    "v_lshlrev_b32 v6, 4, v6\n\t"
    "v_and_b32 v4, 15, v7\n\t"
    "v_permlane16_swap_b32 v34, v35\n\t"
    "v_max_f32 v34, v34, v35\n\t"
    "v_mov_b32 v35, v34\n\t"
    "v_lshl_or_b32 v6, v4, 7, v6\n\t"
    "v_xor_b32 v4, 64, v6\n\t"
    "v_permlane32_swap_b32 v34, v35\n\t"
    "v_max_f32 v34, v34, v35\n\t"
    "v_mul_f32 v34, %[nl], v34\n\t"
    "v_fma_f32 v32, v32, %[lsc], v34\n\t"
    "v_fma_f32 v33, v33, %[lsc], v34\n\t"
    "v_fma_f32 v56, v56, %[lsc], v34\n\t"
    "v_fma_f32 v57, v57, %[lsc], v34\n\t"
    "v_fma_f32 v58, v58, %[lsc], v34\n\t"
    "v_fma_f32 v59, v59, %[lsc], v34\n\t"
    "v_fma_f32 v60, v60, %[lsc], v34\n\t"
    "v_fma_f32 v61, v61, %[lsc], v34\n\t"
    "v_fma_f32 v62, v62, %[lsc], v34\n\t"
    "v_fma_f32 v63, v63, %[lsc], v34\n\t"
    "v_exp_f32 v56, v56\n\t"
    "v_exp_f32 v57, v57\n\t"
    "v_exp_f32 v58, v58\n\t"
    "v_exp_f32 v59, v59\n\t"
    "v_exp_f32 v60, v60\n\t"
    "v_exp_f32 v61, v61\n\t"
    "v_exp_f32 v62, v62\n\t"
    "v_exp_f32 v63, v63\n\t"
    "v_exp_f32 v32, v32\n\t"
    "v_exp_f32 v33, v33\n\t"
    "v_cvt_pk_bf16_f32 v24, v56, v57\n\t"
    "v_cvt_pk_bf16_f32 v25, v58, v59\n\t"
    "v_cvt_pk_bf16_f32 v26, v60, v61\n\t"
    "v_cvt_pk_bf16_f32 v27, v62, v63\n\t"
    "v_cvt_pk_bf16_f32 v28, v32, v33\n\t"
    "s_waitcnt lgkmcnt(0)\n\t"
    "v_mfma_f32_16x16x32_bf16 v[8:11], v[48:51], v[24:27], 0\n\t"
    "v_mov_b32 v56, %[one]\n\t"
    "v_mov_b32 v57, %[one]\n\t"
    "v_mfma_f32_16x16x32_bf16 v[12:15], v[52:55], v[24:27], 0\n\t"
    "v_mov_b32 v58, %[one]\n\t"
    "v_mov_b32 v59, %[one]\n\t"
    "v_mfma_f32_16x16x32_bf16 v[16:19], v[40:43], v[24:27], 0\n\t"
    "v_mov_b32 v29, 0\n\t"
    "v_mov_b32 v30, 0\n\t"
    "v_mfma_f32_16x16x32_bf16 v[60:63], v[56:59], v[24:27], 0\n\t"
    "v_mov_b32 v31, v28\n\t"
    "v_mfma_f32_16x16x32_bf16 v[20:23], v[44:47], v[24:27], 0\n\t"
    "v_mfma_f32_16x16x16_bf16 v[8:11], v[36:37], v[28:29], v[8:11]\n\t"
    "v_mfma_f32_16x16x16_bf16 v[12:15], v[36:37], v[30:31], v[12:15]\n\t"
    "s_nop 1\n\t"
    "v_mfma_f32_16x16x16_bf16 v[16:19], v[38:39], v[28:29], v[16:19]\n\t"
    "v_mfma_f32_16x16x16_bf16 v[60:63], v[56:57], v[28:29], v[60:63]\n\t"
    "s_nop 1\n\t"
    "v_mfma_f32_16x16x16_bf16 v[20:23], v[38:39], v[30:31], v[20:23]\n\t"
    "s_nop 4\n\t"
    "v_rcp_f32 v60, v60\n\t"
    "s_nop 0\n\t"
    "v_pk_mul_f32 v[8:9], v[8:9], v[60:61] op_sel_hi:[1,0]\n\t"
    "v_pk_mul_f32 v[10:11], v[10:11], v[60:61] op_sel_hi:[1,0]\n\t"
    "v_pk_mul_f32 v[12:13], v[12:13], v[60:61] op_sel_hi:[1,0]\n\t"
    "v_pk_mul_f32 v[14:15], v[14:15], v[60:61] op_sel_hi:[1,0]\n\t"
    "v_cvt_pk_bf16_f32 v24, v8, v9\n\t"
    "v_cvt_pk_bf16_f32 v25, v10, v11\n\t"
    "v_cvt_pk_bf16_f32 v26, v12, v13\n\t"
    "v_cvt_pk_bf16_f32 v27, v14, v15\n\t"
    "global_store_dwordx4 v6, v[24:27], %[od] offset:2048 sc1\n\t"
    "v_pk_mul_f32 v[16:17], v[16:17], v[60:61] op_sel_hi:[1,0]\n\t"
    "v_pk_mul_f32 v[18:19], v[18:19], v[60:61] op_sel_hi:[1,0]\n\t"
    "v_pk_mul_f32 v[20:21], v[20:21], v[60:61] op_sel_hi:[1,0]\n\t"
    "v_pk_mul_f32 v[22:23], v[22:23], v[60:61] op_sel_hi:[1,0]\n\t"
    "v_cvt_pk_bf16_f32 v28, v16, v17\n\t"
    "v_cvt_pk_bf16_f32 v29, v18, v19\n\t"
    "v_cvt_pk_bf16_f32 v30, v20, v21\n\t"
    "v_cvt_pk_bf16_f32 v31, v22, v23\n\t"
    "global_store_dwordx4 v4, v[28:31], %[od] offset:2048 sc1\n\t"
    "s_waitcnt lgkmcnt(0)\n\t"
    "s_nop 0"
    :
    : [pb] "s"(pb), [od] "s"(od), [neg] "s"(-3.0e38f), [lo] "s"(-1.0e38f), [one] "s"(0x3F803F80u), [lsc] "s"(LSC), [nl] "s"(-LSC), "{v[64:95]}"(W.w0), "{v[96:127]}"(W.w1), "{v[128:159]}"(W.w2), "{v[160:191]}"(W.w3), "{v[192:223]}"(W.w4), "{v[224:255]}"(W.w5)
    : "v4", "v5", "v6", "v7", "v8", "v9", "v10", "v11", "v12", "v13", "v14", "v15", "v16", "v17", "v18", "v19", "v20", "v21", "v22", "v23", "v24", "v25", "v26", "v27", "v28", "v29", "v30", "v31", "v32", "v33", "v34", "v35", "v36", "v37", "v38", "v39", "v40", "v41", "v42", "v43", "v44", "v45", "v46", "v47", "v48", "v49", "v50", "v51", "v52", "v53", "v54", "v55", "v56", "v57", "v58", "v59", "v60", "v61", "v62", "v63", "memory");
}
template <> __device__ __forceinline__ void seg_attn<2>(const QKW& W, unsigned pb, const char* od, unsigned long long* ts) {
  asm volatile("v_mbcnt_lo_u32_b32 v32, -1, 0\n\t"
    "v_mbcnt_hi_u32_b32 v32, -1, v32\n\t"
    "v_lshlrev_b32 v35, 4, v32\n\t"
    "ds_read_b128 v[24:27], v35 offset:20480\n\t"
    "ds_read_b128 v[28:31], v35 offset:21504\n\t"
    "v_lshrrev_b32 v33, 4, v32\n\t"
    "v_and_b32 v34, 15, v32\n\t"
    "v_xor_b32 v4, v33, v34\n\t"
    "v_lshl_or_b32 v4, v4, 4, %[pb]\n\t"
    "v_lshl_or_b32 v4, v34, 8, v4\n\t"
    "ds_read_b128 v[8:11], v4 offset:49152\n\t"
    "v_xor_b32 v5, 0x40, v4\n\t"
    "ds_read_b128 v[12:15], v5 offset:49152\n\t"
    "v_xor_b32 v6, 0x80, v4\n\t"
    "ds_read_b128 v[16:19], v6 offset:49152\n\t"
    "v_xor_b32 v7, 0xc0, v4\n\t"
    "ds_read_b128 v[20:23], v7 offset:49152\n\t"
    "ds_read_b128 v[40:43], v4 offset:32768\n\t"
    "ds_read_b128 v[44:47], v5 offset:32768\n\t"
    "ds_read_b128 v[48:51], v6 offset:32768\n\t"
    "ds_read_b128 v[52:55], v7 offset:32768\n\t"
    "v_lshlrev_b32 v35, 2, v33\n\t"
    "v_sub_u32 v35, v34, v35\n\t"
    "v_ashrrev_i32 v60, 31, v35\n\t"
    "v_and_b32 v60, %[neg], v60\n\t"
    "v_add_u32 v61, -1, v35\n\t"
    "v_ashrrev_i32 v61, 31, v61\n\t"
    "v_and_b32 v61, %[neg], v61\n\t"
    "v_add_u32 v62, -2, v35\n\t"
    "v_ashrrev_i32 v62, 31, v62\n\t"
    "v_and_b32 v62, %[neg], v62\n\t"
    "v_add_u32 v63, -3, v35\n\t"
    "v_ashrrev_i32 v63, 31, v63\n\t"
    "v_and_b32 v63, %[neg], v63\n\t"
    "s_waitcnt lgkmcnt(6)\n\t"
    "v_mfma_f32_16x16x32_bf16 v[32:35], v[24:27], v[8:11], 0\n\t"
    "v_mfma_f32_16x16x32_bf16 v[36:39], v[24:27], v[12:15], 0\n\t"
    "s_waitcnt lgkmcnt(4)\n\t"
    "v_mfma_f32_16x16x32_bf16 v[32:35], v[28:31], v[16:19], v[32:35]\n\t"
    "v_mfma_f32_16x16x32_bf16 v[36:39], v[28:31], v[20:23], v[36:39]\n\t"
    "s_waitcnt lgkmcnt(3)\n\t"
    "v_mfma_f32_16x16x32_bf16 v[60:63], v[40:43], v[8:11], v[60:63]\n\t"
    "s_waitcnt lgkmcnt(2)\n\t"
    "v_mfma_f32_16x16x32_bf16 v[60:63], v[44:47], v[12:15], v[60:63]\n\t"
    "v_mbcnt_lo_u32_b32 v7, -1, 0\n\t"
    "v_mbcnt_hi_u32_b32 v7, -1, v7\n\t"
    "s_waitcnt lgkmcnt(1)\n\t"
    "v_mfma_f32_16x16x32_bf16 v[60:63], v[48:51], v[16:19], v[60:63]\n\t"
    "v_and_or_b32 v4, v7, 3, 8\n\t"
    "v_lshrrev_b32 v6, 2, v7\n\t"
    "s_waitcnt lgkmcnt(0)\n\t"
    "v_mfma_f32_16x16x32_bf16 v[60:63], v[52:55], v[20:23], v[60:63]\n\t"
    "v_xor_b32 v4, v4, v6\n\t"
    "v_lshl_or_b32 v4, v4, 4, %[pb]\n\t"
    "v_lshl_or_b32 v4, v6, 8, v4\n\t"
    "v_xor_b32 v5, 64, v4\n\t"
    "ds_read_b64_tr_b16 v[48:49], v4 offset:40960\n\t"
    "ds_read_b64_tr_b16 v[52:53], v4 offset:40968\n\t"
    "v_add_f32 v32, v32, v38\n\t"
    "v_add_f32 v33, v33, v39\n\t"
    "v_and_b32 v6, 12, v7\n\t"
    "v_and_b32 v4, 3, v7\n\t"
    "v_lshl_add_u32 v4, v6, 1, v4\n\t"
    "v_lshrrev_b32 v6, 4, v7\n\t"
    "v_lshl_add_u32 v4, v4, 2, v6\n\t"
    "v_lshlrev_b32 v4, 2, v4\n\t"
    "v_add_u32 v4, 19456, v4\n\t"
    "ds_read_b64_tr_b16 v[40:41], v5 offset:40960\n\t"
    "ds_read_b64_tr_b16 v[44:45], v5 offset:40968\n\t"
    "ds_read2_b32 v[36:37], v4 offset0:0 offset1:16\n\t"
    "ds_read2_b32 v[38:39], v4 offset0:128 offset1:144\n\t"
    "v_and_b32 v4, 7, v7\n\t"
    "v_xor_b32 v6, v6, v4\n\t"
    "v_max3_f32 v34, v32, v33, %[lo]\n\t"
    "v_max3_f32 v34, v34, v60, v61\n\t"
    "v_max3_f32 v34, v34, v62, v63\n\t"
    "v_mov_b32 v35, v34\n\t"
    "v_lshlrev_b32 v6, 4, v6\n\t"
    "v_and_b32 v4, 15, v7\n\t"
    "v_permlane16_swap_b32 v34, v35\n\t"
    "v_max_f32 v34, v34, v35\n\t"
    "v_mov_b32 v35, v34\n\t"
    "v_lshl_or_b32 v6, v4, 7, v6\n\t"
    "v_xor_b32 v4, 64, v6\n\t"
    "v_permlane32_swap_b32 v34, v35\n\t"
    "v_max_f32 v34, v34, v35\n\t"
    "v_mul_f32 v34, %[nl], v34\n\t"
    "v_fma_f32 v32, v32, %[lsc], v34\n\t"
    "v_fma_f32 v33, v33, %[lsc], v34\n\t"
    "v_fma_f32 v60, v60, %[lsc], v34\n\t"
    "v_fma_f32 v61, v61, %[lsc], v34\n\t"
    "v_fma_f32 v62, v62, %[lsc], v34\n\t"
    "v_fma_f32 v63, v63, %[lsc], v34\n\t"
    "v_exp_f32 v60, v60\n\t"
    "v_exp_f32 v61, v61\n\t"
    "v_exp_f32 v62, v62\n\t"
    "v_exp_f32 v63, v63\n\t"
    "v_exp_f32 v32, v32\n\t"
    "v_exp_f32 v33, v33\n\t"
    "v_cvt_pk_bf16_f32 v24, v60, v61\n\t"
    "v_cvt_pk_bf16_f32 v25, v62, v63\n\t"
    "v_cvt_pk_bf16_f32 v28, v32, v33\n\t"
    "s_waitcnt lgkmcnt(0)\n\t"
    "v_mfma_f32_16x16x16_bf16 v[8:11], v[48:49], v[24:25], 0\n\t"
    "v_mov_b32 v56, %[one]\n\t"
    "v_mov_b32 v57, %[one]\n\t"
    "v_mfma_f32_16x16x16_bf16 v[12:15], v[52:53], v[24:25], 0\n\t"
    "v_mov_b32 v58, %[one]\n\t"
    "v_mov_b32 v59, %[one]\n\t"
    "v_mfma_f32_16x16x16_bf16 v[16:19], v[40:41], v[24:25], 0\n\t"
    "v_mov_b32 v29, 0\n\t"
    "v_mov_b32 v30, 0\n\t"
    "v_mfma_f32_16x16x16_bf16 v[60:63], v[56:57], v[24:25], 0\n\t"
    "v_mov_b32 v31, v28\n\t"
    "v_mfma_f32_16x16x16_bf16 v[20:23], v[44:45], v[24:25], 0\n\t"
    "v_mfma_f32_16x16x16_bf16 v[8:11], v[36:37], v[28:29], v[8:11]\n\t"
    "v_mfma_f32_16x16x16_bf16 v[12:15], v[36:37], v[30:31], v[12:15]\n\t"
    "s_nop 1\n\t"
    "v_mfma_f32_16x16x16_bf16 v[16:19], v[38:39], v[28:29], v[16:19]\n\t"
    "v_mfma_f32_16x16x16_bf16 v[60:63], v[56:57], v[28:29], v[60:63]\n\t"
    "s_nop 1\n\t"
    "v_mfma_f32_16x16x16_bf16 v[20:23], v[38:39], v[30:31], v[20:23]\n\t"
    "s_nop 4\n\t"
    "v_rcp_f32 v60, v60\n\t"
    "s_nop 0\n\t"
    "v_pk_mul_f32 v[8:9], v[8:9], v[60:61] op_sel_hi:[1,0]\n\t"
    "v_pk_mul_f32 v[10:11], v[10:11], v[60:61] op_sel_hi:[1,0]\n\t"
    "v_pk_mul_f32 v[12:13], v[12:13], v[60:61] op_sel_hi:[1,0]\n\t"
    "v_pk_mul_f32 v[14:15], v[14:15], v[60:61] op_sel_hi:[1,0]\n\t"
    "v_cvt_pk_bf16_f32 v24, v8, v9\n\t"
    "v_cvt_pk_bf16_f32 v25, v10, v11\n\t"
    "v_cvt_pk_bf16_f32 v26, v12, v13\n\t"
    "v_cvt_pk_bf16_f32 v27, v14, v15\n\t"
    "global_store_dwordx4 v6, v[24:27], %[od] offset:0 sc1\n\t"
    "v_pk_mul_f32 v[16:17], v[16:17], v[60:61] op_sel_hi:[1,0]\n\t"
    "v_pk_mul_f32 v[18:19], v[18:19], v[60:61] op_sel_hi:[1,0]\n\t"
    "v_pk_mul_f32 v[20:21], v[20:21], v[60:61] op_sel_hi:[1,0]\n\t"
    "v_pk_mul_f32 v[22:23], v[22:23], v[60:61] op_sel_hi:[1,0]\n\t"
    "v_cvt_pk_bf16_f32 v28, v16, v17\n\t"
    "v_cvt_pk_bf16_f32 v29, v18, v19\n\t"
    "v_cvt_pk_bf16_f32 v30, v20, v21\n\t"
    "v_cvt_pk_bf16_f32 v31, v22, v23\n\t"
    "global_store_dwordx4 v4, v[28:31], %[od] offset:0 sc1\n\t"
    "s_waitcnt lgkmcnt(0)\n\t"
    "s_nop 0"
    :
    : [pb] "s"(pb), [od] "s"(od), [neg] "s"(-3.0e38f), [lo] "s"(-1.0e38f), [one] "s"(0x3F803F80u), [lsc] "s"(LSC), [nl] "s"(-LSC), "{v[64:95]}"(W.w0), "{v[96:127]}"(W.w1), "{v[128:159]}"(W.w2), "{v[160:191]}"(W.w3), "{v[192:223]}"(W.w4), "{v[224:255]}"(W.w5)
    : "v4", "v5", "v6", "v7", "v8", "v9", "v10", "v11", "v12", "v13", "v14", "v15", "v16", "v17", "v18", "v19", "v20", "v21", "v22", "v23", "v24", "v25", "v26", "v27", "v28", "v29", "v30", "v31", "v32", "v33", "v34", "v35", "v36", "v37", "v38", "v39", "v40", "v41", "v42", "v43", "v44", "v45", "v46", "v47", "v48", "v49", "v50", "v51", "v52", "v53", "v54", "v55", "v56", "v57", "v58", "v59", "v60", "v61", "v62", "v63", "memory");
}
template <> __device__ __forceinline__ void seg_attn<3>(const QKW& W, unsigned pb, const char* od, unsigned long long* ts) {
  asm volatile("v_mbcnt_lo_u32_b32 v32, -1, 0\n\t"
    "v_mbcnt_hi_u32_b32 v32, -1, v32\n\t"
    "v_lshlrev_b32 v35, 4, v32\n\t"
    "ds_read_b128 v[24:27], v35 offset:20480\n\t"
    "ds_read_b128 v[28:31], v35 offset:21504\n\t"
    "v_lshrrev_b32 v33, 4, v32\n\t"
    "v_and_b32 v34, 15, v32\n\t"
    "v_xor_b32 v4, v33, v34\n\t"
    "v_lshl_or_b32 v4, v4, 4, %[pb]\n\t"
    "v_lshl_or_b32 v4, v34, 8, v4\n\t"
    "ds_read_b128 v[8:11], v4 offset:53248\n\t"
    "v_xor_b32 v5, 0x40, v4\n\t"
    "ds_read_b128 v[12:15], v5 offset:53248\n\t"
    "v_xor_b32 v6, 0x80, v4\n\t"
    "ds_read_b128 v[16:19], v6 offset:53248\n\t"
    "v_xor_b32 v7, 0xc0, v4\n\t"
    "ds_read_b128 v[20:23], v7 offset:53248\n\t"
    "ds_read_b128 v[40:43], v4 offset:32768\n\t"
    "ds_read_b128 v[44:47], v5 offset:32768\n\t"
    "ds_read_b128 v[48:51], v6 offset:32768\n\t"
    "ds_read_b128 v[52:55], v7 offset:32768\n\t"
    "v_lshlrev_b32 v35, 2, v33\n\t"
    "v_sub_u32 v35, v34, v35\n\t"
    "v_ashrrev_i32 v60, 31, v35\n\t"
    "v_and_b32 v60, %[neg], v60\n\t"
    "v_add_u32 v61, -1, v35\n\t"
    "v_ashrrev_i32 v61, 31, v61\n\t"
    "v_and_b32 v61, %[neg], v61\n\t"
    "v_add_u32 v62, -2, v35\n\t"
    "v_ashrrev_i32 v62, 31, v62\n\t"
    "v_and_b32 v62, %[neg], v62\n\t"
    "v_add_u32 v63, -3, v35\n\t"
    "v_ashrrev_i32 v63, 31, v63\n\t"
    "v_and_b32 v63, %[neg], v63\n\t"
    "s_waitcnt lgkmcnt(6)\n\t"
    "v_mfma_f32_16x16x32_bf16 v[32:35], v[24:27], v[8:11], 0\n\t"
    "v_mfma_f32_16x16x32_bf16 v[36:39], v[24:27], v[12:15], 0\n\t"
    "s_waitcnt lgkmcnt(4)\n\t"
    "v_mfma_f32_16x16x32_bf16 v[32:35], v[28:31], v[16:19], v[32:35]\n\t"
    "v_mfma_f32_16x16x32_bf16 v[36:39], v[28:31], v[20:23], v[36:39]\n\t"
    "ds_read_b128 v[24:27], v4 offset:36864\n\t"
    "ds_read_b128 v[28:31], v5 offset:36864\n\t"
    "s_waitcnt lgkmcnt(5)\n\t"
    "v_mfma_f32_16x16x32_bf16 v[56:59], v[40:43], v[8:11], 0\n\t"
    "s_waitcnt lgkmcnt(4)\n\t"
    "v_mfma_f32_16x16x32_bf16 v[56:59], v[44:47], v[12:15], v[56:59]\n\t"
    "ds_read_b128 v[40:43], v6 offset:36864\n\t"
    "ds_read_b128 v[44:47], v7 offset:36864\n\t"
    "v_mbcnt_lo_u32_b32 v7, -1, 0\n\t"
    "v_mbcnt_hi_u32_b32 v7, -1, v7\n\t"
    "s_waitcnt lgkmcnt(5)\n\t"
    "v_mfma_f32_16x16x32_bf16 v[56:59], v[48:51], v[16:19], v[56:59]\n\t"
    "v_and_or_b32 v4, v7, 3, 8\n\t"
    "v_lshrrev_b32 v6, 2, v7\n\t"
    "s_waitcnt lgkmcnt(4)\n\t"
    "v_mfma_f32_16x16x32_bf16 v[56:59], v[52:55], v[20:23], v[56:59]\n\t"
    "v_xor_b32 v4, v4, v6\n\t"
    "v_lshl_or_b32 v4, v4, 4, %[pb]\n\t"
    "v_lshl_or_b32 v4, v6, 8, v4\n\t"
    "v_xor_b32 v5, 64, v4\n\t"
    "ds_read_b64_tr_b16 v[48:49], v4 offset:40960\n\t"
    "ds_read_b64_tr_b16 v[50:51], v4 offset:45056\n\t"
    "ds_read_b64_tr_b16 v[52:53], v4 offset:40968\n\t"
    "ds_read_b64_tr_b16 v[54:55], v4 offset:45064\n\t"
    "v_add_f32 v32, v32, v38\n\t"
    "v_add_f32 v33, v33, v39\n\t"
    "s_waitcnt lgkmcnt(7)\n\t"
    "v_mfma_f32_16x16x32_bf16 v[60:63], v[24:27], v[8:11], v[60:63]\n\t"
    "v_and_b32 v6, 12, v7\n\t"
    "v_and_b32 v4, 3, v7\n\t"
    "s_waitcnt lgkmcnt(6)\n\t"
    "v_mfma_f32_16x16x32_bf16 v[60:63], v[28:31], v[12:15], v[60:63]\n\t"
    "v_lshl_add_u32 v4, v6, 1, v4\n\t"
    "v_lshrrev_b32 v6, 4, v7\n\t"
    "s_waitcnt lgkmcnt(5)\n\t"
    "v_mfma_f32_16x16x32_bf16 v[60:63], v[40:43], v[16:19], v[60:63]\n\t"
    "v_lshl_add_u32 v4, v4, 2, v6\n\t"
    "v_lshlrev_b32 v4, 2, v4\n\t"
    "s_waitcnt lgkmcnt(4)\n\t"
    "v_mfma_f32_16x16x32_bf16 v[60:63], v[44:47], v[20:23], v[60:63]\n\t"
    "v_add_u32 v4, 19456, v4\n\t"
    "ds_read_b64_tr_b16 v[40:41], v5 offset:40960\n\t"
    "ds_read_b64_tr_b16 v[42:43], v5 offset:45056\n\t"
    "ds_read_b64_tr_b16 v[44:45], v5 offset:40968\n\t"
    "ds_read_b64_tr_b16 v[46:47], v5 offset:45064\n\t"
    "ds_read2_b32 v[36:37], v4 offset0:0 offset1:16\n\t"
    "ds_read2_b32 v[38:39], v4 offset0:128 offset1:144\n\t"
    "v_and_b32 v4, 7, v7\n\t"
    "v_xor_b32 v6, v6, v4\n\t"
    "v_max3_f32 v34, v32, v33, %[lo]\n\t"
    "v_max3_f32 v34, v34, v56, v57\n\t"
    "v_max3_f32 v34, v34, v58, v59\n\t"
    "v_max3_f32 v34, v34, v60, v61\n\t"
    "v_max3_f32 v34, v34, v62, v63\n\t"
    "v_mov_b32 v35, v34\n\t"
    "v_lshlrev_b32 v6, 4, v6\n\t"
    "v_and_b32 v4, 15, v7\n\t"
    "v_permlane16_swap_b32 v34, v35\n\t"
    "v_max_f32 v34, v34, v35\n\t"
    "v_mov_b32 v35, v34\n\t"
    "v_lshl_or_b32 v6, v4, 7, v6\n\t"
    "v_xor_b32 v4, 64, v6\n\t"
    "v_permlane32_swap_b32 v34, v35\n\t"
    "v_max_f32 v34, v34, v35\n\t"
    "v_mul_f32 v34, %[nl], v34\n\t"
    "v_fma_f32 v32, v32, %[lsc], v34\n\t"
    "v_fma_f32 v33, v33, %[lsc], v34\n\t"
    "v_fma_f32 v56, v56, %[lsc], v34\n\t"
    "v_fma_f32 v57, v57, %[lsc], v34\n\t"
    "v_fma_f32 v58, v58, %[lsc], v34\n\t"
    "v_fma_f32 v59, v59, %[lsc], v34\n\t"
    "v_fma_f32 v60, v60, %[lsc], v34\n\t"
    "v_fma_f32 v61, v61, %[lsc], v34\n\t"
    "v_fma_f32 v62, v62, %[lsc], v34\n\t"
    "v_fma_f32 v63, v63, %[lsc], v34\n\t"
    "v_exp_f32 v56, v56\n\t"
    "v_exp_f32 v57, v57\n\t"
    "v_exp_f32 v58, v58\n\t"
    "v_exp_f32 v59, v59\n\t"
    "v_exp_f32 v60, v60\n\t"
    "v_exp_f32 v61, v61\n\t"
    "v_exp_f32 v62, v62\n\t"
    "v_exp_f32 v63, v63\n\t"
    "v_exp_f32 v32, v32\n\t"
    "v_exp_f32 v33, v33\n\t"
    "v_cvt_pk_bf16_f32 v24, v56, v57\n\t"
    "v_cvt_pk_bf16_f32 v25, v58, v59\n\t"
    "v_cvt_pk_bf16_f32 v26, v60, v61\n\t"
    "v_cvt_pk_bf16_f32 v27, v62, v63\n\t"
    "v_cvt_pk_bf16_f32 v28, v32, v33\n\t"
    "s_waitcnt lgkmcnt(0)\n\t"
    "v_mfma_f32_16x16x32_bf16 v[8:11], v[48:51], v[24:27], 0\n\t"
    "v_mov_b32 v56, %[one]\n\t"
    "v_mov_b32 v57, %[one]\n\t"
    "v_mfma_f32_16x16x32_bf16 v[12:15], v[52:55], v[24:27], 0\n\t"
    "v_mov_b32 v58, %[one]\n\t"
    "v_mov_b32 v59, %[one]\n\t"
    "v_mfma_f32_16x16x32_bf16 v[16:19], v[40:43], v[24:27], 0\n\t"
    "v_mov_b32 v29, 0\n\t"
    "v_mov_b32 v30, 0\n\t"
    "v_mfma_f32_16x16x32_bf16 v[60:63], v[56:59], v[24:27], 0\n\t"
    "v_mov_b32 v31, v28\n\t"
    "v_mfma_f32_16x16x32_bf16 v[20:23], v[44:47], v[24:27], 0\n\t"
    "v_mfma_f32_16x16x16_bf16 v[8:11], v[36:37], v[28:29], v[8:11]\n\t"
    "v_mfma_f32_16x16x16_bf16 v[12:15], v[36:37], v[30:31], v[12:15]\n\t"
    "s_nop 1\n\t"
    "v_mfma_f32_16x16x16_bf16 v[16:19], v[38:39], v[28:29], v[16:19]\n\t"
    "v_mfma_f32_16x16x16_bf16 v[60:63], v[56:57], v[28:29], v[60:63]\n\t"
    "s_nop 1\n\t"
    "v_mfma_f32_16x16x16_bf16 v[20:23], v[38:39], v[30:31], v[20:23]\n\t"
    "s_nop 4\n\t"
    "v_rcp_f32 v60, v60\n\t"
    "s_nop 0\n\t"
    "v_pk_mul_f32 v[8:9], v[8:9], v[60:61] op_sel_hi:[1,0]\n\t"
    "v_pk_mul_f32 v[10:11], v[10:11], v[60:61] op_sel_hi:[1,0]\n\t"
    "v_pk_mul_f32 v[12:13], v[12:13], v[60:61] op_sel_hi:[1,0]\n\t"
    "v_pk_mul_f32 v[14:15], v[14:15], v[60:61] op_sel_hi:[1,0]\n\t"
    "v_cvt_pk_bf16_f32 v24, v8, v9\n\t"
    "v_cvt_pk_bf16_f32 v25, v10, v11\n\t"
    "v_cvt_pk_bf16_f32 v26, v12, v13\n\t"
    "v_cvt_pk_bf16_f32 v27, v14, v15\n\t"
    "global_store_dwordx4 v6, v[24:27], %[od] offset:2048 sc1\n\t"
    "v_pk_mul_f32 v[16:17], v[16:17], v[60:61] op_sel_hi:[1,0]\n\t"
    "v_pk_mul_f32 v[18:19], v[18:19], v[60:61] op_sel_hi:[1,0]\n\t"
    "v_pk_mul_f32 v[20:21], v[20:21], v[60:61] op_sel_hi:[1,0]\n\t"
    "v_pk_mul_f32 v[22:23], v[22:23], v[60:61] op_sel_hi:[1,0]\n\t"
    "v_cvt_pk_bf16_f32 v28, v16, v17\n\t"
    "v_cvt_pk_bf16_f32 v29, v18, v19\n\t"
    "v_cvt_pk_bf16_f32 v30, v20, v21\n\t"
    "v_cvt_pk_bf16_f32 v31, v22, v23\n\t"
    "global_store_dwordx4 v4, v[28:31], %[od] offset:2048 sc1\n\t"
    "s_waitcnt lgkmcnt(0)\n\t"
    "s_nop 0"
    :
    : [pb] "s"(pb), [od] "s"(od), [neg] "s"(-3.0e38f), [lo] "s"(-1.0e38f), [one] "s"(0x3F803F80u), [lsc] "s"(LSC), [nl] "s"(-LSC), "{v[64:95]}"(W.w0), "{v[96:127]}"(W.w1), "{v[128:159]}"(W.w2), "{v[160:191]}"(W.w3), "{v[192:223]}"(W.w4), "{v[224:255]}"(W.w5)
    : "v4", "v5", "v6", "v7", "v8", "v9", "v10", "v11", "v12", "v13", "v14", "v15", "v16", "v17", "v18", "v19", "v20", "v21", "v22", "v23", "v24", "v25", "v26", "v27", "v28", "v29", "v30", "v31", "v32", "v33", "v34", "v35", "v36", "v37", "v38", "v39", "v40", "v41", "v42", "v43", "v44", "v45", "v46", "v47", "v48", "v49", "v50", "v51", "v52", "v53", "v54", "v55", "v56", "v57", "v58", "v59", "v60", "v61", "v62", "v63", "memory");
}
template <int I> __device__ __forceinline__ void qk_wload(u32x32& w, unsigned a0, unsigned a1);
template <> __device__ __forceinline__ void qk_wload<0>(u32x32& w, unsigned a0, unsigned a1) {
  asm volatile("ds_read_b64_tr_b16 v[64:65], %1 offset:0\n\t"
    "ds_read_b64_tr_b16 v[66:67], %1 offset:512\n\t"
    "ds_read_b64_tr_b16 v[68:69], %1 offset:8192\n\t"
    "ds_read_b64_tr_b16 v[70:71], %1 offset:8704\n\t"
    "ds_read_b64_tr_b16 v[72:73], %2 offset:0\n\t"
    "ds_read_b64_tr_b16 v[74:75], %2 offset:512\n\t"
    "ds_read_b64_tr_b16 v[76:77], %2 offset:8192\n\t"
    "ds_read_b64_tr_b16 v[78:79], %2 offset:8704\n\t"
    "s_waitcnt lgkmcnt(0)\n\t"
    "ds_read_b64_tr_b16 v[80:81], %1 offset:16384\n\t"
    "ds_read_b64_tr_b16 v[82:83], %1 offset:16896\n\t"
    "ds_read_b64_tr_b16 v[84:85], %1 offset:24576\n\t"
    "ds_read_b64_tr_b16 v[86:87], %1 offset:25088\n\t"
    "ds_read_b64_tr_b16 v[88:89], %2 offset:16384\n\t"
    "ds_read_b64_tr_b16 v[90:91], %2 offset:16896\n\t"
    "ds_read_b64_tr_b16 v[92:93], %2 offset:24576\n\t"
    "ds_read_b64_tr_b16 v[94:95], %2 offset:25088\n\t"
    "s_waitcnt lgkmcnt(0)"
    : "=&{v[64:95]}"(w) : "v"(a0), "v"(a1) : "memory");
}
template <> __device__ __forceinline__ void qk_wload<1>(u32x32& w, unsigned a0, unsigned a1) {
  asm volatile("ds_read_b64_tr_b16 v[96:97], %1 offset:0\n\t"
    "ds_read_b64_tr_b16 v[98:99], %1 offset:512\n\t"
    "ds_read_b64_tr_b16 v[100:101], %1 offset:8192\n\t"
    "ds_read_b64_tr_b16 v[102:103], %1 offset:8704\n\t"
    "ds_read_b64_tr_b16 v[104:105], %2 offset:0\n\t"
    "ds_read_b64_tr_b16 v[106:107], %2 offset:512\n\t"
    "ds_read_b64_tr_b16 v[108:109], %2 offset:8192\n\t"
    "ds_read_b64_tr_b16 v[110:111], %2 offset:8704\n\t"
    "s_waitcnt lgkmcnt(0)\n\t"
    "ds_read_b64_tr_b16 v[112:113], %1 offset:16384\n\t"
    "ds_read_b64_tr_b16 v[114:115], %1 offset:16896\n\t"
    "ds_read_b64_tr_b16 v[116:117], %1 offset:24576\n\t"
    "ds_read_b64_tr_b16 v[118:119], %1 offset:25088\n\t"
    "ds_read_b64_tr_b16 v[120:121], %2 offset:16384\n\t"
    "ds_read_b64_tr_b16 v[122:123], %2 offset:16896\n\t"
    "ds_read_b64_tr_b16 v[124:125], %2 offset:24576\n\t"
    "ds_read_b64_tr_b16 v[126:127], %2 offset:25088\n\t"
    "s_waitcnt lgkmcnt(0)"
    : "=&{v[96:127]}"(w) : "v"(a0), "v"(a1) : "memory");
}
template <> __device__ __forceinline__ void qk_wload<2>(u32x32& w, unsigned a0, unsigned a1) {
  asm volatile("ds_read_b64_tr_b16 v[128:129], %1 offset:0\n\t"
    "ds_read_b64_tr_b16 v[130:131], %1 offset:512\n\t"
    "ds_read_b64_tr_b16 v[132:133], %1 offset:8192\n\t"
    "ds_read_b64_tr_b16 v[134:135], %1 offset:8704\n\t"
    "ds_read_b64_tr_b16 v[136:137], %2 offset:0\n\t"
    "ds_read_b64_tr_b16 v[138:139], %2 offset:512\n\t"
    "ds_read_b64_tr_b16 v[140:141], %2 offset:8192\n\t"
    "ds_read_b64_tr_b16 v[142:143], %2 offset:8704\n\t"
    "s_waitcnt lgkmcnt(0)\n\t"
    "ds_read_b64_tr_b16 v[144:145], %1 offset:16384\n\t"
    "ds_read_b64_tr_b16 v[146:147], %1 offset:16896\n\t"
    "ds_read_b64_tr_b16 v[148:149], %1 offset:24576\n\t"
    "ds_read_b64_tr_b16 v[150:151], %1 offset:25088\n\t"
    "ds_read_b64_tr_b16 v[152:153], %2 offset:16384\n\t"
    "ds_read_b64_tr_b16 v[154:155], %2 offset:16896\n\t"
    "ds_read_b64_tr_b16 v[156:157], %2 offset:24576\n\t"
    "ds_read_b64_tr_b16 v[158:159], %2 offset:25088\n\t"
    "s_waitcnt lgkmcnt(0)"
    : "=&{v[128:159]}"(w) : "v"(a0), "v"(a1) : "memory");
}
template <> __device__ __forceinline__ void qk_wload<3>(u32x32& w, unsigned a0, unsigned a1) {
  asm volatile("ds_read_b64_tr_b16 v[160:161], %1 offset:0\n\t"
    "ds_read_b64_tr_b16 v[162:163], %1 offset:512\n\t"
    "ds_read_b64_tr_b16 v[164:165], %1 offset:8192\n\t"
    "ds_read_b64_tr_b16 v[166:167], %1 offset:8704\n\t"
    "ds_read_b64_tr_b16 v[168:169], %2 offset:0\n\t"
    "ds_read_b64_tr_b16 v[170:171], %2 offset:512\n\t"
    "ds_read_b64_tr_b16 v[172:173], %2 offset:8192\n\t"
    "ds_read_b64_tr_b16 v[174:175], %2 offset:8704\n\t"
    "s_waitcnt lgkmcnt(0)\n\t"
    "ds_read_b64_tr_b16 v[176:177], %1 offset:16384\n\t"
    "ds_read_b64_tr_b16 v[178:179], %1 offset:16896\n\t"
    "ds_read_b64_tr_b16 v[180:181], %1 offset:24576\n\t"
    "ds_read_b64_tr_b16 v[182:183], %1 offset:25088\n\t"
    "ds_read_b64_tr_b16 v[184:185], %2 offset:16384\n\t"
    "ds_read_b64_tr_b16 v[186:187], %2 offset:16896\n\t"
    "ds_read_b64_tr_b16 v[188:189], %2 offset:24576\n\t"
    "ds_read_b64_tr_b16 v[190:191], %2 offset:25088\n\t"
    "s_waitcnt lgkmcnt(0)"
    : "=&{v[160:191]}"(w) : "v"(a0), "v"(a1) : "memory");
}
template <> __device__ __forceinline__ void qk_wload<4>(u32x32& w, unsigned a0, unsigned a1) {
  asm volatile("ds_read_b64_tr_b16 v[192:193], %1 offset:0\n\t"
    "ds_read_b64_tr_b16 v[194:195], %1 offset:512\n\t"
    "ds_read_b64_tr_b16 v[196:197], %1 offset:8192\n\t"
    "ds_read_b64_tr_b16 v[198:199], %1 offset:8704\n\t"
    "ds_read_b64_tr_b16 v[200:201], %2 offset:0\n\t"
    "ds_read_b64_tr_b16 v[202:203], %2 offset:512\n\t"
    "ds_read_b64_tr_b16 v[204:205], %2 offset:8192\n\t"
    "ds_read_b64_tr_b16 v[206:207], %2 offset:8704\n\t"
    "s_waitcnt lgkmcnt(0)\n\t"
    "ds_read_b64_tr_b16 v[208:209], %1 offset:16384\n\t"
    "ds_read_b64_tr_b16 v[210:211], %1 offset:16896\n\t"
    "ds_read_b64_tr_b16 v[212:213], %1 offset:24576\n\t"
    "ds_read_b64_tr_b16 v[214:215], %1 offset:25088\n\t"
    "ds_read_b64_tr_b16 v[216:217], %2 offset:16384\n\t"
    "ds_read_b64_tr_b16 v[218:219], %2 offset:16896\n\t"
    "ds_read_b64_tr_b16 v[220:221], %2 offset:24576\n\t"
    "ds_read_b64_tr_b16 v[222:223], %2 offset:25088\n\t"
    "s_waitcnt lgkmcnt(0)"
    : "=&{v[192:223]}"(w) : "v"(a0), "v"(a1) : "memory");
}
template <> __device__ __forceinline__ void qk_wload<5>(u32x32& w, unsigned a0, unsigned a1) {
  asm volatile("ds_read_b64_tr_b16 v[224:225], %1 offset:0\n\t"
    "ds_read_b64_tr_b16 v[226:227], %1 offset:512\n\t"
    "ds_read_b64_tr_b16 v[228:229], %1 offset:8192\n\t"
    "ds_read_b64_tr_b16 v[230:231], %1 offset:8704\n\t"
    "ds_read_b64_tr_b16 v[232:233], %2 offset:0\n\t"
    "ds_read_b64_tr_b16 v[234:235], %2 offset:512\n\t"
    "ds_read_b64_tr_b16 v[236:237], %2 offset:8192\n\t"
    "ds_read_b64_tr_b16 v[238:239], %2 offset:8704\n\t"
    "s_waitcnt lgkmcnt(0)\n\t"
    "ds_read_b64_tr_b16 v[240:241], %1 offset:16384\n\t"
    "ds_read_b64_tr_b16 v[242:243], %1 offset:16896\n\t"
    "ds_read_b64_tr_b16 v[244:245], %1 offset:24576\n\t"
    "ds_read_b64_tr_b16 v[246:247], %1 offset:25088\n\t"
    "ds_read_b64_tr_b16 v[248:249], %2 offset:16384\n\t"
    "ds_read_b64_tr_b16 v[250:251], %2 offset:16896\n\t"
    "ds_read_b64_tr_b16 v[252:253], %2 offset:24576\n\t"
    "ds_read_b64_tr_b16 v[254:255], %2 offset:25088\n\t"
    "s_waitcnt lgkmcnt(0)"
    : "=&{v[224:255]}"(w) : "v"(a0), "v"(a1) : "memory");
}
#define OREP 2
#define AREP 2
constexpr int V_P1 = 131072, V_PV = 149632, V_P2 = 0;
struct VRW { u32x32 k0, k1, o0, o1, o2, o3; };
__device__ __forceinline__ void vr_v(const VRW& W, const char* ag, const char* kd, const char* vd, unsigned wl, unsigned wa, unsigned wb, unsigned ra, unsigned rb, unsigned& cap, unsigned& sp, unsigned long long& tc, unsigned long long* ts) {
  asm volatile("s_mov_b32 %[sp], 0\n\t"
    "v_mbcnt_lo_u32_b32 v20, -1, 0\n\t"
    "v_mbcnt_hi_u32_b32 v20, -1, v20\n\t"
    "v_lshlrev_b32 v21, 4, v20\n\t"
    "s_nop 0\n\t"
    "global_load_dwordx4 v[24:27], v21, %[ag] sc1\n\t"
    "global_load_dwordx4 v[28:31], v21, %[ag] offset:1024 sc1\n\t"
    "s_memtime %[tc]\n\t"
    "v_lshl_add_u32 v6, v20, 4, %[wl]\n\t"
    "ds_read_b128 v[32:35], v6 offset:0\n\t"
    "ds_read_b128 v[36:39], v6 offset:1024\n\t"
    "ds_read_b128 v[40:43], v6 offset:2048\n\t"
    "ds_read_b128 v[44:47], v6 offset:3072\n\t"
    "ds_read_b128 v[48:51], v6 offset:4096\n\t"
    "ds_read_b128 v[52:55], v6 offset:5120\n\t"
    "ds_read_b128 v[56:59], v6 offset:6144\n\t"
    "ds_read_b128 v[60:63], v6 offset:7168\n\t"
    "s_setprio 3\n\t"
    "v_and_b32 v6, 2, v20\n\t"
    "v_mul_u32_u24 v6, 0x5555, v6\n\t"
    "v_or_b32 v4, %[c44], v6\n\t"
    "v_and_b32 v6, 15, v20\n\t"
    "v_lshl_add_u32 v5, v6, 2, %[wa]\n\t"
    "v_and_b32 v6, 48, v20\n\t"
    "v_mad_u32_u24 v5, v6, 36, v5\n\t"
    "v_and_b32 v6, 32, v20\n\t"
    "v_mul_u32_u24 v7, 18, v6\n\t"
    "v_and_b32 v6, 4, v20\n\t"
    "v_mul_u32_u24 v6, 72, v6\n\t"
    "v_add_u32 v7, v7, v6\n\t"
    "v_and_b32 v6, 16, v20\n\t"
    "v_lshl_add_u32 v7, v6, 3, v7\n\t"
    "v_and_b32 v6, 3, v20\n\t"
    "v_lshl_add_u32 v7, v6, 3, v7\n\t"
    "v_and_b32 v6, 8, v20\n\t"
    "v_mul_u32_u24 v6, 1160, v6\n\t"
    "v_add3_u32 v7, v7, v6, %[ra]\n\t"
    "3:\n\t"
    "v_mov_b32 v8, 0\n\t"
    "v_mov_b32 v9, 0\n\t"
    "v_mov_b32 v10, 0\n\t"
    "v_mov_b32 v11, 0\n\t"
    "v_mov_b32 v12, 0\n\t"
    "v_mov_b32 v13, 0\n\t"
    "v_mov_b32 v14, 0\n\t"
    "v_mov_b32 v15, 0\n\t"
    "v_mov_b32 v16, 0\n\t"
    "v_mov_b32 v17, 0\n\t"
    "v_mov_b32 v18, 0\n\t"
    "v_mov_b32 v19, 0\n\t"
    "v_mov_b32 v20, 0\n\t"
    "v_mov_b32 v21, 0\n\t"
    "v_mov_b32 v22, 0\n\t"
    "v_mov_b32 v23, 0\n\t"
    "s_waitcnt vmcnt(0) lgkmcnt(0)\n\t"
    "v_smfmac_f32_16x16x64_bf16 v[8:11], v[24:27], v[64:71], v4\n\t"
    "v_smfmac_f32_16x16x64_bf16 v[8:11], v[28:31], v[72:79], v4\n\t"
    "v_max3_u32 v6, v24, v25, v26\n\t"
    "v_max3_u32 v6, v6, v27, v28\n\t"
    "v_smfmac_f32_16x16x64_bf16 v[12:15], v[24:27], v[80:87], v4\n\t"
    "v_max3_u32 v6, v6, v29, v30\n\t"
    "v_max_u32 v6, v6, v31\n\t"
    "v_smfmac_f32_16x16x64_bf16 v[12:15], v[28:31], v[88:95], v4\n\t"
    "v_cmp_eq_u32 vcc, -1, v6\n\t"
    "s_cbranch_vccnz 1f\n\t"
    "2:\n\t"
    "v_smfmac_f32_16x16x64_bf16 v[16:19], v[24:27], v[96:103], v4\n\t"
    "v_smfmac_f32_16x16x64_bf16 v[16:19], v[28:31], v[104:111], v4\n\t"
    "v_add_f32 v8, v8, v10\n\t"
    "v_add_f32 v9, v9, v11\n\t"
    "ds_write2_b32 v5, v8, v9 offset0:0 offset1:72\n\t"
    "v_smfmac_f32_16x16x64_bf16 v[20:23], v[24:27], v[112:119], v4\n\t"
    "v_smfmac_f32_16x16x64_bf16 v[20:23], v[28:31], v[120:127], v4\n\t"
    "v_add_f32 v12, v12, v14\n\t"
    "v_add_f32 v13, v13, v15\n\t"
    "ds_write2_b32 v5, v12, v13 offset0:16 offset1:88\n\t"
    "v_add_f32 v16, v16, v18\n\t"
    "v_add_f32 v17, v17, v19\n\t"
    "ds_write2_b32 v5, v16, v17 offset0:32 offset1:104\n\t"
    "s_nop 1\n\t"
    "v_add_f32 v20, v20, v22\n\t"
    "v_add_f32 v21, v21, v23\n\t"
    "ds_write2_b32 v5, v20, v21 offset0:48 offset1:120\n\t"
    "v_mov_b32 v8, 0\n\t"
    "v_mov_b32 v9, 0\n\t"
    "v_mov_b32 v10, 0\n\t"
    "v_mov_b32 v11, 0\n\t"
    "v_mov_b32 v12, 0\n\t"
    "v_mov_b32 v13, 0\n\t"
    "v_mov_b32 v14, 0\n\t"
    "v_mov_b32 v15, 0\n\t"
    "v_mbcnt_lo_u32_b32 v5, -1, 0\n\t"
    "v_mbcnt_hi_u32_b32 v5, -1, v5\n\t"
    "s_waitcnt lgkmcnt(0)\n\t"
    "s_barrier\n\t"
    "ds_read_b64 v[16:17], v7 offset:0\n\t"
    "ds_read_b64 v[18:19], v7 offset:2304\n\t"
    "ds_read_b64 v[20:21], v7 offset:4608\n\t"
    "ds_read_b64 v[22:23], v7 offset:6912\n\t"
    "v_smfmac_f32_16x16x64_bf16 v[8:11], v[24:27], v[32:39], v4\n\t"
    "v_smfmac_f32_16x16x64_bf16 v[8:11], v[28:31], v[40:47], v4\n\t"
    "v_smfmac_f32_16x16x64_bf16 v[12:15], v[24:27], v[48:55], v4\n\t"
    "v_smfmac_f32_16x16x64_bf16 v[12:15], v[28:31], v[56:63], v4\n\t"
    "v_and_b32 v6, 48, v5\n\t"
    "v_and_b32 v7, 7, v5\n\t"
    "v_lshlrev_b32 v7, 2, v7\n\t"
    "v_lshl_or_b32 v7, v6, 1, v7\n\t"
    "s_waitcnt lgkmcnt(2)\n\t"
    "v_pk_add_f32 v[16:17], v[16:17], v[18:19]\n\t"
    "s_waitcnt lgkmcnt(0)\n\t"
    "v_pk_add_f32 v[20:21], v[20:21], v[22:23]\n\t"
    "v_pk_add_f32 v[16:17], v[16:17], v[20:21]\n\t"
    "s_nop 1\n\t"
    "v_add_f32_dpp v16, v16, v16 row_ror:8 row_mask:0xf bank_mask:0xf\n\t"
    "v_add_f32_dpp v17, v17, v17 row_ror:8 row_mask:0xf bank_mask:0xf\n\t"
    "v_cvt_pk_bf16_f32 v6, v16, v17\n\t"
    "v_min_u32 v6, -2, v6\n\t"
    "s_mov_b64 exec, %[em]\n\t"
    "global_store_dword v7, v6, %[kd] sc1\n\t"
    "s_mov_b64 exec, -1\n\t"
    "v_lshl_add_u32 v16, v5, 3, %[wb]\n\t"
    "v_add_f32 v8, v8, v10\n\t"
    "v_add_f32 v9, v9, v11\n\t"
    "v_add_f32 v12, v12, v14\n\t"
    "v_add_f32 v13, v13, v15\n\t"
    "ds_write2st64_b64 v16, v[8:9], v[12:13] offset1:1\n\t"
    "v_and_b32 v6, 8, v5\n\t"
    "v_mul_u32_u24 v6, 528, v6\n\t"
    "v_lshl_add_u32 v5, v7, 1, v6\n\t"
    "v_add_u32 v5, %[rb], v5\n\t"
    "s_cmp_ge_u32 %[wb], 153728\n\t"
    "s_waitcnt lgkmcnt(0)\n\t"
    "s_barrier\n\t"
    "s_cbranch_scc1 8f\n\t"
    "ds_read_b64 v[16:17], v5 offset:0\n\t"
    "ds_read_b64 v[18:19], v5 offset:1024\n\t"
    "ds_read_b64 v[20:21], v5 offset:2048\n\t"
    "ds_read_b64 v[22:23], v5 offset:3072\n\t"
    "s_waitcnt lgkmcnt(2)\n\t"
    "v_pk_add_f32 v[16:17], v[16:17], v[18:19]\n\t"
    "s_waitcnt lgkmcnt(0)\n\t"
    "v_pk_add_f32 v[20:21], v[20:21], v[22:23]\n\t"
    "v_pk_add_f32 v[16:17], v[16:17], v[20:21]\n\t"
    "s_nop 1\n\t"
    "v_add_f32_dpp v16, v16, v16 row_ror:8 row_mask:0xf bank_mask:0xf\n\t"
    "v_add_f32_dpp v17, v17, v17 row_ror:8 row_mask:0xf bank_mask:0xf\n\t"
    "v_cvt_pk_bf16_f32 v6, v16, v17\n\t"
    "v_min_u32 v6, -2, v6\n\t"
    "s_mov_b64 exec, %[em]\n\t"
    "global_store_dword v7, v6, %[vd] sc1\n\t"
    "s_mov_b64 exec, -1\n\t"
    "8:\n\t"
    "s_setprio 2\n\t"
    "s_waitcnt lgkmcnt(0)\n\t"
    "s_branch 9f\n\t"
    "1:\n\t"
    "s_add_u32 %[sp], %[sp], 1\n\t"
    "s_cmp_ge_u32 %[sp], %[cap]\n\t"
    "s_cbranch_scc1 7f\n\t"
    "v_mbcnt_lo_u32_b32 v20, -1, 0\n\t"
    "v_mbcnt_hi_u32_b32 v20, -1, v20\n\t"
    "v_lshlrev_b32 v21, 4, v20\n\t"
    "s_nop 0\n\t"
    "global_load_dwordx4 v[24:27], v21, %[ag] sc1\n\t"
    "global_load_dwordx4 v[28:31], v21, %[ag] offset:1024 sc1\n\t"
    "s_waitcnt lgkmcnt(0)\n\t"
    "s_memtime %[tc]\n\t"
    "s_nop 7\n\t"
    "s_branch 3b\n\t"
    "7:\n\t"
    "s_mov_b32 %[cap], 0\n\t"
    "s_branch 2b\n\t"
    "9:"
    : [tc] "=&s"(tc), [sp] "=&s"(sp), [cap] "+s"(cap)
    : [ag] "s"(ag), [kd] "s"(kd), [vd] "s"(vd), [wl] "s"(wl), [wa] "s"(wa), [wb] "s"(wb), [ra] "s"(ra), [rb] "s"(rb), [c44] "s"(0x4444u), [em] "s"(0x00FF00FF00FF00FFull), "{v[64:95]}"(W.k0), "{v[96:127]}"(W.k1), "{v[128:159]}"(W.o0), "{v[160:191]}"(W.o1), "{v[192:223]}"(W.o2), "{v[224:255]}"(W.o3)
    : "v4", "v5", "v6", "v7", "v8", "v9", "v10", "v11", "v12", "v13", "v14", "v15", "v16", "v17", "v18", "v19", "v20", "v21", "v22", "v23", "v24", "v25", "v26", "v27", "v28", "v29", "v30", "v31", "v32", "v33", "v34", "v35", "v36", "v37", "v38", "v39", "v40", "v41", "v42", "v43", "v44", "v45", "v46", "v47", "v48", "v49", "v50", "v51", "v52", "v53", "v54", "v55", "v56", "v57", "v58", "v59", "v60", "v61", "v62", "v63", "vcc", "scc", "memory");
}
__device__ __forceinline__ void vr_r(const VRW& W, const char* os, const char* ad, unsigned wa, unsigned& cap, unsigned& sp, unsigned long long* ts) {
  asm volatile("v_mbcnt_lo_u32_b32 v4, -1, 0\n\t"
    "v_mbcnt_hi_u32_b32 v4, -1, v4\n\t"
    "v_lshlrev_b32 v5, 4, v4\n\t"
    "global_load_dwordx4 v[12:15], v5, %[os] offset:0 sc1\n\t"
    "global_load_dwordx4 v[16:19], v5, %[os] offset:1024 sc1\n\t"
    "global_load_dwordx4 v[20:23], v5, %[os] offset:2048 sc1\n\t"
    "global_load_dwordx4 v[24:27], v5, %[os] offset:3072 sc1\n\t"
    "s_mov_b32 %[sp], 0\n\t"
    "v_and_b32 v10, 1, v4\n\t"
    "v_mul_u32_u24 v10, 0xaaaa, v10\n\t"
    "v_or_b32 v6, %[c44], v10\n\t"
    "v_and_b32 v10, 15, v4\n\t"
    "v_lshl_add_u32 v7, v10, 2, %[wa]\n\t"
    "v_and_b32 v10, 48, v4\n\t"
    "v_mad_u32_u24 v7, v10, 12, v7\n\t"
    "v_and_b32 v10, 16, v4\n\t"
    "v_mul_u32_u24 v8, 24, v10\n\t"
    "v_and_b32 v10, 4, v4\n\t"
    "v_mad_u32_u24 v8, v10, 48, v8\n\t"
    "v_and_b32 v10, 32, v4\n\t"
    "v_lshl_add_u32 v8, v10, 1, v8\n\t"
    "v_and_b32 v10, 8, v4\n\t"
    "v_lshl_add_u32 v8, v10, 2, v8\n\t"
    "v_and_b32 v10, 3, v4\n\t"
    "v_lshl_add_u32 v8, v10, 3, v8\n\t"
    "v_add_u32 v8, 0, v8\n\t"
    "v_and_b32 v10, 32, v4\n\t"
    "v_add_u32 v9, v10, v4\n\t"
    "v_lshlrev_b32 v9, 2, v9\n\t"
    "3:\n\t"
    "v_mov_b32 v28, 0\n\t"
    "v_mov_b32 v29, 0\n\t"
    "v_mov_b32 v30, 0\n\t"
    "v_mov_b32 v31, 0\n\t"
    "v_mov_b32 v32, 0\n\t"
    "v_mov_b32 v33, 0\n\t"
    "v_mov_b32 v34, 0\n\t"
    "v_mov_b32 v35, 0\n\t"
    "v_mov_b32 v36, 0\n\t"
    "v_mov_b32 v37, 0\n\t"
    "v_mov_b32 v38, 0\n\t"
    "v_mov_b32 v39, 0\n\t"
    "v_mov_b32 v40, 0\n\t"
    "v_mov_b32 v41, 0\n\t"
    "v_mov_b32 v42, 0\n\t"
    "v_mov_b32 v43, 0\n\t"
    "v_mov_b32 v10, 0\n\t"
    "s_waitcnt vmcnt(3)\n\t"
    "v_smfmac_f32_16x16x64_bf16 v[28:31], v[12:15], v[128:135], v6\n\t"
    "v_max3_u32 v10, v10, v12, v13\n\t"
    "v_smfmac_f32_16x16x64_bf16 v[32:35], v[12:15], v[136:143], v6\n\t"
    "v_max3_u32 v10, v10, v14, v15\n\t"
    "v_smfmac_f32_16x16x64_bf16 v[36:39], v[12:15], v[144:151], v6\n\t"
    "v_smfmac_f32_16x16x64_bf16 v[40:43], v[12:15], v[152:159], v6\n\t"
    "s_waitcnt vmcnt(2)\n\t"
    "v_smfmac_f32_16x16x64_bf16 v[28:31], v[16:19], v[160:167], v6\n\t"
    "v_max3_u32 v10, v10, v16, v17\n\t"
    "v_smfmac_f32_16x16x64_bf16 v[32:35], v[16:19], v[168:175], v6\n\t"
    "v_max3_u32 v10, v10, v18, v19\n\t"
    "v_smfmac_f32_16x16x64_bf16 v[36:39], v[16:19], v[176:183], v6\n\t"
    "v_smfmac_f32_16x16x64_bf16 v[40:43], v[16:19], v[184:191], v6\n\t"
    "s_waitcnt vmcnt(1)\n\t"
    "v_smfmac_f32_16x16x64_bf16 v[28:31], v[20:23], v[192:199], v6\n\t"
    "v_max3_u32 v10, v10, v20, v21\n\t"
    "v_smfmac_f32_16x16x64_bf16 v[32:35], v[20:23], v[200:207], v6\n\t"
    "v_max3_u32 v10, v10, v22, v23\n\t"
    "v_smfmac_f32_16x16x64_bf16 v[36:39], v[20:23], v[208:215], v6\n\t"
    "v_smfmac_f32_16x16x64_bf16 v[40:43], v[20:23], v[216:223], v6\n\t"
    "s_waitcnt vmcnt(0)\n\t"
    "v_smfmac_f32_16x16x64_bf16 v[28:31], v[24:27], v[224:231], v6\n\t"
    "v_max3_u32 v10, v10, v24, v25\n\t"
    "v_smfmac_f32_16x16x64_bf16 v[32:35], v[24:27], v[232:239], v6\n\t"
    "v_max3_u32 v10, v10, v26, v27\n\t"
    "v_smfmac_f32_16x16x64_bf16 v[36:39], v[24:27], v[240:247], v6\n\t"
    "v_cmp_eq_u32 vcc, -1, v10\n\t"
    "v_smfmac_f32_16x16x64_bf16 v[40:43], v[24:27], v[248:255], v6\n\t"
    "s_cbranch_vccnz 4f\n\t"
    "5:\n\t"
    "s_cmp_ge_u32 %[wa], 1536\n\t"
    "s_nop 1\n\t"
    "v_add_f32 v28, v28, v29\n\t"
    "v_add_f32 v32, v32, v33\n\t"
    "v_add_f32 v36, v38, v39\n\t"
    "v_add_f32 v28, v28, v36\n\t"
    "v_add_f32 v40, v42, v43\n\t"
    "v_add_f32 v32, v32, v40\n\t"
    "ds_write2_b32 v7, v28, v32 offset1:16\n\t"
    "s_waitcnt lgkmcnt(0)\n\t"
    "s_barrier\n\t"
    "s_cbranch_scc1 9f\n\t"
    "ds_read_b64 v[44:45], v8 offset:0\n\t"
    "ds_read_b64 v[46:47], v8 offset:768\n\t"
    "ds_read_b64 v[48:49], v8 offset:1536\n\t"
    "ds_read_b64 v[50:51], v8 offset:2304\n\t"
    "ds_read_b64 v[52:53], v8 offset:3072\n\t"
    "ds_read_b64 v[54:55], v8 offset:3840\n\t"
    "ds_read_b64 v[56:57], v8 offset:4608\n\t"
    "ds_read_b64 v[58:59], v8 offset:5376\n\t"
    "s_waitcnt lgkmcnt(6)\n\t"
    "v_pk_add_f32 v[44:45], v[44:45], v[46:47]\n\t"
    "s_waitcnt lgkmcnt(4)\n\t"
    "v_pk_add_f32 v[48:49], v[48:49], v[50:51]\n\t"
    "v_pk_add_f32 v[44:45], v[44:45], v[48:49]\n\t"
    "s_waitcnt lgkmcnt(2)\n\t"
    "v_pk_add_f32 v[52:53], v[52:53], v[54:55]\n\t"
    "s_waitcnt lgkmcnt(0)\n\t"
    "v_pk_add_f32 v[56:57], v[56:57], v[58:59]\n\t"
    "v_pk_add_f32 v[52:53], v[52:53], v[56:57]\n\t"
    "v_pk_add_f32 v[44:45], v[44:45], v[52:53]\n\t"
    "v_cvt_pk_bf16_f32 v10, v44, v45\n\t"
    "v_min_u32 v10, -2, v10\n\t"
    "global_store_dword v9, v10, %[ad] sc1\n\t"
    "s_waitcnt lgkmcnt(0)\n\t"
    "s_branch 9f\n\t"
    "4:\n\t"
    "s_add_u32 %[sp], %[sp], 1\n\t"
    "s_cmp_ge_u32 %[sp], %[cap]\n\t"
    "s_cbranch_scc1 7f\n\t"
    "global_load_dwordx4 v[12:15], v5, %[os] offset:0 sc1\n\t"
    "global_load_dwordx4 v[16:19], v5, %[os] offset:1024 sc1\n\t"
    "global_load_dwordx4 v[20:23], v5, %[os] offset:2048 sc1\n\t"
    "global_load_dwordx4 v[24:27], v5, %[os] offset:3072 sc1\n\t"
    "s_nop 3\n\t"
    "s_branch 3b\n\t"
    "7:\n\t"
    "s_mov_b32 %[cap], 0\n\t"
    "s_branch 5b\n\t"
    "9:"
    : [sp] "=&s"(sp), [cap] "+s"(cap)
    : [os] "s"(os), [ad] "s"(ad), [wa] "s"(wa), [c44] "s"(0x4444u), "{v[64:95]}"(W.k0), "{v[96:127]}"(W.k1), "{v[128:159]}"(W.o0), "{v[160:191]}"(W.o1), "{v[192:223]}"(W.o2), "{v[224:255]}"(W.o3)
    : "v4", "v5", "v6", "v7", "v8", "v9", "v10", "v11", "v12", "v13", "v14", "v15", "v16", "v17", "v18", "v19", "v20", "v21", "v22", "v23", "v24", "v25", "v26", "v27", "v28", "v29", "v30", "v31", "v32", "v33", "v34", "v35", "v36", "v37", "v38", "v39", "v40", "v41", "v42", "v43", "v44", "v45", "v46", "v47", "v48", "v49", "v50", "v51", "v52", "v53", "v54", "v55", "v56", "v57", "v58", "v59", "vcc", "scc", "memory");
}
__device__ __forceinline__ void kr_load_k0(VRW& W, unsigned a) {
  asm volatile("ds_read_b64_tr_b16 v[64:65], %[a] offset:0\n\t"
    "ds_read_b64_tr_b16 v[66:67], %[a] offset:256\n\t"
    "ds_read_b64_tr_b16 v[68:69], %[a] offset:4096\n\t"
    "ds_read_b64_tr_b16 v[70:71], %[a] offset:4352\n\t"
    "ds_read_b64_tr_b16 v[80:81], %[a] offset:32\n\t"
    "ds_read_b64_tr_b16 v[82:83], %[a] offset:288\n\t"
    "ds_read_b64_tr_b16 v[84:85], %[a] offset:4128\n\t"
    "ds_read_b64_tr_b16 v[86:87], %[a] offset:4384\n\t"
    "s_waitcnt lgkmcnt(0)\n\t"
    "ds_read_b64_tr_b16 v[96:97], %[a] offset:64\n\t"
    "ds_read_b64_tr_b16 v[98:99], %[a] offset:320\n\t"
    "ds_read_b64_tr_b16 v[100:101], %[a] offset:4160\n\t"
    "ds_read_b64_tr_b16 v[102:103], %[a] offset:4416\n\t"
    "ds_read_b64_tr_b16 v[112:113], %[a] offset:96\n\t"
    "ds_read_b64_tr_b16 v[114:115], %[a] offset:352\n\t"
    "ds_read_b64_tr_b16 v[116:117], %[a] offset:4192\n\t"
    "ds_read_b64_tr_b16 v[118:119], %[a] offset:4448\n\t"
    "s_waitcnt lgkmcnt(0)"
    : "=&{v[64:95]}"(W.k0), "=&{v[96:127]}"(W.k1) : [a] "v"(a) : "memory");
}
__device__ __forceinline__ void kr_load_k1(VRW& W, unsigned a) {
  asm volatile("ds_read_b64_tr_b16 v[72:73], %[a] offset:0\n\t"
    "ds_read_b64_tr_b16 v[74:75], %[a] offset:256\n\t"
    "ds_read_b64_tr_b16 v[76:77], %[a] offset:4096\n\t"
    "ds_read_b64_tr_b16 v[78:79], %[a] offset:4352\n\t"
    "ds_read_b64_tr_b16 v[88:89], %[a] offset:32\n\t"
    "ds_read_b64_tr_b16 v[90:91], %[a] offset:288\n\t"
    "ds_read_b64_tr_b16 v[92:93], %[a] offset:4128\n\t"
    "ds_read_b64_tr_b16 v[94:95], %[a] offset:4384\n\t"
    "s_waitcnt lgkmcnt(0)\n\t"
    "ds_read_b64_tr_b16 v[104:105], %[a] offset:64\n\t"
    "ds_read_b64_tr_b16 v[106:107], %[a] offset:320\n\t"
    "ds_read_b64_tr_b16 v[108:109], %[a] offset:4160\n\t"
    "ds_read_b64_tr_b16 v[110:111], %[a] offset:4416\n\t"
    "ds_read_b64_tr_b16 v[120:121], %[a] offset:96\n\t"
    "ds_read_b64_tr_b16 v[122:123], %[a] offset:352\n\t"
    "ds_read_b64_tr_b16 v[124:125], %[a] offset:4192\n\t"
    "ds_read_b64_tr_b16 v[126:127], %[a] offset:4448\n\t"
    "s_waitcnt lgkmcnt(0)"
    : "+&{v[64:95]}"(W.k0), "+&{v[96:127]}"(W.k1) : [a] "v"(a) : "memory");
}
__device__ __forceinline__ void kr_load_o0(VRW& W, unsigned a) {
  asm volatile("ds_read_b64_tr_b16 v[128:129], %[a] offset:0\n\t"
    "ds_read_b64_tr_b16 v[130:131], %[a] offset:128\n\t"
    "ds_read_b64_tr_b16 v[132:133], %[a] offset:1024\n\t"
    "ds_read_b64_tr_b16 v[134:135], %[a] offset:1152\n\t"
    "ds_read_b64_tr_b16 v[136:137], %[a] offset:32\n\t"
    "ds_read_b64_tr_b16 v[138:139], %[a] offset:160\n\t"
    "ds_read_b64_tr_b16 v[140:141], %[a] offset:1056\n\t"
    "ds_read_b64_tr_b16 v[142:143], %[a] offset:1184\n\t"
    "s_waitcnt lgkmcnt(0)\n\t"
    "ds_read_b64_tr_b16 v[144:145], %[a] offset:4096\n\t"
    "ds_read_b64_tr_b16 v[146:147], %[a] offset:4224\n\t"
    "ds_read_b64_tr_b16 v[148:149], %[a] offset:5120\n\t"
    "ds_read_b64_tr_b16 v[150:151], %[a] offset:5248\n\t"
    "ds_read_b64_tr_b16 v[152:153], %[a] offset:4128\n\t"
    "ds_read_b64_tr_b16 v[154:155], %[a] offset:4256\n\t"
    "ds_read_b64_tr_b16 v[156:157], %[a] offset:5152\n\t"
    "ds_read_b64_tr_b16 v[158:159], %[a] offset:5280\n\t"
    "s_waitcnt lgkmcnt(0)"
    : "=&{v[128:159]}"(W.o0) : [a] "v"(a) : "memory");
}
__device__ __forceinline__ void kr_load_o1(VRW& W, unsigned a) {
  asm volatile("ds_read_b64_tr_b16 v[160:161], %[a] offset:0\n\t"
    "ds_read_b64_tr_b16 v[162:163], %[a] offset:128\n\t"
    "ds_read_b64_tr_b16 v[164:165], %[a] offset:1024\n\t"
    "ds_read_b64_tr_b16 v[166:167], %[a] offset:1152\n\t"
    "ds_read_b64_tr_b16 v[168:169], %[a] offset:32\n\t"
    "ds_read_b64_tr_b16 v[170:171], %[a] offset:160\n\t"
    "ds_read_b64_tr_b16 v[172:173], %[a] offset:1056\n\t"
    "ds_read_b64_tr_b16 v[174:175], %[a] offset:1184\n\t"
    "s_waitcnt lgkmcnt(0)\n\t"
    "ds_read_b64_tr_b16 v[176:177], %[a] offset:4096\n\t"
    "ds_read_b64_tr_b16 v[178:179], %[a] offset:4224\n\t"
    "ds_read_b64_tr_b16 v[180:181], %[a] offset:5120\n\t"
    "ds_read_b64_tr_b16 v[182:183], %[a] offset:5248\n\t"
    "ds_read_b64_tr_b16 v[184:185], %[a] offset:4128\n\t"
    "ds_read_b64_tr_b16 v[186:187], %[a] offset:4256\n\t"
    "ds_read_b64_tr_b16 v[188:189], %[a] offset:5152\n\t"
    "ds_read_b64_tr_b16 v[190:191], %[a] offset:5280\n\t"
    "s_waitcnt lgkmcnt(0)"
    : "=&{v[160:191]}"(W.o1) : [a] "v"(a) : "memory");
}
__device__ __forceinline__ void kr_load_o2(VRW& W, unsigned a) {
  asm volatile("ds_read_b64_tr_b16 v[192:193], %[a] offset:0\n\t"
    "ds_read_b64_tr_b16 v[194:195], %[a] offset:128\n\t"
    "ds_read_b64_tr_b16 v[196:197], %[a] offset:1024\n\t"
    "ds_read_b64_tr_b16 v[198:199], %[a] offset:1152\n\t"
    "ds_read_b64_tr_b16 v[200:201], %[a] offset:32\n\t"
    "ds_read_b64_tr_b16 v[202:203], %[a] offset:160\n\t"
    "ds_read_b64_tr_b16 v[204:205], %[a] offset:1056\n\t"
    "ds_read_b64_tr_b16 v[206:207], %[a] offset:1184\n\t"
    "s_waitcnt lgkmcnt(0)\n\t"
    "ds_read_b64_tr_b16 v[208:209], %[a] offset:4096\n\t"
    "ds_read_b64_tr_b16 v[210:211], %[a] offset:4224\n\t"
    "ds_read_b64_tr_b16 v[212:213], %[a] offset:5120\n\t"
    "ds_read_b64_tr_b16 v[214:215], %[a] offset:5248\n\t"
    "ds_read_b64_tr_b16 v[216:217], %[a] offset:4128\n\t"
    "ds_read_b64_tr_b16 v[218:219], %[a] offset:4256\n\t"
    "ds_read_b64_tr_b16 v[220:221], %[a] offset:5152\n\t"
    "ds_read_b64_tr_b16 v[222:223], %[a] offset:5280\n\t"
    "s_waitcnt lgkmcnt(0)"
    : "=&{v[192:223]}"(W.o2) : [a] "v"(a) : "memory");
}
__device__ __forceinline__ void kr_load_o3(VRW& W, unsigned a) {
  asm volatile("ds_read_b64_tr_b16 v[224:225], %[a] offset:0\n\t"
    "ds_read_b64_tr_b16 v[226:227], %[a] offset:128\n\t"
    "ds_read_b64_tr_b16 v[228:229], %[a] offset:1024\n\t"
    "ds_read_b64_tr_b16 v[230:231], %[a] offset:1152\n\t"
    "ds_read_b64_tr_b16 v[232:233], %[a] offset:32\n\t"
    "ds_read_b64_tr_b16 v[234:235], %[a] offset:160\n\t"
    "ds_read_b64_tr_b16 v[236:237], %[a] offset:1056\n\t"
    "ds_read_b64_tr_b16 v[238:239], %[a] offset:1184\n\t"
    "s_waitcnt lgkmcnt(0)\n\t"
    "ds_read_b64_tr_b16 v[240:241], %[a] offset:4096\n\t"
    "ds_read_b64_tr_b16 v[242:243], %[a] offset:4224\n\t"
    "ds_read_b64_tr_b16 v[244:245], %[a] offset:5120\n\t"
    "ds_read_b64_tr_b16 v[246:247], %[a] offset:5248\n\t"
    "ds_read_b64_tr_b16 v[248:249], %[a] offset:4128\n\t"
    "ds_read_b64_tr_b16 v[250:251], %[a] offset:4256\n\t"
    "ds_read_b64_tr_b16 v[252:253], %[a] offset:5152\n\t"
    "ds_read_b64_tr_b16 v[254:255], %[a] offset:5280\n\t"
    "s_waitcnt lgkmcnt(0)"
    : "=&{v[224:255]}"(W.o3) : [a] "v"(a) : "memory");
}
#define XPTRS(l) const unsigned par_ = ini_resolve(ws, l, tag); char* xch_ = xch_base(ws); \
  const char* xb = uni(xch_ + ((size_t)3 << 21) + (((par_ >> 1) & 1) << 13)); const char* ob = uni(xch_ + ((par_ & 1) << 13)); \
  unsigned* cnt = (unsigned*)(xch_ + ((size_t)4 << 21) + ((par_ >> 2) << 13))
__device__ __forceinline__ void qk_body(char* smem, int wave_id_, int xg, int h, int wg, const u16* __restrict__ Wq, const u16* __restrict__ Wv,
                                        char* ws, unsigned* tmo, unsigned long long tag) {
  const int wave = WAVE_ID;
  const int ty = wave >> 2, w4 = wave & 3;
  const char* wq = uni((const char*)(Wq + h * DH)); const char* wv = uni((const char*)(Wv + h * DH));
  auto stage2 = [&](int i) {
    const int tid = wave * 64 + lane_id();
    if (i < 6) {
      stage_proj<false>(wq + (size_t)i * 128 * HD * 2, Q_RING + ((2 * i) & 3) * 32768, tid, wave);
      if (i < 2) stage_proj<false>(wq + (size_t)(i + 6) * 128 * HD * 2, Q_RING + ((2 * i + 1) & 3) * 32768, tid, wave);
      else stage_proj<true>(wv + (size_t)(i - 2) * 256 * HD * 2, Q_RING + ((2 * i + 1) & 3) * 32768, tid, wave);
    }
  };
  QKW W;
  delay64<QDL>();
  stage2(0); stage2(1);
#define WLD(i, w) do { if ((i) < 5) WAIT_V(8); else WAIT_V(0); \
    WBAR(); { const int l_ = lane_id(); const unsigned o_ = Q_RING + ty * 32768 + ((i) & 1) * 65536; const bool v_ = ty && (i) >= 2; \
      qk_wload<i>(w, proj_base(v_ ? w4 : 2 * w4, l_) + o_, proj_base(v_ ? w4 + 4 : 2 * w4 + 1, l_) + o_); } \
    WBAR(); stage2((i) + 2); if (wave == 0) STS(ws, wg, 1 + (i)); if ((i) == 4 && wave == 7) ini_request(ws, 30720); } while (0)
  WLD(0, W.w0); WLD(1, W.w1); WLD(2, W.w2); WLD(3, W.w3); WLD(4, W.w4); WLD(5, W.w5);
#undef WLD
  XPTRS(smem + 30720);
  if (opaque_tid() == 64) st4_sc1(cnt + (PQKV + h) * 32 + 3, 1u);
  const char* oh = ob + (size_t)(h >> 2) * 16384 + (h & 3) * 1024;
  auto rearm = [&](int ps) {
    unsigned z = SENT; asm volatile("" : "+v"(z));
    const unsigned l16 = lane_id() * 16;
#pragma unroll
    for (int r = 0; r < 2 * OREP; ++r) st16s(oh + (size_t)(ps * 4 + (r >> 1)) * 131072, (r & 1) * 4096u + l16, u32x4{z, z, z, z});
  };
  const char* dbw = xb + (size_t)48 * 16384 + 4096 + h * 128;
  if (wave == 4) *(volatile LAS u32x4*)(smem + Q_TM) = u32x4{0u, 0u, 0u, 0u};
  if (wave == 6) *(u32x4*)(smem + Q_PQ + 4096 + lane_id() * 16) = u32x4{0u, 0u, 0u, 0u};
  BAR();
  if (wave == 0) STS(ws, wg, 0);
  unsigned avail = 0;
  auto stage_kv = [&](int s) {
    if (avail <= (unsigned)s) {
      for (int spins = 0;; ++spins) {
        avail = qkv_avail<0>(cnt + (PQKV + h) * 32);
        if (avail > (unsigned)s) break;
        __builtin_amdgcn_s_sleep(8);
        if (give_up(spins, tmo, 5)) break;
      }
    }
    head_to_lds(uni(ws + (wave == 5 ? OFF_K : (wave == 6 ? OFF_V : OFF_Q)) + ((size_t)s * NH + h) * 8192), lane_id(), Q_K + (s & 1) * 32768 + (wave - 5) * 8192);
  };
  auto loop = [&](auto w_c) __attribute__((always_inline)) {
    constexpr int WV = decltype(w_c)::value;
    if (WV >= 5) stage_kv(0);
    unsigned t0 = 0, cap = 60000, d0 = 105, d1 = 200;
    int dq = D_Q;
    int sl = 0;
    unsigned fc = 0;
    for (int s = 0; s < NSEG; ++s) {
      TSDECL;
      const char* ag = xb + (size_t)((sl * 4 + (AREP == 4 ? xg : xg >> 1)) * 2 + (WV >> 2)) * 16384 + (WV < 4 ? WV * 2048 : WV * 1024);
      const unsigned pb = (s & 1) * 32768;
      const char* vs = WV == 4 ? dbw : WV == 7 ? (const char*)(cnt + (PROG + h) * 32) : xb + (size_t)(24 + sl * 8 + (h >> 2)) * 16384 + (h & 3) * 2048;
      const char* v2 = xb + (size_t)(52 + sl * 4 + (h >> 3)) * 16384 + (h & 7) * 1024 + (WV & 1) * 512;
      const char* od = oh + (size_t)sl * 524288;
      const unsigned sn = (unsigned)(s + 1) << 24;
      if (s > 0) wait_rt(t0, dq);
      TS(0);
      unsigned long long tc; unsigned sp;
      qk_step<WV>(W, ag, vs, v2, od, pb, sn, cap, sp, tc, nullptr, d0, d1);
      dq = (sp & 0xFFFFu) ? min(dq + DQU, D_Q + DQM) : max(dq - DQD, D_Q);
      if (WV >= 5) {
        if (WV == 7) WAIT_V(0);
        const bool st0 = E_S >= 0 && s + 1 < NSEG && avail > (unsigned)(s + 1);
        if (st0) { delay64<(E_S > 0 ? E_S : 0)>(); stage_kv(s + 1); }
        if (RAE && WV == RAW && s > 0 && s < NSEG - 1) rearm(sl == 0 ? 2 : sl - 1);
        WBAR();
        if (!st0 && s + 1 < NSEG) stage_kv(s + 1);
      }
      if (!RAE && WV == RAW && s > 0 && s < NSEG - 1) rearm(sl == 0 ? 2 : sl - 1);
      unsigned long long* tsp = nullptr;
      seg_attn<WV>(W, pb, ws + OFF_O + ((size_t)s * 64 + 2 * h + (WV >> 1)) * 4096, tsp);
      if (WV == 1) TSFLUSH(0x8000);
      if (WV < 4 && s >= SPUB) {
        WAIT_V(0);
        if (lane_id() == 0 && (__hip_atomic_fetch_add((LAS unsigned*)(smem + Q_TM + 8), 1u, __ATOMIC_RELAXED, __HIP_MEMORY_SCOPE_WORKGROUP) & 3u) == 3u)
          st4_sc1(cnt + (PROG + h) * 32, (unsigned)(s + 1));
      }
      if (s < NSEG - 1) {
        if (WV == 4) {
          delay64<E_B>();
          t0 = bell_time(xb + (size_t)(48 + xg) * 16384 + 4096, (lane_id() & 31) * 128, (unsigned)(s + 1), cap);
          *(volatile LAS u32x2*)(smem + Q_TM) = u32x2{(unsigned)(s + 1), t0};
          TS(14);
          TSFLUSH(0x4000);
        } else {
          if (WV >= 5) delay64<PWL>();
          for (;;) {
            const u32x2 v = *(volatile LAS u32x2*)(smem + Q_TM);
            t0 = __builtin_amdgcn_readfirstlane(v.y);
            if (__builtin_amdgcn_readfirstlane(v.x) == (unsigned)(s + 1)) break;
            __builtin_amdgcn_s_sleep(1);
          }
        }
      }
      sl = sl == 2 ? 0 : sl + 1;
    }
    if (cap == 0 && lane_id() == 0) st4_sc1(tmo, 1u);
    FCS();
  };
  switch (wave) {
    case 0: loop(std::integral_constant<int, 0>{}); break;
    case 1: loop(std::integral_constant<int, 1>{}); break;
    case 2: loop(std::integral_constant<int, 2>{}); break;
    case 3: loop(std::integral_constant<int, 3>{}); break;
    case 4: loop(std::integral_constant<int, 4>{}); break;
    case 5: loop(std::integral_constant<int, 5>{}); break;
    case 6: loop(std::integral_constant<int, 6>{}); break;
    default: loop(std::integral_constant<int, 7>{}); break;
  }
  __builtin_amdgcn_s_setprio(0);
}

__device__ __forceinline__ void vr_body(char* smem, int wave_id_, int xg, int h, int c, int wg, const u16* __restrict__ Wk, const u16* __restrict__ Wv,
                                        const u16* __restrict__ Wo, char* ws, unsigned* tmo, unsigned long long tag) {
  const int wave = WAVE_ID;
  const unsigned po = (unsigned)(h >> 4) * 16384u + ((h >> 1) & 7) * 1024 + (h & 1) * 512 + c * 128;
  const unsigned ko_ = (unsigned)(24 + (h >> 2)) * 16384u + (h & 3) * 2048 + c * 1024;
  const unsigned vo_ = (unsigned)(52 + (h >> 3)) * 16384u + (h & 7) * 1024 + c * 512;
  const int hg = (wave + ROTW * ((2 * h + c) & 3)) & 7;
  VRW W;
  {
    const int lane = lane_id();
    const int g = lane >> 4, q4 = (lane & 15) >> 2, p4 = lane & 3, fr = 4 * g + (q4 & 1) + 32 * (q4 >> 1);
    const unsigned lw = wave * 16384;
    const char* sk = uni((const char*)(Wk + (size_t)(128 * wave) * HD + h * DH + 64 * c));
    const char* sv_ = uni((const char*)(Wv + (size_t)(128 * wave) * HD + h * DH + 64 + 32 * c));
    const char* so = uni((const char*)(Wo + (size_t)(512 * hg) * DM + 32 * h));
    const int kp = lane >> 2, sr = 32 * (kp >> 3) + 16 * ((kp >> 2) & 1) + (kp & 3);
    auto issue = [&](int p) {
#pragma unroll
      for (int i = 0; i < 8; ++i) {
        const unsigned d = lw + (p & 1) * 8192 + i * 1024;
        if (p < 2) gldsw(sk + (size_t)(8 * p + i) * 8 * HD * 2, (unsigned)((lane >> 3) * HD * 2 + (lane & 7) * 16), d);
        else if (p < 6 && (NTM & 8)) gldsw(so + (size_t)(128 * (p - 2) + 64 * (i & 1) + 4 * (i >> 1)) * DM * 2, (unsigned)(sr * DM * 2 + p4 * 16), d);
        else if (p < 6) glds16s<0>(so + (size_t)(128 * (p - 2) + 64 * (i & 1) + 4 * (i >> 1)) * DM * 2, (unsigned)(sr * DM * 2 + p4 * 16), d);
        else if (NTM & 16) gldsw(sv_ + (size_t)i * 16 * HD * 2, (unsigned)(kp * HD * 2 + p4 * 16), d);
        else glds16s<0>(sv_ + (size_t)i * 16 * HD * 2, (unsigned)(kp * HD * 2 + p4 * 16), d);
      }
    };
#define VTS(p) do { } while (0)
    const int fm = 16 * (g >> 1) + 4 * (g & 1) + (q4 & 1) + 8 * (q4 >> 1);
    const unsigned ak = lw + fm * 128 + p4 * 8, ao = lw + fr * 64 + p4 * 8;
#define KB() do { if (KFB) WBAR(); } while (0)
    issue(2); KB(); issue(3);
    WAIT_V(8); VTS(1); kr_load_o0(W, ao); KB(); issue(4);
    WAIT_V(8); VTS(2); kr_load_o1(W, ao + 8192); KB(); issue(5);
    WAIT_V(8); VTS(3); kr_load_o2(W, ao); KB(); issue(0);
    WAIT_V(8); VTS(4); kr_load_o3(W, ao + 8192); KB(); issue(1);
    WAIT_V(8); VTS(5); kr_load_k0(W, ak); KB(); issue(6);
    WAIT_V(8); VTS(6); kr_load_k1(W, ak + 8192);
    if (wave == 0) ini_request(ws, V_PV);
    WAIT_V(0);
    u32x8 x[4];
#pragma unroll
    for (int j = 0; j < 4; ++j) {
      const unsigned a = lw + fm * 64 + p4 * 8 + (j & 1) * 4096 + (j >> 1) * 32;
      const u32x2 t0 = trread(smem, a), t1 = trread(smem, a + 128), t2 = trread(smem, a + 2048), t3 = trread(smem, a + 2176);
      x[j] = u32x8{t0.x, t0.y, t1.x, t1.y, t2.x, t2.y, t3.x, t3.y};
    }
    char* d = smem + lw + 8192 + lane * 16;
#pragma unroll
    for (int j = 0; j < 4; ++j) { *(u32x4*)(d + j * 2048) = u32x4{x[j][0], x[j][1], x[j][2], x[j][3]}; *(u32x4*)(d + j * 2048 + 1024) = u32x4{x[j][4], x[j][5], x[j][6], x[j][7]}; }
    VTS(7);
  }
  BAR();
  XPTRS(smem + V_PV);
  (void)cnt;
  auto rearm = [&](int ps) {
    const int lane = lane_id();
    unsigned z = SENT; asm volatile("" : "+v"(z));
    const u32x4 sv = u32x4{z, z, z, z};
    static_assert(AREP == 2, "");
    st16s(xb, lane < 32 ? (unsigned)(ps * 8 + (lane >> 4) * 2) * 16384u + po + ((lane >> 3) & 1) * 256 + (lane & 7) * 16 : (unsigned)ps * 65536u + vo_ + (lane & 31) * 16, sv);
    st16s(xb, (unsigned)ps * 131072u + ko_ + lane * 16, sv);
  };
  if (wave == 0) STS(ws, wg, 0);
  __builtin_amdgcn_s_setprio(2);
  {
    unsigned cap = 60000, tr = 0, fc = 0;
    int da = D_A, dd = D_O;
    int sl = 0;
    for (int s = 0; s < NSEG; ++s) {
      TSDECL;
      const int sn = sl == 2 ? 0 : sl + 1;
      const char* ag = xb + (size_t)((sl * 4 + (AREP == 4 ? xg : xg >> 1)) * 2 + (wave >> 2)) * 16384 + (wave & 3) * 2048;
      const char* kd = xb + (size_t)sl * 131072 + ko_ + wave * 128;
      const char* vd = xb + (size_t)sl * 65536 + vo_ + (wave & 3) * 128;
      const char* os = ob + (size_t)((sl * 4 + ORSEL) * 8 + hg) * 16384 + c * 4096;
      const char* ad = xb + (size_t)((sn * 4 + (wave & 3)) * 2) * 16384 + po;
      if (s > 0) { if (wave < AREP) delay64<E_AG - E_W>(); else delay64<E_AG>(); }
      TS(0);
      unsigned long long tc; unsigned sp;
      unsigned long long* tsp = nullptr;
      vr_v(W, ag, kd, vd, wave * 16384 + 8192, V_P1 + wave * 2304 + (wave >> 2) * 64, V_PV + wave * 1024 + (wave >> 2) * 128, V_P1 + (wave >> 1) * 32 + (wave & 1) * 1152,
           V_PV + (wave & 3) * 256, cap, sp, tc, tsp);
      TN(12, sp + 1);
      if (s > 3 && sp) fc += 1u;
      da = sp ? min(da + DQU, D_A + DQM) : max(da - DQD, D_A);
      if (s == NSEG - 1) break;
      if (wave == 7 && s > 0) rearm(sl == 0 ? 2 : sl - 1);
      wait_clk((unsigned)tc, E_O);
      TS(3);
      const unsigned tb = bell_time(xb + (size_t)(48 + xg) * 16384 + hg * 512, (lane_id() & 3) * 128, (unsigned)(s + 1), cap);
      tr = tb;
      wait_rt(tb, dd);
      TS(4);
      vr_r(W, os, ad, V_P2 + wave * 768, cap, sp, tsp);
      if (s > 3 && sp) fc += 0x10000u;
      dd = sp ? min(dd + DOU, D_O + DOM) : max(dd - DQD, D_O);
      TN(14, (sp & 0xFFFF) + 1);
      if (wave == 1) TSFLUSH(0x6078);
      sl = sn;
    }
    if (cap == 0 && lane_id() == 0) st4_sc1(tmo, 2u);
    FCS();
  }
  __builtin_amdgcn_s_setprio(0);
}
#define NCS 6
__device__ __forceinline__ int calibrate(char* ws, int i) {
  const int t = lane_id(), ci = (i & 15) % 3, w = (i >> 4) * 4 + (i & 15) / 3, chunk = ci == 0 ? 0 : ci + 2;
  char* base = xch_base(ws) + ((size_t)chunk << 21) + 127 * 16384 + w * 512;
  const char* sb[2] = {uni(base + (w & 1) * 8192), uni(base + (~w & 1) * 8192)};
  if ((t & 31) < 24) st16_sc1(base + (t >> 5) * 8192 + (t & 31) * 16, u32x4{SENT, SENT, SENT, SENT});
  unsigned v, best[2] = {1u << 30, 1u << 30};
  asm volatile("s_load_dword %0, %1, 384 glc\n\ts_waitcnt vmcnt(0) lgkmcnt(0)" : "=&s"(v) : "s"(sb[0]) : "memory");
#pragma unroll
  for (int r = 0; r < NCS; ++r) {
    unsigned long long t0, t1;
    asm volatile("s_memtime %0\n\ts_waitcnt lgkmcnt(0)\n\ts_load_dword %2, %3, %4 glc\n\ts_waitcnt lgkmcnt(0)\n\ts_memtime %1\n\ts_waitcnt lgkmcnt(0)"
                 : "=&s"(t0), "=&s"(t1), "=&s"(v) : "s"(sb[r & 1]), "i"((r >> 1) * 128) : "memory");
    best[r & 1] = min(best[r & 1], (unsigned)(t1 - t0));
  }
  return (w & 1) ? (int)best[0] - (int)best[1] : (int)best[1] - (int)best[0];
}
__device__ __forceinline__ void init_sums(char* smem, int t, int i, const u16* __restrict__ cvec, const u16* __restrict__ wmod) {
  const int cg = t & 15, r0 = t >> 4, k0 = (i >> 4) * 256 + r0 * 16;
  u32x4 w[16];
#pragma unroll
  for (int j = 0; j < 16; ++j) w[j] = ldnt<1>(wmod + (size_t)(k0 + j) * (6 * DM) + (i & 15) * 128 + cg * 8);
  const u32x4 c0 = *(const u32x4*)(cvec + k0), c1 = *(const u32x4*)(cvec + k0 + 8);
  f32x2 acc[4] = {f32x2{0, 0}, f32x2{0, 0}, f32x2{0, 0}, f32x2{0, 0}};
#pragma unroll
  for (int j = 0; j < 16; ++j) {
    const unsigned cw = j < 8 ? c0[(j >> 1) & 3] : c1[(j >> 1) & 3];
    const float c = (j & 1) ? bfhi(cw) : bflo(cw), sv = bfr(c / (1.0f + __expf(-c)));
#pragma unroll
    for (int e = 0; e < 4; ++e) acc[e] = pkfma(f32x2{sv, sv}, f32x2{bflo(w[j][e]), bfhi(w[j][e])}, acc[e]);
  }
  float* red = (float*)smem;
  *(f32x4*)(red + r0 * 128 + cg * 8) = f32x4{acc[0][0], acc[0][1], acc[1][0], acc[1][1]};
  *(f32x4*)(red + r0 * 128 + cg * 8 + 4) = f32x4{acc[2][0], acc[2][1], acc[3][0], acc[3][1]};
}
__device__ __forceinline__ void init_role(char* smem, int wave_id_, int i, const u16* __restrict__ mem0, char* ws, unsigned long long tag, unsigned vote) {
  const int t = opaque_tid();
  char* xch = xch_base(ws);
  if (t < 128) {
    const float* red = (const float*)smem;
    float s = 0;
#pragma unroll
    for (int r = 0; r < 16; ++r) s += red[r * 128 + t];
    st4_sc1(ws + OFF_MOD + ((i >> 4) * (2 * DM) + (i & 15) * 128 + t) * 4, __builtin_bit_cast(unsigned, s));
  }
  static_assert(OREP == 2 && AREP == 2, "");
  for (int b = i; b < 105; b += 64) {
    if (b < 101) {
      char* p = b < 48 ? xch + (size_t)((b >> 4) * 32 + (b & 15)) * 16384
              : (b < 56 ? xch + ((size_t)3 << 21) + (size_t)(8 + ((b - 48) >> 2) * 8 + (b & 3)) * 16384
              : (b < 96 ? xch + ((size_t)3 << 21) + (size_t)(b - 56 + 24) * 16384 : xch + ((size_t)4 << 21) + (size_t)(b - 96) * 16384));
      const unsigned v = b < 80 || (b >= 84 && b < 96) ? SENT : 0u;
#pragma unroll
      for (int j = 0; j < 2; ++j) st16_sc1(p + j * 8192 + t * 16, u32x4{v, v, v, v});
    } else {
      const int rep = (b - 101) & 1, par = (b - 101) >> 1;
      if (b == 101 && t == 0) st4_sc1(ws + OFF_TMO, 0u);
#pragma unroll
      for (int j = 0; j < 2; ++j) {
        const int u = t + 512 * j, lane = u & 63, g = lane >> 4, hf = (lane >> 1) & 1, row = ((lane >> 1) & 6) + (lane & 1);
        u32x4 v = *(const u32x4*)(mem0 + row * DM + 64 * (u >> 6) + 16 * g + 8 * hf);
        v.x = nosent(v.x); v.y = nosent(v.y); v.z = nosent(v.z); v.w = nosent(v.w);
        st16_sc1(xch + ((size_t)3 << 21) + (size_t)(rep * 2 + j) * 16384 + par * 8192 + t * 16, v);
      }
    }
  }
  if (wave_id_ == 7) IS(4);
  WAIT_V(0);
  BAR();
  if (wave_id_ == 7) IS(5);
  if (t == 0) st8_sc1(ws + OFF_INI + i * 128, u32x2{(unsigned)tag, ((unsigned)(tag >> 48) << 16) | vote});
}
#define GEMMX gemm7
#define CPF 128
#define PFD 2
#define PFW 0
#define NIMM 3
enum { T_NONE, T_QV, T_KR, T_WO, T_F1, T_F2, T_F2C, T_FIN, T_LN, T_SA };
__device__ __attribute__((aligned(16))) const unsigned ROLES[256][6] = {
  {0x1, 0x0, 0x0, 0x0, 0x0, 0x0},
  {0x11, 0x0, 0x0, 0x0, 0x0, 0x0},
  {0x21, 0x0, 0x0, 0x0, 0x0, 0x0},
  {0x31, 0x0, 0x0, 0x0, 0x0, 0x0},
  {0x41, 0x0, 0x0, 0x0, 0x0, 0x0},
  {0x51, 0x0, 0x0, 0x0, 0x0, 0x0},
  {0x61, 0x0, 0x0, 0x0, 0x0, 0x0},
  {0x71, 0x0, 0x0, 0x0, 0x0, 0x0},
  {0x81, 0x0, 0x0, 0x0, 0x0, 0x0},
  {0x91, 0x0, 0x0, 0x0, 0x0, 0x0},
  {0xa1, 0x0, 0x0, 0x0, 0x0, 0x0},
  {0xb1, 0x0, 0x0, 0x0, 0x0, 0x0},
  {0x2, 0x0, 0x0, 0x0, 0x0, 0x0},
  {0x12, 0x0, 0x0, 0x0, 0x0, 0x0},
  {0x22, 0x0, 0x0, 0x0, 0x0, 0x0},
  {0x32, 0x0, 0x0, 0x0, 0x0, 0x0},
  {0x42, 0x0, 0x0, 0x0, 0x0, 0x0},
  {0x52, 0x0, 0x0, 0x0, 0x0, 0x0},
  {0x62, 0x0, 0x0, 0x0, 0x0, 0x0},
  {0x72, 0x0, 0x0, 0x0, 0x0, 0x0},
  {0x82, 0x0, 0x0, 0x0, 0x0, 0x0},
  {0x92, 0x0, 0x0, 0x0, 0x0, 0x0},
  {0xa2, 0x0, 0x0, 0x0, 0x0, 0x0},
  {0xb2, 0x0, 0x0, 0x0, 0x0, 0x0},
  {0xc2, 0x0, 0x0, 0x0, 0x0, 0x0},
  {0xd2, 0x0, 0x0, 0x0, 0x0, 0x0},
  {0xe2, 0x0, 0x0, 0x0, 0x0, 0x0},
  {0xf2, 0x0, 0x0, 0x0, 0x0, 0x0},
  {0x102, 0x0, 0x0, 0x0, 0x0, 0x0},
  {0x112, 0x0, 0x0, 0x0, 0x0, 0x0},
  {0x122, 0x0, 0x0, 0x0, 0x0, 0x0},
  {0x132, 0x0, 0x0, 0x0, 0x0, 0x0},
  {0xc1, 0x0, 0x0, 0x0, 0x0, 0x0},
  {0xd1, 0x0, 0x0, 0x0, 0x0, 0x0},
  {0xe1, 0x0, 0x0, 0x0, 0x0, 0x0},
  {0xf1, 0x0, 0x0, 0x0, 0x0, 0x0},
  {0x101, 0x0, 0x0, 0x0, 0x0, 0x0},
  {0x111, 0x0, 0x0, 0x0, 0x0, 0x0},
  {0x121, 0x0, 0x0, 0x0, 0x0, 0x0},
  {0x131, 0x0, 0x0, 0x0, 0x0, 0x0},
  {0x142, 0x0, 0x0, 0x0, 0x0, 0x0},
  {0x152, 0x0, 0x0, 0x0, 0x0, 0x0},
  {0x162, 0x0, 0x0, 0x0, 0x0, 0x0},
  {0x172, 0x0, 0x0, 0x0, 0x0, 0x0},
  {0x182, 0x0, 0x0, 0x0, 0x0, 0x0},
  {0x192, 0x0, 0x0, 0x0, 0x0, 0x0},
  {0x1a2, 0x0, 0x0, 0x0, 0x0, 0x0},
  {0x1b2, 0x0, 0x0, 0x0, 0x0, 0x0},
  {0x1c2, 0x0, 0x0, 0x0, 0x0, 0x0},
  {0x1d2, 0x0, 0x0, 0x0, 0x0, 0x0},
  {0x1e2, 0x0, 0x0, 0x0, 0x0, 0x0},
  {0x1f2, 0x0, 0x0, 0x0, 0x0, 0x0},
  {0x202, 0x0, 0x0, 0x0, 0x0, 0x0},
  {0x212, 0x0, 0x0, 0x0, 0x0, 0x0},
  {0x222, 0x0, 0x0, 0x0, 0x0, 0x0},
  {0x232, 0x0, 0x0, 0x0, 0x0, 0x0},
  {0x242, 0x0, 0x0, 0x0, 0x0, 0x0},
  {0x252, 0x0, 0x0, 0x0, 0x0, 0x0},
  {0x262, 0x0, 0x0, 0x0, 0x0, 0x0},
  {0x272, 0x0, 0x0, 0x0, 0x0, 0x0},
  {0x282, 0x0, 0x0, 0x0, 0x0, 0x0},
  {0x292, 0x0, 0x0, 0x0, 0x0, 0x0},
  {0x2a2, 0x0, 0x0, 0x0, 0x0, 0x0},
  {0x2b2, 0x0, 0x0, 0x0, 0x0, 0x0},
  {0x141, 0x0, 0x0, 0x0, 0x0, 0x0},
  {0x151, 0x0, 0x0, 0x0, 0x0, 0x0},
  {0x161, 0x0, 0x0, 0x0, 0x0, 0x0},
  {0x171, 0x0, 0x0, 0x0, 0x0, 0x0},
  {0x181, 0x0, 0x0, 0x0, 0x0, 0x0},
  {0x191, 0x0, 0x0, 0x0, 0x0, 0x0},
  {0x1a1, 0x0, 0x0, 0x0, 0x0, 0x0},
  {0x1b1, 0x0, 0x0, 0x0, 0x0, 0x0},
  {0x1c1, 0x0, 0x0, 0x0, 0x0, 0x0},
  {0x1d1, 0x0, 0x0, 0x0, 0x0, 0x0},
  {0x1e1, 0x0, 0x0, 0x0, 0x0, 0x0},
  {0x1f1, 0x0, 0x0, 0x0, 0x0, 0x0},
  {0x2c2, 0x0, 0x0, 0x0, 0x0, 0x0},
  {0x2d2, 0x0, 0x0, 0x0, 0x0, 0x0},
  {0x2e2, 0x0, 0x0, 0x0, 0x0, 0x0},
  {0x2f2, 0x0, 0x0, 0x0, 0x0, 0x0},
  {0x302, 0x0, 0x0, 0x0, 0x0, 0x0},
  {0x312, 0x0, 0x0, 0x0, 0x0, 0x0},
  {0x322, 0x0, 0x0, 0x0, 0x0, 0x0},
  {0x332, 0x0, 0x0, 0x0, 0x0, 0x0},
  {0x342, 0x0, 0x0, 0x0, 0x0, 0x0},
  {0x352, 0x0, 0x0, 0x0, 0x0, 0x0},
  {0x362, 0x0, 0x0, 0x0, 0x0, 0x0},
  {0x372, 0x0, 0x0, 0x0, 0x0, 0x0},
  {0x382, 0x0, 0x0, 0x0, 0x0, 0x0},
  {0x392, 0x0, 0x0, 0x0, 0x0, 0x0},
  {0x3a2, 0x0, 0x0, 0x0, 0x0, 0x0},
  {0x3b2, 0x0, 0x0, 0x0, 0x0, 0x0},
  {0x3c2, 0x0, 0x0, 0x0, 0x0, 0x0},
  {0x3d2, 0x0, 0x0, 0x0, 0x0, 0x0},
  {0x3e2, 0x0, 0x0, 0x0, 0x0, 0x0},
  {0x3f2, 0x0, 0x0, 0x0, 0x0, 0x0},
  {0x100005, 0x320000, 0x0, 0x0, 0x0, 0x0},
  {0x100015, 0x320004, 0x0, 0x0, 0x0, 0x0},
  {0x100025, 0x320008, 0x0, 0x0, 0x0, 0x0},
  {0x100035, 0x32000c, 0x0, 0x0, 0x0, 0x0},
  {0x100045, 0x320010, 0x0, 0x0, 0x0, 0x0},
  {0x100055, 0x320014, 0x0, 0x0, 0x0, 0x0},
  {0x100065, 0x320018, 0x0, 0x0, 0x0, 0x0},
  {0x100073, 0x16001c, 0x0, 0x0, 0x0, 0x0},
  {0x100075, 0x320020, 0x0, 0x0, 0x0, 0x0},
  {0x100085, 0x320024, 0x0, 0x0, 0x0, 0x0},
  {0x100095, 0x320028, 0x0, 0x0, 0x0, 0x0},
  {0x1000a5, 0x32002c, 0x0, 0x0, 0x0, 0x0},
  {0x1000b5, 0x320030, 0x0, 0x0, 0x0, 0x0},
  {0x1000c5, 0x320034, 0x0, 0x0, 0x0, 0x0},
  {0x1000d5, 0x320038, 0x0, 0x0, 0x0, 0x0},
  {0x1000f3, 0x16003c, 0x0, 0x0, 0x0, 0x0},
  {0x1000e5, 0x320040, 0x0, 0x0, 0x0, 0x0},
  {0x1000f5, 0x320044, 0x0, 0x0, 0x0, 0x0},
  {0x100105, 0x320048, 0x0, 0x0, 0x0, 0x0},
  {0x100115, 0x32004c, 0x0, 0x0, 0x0, 0x0},
  {0x100125, 0x320050, 0x0, 0x0, 0x0, 0x0},
  {0x100135, 0x320054, 0x0, 0x0, 0x0, 0x0},
  {0x100145, 0x320058, 0x0, 0x0, 0x0, 0x0},
  {0x100173, 0x16005c, 0x0, 0x0, 0x0, 0x0},
  {0x100155, 0x320060, 0x0, 0x0, 0x0, 0x0},
  {0x100165, 0x320064, 0x0, 0x0, 0x0, 0x0},
  {0x100175, 0x320068, 0x0, 0x0, 0x0, 0x0},
  {0x100006, 0x32006c, 0x0, 0x0, 0x0, 0x0},
  {0x100016, 0x320070, 0x0, 0x0, 0x0, 0x0},
  {0x100026, 0x320074, 0x0, 0x0, 0x0, 0x0},
  {0x100036, 0x320078, 0x0, 0x0, 0x0, 0x0},
  {0x1001f3, 0x16007c, 0x0, 0x0, 0x0, 0x0},
  {0x100064, 0x5361615, 0x0, 0x0, 0x0, 0x0},
  {0x100074, 0x5361619, 0x0, 0x0, 0x0, 0x0},
  {0x100084, 0x536161d, 0x0, 0x0, 0x0, 0x0},
  {0x100094, 0x536161a, 0x0, 0x0, 0x0, 0x0},
  {0x1000a4, 0x536161e, 0x0, 0x0, 0x0, 0x0},
  {0x1000d4, 0x536161c, 0x0, 0x0, 0x0, 0x0},
  {0x100034, 0x5402a12, 0x0, 0x0, 0x0, 0x0},
  {0x1000b4, 0x5402a16, 0x0, 0x0, 0x0, 0x0},
  {0x200000, 0x400001, 0x1403200, 0x0, 0x0, 0x0},
  {0x200007, 0x400005, 0x1403204, 0x0, 0x0, 0x0},
  {0x200017, 0x400009, 0x1403208, 0x0, 0x0, 0x0},
  {0x200027, 0x40000d, 0x140320c, 0x0, 0x0, 0x0},
  {0x200037, 0x400011, 0x1403210, 0x0, 0x0, 0x0},
  {0x100003, 0x160015, 0x0, 0x0, 0x0, 0x0},
  {0x100013, 0x160019, 0x0, 0x0, 0x0, 0x0},
  {0x100023, 0x16001d, 0x0, 0x0, 0x0, 0x0},
  {0x100004, 0x2a0002, 0x0, 0x0, 0x0, 0x0},
  {0x100014, 0x2a0006, 0x0, 0x0, 0x0, 0x0},
  {0x100024, 0x2a000a, 0x0, 0x0, 0x0, 0x0},
  {0x1000c4, 0x2a000e, 0x0, 0x0, 0x0, 0x0},
  {0x100044, 0x2a0012, 0x0, 0x0, 0x0, 0x0},
  {0x100054, 0x2a0016, 0x0, 0x0, 0x0, 0x0},
  {0x100033, 0x16001a, 0x0, 0x0, 0x0, 0x0},
  {0x100043, 0x16001e, 0x0, 0x0, 0x0, 0x0},
  {0x101053, 0x2403615, 0x0, 0x0, 0x0, 0x0},
  {0x102063, 0x2403619, 0x0, 0x0, 0x0, 0x0},
  {0x303000, 0x1402a02, 0x1402a0a, 0x1403214, 0x0, 0x0},
  {0x304000, 0x1402a06, 0x1402a0e, 0x1403218, 0x0, 0x0},
  {0x105008, 0x240361d, 0x0, 0x0, 0x0, 0x0},
  {0x106018, 0x240361a, 0x0, 0x0, 0x0, 0x0},
  {0x107088, 0x240361e, 0x0, 0x0, 0x0, 0x0},
  {0x108098, 0x240361c, 0x0, 0x0, 0x0, 0x0},
  {0x100144, 0x5361635, 0x0, 0x0, 0x0, 0x0},
  {0x100154, 0x5361639, 0x0, 0x0, 0x0, 0x0},
  {0x100164, 0x536163d, 0x0, 0x0, 0x0, 0x0},
  {0x100174, 0x536163a, 0x0, 0x0, 0x0, 0x0},
  {0x100184, 0x536163e, 0x0, 0x0, 0x0, 0x0},
  {0x100194, 0x536163c, 0x0, 0x0, 0x0, 0x0},
  {0x100134, 0x5402a32, 0x0, 0x0, 0x0, 0x0},
  {0x1001b4, 0x5402a36, 0x0, 0x0, 0x0, 0x0},
  {0x200000, 0x400021, 0x1403220, 0x0, 0x0, 0x0},
  {0x200047, 0x400025, 0x1403224, 0x0, 0x0, 0x0},
  {0x200057, 0x400029, 0x1403228, 0x0, 0x0, 0x0},
  {0x200067, 0x40002d, 0x140322c, 0x0, 0x0, 0x0},
  {0x200077, 0x400031, 0x1403230, 0x0, 0x0, 0x0},
  {0x100083, 0x160035, 0x0, 0x0, 0x0, 0x0},
  {0x100093, 0x160039, 0x0, 0x0, 0x0, 0x0},
  {0x1000a3, 0x16003d, 0x0, 0x0, 0x0, 0x0},
  {0x1000e4, 0x2a0022, 0x0, 0x0, 0x0, 0x0},
  {0x1000f4, 0x2a0026, 0x0, 0x0, 0x0, 0x0},
  {0x100104, 0x2a002a, 0x0, 0x0, 0x0, 0x0},
  {0x100114, 0x2a002e, 0x0, 0x0, 0x0, 0x0},
  {0x100124, 0x2a0032, 0x0, 0x0, 0x0, 0x0},
  {0x1001a4, 0x2a0036, 0x0, 0x0, 0x0, 0x0},
  {0x1000b3, 0x16003a, 0x0, 0x0, 0x0, 0x0},
  {0x1000c3, 0x16003e, 0x0, 0x0, 0x0, 0x0},
  {0x1090d3, 0x2403635, 0x0, 0x0, 0x0, 0x0},
  {0x10a0e3, 0x2403639, 0x0, 0x0, 0x0, 0x0},
  {0x30b000, 0x1402a22, 0x1402a2a, 0x1403234, 0x0, 0x0},
  {0x30c000, 0x1402a26, 0x1402a2e, 0x1403238, 0x0, 0x0},
  {0x10d028, 0x240363d, 0x0, 0x0, 0x0, 0x0},
  {0x10e038, 0x240363a, 0x0, 0x0, 0x0, 0x0},
  {0x10f0a8, 0x240363e, 0x0, 0x0, 0x0, 0x0},
  {0x1100b8, 0x240363c, 0x0, 0x0, 0x0, 0x0},
  {0x100224, 0x5361655, 0x0, 0x0, 0x0, 0x0},
  {0x100284, 0x5361659, 0x0, 0x0, 0x0, 0x0},
  {0x100244, 0x536165d, 0x0, 0x0, 0x0, 0x0},
  {0x100254, 0x536165a, 0x0, 0x0, 0x0, 0x0},
  {0x100264, 0x536165e, 0x0, 0x0, 0x0, 0x0},
  {0x100274, 0x536165c, 0x0, 0x0, 0x0, 0x0},
  {0x100234, 0x5402a52, 0x0, 0x0, 0x0, 0x0},
  {0x100294, 0x5402a56, 0x0, 0x0, 0x0, 0x0},
  {0x200000, 0x400041, 0x1403240, 0x0, 0x0, 0x0},
  {0x200087, 0x400045, 0x1403244, 0x0, 0x0, 0x0},
  {0x200097, 0x400049, 0x1403248, 0x0, 0x0, 0x0},
  {0x2000a7, 0x40004d, 0x140324c, 0x0, 0x0, 0x0},
  {0x2000b7, 0x400051, 0x1403250, 0x0, 0x0, 0x0},
  {0x100103, 0x160055, 0x0, 0x0, 0x0, 0x0},
  {0x100113, 0x160059, 0x0, 0x0, 0x0, 0x0},
  {0x100123, 0x16005d, 0x0, 0x0, 0x0, 0x0},
  {0x1001c4, 0x2a0042, 0x0, 0x0, 0x0, 0x0},
  {0x1001d4, 0x2a0046, 0x0, 0x0, 0x0, 0x0},
  {0x1001e4, 0x2a004a, 0x0, 0x0, 0x0, 0x0},
  {0x1001f4, 0x2a004e, 0x0, 0x0, 0x0, 0x0},
  {0x100204, 0x2a0052, 0x0, 0x0, 0x0, 0x0},
  {0x100214, 0x2a0056, 0x0, 0x0, 0x0, 0x0},
  {0x100133, 0x16005a, 0x0, 0x0, 0x0, 0x0},
  {0x100143, 0x16005e, 0x0, 0x0, 0x0, 0x0},
  {0x111153, 0x2403655, 0x0, 0x0, 0x0, 0x0},
  {0x112163, 0x2403659, 0x0, 0x0, 0x0, 0x0},
  {0x313000, 0x1402a42, 0x1402a4a, 0x1403254, 0x0, 0x0},
  {0x314000, 0x1402a46, 0x1402a4e, 0x1403258, 0x0, 0x0},
  {0x115048, 0x240365d, 0x0, 0x0, 0x0, 0x0},
  {0x116058, 0x240365a, 0x0, 0x0, 0x0, 0x0},
  {0x1170c8, 0x240365e, 0x0, 0x0, 0x0, 0x0},
  {0x1180d8, 0x240365c, 0x0, 0x0, 0x0, 0x0},
  {0x100304, 0x5361675, 0x0, 0x0, 0x0, 0x0},
  {0x100314, 0x5361679, 0x0, 0x0, 0x0, 0x0},
  {0x100324, 0x536167d, 0x0, 0x0, 0x0, 0x0},
  {0x100374, 0x536167a, 0x0, 0x0, 0x0, 0x0},
  {0x100344, 0x536167e, 0x0, 0x0, 0x0, 0x0},
  {0x100354, 0x536167c, 0x0, 0x0, 0x0, 0x0},
  {0x1002b4, 0x5402a72, 0x0, 0x0, 0x0, 0x0},
  {0x100334, 0x5402a76, 0x0, 0x0, 0x0, 0x0},
  {0x200000, 0x400061, 0x1403260, 0x0, 0x0, 0x0},
  {0x2000c7, 0x400065, 0x1403264, 0x0, 0x0, 0x0},
  {0x2000d7, 0x400069, 0x1403268, 0x0, 0x0, 0x0},
  {0x2000e7, 0x40006d, 0x140326c, 0x0, 0x0, 0x0},
  {0x2000f7, 0x400071, 0x1403270, 0x0, 0x0, 0x0},
  {0x100183, 0x160075, 0x0, 0x0, 0x0, 0x0},
  {0x100193, 0x160079, 0x0, 0x0, 0x0, 0x0},
  {0x1001a3, 0x16007d, 0x0, 0x0, 0x0, 0x0},
  {0x1002a4, 0x2a0062, 0x0, 0x0, 0x0, 0x0},
  {0x100364, 0x2a0066, 0x0, 0x0, 0x0, 0x0},
  {0x1002c4, 0x2a006a, 0x0, 0x0, 0x0, 0x0},
  {0x1002d4, 0x2a006e, 0x0, 0x0, 0x0, 0x0},
  {0x1002e4, 0x2a0072, 0x0, 0x0, 0x0, 0x0},
  {0x1002f4, 0x2a0076, 0x0, 0x0, 0x0, 0x0},
  {0x1001b3, 0x16007a, 0x0, 0x0, 0x0, 0x0},
  {0x1001c3, 0x16007e, 0x0, 0x0, 0x0, 0x0},
  {0x1191d3, 0x2403675, 0x0, 0x0, 0x0, 0x0},
  {0x11a1e3, 0x2403679, 0x0, 0x0, 0x0, 0x0},
  {0x31b000, 0x1402a62, 0x1402a6a, 0x1403274, 0x0, 0x0},
  {0x31c000, 0x1402a66, 0x1402a6e, 0x1403278, 0x0, 0x0},
  {0x11d068, 0x240367d, 0x0, 0x0, 0x0, 0x0},
  {0x11e078, 0x240367a, 0x0, 0x0, 0x0, 0x0},
  {0x11f0e8, 0x240367e, 0x0, 0x0, 0x0, 0x0},
  {0x1200f8, 0x240367c, 0x0, 0x0, 0x0, 0x0},
};
constexpr unsigned NQV = 0xc080c;
__device__ __attribute__((aligned(16))) const unsigned PFT[8][128] = {
  {0x40000f, 0x40000e, 0x0, 0x0, 0x41000f, 0x41000e, 0x0, 0x0, 0x42000f, 0x42000e, 0x0, 0x0, 0x43000f, 0x43000e, 0x0, 0x0, 0x44000f, 0x40002d, 0x0, 0x0, 0x45000f, 0x41002d, 0x0, 0x0, 0x46000f, 0x42002d, 0x0, 0x0, 0x47000f, 0x43002d, 0x0, 0x0, 0x48000f, 0x40002e, 0x0, 0x0, 0x49000f, 0x41002e, 0x0, 0x0, 0x4a000f, 0x42002e, 0x0, 0x0, 0x4b000f, 0x43002e, 0x0, 0x0, 0x4c000f, 0x40004d, 0x0, 0x0, 0x4d000f, 0x41004d, 0x0, 0x0, 0x4e000f, 0x42004d, 0x0, 0x0, 0x4f000f, 0x43004d, 0x0, 0x0, 0x40000c, 0x40004e, 0x0, 0x0, 0x41000c, 0x41004e, 0x0, 0x0, 0x42000c, 0x42004e, 0x0, 0x0, 0x43000c, 0x43004e, 0x0, 0x0, 0x40002c, 0x400003, 0x0, 0x0, 0x41002c, 0x400007, 0x0, 0x0, 0x42002c, 0x40000b, 0x0, 0x0, 0x43002c, 0x400013, 0x0, 0x0, 0x40004c, 0x420003, 0x0, 0x0, 0x41004c, 0x420007, 0x0, 0x0, 0x42004c, 0x42000b, 0x0, 0x0, 0x43004c, 0x420013, 0x0, 0x0, 0x40000d, 0x440003, 0x0, 0x0, 0x41000d, 0x440007, 0x0, 0x0, 0x42000d, 0x44000b, 0x0, 0x0, 0x43000d, 0x440013, 0x0, 0x0},
  {0x40006c, 0x400017, 0x0, 0x0, 0x41006c, 0x40001b, 0x0, 0x0, 0x42006c, 0x40001f, 0x0, 0x0, 0x43006c, 0x400023, 0x0, 0x0, 0x40008c, 0x400027, 0x0, 0x0, 0x41008c, 0x40002b, 0x0, 0x0, 0x42008c, 0x420017, 0x0, 0x0, 0x43008c, 0x42001b, 0x0, 0x0, 0x40006d, 0x42001f, 0x0, 0x0, 0x41006d, 0x420023, 0x0, 0x0, 0x42006d, 0x420027, 0x0, 0x0, 0x43006d, 0x42002b, 0x0, 0x0, 0x40006e, 0x440017, 0x0, 0x0, 0x41006e, 0x44001b, 0x0, 0x0, 0x42006e, 0x44001f, 0x0, 0x0, 0x43006e, 0x440023, 0x0, 0x0, 0x40008d, 0x440027, 0x0, 0x0, 0x41008d, 0x44002b, 0x0, 0x0, 0x42008d, 0x460017, 0x0, 0x0, 0x43008d, 0x46001b, 0x0, 0x0, 0x40008e, 0x46001f, 0x0, 0x0, 0x41008e, 0x460023, 0x0, 0x0, 0x42008e, 0x460027, 0x0, 0x0, 0x43008e, 0x46002b, 0x0, 0x0, 0x4000ad, 0x480017, 0x0, 0x0, 0x4100ad, 0x48001b, 0x0, 0x0, 0x4200ad, 0x48001f, 0x0, 0x0, 0x4300ad, 0x480023, 0x0, 0x0, 0x4000ae, 0x480027, 0x0, 0x0, 0x4100ae, 0x48002b, 0x0, 0x0, 0x4200ae, 0x4a0017, 0x0, 0x0, 0x4300ae, 0x4a001b, 0x0, 0x0},
  {0x40002f, 0x4000ce, 0x0, 0x0, 0x41002f, 0x4100ce, 0x0, 0x0, 0x42002f, 0x4200ce, 0x0, 0x0, 0x43002f, 0x4300ce, 0x0, 0x0, 0x44002f, 0x4000ed, 0x0, 0x0, 0x45002f, 0x4100ed, 0x0, 0x0, 0x46002f, 0x4200ed, 0x0, 0x0, 0x47002f, 0x4300ed, 0x0, 0x0, 0x48002f, 0x4000ee, 0x0, 0x0, 0x49002f, 0x4100ee, 0x0, 0x0, 0x4a002f, 0x4200ee, 0x0, 0x0, 0x4b002f, 0x4300ee, 0x0, 0x0, 0x4c002f, 0x4000fe, 0x0, 0x0, 0x4d002f, 0x4100fe, 0x0, 0x0, 0x4e002f, 0x4200fe, 0x0, 0x0, 0x4f002f, 0x4300fe, 0x0, 0x0, 0x4000ac, 0x4000fd, 0x0, 0x0, 0x4100ac, 0x4100fd, 0x0, 0x0, 0x4200ac, 0x4200fd, 0x0, 0x0, 0x4300ac, 0x4300fd, 0x0, 0x0, 0x4000cc, 0x40003f, 0x0, 0x0, 0x4100cc, 0x41003f, 0x0, 0x0, 0x4200cc, 0x42003f, 0x0, 0x0, 0x4300cc, 0x43003f, 0x0, 0x0, 0x4000ec, 0x44003f, 0x0, 0x0, 0x4100ec, 0x45003f, 0x0, 0x0, 0x4200ec, 0x46003f, 0x0, 0x0, 0x4300ec, 0x47003f, 0x0, 0x0, 0x4000cd, 0x48003f, 0x0, 0x0, 0x4100cd, 0x49003f, 0x0, 0x0, 0x4200cd, 0x4a003f, 0x0, 0x0, 0x4300cd, 0x4b003f, 0x0, 0x0},
  {0x40000c, 0x0, 0x0, 0x0, 0x41000c, 0x0, 0x0, 0x0, 0x42000c, 0x0, 0x0, 0x0, 0x43000c, 0x0, 0x0, 0x0, 0x40002c, 0x0, 0x0, 0x0, 0x41002c, 0x0, 0x0, 0x0, 0x42002c, 0x0, 0x0, 0x0, 0x43002c, 0x0, 0x0, 0x0, 0x40004c, 0x0, 0x0, 0x0, 0x41004c, 0x0, 0x0, 0x0, 0x42004c, 0x0, 0x0, 0x0, 0x43004c, 0x0, 0x0, 0x0, 0x40006c, 0x0, 0x0, 0x0, 0x41006c, 0x0, 0x0, 0x0, 0x42006c, 0x0, 0x0, 0x0, 0x43006c, 0x0, 0x0, 0x0, 0x40008c, 0x0, 0x0, 0x0, 0x41008c, 0x0, 0x0, 0x0, 0x42008c, 0x0, 0x0, 0x0, 0x43008c, 0x0, 0x0, 0x0, 0x4000ac, 0x0, 0x0, 0x0, 0x4100ac, 0x0, 0x0, 0x0, 0x4200ac, 0x0, 0x0, 0x0, 0x4300ac, 0x0, 0x0, 0x0, 0x4000cc, 0x0, 0x0, 0x0, 0x4100cc, 0x0, 0x0, 0x0, 0x4200cc, 0x0, 0x0, 0x0, 0x4300cc, 0x0, 0x0, 0x0, 0x4000ec, 0x0, 0x0, 0x0, 0x4100ec, 0x0, 0x0, 0x0, 0x4200ec, 0x0, 0x0, 0x0, 0x4300ec, 0x0, 0x0, 0x0},
  {0x0, 0x0, 0x0, 0x0, 0x0, 0x0, 0x0, 0x0, 0x0, 0x0, 0x0, 0x0, 0x0, 0x0, 0x0, 0x0, 0x0, 0x0, 0x0, 0x0, 0x0, 0x0, 0x0, 0x0, 0x0, 0x0, 0x0, 0x0, 0x0, 0x0, 0x0, 0x0, 0x40000d, 0x0, 0x0, 0x0, 0x41000d, 0x0, 0x0, 0x0, 0x42000d, 0x0, 0x0, 0x0, 0x43000d, 0x0, 0x0, 0x0, 0x40002d, 0x0, 0x0, 0x0, 0x41002d, 0x0, 0x0, 0x0, 0x42002d, 0x0, 0x0, 0x0, 0x43002d, 0x0, 0x0, 0x0, 0x40000e, 0x0, 0x0, 0x0, 0x41000e, 0x0, 0x0, 0x0, 0x42000e, 0x0, 0x0, 0x0, 0x43000e, 0x0, 0x0, 0x0, 0x40002e, 0x0, 0x0, 0x0, 0x41002e, 0x0, 0x0, 0x0, 0x42002e, 0x0, 0x0, 0x0, 0x43002e, 0x0, 0x0, 0x0, 0x0, 0x0, 0x0, 0x0, 0x0, 0x0, 0x0, 0x0, 0x0, 0x0, 0x0, 0x0, 0x0, 0x0, 0x0, 0x0, 0x0, 0x0, 0x0, 0x0, 0x0, 0x0, 0x0, 0x0, 0x0, 0x0, 0x0, 0x0, 0x0, 0x0, 0x0, 0x0},
  {0x0, 0x0, 0x0, 0x0, 0x0, 0x0, 0x0, 0x0, 0x0, 0x0, 0x0, 0x0, 0x0, 0x0, 0x0, 0x0, 0x0, 0x0, 0x0, 0x0, 0x0, 0x0, 0x0, 0x0, 0x0, 0x0, 0x0, 0x0, 0x0, 0x0, 0x0, 0x0, 0x40004d, 0x0, 0x0, 0x0, 0x41004d, 0x0, 0x0, 0x0, 0x42004d, 0x0, 0x0, 0x0, 0x43004d, 0x0, 0x0, 0x0, 0x40006d, 0x0, 0x0, 0x0, 0x41006d, 0x0, 0x0, 0x0, 0x42006d, 0x0, 0x0, 0x0, 0x43006d, 0x0, 0x0, 0x0, 0x40004e, 0x0, 0x0, 0x0, 0x41004e, 0x0, 0x0, 0x0, 0x42004e, 0x0, 0x0, 0x0, 0x43004e, 0x0, 0x0, 0x0, 0x40006e, 0x0, 0x0, 0x0, 0x41006e, 0x0, 0x0, 0x0, 0x42006e, 0x0, 0x0, 0x0, 0x43006e, 0x0, 0x0, 0x0, 0x0, 0x0, 0x0, 0x0, 0x0, 0x0, 0x0, 0x0, 0x0, 0x0, 0x0, 0x0, 0x0, 0x0, 0x0, 0x0, 0x0, 0x0, 0x0, 0x0, 0x0, 0x0, 0x0, 0x0, 0x0, 0x0, 0x0, 0x0, 0x0, 0x0, 0x0, 0x0},
  {0x0, 0x0, 0x0, 0x0, 0x0, 0x0, 0x0, 0x0, 0x0, 0x0, 0x0, 0x0, 0x0, 0x0, 0x0, 0x0, 0x0, 0x0, 0x0, 0x0, 0x0, 0x0, 0x0, 0x0, 0x0, 0x0, 0x0, 0x0, 0x0, 0x0, 0x0, 0x0, 0x40008d, 0x0, 0x0, 0x0, 0x41008d, 0x0, 0x0, 0x0, 0x42008d, 0x0, 0x0, 0x0, 0x43008d, 0x0, 0x0, 0x0, 0x4000ad, 0x0, 0x0, 0x0, 0x4100ad, 0x0, 0x0, 0x0, 0x4200ad, 0x0, 0x0, 0x0, 0x4300ad, 0x0, 0x0, 0x0, 0x40008e, 0x0, 0x0, 0x0, 0x41008e, 0x0, 0x0, 0x0, 0x42008e, 0x0, 0x0, 0x0, 0x43008e, 0x0, 0x0, 0x0, 0x4000ae, 0x0, 0x0, 0x0, 0x4100ae, 0x0, 0x0, 0x0, 0x4200ae, 0x0, 0x0, 0x0, 0x4300ae, 0x0, 0x0, 0x0, 0x0, 0x0, 0x0, 0x0, 0x0, 0x0, 0x0, 0x0, 0x0, 0x0, 0x0, 0x0, 0x0, 0x0, 0x0, 0x0, 0x0, 0x0, 0x0, 0x0, 0x0, 0x0, 0x0, 0x0, 0x0, 0x0, 0x0, 0x0, 0x0, 0x0, 0x0, 0x0},
  {0x0, 0x0, 0x0, 0x0, 0x0, 0x0, 0x0, 0x0, 0x0, 0x0, 0x0, 0x0, 0x0, 0x0, 0x0, 0x0, 0x0, 0x0, 0x0, 0x0, 0x0, 0x0, 0x0, 0x0, 0x0, 0x0, 0x0, 0x0, 0x0, 0x0, 0x0, 0x0, 0x4000cd, 0x0, 0x0, 0x0, 0x4100cd, 0x0, 0x0, 0x0, 0x4200cd, 0x0, 0x0, 0x0, 0x4300cd, 0x0, 0x0, 0x0, 0x4000ed, 0x0, 0x0, 0x0, 0x4100ed, 0x0, 0x0, 0x0, 0x4200ed, 0x0, 0x0, 0x0, 0x4300ed, 0x0, 0x0, 0x0, 0x4000ce, 0x0, 0x0, 0x0, 0x4100ce, 0x0, 0x0, 0x0, 0x4200ce, 0x0, 0x0, 0x0, 0x4300ce, 0x0, 0x0, 0x0, 0x4000ee, 0x0, 0x0, 0x0, 0x4100ee, 0x0, 0x0, 0x0, 0x4200ee, 0x0, 0x0, 0x0, 0x4300ee, 0x0, 0x0, 0x0, 0x0, 0x0, 0x0, 0x0, 0x0, 0x0, 0x0, 0x0, 0x0, 0x0, 0x0, 0x0, 0x0, 0x0, 0x0, 0x0, 0x0, 0x0, 0x0, 0x0, 0x0, 0x0, 0x0, 0x0, 0x0, 0x0, 0x0, 0x0, 0x0, 0x0, 0x0, 0x0},
};
__global__ void __launch_bounds__(512, 2) k_mega(const u16* __restrict__ Wq, const u16* __restrict__ Wk, const u16* __restrict__ Wv,
                                                 const u16* __restrict__ Wo, const u16* __restrict__ w1, const u16* __restrict__ w3,
                                                 const u16* __restrict__ w2, const u16* __restrict__ x, const u16* __restrict__ cvec,
                                                 const u16* __restrict__ wmod, const u16* __restrict__ mem0, char* ws, u16* out) {
  extern __shared__ __attribute__((aligned(16))) char smem[];
  int wave_id_ = __builtin_amdgcn_readfirstlane((int)threadIdx.x >> 6);
  asm volatile("" : "+s"(wave_id_));
  unsigned* tmo = (unsigned*)(ws + OFF_TMO);
  unsigned long long* mts = nullptr;
  const float* mod = (const float*)(ws + OFF_MOD);
  volatile LAS unsigned* tslot = (volatile LAS unsigned*)(smem + L_SLOT + 768);
  unsigned xcc; asm volatile("s_getreg_b32 %0, hwreg(HW_REG_XCC_ID)" : "=s"(xcc)); xcc &= 7;
  auto ini_of = [](int sl) { return sl >= 128 && ((sl + 8) & 31) < 16 ? ((sl >> 5) - 4) * 16 + (sl & 15) : -1; };
  const int guess = xcc * 32 + ((blockIdx.x >> 3) & 31);
  if (wave_id_ == 3) {
    const char* pc; asm volatile("s_getpc_b64 %0" : "=s"(pc));
    const unsigned o = (unsigned)(lane_id() * 32 + ((blockIdx.x >> 3) & 31)) * 128;
    if (o < CPF * 1024) { const unsigned v = *(const unsigned*)(pc + o); asm volatile("" :: "v"(v)); }
  }
  if (wave_id_ >= 4 && ini_of(guess) >= 0) { init_sums(smem, opaque_tid() - 256, ini_of(guess), cvec, wmod); if (wave_id_ == 7) IS(3); }
  u32x2 rt_; u32x4 pfe;
  unsigned long long tag;
  asm volatile("s_load_dwordx2 %0, %3, %6\n\ts_load_dwordx2 %1, %4, 0\n\ts_load_dwordx4 %2, %5, 0\n\ts_waitcnt lgkmcnt(0)" : "=&s"(tag), "=&s"(rt_), "=&s"(pfe)
               : "s"(ws), "s"(&ROLES[guess][0]), "s"(&PFT[xcc][((blockIdx.x >> 3) & 31) * 4]), "i"((int)OFF_EP) : "memory");
  tag = (tag ^ (tag >> 31) ^ 0x9E3779B97F4A7C15ull) * 0xBF58476D1CE4E5B9ull; tag = (tag ^ (tag >> 29)) * 0x94D049BB133111EBull; tag ^= tag >> 32;
  if (wave_id_ == 0) {
    if (lane_id() == 0) {
      const unsigned long long g = tag; IS(0);
      unsigned sl = 0xFFFFu;
      for (int i = 0; i < 256; ++i)
        if (__hip_atomic_exchange((unsigned long long*)(ws + OFF_CLM) + ((guess + i) & 255), g, __ATOMIC_RELAXED, __HIP_MEMORY_SCOPE_AGENT) != g) { sl = (guess + i) & 255; break; }
      IS(1);
      tslot[0] = sl;
    }
  } else if (wave_id_ == 1) {
    const int ig = ini_of(guess);
    tslot[1] = ig >= 0 && (ig & 15) < 12 ? (unsigned)calibrate(ws, ig) : 0u;
    IS(2);
  } else if (ini_of(guess) >= 0) {
    if (wave_id_ < 4) { const unsigned v = *(const unsigned*)(mem0 + (opaque_tid() - 128) * 64); asm volatile("" :: "v"(v)); }
  } else if (wave_id_ >= 4) {
    delay64<PFD>();
#pragma unroll
    for (int i = 0; i < 4; ++i) {
      const unsigned e = pfe[i];
      const int t = opaque_tid() - 256, m = e & 3, wide = (e >> 21) & 1, row = ((e >> 8) & 8191) + (wide ? t >> 4 : t), cl = ((e >> 2) & 63) + (wide ? t & 15 : 0);
      const u16* p = (m == 0 ? Wq : (m == 1 ? Wk : (m == 2 ? Wv : Wo))) + (size_t)row * (m == 3 ? DM : HD) + cl * 64;
      if (e) asm volatile("s_mov_b32 m0, %1\n\ts_nop 0\n\tglobal_load_lds_dword %0, off" :: "v"(p), "s"((rt_.x & 15) == T_QV ? 31232 : L_GB + 8192) : "memory", "m0");
    }
    if (PFW) WAIT_V(0);
  }
  BAR();
  const int slot = (int)__builtin_amdgcn_readfirstlane(tslot[0]);
  const int cd = (int)__builtin_amdgcn_readfirstlane(tslot[1]);
  if (slot != guess) { rt_ = *(const u32x2*)ROLES[slot & 255]; WAIT_V(0); }
  BAR();
  asm volatile("" : "+s"(wave_id_));
  if (slot >= 256) return;
  const int xg = slot >> 5;
  const unsigned rw = rt_[0];
  const int role = rw & 15, idx = (rw >> 4) & 255;
  if (role == T_QV || role == T_KR) {
    const int wg = role == T_QV ? (idx >> 3) * 32 + (idx & 7) : (idx >> 4) * 32 + 8 + (idx & 15);
    if (role == T_QV) qk_body(smem, wave_id_, xg, idx, wg, Wq, Wv, ws, tmo, tag);
    else vr_body(smem, wave_id_, xg, idx >> 1, idx & 1, wg, Wk, Wv, Wo, ws, tmo, tag);
    return;
  }
  unsigned* cnt = (unsigned*)(xch_base(ws) + ((size_t)4 << 21));
  const int ini = ini_of(slot);
  if (ini >= 0) {
    if (slot != guess) { if (wave_id_ < 4) init_sums(smem, opaque_tid(), ini, cvec, wmod); BAR(); }
    init_role(smem, wave_id_, ini, mem0, ws, tag, slot != guess || cd == 0 ? 0u : ((cd > 0) == !(xcc & 4) ? 1u : 2u));
    if ((rw >> 12) & 255) l1_role(smem, wave_id_, ((rw >> 12) & 255) - 1, x, mod, cvec, wmod, ws + OFF_H1, cnt, tmo, mts, tag);
    else cnt += (ini_resolve1(smem, wave_id_, ws, tag) >> 2) << 11;
  }
  const int ntask = (rw >> 20) & 15;
  for (int it = 0; it < ntask; ++it) {
    const unsigned tk = it == 0 ? rt_[1] : ROLES[slot][1 + it];
    const int m = tk & 3, h = (tk >> 2) & 31, bo = (tk >> 8) & 255, ns = ((tk >> 16) & 255) - bo, part = (tk >> 24) & 3;
    const int lane = lane_id();
    const Dep d{uni((const char*)cnt), (unsigned)(PL1 + (lane & 31)) * 128, 8u, (unsigned)-bo};
    unsigned par = 0;
    if (wave_id_ == 0) {
      if ((tk >> 26) & 1) wait_k0(cnt, h, tmo);
      if (bo) wait_l1(cnt, 8u, tmo);
    }
    BAR();
    gemm7<R_QKV>(smem, wave_id_, (m == 0 ? Wq : (m == 1 ? Wk : Wv)) + h * DH, nullptr, HD, ws + OFF_H1 + (size_t)bo * 65536, 65536, d,
                    ws + (m == 0 ? OFF_Q : (m == 1 ? OFF_K : OFF_V)) + h * 8192 + (size_t)bo * 32 * 8192, 32 * 8192,
                    cnt + (PQKV + h) * 32 + 4 * part + m, tmo, part ? 319 : (h >> 3) * 32 + m * 8 + (h & 7), mts, false,
                    bo ? 0 : NIMM, ns, bo ? ns : 0, bo * 257, tag, &par);
    cnt += (par >> 2) << 11;
  }
  __syncthreads();
  const char* cb = uni((const char*)cnt);
  if (role == T_WO || role == T_F2) {
    const bool wo = role == T_WO;
    const int ks = idx >> 3, cn = idx & 7;
    const int lane = lane_id();
    const Dep d{cb, (unsigned)(wo ? PROG + 8 * ks + (lane & 7) : PF1 + 16 * ks + (lane & 15)) * 128, 1u, 0u};
    gemm7<R_PART>(smem, wave_id_, (wo ? Wo : w2) + (size_t)ks * 1024 * DM + cn * 128, nullptr, DM, ws + (wo ? OFF_O : OFF_G) + ks * 16 * 4096, (wo ? 64 : 56) * 4096, d,
                  ws + (wo ? OFF_P : OFF_P2) + (ks * DM + cn * 128) * (TLP ? 64 : 2), (size_t)32 * PROW, cnt + ((wo ? PWO : PF2) + idx) * 32, tmo,
                  wo ? 128 + ks * 32 + cn : 128 + (idx / 7) * 32 + 22 + idx % 7, mts, !wo);
  } else if (role == T_F2C) {
    const int lane = lane_id();
    const Dep d{cb, (unsigned)(PF1 + 48 + (lane & 7)) * 128, 1u, 0u};
    gemm7<R_PART2>(smem, wave_id_, w2 + (size_t)3072 * DM + idx * 256, nullptr, DM, ws + OFF_G + 48 * 4096, 56 * 4096, d,
                   ws + OFF_P2 + (3 * DM + idx * 256) * (TLP ? 64 : 2), (size_t)32 * PROW, cnt + (PF2 + 24 + idx) * 32, tmo, 128 + 96 + 25 + idx, mts, true);
  } else if (role == T_F1) {
    const int lane = lane_id();
    const Dep d{cb, (unsigned)(PLN + (lane & 15)) * 128, 2u, (unsigned)((lane >> 3) & 1)};
    gemm7<R_F1>(smem, wave_id_, w1 + 64 * idx, w3 + 64 * idx, FF, ws + OFF_H2, 65536, d, ws + OFF_G + idx * 4096, 56 * 4096, cnt + (PF1 + idx) * 32, tmo,
                128 + (idx / 14) * 32 + 8 + idx % 14, mts, false);
  } else if (role == T_FIN) {
    fin_role(smem, wave_id_, idx, mod, ws + OFF_P2, (const u16*)(ws + OFF_X1), out, cnt, tmo, mts);
    if (idx == 15 && opaque_tid() == 0) st8_sc1(ws + OFF_EP, u32x2{(unsigned)tag, (unsigned)(tag >> 32)});
  } else if (role == T_LN) {
    row_role(smem, wave_id_, idx & 7, idx >> 3, mod, ws + OFF_P, x, (u16*)(ws + OFF_X1), ws + OFF_H2, cnt, tmo, 256 + ((idx >> 1) & 3), mts);
  }
}

extern "C" void kernel_launch(void* const* d_in, const int* in_sizes, int n_in, void* d_out, int out_size, void* d_ws, size_t ws_size,
                              hipStream_t stream) {
  const u16* x    = (const u16*)d_in[0];
  const u16* mem0 = (const u16*)d_in[1];
  const u16* Wq   = (const u16*)d_in[2];
  const u16* Wk   = (const u16*)d_in[3];
  const u16* Wv   = (const u16*)d_in[4];
  const u16* Wo   = (const u16*)d_in[5];
  const u16* w1   = (const u16*)d_in[8];
  const u16* w3   = (const u16*)d_in[9];
  const u16* w2   = (const u16*)d_in[10];
  const u16* c    = (const u16*)d_in[11];
  const u16* wmod = (const u16*)d_in[12];
#define WSHIFT 0
  char* ws = (char*)d_ws + ((size_t)WSHIFT << 20);
  hipFuncSetAttribute((const void*)k_mega, hipFuncAttributeMaxDynamicSharedMemorySize, MEGA_LDS);
  k_mega<<<256, 512, MEGA_LDS, stream>>>(Wq, Wk, Wv, Wo, w1, w3, w2, x, c, wmod, mem0, ws, (u16*)d_out);
}
```

```cpp
#include <hip/hip_runtime.h>
#include <hip/hip_bf16.h>
#include <stdint.h>
#include <type_traits>
#pragma clang diagnostic ignored "-Winline-asm"
#pragma clang diagnostic ignored "-Wunused-value"

typedef unsigned short u16;
typedef short s16x4 __attribute__((ext_vector_type(4)));
typedef __bf16 bf16x8 __attribute__((ext_vector_type(8)));
typedef float f32x4 __attribute__((ext_vector_type(4)));
typedef float f32x2 __attribute__((ext_vector_type(2)));
typedef unsigned u32x4 __attribute__((ext_vector_type(4)));
typedef unsigned u32x2 __attribute__((ext_vector_type(2)));
#define LAS __attribute__((address_space(3)))

constexpr int SEQ = 2048, DM = 1024, NH = 32, DH = 128, HD = 4096, NMEM = 8, SEG = 32, NSEG = 64, FF = 3584;
constexpr unsigned SENT = 0xFFFFFFFFu;

__device__ __forceinline__ int lane_id() { int l; asm volatile("v_mbcnt_lo_u32_b32 %0, -1, 0\n\tv_mbcnt_hi_u32_b32 %0, -1, %0" : "=v"(l)); return l; }
#define WAVE_ID wave_id_
#define opaque_tid() (WAVE_ID * 64 + lane_id())
__device__ __forceinline__ float bf2f(u16 v) { return __builtin_bit_cast(float, (unsigned)v << 16); }
__device__ __forceinline__ float bflo(unsigned v) { return __builtin_bit_cast(float, v << 16); }
__device__ __forceinline__ float bfhi(unsigned v) { return __builtin_bit_cast(float, v & 0xffff0000u); }
__device__ __forceinline__ float bfr(float x) { return (float)(__bf16)x; }
__device__ __forceinline__ unsigned pack2(float a, float b) {
  typedef __bf16 bf16x2 __attribute__((ext_vector_type(2)));
  bf16x2 v; v[0] = (__bf16)a; v[1] = (__bf16)b; return __builtin_bit_cast(unsigned, v);
}
__device__ __forceinline__ unsigned nosent(unsigned v) { return v == SENT ? 0x7FC07FC0u : v; }
__device__ __forceinline__ bf16x8 asbf8(u32x4 v) { return __builtin_bit_cast(bf16x8, v); }
__device__ __forceinline__ s16x4 ass4(u32x2 v) { return __builtin_bit_cast(s16x4, v); }
__device__ __forceinline__ u32x2 trread(const char* lds_base, unsigned off) {
  s16x4 v = __builtin_amdgcn_ds_read_tr16_b64_v4i16((LAS s16x4*)(lds_base + off));
  return __builtin_bit_cast(u32x2, v);
}
__device__ __forceinline__ void glds16(const void* gsrc, unsigned lds_dst) {
  unsigned keep;
  asm volatile("s_mov_b32 %0, m0\n\ts_mov_b32 m0, %2\n\ts_nop 0\n\tglobal_load_lds_dwordx4 %1, off\n\ts_mov_b32 m0, %0"
               : "=&s"(keep) : "v"(gsrc), "s"(lds_dst) : "memory");
}
#define CNT_ " nt"
#define WNT_ " nt"
__device__ __forceinline__ void glds16_w(const void* gsrc, unsigned lds_dst) {
  unsigned keep;
  asm volatile("s_mov_b32 %0, m0\n\ts_mov_b32 m0, %2\n\ts_nop 0\n\tglobal_load_lds_dwordx4 %1, off" CNT_ "\n\ts_mov_b32 m0, %0"
               : "=&s"(keep) : "v"(gsrc), "s"(lds_dst) : "memory");
}
__device__ __forceinline__ void glds16_sc1(const void* gsrc, unsigned lds_dst) {
  unsigned keep;
  asm volatile("s_mov_b32 %0, m0\n\ts_mov_b32 m0, %2\n\ts_nop 0\n\tglobal_load_lds_dwordx4 %1, off sc1\n\ts_mov_b32 m0, %0"
               : "=&s"(keep) : "v"(gsrc), "s"(lds_dst) : "memory");
}
#define NTM 1
template <int B> __device__ __forceinline__ u32x4 ldnt(const void* p) { if (NTM & B) return __builtin_nontemporal_load((const u32x4*)p); else return *(const u32x4*)p; }
#define MFMA32(a, b, c) __builtin_amdgcn_mfma_f32_16x16x32_bf16((a), (b), (c), 0, 0, 0)

__device__ __forceinline__ void st16_sc1(void* p, u32x4 v) { asm volatile("global_store_dwordx4 %0, %1, off sc1\n\ts_nop 1" :: "v"(p), "v"(v) : "memory"); }
__device__ __forceinline__ void st8_sc1(void* p, u32x2 v) { asm volatile("global_store_dwordx2 %0, %1, off sc1\n\ts_nop 1" :: "v"(p), "v"(v) : "memory"); }
template <bool WT> __device__ __forceinline__ void st8(void* p, u32x2 v) { if (WT) st8_sc1(p, v); else *(u32x2*)p = v; }
template <bool WT> __device__ __forceinline__ void st16(void* p, u32x4 v) { if (WT) st16_sc1(p, v); else *(u32x4*)p = v; }
__device__ __forceinline__ u32x4 ld16_sc1(const void* p) {
  u32x4 r; asm volatile("global_load_dwordx4 %0, %1, off sc1\n\ts_waitcnt vmcnt(0)" : "=&v"(r) : "v"(p) : "memory"); return r;
}
__device__ __forceinline__ void ld16x4_sc1(const void* p, u32x4& a, u32x4& b, u32x4& c, u32x4& d) {
  asm volatile("global_load_dwordx4 %0, %4, off sc1\n\tglobal_load_dwordx4 %1, %4, off offset:1024 sc1\n\t"
               "global_load_dwordx4 %2, %4, off offset:2048 sc1\n\tglobal_load_dwordx4 %3, %4, off offset:3072 sc1\n\ts_waitcnt vmcnt(0)"
               : "=&v"(a), "=&v"(b), "=&v"(c), "=&v"(d) : "v"(p) : "memory");
}
typedef __amdgpu_buffer_rsrc_t rsrc_t;
__device__ __forceinline__ rsrc_t mkrs(const void* p) { return __builtin_amdgcn_make_buffer_rsrc((void*)p, 0, 0x7FFFFFFF, 0x00020000); }
__device__ __forceinline__ u32x4 bld16(rsrc_t r, unsigned off) { return __builtin_amdgcn_raw_buffer_load_b128(r, off, 0, 16); }
__device__ __forceinline__ u32x2 bld8(rsrc_t r, unsigned off) { return __builtin_amdgcn_raw_buffer_load_b64(r, off, 0, 16); }
__device__ __forceinline__ bool ok4(u32x4 v) { return v.x != SENT && v.y != SENT && v.z != SENT && v.w != SENT; }
constexpr int SPIN_CAP = 2000000;
__device__ __forceinline__ bool give_up(int spins, unsigned* tmo, unsigned code) {
  if ((spins & 4095) != 4095) return false;
  if (spins > SPIN_CAP) { __hip_atomic_store(tmo, code, __ATOMIC_RELAXED, __HIP_MEMORY_SCOPE_AGENT); return true; }
  return __hip_atomic_load(tmo, __ATOMIC_RELAXED, __HIP_MEMORY_SCOPE_AGENT) != 0u;
}
template <bool ACQ = true>
__device__ __forceinline__ void wait_cnt(unsigned* c, unsigned n, unsigned* tmo) {
  for (int spins = 0;; ++spins) {
    const unsigned v = __hip_atomic_load(c, __ATOMIC_RELAXED, __HIP_MEMORY_SCOPE_AGENT);
    if (v >= n) break;
    __builtin_amdgcn_s_sleep(8);
    if (give_up(spins, tmo, 5)) break;
  }
  if (ACQ) {
    __builtin_amdgcn_fence(__ATOMIC_ACQUIRE, "agent");
    asm volatile("s_waitcnt vmcnt(0)" ::: "memory");
  }
}

__device__ __forceinline__ const char* uni(const char* p) {
  const unsigned long long v = (unsigned long long)p;
  const unsigned lo = __builtin_amdgcn_readfirstlane((unsigned)v), hi = __builtin_amdgcn_readfirstlane((unsigned)(v >> 32));
  return (const char*)(((unsigned long long)hi << 32) | lo);
}
#define TSCLK "s_memrealtime"
#define TC(k) do { } while (0)
#define TS(k) do { } while (0)
#define TSDECL do { } while (0)
#define TSFLUSH(mask) do { } while (0)

constexpr size_t OFF_MOD  = 0;
constexpr size_t OFF_MOD2 = 65536;
constexpr size_t OFF_TMO  = 98304;
constexpr size_t OFF_EP   = 98304 + 256;
constexpr size_t OFF_CLM  = 102400;
constexpr size_t OFF_INI  = 131072;
constexpr size_t OFF_H1   = 262144;
constexpr size_t OFF_Q    = OFF_H1 + (size_t)SEQ * DM * 2;
constexpr size_t OFF_K    = OFF_Q + (size_t)SEQ * HD * 2;
constexpr size_t OFF_V    = OFF_K + (size_t)SEQ * HD * 2;
constexpr size_t OFF_O    = OFF_V + (size_t)SEQ * HD * 2;
constexpr size_t OFF_X1   = OFF_O + (size_t)SEQ * HD * 2;
constexpr size_t OFF_H2   = OFF_X1 + (size_t)SEQ * DM * 2;
constexpr size_t OFF_G    = OFF_H2 + (size_t)SEQ * DM * 2;
constexpr size_t OFF_XCH  = (size_t)100 << 20;
constexpr size_t OFF_P    = (size_t)112 << 20;
constexpr size_t OFF_P2   = (size_t)128 << 20;
constexpr size_t OFF_DUMP = (size_t)144 << 20;
constexpr int PROW = 4 * DM * 2;
#define TLQ 1
#define TLP 1
static_assert(OFF_G + (size_t)SEQ * FF * 2 <= OFF_XCH, "layout");
__device__ __forceinline__ char* xch_base(char* ws) { return (char*)(((size_t)(ws + OFF_XCH) + ((2u << 20) - 1)) & ~(size_t)((2u << 20) - 1)); }
constexpr int PROG = 16;
constexpr int PQKV = 64;
constexpr int PL1 = 96;
constexpr int PWO = 192;
constexpr int PLN = 224;
constexpr int PF2 = 320;
constexpr int PF1 = 448;
constexpr int L_RED = 131072;
constexpr int L_SLOT = L_RED + 16384;
constexpr int L_GB = L_SLOT + 1024;
constexpr int MEGA_LDS = 163840;
static_assert(L_GB + 12288 + 2048 <= MEGA_LDS, "lds");
__device__ __forceinline__ int bswz(int row) { return (((row >> 1) & 1) | (((row >> 3) & 1) << 1)) << 1; }
template <int OFF, bool SC1 = false>
__device__ __forceinline__ void glds16s(const char* sbase, unsigned voff, unsigned lds_base) {
  unsigned keep;
  if (SC1)
    asm volatile("s_mov_b32 %0, m0\n\ts_add_u32 m0, %3, %4\n\ts_nop 0\n\tglobal_load_lds_dwordx4 %1, %2 sc1\n\ts_mov_b32 m0, %0"
                 : "=&s"(keep) : "v"(voff), "s"(sbase), "s"(lds_base), "i"(OFF) : "memory", "scc");
  else
    asm volatile("s_mov_b32 %0, m0\n\ts_add_u32 m0, %3, %4\n\ts_nop 0\n\tglobal_load_lds_dwordx4 %1, %2\n\ts_mov_b32 m0, %0"
                 : "=&s"(keep) : "v"(voff), "s"(sbase), "s"(lds_base), "i"(OFF) : "memory", "scc");
}
template <int OFF>
__device__ __forceinline__ void glds16x2s(const char* sbase, unsigned voff, unsigned lds_base) {
  unsigned keep;
  asm volatile("s_mov_b32 %0, m0\n\ts_add_u32 m0, %3, %4\n\ts_nop 0\n\tglobal_load_lds_dwordx4 %1, %2 sc1\n\tglobal_load_lds_dwordx4 %1, %2 offset:1024 sc1\n\ts_mov_b32 m0, %0"
               : "=&s"(keep) : "v"(voff), "s"(sbase), "s"(lds_base), "i"(OFF) : "memory", "scc");
}
__device__ __forceinline__ void st16s(const char* sb, unsigned vo, u32x4 v) { asm volatile("global_store_dwordx4 %0, %1, %2 sc1\n\ts_nop 1" :: "v"(vo), "v"(v), "s"(sb) : "memory"); }
__device__ __forceinline__ void head_to_lds(const char* src, int lane, unsigned lds) {
  if (TLQ) {
    const int c = (lane & 15) ^ (lane >> 4);
    const unsigned v0 = (unsigned)((c >> 2) * 2048 + (lane >> 4) * 64 + (c & 3) * 16);
#pragma unroll
    for (int i = 0; i < 8; ++i) glds16s<0, true>(src + i * 256, v0 ^ ((i & 3) * 2048), lds + i * 1024);
  } else {
#pragma unroll
    for (int i = 0; i < 4; ++i) glds16x2s<0>(src + i * 2048, (unsigned)lane * 16, lds + i * 2048);
  }
}
#define WAIT_V(n) asm volatile("s_waitcnt vmcnt(" #n ")" ::: "memory")
#define BAR() do { asm volatile("s_waitcnt lgkmcnt(0)" ::: "memory"); __builtin_amdgcn_s_barrier(); asm volatile("" ::: "memory"); } while (0)
__device__ __forceinline__ void st4_sc1(void* p, unsigned v) { asm volatile("global_store_dword %0, %1, off sc1" :: "v"(p), "v"(v) : "memory"); }
__device__ __forceinline__ unsigned ld4_sc1(const void* p) {
  unsigned r; asm volatile("global_load_dword %0, %1, off sc1\n\ts_waitcnt vmcnt(0)" : "=&v"(r) : "v"(p) : "memory"); return r;
}
template <int CTRL> __device__ __forceinline__ unsigned dppu(unsigned x) { return (unsigned)__builtin_amdgcn_update_dpp(0, (int)x, CTRL, 0xf, 0xf, false); }
__device__ __forceinline__ unsigned rowmin16(unsigned v) {
  v = min(v, dppu<0x128>(v)); v = min(v, dppu<0x124>(v)); v = min(v, dppu<0x122>(v)); v = min(v, dppu<0x121>(v));
  return v;
}
__device__ __forceinline__ unsigned rdl(unsigned v, int l) { return (unsigned)__builtin_amdgcn_readlane((int)v, l); }
constexpr float SCALE = 0.08838834764831845f;
constexpr float LOG2E = 1.4426950408889634f;
template <int N> __device__ __forceinline__ void delay64() {
  if (N >= 16) { __builtin_amdgcn_s_sleep(15); delay64<(N >= 16 ? N - 15 : 0)>(); } else if (N > 0) __builtin_amdgcn_s_sleep(N);
}
__device__ __forceinline__ void sw16(float x, float& a, float& b) {
  a = x; b = x; asm volatile("s_nop 1\n\tv_permlane16_swap_b32 %0, %1" : "+v"(a), "+v"(b));
}
__device__ __forceinline__ void sw32(float x, float& a, float& b) {
  a = x; b = x; asm volatile("s_nop 1\n\tv_permlane32_swap_b32 %0, %1" : "+v"(a), "+v"(b));
}
__device__ __forceinline__ float red16_32_max(float x) {
  float a, b; sw16(x, a, b); x = fmaxf(a, b); sw32(x, a, b); return fmaxf(a, b);
}
__device__ __forceinline__ float red16_32_add(float x) {
  float a, b; sw16(x, a, b); x = a + b; sw32(x, a, b); return a + b;
}
__device__ __forceinline__ unsigned cvtpk(float a, float b) { unsigned r; asm("v_cvt_pk_bf16_f32 %0, %1, %2" : "=v"(r) : "v"(a), "v"(b)); return r; }
__device__ __forceinline__ float max3(float a, float b, float c) { float r; asm("v_max3_f32 %0, %1, %2, %3" : "=v"(r) : "v"(a), "v"(b), "v"(c)); return r; }
__device__ __forceinline__ float max2(float a, float b) { float r; asm("v_max_f32 %0, %1, %2" : "=v"(r) : "v"(a), "v"(b)); return r; }
__device__ __forceinline__ unsigned ror8u(unsigned x) { return (unsigned)__builtin_amdgcn_update_dpp(0, (int)x, 0x128, 0xf, 0xf, false); }
__device__ __forceinline__ float ror8(float x) { return __builtin_bit_cast(float, ror8u(__builtin_bit_cast(unsigned, x))); }
__device__ __forceinline__ float add_ror8(float x) {
  return x + __builtin_bit_cast(float, __builtin_amdgcn_update_dpp(0, __builtin_bit_cast(int, x), 0x128, 0xf, 0xf, false));
}

#define RSL 4
__device__ __forceinline__ void ini_request(const char* ws, unsigned lds) {
  const char* p = uni(ws + OFF_INI);
  asm volatile("s_mov_b32 m0, %2\n\ts_nop 3\n\tglobal_load_lds_dword %0, %1 sc1\n\ts_add_u32 m0, %2, 252\n\ts_nop 0\n\tglobal_load_lds_dword %0, %1 offset:4 sc1"
               :: "v"(lane_id() * 128), "s"(p), "s"(lds) : "memory", "m0", "scc");
}
__device__ __forceinline__ unsigned ini_resolve(const char* ws, const char* l, unsigned long long tag) {
  const int lane = lane_id();
  u32x2 v = u32x2{~(unsigned)tag, 0u};
  if (l) v = u32x2{*(const volatile LAS unsigned*)(l + lane * 4), *(const volatile LAS unsigned*)(l + 256 + lane * 4)};
  for (int spins = 0; !__all(v.x == (unsigned)tag && (v.y >> 16) == (unsigned)(tag >> 48)) && spins < SPIN_CAP; ++spins) {
    if (spins) __builtin_amdgcn_s_sleep(RSL);
    asm volatile("global_load_dwordx2 %0, %1, off sc1\n\ts_waitcnt vmcnt(0)" : "=&v"(v) : "v"(ws + OFF_INI + lane * 128) : "memory");
  }
  const unsigned long long up = __ballot((v.y & 3u) == 1u), dn = __ballot((v.y & 3u) == 2u);
  unsigned par = 0;
#pragma unroll
  for (int c = 0; c < 3; ++c) { const unsigned long long m = (0x249ull << c) * 0x0001000100010001ull; par |= (__popcll(up & m) > __popcll(dn & m) ? 0u : 1u) << c; }
  par = __builtin_amdgcn_readfirstlane(par);
  asm volatile("" : "+s"(par));
  return par;
}
__device__ __forceinline__ unsigned ini_resolve1(char* smem, int wave_id_, const char* ws, unsigned long long tag) {
  volatile LAS unsigned* w = (volatile LAS unsigned*)(smem + L_SLOT + 800);
  if (WAVE_ID == 1) *w = ini_resolve(ws, nullptr, tag);
  BAR();
  return __builtin_amdgcn_readfirstlane(*w);
}
template <int M0> __device__ __forceinline__ unsigned qkv_avail(const unsigned* line) {
  const unsigned v = ld4_sc1(line + (lane_id() & 15));
  unsigned r = NSEG;
#pragma unroll
  for (int m = M0; m < 3; ++m) {
    unsigned a = rdl(v, m);
#pragma unroll
    for (int p = 1; p < 3; ++p) { const unsigned w = rdl(v, 4 * p + m); if (a >= (w >> 8)) a = max(a, w & 255u); }
    r = min(r, a);
  }
  return r;
}
__device__ __forceinline__ void wait_l1(const unsigned* cnt, unsigned n, unsigned* tmo) {
  const int lane = lane_id();
  for (int spins = 0;; ++spins) {
    const unsigned v = ld4_sc1(cnt + (PL1 + (lane & 31)) * 32);
    if (__all(v >= n)) break;
    __builtin_amdgcn_s_sleep(8);
    if (give_up(spins, tmo, 12)) break;
  }
}
#define GKV 1
__device__ __forceinline__ void wait_k0(const unsigned* cnt, int h, unsigned* tmo, bool kv = true) {
  for (int spins = 0;; ++spins) {
    const int l = lane_id();
    const unsigned v = ld4_sc1(cnt + (PQKV + h) * 32 + (l == 0 || !kv ? 3 : 1 + (l & 1)));
    if (__all(v >= (l == 0 ? 1u : (unsigned)GKV))) break;
    __builtin_amdgcn_s_sleep(32);
    if (give_up(spins, tmo, 13)) break;
  }
}
#define IS(k) do { } while (0)
#define KTON 0
#define WCLK(slot, b) do { } while (0)
#define WTM(slot, b, k) do { } while (0)
#define WTS(slot, b, k) do { } while (0)
#define KTS(k_) do { } while (0)
#define STS(ws_, slot_, k_) do { } while (0)
typedef __bf16 bf16x2 __attribute__((ext_vector_type(2)));
__device__ __forceinline__ f32x2 pkfma(f32x2 a, f32x2 b, f32x2 c) { return __builtin_elementwise_fma(a, b, c); }
__device__ __forceinline__ float wavesum(float v) {
  v += __builtin_bit_cast(float, dppu<0x128>(__builtin_bit_cast(unsigned, v))); v += __builtin_bit_cast(float, dppu<0x124>(__builtin_bit_cast(unsigned, v)));
  v += __builtin_bit_cast(float, dppu<0x122>(__builtin_bit_cast(unsigned, v))); v += __builtin_bit_cast(float, dppu<0x121>(__builtin_bit_cast(unsigned, v)));
  return red16_32_add(v);
}
__device__ __forceinline__ void bst16(rsrc_t r, unsigned off, unsigned soff, u32x4 v) { __builtin_amdgcn_raw_buffer_store_b128(v, r, off + soff, 0, 16); }
__device__ __forceinline__ float adds(unsigned v, unsigned sel, float c) { return __builtin_amdgcn_fdot2_f32_bf16(__builtin_bit_cast(bf16x2, v), __builtin_bit_cast(bf16x2, sel), c, false); }
__device__ __forceinline__ void sum4(const u32x4 (&p)[4], f32x2 (&a)[4]) {
  unsigned slo = 0x00003F80u, shi = 0x3F800000u;
  asm("" : "+s"(slo), "+s"(shi));
#pragma unroll
  for (int e = 0; e < 4; ++e) {
    float lo = bflo(p[0][e]), hi = bfhi(p[0][e]);
#pragma unroll
    for (int q = 1; q < 4; ++q) { lo = adds(p[q][e], slo, lo); hi = adds(p[q][e], shi, hi); }
    a[e] = f32x2{lo, hi};
  }
}
__device__ __forceinline__ u32x4 norm8(const f32x2 (&v)[4], const char* mt, float mb, float rden) {
  const f32x4 h0 = *(const f32x4*)(mt + 4096), h1 = *(const f32x4*)(mt + 4096 + 1024), c0 = *(const f32x4*)(mt + 8192), c1 = *(const f32x4*)(mt + 8192 + 1024);
  const f32x2 m2 = f32x2{mb, mb}, d2 = f32x2{rden, rden};
  const f32x2 r0 = pkfma(v[0] - m2, f32x2{c0[0], c0[1]} * d2, f32x2{h0[0], h0[1]}), r1 = pkfma(v[1] - m2, f32x2{c0[2], c0[3]} * d2, f32x2{h0[2], h0[3]});
  const f32x2 r2 = pkfma(v[2] - m2, f32x2{c1[0], c1[1]} * d2, f32x2{h1[0], h1[1]}), r3 = pkfma(v[3] - m2, f32x2{c1[2], c1[3]} * d2, f32x2{h1[2], h1[3]});
  return u32x4{pack2(r0[0], r0[1]), pack2(r1[0], r1[1]), pack2(r2[0], r2[1]), pack2(r3[0], r3[1])};
}
__device__ __forceinline__ void half_load(const char* P, const u16* res, int row, int hf, int lane, f32x2 (&a)[4], u32x4& rv) {
  const rsrc_t rp = mkrs(P), rr = mkrs(res);
  const unsigned po = TLP ? (unsigned)((row >> 5) * 32 * PROW + (hf * 16 + (lane >> 2)) * 2048 + (row & 31) * 64 + (lane & 3) * 16) : (unsigned)(row * PROW + hf * 1024 + lane * 16), ro = (unsigned)(row * DM * 2 + hf * 1024 + lane * 16);
  u32x4 p[4];
#pragma unroll
  for (int q = 0; q < 4; ++q) p[q] = bld16(rp, po + q * (TLP ? 65536 : 2048));
  if (res) rv = bld16(rr, ro);
  sum4(p, a);
}
__device__ __forceinline__ void fin_half(const char* P, const u16* res, const char* gbt, u16* __restrict__ o1, int row, int hf, int lane) {
  f32x2 a[4]; u32x4 rv;
  half_load(P, res, row, hf, lane, a, rv);
  const u32x4 gv = *(const u32x4*)(gbt + hf * 1024 + lane * 16);
  u32x4 o;
#pragma unroll
  for (int e = 0; e < 4; ++e) { const f32x2 s = pkfma(f32x2{bflo(gv[e]), bfhi(gv[e])}, a[e], f32x2{bflo(rv[e]), bfhi(rv[e])}); o[e] = pack2(s[0], s[1]); }
  st16_sc1(o1 + (size_t)row * DM + hf * 512 + lane * 8, o);
}
__device__ __forceinline__ void ln_half(const u32x4 (&p)[4], u32x4 rv, const f32x4 (&tb)[6], volatile LAS float* xs, const char* x1, unsigned xo, const char* h2, unsigned ho,
                                        int wave, int lane) {
  f32x2 a[4], v[4];
  sum4(p, a);
  u32x4 o;
#pragma unroll
  for (int e = 0; e < 4; ++e) {
    const f32x2 s = pkfma(f32x2{tb[e >> 1][2 * (e & 1)], tb[e >> 1][2 * (e & 1) + 1]}, a[e], f32x2{bflo(rv[e]), bfhi(rv[e])});
    o[e] = pack2(s[0], s[1]);
    v[e] = f32x2{bflo(o[e]), bfhi(o[e])};
  }
  const float c = __builtin_bit_cast(float, __builtin_amdgcn_readfirstlane(__builtin_bit_cast(int, v[0][0])));
  f32x2 s2 = f32x2{0, 0}, q2 = s2;
#pragma unroll
  for (int i = 0; i < 4; ++i) { const f32x2 d = v[i] - f32x2{c, c}; s2 += d; q2 = pkfma(d, d, q2); }
  float s = s2[0] + s2[1], q = q2[0] + q2[1];
#define DA(x, k) x += __builtin_bit_cast(float, dppu<k>(__builtin_bit_cast(unsigned, x)))
  DA(s, 0x128); DA(q, 0x128); DA(s, 0x124); DA(q, 0x124); DA(s, 0x122); DA(q, 0x122); DA(s, 0x121); DA(q, 0x121);
#undef DA
  {
    asm volatile("s_nop 1\n\tv_permlane16_swap_b32 %0, %1" : "+v"(s), "+v"(q));
    s += q; q = s;
    asm volatile("s_nop 1\n\tv_permlane32_swap_b32 %0, %1" : "+v"(s), "+v"(q));
    s += q;
  }
  if (lane < 48 && (lane & 15) == 0) xs[wave * 4 + (lane >> 4)] = lane == 32 ? c : s;
  st16s(x1, xo, o);
  BAR();
  const f32x4 u0 = *(const volatile LAS f32x4*)(xs + (wave & 6) * 4), u1 = *(const volatile LAS f32x4*)(xs + (wave | 1) * 4);
  const float m0 = u0[0] * (2.0f / DM), m1 = u1[0] * (2.0f / DM);
  const float dm = (u0[2] - u1[2]) + (m0 - m1);
  const float mean = ((u0[2] + m0) + (u1[2] + m1)) * 0.5f;
  const float qq = ((u0[1] - u0[0] * m0) + (u1[1] - u1[0] * m1)) + dm * dm * (DM / 4);
  const float mb = bfr(mean), vb = bfr(fmaxf(qq, 0.0f) * (1.0f / DM));
  const float rden = __builtin_amdgcn_rcpf(bfr(__builtin_amdgcn_sqrtf(bfr(vb + 1e-5f))));
  const f32x2 m2 = f32x2{mb, mb}, d2 = f32x2{rden, rden};
  u32x4 r;
#pragma unroll
  for (int e = 0; e < 4; ++e) {
    const f32x2 t = pkfma(v[e] - m2, f32x2{tb[4 + (e >> 1)][2 * (e & 1)], tb[4 + (e >> 1)][2 * (e & 1) + 1]} * d2, f32x2{tb[2 + (e >> 1)][2 * (e & 1)], tb[2 + (e >> 1)][2 * (e & 1) + 1]});
    r[e] = pack2(t[0], t[1]);
  }
  st16s(h2, ho, r);
}
__device__ __forceinline__ void row_table(char* tab, const float* mod, const unsigned* cnt, unsigned* tmo, int tid) {
  wait_l1(cnt, 9u, tmo);
  const rsrc_t rm = mkrs((const char*)mod + OFF_MOD2);
#pragma unroll
  for (int i = 0; i < 2; ++i) {
    const int u = tid + 512 * i, a = u >> 8, c = u & 255;
    f32x4 v = __builtin_bit_cast(f32x4, bld16(rm, (unsigned)u * 16));
    v = f32x4{bfr(v[0]), bfr(v[1]), bfr(v[2]), bfr(v[3])};
    if (a == 2) v = f32x4{bfr(1.0f + v[0]), bfr(1.0f + v[1]), bfr(1.0f + v[2]), bfr(1.0f + v[3])};
    if (a < 3) *(f32x4*)(tab + a * 4096 + ((c >> 7) * 2 + (c & 1)) * 1024 + ((c >> 1) & 63) * 16) = v;
    else *(u32x2*)(tab + 12288 + c * 8) = u32x2{pack2(v[0], v[1]), pack2(v[2], v[3])};
  }
  __syncthreads();
}
#define LS(i) do { } while (0)
#define LNSL 2
__device__ __forceinline__ void row_role(char* smem, int wave_id_, int grp, int set, const float* mod, const char* P, const u16* x, u16* x1, char* h2c,
                                         unsigned* cnt, unsigned* tmo, int slot, unsigned long long* mts) {
  const int tid = opaque_tid(), lane = tid & 63, wave = WAVE_ID;
  if (tid == 0) *(volatile LAS unsigned*)(smem + 16384 + 256) = 0u;
  row_table(smem, mod, cnt, tmo, tid);
  static_assert(TLP, "");
  const int hf = wave & 1, r4 = 4 * grp + (wave >> 1);
  const unsigned pw2 = (unsigned)(PWO + ((lane >> 2) & 3) * 8 + hf * 4 + (lane & 3)) * 128;
  unsigned* myp = cnt + (PLN + 8 * set + grp) * 32;
  unsigned pv = 0;
  u32x4 p[4], rv, rn;
  bool req = false;
  const char* pb = uni(P); const char* cb = uni((const char*)cnt); const char* xs_ = uni((const char*)x);
  auto publish = [&](int n) {
    if (lane == 0 && (__hip_atomic_fetch_add((LAS unsigned*)(smem + 16384 + 256), 1u, __ATOMIC_RELAXED, __HIP_MEMORY_SCOPE_WORKGROUP) & 7u) == 7u) st4_sc1(myp, (unsigned)n);
  };
  f32x4 tb[6];
#pragma unroll
  for (int i = 0; i < 6; ++i) tb[i] = *(const f32x4*)(smem + (i >> 1) * 4096 + hf * 2048 + (i & 1) * 1024 + lane * 16);
  const unsigned po = (unsigned)((hf * 16 + (lane >> 2)) * 2048 + r4 * 64 + (lane & 3) * 16), xo = (unsigned)(r4 * DM * 2 + hf * 1024 + lane * 16);
  const unsigned ho = (unsigned)(r4 * 128 + (((lane & 7) ^ (r4 & 7)) << 4) + (lane >> 3) * 4096 + hf * 32768);
  rn = *(const u32x4*)(xs_ + set * 65536 + xo);
  for (int bb = set; bb < NSEG; bb += 2) {
    if (!req) {
      for (int spins = 0; !__all(pv > (unsigned)bb); ++spins) {
        pv = ld4_sc1((const char*)cnt + pw2);
        if (__all(pv > (unsigned)bb)) break;
        __builtin_amdgcn_s_sleep(LNSL);
        if (give_up(spins, tmo, 11)) break;
      }
    }
    WTM(slot, bb, 0);
    LS(0);
    asm volatile("s_nop 4\n\tglobal_load_dwordx4 %0, %5, %9 sc1\n\tglobal_load_dwordx4 %1, %6, %9 sc1\n\tglobal_load_dwordx4 %2, %7, %9 sc1\n\tglobal_load_dwordx4 %3, %8, %9 sc1\n\t"
                 "global_load_dword %4, %10, %11 sc1\n\ts_waitcnt vmcnt(0)"
                 : "=&v"(p[0]), "=&v"(p[1]), "=&v"(p[2]), "=&v"(p[3]), "=&v"(pv)
                 : "v"(po), "v"(po + 65536), "v"(po + 131072), "v"(po + 196608), "s"(pb + (size_t)bb * 32 * PROW), "v"(pw2), "s"(cb) : "memory");
    asm volatile("v_mov_b32 %0, %4\n\tv_mov_b32 %1, %5\n\tv_mov_b32 %2, %6\n\tv_mov_b32 %3, %7" : "=&v"(rv.x), "=&v"(rv.y), "=&v"(rv.z), "=&v"(rv.w) : "v"(rn.x), "v"(rn.y), "v"(rn.z), "v"(rn.w) : "memory");
    if (bb + 2 < NSEG) rn = *(const u32x4*)(xs_ + (size_t)(bb + 2) * 65536 + xo);
    LS(1);
    if (req) publish(bb >> 1);
    ln_half(p, rv, tb, (volatile LAS float*)(smem + 16384 + ((bb >> 1) & 1) * 128), uni((const char*)x1) + (size_t)bb * 65536, xo, uni(h2c) + (size_t)bb * 65536, ho, wave, lane);
    LS(2);
    req = bb + 2 < NSEG && __all(pv > (unsigned)(bb + 2));
    if (!req) {
      if (bb + 2 < NSEG) pv = ld4_sc1((const char*)cnt + pw2);
      WAIT_V(0);
      LS(3);
      publish((bb >> 1) + 1);
    }
    WTM(slot, bb, 2);
  }
}
#define L1G 0
__device__ __forceinline__ void l1_role(char* smem, int wave_id_, int c, const u16* __restrict__ x, const float* __restrict__ mod, const u16* __restrict__ cvec,
                                        const u16* __restrict__ wmod, char* __restrict__ h1c, unsigned*& cnt, unsigned* tmo, unsigned long long* mts, unsigned long long tag) {
  const int tid = opaque_tid(), lane = tid & 63, wave = WAVE_ID;
  u32x4 xr[8][2];
  const u16* src = x + (size_t)(8 * c + wave) * DM + lane * 8;
  xr[0][0] = ldnt<2>(src); xr[0][1] = ldnt<2>(src + 512);
  {
    const float sva = bf2f(cvec[tid]), svb = bf2f(cvec[tid + 512]);
    cnt += (ini_resolve1(smem, wave_id_, (const char*)mod - OFF_MOD, tag) >> 2) << 11;
    if (wave == 7) IS(6);
    f32x4 m[4];
    const rsrc_t rm = mkrs(mod);
#pragma unroll
    for (int k = 0; k < 4; ++k) m[k] = __builtin_bit_cast(f32x4, bld16(rm, (unsigned)(k * (2 * DM) + tid * 4) * 4));
    f32x4 v = (m[0] + m[1]) + (m[2] + m[3]);
    v = f32x4{bfr(v[0]), bfr(v[1]), bfr(v[2]), bfr(v[3])};
    const int a = tid >> 8, cc = tid & 255;
    if (a) v = f32x4{bfr(1.0f + v[0]), bfr(1.0f + v[1]), bfr(1.0f + v[2]), bfr(1.0f + v[3])};
    *(f32x4*)(smem + 4096 + a * 4096 + ((cc >> 7) * 2 + (cc & 1)) * 1024 + ((cc >> 1) & 63) * 16) = v;
    float* sv = (float*)(smem + 12288);
    sv[tid] = bfr(sva / (1.0f + __expf(-sva))); sv[tid + 512] = bfr(svb / (1.0f + __expf(-svb)));
  }
  __syncthreads();
  if (wave == 7) IS(7);
#pragma unroll
  for (int p = 0; p < 8; ++p) {
    f32x2 v[2][4];
#pragma unroll
    for (int i = 0; i < 2; ++i)
#pragma unroll
      for (int e = 0; e < 4; ++e) v[i][e] = f32x2{bflo(xr[p][i][e]), bfhi(xr[p][i][e])};
    const f32x2 s2 = ((v[0][0] + v[0][1]) + (v[0][2] + v[0][3])) + ((v[1][0] + v[1][1]) + (v[1][2] + v[1][3]));
    const float mean = wavesum(s2[0] + s2[1]) * (1.0f / DM);
    f32x2 q2 = f32x2{0, 0};
#pragma unroll
    for (int i = 0; i < 8; ++i) { const f32x2 d = v[i >> 2][i & 3] - f32x2{mean, mean}; q2 = pkfma(d, d, q2); }
    const float q = wavesum(q2[0] + q2[1]);
    if (p == 0 && wave == 0) STS((char*)tmo - OFF_TMO, 128 + (c >> 3) * 32 + 24 + (c & 7), 0);
    const float mb = bfr(mean), vb = bfr(q * (1.0f / DM));
    const float rden = 1.0f / bfr(sqrtf(bfr(vb + 1e-5f)));
    const int row = 256 * p + 8 * c + wave, rb = row & 31;
    char* dst = h1c + (size_t)(row >> 5) * 65536 + (lane >> 3) * 4096 + rb * 128 + (((lane & 7) ^ (rb & 7)) << 4);
    const char* ml = smem + lane * 16;
#pragma unroll
    for (int i = 0; i < 2; ++i) st16_sc1(dst + i * 32768, norm8(v[i], ml + i * 2048, mb, rden));
    WAIT_V(0);
    __builtin_amdgcn_s_barrier();
    if (tid == 0) st4_sc1(cnt + (PL1 + c) * 32, (unsigned)(p + 1));
    if (p == 0) {
      if (L1G) wait_k0(cnt, c, tmo, L1G == 1);
#pragma unroll
      for (int pp = 1; pp < 8; ++pp) { xr[pp][0] = ldnt<2>(src + (size_t)pp * 256 * DM); xr[pp][1] = ldnt<2>(src + (size_t)pp * 256 * DM + 512); }
    }
    if (wave == 0) WTS(c, p, 0);
    if (p == 0 && wave == 0) STS((char*)tmo - OFF_TMO, 128 + (c >> 3) * 32 + 24 + (c & 7), 1);
  }
  wait_k0(cnt, c, tmo);
  {
    const int cg = tid & 15, r0 = tid >> 4;
    const u16* wp = wmod + (size_t)r0 * (6 * DM) + 2 * DM + 128 * c + cg * 8;
    const float* sv = (const float*)(smem + 12288) + r0;
    f32x2 acc[4] = {f32x2{0, 0}, f32x2{0, 0}, f32x2{0, 0}, f32x2{0, 0}};
#pragma unroll 1
    for (int o = 0; o < 4; ++o) {
      u32x4 w[8];
#pragma unroll
      for (int i = 0; i < 8; ++i) w[i] = ldnt<1>(wp + (size_t)(o * 8 + i) * 32 * (6 * DM));
#pragma unroll
      for (int i = 0; i < 8; ++i) {
        const float s = sv[(o * 8 + i) * 32];
#pragma unroll
        for (int e = 0; e < 4; ++e) acc[e] = pkfma(f32x2{s, s}, f32x2{bflo(w[i][e]), bfhi(w[i][e])}, acc[e]);
      }
    }
    float* red = (float*)(smem + 16384);
    *(f32x4*)(red + r0 * 128 + cg * 8) = f32x4{acc[0][0], acc[0][1], acc[1][0], acc[1][1]};
    *(f32x4*)(red + r0 * 128 + cg * 8 + 4) = f32x4{acc[2][0], acc[2][1], acc[3][0], acc[3][1]};
    __syncthreads();
    if (tid < 128) {
      float s = 0;
#pragma unroll
      for (int r = 0; r < 32; ++r) s += red[r * 128 + tid];
      st4_sc1((char*)mod + OFF_MOD2 + (128 * c + tid) * 4, __builtin_bit_cast(unsigned, s));
    }
    WAIT_V(0);
    __builtin_amdgcn_s_barrier();
    if (tid == 0) st4_sc1(cnt + (PL1 + c) * 32, 9u);
  }
}
enum { R_QKV = 0, R_PART = 1, R_F1 = 2, R_PART2 = 3 };
struct Dep {
  const char* base; unsigned voff; unsigned mul, add;
};
__device__ __forceinline__ unsigned dep_eval(unsigned v, const Dep& d) {
  v = rowmin16(v * d.mul + d.add);
  return min(min(rdl(v, 0), rdl(v, 16)), min(rdl(v, 32), rdl(v, 48)));
}
typedef unsigned u32x32 __attribute__((ext_vector_type(32)));
typedef unsigned u32x16 __attribute__((ext_vector_type(16)));
__device__ __forceinline__ void fast_ld0(const u32x32 (&W)[4], u32x16& pd, unsigned ea, unsigned da, int& b, int& lf, int cp, unsigned sa, unsigned mu, unsigned ad,
    unsigned rw, unsigned ab, const char* p1, const char* p0, unsigned l1, unsigned l0, const unsigned (&av)[8]) {
  asm volatile("s_setprio 0\n\t"
    "s_waitcnt vmcnt(12)\n\t"
    "s_barrier\n\t"
    "ds_read_b128 v[96:99], %[ea] offset:0\n\t"
    "ds_read_b128 v[100:103], %[ea] offset:2048\n\t"
    "ds_read_b128 v[104:107], %[da] offset:0\n\t"
    "ds_read_b128 v[108:111], %[da] offset:2048\n\t"
    "ds_read_b128 v[112:115], %[ea] offset:4096\n\t"
    "ds_read_b128 v[116:119], %[ea] offset:6144\n\t"
    "s_branch 2f\n\t"
    "1:\n\t"
    "s_waitcnt vmcnt(12)\n\t"
    "s_nop 1\n\t"
    "s_barrier\n\t"
    "2:\n\t"
    "s_waitcnt lgkmcnt(4)\n\t"
    "v_mfma_f32_16x16x32_bf16 v[64:67], v[128:131], v[96:99], 0\n\t"
    "ds_read_b128 v[120:123], %[da] offset:4096\n\t"
    "s_add_u32 m0, %[l1], 0\n\t"
    "ds_write_b128 %[rw], v[80:83] offset:0\n\t"
    "v_mfma_f32_16x16x32_bf16 v[68:71], v[132:135], v[96:99], 0\n\t"
    "ds_read_b128 v[124:127], %[da] offset:6144\n\t"
    "global_load_lds_dwordx4 %[av0], s[36:37] sc1\n\t"
    "ds_write_b128 %[rw], v[84:87] offset:1024\n\t"
    "v_mfma_f32_16x16x32_bf16 v[72:75], v[128:131], v[100:103], 0\n\t"
    "s_add_u32 m0, %[l1], 32768\n\t"
    "ds_write_b128 %[rw], v[88:91] offset:2048\n\t"
    "v_mfma_f32_16x16x32_bf16 v[76:79], v[132:135], v[100:103], 0\n\t"
    "global_load_lds_dwordx4 %[av1], s[36:37] sc1\n\t"
    "ds_write_b128 %[rw], v[92:95] offset:3072\n\t"
    "s_waitcnt lgkmcnt(8)\n\t"
    "v_mfma_f32_16x16x32_bf16 v[64:67], v[136:139], v[104:107], v[64:67]\n\t"
    "ds_read_b128 v[96:99], %[ea] offset:8192\n\t"
    "s_add_u32 m0, %[l1], 4096\n\t"
    "v_mfma_f32_16x16x32_bf16 v[68:71], v[140:143], v[104:107], v[68:71]\n\t"
    "ds_read_b128 v[100:103], %[ea] offset:10240\n\t"
    "global_load_lds_dwordx4 %[av2], s[36:37] sc1\n\t"
    "v_mfma_f32_16x16x32_bf16 v[72:75], v[136:139], v[108:111], v[72:75]\n\t"
    "s_add_u32 m0, %[l1], 36864\n\t"
    "v_mfma_f32_16x16x32_bf16 v[76:79], v[140:143], v[108:111], v[76:79]\n\t"
    "global_load_lds_dwordx4 %[av3], s[36:37] sc1\n\t"
    "s_waitcnt lgkmcnt(8)\n\t"
    "v_mfma_f32_16x16x32_bf16 v[64:67], v[144:147], v[112:115], v[64:67]\n\t"
    "ds_read_b128 v[104:107], %[da] offset:8192\n\t"
    "s_add_u32 m0, %[l1], 8192\n\t"
    "v_mfma_f32_16x16x32_bf16 v[68:71], v[148:151], v[112:115], v[68:71]\n\t"
    "ds_read_b128 v[108:111], %[da] offset:10240\n\t"
    "global_load_lds_dwordx4 %[av4], s[36:37] sc1\n\t"
    "v_mfma_f32_16x16x32_bf16 v[72:75], v[144:147], v[116:119], v[72:75]\n\t"
    "s_add_u32 m0, %[l1], 40960\n\t"
    "v_mfma_f32_16x16x32_bf16 v[76:79], v[148:151], v[116:119], v[76:79]\n\t"
    "global_load_lds_dwordx4 %[av5], s[36:37] sc1\n\t"
    "s_waitcnt lgkmcnt(7)\n\t"
    "v_mfma_f32_16x16x32_bf16 v[64:67], v[152:155], v[120:123], v[64:67]\n\t"
    "ds_read_b128 v[112:115], %[ea] offset:12288\n\t"
    "s_add_u32 m0, %[l1], 12288\n\t"
    "v_mfma_f32_16x16x32_bf16 v[68:71], v[156:159], v[120:123], v[68:71]\n\t"
    "ds_read_b128 v[116:119], %[ea] offset:14336\n\t"
    "global_load_lds_dwordx4 %[av6], s[36:37] sc1\n\t"
    "v_mfma_f32_16x16x32_bf16 v[72:75], v[152:155], v[124:127], v[72:75]\n\t"
    "s_add_u32 m0, %[l1], 45056\n\t"
    "v_mfma_f32_16x16x32_bf16 v[76:79], v[156:159], v[124:127], v[76:79]\n\t"
    "global_load_lds_dwordx4 %[av7], s[36:37] sc1\n\t"
    "s_waitcnt lgkmcnt(4)\n\t"
    "v_mfma_f32_16x16x32_bf16 v[64:67], v[160:163], v[96:99], v[64:67]\n\t"
    "ds_read_b128 v[120:123], %[da] offset:12288\n\t"
    "v_mfma_f32_16x16x32_bf16 v[68:71], v[164:167], v[96:99], v[68:71]\n\t"
    "ds_read_b128 v[124:127], %[da] offset:14336\n\t"
    "v_mfma_f32_16x16x32_bf16 v[72:75], v[160:163], v[100:103], v[72:75]\n\t"
    "v_mfma_f32_16x16x32_bf16 v[76:79], v[164:167], v[100:103], v[76:79]\n\t"
    "s_waitcnt lgkmcnt(4)\n\t"
    "v_mfma_f32_16x16x32_bf16 v[64:67], v[168:171], v[104:107], v[64:67]\n\t"
    "ds_read_b128 v[96:99], %[ea] offset:16384\n\t"
    "v_mfma_f32_16x16x32_bf16 v[68:71], v[172:175], v[104:107], v[68:71]\n\t"
    "ds_read_b128 v[100:103], %[ea] offset:18432\n\t"
    "v_mfma_f32_16x16x32_bf16 v[72:75], v[168:171], v[108:111], v[72:75]\n\t"
    "v_mfma_f32_16x16x32_bf16 v[76:79], v[172:175], v[108:111], v[76:79]\n\t"
    "s_waitcnt lgkmcnt(4)\n\t"
    "v_mfma_f32_16x16x32_bf16 v[64:67], v[176:179], v[112:115], v[64:67]\n\t"
    "ds_read_b128 v[104:107], %[da] offset:16384\n\t"
    "v_mfma_f32_16x16x32_bf16 v[68:71], v[180:183], v[112:115], v[68:71]\n\t"
    "ds_read_b128 v[108:111], %[da] offset:18432\n\t"
    "v_mfma_f32_16x16x32_bf16 v[72:75], v[176:179], v[116:119], v[72:75]\n\t"
    "v_mfma_f32_16x16x32_bf16 v[76:79], v[180:183], v[116:119], v[76:79]\n\t"
    "s_waitcnt lgkmcnt(4)\n\t"
    "v_mfma_f32_16x16x32_bf16 v[64:67], v[184:187], v[120:123], v[64:67]\n\t"
    "ds_read_b128 v[112:115], %[ea] offset:20480\n\t"
    "v_mfma_f32_16x16x32_bf16 v[68:71], v[188:191], v[120:123], v[68:71]\n\t"
    "ds_read_b128 v[116:119], %[ea] offset:22528\n\t"
    "v_mfma_f32_16x16x32_bf16 v[72:75], v[184:187], v[124:127], v[72:75]\n\t"
    "v_mfma_f32_16x16x32_bf16 v[76:79], v[188:191], v[124:127], v[76:79]\n\t"
    "s_waitcnt vmcnt(12)\n\t"
    "s_barrier\n\t"
    "s_waitcnt lgkmcnt(4)\n\t"
    "v_mfma_f32_16x16x32_bf16 v[64:67], v[192:195], v[96:99], v[64:67]\n\t"
    "ds_read_b128 v[120:123], %[da] offset:20480\n\t"
    "s_add_u32 m0, %[l0], 0\n\t"
    "v_mfma_f32_16x16x32_bf16 v[68:71], v[196:199], v[96:99], v[68:71]\n\t"
    "ds_read_b128 v[124:127], %[da] offset:22528\n\t"
    "global_load_lds_dwordx4 %[av0], s[38:39] sc1\n\t"
    "v_mfma_f32_16x16x32_bf16 v[72:75], v[192:195], v[100:103], v[72:75]\n\t"
    "s_add_u32 m0, %[l0], 32768\n\t"
    "v_mfma_f32_16x16x32_bf16 v[76:79], v[196:199], v[100:103], v[76:79]\n\t"
    "global_load_lds_dwordx4 %[av1], s[38:39] sc1\n\t"
    "s_waitcnt lgkmcnt(4)\n\t"
    "v_mfma_f32_16x16x32_bf16 v[64:67], v[200:203], v[104:107], v[64:67]\n\t"
    "ds_read_b128 v[96:99], %[ea] offset:24576\n\t"
    "s_add_u32 m0, %[l0], 4096\n\t"
    "v_mfma_f32_16x16x32_bf16 v[68:71], v[204:207], v[104:107], v[68:71]\n\t"
    "ds_read_b128 v[100:103], %[ea] offset:26624\n\t"
    "global_load_lds_dwordx4 %[av2], s[38:39] sc1\n\t"
    "v_mfma_f32_16x16x32_bf16 v[72:75], v[200:203], v[108:111], v[72:75]\n\t"
    "s_add_u32 m0, %[l0], 36864\n\t"
    "v_mfma_f32_16x16x32_bf16 v[76:79], v[204:207], v[108:111], v[76:79]\n\t"
    "global_load_lds_dwordx4 %[av3], s[38:39] sc1\n\t"
    "s_add_u32 s36, s36, %[ab]\n\t"
    "s_addc_u32 s37, s37, 0\n\t"
    "s_waitcnt lgkmcnt(4)\n\t"
    "v_mfma_f32_16x16x32_bf16 v[64:67], v[208:211], v[112:115], v[64:67]\n\t"
    "ds_read_b128 v[104:107], %[da] offset:24576\n\t"
    "s_add_u32 m0, %[l0], 8192\n\t"
    "v_mfma_f32_16x16x32_bf16 v[68:71], v[212:215], v[112:115], v[68:71]\n\t"
    "ds_read_b128 v[108:111], %[da] offset:26624\n\t"
    "global_load_lds_dwordx4 %[av4], s[38:39] sc1\n\t"
    "v_mfma_f32_16x16x32_bf16 v[72:75], v[208:211], v[116:119], v[72:75]\n\t"
    "ds_read_b32 v39, %[sa]\n\t"
    "s_add_u32 m0, %[l0], 40960\n\t"
    "v_mfma_f32_16x16x32_bf16 v[76:79], v[212:215], v[116:119], v[76:79]\n\t"
    "v_xor_b32 %[sa], 0x100, %[sa]\n\t"
    "global_load_lds_dwordx4 %[av5], s[38:39] sc1\n\t"
    "s_xor_b32 %[l1], %[l1], 0x10000\n\t"
    "s_waitcnt lgkmcnt(5)\n\t"
    "v_mfma_f32_16x16x32_bf16 v[64:67], v[216:219], v[120:123], v[64:67]\n\t"
    "ds_read_b128 v[112:115], %[ea] offset:28672\n\t"
    "s_add_u32 m0, %[l0], 12288\n\t"
    "v_mfma_f32_16x16x32_bf16 v[68:71], v[220:223], v[120:123], v[68:71]\n\t"
    "ds_read_b128 v[116:119], %[ea] offset:30720\n\t"
    "global_load_lds_dwordx4 %[av6], s[38:39] sc1\n\t"
    "v_mfma_f32_16x16x32_bf16 v[72:75], v[216:219], v[124:127], v[72:75]\n\t"
    "v_xor_b32 %[ea], 0x10000, %[ea]\n\t"
    "s_add_u32 m0, %[l0], 45056\n\t"
    "v_mfma_f32_16x16x32_bf16 v[76:79], v[220:223], v[124:127], v[76:79]\n\t"
    "global_load_lds_dwordx4 %[av7], s[38:39] sc1\n\t"
    "s_waitcnt lgkmcnt(5)\n\t"
    "v_mfma_f32_16x16x32_bf16 v[64:67], v[224:227], v[96:99], v[64:67]\n\t"
    "ds_read_b128 v[120:123], %[da] offset:28672\n\t"
    "v_mfma_f32_16x16x32_bf16 v[68:71], v[228:231], v[96:99], v[68:71]\n\t"
    "ds_read_b128 v[124:127], %[da] offset:30720\n\t"
    "v_mfma_f32_16x16x32_bf16 v[72:75], v[224:227], v[100:103], v[72:75]\n\t"
    "v_xor_b32 %[da], 0x10000, %[da]\n\t"
    "v_mfma_f32_16x16x32_bf16 v[76:79], v[228:231], v[100:103], v[76:79]\n\t"
    "s_waitcnt lgkmcnt(5)\n\t"
    "v_mfma_f32_16x16x32_bf16 v[64:67], v[232:235], v[104:107], v[64:67]\n\t"
    "ds_read_b128 v[96:99], %[ea] offset:0\n\t"
    "v_mfma_f32_16x16x32_bf16 v[68:71], v[236:239], v[104:107], v[68:71]\n\t"
    "ds_read_b128 v[100:103], %[ea] offset:2048\n\t"
    "v_mfma_f32_16x16x32_bf16 v[72:75], v[232:235], v[108:111], v[72:75]\n\t"
    "v_mfma_f32_16x16x32_bf16 v[76:79], v[236:239], v[108:111], v[76:79]\n\t"
    "s_waitcnt lgkmcnt(4)\n\t"
    "v_mfma_f32_16x16x32_bf16 v[64:67], v[240:243], v[112:115], v[64:67]\n\t"
    "ds_read_b128 v[104:107], %[da] offset:0\n\t"
    "v_mad_u32_u24 v39, v39, %[mu], %[ad]\n\t"
    "v_mfma_f32_16x16x32_bf16 v[68:71], v[244:247], v[112:115], v[68:71]\n\t"
    "ds_read_b128 v[108:111], %[da] offset:2048\n\t"
    "v_cmp_ge_i32 vcc, %[lf], v39\n\t"
    "s_cmp_eq_u64 vcc, 0\n\t"
    "s_addc_u32 %[lf], %[lf], 0\n\t"
    "v_mfma_f32_16x16x32_bf16 v[72:75], v[240:243], v[116:119], v[72:75]\n\t"
    "v_mfma_f32_16x16x32_bf16 v[76:79], v[244:247], v[116:119], v[76:79]\n\t"
    "v_cmp_ge_i32 vcc, %[lf], v39\n\t"
    "s_cmp_eq_u64 vcc, 0\n\t"
    "s_addc_u32 %[lf], %[lf], 0\n\t"
    "s_waitcnt lgkmcnt(4)\n\t"
    "v_mfma_f32_16x16x32_bf16 v[80:83], v[248:251], v[120:123], v[64:67]\n\t"
    "ds_read_b128 v[112:115], %[ea] offset:4096\n\t"
    "s_min_i32 %[lf], %[lf], %[cp]\n\t"
    "s_add_u32 %[b], %[b], 1\n\t"
    "v_mfma_f32_16x16x32_bf16 v[84:87], v[252:255], v[120:123], v[68:71]\n\t"
    "ds_read_b128 v[116:119], %[ea] offset:6144\n\t"
    "v_mfma_f32_16x16x32_bf16 v[88:91], v[248:251], v[124:127], v[72:75]\n\t"
    "v_mfma_f32_16x16x32_bf16 v[92:95], v[252:255], v[124:127], v[76:79]\n\t"
    "s_add_u32 s38, s38, %[ab]\n\t"
    "s_addc_u32 s39, s39, 0\n\t"
    "s_xor_b32 %[l0], %[l0], 0x10000\n\t"
    "s_cmp_lt_i32 %[b], %[lf]\n\t"
    "s_cbranch_scc1 1b\n\t"
    "s_waitcnt lgkmcnt(0)\n\t"
    "s_nop 4"
    : "=&{v[80:95]}"(pd), [ea] "+&v"(ea), [da] "+&v"(da), [b] "+&s"(b), [lf] "+&s"(lf), "+{s[36:37]}"(p1), "+{s[38:39]}"(p0), [sa] "+&v"(sa), [l1] "+&s"(l1), [l0] "+&s"(l0)
    : [rw] "v"(rw), [ab] "s"(ab), [cp] "s"(cp), [mu] "s"(mu), [ad] "v"(ad), "{v[128:159]}"(W[0]), "{v[160:191]}"(W[1]), "{v[192:223]}"(W[2]), "{v[224:255]}"(W[3]), [av0] "v"(av[0]), [av1] "v"(av[1]), [av2] "v"(av[2]), [av3] "v"(av[3]), [av4] "v"(av[4]), [av5] "v"(av[5]), [av6] "v"(av[6]), [av7] "v"(av[7])
    : "v39", "v64", "v65", "v66", "v67", "v68", "v69", "v70", "v71", "v72", "v73", "v74", "v75", "v76", "v77", "v78", "v79", "v96", "v97", "v98", "v99", "v100", "v101", "v102", "v103", "v104", "v105", "v106", "v107", "v108", "v109", "v110", "v111", "v112", "v113", "v114", "v115", "v116", "v117", "v118", "v119", "v120", "v121", "v122", "v123", "v124", "v125", "v126", "v127", "vcc", "scc", "m0", "memory");
}
__device__ __forceinline__ void fast_ld2(const u32x32 (&W)[4], u32x16& pd, unsigned ea, unsigned da, int& b, int& lf, int cp, unsigned sa, unsigned mu, unsigned ad,
    unsigned rw, unsigned ab, const char* p1, const char* p0, unsigned l1, unsigned l0, const unsigned (&av)[8]) {
  asm volatile("s_setprio 0\n\t"
    "s_waitcnt vmcnt(6)\n\t"
    "s_barrier\n\t"
    "ds_read_b128 v[96:99], %[ea] offset:0\n\t"
    "ds_read_b128 v[100:103], %[ea] offset:2048\n\t"
    "ds_read_b128 v[104:107], %[da] offset:0\n\t"
    "ds_read_b128 v[108:111], %[da] offset:2048\n\t"
    "ds_read_b128 v[112:115], %[ea] offset:4096\n\t"
    "ds_read_b128 v[116:119], %[ea] offset:6144\n\t"
    "s_branch 2f\n\t"
    "1:\n\t"
    "s_waitcnt vmcnt(6)\n\t"
    "s_nop 1\n\t"
    "s_barrier\n\t"
    "2:\n\t"
    "s_waitcnt lgkmcnt(4)\n\t"
    "v_mfma_f32_16x16x32_bf16 v[64:67], v[128:131], v[96:99], 0\n\t"
    "ds_read_b128 v[120:123], %[da] offset:4096\n\t"
    "s_add_u32 m0, %[l1], 0\n\t"
    "ds_write_b128 %[rw], v[80:83] offset:0\n\t"
    "v_mfma_f32_16x16x32_bf16 v[68:71], v[132:135], v[96:99], 0\n\t"
    "ds_read_b128 v[124:127], %[da] offset:6144\n\t"
    "global_load_lds_dwordx4 %[av0], s[36:37] sc1\n\t"
    "ds_write_b128 %[rw], v[84:87] offset:1024\n\t"
    "v_mfma_f32_16x16x32_bf16 v[72:75], v[128:131], v[100:103], 0\n\t"
    "ds_write_b128 %[rw], v[88:91] offset:2048\n\t"
    "v_mfma_f32_16x16x32_bf16 v[76:79], v[132:135], v[100:103], 0\n\t"
    "ds_write_b128 %[rw], v[92:95] offset:3072\n\t"
    "s_waitcnt lgkmcnt(8)\n\t"
    "v_mfma_f32_16x16x32_bf16 v[64:67], v[136:139], v[104:107], v[64:67]\n\t"
    "ds_read_b128 v[96:99], %[ea] offset:8192\n\t"
    "s_add_u32 m0, %[l1], 4096\n\t"
    "v_mfma_f32_16x16x32_bf16 v[68:71], v[140:143], v[104:107], v[68:71]\n\t"
    "ds_read_b128 v[100:103], %[ea] offset:10240\n\t"
    "global_load_lds_dwordx4 %[av2], s[36:37] sc1\n\t"
    "v_mfma_f32_16x16x32_bf16 v[72:75], v[136:139], v[108:111], v[72:75]\n\t"
    "v_mfma_f32_16x16x32_bf16 v[76:79], v[140:143], v[108:111], v[76:79]\n\t"
    "s_waitcnt lgkmcnt(8)\n\t"
    "v_mfma_f32_16x16x32_bf16 v[64:67], v[144:147], v[112:115], v[64:67]\n\t"
    "ds_read_b128 v[104:107], %[da] offset:8192\n\t"
    "s_add_u32 m0, %[l1], 8192\n\t"
    "v_mfma_f32_16x16x32_bf16 v[68:71], v[148:151], v[112:115], v[68:71]\n\t"
    "ds_read_b128 v[108:111], %[da] offset:10240\n\t"
    "global_load_lds_dwordx4 %[av4], s[36:37] sc1\n\t"
    "v_mfma_f32_16x16x32_bf16 v[72:75], v[144:147], v[116:119], v[72:75]\n\t"
    "v_mfma_f32_16x16x32_bf16 v[76:79], v[148:151], v[116:119], v[76:79]\n\t"
    "s_waitcnt lgkmcnt(7)\n\t"
    "v_mfma_f32_16x16x32_bf16 v[64:67], v[152:155], v[120:123], v[64:67]\n\t"
    "ds_read_b128 v[112:115], %[ea] offset:12288\n\t"
    "s_add_u32 m0, %[l1], 12288\n\t"
    "v_mfma_f32_16x16x32_bf16 v[68:71], v[156:159], v[120:123], v[68:71]\n\t"
    "ds_read_b128 v[116:119], %[ea] offset:14336\n\t"
    "global_load_lds_dwordx4 %[av6], s[36:37] sc1\n\t"
    "v_mfma_f32_16x16x32_bf16 v[72:75], v[152:155], v[124:127], v[72:75]\n\t"
    "v_mfma_f32_16x16x32_bf16 v[76:79], v[156:159], v[124:127], v[76:79]\n\t"
    "s_waitcnt lgkmcnt(4)\n\t"
    "v_mfma_f32_16x16x32_bf16 v[64:67], v[160:163], v[96:99], v[64:67]\n\t"
    "ds_read_b128 v[120:123], %[da] offset:12288\n\t"
    "v_mfma_f32_16x16x32_bf16 v[68:71], v[164:167], v[96:99], v[68:71]\n\t"
    "ds_read_b128 v[124:127], %[da] offset:14336\n\t"
    "v_mfma_f32_16x16x32_bf16 v[72:75], v[160:163], v[100:103], v[72:75]\n\t"
    "v_mfma_f32_16x16x32_bf16 v[76:79], v[164:167], v[100:103], v[76:79]\n\t"
    "s_waitcnt lgkmcnt(4)\n\t"
    "v_mfma_f32_16x16x32_bf16 v[64:67], v[168:171], v[104:107], v[64:67]\n\t"
    "ds_read_b128 v[96:99], %[ea] offset:16384\n\t"
    "v_mfma_f32_16x16x32_bf16 v[68:71], v[172:175], v[104:107], v[68:71]\n\t"
    "ds_read_b128 v[100:103], %[ea] offset:18432\n\t"
    "v_mfma_f32_16x16x32_bf16 v[72:75], v[168:171], v[108:111], v[72:75]\n\t"
    "v_mfma_f32_16x16x32_bf16 v[76:79], v[172:175], v[108:111], v[76:79]\n\t"
    "s_waitcnt lgkmcnt(4)\n\t"
    "v_mfma_f32_16x16x32_bf16 v[64:67], v[176:179], v[112:115], v[64:67]\n\t"
    "ds_read_b128 v[104:107], %[da] offset:16384\n\t"
    "v_mfma_f32_16x16x32_bf16 v[68:71], v[180:183], v[112:115], v[68:71]\n\t"
    "ds_read_b128 v[108:111], %[da] offset:18432\n\t"
    "v_mfma_f32_16x16x32_bf16 v[72:75], v[176:179], v[116:119], v[72:75]\n\t"
    "v_mfma_f32_16x16x32_bf16 v[76:79], v[180:183], v[116:119], v[76:79]\n\t"
    "s_waitcnt lgkmcnt(4)\n\t"
    "v_mfma_f32_16x16x32_bf16 v[64:67], v[184:187], v[120:123], v[64:67]\n\t"
    "ds_read_b128 v[112:115], %[ea] offset:20480\n\t"
    "v_mfma_f32_16x16x32_bf16 v[68:71], v[188:191], v[120:123], v[68:71]\n\t"
    "ds_read_b128 v[116:119], %[ea] offset:22528\n\t"
    "v_mfma_f32_16x16x32_bf16 v[72:75], v[184:187], v[124:127], v[72:75]\n\t"
    "v_mfma_f32_16x16x32_bf16 v[76:79], v[188:191], v[124:127], v[76:79]\n\t"
    "s_waitcnt vmcnt(6)\n\t"
    "s_barrier\n\t"
    "s_waitcnt lgkmcnt(4)\n\t"
    "v_mfma_f32_16x16x32_bf16 v[64:67], v[192:195], v[96:99], v[64:67]\n\t"
    "ds_read_b128 v[120:123], %[da] offset:20480\n\t"
    "s_add_u32 m0, %[l0], 0\n\t"
    "v_mfma_f32_16x16x32_bf16 v[68:71], v[196:199], v[96:99], v[68:71]\n\t"
    "ds_read_b128 v[124:127], %[da] offset:22528\n\t"
    "global_load_lds_dwordx4 %[av0], s[38:39] sc1\n\t"
    "v_mfma_f32_16x16x32_bf16 v[72:75], v[192:195], v[100:103], v[72:75]\n\t"
    "v_mfma_f32_16x16x32_bf16 v[76:79], v[196:199], v[100:103], v[76:79]\n\t"
    "s_waitcnt lgkmcnt(4)\n\t"
    "v_mfma_f32_16x16x32_bf16 v[64:67], v[200:203], v[104:107], v[64:67]\n\t"
    "ds_read_b128 v[96:99], %[ea] offset:24576\n\t"
    "s_add_u32 m0, %[l0], 4096\n\t"
    "v_mfma_f32_16x16x32_bf16 v[68:71], v[204:207], v[104:107], v[68:71]\n\t"
    "ds_read_b128 v[100:103], %[ea] offset:26624\n\t"
    "global_load_lds_dwordx4 %[av2], s[38:39] sc1\n\t"
    "v_mfma_f32_16x16x32_bf16 v[72:75], v[200:203], v[108:111], v[72:75]\n\t"
    "v_mfma_f32_16x16x32_bf16 v[76:79], v[204:207], v[108:111], v[76:79]\n\t"
    "s_add_u32 s36, s36, %[ab]\n\t"
    "s_addc_u32 s37, s37, 0\n\t"
    "s_waitcnt lgkmcnt(4)\n\t"
    "v_mfma_f32_16x16x32_bf16 v[64:67], v[208:211], v[112:115], v[64:67]\n\t"
    "ds_read_b128 v[104:107], %[da] offset:24576\n\t"
    "s_add_u32 m0, %[l0], 8192\n\t"
    "v_mfma_f32_16x16x32_bf16 v[68:71], v[212:215], v[112:115], v[68:71]\n\t"
    "ds_read_b128 v[108:111], %[da] offset:26624\n\t"
    "global_load_lds_dwordx4 %[av4], s[38:39] sc1\n\t"
    "v_mfma_f32_16x16x32_bf16 v[72:75], v[208:211], v[116:119], v[72:75]\n\t"
    "ds_read_b32 v39, %[sa]\n\t"
    "v_mfma_f32_16x16x32_bf16 v[76:79], v[212:215], v[116:119], v[76:79]\n\t"
    "v_xor_b32 %[sa], 0x100, %[sa]\n\t"
    "s_xor_b32 %[l1], %[l1], 0x10000\n\t"
    "s_waitcnt lgkmcnt(5)\n\t"
    "v_mfma_f32_16x16x32_bf16 v[64:67], v[216:219], v[120:123], v[64:67]\n\t"
    "ds_read_b128 v[112:115], %[ea] offset:28672\n\t"
    "s_add_u32 m0, %[l0], 12288\n\t"
    "v_mfma_f32_16x16x32_bf16 v[68:71], v[220:223], v[120:123], v[68:71]\n\t"
    "ds_read_b128 v[116:119], %[ea] offset:30720\n\t"
    "global_load_lds_dwordx4 %[av6], s[38:39] sc1\n\t"
    "v_mfma_f32_16x16x32_bf16 v[72:75], v[216:219], v[124:127], v[72:75]\n\t"
    "v_xor_b32 %[ea], 0x10000, %[ea]\n\t"
    "v_mfma_f32_16x16x32_bf16 v[76:79], v[220:223], v[124:127], v[76:79]\n\t"
    "s_waitcnt lgkmcnt(5)\n\t"
    "v_mfma_f32_16x16x32_bf16 v[64:67], v[224:227], v[96:99], v[64:67]\n\t"
    "ds_read_b128 v[120:123], %[da] offset:28672\n\t"
    "v_mfma_f32_16x16x32_bf16 v[68:71], v[228:231], v[96:99], v[68:71]\n\t"
    "ds_read_b128 v[124:127], %[da] offset:30720\n\t"
    "v_mfma_f32_16x16x32_bf16 v[72:75], v[224:227], v[100:103], v[72:75]\n\t"
    "v_xor_b32 %[da], 0x10000, %[da]\n\t"
    "v_mfma_f32_16x16x32_bf16 v[76:79], v[228:231], v[100:103], v[76:79]\n\t"
    "s_waitcnt lgkmcnt(5)\n\t"
    "v_mfma_f32_16x16x32_bf16 v[64:67], v[232:235], v[104:107], v[64:67]\n\t"
    "ds_read_b128 v[96:99], %[ea] offset:0\n\t"
    "v_mfma_f32_16x16x32_bf16 v[68:71], v[236:239], v[104:107], v[68:71]\n\t"
    "ds_read_b128 v[100:103], %[ea] offset:2048\n\t"
    "v_mfma_f32_16x16x32_bf16 v[72:75], v[232:235], v[108:111], v[72:75]\n\t"
    "v_mfma_f32_16x16x32_bf16 v[76:79], v[236:239], v[108:111], v[76:79]\n\t"
    "s_waitcnt lgkmcnt(4)\n\t"
    "v_mfma_f32_16x16x32_bf16 v[64:67], v[240:243], v[112:115], v[64:67]\n\t"
    "ds_read_b128 v[104:107], %[da] offset:0\n\t"
    "v_mad_u32_u24 v39, v39, %[mu], %[ad]\n\t"
    "v_mfma_f32_16x16x32_bf16 v[68:71], v[244:247], v[112:115], v[68:71]\n\t"
    "ds_read_b128 v[108:111], %[da] offset:2048\n\t"
    "v_cmp_ge_i32 vcc, %[lf], v39\n\t"
    "s_cmp_eq_u64 vcc, 0\n\t"
    "s_addc_u32 %[lf], %[lf], 0\n\t"
    "v_mfma_f32_16x16x32_bf16 v[72:75], v[240:243], v[116:119], v[72:75]\n\t"
    "v_mfma_f32_16x16x32_bf16 v[76:79], v[244:247], v[116:119], v[76:79]\n\t"
    "v_cmp_ge_i32 vcc, %[lf], v39\n\t"
    "s_cmp_eq_u64 vcc, 0\n\t"
    "s_addc_u32 %[lf], %[lf], 0\n\t"
    "s_waitcnt lgkmcnt(4)\n\t"
    "v_mfma_f32_16x16x32_bf16 v[80:83], v[248:251], v[120:123], v[64:67]\n\t"
    "ds_read_b128 v[112:115], %[ea] offset:4096\n\t"
    "s_min_i32 %[lf], %[lf], %[cp]\n\t"
    "s_add_u32 %[b], %[b], 1\n\t"
    "v_mfma_f32_16x16x32_bf16 v[84:87], v[252:255], v[120:123], v[68:71]\n\t"
    "ds_read_b128 v[116:119], %[ea] offset:6144\n\t"
    "v_mfma_f32_16x16x32_bf16 v[88:91], v[248:251], v[124:127], v[72:75]\n\t"
    "v_mfma_f32_16x16x32_bf16 v[92:95], v[252:255], v[124:127], v[76:79]\n\t"
    "s_add_u32 s38, s38, %[ab]\n\t"
    "s_addc_u32 s39, s39, 0\n\t"
    "s_xor_b32 %[l0], %[l0], 0x10000\n\t"
    "s_cmp_lt_i32 %[b], %[lf]\n\t"
    "s_cbranch_scc1 1b\n\t"
    "s_waitcnt lgkmcnt(0)\n\t"
    "s_nop 4"
    : "=&{v[80:95]}"(pd), [ea] "+&v"(ea), [da] "+&v"(da), [b] "+&s"(b), [lf] "+&s"(lf), "+{s[36:37]}"(p1), "+{s[38:39]}"(p0), [sa] "+&v"(sa), [l1] "+&s"(l1), [l0] "+&s"(l0)
    : [rw] "v"(rw), [ab] "s"(ab), [cp] "s"(cp), [mu] "s"(mu), [ad] "v"(ad), "{v[128:159]}"(W[0]), "{v[160:191]}"(W[1]), "{v[192:223]}"(W[2]), "{v[224:255]}"(W[3]), [av0] "v"(av[0]), [av1] "v"(av[1]), [av2] "v"(av[2]), [av3] "v"(av[3]), [av4] "v"(av[4]), [av5] "v"(av[5]), [av6] "v"(av[6]), [av7] "v"(av[7])
    : "v39", "v64", "v65", "v66", "v67", "v68", "v69", "v70", "v71", "v72", "v73", "v74", "v75", "v76", "v77", "v78", "v79", "v96", "v97", "v98", "v99", "v100", "v101", "v102", "v103", "v104", "v105", "v106", "v107", "v108", "v109", "v110", "v111", "v112", "v113", "v114", "v115", "v116", "v117", "v118", "v119", "v120", "v121", "v122", "v123", "v124", "v125", "v126", "v127", "vcc", "scc", "m0", "memory");
}
__device__ __forceinline__ void fast_st0(const u32x32 (&W)[4], u32x16& pd, unsigned ea, unsigned da, int& b, int& lf, int cp, unsigned sa, unsigned mu, unsigned ad,
    unsigned rw, unsigned ab, const char* p1, const char* p0, unsigned co0, unsigned co1, int po, int pf, unsigned w0, unsigned sl, unsigned dv, const char* db, const char* pg) {
  unsigned st;
  asm volatile("s_barrier\n\t"
    "ds_read_b128 v[96:99], %[ea] offset:0\n\t"
    "ds_read_b128 v[100:103], %[ea] offset:2048\n\t"
    "ds_read_b128 v[104:107], %[da] offset:0\n\t"
    "ds_read_b128 v[108:111], %[da] offset:2048\n\t"
    "ds_read_b128 v[112:115], %[ea] offset:4096\n\t"
    "ds_read_b128 v[116:119], %[ea] offset:6144\n\t"
    "s_branch 2f\n\t"
    "1:\n\t"
    "s_barrier\n\t"
    "2:\n\t"
    "s_waitcnt lgkmcnt(4)\n\t"
    "v_mfma_f32_16x16x32_bf16 v[64:67], v[128:131], v[96:99], 0\n\t"
    "ds_read_b128 v[120:123], %[da] offset:4096\n\t"
    "v_mfma_f32_16x16x32_bf16 v[68:71], v[132:135], v[96:99], 0\n\t"
    "ds_read_b128 v[124:127], %[da] offset:6144\n\t"
    "v_mfma_f32_16x16x32_bf16 v[72:75], v[128:131], v[100:103], 0\n\t"
    "v_mfma_f32_16x16x32_bf16 v[76:79], v[132:135], v[100:103], 0\n\t"
    "s_waitcnt lgkmcnt(4)\n\t"
    "v_mfma_f32_16x16x32_bf16 v[64:67], v[136:139], v[104:107], v[64:67]\n\t"
    "ds_read_b128 v[96:99], %[ea] offset:8192\n\t"
    "v_mfma_f32_16x16x32_bf16 v[68:71], v[140:143], v[104:107], v[68:71]\n\t"
    "ds_read_b128 v[100:103], %[ea] offset:10240\n\t"
    "v_mfma_f32_16x16x32_bf16 v[72:75], v[136:139], v[108:111], v[72:75]\n\t"
    "v_mfma_f32_16x16x32_bf16 v[76:79], v[140:143], v[108:111], v[76:79]\n\t"
    "s_waitcnt lgkmcnt(4)\n\t"
    "v_mfma_f32_16x16x32_bf16 v[64:67], v[144:147], v[112:115], v[64:67]\n\t"
    "ds_read_b128 v[104:107], %[da] offset:8192\n\t"
    "v_mfma_f32_16x16x32_bf16 v[68:71], v[148:151], v[112:115], v[68:71]\n\t"
    "ds_read_b128 v[108:111], %[da] offset:10240\n\t"
    "v_mfma_f32_16x16x32_bf16 v[72:75], v[144:147], v[116:119], v[72:75]\n\t"
    "v_mfma_f32_16x16x32_bf16 v[76:79], v[148:151], v[116:119], v[76:79]\n\t"
    "s_waitcnt lgkmcnt(4)\n\t"
    "v_mfma_f32_16x16x32_bf16 v[64:67], v[152:155], v[120:123], v[64:67]\n\t"
    "ds_read_b128 v[112:115], %[ea] offset:12288\n\t"
    "v_mfma_f32_16x16x32_bf16 v[68:71], v[156:159], v[120:123], v[68:71]\n\t"
    "ds_read_b128 v[116:119], %[ea] offset:14336\n\t"
    "v_mfma_f32_16x16x32_bf16 v[72:75], v[152:155], v[124:127], v[72:75]\n\t"
    "v_mfma_f32_16x16x32_bf16 v[76:79], v[156:159], v[124:127], v[76:79]\n\t"
    "s_waitcnt lgkmcnt(4)\n\t"
    "v_mfma_f32_16x16x32_bf16 v[64:67], v[160:163], v[96:99], v[64:67]\n\t"
    "ds_read_b128 v[120:123], %[da] offset:12288\n\t"
    "v_mfma_f32_16x16x32_bf16 v[68:71], v[164:167], v[96:99], v[68:71]\n\t"
    "ds_read_b128 v[124:127], %[da] offset:14336\n\t"
    "v_mfma_f32_16x16x32_bf16 v[72:75], v[160:163], v[100:103], v[72:75]\n\t"
    "v_mfma_f32_16x16x32_bf16 v[76:79], v[164:167], v[100:103], v[76:79]\n\t"
    "s_waitcnt lgkmcnt(4)\n\t"
    "v_mfma_f32_16x16x32_bf16 v[64:67], v[168:171], v[104:107], v[64:67]\n\t"
    "ds_read_b128 v[96:99], %[ea] offset:16384\n\t"
    "v_mfma_f32_16x16x32_bf16 v[68:71], v[172:175], v[104:107], v[68:71]\n\t"
    "ds_read_b128 v[100:103], %[ea] offset:18432\n\t"
    "v_mfma_f32_16x16x32_bf16 v[72:75], v[168:171], v[108:111], v[72:75]\n\t"
    "v_mfma_f32_16x16x32_bf16 v[76:79], v[172:175], v[108:111], v[76:79]\n\t"
    "s_waitcnt lgkmcnt(4)\n\t"
    "v_mfma_f32_16x16x32_bf16 v[64:67], v[176:179], v[112:115], v[64:67]\n\t"
    "ds_read_b128 v[104:107], %[da] offset:16384\n\t"
    "v_mfma_f32_16x16x32_bf16 v[68:71], v[180:183], v[112:115], v[68:71]\n\t"
    "ds_read_b128 v[108:111], %[da] offset:18432\n\t"
    "v_mfma_f32_16x16x32_bf16 v[72:75], v[176:179], v[116:119], v[72:75]\n\t"
    "v_mfma_f32_16x16x32_bf16 v[76:79], v[180:183], v[116:119], v[76:79]\n\t"
    "s_waitcnt lgkmcnt(4)\n\t"
    "v_mfma_f32_16x16x32_bf16 v[64:67], v[184:187], v[120:123], v[64:67]\n\t"
    "ds_read_b128 v[112:115], %[ea] offset:20480\n\t"
    "v_mfma_f32_16x16x32_bf16 v[68:71], v[188:191], v[120:123], v[68:71]\n\t"
    "ds_read_b128 v[116:119], %[ea] offset:22528\n\t"
    "v_mfma_f32_16x16x32_bf16 v[72:75], v[184:187], v[124:127], v[72:75]\n\t"
    "v_mfma_f32_16x16x32_bf16 v[76:79], v[188:191], v[124:127], v[76:79]\n\t"
    "s_waitcnt vmcnt(0)\n\t"
    "s_barrier\n\t"
    "s_waitcnt lgkmcnt(4)\n\t"
    "v_mfma_f32_16x16x32_bf16 v[64:67], v[192:195], v[96:99], v[64:67]\n\t"
    "ds_read_b128 v[120:123], %[da] offset:20480\n\t"
    "v_mfma_f32_16x16x32_bf16 v[68:71], v[196:199], v[96:99], v[68:71]\n\t"
    "ds_read_b128 v[124:127], %[da] offset:22528\n\t"
    "v_mfma_f32_16x16x32_bf16 v[72:75], v[192:195], v[100:103], v[72:75]\n\t"
    "ds_read_b128 v[48:51], %[rw] offset:0\n\t"
    "v_mfma_f32_16x16x32_bf16 v[76:79], v[196:199], v[100:103], v[76:79]\n\t"
    "ds_read_b128 v[52:55], %[rw] offset:1024\n\t"
    "s_cmp_eq_u32 %[w0], 0\n\t"
    "s_cbranch_scc1 5f\n\t"
    "6:\n\t"
    "s_waitcnt lgkmcnt(6)\n\t"
    "v_mfma_f32_16x16x32_bf16 v[64:67], v[200:203], v[104:107], v[64:67]\n\t"
    "ds_read_b128 v[96:99], %[ea] offset:24576\n\t"
    "v_mfma_f32_16x16x32_bf16 v[68:71], v[204:207], v[104:107], v[68:71]\n\t"
    "ds_read_b128 v[100:103], %[ea] offset:26624\n\t"
    "v_mfma_f32_16x16x32_bf16 v[72:75], v[200:203], v[108:111], v[72:75]\n\t"
    "ds_read_b128 v[56:59], %[rw] offset:2048\n\t"
    "v_mfma_f32_16x16x32_bf16 v[76:79], v[204:207], v[108:111], v[76:79]\n\t"
    "ds_read_b128 v[60:63], %[rw] offset:3072\n\t"
    "s_waitcnt lgkmcnt(0)\n\t"
    "v_mfma_f32_16x16x32_bf16 v[64:67], v[208:211], v[112:115], v[64:67]\n\t"
    "ds_read_b128 v[104:107], %[da] offset:24576\n\t"
    "v_add_f32 v48, v48, v80\n\t"
    "v_add_f32 v49, v49, v81\n\t"
    "v_mfma_f32_16x16x32_bf16 v[68:71], v[212:215], v[112:115], v[68:71]\n\t"
    "ds_read_b128 v[108:111], %[da] offset:26624\n\t"
    "v_add_f32 v50, v50, v82\n\t"
    "v_mfma_f32_16x16x32_bf16 v[72:75], v[208:211], v[116:119], v[72:75]\n\t"
    "ds_read_b32 v39, %[sa]\n\t"
    "v_add_f32 v51, v51, v83\n\t"
    "v_mfma_f32_16x16x32_bf16 v[76:79], v[212:215], v[116:119], v[76:79]\n\t"
    "v_xor_b32 %[sa], 0x100, %[sa]\n\t"
    "v_add_f32 v52, v52, v84\n\t"
    "v_add_f32 v53, v53, v85\n\t"
    "v_mfma_f32_16x16x32_bf16 v[64:67], v[216:219], v[120:123], v[64:67]\n\t"
    "ds_read_b128 v[112:115], %[ea] offset:28672\n\t"
    "v_add_f32 v54, v54, v86\n\t"
    "v_mfma_f32_16x16x32_bf16 v[68:71], v[220:223], v[120:123], v[68:71]\n\t"
    "ds_read_b128 v[116:119], %[ea] offset:30720\n\t"
    "v_add_f32 v55, v55, v87\n\t"
    "v_mfma_f32_16x16x32_bf16 v[72:75], v[216:219], v[124:127], v[72:75]\n\t"
    "v_xor_b32 %[ea], 0x10000, %[ea]\n\t"
    "v_add_f32 v56, v56, v88\n\t"
    "v_add_f32 v57, v57, v89\n\t"
    "v_mfma_f32_16x16x32_bf16 v[76:79], v[220:223], v[124:127], v[76:79]\n\t"
    "v_add_f32 v58, v58, v90\n\t"
    "v_mfma_f32_16x16x32_bf16 v[64:67], v[224:227], v[96:99], v[64:67]\n\t"
    "ds_read_b128 v[120:123], %[da] offset:28672\n\t"
    "v_add_f32 v59, v59, v91\n\t"
    "v_mfma_f32_16x16x32_bf16 v[68:71], v[228:231], v[96:99], v[68:71]\n\t"
    "ds_read_b128 v[124:127], %[da] offset:30720\n\t"
    "v_add_f32 v60, v60, v92\n\t"
    "v_mfma_f32_16x16x32_bf16 v[72:75], v[224:227], v[100:103], v[72:75]\n\t"
    "v_xor_b32 %[da], 0x10000, %[da]\n\t"
    "v_add_f32 v61, v61, v93\n\t"
    "v_add_f32 v62, v62, v94\n\t"
    "v_mfma_f32_16x16x32_bf16 v[76:79], v[228:231], v[100:103], v[76:79]\n\t"
    "v_add_f32 v63, v63, v95\n\t"
    "s_waitcnt lgkmcnt(5)\n\t"
    "v_mfma_f32_16x16x32_bf16 v[64:67], v[232:235], v[104:107], v[64:67]\n\t"
    "ds_read_b128 v[96:99], %[ea] offset:0\n\t"
    "v_cvt_pk_bf16_f32 v40, v48, v49\n\t"
    "v_mfma_f32_16x16x32_bf16 v[68:71], v[236:239], v[104:107], v[68:71]\n\t"
    "ds_read_b128 v[100:103], %[ea] offset:2048\n\t"
    "v_cvt_pk_bf16_f32 v41, v50, v51\n\t"
    "v_cvt_pk_bf16_f32 v42, v52, v53\n\t"
    "v_mfma_f32_16x16x32_bf16 v[72:75], v[232:235], v[108:111], v[72:75]\n\t"
    "v_cvt_pk_bf16_f32 v43, v54, v55\n\t"
    "v_mfma_f32_16x16x32_bf16 v[76:79], v[236:239], v[108:111], v[76:79]\n\t"
    "global_store_dwordx4 %[co0], v[40:43], s[36:37] sc1\n\t"
    "s_waitcnt lgkmcnt(4)\n\t"
    "v_mfma_f32_16x16x32_bf16 v[64:67], v[240:243], v[112:115], v[64:67]\n\t"
    "ds_read_b128 v[104:107], %[da] offset:0\n\t"
    "v_mad_u32_u24 v39, v39, %[mu], %[ad]\n\t"
    "v_cvt_pk_bf16_f32 v44, v56, v57\n\t"
    "v_cvt_pk_bf16_f32 v45, v58, v59\n\t"
    "v_mfma_f32_16x16x32_bf16 v[68:71], v[244:247], v[112:115], v[68:71]\n\t"
    "ds_read_b128 v[108:111], %[da] offset:2048\n\t"
    "v_cmp_ge_i32 vcc, %[lf], v39\n\t"
    "s_cmp_eq_u64 vcc, 0\n\t"
    "s_addc_u32 %[lf], %[lf], 0\n\t"
    "v_cvt_pk_bf16_f32 v46, v60, v61\n\t"
    "v_mfma_f32_16x16x32_bf16 v[72:75], v[240:243], v[116:119], v[72:75]\n\t"
    "v_cvt_pk_bf16_f32 v47, v62, v63\n\t"
    "v_mfma_f32_16x16x32_bf16 v[76:79], v[244:247], v[116:119], v[76:79]\n\t"
    "v_cmp_ge_i32 vcc, %[lf], v39\n\t"
    "s_cmp_eq_u64 vcc, 0\n\t"
    "s_addc_u32 %[lf], %[lf], 0\n\t"
    "global_store_dwordx4 %[co1], v[44:47], s[36:37] sc1\n\t"
    "s_waitcnt lgkmcnt(4)\n\t"
    "v_mfma_f32_16x16x32_bf16 v[80:83], v[248:251], v[120:123], v[64:67]\n\t"
    "ds_read_b128 v[112:115], %[ea] offset:4096\n\t"
    "s_min_i32 %[lf], %[lf], %[cp]\n\t"
    "s_add_u32 %[b], %[b], 1\n\t"
    "v_mfma_f32_16x16x32_bf16 v[84:87], v[252:255], v[120:123], v[68:71]\n\t"
    "ds_read_b128 v[116:119], %[ea] offset:6144\n\t"
    "v_mfma_f32_16x16x32_bf16 v[88:91], v[248:251], v[124:127], v[72:75]\n\t"
    "v_mfma_f32_16x16x32_bf16 v[92:95], v[252:255], v[124:127], v[76:79]\n\t"
    "s_mov_b64 s[36:37], s[38:39]\n\t"
    "s_add_u32 s38, s38, %[ab]\n\t"
    "s_addc_u32 s39, s39, 0\n\t"
    "s_cmp_lt_i32 %[b], %[lf]\n\t"
    "s_cbranch_scc1 1b\n\t"
    "s_waitcnt lgkmcnt(0)\n\t"
    "s_nop 4\n\t"
    "s_branch 9f\n\t"
    "5:\n\t"
    "s_add_u32 %[st], %[b], %[po]\n\t"
    "s_max_i32 %[st], %[st], %[p0]\n\t"
    "v_mov_b32 v39, %[st]\n\t"
    "v_mov_b32 v38, 0\n\t"
    "s_mov_b64 exec, 1\n\t"
    "global_store_dword v38, v39, %[pg] sc1\n\t"
    "s_mov_b64 exec, -1\n\t"
    "s_mov_b32 %[p0], %[p1]\n\t"
    "s_xor_b32 %[sl], %[sl], 0x100\n\t"
    "s_cmp_ge_i32 %[lf], %[cp]\n\t"
    "s_cbranch_scc1 6b\n\t"
    "s_mov_b32 m0, %[sl]\n\t"
    "s_nop 0\n\t"
    "global_load_lds_dword %[dv], %[db] sc1\n\t"
    "s_branch 6b\n\t"
    "9:"
    : "=&{v[80:95]}"(pd), [ea] "+&v"(ea), [da] "+&v"(da), [b] "+&s"(b), [lf] "+&s"(lf), "+{s[36:37]}"(p1), "+{s[38:39]}"(p0), [sa] "+&v"(sa), [st] "=&s"(st), [sl] "+&s"(sl), [p0] "+&s"(pf)
    : [rw] "v"(rw), [ab] "s"(ab), [cp] "s"(cp), [mu] "s"(mu), [ad] "v"(ad), "{v[128:159]}"(W[0]), "{v[160:191]}"(W[1]), "{v[192:223]}"(W[2]), "{v[224:255]}"(W[3]), [co0] "v"(co0), [w0] "s"(w0), [dv] "v"(dv), [db] "s"(db), [pg] "s"(pg), [po] "s"(po - 1), [p1] "s"(b + po), [co1] "v"(co1)
    : "v38", "v39", "v40", "v41", "v42", "v43", "v44", "v45", "v46", "v47", "v48", "v49", "v50", "v51", "v52", "v53", "v54", "v55", "v56", "v57", "v58", "v59", "v60", "v61", "v62", "v63", "v64", "v65", "v66", "v67", "v68", "v69", "v70", "v71", "v72", "v73", "v74", "v75", "v76", "v77", "v78", "v79", "v96", "v97", "v98", "v99", "v100", "v101", "v102", "v103", "v104", "v105", "v106", "v107", "v108", "v109", "v110", "v111", "v112", "v113", "v114", "v115", "v116", "v117", "v118", "v119", "v120", "v121", "v122", "v123", "v124", "v125", "v126", "v127", "vcc", "scc", "m0", "memory");
}
__device__ __forceinline__ void fast_st1(const u32x32 (&W)[4], u32x16& pd, unsigned ea, unsigned da, int& b, int& lf, int cp, unsigned sa, unsigned mu, unsigned ad,
    unsigned rw, unsigned ab, const char* p1, const char* p0, unsigned co0, unsigned co1, int po, int pf, unsigned w0, unsigned sl, unsigned dv, const char* db, const char* pg) {
  unsigned st;
  asm volatile("s_barrier\n\t"
    "ds_read_b128 v[96:99], %[ea] offset:0\n\t"
    "ds_read_b128 v[100:103], %[ea] offset:2048\n\t"
    "ds_read_b128 v[104:107], %[da] offset:0\n\t"
    "ds_read_b128 v[108:111], %[da] offset:2048\n\t"
    "ds_read_b128 v[112:115], %[ea] offset:4096\n\t"
    "ds_read_b128 v[116:119], %[ea] offset:6144\n\t"
    "s_branch 2f\n\t"
    "1:\n\t"
    "s_barrier\n\t"
    "2:\n\t"
    "s_waitcnt lgkmcnt(4)\n\t"
    "v_mfma_f32_16x16x32_bf16 v[64:67], v[128:131], v[96:99], 0\n\t"
    "ds_read_b128 v[120:123], %[da] offset:4096\n\t"
    "v_mfma_f32_16x16x32_bf16 v[68:71], v[132:135], v[96:99], 0\n\t"
    "ds_read_b128 v[124:127], %[da] offset:6144\n\t"
    "v_mfma_f32_16x16x32_bf16 v[72:75], v[128:131], v[100:103], 0\n\t"
    "v_mfma_f32_16x16x32_bf16 v[76:79], v[132:135], v[100:103], 0\n\t"
    "s_waitcnt lgkmcnt(4)\n\t"
    "v_mfma_f32_16x16x32_bf16 v[64:67], v[136:139], v[104:107], v[64:67]\n\t"
    "ds_read_b128 v[96:99], %[ea] offset:8192\n\t"
    "v_mfma_f32_16x16x32_bf16 v[68:71], v[140:143], v[104:107], v[68:71]\n\t"
    "ds_read_b128 v[100:103], %[ea] offset:10240\n\t"
    "v_mfma_f32_16x16x32_bf16 v[72:75], v[136:139], v[108:111], v[72:75]\n\t"
    "v_mfma_f32_16x16x32_bf16 v[76:79], v[140:143], v[108:111], v[76:79]\n\t"
    "s_waitcnt lgkmcnt(4)\n\t"
    "v_mfma_f32_16x16x32_bf16 v[64:67], v[144:147], v[112:115], v[64:67]\n\t"
    "ds_read_b128 v[104:107], %[da] offset:8192\n\t"
    "v_mfma_f32_16x16x32_bf16 v[68:71], v[148:151], v[112:115], v[68:71]\n\t"
    "ds_read_b128 v[108:111], %[da] offset:10240\n\t"
    "v_mfma_f32_16x16x32_bf16 v[72:75], v[144:147], v[116:119], v[72:75]\n\t"
    "v_mfma_f32_16x16x32_bf16 v[76:79], v[148:151], v[116:119], v[76:79]\n\t"
    "s_waitcnt lgkmcnt(4)\n\t"
    "v_mfma_f32_16x16x32_bf16 v[64:67], v[152:155], v[120:123], v[64:67]\n\t"
    "ds_read_b128 v[112:115], %[ea] offset:12288\n\t"
    "v_mfma_f32_16x16x32_bf16 v[68:71], v[156:159], v[120:123], v[68:71]\n\t"
    "ds_read_b128 v[116:119], %[ea] offset:14336\n\t"
    "v_mfma_f32_16x16x32_bf16 v[72:75], v[152:155], v[124:127], v[72:75]\n\t"
    "v_mfma_f32_16x16x32_bf16 v[76:79], v[156:159], v[124:127], v[76:79]\n\t"
    "s_waitcnt lgkmcnt(4)\n\t"
    "v_mfma_f32_16x16x32_bf16 v[64:67], v[160:163], v[96:99], v[64:67]\n\t"
    "ds_read_b128 v[120:123], %[da] offset:12288\n\t"
    "v_mfma_f32_16x16x32_bf16 v[68:71], v[164:167], v[96:99], v[68:71]\n\t"
    "ds_read_b128 v[124:127], %[da] offset:14336\n\t"
    "v_mfma_f32_16x16x32_bf16 v[72:75], v[160:163], v[100:103], v[72:75]\n\t"
    "v_mfma_f32_16x16x32_bf16 v[76:79], v[164:167], v[100:103], v[76:79]\n\t"
    "s_waitcnt lgkmcnt(4)\n\t"
    "v_mfma_f32_16x16x32_bf16 v[64:67], v[168:171], v[104:107], v[64:67]\n\t"
    "ds_read_b128 v[96:99], %[ea] offset:16384\n\t"
    "v_mfma_f32_16x16x32_bf16 v[68:71], v[172:175], v[104:107], v[68:71]\n\t"
    "ds_read_b128 v[100:103], %[ea] offset:18432\n\t"
    "v_mfma_f32_16x16x32_bf16 v[72:75], v[168:171], v[108:111], v[72:75]\n\t"
    "v_mfma_f32_16x16x32_bf16 v[76:79], v[172:175], v[108:111], v[76:79]\n\t"
    "s_waitcnt lgkmcnt(4)\n\t"
    "v_mfma_f32_16x16x32_bf16 v[64:67], v[176:179], v[112:115], v[64:67]\n\t"
    "ds_read_b128 v[104:107], %[da] offset:16384\n\t"
    "v_mfma_f32_16x16x32_bf16 v[68:71], v[180:183], v[112:115], v[68:71]\n\t"
    "ds_read_b128 v[108:111], %[da] offset:18432\n\t"
    "v_mfma_f32_16x16x32_bf16 v[72:75], v[176:179], v[116:119], v[72:75]\n\t"
    "v_mfma_f32_16x16x32_bf16 v[76:79], v[180:183], v[116:119], v[76:79]\n\t"
    "s_waitcnt lgkmcnt(4)\n\t"
    "v_mfma_f32_16x16x32_bf16 v[64:67], v[184:187], v[120:123], v[64:67]\n\t"
    "ds_read_b128 v[112:115], %[ea] offset:20480\n\t"
    "v_mfma_f32_16x16x32_bf16 v[68:71], v[188:191], v[120:123], v[68:71]\n\t"
    "ds_read_b128 v[116:119], %[ea] offset:22528\n\t"
    "v_mfma_f32_16x16x32_bf16 v[72:75], v[184:187], v[124:127], v[72:75]\n\t"
    "v_mfma_f32_16x16x32_bf16 v[76:79], v[188:191], v[124:127], v[76:79]\n\t"
    "s_waitcnt vmcnt(0)\n\t"
    "s_barrier\n\t"
    "s_waitcnt lgkmcnt(4)\n\t"
    "v_mfma_f32_16x16x32_bf16 v[64:67], v[192:195], v[96:99], v[64:67]\n\t"
    "ds_read_b128 v[120:123], %[da] offset:20480\n\t"
    "v_mfma_f32_16x16x32_bf16 v[68:71], v[196:199], v[96:99], v[68:71]\n\t"
    "ds_read_b128 v[124:127], %[da] offset:22528\n\t"
    "v_mfma_f32_16x16x32_bf16 v[72:75], v[192:195], v[100:103], v[72:75]\n\t"
    "ds_read_b128 v[48:51], %[rw] offset:0\n\t"
    "v_mfma_f32_16x16x32_bf16 v[76:79], v[196:199], v[100:103], v[76:79]\n\t"
    "ds_read_b128 v[52:55], %[rw] offset:1024\n\t"
    "s_cmp_eq_u32 %[w0], 0\n\t"
    "s_cbranch_scc1 5f\n\t"
    "6:\n\t"
    "s_waitcnt lgkmcnt(6)\n\t"
    "v_mfma_f32_16x16x32_bf16 v[64:67], v[200:203], v[104:107], v[64:67]\n\t"
    "ds_read_b128 v[96:99], %[ea] offset:24576\n\t"
    "v_mfma_f32_16x16x32_bf16 v[68:71], v[204:207], v[104:107], v[68:71]\n\t"
    "ds_read_b128 v[100:103], %[ea] offset:26624\n\t"
    "v_mfma_f32_16x16x32_bf16 v[72:75], v[200:203], v[108:111], v[72:75]\n\t"
    "ds_read_b128 v[56:59], %[rw] offset:2048\n\t"
    "v_mfma_f32_16x16x32_bf16 v[76:79], v[204:207], v[108:111], v[76:79]\n\t"
    "ds_read_b128 v[60:63], %[rw] offset:3072\n\t"
    "s_waitcnt lgkmcnt(0)\n\t"
    "v_mfma_f32_16x16x32_bf16 v[64:67], v[208:211], v[112:115], v[64:67]\n\t"
    "ds_read_b128 v[104:107], %[da] offset:24576\n\t"
    "v_add_f32 v48, v48, v80\n\t"
    "v_add_f32 v49, v49, v81\n\t"
    "v_add_f32 v50, v50, v82\n\t"
    "v_mfma_f32_16x16x32_bf16 v[68:71], v[212:215], v[112:115], v[68:71]\n\t"
    "ds_read_b128 v[108:111], %[da] offset:26624\n\t"
    "v_add_f32 v51, v51, v83\n\t"
    "v_add_f32 v52, v52, v84\n\t"
    "v_add_f32 v53, v53, v85\n\t"
    "v_mfma_f32_16x16x32_bf16 v[72:75], v[208:211], v[116:119], v[72:75]\n\t"
    "ds_read_b32 v39, %[sa]\n\t"
    "v_add_f32 v54, v54, v86\n\t"
    "v_add_f32 v55, v55, v87\n\t"
    "v_add_f32 v56, v56, v88\n\t"
    "v_mfma_f32_16x16x32_bf16 v[76:79], v[212:215], v[116:119], v[76:79]\n\t"
    "v_xor_b32 %[sa], 0x100, %[sa]\n\t"
    "v_add_f32 v57, v57, v89\n\t"
    "v_add_f32 v58, v58, v90\n\t"
    "v_add_f32 v59, v59, v91\n\t"
    "v_mfma_f32_16x16x32_bf16 v[64:67], v[216:219], v[120:123], v[64:67]\n\t"
    "ds_read_b128 v[112:115], %[ea] offset:28672\n\t"
    "v_add_f32 v60, v60, v92\n\t"
    "v_add_f32 v61, v61, v93\n\t"
    "v_add_f32 v62, v62, v94\n\t"
    "v_mfma_f32_16x16x32_bf16 v[68:71], v[220:223], v[120:123], v[68:71]\n\t"
    "ds_read_b128 v[116:119], %[ea] offset:30720\n\t"
    "v_add_f32 v63, v63, v95\n\t"
    "v_mul_f32 v40, %[nl], v48\n\t"
    "v_mul_f32 v41, %[nl], v49\n\t"
    "v_mfma_f32_16x16x32_bf16 v[72:75], v[216:219], v[124:127], v[72:75]\n\t"
    "v_xor_b32 %[ea], 0x10000, %[ea]\n\t"
    "v_mul_f32 v42, %[nl], v50\n\t"
    "v_mul_f32 v43, %[nl], v51\n\t"
    "v_mul_f32 v44, %[nl], v56\n\t"
    "v_mfma_f32_16x16x32_bf16 v[76:79], v[220:223], v[124:127], v[76:79]\n\t"
    "v_mul_f32 v45, %[nl], v57\n\t"
    "v_mul_f32 v46, %[nl], v58\n\t"
    "v_mul_f32 v47, %[nl], v59\n\t"
    "v_mfma_f32_16x16x32_bf16 v[64:67], v[224:227], v[96:99], v[64:67]\n\t"
    "ds_read_b128 v[120:123], %[da] offset:28672\n\t"
    "v_exp_f32 v40, v40\n\t"
    "v_exp_f32 v41, v41\n\t"
    "v_exp_f32 v42, v42\n\t"
    "v_mfma_f32_16x16x32_bf16 v[68:71], v[228:231], v[96:99], v[68:71]\n\t"
    "ds_read_b128 v[124:127], %[da] offset:30720\n\t"
    "v_exp_f32 v43, v43\n\t"
    "v_exp_f32 v44, v44\n\t"
    "v_exp_f32 v45, v45\n\t"
    "v_mfma_f32_16x16x32_bf16 v[72:75], v[224:227], v[100:103], v[72:75]\n\t"
    "v_xor_b32 %[da], 0x10000, %[da]\n\t"
    "v_exp_f32 v46, v46\n\t"
    "v_exp_f32 v47, v47\n\t"
    "v_add_f32 v40, 1.0, v40\n\t"
    "v_mfma_f32_16x16x32_bf16 v[76:79], v[228:231], v[100:103], v[76:79]\n\t"
    "v_add_f32 v41, 1.0, v41\n\t"
    "v_add_f32 v42, 1.0, v42\n\t"
    "v_add_f32 v43, 1.0, v43\n\t"
    "s_waitcnt lgkmcnt(5)\n\t"
    "v_mfma_f32_16x16x32_bf16 v[64:67], v[232:235], v[104:107], v[64:67]\n\t"
    "ds_read_b128 v[96:99], %[ea] offset:0\n\t"
    "v_add_f32 v44, 1.0, v44\n\t"
    "v_add_f32 v45, 1.0, v45\n\t"
    "v_add_f32 v46, 1.0, v46\n\t"
    "v_mfma_f32_16x16x32_bf16 v[68:71], v[236:239], v[104:107], v[68:71]\n\t"
    "ds_read_b128 v[100:103], %[ea] offset:2048\n\t"
    "v_add_f32 v47, 1.0, v47\n\t"
    "v_rcp_f32 v40, v40\n\t"
    "v_rcp_f32 v41, v41\n\t"
    "v_mfma_f32_16x16x32_bf16 v[72:75], v[232:235], v[108:111], v[72:75]\n\t"
    "v_rcp_f32 v42, v42\n\t"
    "v_rcp_f32 v43, v43\n\t"
    "v_rcp_f32 v44, v44\n\t"
    "v_mfma_f32_16x16x32_bf16 v[76:79], v[236:239], v[108:111], v[76:79]\n\t"
    "v_rcp_f32 v45, v45\n\t"
    "v_rcp_f32 v46, v46\n\t"
    "v_rcp_f32 v47, v47\n\t"
    "s_waitcnt lgkmcnt(4)\n\t"
    "v_mfma_f32_16x16x32_bf16 v[64:67], v[240:243], v[112:115], v[64:67]\n\t"
    "ds_read_b128 v[104:107], %[da] offset:0\n\t"
    "v_mad_u32_u24 v39, v39, %[mu], %[ad]\n\t"
    "v_mul_f32 v40, v48, v40\n\t"
    "v_mul_f32 v41, v49, v41\n\t"
    "v_mul_f32 v42, v50, v42\n\t"
    "v_mfma_f32_16x16x32_bf16 v[68:71], v[244:247], v[112:115], v[68:71]\n\t"
    "ds_read_b128 v[108:111], %[da] offset:2048\n\t"
    "v_cmp_ge_i32 vcc, %[lf], v39\n\t"
    "s_cmp_eq_u64 vcc, 0\n\t"
    "s_addc_u32 %[lf], %[lf], 0\n\t"
    "v_mul_f32 v43, v51, v43\n\t"
    "v_mul_f32 v44, v56, v44\n\t"
    "v_mul_f32 v45, v57, v45\n\t"
    "v_mfma_f32_16x16x32_bf16 v[72:75], v[240:243], v[116:119], v[72:75]\n\t"
    "v_mul_f32 v46, v58, v46\n\t"
    "v_mul_f32 v47, v59, v47\n\t"
    "v_mul_f32 v40, v40, v52\n\t"
    "v_mfma_f32_16x16x32_bf16 v[76:79], v[244:247], v[116:119], v[76:79]\n\t"
    "v_cmp_ge_i32 vcc, %[lf], v39\n\t"
    "s_cmp_eq_u64 vcc, 0\n\t"
    "s_addc_u32 %[lf], %[lf], 0\n\t"
    "v_mul_f32 v41, v41, v53\n\t"
    "v_mul_f32 v42, v42, v54\n\t"
    "v_mul_f32 v43, v43, v55\n\t"
    "s_waitcnt lgkmcnt(4)\n\t"
    "v_mfma_f32_16x16x32_bf16 v[80:83], v[248:251], v[120:123], v[64:67]\n\t"
    "ds_read_b128 v[112:115], %[ea] offset:4096\n\t"
    "s_min_i32 %[lf], %[lf], %[cp]\n\t"
    "s_add_u32 %[b], %[b], 1\n\t"
    "v_mul_f32 v44, v44, v60\n\t"
    "v_mul_f32 v45, v45, v61\n\t"
    "v_mul_f32 v46, v46, v62\n\t"
    "v_mfma_f32_16x16x32_bf16 v[84:87], v[252:255], v[120:123], v[68:71]\n\t"
    "ds_read_b128 v[116:119], %[ea] offset:6144\n\t"
    "v_mul_f32 v47, v47, v63\n\t"
    "v_cvt_pk_bf16_f32 v48, v40, v41\n\t"
    "v_cvt_pk_bf16_f32 v49, v42, v43\n\t"
    "v_mfma_f32_16x16x32_bf16 v[88:91], v[248:251], v[124:127], v[72:75]\n\t"
    "v_cvt_pk_bf16_f32 v50, v44, v45\n\t"
    "v_cvt_pk_bf16_f32 v51, v46, v47\n\t"
    "s_nop 1\n\t"
    "v_mfma_f32_16x16x32_bf16 v[92:95], v[252:255], v[124:127], v[76:79]\n\t"
    "v_permlane16_swap_b32 v48, v50\n\t"
    "v_permlane16_swap_b32 v49, v51\n\t"
    "global_store_dwordx4 %[co0], v[48:51], s[36:37] sc1\n\t"
    "s_mov_b64 s[36:37], s[38:39]\n\t"
    "s_add_u32 s38, s38, %[ab]\n\t"
    "s_addc_u32 s39, s39, 0\n\t"
    "s_cmp_lt_i32 %[b], %[lf]\n\t"
    "s_cbranch_scc1 1b\n\t"
    "s_waitcnt lgkmcnt(0)\n\t"
    "s_nop 4\n\t"
    "s_branch 9f\n\t"
    "5:\n\t"
    "s_add_u32 %[st], %[b], %[po]\n\t"
    "s_max_i32 %[st], %[st], %[p0]\n\t"
    "v_mov_b32 v39, %[st]\n\t"
    "v_mov_b32 v38, 0\n\t"
    "s_mov_b64 exec, 1\n\t"
    "global_store_dword v38, v39, %[pg] sc1\n\t"
    "s_mov_b64 exec, -1\n\t"
    "s_mov_b32 %[p0], %[p1]\n\t"
    "s_xor_b32 %[sl], %[sl], 0x100\n\t"
    "s_cmp_ge_i32 %[lf], %[cp]\n\t"
    "s_cbranch_scc1 6b\n\t"
    "s_mov_b32 m0, %[sl]\n\t"
    "s_nop 0\n\t"
    "global_load_lds_dword %[dv], %[db] sc1\n\t"
    "s_branch 6b\n\t"
    "9:"
    : "=&{v[80:95]}"(pd), [ea] "+&v"(ea), [da] "+&v"(da), [b] "+&s"(b), [lf] "+&s"(lf), "+{s[36:37]}"(p1), "+{s[38:39]}"(p0), [sa] "+&v"(sa), [st] "=&s"(st), [sl] "+&s"(sl), [p0] "+&s"(pf)
    : [rw] "v"(rw), [ab] "s"(ab), [cp] "s"(cp), [mu] "s"(mu), [ad] "v"(ad), "{v[128:159]}"(W[0]), "{v[160:191]}"(W[1]), "{v[192:223]}"(W[2]), "{v[224:255]}"(W[3]), [co0] "v"(co0), [w0] "s"(w0), [dv] "v"(dv), [db] "s"(db), [pg] "s"(pg), [po] "s"(po - 1), [p1] "s"(b + po), [nl] "s"(-LOG2E)
    : "v38", "v39", "v40", "v41", "v42", "v43", "v44", "v45", "v46", "v47", "v48", "v49", "v50", "v51", "v52", "v53", "v54", "v55", "v56", "v57", "v58", "v59", "v60", "v61", "v62", "v63", "v64", "v65", "v66", "v67", "v68", "v69", "v70", "v71", "v72", "v73", "v74", "v75", "v76", "v77", "v78", "v79", "v96", "v97", "v98", "v99", "v100", "v101", "v102", "v103", "v104", "v105", "v106", "v107", "v108", "v109", "v110", "v111", "v112", "v113", "v114", "v115", "v116", "v117", "v118", "v119", "v120", "v121", "v122", "v123", "v124", "v125", "v126", "v127", "vcc", "scc", "m0", "memory");
}
__device__ __forceinline__ void fast_st2(const u32x32 (&W)[4], u32x16& pd, unsigned ea, unsigned da, int& b, int& lf, int cp, unsigned sa, unsigned mu, unsigned ad,
    unsigned rw, unsigned ab, const char* p1, const char* p0, unsigned co0, unsigned co1, int po, int pf, unsigned w0, unsigned sl, unsigned dv, const char* db, const char* pg) {
  unsigned st;
  asm volatile("s_barrier\n\t"
    "ds_read_b128 v[96:99], %[ea] offset:0\n\t"
    "ds_read_b128 v[100:103], %[ea] offset:2048\n\t"
    "ds_read_b128 v[104:107], %[da] offset:0\n\t"
    "ds_read_b128 v[108:111], %[da] offset:2048\n\t"
    "ds_read_b128 v[112:115], %[ea] offset:4096\n\t"
    "ds_read_b128 v[116:119], %[ea] offset:6144\n\t"
    "s_branch 2f\n\t"
    "1:\n\t"
    "s_barrier\n\t"
    "2:\n\t"
    "s_waitcnt lgkmcnt(4)\n\t"
    "v_mfma_f32_16x16x32_bf16 v[64:67], v[128:131], v[96:99], 0\n\t"
    "ds_read_b128 v[120:123], %[da] offset:4096\n\t"
    "v_mfma_f32_16x16x32_bf16 v[68:71], v[132:135], v[96:99], 0\n\t"
    "ds_read_b128 v[124:127], %[da] offset:6144\n\t"
    "v_mfma_f32_16x16x32_bf16 v[72:75], v[128:131], v[100:103], 0\n\t"
    "v_mfma_f32_16x16x32_bf16 v[76:79], v[132:135], v[100:103], 0\n\t"
    "s_waitcnt lgkmcnt(4)\n\t"
    "v_mfma_f32_16x16x32_bf16 v[64:67], v[136:139], v[104:107], v[64:67]\n\t"
    "ds_read_b128 v[96:99], %[ea] offset:8192\n\t"
    "v_mfma_f32_16x16x32_bf16 v[68:71], v[140:143], v[104:107], v[68:71]\n\t"
    "ds_read_b128 v[100:103], %[ea] offset:10240\n\t"
    "v_mfma_f32_16x16x32_bf16 v[72:75], v[136:139], v[108:111], v[72:75]\n\t"
    "v_mfma_f32_16x16x32_bf16 v[76:79], v[140:143], v[108:111], v[76:79]\n\t"
    "s_waitcnt lgkmcnt(4)\n\t"
    "v_mfma_f32_16x16x32_bf16 v[64:67], v[144:147], v[112:115], v[64:67]\n\t"
    "ds_read_b128 v[104:107], %[da] offset:8192\n\t"
    "v_mfma_f32_16x16x32_bf16 v[68:71], v[148:151], v[112:115], v[68:71]\n\t"
    "ds_read_b128 v[108:111], %[da] offset:10240\n\t"
    "v_mfma_f32_16x16x32_bf16 v[72:75], v[144:147], v[116:119], v[72:75]\n\t"
    "v_mfma_f32_16x16x32_bf16 v[76:79], v[148:151], v[116:119], v[76:79]\n\t"
    "s_waitcnt lgkmcnt(4)\n\t"
    "v_mfma_f32_16x16x32_bf16 v[64:67], v[152:155], v[120:123], v[64:67]\n\t"
    "ds_read_b128 v[112:115], %[ea] offset:12288\n\t"
    "v_mfma_f32_16x16x32_bf16 v[68:71], v[156:159], v[120:123], v[68:71]\n\t"
    "ds_read_b128 v[116:119], %[ea] offset:14336\n\t"
    "v_mfma_f32_16x16x32_bf16 v[72:75], v[152:155], v[124:127], v[72:75]\n\t"
    "v_mfma_f32_16x16x32_bf16 v[76:79], v[156:159], v[124:127], v[76:79]\n\t"
    "s_waitcnt lgkmcnt(4)\n\t"
    "v_mfma_f32_16x16x32_bf16 v[64:67], v[160:163], v[96:99], v[64:67]\n\t"
    "ds_read_b128 v[120:123], %[da] offset:12288\n\t"
    "v_mfma_f32_16x16x32_bf16 v[68:71], v[164:167], v[96:99], v[68:71]\n\t"
    "ds_read_b128 v[124:127], %[da] offset:14336\n\t"
    "v_mfma_f32_16x16x32_bf16 v[72:75], v[160:163], v[100:103], v[72:75]\n\t"
    "v_mfma_f32_16x16x32_bf16 v[76:79], v[164:167], v[100:103], v[76:79]\n\t"
    "s_waitcnt lgkmcnt(4)\n\t"
    "v_mfma_f32_16x16x32_bf16 v[64:67], v[168:171], v[104:107], v[64:67]\n\t"
    "ds_read_b128 v[96:99], %[ea] offset:16384\n\t"
    "v_mfma_f32_16x16x32_bf16 v[68:71], v[172:175], v[104:107], v[68:71]\n\t"
    "ds_read_b128 v[100:103], %[ea] offset:18432\n\t"
    "v_mfma_f32_16x16x32_bf16 v[72:75], v[168:171], v[108:111], v[72:75]\n\t"
    "v_mfma_f32_16x16x32_bf16 v[76:79], v[172:175], v[108:111], v[76:79]\n\t"
    "s_waitcnt lgkmcnt(4)\n\t"
    "v_mfma_f32_16x16x32_bf16 v[64:67], v[176:179], v[112:115], v[64:67]\n\t"
    "ds_read_b128 v[104:107], %[da] offset:16384\n\t"
    "v_mfma_f32_16x16x32_bf16 v[68:71], v[180:183], v[112:115], v[68:71]\n\t"
    "ds_read_b128 v[108:111], %[da] offset:18432\n\t"
    "v_mfma_f32_16x16x32_bf16 v[72:75], v[176:179], v[116:119], v[72:75]\n\t"
    "v_mfma_f32_16x16x32_bf16 v[76:79], v[180:183], v[116:119], v[76:79]\n\t"
    "s_waitcnt lgkmcnt(4)\n\t"
    "v_mfma_f32_16x16x32_bf16 v[64:67], v[184:187], v[120:123], v[64:67]\n\t"
    "ds_read_b128 v[112:115], %[ea] offset:20480\n\t"
    "v_mfma_f32_16x16x32_bf16 v[68:71], v[188:191], v[120:123], v[68:71]\n\t"
    "ds_read_b128 v[116:119], %[ea] offset:22528\n\t"
    "v_mfma_f32_16x16x32_bf16 v[72:75], v[184:187], v[124:127], v[72:75]\n\t"
    "v_mfma_f32_16x16x32_bf16 v[76:79], v[188:191], v[124:127], v[76:79]\n\t"
    "s_waitcnt vmcnt(0)\n\t"
    "s_barrier\n\t"
    "s_waitcnt lgkmcnt(4)\n\t"
    "v_mfma_f32_16x16x32_bf16 v[64:67], v[192:195], v[96:99], v[64:67]\n\t"
    "ds_read_b128 v[120:123], %[da] offset:20480\n\t"
    "v_mfma_f32_16x16x32_bf16 v[68:71], v[196:199], v[96:99], v[68:71]\n\t"
    "ds_read_b128 v[124:127], %[da] offset:22528\n\t"
    "v_mfma_f32_16x16x32_bf16 v[72:75], v[192:195], v[100:103], v[72:75]\n\t"
    "ds_read_b128 v[48:51], %[rw] offset:0\n\t"
    "v_mfma_f32_16x16x32_bf16 v[76:79], v[196:199], v[100:103], v[76:79]\n\t"
    "ds_read_b128 v[52:55], %[rw] offset:1024\n\t"
    "s_cmp_eq_u32 %[w0], 0\n\t"
    "s_cbranch_scc1 5f\n\t"
    "6:\n\t"
    "s_waitcnt lgkmcnt(6)\n\t"
    "v_mfma_f32_16x16x32_bf16 v[64:67], v[200:203], v[104:107], v[64:67]\n\t"
    "ds_read_b128 v[96:99], %[ea] offset:24576\n\t"
    "v_mfma_f32_16x16x32_bf16 v[68:71], v[204:207], v[104:107], v[68:71]\n\t"
    "ds_read_b128 v[100:103], %[ea] offset:26624\n\t"
    "v_mfma_f32_16x16x32_bf16 v[72:75], v[200:203], v[108:111], v[72:75]\n\t"
    "ds_read_b128 v[56:59], %[rw] offset:2048\n\t"
    "v_mfma_f32_16x16x32_bf16 v[76:79], v[204:207], v[108:111], v[76:79]\n\t"
    "ds_read_b128 v[60:63], %[rw] offset:3072\n\t"
    "s_waitcnt lgkmcnt(0)\n\t"
    "v_mfma_f32_16x16x32_bf16 v[64:67], v[208:211], v[112:115], v[64:67]\n\t"
    "ds_read_b128 v[104:107], %[da] offset:24576\n\t"
    "v_cvt_pk_bf16_f32 v40, v80, v81\n\t"
    "v_mfma_f32_16x16x32_bf16 v[68:71], v[212:215], v[112:115], v[68:71]\n\t"
    "ds_read_b128 v[108:111], %[da] offset:26624\n\t"
    "v_cvt_pk_bf16_f32 v41, v82, v83\n\t"
    "v_mfma_f32_16x16x32_bf16 v[72:75], v[208:211], v[116:119], v[72:75]\n\t"
    "ds_read_b32 v39, %[sa]\n\t"
    "v_cvt_pk_bf16_f32 v42, v84, v85\n\t"
    "v_mfma_f32_16x16x32_bf16 v[76:79], v[212:215], v[116:119], v[76:79]\n\t"
    "v_xor_b32 %[sa], 0x100, %[sa]\n\t"
    "v_cvt_pk_bf16_f32 v43, v86, v87\n\t"
    "v_mfma_f32_16x16x32_bf16 v[64:67], v[216:219], v[120:123], v[64:67]\n\t"
    "ds_read_b128 v[112:115], %[ea] offset:28672\n\t"
    "global_store_dwordx4 %[co0], v[40:43], s[36:37] sc1\n\t"
    "v_mfma_f32_16x16x32_bf16 v[68:71], v[220:223], v[120:123], v[68:71]\n\t"
    "ds_read_b128 v[116:119], %[ea] offset:30720\n\t"
    "v_cvt_pk_bf16_f32 v44, v88, v89\n\t"
    "v_mfma_f32_16x16x32_bf16 v[72:75], v[216:219], v[124:127], v[72:75]\n\t"
    "v_xor_b32 %[ea], 0x10000, %[ea]\n\t"
    "v_cvt_pk_bf16_f32 v45, v90, v91\n\t"
    "v_mfma_f32_16x16x32_bf16 v[76:79], v[220:223], v[124:127], v[76:79]\n\t"
    "v_cvt_pk_bf16_f32 v46, v92, v93\n\t"
    "v_mfma_f32_16x16x32_bf16 v[64:67], v[224:227], v[96:99], v[64:67]\n\t"
    "ds_read_b128 v[120:123], %[da] offset:28672\n\t"
    "v_cvt_pk_bf16_f32 v47, v94, v95\n\t"
    "v_mfma_f32_16x16x32_bf16 v[68:71], v[228:231], v[96:99], v[68:71]\n\t"
    "ds_read_b128 v[124:127], %[da] offset:30720\n\t"
    "global_store_dwordx4 %[co1], v[44:47], s[36:37] sc1\n\t"
    "v_mfma_f32_16x16x32_bf16 v[72:75], v[224:227], v[100:103], v[72:75]\n\t"
    "v_xor_b32 %[da], 0x10000, %[da]\n\t"
    "v_cvt_pk_bf16_f32 v48, v48, v49\n\t"
    "v_mfma_f32_16x16x32_bf16 v[76:79], v[228:231], v[100:103], v[76:79]\n\t"
    "v_cvt_pk_bf16_f32 v49, v50, v51\n\t"
    "s_waitcnt lgkmcnt(5)\n\t"
    "v_mfma_f32_16x16x32_bf16 v[64:67], v[232:235], v[104:107], v[64:67]\n\t"
    "ds_read_b128 v[96:99], %[ea] offset:0\n\t"
    "v_cvt_pk_bf16_f32 v50, v52, v53\n\t"
    "v_mfma_f32_16x16x32_bf16 v[68:71], v[236:239], v[104:107], v[68:71]\n\t"
    "ds_read_b128 v[100:103], %[ea] offset:2048\n\t"
    "v_cvt_pk_bf16_f32 v51, v54, v55\n\t"
    "v_mfma_f32_16x16x32_bf16 v[72:75], v[232:235], v[108:111], v[72:75]\n\t"
    "global_store_dwordx4 %[co0], v[48:51], s[36:37] offset:2048 sc1\n\t"
    "v_mfma_f32_16x16x32_bf16 v[76:79], v[236:239], v[108:111], v[76:79]\n\t"
    "v_cvt_pk_bf16_f32 v56, v56, v57\n\t"
    "s_waitcnt lgkmcnt(4)\n\t"
    "v_mfma_f32_16x16x32_bf16 v[64:67], v[240:243], v[112:115], v[64:67]\n\t"
    "ds_read_b128 v[104:107], %[da] offset:0\n\t"
    "v_mad_u32_u24 v39, v39, %[mu], %[ad]\n\t"
    "v_cvt_pk_bf16_f32 v57, v58, v59\n\t"
    "v_mfma_f32_16x16x32_bf16 v[68:71], v[244:247], v[112:115], v[68:71]\n\t"
    "ds_read_b128 v[108:111], %[da] offset:2048\n\t"
    "v_cmp_ge_i32 vcc, %[lf], v39\n\t"
    "s_cmp_eq_u64 vcc, 0\n\t"
    "s_addc_u32 %[lf], %[lf], 0\n\t"
    "v_cvt_pk_bf16_f32 v58, v60, v61\n\t"
    "v_mfma_f32_16x16x32_bf16 v[72:75], v[240:243], v[116:119], v[72:75]\n\t"
    "v_cvt_pk_bf16_f32 v59, v62, v63\n\t"
    "v_mfma_f32_16x16x32_bf16 v[76:79], v[244:247], v[116:119], v[76:79]\n\t"
    "v_cmp_ge_i32 vcc, %[lf], v39\n\t"
    "s_cmp_eq_u64 vcc, 0\n\t"
    "s_addc_u32 %[lf], %[lf], 0\n\t"
    "global_store_dwordx4 %[co1], v[56:59], s[36:37] offset:2048 sc1\n\t"
    "s_waitcnt lgkmcnt(4)\n\t"
    "v_mfma_f32_16x16x32_bf16 v[80:83], v[248:251], v[120:123], v[64:67]\n\t"
    "ds_read_b128 v[112:115], %[ea] offset:4096\n\t"
    "s_min_i32 %[lf], %[lf], %[cp]\n\t"
    "s_add_u32 %[b], %[b], 1\n\t"
    "v_mfma_f32_16x16x32_bf16 v[84:87], v[252:255], v[120:123], v[68:71]\n\t"
    "ds_read_b128 v[116:119], %[ea] offset:6144\n\t"
    "v_mfma_f32_16x16x32_bf16 v[88:91], v[248:251], v[124:127], v[72:75]\n\t"
    "v_mfma_f32_16x16x32_bf16 v[92:95], v[252:255], v[124:127], v[76:79]\n\t"
    "s_mov_b64 s[36:37], s[38:39]\n\t"
    "s_add_u32 s38, s38, %[ab]\n\t"
    "s_addc_u32 s39, s39, 0\n\t"
    "s_cmp_lt_i32 %[b], %[lf]\n\t"
    "s_cbranch_scc1 1b\n\t"
    "s_waitcnt lgkmcnt(0)\n\t"
    "s_nop 4\n\t"
    "s_branch 9f\n\t"
    "5:\n\t"
    "s_add_u32 %[st], %[b], %[po]\n\t"
    "s_max_i32 %[st], %[st], %[p0]\n\t"
    "v_mov_b32 v39, %[st]\n\t"
    "v_mov_b32 v38, 0\n\t"
    "s_mov_b64 exec, 1\n\t"
    "global_store_dword v38, v39, %[pg] sc1\n\t"
    "s_mov_b64 exec, -1\n\t"
    "s_mov_b32 %[p0], %[p1]\n\t"
    "s_xor_b32 %[sl], %[sl], 0x100\n\t"
    "s_cmp_ge_i32 %[lf], %[cp]\n\t"
    "s_cbranch_scc1 6b\n\t"
    "s_mov_b32 m0, %[sl]\n\t"
    "s_nop 0\n\t"
    "global_load_lds_dword %[dv], %[db] sc1\n\t"
    "s_branch 6b\n\t"
    "9:"
    : "=&{v[80:95]}"(pd), [ea] "+&v"(ea), [da] "+&v"(da), [b] "+&s"(b), [lf] "+&s"(lf), "+{s[36:37]}"(p1), "+{s[38:39]}"(p0), [sa] "+&v"(sa), [st] "=&s"(st), [sl] "+&s"(sl), [p0] "+&s"(pf)
    : [rw] "v"(rw), [ab] "s"(ab), [cp] "s"(cp), [mu] "s"(mu), [ad] "v"(ad), "{v[128:159]}"(W[0]), "{v[160:191]}"(W[1]), "{v[192:223]}"(W[2]), "{v[224:255]}"(W[3]), [co0] "v"(co0), [w0] "s"(w0), [dv] "v"(dv), [db] "s"(db), [pg] "s"(pg), [po] "s"(po - 1), [p1] "s"(b + po), [co1] "v"(co1)
    : "v38", "v39", "v40", "v41", "v42", "v43", "v44", "v45", "v46", "v47", "v48", "v49", "v50", "v51", "v52", "v53", "v54", "v55", "v56", "v57", "v58", "v59", "v60", "v61", "v62", "v63", "v64", "v65", "v66", "v67", "v68", "v69", "v70", "v71", "v72", "v73", "v74", "v75", "v76", "v77", "v78", "v79", "v96", "v97", "v98", "v99", "v100", "v101", "v102", "v103", "v104", "v105", "v106", "v107", "v108", "v109", "v110", "v111", "v112", "v113", "v114", "v115", "v116", "v117", "v118", "v119", "v120", "v121", "v122", "v123", "v124", "v125", "v126", "v127", "vcc", "scc", "m0", "memory");
}
__device__ __forceinline__ void near_ld0(const u32x32 (&W)[4], u32x16& pd, unsigned ea, unsigned da, int& b, int& lf, int cp, unsigned sa, unsigned mu, unsigned ad,
    unsigned rw, unsigned ab, const char* p1, const char* p0, unsigned l1, unsigned l0, const unsigned (&av)[8]) {
  asm volatile("s_setprio 0\n\t"
    "s_waitcnt vmcnt(4)\n\t"
    "s_barrier\n\t"
    "ds_read_b128 v[96:99], %[ea] offset:0\n\t"
    "ds_read_b128 v[100:103], %[ea] offset:2048\n\t"
    "ds_read_b128 v[104:107], %[da] offset:0\n\t"
    "ds_read_b128 v[108:111], %[da] offset:2048\n\t"
    "ds_read_b128 v[112:115], %[ea] offset:4096\n\t"
    "ds_read_b128 v[116:119], %[ea] offset:6144\n\t"
    "s_branch 2f\n\t"
    "1:\n\t"
    "s_waitcnt vmcnt(4)\n\t"
    "s_nop 1\n\t"
    "s_barrier\n\t"
    "2:\n\t"
    "s_waitcnt lgkmcnt(4)\n\t"
    "v_mfma_f32_16x16x32_bf16 v[64:67], v[128:131], v[96:99], 0\n\t"
    "ds_read_b128 v[120:123], %[da] offset:4096\n\t"
    "s_add_u32 m0, %[l1], 0\n\t"
    "ds_write_b128 %[rw], v[80:83] offset:0\n\t"
    "v_mfma_f32_16x16x32_bf16 v[68:71], v[132:135], v[96:99], 0\n\t"
    "ds_read_b128 v[124:127], %[da] offset:6144\n\t"
    "global_load_lds_dwordx4 %[av0], s[36:37] sc1\n\t"
    "ds_write_b128 %[rw], v[84:87] offset:1024\n\t"
    "v_mfma_f32_16x16x32_bf16 v[72:75], v[128:131], v[100:103], 0\n\t"
    "s_add_u32 m0, %[l1], 32768\n\t"
    "ds_write_b128 %[rw], v[88:91] offset:2048\n\t"
    "v_mfma_f32_16x16x32_bf16 v[76:79], v[132:135], v[100:103], 0\n\t"
    "global_load_lds_dwordx4 %[av1], s[36:37] sc1\n\t"
    "ds_write_b128 %[rw], v[92:95] offset:3072\n\t"
    "s_waitcnt lgkmcnt(8)\n\t"
    "v_mfma_f32_16x16x32_bf16 v[64:67], v[136:139], v[104:107], v[64:67]\n\t"
    "ds_read_b128 v[96:99], %[ea] offset:8192\n\t"
    "s_add_u32 m0, %[l1], 4096\n\t"
    "v_mfma_f32_16x16x32_bf16 v[68:71], v[140:143], v[104:107], v[68:71]\n\t"
    "ds_read_b128 v[100:103], %[ea] offset:10240\n\t"
    "global_load_lds_dwordx4 %[av2], s[36:37] sc1\n\t"
    "v_mfma_f32_16x16x32_bf16 v[72:75], v[136:139], v[108:111], v[72:75]\n\t"
    "s_add_u32 m0, %[l1], 36864\n\t"
    "v_mfma_f32_16x16x32_bf16 v[76:79], v[140:143], v[108:111], v[76:79]\n\t"
    "global_load_lds_dwordx4 %[av3], s[36:37] sc1\n\t"
    "s_waitcnt lgkmcnt(2)\n\t"
    "v_mfma_f32_16x16x32_bf16 v[64:67], v[144:147], v[112:115], v[64:67]\n\t"
    "ds_read_b128 v[104:107], %[da] offset:8192\n\t"
    "s_add_u32 m0, %[l1], 8192\n\t"
    "v_mfma_f32_16x16x32_bf16 v[68:71], v[148:151], v[112:115], v[68:71]\n\t"
    "ds_read_b128 v[108:111], %[da] offset:10240\n\t"
    "global_load_lds_dwordx4 %[av4], s[36:37] sc1\n\t"
    "v_mfma_f32_16x16x32_bf16 v[72:75], v[144:147], v[116:119], v[72:75]\n\t"
    "s_add_u32 m0, %[l1], 40960\n\t"
    "v_mfma_f32_16x16x32_bf16 v[76:79], v[148:151], v[116:119], v[76:79]\n\t"
    "global_load_lds_dwordx4 %[av5], s[36:37] sc1\n\t"
    "s_waitcnt vmcnt(6)\n\t"
    "s_barrier\n\t"
    "v_mfma_f32_16x16x32_bf16 v[64:67], v[152:155], v[120:123], v[64:67]\n\t"
    "ds_read_b128 v[112:115], %[ea] offset:12288\n\t"
    "s_add_u32 m0, %[l1], 12288\n\t"
    "v_mfma_f32_16x16x32_bf16 v[68:71], v[156:159], v[120:123], v[68:71]\n\t"
    "ds_read_b128 v[116:119], %[ea] offset:14336\n\t"
    "global_load_lds_dwordx4 %[av6], s[36:37] sc1\n\t"
    "v_mfma_f32_16x16x32_bf16 v[72:75], v[152:155], v[124:127], v[72:75]\n\t"
    "s_add_u32 m0, %[l1], 45056\n\t"
    "v_mfma_f32_16x16x32_bf16 v[76:79], v[156:159], v[124:127], v[76:79]\n\t"
    "global_load_lds_dwordx4 %[av7], s[36:37] sc1\n\t"
    "s_waitcnt lgkmcnt(4)\n\t"
    "v_mfma_f32_16x16x32_bf16 v[64:67], v[160:163], v[96:99], v[64:67]\n\t"
    "ds_read_b128 v[120:123], %[da] offset:12288\n\t"
    "s_add_u32 m0, %[l0], 0\n\t"
    "v_mfma_f32_16x16x32_bf16 v[68:71], v[164:167], v[96:99], v[68:71]\n\t"
    "ds_read_b128 v[124:127], %[da] offset:14336\n\t"
    "global_load_lds_dwordx4 %[av0], s[38:39] sc1\n\t"
    "v_mfma_f32_16x16x32_bf16 v[72:75], v[160:163], v[100:103], v[72:75]\n\t"
    "s_add_u32 m0, %[l0], 32768\n\t"
    "v_mfma_f32_16x16x32_bf16 v[76:79], v[164:167], v[100:103], v[76:79]\n\t"
    "global_load_lds_dwordx4 %[av1], s[38:39] sc1\n\t"
    "s_waitcnt lgkmcnt(4)\n\t"
    "v_mfma_f32_16x16x32_bf16 v[64:67], v[168:171], v[104:107], v[64:67]\n\t"
    "ds_read_b128 v[96:99], %[ea] offset:16384\n\t"
    "s_add_u32 m0, %[l0], 4096\n\t"
    "v_mfma_f32_16x16x32_bf16 v[68:71], v[172:175], v[104:107], v[68:71]\n\t"
    "ds_read_b128 v[100:103], %[ea] offset:18432\n\t"
    "global_load_lds_dwordx4 %[av2], s[38:39] sc1\n\t"
    "v_mfma_f32_16x16x32_bf16 v[72:75], v[168:171], v[108:111], v[72:75]\n\t"
    "s_add_u32 m0, %[l0], 36864\n\t"
    "v_mfma_f32_16x16x32_bf16 v[76:79], v[172:175], v[108:111], v[76:79]\n\t"
    "global_load_lds_dwordx4 %[av3], s[38:39] sc1\n\t"
    "s_waitcnt lgkmcnt(4)\n\t"
    "v_mfma_f32_16x16x32_bf16 v[64:67], v[176:179], v[112:115], v[64:67]\n\t"
    "ds_read_b128 v[104:107], %[da] offset:16384\n\t"
    "s_add_u32 m0, %[l0], 8192\n\t"
    "v_mfma_f32_16x16x32_bf16 v[68:71], v[180:183], v[112:115], v[68:71]\n\t"
    "ds_read_b128 v[108:111], %[da] offset:18432\n\t"
    "global_load_lds_dwordx4 %[av4], s[38:39] sc1\n\t"
    "v_mfma_f32_16x16x32_bf16 v[72:75], v[176:179], v[116:119], v[72:75]\n\t"
    "s_add_u32 m0, %[l0], 40960\n\t"
    "v_mfma_f32_16x16x32_bf16 v[76:79], v[180:183], v[116:119], v[76:79]\n\t"
    "global_load_lds_dwordx4 %[av5], s[38:39] sc1\n\t"
    "s_waitcnt lgkmcnt(4)\n\t"
    "v_mfma_f32_16x16x32_bf16 v[64:67], v[184:187], v[120:123], v[64:67]\n\t"
    "ds_read_b128 v[112:115], %[ea] offset:20480\n\t"
    "s_add_u32 m0, %[l0], 12288\n\t"
    "v_mfma_f32_16x16x32_bf16 v[68:71], v[188:191], v[120:123], v[68:71]\n\t"
    "ds_read_b128 v[116:119], %[ea] offset:22528\n\t"
    "global_load_lds_dwordx4 %[av6], s[38:39] sc1\n\t"
    "v_mfma_f32_16x16x32_bf16 v[72:75], v[184:187], v[124:127], v[72:75]\n\t"
    "s_add_u32 m0, %[l0], 45056\n\t"
    "v_mfma_f32_16x16x32_bf16 v[76:79], v[188:191], v[124:127], v[76:79]\n\t"
    "global_load_lds_dwordx4 %[av7], s[38:39] sc1\n\t"
    "s_waitcnt lgkmcnt(4)\n\t"
    "v_mfma_f32_16x16x32_bf16 v[64:67], v[192:195], v[96:99], v[64:67]\n\t"
    "ds_read_b128 v[120:123], %[da] offset:20480\n\t"
    "v_mfma_f32_16x16x32_bf16 v[68:71], v[196:199], v[96:99], v[68:71]\n\t"
    "ds_read_b128 v[124:127], %[da] offset:22528\n\t"
    "v_mfma_f32_16x16x32_bf16 v[72:75], v[192:195], v[100:103], v[72:75]\n\t"
    "v_mfma_f32_16x16x32_bf16 v[76:79], v[196:199], v[100:103], v[76:79]\n\t"
    "s_waitcnt lgkmcnt(4)\n\t"
    "v_mfma_f32_16x16x32_bf16 v[64:67], v[200:203], v[104:107], v[64:67]\n\t"
    "ds_read_b128 v[96:99], %[ea] offset:24576\n\t"
    "v_mfma_f32_16x16x32_bf16 v[68:71], v[204:207], v[104:107], v[68:71]\n\t"
    "ds_read_b128 v[100:103], %[ea] offset:26624\n\t"
    "v_mfma_f32_16x16x32_bf16 v[72:75], v[200:203], v[108:111], v[72:75]\n\t"
    "v_mfma_f32_16x16x32_bf16 v[76:79], v[204:207], v[108:111], v[76:79]\n\t"
    "s_add_u32 s36, s36, %[ab]\n\t"
    "s_addc_u32 s37, s37, 0\n\t"
    "s_waitcnt lgkmcnt(4)\n\t"
    "v_mfma_f32_16x16x32_bf16 v[64:67], v[208:211], v[112:115], v[64:67]\n\t"
    "ds_read_b128 v[104:107], %[da] offset:24576\n\t"
    "v_mfma_f32_16x16x32_bf16 v[68:71], v[212:215], v[112:115], v[68:71]\n\t"
    "ds_read_b128 v[108:111], %[da] offset:26624\n\t"
    "v_mfma_f32_16x16x32_bf16 v[72:75], v[208:211], v[116:119], v[72:75]\n\t"
    "v_mfma_f32_16x16x32_bf16 v[76:79], v[212:215], v[116:119], v[76:79]\n\t"
    "s_xor_b32 %[l1], %[l1], 0x10000\n\t"
    "s_waitcnt lgkmcnt(4)\n\t"
    "v_mfma_f32_16x16x32_bf16 v[64:67], v[216:219], v[120:123], v[64:67]\n\t"
    "ds_read_b128 v[112:115], %[ea] offset:28672\n\t"
    "v_mfma_f32_16x16x32_bf16 v[68:71], v[220:223], v[120:123], v[68:71]\n\t"
    "ds_read_b128 v[116:119], %[ea] offset:30720\n\t"
    "v_mfma_f32_16x16x32_bf16 v[72:75], v[216:219], v[124:127], v[72:75]\n\t"
    "v_xor_b32 %[ea], 0x10000, %[ea]\n\t"
    "v_mfma_f32_16x16x32_bf16 v[76:79], v[220:223], v[124:127], v[76:79]\n\t"
    "s_waitcnt vmcnt(12)\n\t"
    "s_barrier\n\t"
    "s_waitcnt lgkmcnt(4)\n\t"
    "v_mfma_f32_16x16x32_bf16 v[64:67], v[224:227], v[96:99], v[64:67]\n\t"
    "ds_read_b32 v39, %[sa]\n\t"
    "ds_read_b128 v[120:123], %[da] offset:28672\n\t"
    "v_mfma_f32_16x16x32_bf16 v[68:71], v[228:231], v[96:99], v[68:71]\n\t"
    "ds_read_b128 v[124:127], %[da] offset:30720\n\t"
    "v_mfma_f32_16x16x32_bf16 v[72:75], v[224:227], v[100:103], v[72:75]\n\t"
    "v_xor_b32 %[da], 0x10000, %[da]\n\t"
    "v_mfma_f32_16x16x32_bf16 v[76:79], v[228:231], v[100:103], v[76:79]\n\t"
    "s_waitcnt lgkmcnt(5)\n\t"
    "v_mfma_f32_16x16x32_bf16 v[64:67], v[232:235], v[104:107], v[64:67]\n\t"
    "ds_read_b128 v[96:99], %[ea] offset:0\n\t"
    "v_mfma_f32_16x16x32_bf16 v[68:71], v[236:239], v[104:107], v[68:71]\n\t"
    "ds_read_b128 v[100:103], %[ea] offset:2048\n\t"
    "v_mfma_f32_16x16x32_bf16 v[72:75], v[232:235], v[108:111], v[72:75]\n\t"
    "v_mfma_f32_16x16x32_bf16 v[76:79], v[236:239], v[108:111], v[76:79]\n\t"
    "s_waitcnt lgkmcnt(4)\n\t"
    "v_mfma_f32_16x16x32_bf16 v[64:67], v[240:243], v[112:115], v[64:67]\n\t"
    "ds_read_b128 v[104:107], %[da] offset:0\n\t"
    "v_mad_u32_u24 v39, v39, %[mu], %[ad]\n\t"
    "v_mfma_f32_16x16x32_bf16 v[68:71], v[244:247], v[112:115], v[68:71]\n\t"
    "ds_read_b128 v[108:111], %[da] offset:2048\n\t"
    "v_cmp_ge_i32 vcc, %[lf], v39\n\t"
    "s_cmp_eq_u64 vcc, 0\n\t"
    "s_addc_u32 %[lf], %[lf], 0\n\t"
    "v_mfma_f32_16x16x32_bf16 v[72:75], v[240:243], v[116:119], v[72:75]\n\t"
    "v_mfma_f32_16x16x32_bf16 v[76:79], v[244:247], v[116:119], v[76:79]\n\t"
    "v_cmp_ge_i32 vcc, %[lf], v39\n\t"
    "s_cmp_eq_u64 vcc, 0\n\t"
    "s_addc_u32 %[lf], %[lf], 0\n\t"
    "s_waitcnt lgkmcnt(4)\n\t"
    "v_mfma_f32_16x16x32_bf16 v[80:83], v[248:251], v[120:123], v[64:67]\n\t"
    "ds_read_b128 v[112:115], %[ea] offset:4096\n\t"
    "s_min_i32 %[lf], %[lf], %[cp]\n\t"
    "s_add_u32 %[b], %[b], 1\n\t"
    "v_mfma_f32_16x16x32_bf16 v[84:87], v[252:255], v[120:123], v[68:71]\n\t"
    "ds_read_b128 v[116:119], %[ea] offset:6144\n\t"
    "v_mfma_f32_16x16x32_bf16 v[88:91], v[248:251], v[124:127], v[72:75]\n\t"
    "v_mfma_f32_16x16x32_bf16 v[92:95], v[252:255], v[124:127], v[76:79]\n\t"
    "s_add_u32 s38, s38, %[ab]\n\t"
    "s_addc_u32 s39, s39, 0\n\t"
    "s_xor_b32 %[l0], %[l0], 0x10000\n\t"
    "s_cmp_lt_i32 %[b], %[lf]\n\t"
    "s_cbranch_scc1 1b\n\t"
    "s_waitcnt lgkmcnt(0)\n\t"
    "s_nop 4"
    : "=&{v[80:95]}"(pd), [ea] "+&v"(ea), [da] "+&v"(da), [b] "+&s"(b), [lf] "+&s"(lf), "+{s[36:37]}"(p1), "+{s[38:39]}"(p0), [sa] "+&v"(sa), [l1] "+&s"(l1), [l0] "+&s"(l0)
    : [rw] "v"(rw), [ab] "s"(ab), [cp] "s"(cp), [mu] "s"(mu), [ad] "v"(ad), "{v[128:159]}"(W[0]), "{v[160:191]}"(W[1]), "{v[192:223]}"(W[2]), "{v[224:255]}"(W[3]), [av0] "v"(av[0]), [av1] "v"(av[1]), [av2] "v"(av[2]), [av3] "v"(av[3]), [av4] "v"(av[4]), [av5] "v"(av[5]), [av6] "v"(av[6]), [av7] "v"(av[7])
    : "v39", "v64", "v65", "v66", "v67", "v68", "v69", "v70", "v71", "v72", "v73", "v74", "v75", "v76", "v77", "v78", "v79", "v96", "v97", "v98", "v99", "v100", "v101", "v102", "v103", "v104", "v105", "v106", "v107", "v108", "v109", "v110", "v111", "v112", "v113", "v114", "v115", "v116", "v117", "v118", "v119", "v120", "v121", "v122", "v123", "v124", "v125", "v126", "v127", "vcc", "scc", "m0", "memory");
}
__device__ __forceinline__ void near_ld2(const u32x32 (&W)[4], u32x16& pd, unsigned ea, unsigned da, int& b, int& lf, int cp, unsigned sa, unsigned mu, unsigned ad,
    unsigned rw, unsigned ab, const char* p1, const char* p0, unsigned l1, unsigned l0, const unsigned (&av)[8]) {
  asm volatile("s_setprio 0\n\t"
    "s_waitcnt vmcnt(2)\n\t"
    "s_barrier\n\t"
    "ds_read_b128 v[96:99], %[ea] offset:0\n\t"
    "ds_read_b128 v[100:103], %[ea] offset:2048\n\t"
    "ds_read_b128 v[104:107], %[da] offset:0\n\t"
    "ds_read_b128 v[108:111], %[da] offset:2048\n\t"
    "ds_read_b128 v[112:115], %[ea] offset:4096\n\t"
    "ds_read_b128 v[116:119], %[ea] offset:6144\n\t"
    "s_branch 2f\n\t"
    "1:\n\t"
    "s_waitcnt vmcnt(2)\n\t"
    "s_nop 1\n\t"
    "s_barrier\n\t"
    "2:\n\t"
    "s_waitcnt lgkmcnt(4)\n\t"
    "v_mfma_f32_16x16x32_bf16 v[64:67], v[128:131], v[96:99], 0\n\t"
    "ds_read_b128 v[120:123], %[da] offset:4096\n\t"
    "s_add_u32 m0, %[l1], 0\n\t"
    "ds_write_b128 %[rw], v[80:83] offset:0\n\t"
    "v_mfma_f32_16x16x32_bf16 v[68:71], v[132:135], v[96:99], 0\n\t"
    "ds_read_b128 v[124:127], %[da] offset:6144\n\t"
    "global_load_lds_dwordx4 %[av0], s[36:37] sc1\n\t"
    "ds_write_b128 %[rw], v[84:87] offset:1024\n\t"
    "v_mfma_f32_16x16x32_bf16 v[72:75], v[128:131], v[100:103], 0\n\t"
    "ds_write_b128 %[rw], v[88:91] offset:2048\n\t"
    "v_mfma_f32_16x16x32_bf16 v[76:79], v[132:135], v[100:103], 0\n\t"
    "ds_write_b128 %[rw], v[92:95] offset:3072\n\t"
    "s_waitcnt lgkmcnt(8)\n\t"
    "v_mfma_f32_16x16x32_bf16 v[64:67], v[136:139], v[104:107], v[64:67]\n\t"
    "ds_read_b128 v[96:99], %[ea] offset:8192\n\t"
    "s_add_u32 m0, %[l1], 4096\n\t"
    "v_mfma_f32_16x16x32_bf16 v[68:71], v[140:143], v[104:107], v[68:71]\n\t"
    "ds_read_b128 v[100:103], %[ea] offset:10240\n\t"
    "global_load_lds_dwordx4 %[av2], s[36:37] sc1\n\t"
    "v_mfma_f32_16x16x32_bf16 v[72:75], v[136:139], v[108:111], v[72:75]\n\t"
    "v_mfma_f32_16x16x32_bf16 v[76:79], v[140:143], v[108:111], v[76:79]\n\t"
    "s_waitcnt lgkmcnt(2)\n\t"
    "v_mfma_f32_16x16x32_bf16 v[64:67], v[144:147], v[112:115], v[64:67]\n\t"
    "ds_read_b128 v[104:107], %[da] offset:8192\n\t"
    "s_add_u32 m0, %[l1], 8192\n\t"
    "v_mfma_f32_16x16x32_bf16 v[68:71], v[148:151], v[112:115], v[68:71]\n\t"
    "ds_read_b128 v[108:111], %[da] offset:10240\n\t"
    "global_load_lds_dwordx4 %[av4], s[36:37] sc1\n\t"
    "v_mfma_f32_16x16x32_bf16 v[72:75], v[144:147], v[116:119], v[72:75]\n\t"
    "v_mfma_f32_16x16x32_bf16 v[76:79], v[148:151], v[116:119], v[76:79]\n\t"
    "s_waitcnt vmcnt(3)\n\t"
    "s_barrier\n\t"
    "v_mfma_f32_16x16x32_bf16 v[64:67], v[152:155], v[120:123], v[64:67]\n\t"
    "ds_read_b128 v[112:115], %[ea] offset:12288\n\t"
    "s_add_u32 m0, %[l1], 12288\n\t"
    "v_mfma_f32_16x16x32_bf16 v[68:71], v[156:159], v[120:123], v[68:71]\n\t"
    "ds_read_b128 v[116:119], %[ea] offset:14336\n\t"
    "global_load_lds_dwordx4 %[av6], s[36:37] sc1\n\t"
    "v_mfma_f32_16x16x32_bf16 v[72:75], v[152:155], v[124:127], v[72:75]\n\t"
    "v_mfma_f32_16x16x32_bf16 v[76:79], v[156:159], v[124:127], v[76:79]\n\t"
    "s_waitcnt lgkmcnt(4)\n\t"
    "v_mfma_f32_16x16x32_bf16 v[64:67], v[160:163], v[96:99], v[64:67]\n\t"
    "ds_read_b128 v[120:123], %[da] offset:12288\n\t"
    "s_add_u32 m0, %[l0], 0\n\t"
    "v_mfma_f32_16x16x32_bf16 v[68:71], v[164:167], v[96:99], v[68:71]\n\t"
    "ds_read_b128 v[124:127], %[da] offset:14336\n\t"
    "global_load_lds_dwordx4 %[av0], s[38:39] sc1\n\t"
    "v_mfma_f32_16x16x32_bf16 v[72:75], v[160:163], v[100:103], v[72:75]\n\t"
    "v_mfma_f32_16x16x32_bf16 v[76:79], v[164:167], v[100:103], v[76:79]\n\t"
    "s_waitcnt lgkmcnt(4)\n\t"
    "v_mfma_f32_16x16x32_bf16 v[64:67], v[168:171], v[104:107], v[64:67]\n\t"
    "ds_read_b128 v[96:99], %[ea] offset:16384\n\t"
    "s_add_u32 m0, %[l0], 4096\n\t"
    "v_mfma_f32_16x16x32_bf16 v[68:71], v[172:175], v[104:107], v[68:71]\n\t"
    "ds_read_b128 v[100:103], %[ea] offset:18432\n\t"
    "global_load_lds_dwordx4 %[av2], s[38:39] sc1\n\t"
    "v_mfma_f32_16x16x32_bf16 v[72:75], v[168:171], v[108:111], v[72:75]\n\t"
    "v_mfma_f32_16x16x32_bf16 v[76:79], v[172:175], v[108:111], v[76:79]\n\t"
    "s_waitcnt lgkmcnt(4)\n\t"
    "v_mfma_f32_16x16x32_bf16 v[64:67], v[176:179], v[112:115], v[64:67]\n\t"
    "ds_read_b128 v[104:107], %[da] offset:16384\n\t"
    "s_add_u32 m0, %[l0], 8192\n\t"
    "v_mfma_f32_16x16x32_bf16 v[68:71], v[180:183], v[112:115], v[68:71]\n\t"
    "ds_read_b128 v[108:111], %[da] offset:18432\n\t"
    "global_load_lds_dwordx4 %[av4], s[38:39] sc1\n\t"
    "v_mfma_f32_16x16x32_bf16 v[72:75], v[176:179], v[116:119], v[72:75]\n\t"
    "v_mfma_f32_16x16x32_bf16 v[76:79], v[180:183], v[116:119], v[76:79]\n\t"
    "s_waitcnt lgkmcnt(4)\n\t"
    "v_mfma_f32_16x16x32_bf16 v[64:67], v[184:187], v[120:123], v[64:67]\n\t"
    "ds_read_b128 v[112:115], %[ea] offset:20480\n\t"
    "s_add_u32 m0, %[l0], 12288\n\t"
    "v_mfma_f32_16x16x32_bf16 v[68:71], v[188:191], v[120:123], v[68:71]\n\t"
    "ds_read_b128 v[116:119], %[ea] offset:22528\n\t"
    "global_load_lds_dwordx4 %[av6], s[38:39] sc1\n\t"
    "v_mfma_f32_16x16x32_bf16 v[72:75], v[184:187], v[124:127], v[72:75]\n\t"
    "v_mfma_f32_16x16x32_bf16 v[76:79], v[188:191], v[124:127], v[76:79]\n\t"
    "s_waitcnt lgkmcnt(4)\n\t"
    "v_mfma_f32_16x16x32_bf16 v[64:67], v[192:195], v[96:99], v[64:67]\n\t"
    "ds_read_b128 v[120:123], %[da] offset:20480\n\t"
    "v_mfma_f32_16x16x32_bf16 v[68:71], v[196:199], v[96:99], v[68:71]\n\t"
    "ds_read_b128 v[124:127], %[da] offset:22528\n\t"
    "v_mfma_f32_16x16x32_bf16 v[72:75], v[192:195], v[100:103], v[72:75]\n\t"
    "v_mfma_f32_16x16x32_bf16 v[76:79], v[196:199], v[100:103], v[76:79]\n\t"
    "s_waitcnt lgkmcnt(4)\n\t"
    "v_mfma_f32_16x16x32_bf16 v[64:67], v[200:203], v[104:107], v[64:67]\n\t"
    "ds_read_b128 v[96:99], %[ea] offset:24576\n\t"
    "v_mfma_f32_16x16x32_bf16 v[68:71], v[204:207], v[104:107], v[68:71]\n\t"
    "ds_read_b128 v[100:103], %[ea] offset:26624\n\t"
    "v_mfma_f32_16x16x32_bf16 v[72:75], v[200:203], v[108:111], v[72:75]\n\t"
    "v_mfma_f32_16x16x32_bf16 v[76:79], v[204:207], v[108:111], v[76:79]\n\t"
    "s_add_u32 s36, s36, %[ab]\n\t"
    "s_addc_u32 s37, s37, 0\n\t"
    "s_waitcnt lgkmcnt(4)\n\t"
    "v_mfma_f32_16x16x32_bf16 v[64:67], v[208:211], v[112:115], v[64:67]\n\t"
    "ds_read_b128 v[104:107], %[da] offset:24576\n\t"
    "v_mfma_f32_16x16x32_bf16 v[68:71], v[212:215], v[112:115], v[68:71]\n\t"
    "ds_read_b128 v[108:111], %[da] offset:26624\n\t"
    "v_mfma_f32_16x16x32_bf16 v[72:75], v[208:211], v[116:119], v[72:75]\n\t"
    "v_mfma_f32_16x16x32_bf16 v[76:79], v[212:215], v[116:119], v[76:79]\n\t"
    "s_xor_b32 %[l1], %[l1], 0x10000\n\t"
    "s_waitcnt lgkmcnt(4)\n\t"
    "v_mfma_f32_16x16x32_bf16 v[64:67], v[216:219], v[120:123], v[64:67]\n\t"
    "ds_read_b128 v[112:115], %[ea] offset:28672\n\t"
    "v_mfma_f32_16x16x32_bf16 v[68:71], v[220:223], v[120:123], v[68:71]\n\t"
    "ds_read_b128 v[116:119], %[ea] offset:30720\n\t"
    "v_mfma_f32_16x16x32_bf16 v[72:75], v[216:219], v[124:127], v[72:75]\n\t"
    "v_xor_b32 %[ea], 0x10000, %[ea]\n\t"
    "v_mfma_f32_16x16x32_bf16 v[76:79], v[220:223], v[124:127], v[76:79]\n\t"
    "s_waitcnt vmcnt(6)\n\t"
    "s_barrier\n\t"
    "s_waitcnt lgkmcnt(4)\n\t"
    "v_mfma_f32_16x16x32_bf16 v[64:67], v[224:227], v[96:99], v[64:67]\n\t"
    "ds_read_b32 v39, %[sa]\n\t"
    "ds_read_b128 v[120:123], %[da] offset:28672\n\t"
    "v_mfma_f32_16x16x32_bf16 v[68:71], v[228:231], v[96:99], v[68:71]\n\t"
    "ds_read_b128 v[124:127], %[da] offset:30720\n\t"
    "v_mfma_f32_16x16x32_bf16 v[72:75], v[224:227], v[100:103], v[72:75]\n\t"
    "v_xor_b32 %[da], 0x10000, %[da]\n\t"
    "v_mfma_f32_16x16x32_bf16 v[76:79], v[228:231], v[100:103], v[76:79]\n\t"
    "s_waitcnt lgkmcnt(5)\n\t"
    "v_mfma_f32_16x16x32_bf16 v[64:67], v[232:235], v[104:107], v[64:67]\n\t"
    "ds_read_b128 v[96:99], %[ea] offset:0\n\t"
    "v_mfma_f32_16x16x32_bf16 v[68:71], v[236:239], v[104:107], v[68:71]\n\t"
    "ds_read_b128 v[100:103], %[ea] offset:2048\n\t"
    "v_mfma_f32_16x16x32_bf16 v[72:75], v[232:235], v[108:111], v[72:75]\n\t"
    "v_mfma_f32_16x16x32_bf16 v[76:79], v[236:239], v[108:111], v[76:79]\n\t"
    "s_waitcnt lgkmcnt(4)\n\t"
    "v_mfma_f32_16x16x32_bf16 v[64:67], v[240:243], v[112:115], v[64:67]\n\t"
    "ds_read_b128 v[104:107], %[da] offset:0\n\t"
    "v_mad_u32_u24 v39, v39, %[mu], %[ad]\n\t"
    "v_mfma_f32_16x16x32_bf16 v[68:71], v[244:247], v[112:115], v[68:71]\n\t"
    "ds_read_b128 v[108:111], %[da] offset:2048\n\t"
    "v_cmp_ge_i32 vcc, %[lf], v39\n\t"
    "s_cmp_eq_u64 vcc, 0\n\t"
    "s_addc_u32 %[lf], %[lf], 0\n\t"
    "v_mfma_f32_16x16x32_bf16 v[72:75], v[240:243], v[116:119], v[72:75]\n\t"
    "v_mfma_f32_16x16x32_bf16 v[76:79], v[244:247], v[116:119], v[76:79]\n\t"
    "v_cmp_ge_i32 vcc, %[lf], v39\n\t"
    "s_cmp_eq_u64 vcc, 0\n\t"
    "s_addc_u32 %[lf], %[lf], 0\n\t"
    "s_waitcnt lgkmcnt(4)\n\t"
    "v_mfma_f32_16x16x32_bf16 v[80:83], v[248:251], v[120:123], v[64:67]\n\t"
    "ds_read_b128 v[112:115], %[ea] offset:4096\n\t"
    "s_min_i32 %[lf], %[lf], %[cp]\n\t"
    "s_add_u32 %[b], %[b], 1\n\t"
    "v_mfma_f32_16x16x32_bf16 v[84:87], v[252:255], v[120:123], v[68:71]\n\t"
    "ds_read_b128 v[116:119], %[ea] offset:6144\n\t"
    "v_mfma_f32_16x16x32_bf16 v[88:91], v[248:251], v[124:127], v[72:75]\n\t"
    "v_mfma_f32_16x16x32_bf16 v[92:95], v[252:255], v[124:127], v[76:79]\n\t"
    "s_add_u32 s38, s38, %[ab]\n\t"
    "s_addc_u32 s39, s39, 0\n\t"
    "s_xor_b32 %[l0], %[l0], 0x10000\n\t"
    "s_cmp_lt_i32 %[b], %[lf]\n\t"
    "s_cbranch_scc1 1b\n\t"
    "s_waitcnt lgkmcnt(0)\n\t"
    "s_nop 4"
    : "=&{v[80:95]}"(pd), [ea] "+&v"(ea), [da] "+&v"(da), [b] "+&s"(b), [lf] "+&s"(lf), "+{s[36:37]}"(p1), "+{s[38:39]}"(p0), [sa] "+&v"(sa), [l1] "+&s"(l1), [l0] "+&s"(l0)
    : [rw] "v"(rw), [ab] "s"(ab), [cp] "s"(cp), [mu] "s"(mu), [ad] "v"(ad), "{v[128:159]}"(W[0]), "{v[160:191]}"(W[1]), "{v[192:223]}"(W[2]), "{v[224:255]}"(W[3]), [av0] "v"(av[0]), [av1] "v"(av[1]), [av2] "v"(av[2]), [av3] "v"(av[3]), [av4] "v"(av[4]), [av5] "v"(av[5]), [av6] "v"(av[6]), [av7] "v"(av[7])
    : "v39", "v64", "v65", "v66", "v67", "v68", "v69", "v70", "v71", "v72", "v73", "v74", "v75", "v76", "v77", "v78", "v79", "v96", "v97", "v98", "v99", "v100", "v101", "v102", "v103", "v104", "v105", "v106", "v107", "v108", "v109", "v110", "v111", "v112", "v113", "v114", "v115", "v116", "v117", "v118", "v119", "v120", "v121", "v122", "v123", "v124", "v125", "v126", "v127", "vcc", "scc", "m0", "memory");
}
__device__ __forceinline__ void near_st0(const u32x32 (&W)[4], u32x16& pd, unsigned ea, unsigned da, int& b, int& lf, int cp, unsigned sa, unsigned mu, unsigned ad,
    unsigned rw, unsigned ab, const char* p1, const char* p0, unsigned co0, unsigned co1, int po, int pf, unsigned w0, unsigned sl, unsigned dv, const char* db, const char* pg) {
  unsigned st;
  asm volatile("s_waitcnt vmcnt(0)\n\t"
    "s_barrier\n\t"
    "ds_read_b128 v[96:99], %[ea] offset:0\n\t"
    "ds_read_b128 v[100:103], %[ea] offset:2048\n\t"
    "ds_read_b128 v[104:107], %[da] offset:0\n\t"
    "ds_read_b128 v[108:111], %[da] offset:2048\n\t"
    "ds_read_b128 v[112:115], %[ea] offset:4096\n\t"
    "ds_read_b128 v[116:119], %[ea] offset:6144\n\t"
    "s_branch 2f\n\t"
    "1:\n\t"
    "s_waitcnt vmcnt(0)\n\t"
    "s_barrier\n\t"
    "2:\n\t"
    "s_waitcnt lgkmcnt(4)\n\t"
    "v_mfma_f32_16x16x32_bf16 v[64:67], v[128:131], v[96:99], 0\n\t"
    "ds_read_b128 v[120:123], %[da] offset:4096\n\t"
    "v_mfma_f32_16x16x32_bf16 v[68:71], v[132:135], v[96:99], 0\n\t"
    "ds_read_b128 v[124:127], %[da] offset:6144\n\t"
    "v_mfma_f32_16x16x32_bf16 v[72:75], v[128:131], v[100:103], 0\n\t"
    "v_mfma_f32_16x16x32_bf16 v[76:79], v[132:135], v[100:103], 0\n\t"
    "s_cmp_eq_u32 %[w0], 0\n\t"
    "s_cbranch_scc1 5f\n\t"
    "6:\n\t"
    "s_waitcnt lgkmcnt(4)\n\t"
    "v_mfma_f32_16x16x32_bf16 v[64:67], v[136:139], v[104:107], v[64:67]\n\t"
    "ds_read_b128 v[96:99], %[ea] offset:8192\n\t"
    "v_mfma_f32_16x16x32_bf16 v[68:71], v[140:143], v[104:107], v[68:71]\n\t"
    "ds_read_b128 v[100:103], %[ea] offset:10240\n\t"
    "v_mfma_f32_16x16x32_bf16 v[72:75], v[136:139], v[108:111], v[72:75]\n\t"
    "v_mfma_f32_16x16x32_bf16 v[76:79], v[140:143], v[108:111], v[76:79]\n\t"
    "s_waitcnt lgkmcnt(4)\n\t"
    "v_mfma_f32_16x16x32_bf16 v[64:67], v[144:147], v[112:115], v[64:67]\n\t"
    "ds_read_b128 v[104:107], %[da] offset:8192\n\t"
    "v_mfma_f32_16x16x32_bf16 v[68:71], v[148:151], v[112:115], v[68:71]\n\t"
    "ds_read_b128 v[108:111], %[da] offset:10240\n\t"
    "v_mfma_f32_16x16x32_bf16 v[72:75], v[144:147], v[116:119], v[72:75]\n\t"
    "v_mfma_f32_16x16x32_bf16 v[76:79], v[148:151], v[116:119], v[76:79]\n\t"
    "s_barrier\n\t"
    "s_waitcnt lgkmcnt(4)\n\t"
    "v_mfma_f32_16x16x32_bf16 v[64:67], v[152:155], v[120:123], v[64:67]\n\t"
    "ds_read_b128 v[112:115], %[ea] offset:12288\n\t"
    "v_mfma_f32_16x16x32_bf16 v[68:71], v[156:159], v[120:123], v[68:71]\n\t"
    "ds_read_b128 v[116:119], %[ea] offset:14336\n\t"
    "v_mfma_f32_16x16x32_bf16 v[72:75], v[152:155], v[124:127], v[72:75]\n\t"
    "ds_read_b128 v[48:51], %[rw] offset:0\n\t"
    "v_mfma_f32_16x16x32_bf16 v[76:79], v[156:159], v[124:127], v[76:79]\n\t"
    "ds_read_b128 v[52:55], %[rw] offset:1024\n\t"
    "s_waitcnt lgkmcnt(6)\n\t"
    "v_mfma_f32_16x16x32_bf16 v[64:67], v[160:163], v[96:99], v[64:67]\n\t"
    "ds_read_b128 v[120:123], %[da] offset:12288\n\t"
    "v_mfma_f32_16x16x32_bf16 v[68:71], v[164:167], v[96:99], v[68:71]\n\t"
    "ds_read_b128 v[124:127], %[da] offset:14336\n\t"
    "v_mfma_f32_16x16x32_bf16 v[72:75], v[160:163], v[100:103], v[72:75]\n\t"
    "ds_read_b128 v[56:59], %[rw] offset:2048\n\t"
    "v_mfma_f32_16x16x32_bf16 v[76:79], v[164:167], v[100:103], v[76:79]\n\t"
    "ds_read_b128 v[60:63], %[rw] offset:3072\n\t"
    "s_waitcnt lgkmcnt(0)\n\t"
    "v_mfma_f32_16x16x32_bf16 v[64:67], v[168:171], v[104:107], v[64:67]\n\t"
    "ds_read_b128 v[96:99], %[ea] offset:16384\n\t"
    "v_add_f32 v48, v48, v80\n\t"
    "v_add_f32 v49, v49, v81\n\t"
    "v_mfma_f32_16x16x32_bf16 v[68:71], v[172:175], v[104:107], v[68:71]\n\t"
    "ds_read_b128 v[100:103], %[ea] offset:18432\n\t"
    "v_add_f32 v50, v50, v82\n\t"
    "v_mfma_f32_16x16x32_bf16 v[72:75], v[168:171], v[108:111], v[72:75]\n\t"
    "v_add_f32 v51, v51, v83\n\t"
    "v_mfma_f32_16x16x32_bf16 v[76:79], v[172:175], v[108:111], v[76:79]\n\t"
    "v_add_f32 v52, v52, v84\n\t"
    "v_add_f32 v53, v53, v85\n\t"
    "v_mfma_f32_16x16x32_bf16 v[64:67], v[176:179], v[112:115], v[64:67]\n\t"
    "ds_read_b128 v[104:107], %[da] offset:16384\n\t"
    "v_add_f32 v54, v54, v86\n\t"
    "v_mfma_f32_16x16x32_bf16 v[68:71], v[180:183], v[112:115], v[68:71]\n\t"
    "ds_read_b128 v[108:111], %[da] offset:18432\n\t"
    "v_add_f32 v55, v55, v87\n\t"
    "v_mfma_f32_16x16x32_bf16 v[72:75], v[176:179], v[116:119], v[72:75]\n\t"
    "v_add_f32 v56, v56, v88\n\t"
    "v_add_f32 v57, v57, v89\n\t"
    "v_mfma_f32_16x16x32_bf16 v[76:79], v[180:183], v[116:119], v[76:79]\n\t"
    "v_add_f32 v58, v58, v90\n\t"
    "v_mfma_f32_16x16x32_bf16 v[64:67], v[184:187], v[120:123], v[64:67]\n\t"
    "ds_read_b128 v[112:115], %[ea] offset:20480\n\t"
    "v_add_f32 v59, v59, v91\n\t"
    "v_mfma_f32_16x16x32_bf16 v[68:71], v[188:191], v[120:123], v[68:71]\n\t"
    "ds_read_b128 v[116:119], %[ea] offset:22528\n\t"
    "v_add_f32 v60, v60, v92\n\t"
    "v_mfma_f32_16x16x32_bf16 v[72:75], v[184:187], v[124:127], v[72:75]\n\t"
    "v_add_f32 v61, v61, v93\n\t"
    "v_add_f32 v62, v62, v94\n\t"
    "v_mfma_f32_16x16x32_bf16 v[76:79], v[188:191], v[124:127], v[76:79]\n\t"
    "v_add_f32 v63, v63, v95\n\t"
    "s_waitcnt lgkmcnt(4)\n\t"
    "v_mfma_f32_16x16x32_bf16 v[64:67], v[192:195], v[96:99], v[64:67]\n\t"
    "ds_read_b128 v[120:123], %[da] offset:20480\n\t"
    "v_cvt_pk_bf16_f32 v40, v48, v49\n\t"
    "v_mfma_f32_16x16x32_bf16 v[68:71], v[196:199], v[96:99], v[68:71]\n\t"
    "ds_read_b128 v[124:127], %[da] offset:22528\n\t"
    "v_cvt_pk_bf16_f32 v41, v50, v51\n\t"
    "v_cvt_pk_bf16_f32 v42, v52, v53\n\t"
    "v_mfma_f32_16x16x32_bf16 v[72:75], v[192:195], v[100:103], v[72:75]\n\t"
    "v_cvt_pk_bf16_f32 v43, v54, v55\n\t"
    "v_mfma_f32_16x16x32_bf16 v[76:79], v[196:199], v[100:103], v[76:79]\n\t"
    "global_store_dwordx4 %[co0], v[40:43], s[36:37] sc1\n\t"
    "s_waitcnt lgkmcnt(4)\n\t"
    "v_mfma_f32_16x16x32_bf16 v[64:67], v[200:203], v[104:107], v[64:67]\n\t"
    "ds_read_b128 v[96:99], %[ea] offset:24576\n\t"
    "v_cvt_pk_bf16_f32 v44, v56, v57\n\t"
    "v_cvt_pk_bf16_f32 v45, v58, v59\n\t"
    "v_mfma_f32_16x16x32_bf16 v[68:71], v[204:207], v[104:107], v[68:71]\n\t"
    "ds_read_b128 v[100:103], %[ea] offset:26624\n\t"
    "v_cvt_pk_bf16_f32 v46, v60, v61\n\t"
    "v_mfma_f32_16x16x32_bf16 v[72:75], v[200:203], v[108:111], v[72:75]\n\t"
    "v_cvt_pk_bf16_f32 v47, v62, v63\n\t"
    "v_mfma_f32_16x16x32_bf16 v[76:79], v[204:207], v[108:111], v[76:79]\n\t"
    "global_store_dwordx4 %[co1], v[44:47], s[36:37] sc1\n\t"
    "s_waitcnt lgkmcnt(4)\n\t"
    "v_mfma_f32_16x16x32_bf16 v[64:67], v[208:211], v[112:115], v[64:67]\n\t"
    "ds_read_b128 v[104:107], %[da] offset:24576\n\t"
    "v_mfma_f32_16x16x32_bf16 v[68:71], v[212:215], v[112:115], v[68:71]\n\t"
    "ds_read_b128 v[108:111], %[da] offset:26624\n\t"
    "v_mfma_f32_16x16x32_bf16 v[72:75], v[208:211], v[116:119], v[72:75]\n\t"
    "v_mfma_f32_16x16x32_bf16 v[76:79], v[212:215], v[116:119], v[76:79]\n\t"
    "s_waitcnt lgkmcnt(4)\n\t"
    "v_mfma_f32_16x16x32_bf16 v[64:67], v[216:219], v[120:123], v[64:67]\n\t"
    "ds_read_b128 v[112:115], %[ea] offset:28672\n\t"
    "v_mfma_f32_16x16x32_bf16 v[68:71], v[220:223], v[120:123], v[68:71]\n\t"
    "ds_read_b128 v[116:119], %[ea] offset:30720\n\t"
    "v_mfma_f32_16x16x32_bf16 v[72:75], v[216:219], v[124:127], v[72:75]\n\t"
    "v_xor_b32 %[ea], 0x10000, %[ea]\n\t"
    "v_mfma_f32_16x16x32_bf16 v[76:79], v[220:223], v[124:127], v[76:79]\n\t"
    "s_waitcnt vmcnt(2)\n\t"
    "s_barrier\n\t"
    "s_waitcnt lgkmcnt(4)\n\t"
    "v_mfma_f32_16x16x32_bf16 v[64:67], v[224:227], v[96:99], v[64:67]\n\t"
    "ds_read_b32 v39, %[sa]\n\t"
    "ds_read_b128 v[120:123], %[da] offset:28672\n\t"
    "v_mfma_f32_16x16x32_bf16 v[68:71], v[228:231], v[96:99], v[68:71]\n\t"
    "ds_read_b128 v[124:127], %[da] offset:30720\n\t"
    "v_mfma_f32_16x16x32_bf16 v[72:75], v[224:227], v[100:103], v[72:75]\n\t"
    "v_xor_b32 %[da], 0x10000, %[da]\n\t"
    "v_mfma_f32_16x16x32_bf16 v[76:79], v[228:231], v[100:103], v[76:79]\n\t"
    "s_waitcnt lgkmcnt(5)\n\t"
    "v_mfma_f32_16x16x32_bf16 v[64:67], v[232:235], v[104:107], v[64:67]\n\t"
    "ds_read_b128 v[96:99], %[ea] offset:0\n\t"
    "v_mfma_f32_16x16x32_bf16 v[68:71], v[236:239], v[104:107], v[68:71]\n\t"
    "ds_read_b128 v[100:103], %[ea] offset:2048\n\t"
    "v_mfma_f32_16x16x32_bf16 v[72:75], v[232:235], v[108:111], v[72:75]\n\t"
    "v_mfma_f32_16x16x32_bf16 v[76:79], v[236:239], v[108:111], v[76:79]\n\t"
    "s_waitcnt lgkmcnt(4)\n\t"
    "v_mfma_f32_16x16x32_bf16 v[64:67], v[240:243], v[112:115], v[64:67]\n\t"
    "ds_read_b128 v[104:107], %[da] offset:0\n\t"
    "v_mad_u32_u24 v39, v39, %[mu], %[ad]\n\t"
    "v_mfma_f32_16x16x32_bf16 v[68:71], v[244:247], v[112:115], v[68:71]\n\t"
    "ds_read_b128 v[108:111], %[da] offset:2048\n\t"
    "v_cmp_ge_i32 vcc, %[lf], v39\n\t"
    "s_cmp_eq_u64 vcc, 0\n\t"
    "s_addc_u32 %[lf], %[lf], 0\n\t"
    "v_mfma_f32_16x16x32_bf16 v[72:75], v[240:243], v[116:119], v[72:75]\n\t"
    "v_mfma_f32_16x16x32_bf16 v[76:79], v[244:247], v[116:119], v[76:79]\n\t"
    "v_cmp_ge_i32 vcc, %[lf], v39\n\t"
    "s_cmp_eq_u64 vcc, 0\n\t"
    "s_addc_u32 %[lf], %[lf], 0\n\t"
    "s_waitcnt lgkmcnt(4)\n\t"
    "v_mfma_f32_16x16x32_bf16 v[80:83], v[248:251], v[120:123], v[64:67]\n\t"
    "ds_read_b128 v[112:115], %[ea] offset:4096\n\t"
    "s_min_i32 %[lf], %[lf], %[cp]\n\t"
    "s_add_u32 %[b], %[b], 1\n\t"
    "v_mfma_f32_16x16x32_bf16 v[84:87], v[252:255], v[120:123], v[68:71]\n\t"
    "ds_read_b128 v[116:119], %[ea] offset:6144\n\t"
    "v_mfma_f32_16x16x32_bf16 v[88:91], v[248:251], v[124:127], v[72:75]\n\t"
    "v_mfma_f32_16x16x32_bf16 v[92:95], v[252:255], v[124:127], v[76:79]\n\t"
    "s_mov_b64 s[36:37], s[38:39]\n\t"
    "s_add_u32 s38, s38, %[ab]\n\t"
    "s_addc_u32 s39, s39, 0\n\t"
    "s_cmp_lt_i32 %[b], %[lf]\n\t"
    "s_cbranch_scc1 1b\n\t"
    "s_waitcnt lgkmcnt(0)\n\t"
    "s_nop 4\n\t"
    "s_branch 9f\n\t"
    "5:\n\t"
    "s_add_u32 %[st], %[b], %[po]\n\t"
    "s_max_i32 %[st], %[st], %[p0]\n\t"
    "v_mov_b32 v39, %[st]\n\t"
    "v_mov_b32 v38, 0\n\t"
    "s_mov_b64 exec, 1\n\t"
    "global_store_dword v38, v39, %[pg] sc1\n\t"
    "s_mov_b64 exec, -1\n\t"
    "s_mov_b32 %[p0], %[p1]\n\t"
    "s_cmp_ge_i32 %[lf], %[cp]\n\t"
    "s_cbranch_scc1 6b\n\t"
    "s_mov_b32 m0, %[sl]\n\t"
    "s_nop 0\n\t"
    "global_load_lds_dword %[dv], %[db] sc1\n\t"
    "s_branch 6b\n\t"
    "9:"
    : "=&{v[80:95]}"(pd), [ea] "+&v"(ea), [da] "+&v"(da), [b] "+&s"(b), [lf] "+&s"(lf), "+{s[36:37]}"(p1), "+{s[38:39]}"(p0), [sa] "+&v"(sa), [st] "=&s"(st), [sl] "+&s"(sl), [p0] "+&s"(pf)
    : [rw] "v"(rw), [ab] "s"(ab), [cp] "s"(cp), [mu] "s"(mu), [ad] "v"(ad), "{v[128:159]}"(W[0]), "{v[160:191]}"(W[1]), "{v[192:223]}"(W[2]), "{v[224:255]}"(W[3]), [co0] "v"(co0), [w0] "s"(w0), [dv] "v"(dv), [db] "s"(db), [pg] "s"(pg), [po] "s"(po - 1), [p1] "s"(b + po), [co1] "v"(co1)
    : "v38", "v39", "v40", "v41", "v42", "v43", "v44", "v45", "v46", "v47", "v48", "v49", "v50", "v51", "v52", "v53", "v54", "v55", "v56", "v57", "v58", "v59", "v60", "v61", "v62", "v63", "v64", "v65", "v66", "v67", "v68", "v69", "v70", "v71", "v72", "v73", "v74", "v75", "v76", "v77", "v78", "v79", "v96", "v97", "v98", "v99", "v100", "v101", "v102", "v103", "v104", "v105", "v106", "v107", "v108", "v109", "v110", "v111", "v112", "v113", "v114", "v115", "v116", "v117", "v118", "v119", "v120", "v121", "v122", "v123", "v124", "v125", "v126", "v127", "vcc", "scc", "m0", "memory");
}
__device__ __forceinline__ void near_st1(const u32x32 (&W)[4], u32x16& pd, unsigned ea, unsigned da, int& b, int& lf, int cp, unsigned sa, unsigned mu, unsigned ad,
    unsigned rw, unsigned ab, const char* p1, const char* p0, unsigned co0, unsigned co1, int po, int pf, unsigned w0, unsigned sl, unsigned dv, const char* db, const char* pg) {
  unsigned st;
  asm volatile("s_waitcnt vmcnt(0)\n\t"
    "s_barrier\n\t"
    "ds_read_b128 v[96:99], %[ea] offset:0\n\t"
    "ds_read_b128 v[100:103], %[ea] offset:2048\n\t"
    "ds_read_b128 v[104:107], %[da] offset:0\n\t"
    "ds_read_b128 v[108:111], %[da] offset:2048\n\t"
    "ds_read_b128 v[112:115], %[ea] offset:4096\n\t"
    "ds_read_b128 v[116:119], %[ea] offset:6144\n\t"
    "s_branch 2f\n\t"
    "1:\n\t"
    "s_waitcnt vmcnt(0)\n\t"
    "s_barrier\n\t"
    "2:\n\t"
    "s_waitcnt lgkmcnt(4)\n\t"
    "v_mfma_f32_16x16x32_bf16 v[64:67], v[128:131], v[96:99], 0\n\t"
    "ds_read_b128 v[120:123], %[da] offset:4096\n\t"
    "v_mfma_f32_16x16x32_bf16 v[68:71], v[132:135], v[96:99], 0\n\t"
    "ds_read_b128 v[124:127], %[da] offset:6144\n\t"
    "v_mfma_f32_16x16x32_bf16 v[72:75], v[128:131], v[100:103], 0\n\t"
    "v_mfma_f32_16x16x32_bf16 v[76:79], v[132:135], v[100:103], 0\n\t"
    "s_cmp_eq_u32 %[w0], 0\n\t"
    "s_cbranch_scc1 5f\n\t"
    "6:\n\t"
    "s_waitcnt lgkmcnt(4)\n\t"
    "v_mfma_f32_16x16x32_bf16 v[64:67], v[136:139], v[104:107], v[64:67]\n\t"
    "ds_read_b128 v[96:99], %[ea] offset:8192\n\t"
    "v_mfma_f32_16x16x32_bf16 v[68:71], v[140:143], v[104:107], v[68:71]\n\t"
    "ds_read_b128 v[100:103], %[ea] offset:10240\n\t"
    "v_mfma_f32_16x16x32_bf16 v[72:75], v[136:139], v[108:111], v[72:75]\n\t"
    "v_mfma_f32_16x16x32_bf16 v[76:79], v[140:143], v[108:111], v[76:79]\n\t"
    "s_waitcnt lgkmcnt(4)\n\t"
    "v_mfma_f32_16x16x32_bf16 v[64:67], v[144:147], v[112:115], v[64:67]\n\t"
    "ds_read_b128 v[104:107], %[da] offset:8192\n\t"
    "v_mfma_f32_16x16x32_bf16 v[68:71], v[148:151], v[112:115], v[68:71]\n\t"
    "ds_read_b128 v[108:111], %[da] offset:10240\n\t"
    "v_mfma_f32_16x16x32_bf16 v[72:75], v[144:147], v[116:119], v[72:75]\n\t"
    "v_mfma_f32_16x16x32_bf16 v[76:79], v[148:151], v[116:119], v[76:79]\n\t"
    "s_barrier\n\t"
    "s_waitcnt lgkmcnt(4)\n\t"
    "v_mfma_f32_16x16x32_bf16 v[64:67], v[152:155], v[120:123], v[64:67]\n\t"
    "ds_read_b128 v[112:115], %[ea] offset:12288\n\t"
    "v_mfma_f32_16x16x32_bf16 v[68:71], v[156:159], v[120:123], v[68:71]\n\t"
    "ds_read_b128 v[116:119], %[ea] offset:14336\n\t"
    "v_mfma_f32_16x16x32_bf16 v[72:75], v[152:155], v[124:127], v[72:75]\n\t"
    "ds_read_b128 v[48:51], %[rw] offset:0\n\t"
    "v_mfma_f32_16x16x32_bf16 v[76:79], v[156:159], v[124:127], v[76:79]\n\t"
    "ds_read_b128 v[52:55], %[rw] offset:1024\n\t"
    "s_waitcnt lgkmcnt(6)\n\t"
    "v_mfma_f32_16x16x32_bf16 v[64:67], v[160:163], v[96:99], v[64:67]\n\t"
    "ds_read_b128 v[120:123], %[da] offset:12288\n\t"
    "v_mfma_f32_16x16x32_bf16 v[68:71], v[164:167], v[96:99], v[68:71]\n\t"
    "ds_read_b128 v[124:127], %[da] offset:14336\n\t"
    "v_mfma_f32_16x16x32_bf16 v[72:75], v[160:163], v[100:103], v[72:75]\n\t"
    "ds_read_b128 v[56:59], %[rw] offset:2048\n\t"
    "v_mfma_f32_16x16x32_bf16 v[76:79], v[164:167], v[100:103], v[76:79]\n\t"
    "ds_read_b128 v[60:63], %[rw] offset:3072\n\t"
    "s_waitcnt lgkmcnt(0)\n\t"
    "v_mfma_f32_16x16x32_bf16 v[64:67], v[168:171], v[104:107], v[64:67]\n\t"
    "ds_read_b128 v[96:99], %[ea] offset:16384\n\t"
    "v_add_f32 v48, v48, v80\n\t"
    "v_add_f32 v49, v49, v81\n\t"
    "v_add_f32 v50, v50, v82\n\t"
    "v_mfma_f32_16x16x32_bf16 v[68:71], v[172:175], v[104:107], v[68:71]\n\t"
    "ds_read_b128 v[100:103], %[ea] offset:18432\n\t"
    "v_add_f32 v51, v51, v83\n\t"
    "v_add_f32 v52, v52, v84\n\t"
    "v_add_f32 v53, v53, v85\n\t"
    "v_mfma_f32_16x16x32_bf16 v[72:75], v[168:171], v[108:111], v[72:75]\n\t"
    "v_add_f32 v54, v54, v86\n\t"
    "v_add_f32 v55, v55, v87\n\t"
    "v_add_f32 v56, v56, v88\n\t"
    "v_mfma_f32_16x16x32_bf16 v[76:79], v[172:175], v[108:111], v[76:79]\n\t"
    "v_add_f32 v57, v57, v89\n\t"
    "v_add_f32 v58, v58, v90\n\t"
    "v_add_f32 v59, v59, v91\n\t"
    "v_mfma_f32_16x16x32_bf16 v[64:67], v[176:179], v[112:115], v[64:67]\n\t"
    "ds_read_b128 v[104:107], %[da] offset:16384\n\t"
    "v_add_f32 v60, v60, v92\n\t"
    "v_add_f32 v61, v61, v93\n\t"
    "v_add_f32 v62, v62, v94\n\t"
    "v_mfma_f32_16x16x32_bf16 v[68:71], v[180:183], v[112:115], v[68:71]\n\t"
    "ds_read_b128 v[108:111], %[da] offset:18432\n\t"
    "v_add_f32 v63, v63, v95\n\t"
    "v_mul_f32 v40, %[nl], v48\n\t"
    "v_mul_f32 v41, %[nl], v49\n\t"
    "v_mfma_f32_16x16x32_bf16 v[72:75], v[176:179], v[116:119], v[72:75]\n\t"
    "v_mul_f32 v42, %[nl], v50\n\t"
    "v_mul_f32 v43, %[nl], v51\n\t"
    "v_mul_f32 v44, %[nl], v56\n\t"
    "v_mfma_f32_16x16x32_bf16 v[76:79], v[180:183], v[116:119], v[76:79]\n\t"
    "v_mul_f32 v45, %[nl], v57\n\t"
    "v_mul_f32 v46, %[nl], v58\n\t"
    "v_mul_f32 v47, %[nl], v59\n\t"
    "v_mfma_f32_16x16x32_bf16 v[64:67], v[184:187], v[120:123], v[64:67]\n\t"
    "ds_read_b128 v[112:115], %[ea] offset:20480\n\t"
    "v_exp_f32 v40, v40\n\t"
    "v_exp_f32 v41, v41\n\t"
    "v_exp_f32 v42, v42\n\t"
    "v_mfma_f32_16x16x32_bf16 v[68:71], v[188:191], v[120:123], v[68:71]\n\t"
    "ds_read_b128 v[116:119], %[ea] offset:22528\n\t"
    "v_exp_f32 v43, v43\n\t"
    "v_exp_f32 v44, v44\n\t"
    "v_exp_f32 v45, v45\n\t"
    "v_mfma_f32_16x16x32_bf16 v[72:75], v[184:187], v[124:127], v[72:75]\n\t"
    "v_exp_f32 v46, v46\n\t"
    "v_exp_f32 v47, v47\n\t"
    "v_add_f32 v40, 1.0, v40\n\t"
    "v_mfma_f32_16x16x32_bf16 v[76:79], v[188:191], v[124:127], v[76:79]\n\t"
    "v_add_f32 v41, 1.0, v41\n\t"
    "v_add_f32 v42, 1.0, v42\n\t"
    "v_add_f32 v43, 1.0, v43\n\t"
    "s_waitcnt lgkmcnt(4)\n\t"
    "v_mfma_f32_16x16x32_bf16 v[64:67], v[192:195], v[96:99], v[64:67]\n\t"
    "ds_read_b128 v[120:123], %[da] offset:20480\n\t"
    "v_add_f32 v44, 1.0, v44\n\t"
    "v_add_f32 v45, 1.0, v45\n\t"
    "v_add_f32 v46, 1.0, v46\n\t"
    "v_mfma_f32_16x16x32_bf16 v[68:71], v[196:199], v[96:99], v[68:71]\n\t"
    "ds_read_b128 v[124:127], %[da] offset:22528\n\t"
    "v_add_f32 v47, 1.0, v47\n\t"
    "v_rcp_f32 v40, v40\n\t"
    "v_rcp_f32 v41, v41\n\t"
    "v_mfma_f32_16x16x32_bf16 v[72:75], v[192:195], v[100:103], v[72:75]\n\t"
    "v_rcp_f32 v42, v42\n\t"
    "v_rcp_f32 v43, v43\n\t"
    "v_rcp_f32 v44, v44\n\t"
    "v_mfma_f32_16x16x32_bf16 v[76:79], v[196:199], v[100:103], v[76:79]\n\t"
    "v_rcp_f32 v45, v45\n\t"
    "v_rcp_f32 v46, v46\n\t"
    "v_rcp_f32 v47, v47\n\t"
    "s_waitcnt lgkmcnt(4)\n\t"
    "v_mfma_f32_16x16x32_bf16 v[64:67], v[200:203], v[104:107], v[64:67]\n\t"
    "ds_read_b128 v[96:99], %[ea] offset:24576\n\t"
    "v_mul_f32 v40, v48, v40\n\t"
    "v_mul_f32 v41, v49, v41\n\t"
    "v_mul_f32 v42, v50, v42\n\t"
    "v_mfma_f32_16x16x32_bf16 v[68:71], v[204:207], v[104:107], v[68:71]\n\t"
    "ds_read_b128 v[100:103], %[ea] offset:26624\n\t"
    "v_mul_f32 v43, v51, v43\n\t"
    "v_mul_f32 v44, v56, v44\n\t"
    "v_mul_f32 v45, v57, v45\n\t"
    "v_mfma_f32_16x16x32_bf16 v[72:75], v[200:203], v[108:111], v[72:75]\n\t"
    "v_mul_f32 v46, v58, v46\n\t"
    "v_mul_f32 v47, v59, v47\n\t"
    "v_mul_f32 v40, v40, v52\n\t"
    "v_mfma_f32_16x16x32_bf16 v[76:79], v[204:207], v[108:111], v[76:79]\n\t"
    "v_mul_f32 v41, v41, v53\n\t"
    "v_mul_f32 v42, v42, v54\n\t"
    "v_mul_f32 v43, v43, v55\n\t"
    "s_waitcnt lgkmcnt(4)\n\t"
    "v_mfma_f32_16x16x32_bf16 v[64:67], v[208:211], v[112:115], v[64:67]\n\t"
    "ds_read_b128 v[104:107], %[da] offset:24576\n\t"
    "v_mul_f32 v44, v44, v60\n\t"
    "v_mul_f32 v45, v45, v61\n\t"
    "v_mul_f32 v46, v46, v62\n\t"
    "v_mfma_f32_16x16x32_bf16 v[68:71], v[212:215], v[112:115], v[68:71]\n\t"
    "ds_read_b128 v[108:111], %[da] offset:26624\n\t"
    "v_mul_f32 v47, v47, v63\n\t"
    "v_cvt_pk_bf16_f32 v48, v40, v41\n\t"
    "v_cvt_pk_bf16_f32 v49, v42, v43\n\t"
    "v_mfma_f32_16x16x32_bf16 v[72:75], v[208:211], v[116:119], v[72:75]\n\t"
    "v_cvt_pk_bf16_f32 v50, v44, v45\n\t"
    "v_cvt_pk_bf16_f32 v51, v46, v47\n\t"
    "s_nop 1\n\t"
    "v_mfma_f32_16x16x32_bf16 v[76:79], v[212:215], v[116:119], v[76:79]\n\t"
    "v_permlane16_swap_b32 v48, v50\n\t"
    "v_permlane16_swap_b32 v49, v51\n\t"
    "global_store_dwordx4 %[co0], v[48:51], s[36:37] sc1\n\t"
    "s_waitcnt lgkmcnt(4)\n\t"
    "v_mfma_f32_16x16x32_bf16 v[64:67], v[216:219], v[120:123], v[64:67]\n\t"
    "ds_read_b128 v[112:115], %[ea] offset:28672\n\t"
    "v_mfma_f32_16x16x32_bf16 v[68:71], v[220:223], v[120:123], v[68:71]\n\t"
    "ds_read_b128 v[116:119], %[ea] offset:30720\n\t"
    "v_mfma_f32_16x16x32_bf16 v[72:75], v[216:219], v[124:127], v[72:75]\n\t"
    "v_xor_b32 %[ea], 0x10000, %[ea]\n\t"
    "v_mfma_f32_16x16x32_bf16 v[76:79], v[220:223], v[124:127], v[76:79]\n\t"
    "s_waitcnt vmcnt(1)\n\t"
    "s_barrier\n\t"
    "s_waitcnt lgkmcnt(4)\n\t"
    "v_mfma_f32_16x16x32_bf16 v[64:67], v[224:227], v[96:99], v[64:67]\n\t"
    "ds_read_b32 v39, %[sa]\n\t"
    "ds_read_b128 v[120:123], %[da] offset:28672\n\t"
    "v_mfma_f32_16x16x32_bf16 v[68:71], v[228:231], v[96:99], v[68:71]\n\t"
    "ds_read_b128 v[124:127], %[da] offset:30720\n\t"
    "v_mfma_f32_16x16x32_bf16 v[72:75], v[224:227], v[100:103], v[72:75]\n\t"
    "v_xor_b32 %[da], 0x10000, %[da]\n\t"
    "v_mfma_f32_16x16x32_bf16 v[76:79], v[228:231], v[100:103], v[76:79]\n\t"
    "s_waitcnt lgkmcnt(5)\n\t"
    "v_mfma_f32_16x16x32_bf16 v[64:67], v[232:235], v[104:107], v[64:67]\n\t"
    "ds_read_b128 v[96:99], %[ea] offset:0\n\t"
    "v_mfma_f32_16x16x32_bf16 v[68:71], v[236:239], v[104:107], v[68:71]\n\t"
    "ds_read_b128 v[100:103], %[ea] offset:2048\n\t"
    "v_mfma_f32_16x16x32_bf16 v[72:75], v[232:235], v[108:111], v[72:75]\n\t"
    "v_mfma_f32_16x16x32_bf16 v[76:79], v[236:239], v[108:111], v[76:79]\n\t"
    "s_waitcnt lgkmcnt(4)\n\t"
    "v_mfma_f32_16x16x32_bf16 v[64:67], v[240:243], v[112:115], v[64:67]\n\t"
    "ds_read_b128 v[104:107], %[da] offset:0\n\t"
    "v_mad_u32_u24 v39, v39, %[mu], %[ad]\n\t"
    "v_mfma_f32_16x16x32_bf16 v[68:71], v[244:247], v[112:115], v[68:71]\n\t"
    "ds_read_b128 v[108:111], %[da] offset:2048\n\t"
    "v_cmp_ge_i32 vcc, %[lf], v39\n\t"
    "s_cmp_eq_u64 vcc, 0\n\t"
    "s_addc_u32 %[lf], %[lf], 0\n\t"
    "v_mfma_f32_16x16x32_bf16 v[72:75], v[240:243], v[116:119], v[72:75]\n\t"
    "v_mfma_f32_16x16x32_bf16 v[76:79], v[244:247], v[116:119], v[76:79]\n\t"
    "v_cmp_ge_i32 vcc, %[lf], v39\n\t"
    "s_cmp_eq_u64 vcc, 0\n\t"
    "s_addc_u32 %[lf], %[lf], 0\n\t"
    "s_waitcnt lgkmcnt(4)\n\t"
    "v_mfma_f32_16x16x32_bf16 v[80:83], v[248:251], v[120:123], v[64:67]\n\t"
    "ds_read_b128 v[112:115], %[ea] offset:4096\n\t"
    "s_min_i32 %[lf], %[lf], %[cp]\n\t"
    "s_add_u32 %[b], %[b], 1\n\t"
    "v_mfma_f32_16x16x32_bf16 v[84:87], v[252:255], v[120:123], v[68:71]\n\t"
    "ds_read_b128 v[116:119], %[ea] offset:6144\n\t"
    "v_mfma_f32_16x16x32_bf16 v[88:91], v[248:251], v[124:127], v[72:75]\n\t"
    "v_mfma_f32_16x16x32_bf16 v[92:95], v[252:255], v[124:127], v[76:79]\n\t"
    "s_mov_b64 s[36:37], s[38:39]\n\t"
    "s_add_u32 s38, s38, %[ab]\n\t"
    "s_addc_u32 s39, s39, 0\n\t"
    "s_cmp_lt_i32 %[b], %[lf]\n\t"
    "s_cbranch_scc1 1b\n\t"
    "s_waitcnt lgkmcnt(0)\n\t"
    "s_nop 4\n\t"
    "s_branch 9f\n\t"
    "5:\n\t"
    "s_add_u32 %[st], %[b], %[po]\n\t"
    "s_max_i32 %[st], %[st], %[p0]\n\t"
    "v_mov_b32 v39, %[st]\n\t"
    "v_mov_b32 v38, 0\n\t"
    "s_mov_b64 exec, 1\n\t"
    "global_store_dword v38, v39, %[pg] sc1\n\t"
    "s_mov_b64 exec, -1\n\t"
    "s_mov_b32 %[p0], %[p1]\n\t"
    "s_cmp_ge_i32 %[lf], %[cp]\n\t"
    "s_cbranch_scc1 6b\n\t"
    "s_mov_b32 m0, %[sl]\n\t"
    "s_nop 0\n\t"
    "global_load_lds_dword %[dv], %[db] sc1\n\t"
    "s_branch 6b\n\t"
    "9:"
    : "=&{v[80:95]}"(pd), [ea] "+&v"(ea), [da] "+&v"(da), [b] "+&s"(b), [lf] "+&s"(lf), "+{s[36:37]}"(p1), "+{s[38:39]}"(p0), [sa] "+&v"(sa), [st] "=&s"(st), [sl] "+&s"(sl), [p0] "+&s"(pf)
    : [rw] "v"(rw), [ab] "s"(ab), [cp] "s"(cp), [mu] "s"(mu), [ad] "v"(ad), "{v[128:159]}"(W[0]), "{v[160:191]}"(W[1]), "{v[192:223]}"(W[2]), "{v[224:255]}"(W[3]), [co0] "v"(co0), [w0] "s"(w0), [dv] "v"(dv), [db] "s"(db), [pg] "s"(pg), [po] "s"(po - 1), [p1] "s"(b + po), [nl] "s"(-LOG2E)
    : "v38", "v39", "v40", "v41", "v42", "v43", "v44", "v45", "v46", "v47", "v48", "v49", "v50", "v51", "v52", "v53", "v54", "v55", "v56", "v57", "v58", "v59", "v60", "v61", "v62", "v63", "v64", "v65", "v66", "v67", "v68", "v69", "v70", "v71", "v72", "v73", "v74", "v75", "v76", "v77", "v78", "v79", "v96", "v97", "v98", "v99", "v100", "v101", "v102", "v103", "v104", "v105", "v106", "v107", "v108", "v109", "v110", "v111", "v112", "v113", "v114", "v115", "v116", "v117", "v118", "v119", "v120", "v121", "v122", "v123", "v124", "v125", "v126", "v127", "vcc", "scc", "m0", "memory");
}
__device__ __forceinline__ void near_st2(const u32x32 (&W)[4], u32x16& pd, unsigned ea, unsigned da, int& b, int& lf, int cp, unsigned sa, unsigned mu, unsigned ad,
    unsigned rw, unsigned ab, const char* p1, const char* p0, unsigned co0, unsigned co1, int po, int pf, unsigned w0, unsigned sl, unsigned dv, const char* db, const char* pg) {
  unsigned st;
  asm volatile("s_waitcnt vmcnt(0)\n\t"
    "s_barrier\n\t"
    "ds_read_b128 v[96:99], %[ea] offset:0\n\t"
    "ds_read_b128 v[100:103], %[ea] offset:2048\n\t"
    "ds_read_b128 v[104:107], %[da] offset:0\n\t"
    "ds_read_b128 v[108:111], %[da] offset:2048\n\t"
    "ds_read_b128 v[112:115], %[ea] offset:4096\n\t"
    "ds_read_b128 v[116:119], %[ea] offset:6144\n\t"
    "s_branch 2f\n\t"
    "1:\n\t"
    "s_waitcnt vmcnt(0)\n\t"
    "s_barrier\n\t"
    "2:\n\t"
    "s_waitcnt lgkmcnt(4)\n\t"
    "v_mfma_f32_16x16x32_bf16 v[64:67], v[128:131], v[96:99], 0\n\t"
    "ds_read_b128 v[120:123], %[da] offset:4096\n\t"
    "v_mfma_f32_16x16x32_bf16 v[68:71], v[132:135], v[96:99], 0\n\t"
    "ds_read_b128 v[124:127], %[da] offset:6144\n\t"
    "v_mfma_f32_16x16x32_bf16 v[72:75], v[128:131], v[100:103], 0\n\t"
    "v_mfma_f32_16x16x32_bf16 v[76:79], v[132:135], v[100:103], 0\n\t"
    "s_cmp_eq_u32 %[w0], 0\n\t"
    "s_cbranch_scc1 5f\n\t"
    "6:\n\t"
    "s_waitcnt lgkmcnt(4)\n\t"
    "v_mfma_f32_16x16x32_bf16 v[64:67], v[136:139], v[104:107], v[64:67]\n\t"
    "ds_read_b128 v[96:99], %[ea] offset:8192\n\t"
    "v_mfma_f32_16x16x32_bf16 v[68:71], v[140:143], v[104:107], v[68:71]\n\t"
    "ds_read_b128 v[100:103], %[ea] offset:10240\n\t"
    "v_mfma_f32_16x16x32_bf16 v[72:75], v[136:139], v[108:111], v[72:75]\n\t"
    "v_mfma_f32_16x16x32_bf16 v[76:79], v[140:143], v[108:111], v[76:79]\n\t"
    "s_waitcnt lgkmcnt(4)\n\t"
    "v_mfma_f32_16x16x32_bf16 v[64:67], v[144:147], v[112:115], v[64:67]\n\t"
    "ds_read_b128 v[104:107], %[da] offset:8192\n\t"
    "v_mfma_f32_16x16x32_bf16 v[68:71], v[148:151], v[112:115], v[68:71]\n\t"
    "ds_read_b128 v[108:111], %[da] offset:10240\n\t"
    "v_mfma_f32_16x16x32_bf16 v[72:75], v[144:147], v[116:119], v[72:75]\n\t"
    "v_mfma_f32_16x16x32_bf16 v[76:79], v[148:151], v[116:119], v[76:79]\n\t"
    "s_barrier\n\t"
    "s_waitcnt lgkmcnt(4)\n\t"
    "v_mfma_f32_16x16x32_bf16 v[64:67], v[152:155], v[120:123], v[64:67]\n\t"
    "ds_read_b128 v[112:115], %[ea] offset:12288\n\t"
    "v_mfma_f32_16x16x32_bf16 v[68:71], v[156:159], v[120:123], v[68:71]\n\t"
    "ds_read_b128 v[116:119], %[ea] offset:14336\n\t"
    "v_mfma_f32_16x16x32_bf16 v[72:75], v[152:155], v[124:127], v[72:75]\n\t"
    "ds_read_b128 v[48:51], %[rw] offset:0\n\t"
    "v_mfma_f32_16x16x32_bf16 v[76:79], v[156:159], v[124:127], v[76:79]\n\t"
    "ds_read_b128 v[52:55], %[rw] offset:1024\n\t"
    "s_waitcnt lgkmcnt(6)\n\t"
    "v_mfma_f32_16x16x32_bf16 v[64:67], v[160:163], v[96:99], v[64:67]\n\t"
    "ds_read_b128 v[120:123], %[da] offset:12288\n\t"
    "v_mfma_f32_16x16x32_bf16 v[68:71], v[164:167], v[96:99], v[68:71]\n\t"
    "ds_read_b128 v[124:127], %[da] offset:14336\n\t"
    "v_mfma_f32_16x16x32_bf16 v[72:75], v[160:163], v[100:103], v[72:75]\n\t"
    "ds_read_b128 v[56:59], %[rw] offset:2048\n\t"
    "v_mfma_f32_16x16x32_bf16 v[76:79], v[164:167], v[100:103], v[76:79]\n\t"
    "ds_read_b128 v[60:63], %[rw] offset:3072\n\t"
    "s_waitcnt lgkmcnt(0)\n\t"
    "v_mfma_f32_16x16x32_bf16 v[64:67], v[168:171], v[104:107], v[64:67]\n\t"
    "ds_read_b128 v[96:99], %[ea] offset:16384\n\t"
    "v_cvt_pk_bf16_f32 v40, v80, v81\n\t"
    "v_mfma_f32_16x16x32_bf16 v[68:71], v[172:175], v[104:107], v[68:71]\n\t"
    "ds_read_b128 v[100:103], %[ea] offset:18432\n\t"
    "v_cvt_pk_bf16_f32 v41, v82, v83\n\t"
    "v_mfma_f32_16x16x32_bf16 v[72:75], v[168:171], v[108:111], v[72:75]\n\t"
    "v_cvt_pk_bf16_f32 v42, v84, v85\n\t"
    "v_mfma_f32_16x16x32_bf16 v[76:79], v[172:175], v[108:111], v[76:79]\n\t"
    "v_cvt_pk_bf16_f32 v43, v86, v87\n\t"
    "v_mfma_f32_16x16x32_bf16 v[64:67], v[176:179], v[112:115], v[64:67]\n\t"
    "ds_read_b128 v[104:107], %[da] offset:16384\n\t"
    "global_store_dwordx4 %[co0], v[40:43], s[36:37] sc1\n\t"
    "v_mfma_f32_16x16x32_bf16 v[68:71], v[180:183], v[112:115], v[68:71]\n\t"
    "ds_read_b128 v[108:111], %[da] offset:18432\n\t"
    "v_cvt_pk_bf16_f32 v44, v88, v89\n\t"
    "v_mfma_f32_16x16x32_bf16 v[72:75], v[176:179], v[116:119], v[72:75]\n\t"
    "v_cvt_pk_bf16_f32 v45, v90, v91\n\t"
    "v_mfma_f32_16x16x32_bf16 v[76:79], v[180:183], v[116:119], v[76:79]\n\t"
    "v_cvt_pk_bf16_f32 v46, v92, v93\n\t"
    "v_mfma_f32_16x16x32_bf16 v[64:67], v[184:187], v[120:123], v[64:67]\n\t"
    "ds_read_b128 v[112:115], %[ea] offset:20480\n\t"
    "v_cvt_pk_bf16_f32 v47, v94, v95\n\t"
    "v_mfma_f32_16x16x32_bf16 v[68:71], v[188:191], v[120:123], v[68:71]\n\t"
    "ds_read_b128 v[116:119], %[ea] offset:22528\n\t"
    "global_store_dwordx4 %[co1], v[44:47], s[36:37] sc1\n\t"
    "v_mfma_f32_16x16x32_bf16 v[72:75], v[184:187], v[124:127], v[72:75]\n\t"
    "v_cvt_pk_bf16_f32 v48, v48, v49\n\t"
    "v_mfma_f32_16x16x32_bf16 v[76:79], v[188:191], v[124:127], v[76:79]\n\t"
    "v_cvt_pk_bf16_f32 v49, v50, v51\n\t"
    "s_waitcnt lgkmcnt(4)\n\t"
    "v_mfma_f32_16x16x32_bf16 v[64:67], v[192:195], v[96:99], v[64:67]\n\t"
    "ds_read_b128 v[120:123], %[da] offset:20480\n\t"
    "v_cvt_pk_bf16_f32 v50, v52, v53\n\t"
    "v_mfma_f32_16x16x32_bf16 v[68:71], v[196:199], v[96:99], v[68:71]\n\t"
    "ds_read_b128 v[124:127], %[da] offset:22528\n\t"
    "v_cvt_pk_bf16_f32 v51, v54, v55\n\t"
    "v_mfma_f32_16x16x32_bf16 v[72:75], v[192:195], v[100:103], v[72:75]\n\t"
    "global_store_dwordx4 %[co0], v[48:51], s[36:37] offset:2048 sc1\n\t"
    "v_mfma_f32_16x16x32_bf16 v[76:79], v[196:199], v[100:103], v[76:79]\n\t"
    "v_cvt_pk_bf16_f32 v56, v56, v57\n\t"
    "s_waitcnt lgkmcnt(4)\n\t"
    "v_mfma_f32_16x16x32_bf16 v[64:67], v[200:203], v[104:107], v[64:67]\n\t"
    "ds_read_b128 v[96:99], %[ea] offset:24576\n\t"
    "v_cvt_pk_bf16_f32 v57, v58, v59\n\t"
    "v_mfma_f32_16x16x32_bf16 v[68:71], v[204:207], v[104:107], v[68:71]\n\t"
    "ds_read_b128 v[100:103], %[ea] offset:26624\n\t"
    "v_cvt_pk_bf16_f32 v58, v60, v61\n\t"
    "v_mfma_f32_16x16x32_bf16 v[72:75], v[200:203], v[108:111], v[72:75]\n\t"
    "v_cvt_pk_bf16_f32 v59, v62, v63\n\t"
    "v_mfma_f32_16x16x32_bf16 v[76:79], v[204:207], v[108:111], v[76:79]\n\t"
    "global_store_dwordx4 %[co1], v[56:59], s[36:37] offset:2048 sc1\n\t"
    "s_waitcnt lgkmcnt(4)\n\t"
    "v_mfma_f32_16x16x32_bf16 v[64:67], v[208:211], v[112:115], v[64:67]\n\t"
    "ds_read_b128 v[104:107], %[da] offset:24576\n\t"
    "v_mfma_f32_16x16x32_bf16 v[68:71], v[212:215], v[112:115], v[68:71]\n\t"
    "ds_read_b128 v[108:111], %[da] offset:26624\n\t"
    "v_mfma_f32_16x16x32_bf16 v[72:75], v[208:211], v[116:119], v[72:75]\n\t"
    "v_mfma_f32_16x16x32_bf16 v[76:79], v[212:215], v[116:119], v[76:79]\n\t"
    "s_waitcnt lgkmcnt(4)\n\t"
    "v_mfma_f32_16x16x32_bf16 v[64:67], v[216:219], v[120:123], v[64:67]\n\t"
    "ds_read_b128 v[112:115], %[ea] offset:28672\n\t"
    "v_mfma_f32_16x16x32_bf16 v[68:71], v[220:223], v[120:123], v[68:71]\n\t"
    "ds_read_b128 v[116:119], %[ea] offset:30720\n\t"
    "v_mfma_f32_16x16x32_bf16 v[72:75], v[216:219], v[124:127], v[72:75]\n\t"
    "v_xor_b32 %[ea], 0x10000, %[ea]\n\t"
    "v_mfma_f32_16x16x32_bf16 v[76:79], v[220:223], v[124:127], v[76:79]\n\t"
    "s_waitcnt vmcnt(4)\n\t"
    "s_barrier\n\t"
    "s_waitcnt lgkmcnt(4)\n\t"
    "v_mfma_f32_16x16x32_bf16 v[64:67], v[224:227], v[96:99], v[64:67]\n\t"
    "ds_read_b32 v39, %[sa]\n\t"
    "ds_read_b128 v[120:123], %[da] offset:28672\n\t"
    "v_mfma_f32_16x16x32_bf16 v[68:71], v[228:231], v[96:99], v[68:71]\n\t"
    "ds_read_b128 v[124:127], %[da] offset:30720\n\t"
    "v_mfma_f32_16x16x32_bf16 v[72:75], v[224:227], v[100:103], v[72:75]\n\t"
    "v_xor_b32 %[da], 0x10000, %[da]\n\t"
    "v_mfma_f32_16x16x32_bf16 v[76:79], v[228:231], v[100:103], v[76:79]\n\t"
    "s_waitcnt lgkmcnt(5)\n\t"
    "v_mfma_f32_16x16x32_bf16 v[64:67], v[232:235], v[104:107], v[64:67]\n\t"
    "ds_read_b128 v[96:99], %[ea] offset:0\n\t"
    "v_mfma_f32_16x16x32_bf16 v[68:71], v[236:239], v[104:107], v[68:71]\n\t"
    "ds_read_b128 v[100:103], %[ea] offset:2048\n\t"
    "v_mfma_f32_16x16x32_bf16 v[72:75], v[232:235], v[108:111], v[72:75]\n\t"
    "v_mfma_f32_16x16x32_bf16 v[76:79], v[236:239], v[108:111], v[76:79]\n\t"
    "s_waitcnt lgkmcnt(4)\n\t"
    "v_mfma_f32_16x16x32_bf16 v[64:67], v[240:243], v[112:115], v[64:67]\n\t"
    "ds_read_b128 v[104:107], %[da] offset:0\n\t"
    "v_mad_u32_u24 v39, v39, %[mu], %[ad]\n\t"
    "v_mfma_f32_16x16x32_bf16 v[68:71], v[244:247], v[112:115], v[68:71]\n\t"
    "ds_read_b128 v[108:111], %[da] offset:2048\n\t"
    "v_cmp_ge_i32 vcc, %[lf], v39\n\t"
    "s_cmp_eq_u64 vcc, 0\n\t"
    "s_addc_u32 %[lf], %[lf], 0\n\t"
    "v_mfma_f32_16x16x32_bf16 v[72:75], v[240:243], v[116:119], v[72:75]\n\t"
    "v_mfma_f32_16x16x32_bf16 v[76:79], v[244:247], v[116:119], v[76:79]\n\t"
    "v_cmp_ge_i32 vcc, %[lf], v39\n\t"
    "s_cmp_eq_u64 vcc, 0\n\t"
    "s_addc_u32 %[lf], %[lf], 0\n\t"
    "s_waitcnt lgkmcnt(4)\n\t"
    "v_mfma_f32_16x16x32_bf16 v[80:83], v[248:251], v[120:123], v[64:67]\n\t"
    "ds_read_b128 v[112:115], %[ea] offset:4096\n\t"
    "s_min_i32 %[lf], %[lf], %[cp]\n\t"
    "s_add_u32 %[b], %[b], 1\n\t"
    "v_mfma_f32_16x16x32_bf16 v[84:87], v[252:255], v[120:123], v[68:71]\n\t"
    "ds_read_b128 v[116:119], %[ea] offset:6144\n\t"
    "v_mfma_f32_16x16x32_bf16 v[88:91], v[248:251], v[124:127], v[72:75]\n\t"
    "v_mfma_f32_16x16x32_bf16 v[92:95], v[252:255], v[124:127], v[76:79]\n\t"
    "s_mov_b64 s[36:37], s[38:39]\n\t"
    "s_add_u32 s38, s38, %[ab]\n\t"
    "s_addc_u32 s39, s39, 0\n\t"
    "s_cmp_lt_i32 %[b], %[lf]\n\t"
    "s_cbranch_scc1 1b\n\t"
    "s_waitcnt lgkmcnt(0)\n\t"
    "s_nop 4\n\t"
    "s_branch 9f\n\t"
    "5:\n\t"
    "s_add_u32 %[st], %[b], %[po]\n\t"
    "s_max_i32 %[st], %[st], %[p0]\n\t"
    "v_mov_b32 v39, %[st]\n\t"
    "v_mov_b32 v38, 0\n\t"
    "s_mov_b64 exec, 1\n\t"
    "global_store_dword v38, v39, %[pg] sc1\n\t"
    "s_mov_b64 exec, -1\n\t"
    "s_mov_b32 %[p0], %[p1]\n\t"
    "s_cmp_ge_i32 %[lf], %[cp]\n\t"
    "s_cbranch_scc1 6b\n\t"
    "s_mov_b32 m0, %[sl]\n\t"
    "s_nop 0\n\t"
    "global_load_lds_dword %[dv], %[db] sc1\n\t"
    "s_branch 6b\n\t"
    "9:"
    : "=&{v[80:95]}"(pd), [ea] "+&v"(ea), [da] "+&v"(da), [b] "+&s"(b), [lf] "+&s"(lf), "+{s[36:37]}"(p1), "+{s[38:39]}"(p0), [sa] "+&v"(sa), [st] "=&s"(st), [sl] "+&s"(sl), [p0] "+&s"(pf)
    : [rw] "v"(rw), [ab] "s"(ab), [cp] "s"(cp), [mu] "s"(mu), [ad] "v"(ad), "{v[128:159]}"(W[0]), "{v[160:191]}"(W[1]), "{v[192:223]}"(W[2]), "{v[224:255]}"(W[3]), [co0] "v"(co0), [w0] "s"(w0), [dv] "v"(dv), [db] "s"(db), [pg] "s"(pg), [po] "s"(po - 1), [p1] "s"(b + po), [co1] "v"(co1)
    : "v38", "v39", "v40", "v41", "v42", "v43", "v44", "v45", "v46", "v47", "v48", "v49", "v50", "v51", "v52", "v53", "v54", "v55", "v56", "v57", "v58", "v59", "v60", "v61", "v62", "v63", "v64", "v65", "v66", "v67", "v68", "v69", "v70", "v71", "v72", "v73", "v74", "v75", "v76", "v77", "v78", "v79", "v96", "v97", "v98", "v99", "v100", "v101", "v102", "v103", "v104", "v105", "v106", "v107", "v108", "v109", "v110", "v111", "v112", "v113", "v114", "v115", "v116", "v117", "v118", "v119", "v120", "v121", "v122", "v123", "v124", "v125", "v126", "v127", "vcc", "scc", "m0", "memory");
}
__device__ __forceinline__ void lean_ld0(const u32x32 (&W)[4], u32x16& pd, unsigned ea, unsigned da, unsigned x, unsigned lb, const char* s0, const char* s1, const unsigned (&av)[8], unsigned sa, unsigned mu, unsigned ad, int th, unsigned s2, unsigned dv, const char* db) {
  asm volatile("s_setprio 2\n\t"
    "ds_read_b128 v[96:99], %[ea] offset:0\n\t"
    "ds_read_b128 v[100:103], %[ea] offset:2048\n\t"
    "ds_read_b128 v[104:107], %[da] offset:0\n\t"
    "ds_read_b128 v[108:111], %[da] offset:2048\n\t"
    "ds_read_b128 v[112:115], %[ea] offset:4096\n\t"
    "ds_read_b128 v[116:119], %[ea] offset:6144\n\t"
    "s_waitcnt lgkmcnt(4)\n\t"
    "v_mfma_f32_16x16x32_bf16 v[80:83], v[128:131], v[96:99], 0\n\t"
    "ds_read_b128 v[120:123], %[da] offset:4096\n\t"
    "v_mfma_f32_16x16x32_bf16 v[84:87], v[132:135], v[96:99], 0\n\t"
    "ds_read_b128 v[124:127], %[da] offset:6144\n\t"
    "v_mfma_f32_16x16x32_bf16 v[88:91], v[128:131], v[100:103], 0\n\t"
    "v_mfma_f32_16x16x32_bf16 v[92:95], v[132:135], v[100:103], 0\n\t"
    "s_waitcnt vmcnt(24)\n\t"
    "s_cmp_lt_u32 %[x], 2\n\t"
    "s_cbranch_scc0 41f\n\t"
    "s_waitcnt vmcnt(8)\n\t"
    "41:\n\t"
    "s_barrier\n\t"
    "s_waitcnt lgkmcnt(4)\n\t"
    "v_mfma_f32_16x16x32_bf16 v[80:83], v[136:139], v[104:107], v[80:83]\n\t"
    "ds_read_b128 v[96:99], %[ea] offset:8192\n\t"
    "v_mfma_f32_16x16x32_bf16 v[84:87], v[140:143], v[104:107], v[84:87]\n\t"
    "ds_read_b128 v[100:103], %[ea] offset:10240\n\t"
    "v_mfma_f32_16x16x32_bf16 v[88:91], v[136:139], v[108:111], v[88:91]\n\t"
    "v_mfma_f32_16x16x32_bf16 v[92:95], v[140:143], v[108:111], v[92:95]\n\t"
    "s_waitcnt lgkmcnt(4)\n\t"
    "v_mfma_f32_16x16x32_bf16 v[80:83], v[144:147], v[112:115], v[80:83]\n\t"
    "ds_read_b128 v[104:107], %[da] offset:8192\n\t"
    "v_mfma_f32_16x16x32_bf16 v[84:87], v[148:151], v[112:115], v[84:87]\n\t"
    "ds_read_b128 v[108:111], %[da] offset:10240\n\t"
    "v_mfma_f32_16x16x32_bf16 v[88:91], v[144:147], v[116:119], v[88:91]\n\t"
    "v_mfma_f32_16x16x32_bf16 v[92:95], v[148:151], v[116:119], v[92:95]\n\t"
    "s_waitcnt lgkmcnt(4)\n\t"
    "v_mfma_f32_16x16x32_bf16 v[80:83], v[152:155], v[120:123], v[80:83]\n\t"
    "ds_read_b128 v[112:115], %[ea] offset:12288\n\t"
    "v_mfma_f32_16x16x32_bf16 v[84:87], v[156:159], v[120:123], v[84:87]\n\t"
    "ds_read_b128 v[116:119], %[ea] offset:14336\n\t"
    "v_mfma_f32_16x16x32_bf16 v[88:91], v[152:155], v[124:127], v[88:91]\n\t"
    "v_mfma_f32_16x16x32_bf16 v[92:95], v[156:159], v[124:127], v[92:95]\n\t"
    "s_waitcnt lgkmcnt(4)\n\t"
    "v_mfma_f32_16x16x32_bf16 v[80:83], v[160:163], v[96:99], v[80:83]\n\t"
    "ds_read_b128 v[120:123], %[da] offset:12288\n\t"
    "v_mfma_f32_16x16x32_bf16 v[84:87], v[164:167], v[96:99], v[84:87]\n\t"
    "ds_read_b128 v[124:127], %[da] offset:14336\n\t"
    "v_mfma_f32_16x16x32_bf16 v[88:91], v[160:163], v[100:103], v[88:91]\n\t"
    "v_mfma_f32_16x16x32_bf16 v[92:95], v[164:167], v[100:103], v[92:95]\n\t"
    "s_waitcnt vmcnt(20)\n\t"
    "s_cmp_lt_u32 %[x], 2\n\t"
    "s_cbranch_scc0 45f\n\t"
    "s_waitcnt vmcnt(4)\n\t"
    "45:\n\t"
    "s_barrier\n\t"
    "s_waitcnt lgkmcnt(4)\n\t"
    "v_mfma_f32_16x16x32_bf16 v[80:83], v[168:171], v[104:107], v[80:83]\n\t"
    "ds_read_b128 v[96:99], %[ea] offset:16384\n\t"
    "v_mfma_f32_16x16x32_bf16 v[84:87], v[172:175], v[104:107], v[84:87]\n\t"
    "ds_read_b128 v[100:103], %[ea] offset:18432\n\t"
    "v_mfma_f32_16x16x32_bf16 v[88:91], v[168:171], v[108:111], v[88:91]\n\t"
    "v_mfma_f32_16x16x32_bf16 v[92:95], v[172:175], v[108:111], v[92:95]\n\t"
    "s_waitcnt lgkmcnt(4)\n\t"
    "v_mfma_f32_16x16x32_bf16 v[80:83], v[176:179], v[112:115], v[80:83]\n\t"
    "ds_read_b128 v[104:107], %[da] offset:16384\n\t"
    "v_mfma_f32_16x16x32_bf16 v[84:87], v[180:183], v[112:115], v[84:87]\n\t"
    "ds_read_b128 v[108:111], %[da] offset:18432\n\t"
    "v_mfma_f32_16x16x32_bf16 v[88:91], v[176:179], v[116:119], v[88:91]\n\t"
    "v_mfma_f32_16x16x32_bf16 v[92:95], v[180:183], v[116:119], v[92:95]\n\t"
    "s_waitcnt lgkmcnt(4)\n\t"
    "v_mfma_f32_16x16x32_bf16 v[80:83], v[184:187], v[120:123], v[80:83]\n\t"
    "ds_read_b128 v[112:115], %[ea] offset:20480\n\t"
    "v_mfma_f32_16x16x32_bf16 v[84:87], v[188:191], v[120:123], v[84:87]\n\t"
    "ds_read_b128 v[116:119], %[ea] offset:22528\n\t"
    "v_mfma_f32_16x16x32_bf16 v[88:91], v[184:187], v[124:127], v[88:91]\n\t"
    "v_mfma_f32_16x16x32_bf16 v[92:95], v[188:191], v[124:127], v[92:95]\n\t"
    "s_waitcnt lgkmcnt(4)\n\t"
    "v_mfma_f32_16x16x32_bf16 v[80:83], v[192:195], v[96:99], v[80:83]\n\t"
    "ds_read_b128 v[120:123], %[da] offset:20480\n\t"
    "v_mfma_f32_16x16x32_bf16 v[84:87], v[196:199], v[96:99], v[84:87]\n\t"
    "ds_read_b128 v[124:127], %[da] offset:22528\n\t"
    "v_mfma_f32_16x16x32_bf16 v[88:91], v[192:195], v[100:103], v[88:91]\n\t"
    "v_mfma_f32_16x16x32_bf16 v[92:95], v[196:199], v[100:103], v[92:95]\n\t"
    "s_waitcnt vmcnt(16)\n\t"
    "s_cmp_lt_u32 %[x], 2\n\t"
    "s_cbranch_scc0 49f\n\t"
    "s_waitcnt vmcnt(0)\n\t"
    "49:\n\t"
    "s_barrier\n\t"
    "ds_read_b32 v79, %[sa]\n\t"
    "s_waitcnt lgkmcnt(5)\n\t"
    "v_mfma_f32_16x16x32_bf16 v[80:83], v[200:203], v[104:107], v[80:83]\n\t"
    "ds_read_b128 v[96:99], %[ea] offset:24576\n\t"
    "v_mfma_f32_16x16x32_bf16 v[84:87], v[204:207], v[104:107], v[84:87]\n\t"
    "ds_read_b128 v[100:103], %[ea] offset:26624\n\t"
    "v_mfma_f32_16x16x32_bf16 v[88:91], v[200:203], v[108:111], v[88:91]\n\t"
    "v_mfma_f32_16x16x32_bf16 v[92:95], v[204:207], v[108:111], v[92:95]\n\t"
    "s_waitcnt lgkmcnt(5)\n\t"
    "v_mfma_f32_16x16x32_bf16 v[80:83], v[208:211], v[112:115], v[80:83]\n\t"
    "ds_read_b128 v[104:107], %[da] offset:24576\n\t"
    "v_mfma_f32_16x16x32_bf16 v[84:87], v[212:215], v[112:115], v[84:87]\n\t"
    "ds_read_b128 v[108:111], %[da] offset:26624\n\t"
    "v_mfma_f32_16x16x32_bf16 v[88:91], v[208:211], v[116:119], v[88:91]\n\t"
    "v_mfma_f32_16x16x32_bf16 v[92:95], v[212:215], v[116:119], v[92:95]\n\t"
    "s_cmp_eq_u32 %[s2], 0\n\t"
    "s_cbranch_scc1 7f\n\t"
    "s_mov_b32 m0, %[s2]\n\t"
    "s_nop 0\n\t"
    "global_load_lds_dword %[dv], %[db] sc1\n\t"
    "7:\n\t"
    "s_waitcnt lgkmcnt(4)\n\t"
    "v_mfma_f32_16x16x32_bf16 v[80:83], v[216:219], v[120:123], v[80:83]\n\t"
    "ds_read_b128 v[112:115], %[ea] offset:28672\n\t"
    "v_mfma_f32_16x16x32_bf16 v[84:87], v[220:223], v[120:123], v[84:87]\n\t"
    "ds_read_b128 v[116:119], %[ea] offset:30720\n\t"
    "v_mfma_f32_16x16x32_bf16 v[88:91], v[216:219], v[124:127], v[88:91]\n\t"
    "v_mad_u32_u24 v79, v79, %[mu], %[ad]\n\t"
    "v_cmp_ge_i32 vcc, %[th], v79\n\t"
    "v_mfma_f32_16x16x32_bf16 v[92:95], v[220:223], v[124:127], v[92:95]\n\t"
    "s_cbranch_vccz 5f\n\t"
    "s_waitcnt lgkmcnt(4)\n\t"
    "v_mfma_f32_16x16x32_bf16 v[80:83], v[224:227], v[96:99], v[80:83]\n\t"
    "ds_read_b128 v[120:123], %[da] offset:28672\n\t"
    "v_mfma_f32_16x16x32_bf16 v[84:87], v[228:231], v[96:99], v[84:87]\n\t"
    "ds_read_b128 v[124:127], %[da] offset:30720\n\t"
    "v_mfma_f32_16x16x32_bf16 v[88:91], v[224:227], v[100:103], v[88:91]\n\t"
    "v_mfma_f32_16x16x32_bf16 v[92:95], v[228:231], v[100:103], v[92:95]\n\t"
    "s_waitcnt lgkmcnt(4)\n\t"
    "v_mfma_f32_16x16x32_bf16 v[80:83], v[232:235], v[104:107], v[80:83]\n\t"
    "v_mfma_f32_16x16x32_bf16 v[84:87], v[236:239], v[104:107], v[84:87]\n\t"
    "v_mfma_f32_16x16x32_bf16 v[88:91], v[232:235], v[108:111], v[88:91]\n\t"
    "v_mfma_f32_16x16x32_bf16 v[92:95], v[236:239], v[108:111], v[92:95]\n\t"
    "s_waitcnt lgkmcnt(2)\n\t"
    "v_mfma_f32_16x16x32_bf16 v[80:83], v[240:243], v[112:115], v[80:83]\n\t"
    "v_mfma_f32_16x16x32_bf16 v[84:87], v[244:247], v[112:115], v[84:87]\n\t"
    "v_mfma_f32_16x16x32_bf16 v[88:91], v[240:243], v[116:119], v[88:91]\n\t"
    "v_mfma_f32_16x16x32_bf16 v[92:95], v[244:247], v[116:119], v[92:95]\n\t"
    "s_waitcnt lgkmcnt(0)\n\t"
    "v_mfma_f32_16x16x32_bf16 v[80:83], v[248:251], v[120:123], v[80:83]\n\t"
    "v_mfma_f32_16x16x32_bf16 v[84:87], v[252:255], v[120:123], v[84:87]\n\t"
    "v_mfma_f32_16x16x32_bf16 v[88:91], v[248:251], v[124:127], v[88:91]\n\t"
    "v_mfma_f32_16x16x32_bf16 v[92:95], v[252:255], v[124:127], v[92:95]\n\t"
    "s_branch 6f\n\t"
    "5:\n\t"
    "s_waitcnt lgkmcnt(4)\n\t"
    "v_mfma_f32_16x16x32_bf16 v[80:83], v[224:227], v[96:99], v[80:83]\n\t"
    "ds_read_b128 v[120:123], %[da] offset:28672\n\t"
    "s_add_u32 m0, %[lb], 0\n\t"
    "s_nop 0\n\t"
    "global_load_lds_dwordx4 %[av0], %[s0] sc1\n\t"
    "v_mfma_f32_16x16x32_bf16 v[84:87], v[228:231], v[96:99], v[84:87]\n\t"
    "ds_read_b128 v[124:127], %[da] offset:30720\n\t"
    "s_add_u32 m0, %[lb], 32768\n\t"
    "s_nop 0\n\t"
    "global_load_lds_dwordx4 %[av1], %[s0] sc1\n\t"
    "v_mfma_f32_16x16x32_bf16 v[88:91], v[224:227], v[100:103], v[88:91]\n\t"
    "s_add_u32 m0, %[lb], 4096\n\t"
    "s_nop 0\n\t"
    "global_load_lds_dwordx4 %[av2], %[s0] sc1\n\t"
    "v_mfma_f32_16x16x32_bf16 v[92:95], v[228:231], v[100:103], v[92:95]\n\t"
    "s_add_u32 m0, %[lb], 36864\n\t"
    "s_nop 0\n\t"
    "global_load_lds_dwordx4 %[av3], %[s0] sc1\n\t"
    "s_waitcnt lgkmcnt(4)\n\t"
    "v_mfma_f32_16x16x32_bf16 v[80:83], v[232:235], v[104:107], v[80:83]\n\t"
    "s_add_u32 m0, %[lb], 8192\n\t"
    "s_nop 0\n\t"
    "global_load_lds_dwordx4 %[av4], %[s0] sc1\n\t"
    "v_mfma_f32_16x16x32_bf16 v[84:87], v[236:239], v[104:107], v[84:87]\n\t"
    "s_add_u32 m0, %[lb], 40960\n\t"
    "s_nop 0\n\t"
    "global_load_lds_dwordx4 %[av5], %[s0] sc1\n\t"
    "v_mfma_f32_16x16x32_bf16 v[88:91], v[232:235], v[108:111], v[88:91]\n\t"
    "s_add_u32 m0, %[lb], 12288\n\t"
    "s_nop 0\n\t"
    "global_load_lds_dwordx4 %[av6], %[s0] sc1\n\t"
    "v_mfma_f32_16x16x32_bf16 v[92:95], v[236:239], v[108:111], v[92:95]\n\t"
    "s_add_u32 m0, %[lb], 45056\n\t"
    "s_nop 0\n\t"
    "global_load_lds_dwordx4 %[av7], %[s0] sc1\n\t"
    "s_waitcnt lgkmcnt(2)\n\t"
    "v_mfma_f32_16x16x32_bf16 v[80:83], v[240:243], v[112:115], v[80:83]\n\t"
    "s_add_u32 m0, %[lb], 16384\n\t"
    "s_nop 0\n\t"
    "global_load_lds_dwordx4 %[av0], %[s1] sc1\n\t"
    "v_mfma_f32_16x16x32_bf16 v[84:87], v[244:247], v[112:115], v[84:87]\n\t"
    "s_add_u32 m0, %[lb], 49152\n\t"
    "s_nop 0\n\t"
    "global_load_lds_dwordx4 %[av1], %[s1] sc1\n\t"
    "v_mfma_f32_16x16x32_bf16 v[88:91], v[240:243], v[116:119], v[88:91]\n\t"
    "s_add_u32 m0, %[lb], 20480\n\t"
    "s_nop 0\n\t"
    "global_load_lds_dwordx4 %[av2], %[s1] sc1\n\t"
    "v_mfma_f32_16x16x32_bf16 v[92:95], v[244:247], v[116:119], v[92:95]\n\t"
    "s_add_u32 m0, %[lb], 53248\n\t"
    "s_nop 0\n\t"
    "global_load_lds_dwordx4 %[av3], %[s1] sc1\n\t"
    "s_waitcnt lgkmcnt(0)\n\t"
    "v_mfma_f32_16x16x32_bf16 v[80:83], v[248:251], v[120:123], v[80:83]\n\t"
    "s_add_u32 m0, %[lb], 24576\n\t"
    "s_nop 0\n\t"
    "global_load_lds_dwordx4 %[av4], %[s1] sc1\n\t"
    "v_mfma_f32_16x16x32_bf16 v[84:87], v[252:255], v[120:123], v[84:87]\n\t"
    "s_add_u32 m0, %[lb], 57344\n\t"
    "s_nop 0\n\t"
    "global_load_lds_dwordx4 %[av5], %[s1] sc1\n\t"
    "v_mfma_f32_16x16x32_bf16 v[88:91], v[248:251], v[124:127], v[88:91]\n\t"
    "s_add_u32 m0, %[lb], 28672\n\t"
    "s_nop 0\n\t"
    "global_load_lds_dwordx4 %[av6], %[s1] sc1\n\t"
    "v_mfma_f32_16x16x32_bf16 v[92:95], v[252:255], v[124:127], v[92:95]\n\t"
    "s_add_u32 m0, %[lb], 61440\n\t"
    "s_nop 0\n\t"
    "global_load_lds_dwordx4 %[av7], %[s1] sc1\n\t"
    "6:\n\t"
    "s_nop 6\n\t"
    "s_branch 9f\n\t"
    "3:\n\t"
    "9:"
    : "=&{v[80:95]}"(pd)
    : [ea] "v"(ea), [da] "v"(da), [x] "s"(x), "{v[128:159]}"(W[0]), "{v[160:191]}"(W[1]), "{v[192:223]}"(W[2]), "{v[224:255]}"(W[3]), [lb] "s"(lb), [s0] "s"(s0), [s1] "s"(s1), [av0] "v"(av[0]), [av1] "v"(av[1]), [av2] "v"(av[2]), [av3] "v"(av[3]), [av4] "v"(av[4]), [av5] "v"(av[5]), [av6] "v"(av[6]), [av7] "v"(av[7]), [sa] "v"(sa), [mu] "s"(mu), [ad] "v"(ad), [th] "s"(th), [s2] "s"(s2), [dv] "v"(dv), [db] "s"(db)
    : "v96", "v97", "v98", "v99", "v100", "v101", "v102", "v103", "v104", "v105", "v106", "v107", "v108", "v109", "v110", "v111", "v112", "v113", "v114", "v115", "v116", "v117", "v118", "v119", "v120", "v121", "v122", "v123", "v124", "v125", "v126", "v127", "v79", "vcc", "scc", "m0", "memory");
}
__device__ __forceinline__ void lean_dm0(const u32x32 (&W)[4], u32x16& pd, unsigned ea, unsigned da, unsigned x, unsigned lb, const char* s0, const char* s1, const unsigned (&av)[8]) {
  asm volatile("s_setprio 2\n\t"
    "ds_read_b128 v[96:99], %[ea] offset:0\n\t"
    "ds_read_b128 v[100:103], %[ea] offset:2048\n\t"
    "ds_read_b128 v[104:107], %[da] offset:0\n\t"
    "ds_read_b128 v[108:111], %[da] offset:2048\n\t"
    "ds_read_b128 v[112:115], %[ea] offset:4096\n\t"
    "ds_read_b128 v[116:119], %[ea] offset:6144\n\t"
    "s_waitcnt lgkmcnt(4)\n\t"
    "v_mfma_f32_16x16x32_bf16 v[80:83], v[128:131], v[96:99], 0\n\t"
    "ds_read_b128 v[120:123], %[da] offset:4096\n\t"
    "s_add_u32 m0, %[lb], 0\n\t"
    "v_mfma_f32_16x16x32_bf16 v[84:87], v[132:135], v[96:99], 0\n\t"
    "ds_read_b128 v[124:127], %[da] offset:6144\n\t"
    "global_load_lds_dwordx4 %[av0], %[s0] sc1\n\t"
    "v_mfma_f32_16x16x32_bf16 v[88:91], v[128:131], v[100:103], 0\n\t"
    "s_add_u32 m0, %[lb], 32768\n\t"
    "v_mfma_f32_16x16x32_bf16 v[92:95], v[132:135], v[100:103], 0\n\t"
    "global_load_lds_dwordx4 %[av1], %[s0] sc1\n\t"
    "s_waitcnt vmcnt(10)\n\t"
    "s_barrier\n\t"
    "s_waitcnt lgkmcnt(4)\n\t"
    "v_mfma_f32_16x16x32_bf16 v[80:83], v[136:139], v[104:107], v[80:83]\n\t"
    "ds_read_b128 v[96:99], %[ea] offset:8192\n\t"
    "s_add_u32 m0, %[lb], 4096\n\t"
    "v_mfma_f32_16x16x32_bf16 v[84:87], v[140:143], v[104:107], v[84:87]\n\t"
    "ds_read_b128 v[100:103], %[ea] offset:10240\n\t"
    "global_load_lds_dwordx4 %[av2], %[s0] sc1\n\t"
    "v_mfma_f32_16x16x32_bf16 v[88:91], v[136:139], v[108:111], v[88:91]\n\t"
    "s_add_u32 m0, %[lb], 36864\n\t"
    "v_mfma_f32_16x16x32_bf16 v[92:95], v[140:143], v[108:111], v[92:95]\n\t"
    "global_load_lds_dwordx4 %[av3], %[s0] sc1\n\t"
    "s_waitcnt lgkmcnt(4)\n\t"
    "v_mfma_f32_16x16x32_bf16 v[80:83], v[144:147], v[112:115], v[80:83]\n\t"
    "ds_read_b128 v[104:107], %[da] offset:8192\n\t"
    "s_add_u32 m0, %[lb], 8192\n\t"
    "v_mfma_f32_16x16x32_bf16 v[84:87], v[148:151], v[112:115], v[84:87]\n\t"
    "ds_read_b128 v[108:111], %[da] offset:10240\n\t"
    "global_load_lds_dwordx4 %[av4], %[s0] sc1\n\t"
    "v_mfma_f32_16x16x32_bf16 v[88:91], v[144:147], v[116:119], v[88:91]\n\t"
    "s_add_u32 m0, %[lb], 40960\n\t"
    "v_mfma_f32_16x16x32_bf16 v[92:95], v[148:151], v[116:119], v[92:95]\n\t"
    "global_load_lds_dwordx4 %[av5], %[s0] sc1\n\t"
    "s_waitcnt lgkmcnt(4)\n\t"
    "v_mfma_f32_16x16x32_bf16 v[80:83], v[152:155], v[120:123], v[80:83]\n\t"
    "ds_read_b128 v[112:115], %[ea] offset:12288\n\t"
    "s_add_u32 m0, %[lb], 12288\n\t"
    "v_mfma_f32_16x16x32_bf16 v[84:87], v[156:159], v[120:123], v[84:87]\n\t"
    "ds_read_b128 v[116:119], %[ea] offset:14336\n\t"
    "global_load_lds_dwordx4 %[av6], %[s0] sc1\n\t"
    "v_mfma_f32_16x16x32_bf16 v[88:91], v[152:155], v[124:127], v[88:91]\n\t"
    "s_add_u32 m0, %[lb], 45056\n\t"
    "v_mfma_f32_16x16x32_bf16 v[92:95], v[156:159], v[124:127], v[92:95]\n\t"
    "global_load_lds_dwordx4 %[av7], %[s0] sc1\n\t"
    "s_waitcnt lgkmcnt(4)\n\t"
    "v_mfma_f32_16x16x32_bf16 v[80:83], v[160:163], v[96:99], v[80:83]\n\t"
    "ds_read_b128 v[120:123], %[da] offset:12288\n\t"
    "s_add_u32 m0, %[lb], 16384\n\t"
    "v_mfma_f32_16x16x32_bf16 v[84:87], v[164:167], v[96:99], v[84:87]\n\t"
    "ds_read_b128 v[124:127], %[da] offset:14336\n\t"
    "global_load_lds_dwordx4 %[av0], %[s1] sc1\n\t"
    "v_mfma_f32_16x16x32_bf16 v[88:91], v[160:163], v[100:103], v[88:91]\n\t"
    "s_add_u32 m0, %[lb], 49152\n\t"
    "v_mfma_f32_16x16x32_bf16 v[92:95], v[164:167], v[100:103], v[92:95]\n\t"
    "global_load_lds_dwordx4 %[av1], %[s1] sc1\n\t"
    "s_waitcnt vmcnt(14)\n\t"
    "s_barrier\n\t"
    "s_waitcnt lgkmcnt(4)\n\t"
    "v_mfma_f32_16x16x32_bf16 v[80:83], v[168:171], v[104:107], v[80:83]\n\t"
    "ds_read_b128 v[96:99], %[ea] offset:16384\n\t"
    "s_add_u32 m0, %[lb], 20480\n\t"
    "v_mfma_f32_16x16x32_bf16 v[84:87], v[172:175], v[104:107], v[84:87]\n\t"
    "ds_read_b128 v[100:103], %[ea] offset:18432\n\t"
    "global_load_lds_dwordx4 %[av2], %[s1] sc1\n\t"
    "v_mfma_f32_16x16x32_bf16 v[88:91], v[168:171], v[108:111], v[88:91]\n\t"
    "s_add_u32 m0, %[lb], 53248\n\t"
    "v_mfma_f32_16x16x32_bf16 v[92:95], v[172:175], v[108:111], v[92:95]\n\t"
    "global_load_lds_dwordx4 %[av3], %[s1] sc1\n\t"
    "s_waitcnt lgkmcnt(4)\n\t"
    "v_mfma_f32_16x16x32_bf16 v[80:83], v[176:179], v[112:115], v[80:83]\n\t"
    "ds_read_b128 v[104:107], %[da] offset:16384\n\t"
    "s_add_u32 m0, %[lb], 24576\n\t"
    "v_mfma_f32_16x16x32_bf16 v[84:87], v[180:183], v[112:115], v[84:87]\n\t"
    "ds_read_b128 v[108:111], %[da] offset:18432\n\t"
    "global_load_lds_dwordx4 %[av4], %[s1] sc1\n\t"
    "v_mfma_f32_16x16x32_bf16 v[88:91], v[176:179], v[116:119], v[88:91]\n\t"
    "s_add_u32 m0, %[lb], 57344\n\t"
    "v_mfma_f32_16x16x32_bf16 v[92:95], v[180:183], v[116:119], v[92:95]\n\t"
    "global_load_lds_dwordx4 %[av5], %[s1] sc1\n\t"
    "s_waitcnt lgkmcnt(4)\n\t"
    "v_mfma_f32_16x16x32_bf16 v[80:83], v[184:187], v[120:123], v[80:83]\n\t"
    "ds_read_b128 v[112:115], %[ea] offset:20480\n\t"
    "s_add_u32 m0, %[lb], 28672\n\t"
    "v_mfma_f32_16x16x32_bf16 v[84:87], v[188:191], v[120:123], v[84:87]\n\t"
    "ds_read_b128 v[116:119], %[ea] offset:22528\n\t"
    "global_load_lds_dwordx4 %[av6], %[s1] sc1\n\t"
    "v_mfma_f32_16x16x32_bf16 v[88:91], v[184:187], v[124:127], v[88:91]\n\t"
    "s_add_u32 m0, %[lb], 61440\n\t"
    "v_mfma_f32_16x16x32_bf16 v[92:95], v[188:191], v[124:127], v[92:95]\n\t"
    "global_load_lds_dwordx4 %[av7], %[s1] sc1\n\t"
    "s_waitcnt lgkmcnt(4)\n\t"
    "v_mfma_f32_16x16x32_bf16 v[80:83], v[192:195], v[96:99], v[80:83]\n\t"
    "ds_read_b128 v[120:123], %[da] offset:20480\n\t"
    "v_mfma_f32_16x16x32_bf16 v[84:87], v[196:199], v[96:99], v[84:87]\n\t"
    "ds_read_b128 v[124:127], %[da] offset:22528\n\t"
    "v_mfma_f32_16x16x32_bf16 v[88:91], v[192:195], v[100:103], v[88:91]\n\t"
    "v_mfma_f32_16x16x32_bf16 v[92:95], v[196:199], v[100:103], v[92:95]\n\t"
    "s_waitcnt vmcnt(16)\n\t"
    "s_barrier\n\t"
    "s_waitcnt lgkmcnt(4)\n\t"
    "v_mfma_f32_16x16x32_bf16 v[80:83], v[200:203], v[104:107], v[80:83]\n\t"
    "ds_read_b128 v[96:99], %[ea] offset:24576\n\t"
    "v_mfma_f32_16x16x32_bf16 v[84:87], v[204:207], v[104:107], v[84:87]\n\t"
    "ds_read_b128 v[100:103], %[ea] offset:26624\n\t"
    "v_mfma_f32_16x16x32_bf16 v[88:91], v[200:203], v[108:111], v[88:91]\n\t"
    "v_mfma_f32_16x16x32_bf16 v[92:95], v[204:207], v[108:111], v[92:95]\n\t"
    "s_waitcnt lgkmcnt(4)\n\t"
    "v_mfma_f32_16x16x32_bf16 v[80:83], v[208:211], v[112:115], v[80:83]\n\t"
    "ds_read_b128 v[104:107], %[da] offset:24576\n\t"
    "v_mfma_f32_16x16x32_bf16 v[84:87], v[212:215], v[112:115], v[84:87]\n\t"
    "ds_read_b128 v[108:111], %[da] offset:26624\n\t"
    "v_mfma_f32_16x16x32_bf16 v[88:91], v[208:211], v[116:119], v[88:91]\n\t"
    "v_mfma_f32_16x16x32_bf16 v[92:95], v[212:215], v[116:119], v[92:95]\n\t"
    "s_waitcnt lgkmcnt(4)\n\t"
    "v_mfma_f32_16x16x32_bf16 v[80:83], v[216:219], v[120:123], v[80:83]\n\t"
    "ds_read_b128 v[112:115], %[ea] offset:28672\n\t"
    "v_mfma_f32_16x16x32_bf16 v[84:87], v[220:223], v[120:123], v[84:87]\n\t"
    "ds_read_b128 v[116:119], %[ea] offset:30720\n\t"
    "v_mfma_f32_16x16x32_bf16 v[88:91], v[216:219], v[124:127], v[88:91]\n\t"
    "v_mfma_f32_16x16x32_bf16 v[92:95], v[220:223], v[124:127], v[92:95]\n\t"
    "s_waitcnt lgkmcnt(4)\n\t"
    "v_mfma_f32_16x16x32_bf16 v[80:83], v[224:227], v[96:99], v[80:83]\n\t"
    "ds_read_b128 v[120:123], %[da] offset:28672\n\t"
    "v_mfma_f32_16x16x32_bf16 v[84:87], v[228:231], v[96:99], v[84:87]\n\t"
    "ds_read_b128 v[124:127], %[da] offset:30720\n\t"
    "v_mfma_f32_16x16x32_bf16 v[88:91], v[224:227], v[100:103], v[88:91]\n\t"
    "v_mfma_f32_16x16x32_bf16 v[92:95], v[228:231], v[100:103], v[92:95]\n\t"
    "s_waitcnt lgkmcnt(4)\n\t"
    "v_mfma_f32_16x16x32_bf16 v[80:83], v[232:235], v[104:107], v[80:83]\n\t"
    "v_mfma_f32_16x16x32_bf16 v[84:87], v[236:239], v[104:107], v[84:87]\n\t"
    "v_mfma_f32_16x16x32_bf16 v[88:91], v[232:235], v[108:111], v[88:91]\n\t"
    "v_mfma_f32_16x16x32_bf16 v[92:95], v[236:239], v[108:111], v[92:95]\n\t"
    "s_waitcnt lgkmcnt(2)\n\t"
    "v_mfma_f32_16x16x32_bf16 v[80:83], v[240:243], v[112:115], v[80:83]\n\t"
    "v_mfma_f32_16x16x32_bf16 v[84:87], v[244:247], v[112:115], v[84:87]\n\t"
    "v_mfma_f32_16x16x32_bf16 v[88:91], v[240:243], v[116:119], v[88:91]\n\t"
    "v_mfma_f32_16x16x32_bf16 v[92:95], v[244:247], v[116:119], v[92:95]\n\t"
    "s_waitcnt lgkmcnt(0)\n\t"
    "v_mfma_f32_16x16x32_bf16 v[80:83], v[248:251], v[120:123], v[80:83]\n\t"
    "v_mfma_f32_16x16x32_bf16 v[84:87], v[252:255], v[120:123], v[84:87]\n\t"
    "v_mfma_f32_16x16x32_bf16 v[88:91], v[248:251], v[124:127], v[88:91]\n\t"
    "v_mfma_f32_16x16x32_bf16 v[92:95], v[252:255], v[124:127], v[92:95]\n\t"
    "s_nop 6\n\t"
    "s_branch 9f\n\t"
    "3:\n\t"
    "9:"
    : "=&{v[80:95]}"(pd)
    : [ea] "v"(ea), [da] "v"(da), [x] "s"(x), "{v[128:159]}"(W[0]), "{v[160:191]}"(W[1]), "{v[192:223]}"(W[2]), "{v[224:255]}"(W[3]), [lb] "s"(lb), [s0] "s"(s0), [s1] "s"(s1), [av0] "v"(av[0]), [av1] "v"(av[1]), [av2] "v"(av[2]), [av3] "v"(av[3]), [av4] "v"(av[4]), [av5] "v"(av[5]), [av6] "v"(av[6]), [av7] "v"(av[7])
    : "v96", "v97", "v98", "v99", "v100", "v101", "v102", "v103", "v104", "v105", "v106", "v107", "v108", "v109", "v110", "v111", "v112", "v113", "v114", "v115", "v116", "v117", "v118", "v119", "v120", "v121", "v122", "v123", "v124", "v125", "v126", "v127", "vcc", "scc", "m0", "memory");
}
__device__ __forceinline__ void lean_ld1(const u32x32 (&W)[4], u32x16& pd, unsigned ea, unsigned da, unsigned x, unsigned lb, const char* s0, const char* s1, const unsigned (&av)[8], unsigned sa, unsigned mu, unsigned ad, int th, unsigned s2, unsigned dv, const char* db) {
  asm volatile("s_setprio 2\n\t"
    "ds_read_b128 v[96:99], %[ea] offset:0\n\t"
    "ds_read_b128 v[100:103], %[ea] offset:2048\n\t"
    "ds_read_b128 v[104:107], %[da] offset:0\n\t"
    "ds_read_b128 v[108:111], %[da] offset:2048\n\t"
    "ds_read_b128 v[112:115], %[ea] offset:4096\n\t"
    "ds_read_b128 v[116:119], %[ea] offset:6144\n\t"
    "s_waitcnt lgkmcnt(4)\n\t"
    "v_mfma_f32_16x16x32_bf16 v[80:83], v[128:131], v[96:99], 0\n\t"
    "ds_read_b128 v[120:123], %[da] offset:4096\n\t"
    "v_mfma_f32_16x16x32_bf16 v[84:87], v[132:135], v[96:99], 0\n\t"
    "ds_read_b128 v[124:127], %[da] offset:6144\n\t"
    "v_mfma_f32_16x16x32_bf16 v[88:91], v[128:131], v[100:103], 0\n\t"
    "v_mfma_f32_16x16x32_bf16 v[92:95], v[132:135], v[100:103], 0\n\t"
    "s_waitcnt vmcnt(12)\n\t"
    "s_cmp_lt_u32 %[x], 2\n\t"
    "s_cbranch_scc0 41f\n\t"
    "s_waitcnt vmcnt(4)\n\t"
    "41:\n\t"
    "s_barrier\n\t"
    "s_waitcnt lgkmcnt(4)\n\t"
    "v_mfma_f32_16x16x32_bf16 v[80:83], v[136:139], v[104:107], v[80:83]\n\t"
    "ds_read_b128 v[96:99], %[ea] offset:8192\n\t"
    "v_mfma_f32_16x16x32_bf16 v[84:87], v[140:143], v[104:107], v[84:87]\n\t"
    "ds_read_b128 v[100:103], %[ea] offset:10240\n\t"
    "v_mfma_f32_16x16x32_bf16 v[88:91], v[136:139], v[108:111], v[88:91]\n\t"
    "v_mfma_f32_16x16x32_bf16 v[92:95], v[140:143], v[108:111], v[92:95]\n\t"
    "s_waitcnt lgkmcnt(4)\n\t"
    "v_mfma_f32_16x16x32_bf16 v[80:83], v[144:147], v[112:115], v[80:83]\n\t"
    "ds_read_b128 v[104:107], %[da] offset:8192\n\t"
    "v_mfma_f32_16x16x32_bf16 v[84:87], v[148:151], v[112:115], v[84:87]\n\t"
    "ds_read_b128 v[108:111], %[da] offset:10240\n\t"
    "v_mfma_f32_16x16x32_bf16 v[88:91], v[144:147], v[116:119], v[88:91]\n\t"
    "v_mfma_f32_16x16x32_bf16 v[92:95], v[148:151], v[116:119], v[92:95]\n\t"
    "s_waitcnt lgkmcnt(4)\n\t"
    "v_mfma_f32_16x16x32_bf16 v[80:83], v[152:155], v[120:123], v[80:83]\n\t"
    "ds_read_b128 v[112:115], %[ea] offset:12288\n\t"
    "v_mfma_f32_16x16x32_bf16 v[84:87], v[156:159], v[120:123], v[84:87]\n\t"
    "ds_read_b128 v[116:119], %[ea] offset:14336\n\t"
    "v_mfma_f32_16x16x32_bf16 v[88:91], v[152:155], v[124:127], v[88:91]\n\t"
    "v_mfma_f32_16x16x32_bf16 v[92:95], v[156:159], v[124:127], v[92:95]\n\t"
    "s_waitcnt lgkmcnt(4)\n\t"
    "v_mfma_f32_16x16x32_bf16 v[80:83], v[160:163], v[96:99], v[80:83]\n\t"
    "ds_read_b128 v[120:123], %[da] offset:12288\n\t"
    "v_mfma_f32_16x16x32_bf16 v[84:87], v[164:167], v[96:99], v[84:87]\n\t"
    "ds_read_b128 v[124:127], %[da] offset:14336\n\t"
    "v_mfma_f32_16x16x32_bf16 v[88:91], v[160:163], v[100:103], v[88:91]\n\t"
    "v_mfma_f32_16x16x32_bf16 v[92:95], v[164:167], v[100:103], v[92:95]\n\t"
    "s_waitcnt vmcnt(10)\n\t"
    "s_cmp_lt_u32 %[x], 2\n\t"
    "s_cbranch_scc0 45f\n\t"
    "s_waitcnt vmcnt(2)\n\t"
    "45:\n\t"
    "s_barrier\n\t"
    "s_waitcnt lgkmcnt(4)\n\t"
    "v_mfma_f32_16x16x32_bf16 v[80:83], v[168:171], v[104:107], v[80:83]\n\t"
    "ds_read_b128 v[96:99], %[ea] offset:16384\n\t"
    "v_mfma_f32_16x16x32_bf16 v[84:87], v[172:175], v[104:107], v[84:87]\n\t"
    "ds_read_b128 v[100:103], %[ea] offset:18432\n\t"
    "v_mfma_f32_16x16x32_bf16 v[88:91], v[168:171], v[108:111], v[88:91]\n\t"
    "v_mfma_f32_16x16x32_bf16 v[92:95], v[172:175], v[108:111], v[92:95]\n\t"
    "s_waitcnt lgkmcnt(4)\n\t"
    "v_mfma_f32_16x16x32_bf16 v[80:83], v[176:179], v[112:115], v[80:83]\n\t"
    "ds_read_b128 v[104:107], %[da] offset:16384\n\t"
    "v_mfma_f32_16x16x32_bf16 v[84:87], v[180:183], v[112:115], v[84:87]\n\t"
    "ds_read_b128 v[108:111], %[da] offset:18432\n\t"
    "v_mfma_f32_16x16x32_bf16 v[88:91], v[176:179], v[116:119], v[88:91]\n\t"
    "v_mfma_f32_16x16x32_bf16 v[92:95], v[180:183], v[116:119], v[92:95]\n\t"
    "s_waitcnt lgkmcnt(4)\n\t"
    "v_mfma_f32_16x16x32_bf16 v[80:83], v[184:187], v[120:123], v[80:83]\n\t"
    "ds_read_b128 v[112:115], %[ea] offset:20480\n\t"
    "v_mfma_f32_16x16x32_bf16 v[84:87], v[188:191], v[120:123], v[84:87]\n\t"
    "ds_read_b128 v[116:119], %[ea] offset:22528\n\t"
    "v_mfma_f32_16x16x32_bf16 v[88:91], v[184:187], v[124:127], v[88:91]\n\t"
    "v_mfma_f32_16x16x32_bf16 v[92:95], v[188:191], v[124:127], v[92:95]\n\t"
    "s_waitcnt lgkmcnt(4)\n\t"
    "v_mfma_f32_16x16x32_bf16 v[80:83], v[192:195], v[96:99], v[80:83]\n\t"
    "ds_read_b128 v[120:123], %[da] offset:20480\n\t"
    "v_mfma_f32_16x16x32_bf16 v[84:87], v[196:199], v[96:99], v[84:87]\n\t"
    "ds_read_b128 v[124:127], %[da] offset:22528\n\t"
    "v_mfma_f32_16x16x32_bf16 v[88:91], v[192:195], v[100:103], v[88:91]\n\t"
    "v_mfma_f32_16x16x32_bf16 v[92:95], v[196:199], v[100:103], v[92:95]\n\t"
    "s_waitcnt vmcnt(8)\n\t"
    "s_cmp_lt_u32 %[x], 2\n\t"
    "s_cbranch_scc0 49f\n\t"
    "s_waitcnt vmcnt(0)\n\t"
    "49:\n\t"
    "s_barrier\n\t"
    "ds_read_b32 v79, %[sa]\n\t"
    "s_waitcnt lgkmcnt(5)\n\t"
    "v_mfma_f32_16x16x32_bf16 v[80:83], v[200:203], v[104:107], v[80:83]\n\t"
    "ds_read_b128 v[96:99], %[ea] offset:24576\n\t"
    "v_mfma_f32_16x16x32_bf16 v[84:87], v[204:207], v[104:107], v[84:87]\n\t"
    "ds_read_b128 v[100:103], %[ea] offset:26624\n\t"
    "v_mfma_f32_16x16x32_bf16 v[88:91], v[200:203], v[108:111], v[88:91]\n\t"
    "v_mfma_f32_16x16x32_bf16 v[92:95], v[204:207], v[108:111], v[92:95]\n\t"
    "s_waitcnt lgkmcnt(5)\n\t"
    "v_mfma_f32_16x16x32_bf16 v[80:83], v[208:211], v[112:115], v[80:83]\n\t"
    "ds_read_b128 v[104:107], %[da] offset:24576\n\t"
    "v_mfma_f32_16x16x32_bf16 v[84:87], v[212:215], v[112:115], v[84:87]\n\t"
    "ds_read_b128 v[108:111], %[da] offset:26624\n\t"
    "v_mfma_f32_16x16x32_bf16 v[88:91], v[208:211], v[116:119], v[88:91]\n\t"
    "v_mfma_f32_16x16x32_bf16 v[92:95], v[212:215], v[116:119], v[92:95]\n\t"
    "s_cmp_eq_u32 %[s2], 0\n\t"
    "s_cbranch_scc1 7f\n\t"
    "s_mov_b32 m0, %[s2]\n\t"
    "s_nop 0\n\t"
    "global_load_lds_dword %[dv], %[db] sc1\n\t"
    "7:\n\t"
    "s_waitcnt lgkmcnt(4)\n\t"
    "v_mfma_f32_16x16x32_bf16 v[80:83], v[216:219], v[120:123], v[80:83]\n\t"
    "ds_read_b128 v[112:115], %[ea] offset:28672\n\t"
    "v_mfma_f32_16x16x32_bf16 v[84:87], v[220:223], v[120:123], v[84:87]\n\t"
    "ds_read_b128 v[116:119], %[ea] offset:30720\n\t"
    "v_mfma_f32_16x16x32_bf16 v[88:91], v[216:219], v[124:127], v[88:91]\n\t"
    "v_mad_u32_u24 v79, v79, %[mu], %[ad]\n\t"
    "v_cmp_ge_i32 vcc, %[th], v79\n\t"
    "v_mfma_f32_16x16x32_bf16 v[92:95], v[220:223], v[124:127], v[92:95]\n\t"
    "s_cbranch_vccz 5f\n\t"
    "s_waitcnt lgkmcnt(4)\n\t"
    "v_mfma_f32_16x16x32_bf16 v[80:83], v[224:227], v[96:99], v[80:83]\n\t"
    "ds_read_b128 v[120:123], %[da] offset:28672\n\t"
    "v_mfma_f32_16x16x32_bf16 v[84:87], v[228:231], v[96:99], v[84:87]\n\t"
    "ds_read_b128 v[124:127], %[da] offset:30720\n\t"
    "v_mfma_f32_16x16x32_bf16 v[88:91], v[224:227], v[100:103], v[88:91]\n\t"
    "v_mfma_f32_16x16x32_bf16 v[92:95], v[228:231], v[100:103], v[92:95]\n\t"
    "s_waitcnt lgkmcnt(4)\n\t"
    "v_mfma_f32_16x16x32_bf16 v[80:83], v[232:235], v[104:107], v[80:83]\n\t"
    "v_mfma_f32_16x16x32_bf16 v[84:87], v[236:239], v[104:107], v[84:87]\n\t"
    "v_mfma_f32_16x16x32_bf16 v[88:91], v[232:235], v[108:111], v[88:91]\n\t"
    "v_mfma_f32_16x16x32_bf16 v[92:95], v[236:239], v[108:111], v[92:95]\n\t"
    "s_waitcnt lgkmcnt(2)\n\t"
    "v_mfma_f32_16x16x32_bf16 v[80:83], v[240:243], v[112:115], v[80:83]\n\t"
    "v_mfma_f32_16x16x32_bf16 v[84:87], v[244:247], v[112:115], v[84:87]\n\t"
    "v_mfma_f32_16x16x32_bf16 v[88:91], v[240:243], v[116:119], v[88:91]\n\t"
    "v_mfma_f32_16x16x32_bf16 v[92:95], v[244:247], v[116:119], v[92:95]\n\t"
    "s_waitcnt lgkmcnt(0)\n\t"
    "v_mfma_f32_16x16x32_bf16 v[80:83], v[248:251], v[120:123], v[80:83]\n\t"
    "v_mfma_f32_16x16x32_bf16 v[84:87], v[252:255], v[120:123], v[84:87]\n\t"
    "v_mfma_f32_16x16x32_bf16 v[88:91], v[248:251], v[124:127], v[88:91]\n\t"
    "v_mfma_f32_16x16x32_bf16 v[92:95], v[252:255], v[124:127], v[92:95]\n\t"
    "s_branch 6f\n\t"
    "5:\n\t"
    "s_waitcnt lgkmcnt(4)\n\t"
    "v_mfma_f32_16x16x32_bf16 v[80:83], v[224:227], v[96:99], v[80:83]\n\t"
    "ds_read_b128 v[120:123], %[da] offset:28672\n\t"
    "s_add_u32 m0, %[lb], 0\n\t"
    "s_nop 0\n\t"
    "global_load_lds_dwordx4 %[av0], %[s0] sc1\n\t"
    "v_mfma_f32_16x16x32_bf16 v[84:87], v[228:231], v[96:99], v[84:87]\n\t"
    "ds_read_b128 v[124:127], %[da] offset:30720\n\t"
    "v_mfma_f32_16x16x32_bf16 v[88:91], v[224:227], v[100:103], v[88:91]\n\t"
    "s_add_u32 m0, %[lb], 4096\n\t"
    "s_nop 0\n\t"
    "global_load_lds_dwordx4 %[av2], %[s0] sc1\n\t"
    "v_mfma_f32_16x16x32_bf16 v[92:95], v[228:231], v[100:103], v[92:95]\n\t"
    "s_waitcnt lgkmcnt(4)\n\t"
    "v_mfma_f32_16x16x32_bf16 v[80:83], v[232:235], v[104:107], v[80:83]\n\t"
    "s_add_u32 m0, %[lb], 8192\n\t"
    "s_nop 0\n\t"
    "global_load_lds_dwordx4 %[av4], %[s0] sc1\n\t"
    "v_mfma_f32_16x16x32_bf16 v[84:87], v[236:239], v[104:107], v[84:87]\n\t"
    "v_mfma_f32_16x16x32_bf16 v[88:91], v[232:235], v[108:111], v[88:91]\n\t"
    "s_add_u32 m0, %[lb], 12288\n\t"
    "s_nop 0\n\t"
    "global_load_lds_dwordx4 %[av6], %[s0] sc1\n\t"
    "v_mfma_f32_16x16x32_bf16 v[92:95], v[236:239], v[108:111], v[92:95]\n\t"
    "s_waitcnt lgkmcnt(2)\n\t"
    "v_mfma_f32_16x16x32_bf16 v[80:83], v[240:243], v[112:115], v[80:83]\n\t"
    "s_add_u32 m0, %[lb], 16384\n\t"
    "s_nop 0\n\t"
    "global_load_lds_dwordx4 %[av0], %[s1] sc1\n\t"
    "v_mfma_f32_16x16x32_bf16 v[84:87], v[244:247], v[112:115], v[84:87]\n\t"
    "v_mfma_f32_16x16x32_bf16 v[88:91], v[240:243], v[116:119], v[88:91]\n\t"
    "s_add_u32 m0, %[lb], 20480\n\t"
    "s_nop 0\n\t"
    "global_load_lds_dwordx4 %[av2], %[s1] sc1\n\t"
    "v_mfma_f32_16x16x32_bf16 v[92:95], v[244:247], v[116:119], v[92:95]\n\t"
    "s_waitcnt lgkmcnt(0)\n\t"
    "v_mfma_f32_16x16x32_bf16 v[80:83], v[248:251], v[120:123], v[80:83]\n\t"
    "s_add_u32 m0, %[lb], 24576\n\t"
    "s_nop 0\n\t"
    "global_load_lds_dwordx4 %[av4], %[s1] sc1\n\t"
    "v_mfma_f32_16x16x32_bf16 v[84:87], v[252:255], v[120:123], v[84:87]\n\t"
    "v_mfma_f32_16x16x32_bf16 v[88:91], v[248:251], v[124:127], v[88:91]\n\t"
    "s_add_u32 m0, %[lb], 28672\n\t"
    "s_nop 0\n\t"
    "global_load_lds_dwordx4 %[av6], %[s1] sc1\n\t"
    "v_mfma_f32_16x16x32_bf16 v[92:95], v[252:255], v[124:127], v[92:95]\n\t"
    "6:\n\t"
    "s_nop 6\n\t"
    "s_branch 9f\n\t"
    "3:\n\t"
    "9:"
    : "=&{v[80:95]}"(pd)
    : [ea] "v"(ea), [da] "v"(da), [x] "s"(x), "{v[128:159]}"(W[0]), "{v[160:191]}"(W[1]), "{v[192:223]}"(W[2]), "{v[224:255]}"(W[3]), [lb] "s"(lb), [s0] "s"(s0), [s1] "s"(s1), [av0] "v"(av[0]), [av1] "v"(av[1]), [av2] "v"(av[2]), [av3] "v"(av[3]), [av4] "v"(av[4]), [av5] "v"(av[5]), [av6] "v"(av[6]), [av7] "v"(av[7]), [sa] "v"(sa), [mu] "s"(mu), [ad] "v"(ad), [th] "s"(th), [s2] "s"(s2), [dv] "v"(dv), [db] "s"(db)
    : "v96", "v97", "v98", "v99", "v100", "v101", "v102", "v103", "v104", "v105", "v106", "v107", "v108", "v109", "v110", "v111", "v112", "v113", "v114", "v115", "v116", "v117", "v118", "v119", "v120", "v121", "v122", "v123", "v124", "v125", "v126", "v127", "v79", "vcc", "scc", "m0", "memory");
}
__device__ __forceinline__ void lean_dm1(const u32x32 (&W)[4], u32x16& pd, unsigned ea, unsigned da, unsigned x, unsigned lb, const char* s0, const char* s1, const unsigned (&av)[8]) {
  asm volatile("s_setprio 2\n\t"
    "ds_read_b128 v[96:99], %[ea] offset:0\n\t"
    "ds_read_b128 v[100:103], %[ea] offset:2048\n\t"
    "ds_read_b128 v[104:107], %[da] offset:0\n\t"
    "ds_read_b128 v[108:111], %[da] offset:2048\n\t"
    "ds_read_b128 v[112:115], %[ea] offset:4096\n\t"
    "ds_read_b128 v[116:119], %[ea] offset:6144\n\t"
    "s_waitcnt lgkmcnt(4)\n\t"
    "v_mfma_f32_16x16x32_bf16 v[80:83], v[128:131], v[96:99], 0\n\t"
    "ds_read_b128 v[120:123], %[da] offset:4096\n\t"
    "s_add_u32 m0, %[lb], 0\n\t"
    "v_mfma_f32_16x16x32_bf16 v[84:87], v[132:135], v[96:99], 0\n\t"
    "ds_read_b128 v[124:127], %[da] offset:6144\n\t"
    "global_load_lds_dwordx4 %[av0], %[s0] sc1\n\t"
    "v_mfma_f32_16x16x32_bf16 v[88:91], v[128:131], v[100:103], 0\n\t"
    "v_mfma_f32_16x16x32_bf16 v[92:95], v[132:135], v[100:103], 0\n\t"
    "s_waitcnt vmcnt(5)\n\t"
    "s_barrier\n\t"
    "s_waitcnt lgkmcnt(4)\n\t"
    "v_mfma_f32_16x16x32_bf16 v[80:83], v[136:139], v[104:107], v[80:83]\n\t"
    "ds_read_b128 v[96:99], %[ea] offset:8192\n\t"
    "s_add_u32 m0, %[lb], 4096\n\t"
    "v_mfma_f32_16x16x32_bf16 v[84:87], v[140:143], v[104:107], v[84:87]\n\t"
    "ds_read_b128 v[100:103], %[ea] offset:10240\n\t"
    "global_load_lds_dwordx4 %[av2], %[s0] sc1\n\t"
    "v_mfma_f32_16x16x32_bf16 v[88:91], v[136:139], v[108:111], v[88:91]\n\t"
    "v_mfma_f32_16x16x32_bf16 v[92:95], v[140:143], v[108:111], v[92:95]\n\t"
    "s_waitcnt lgkmcnt(4)\n\t"
    "v_mfma_f32_16x16x32_bf16 v[80:83], v[144:147], v[112:115], v[80:83]\n\t"
    "ds_read_b128 v[104:107], %[da] offset:8192\n\t"
    "s_add_u32 m0, %[lb], 8192\n\t"
    "v_mfma_f32_16x16x32_bf16 v[84:87], v[148:151], v[112:115], v[84:87]\n\t"
    "ds_read_b128 v[108:111], %[da] offset:10240\n\t"
    "global_load_lds_dwordx4 %[av4], %[s0] sc1\n\t"
    "v_mfma_f32_16x16x32_bf16 v[88:91], v[144:147], v[116:119], v[88:91]\n\t"
    "v_mfma_f32_16x16x32_bf16 v[92:95], v[148:151], v[116:119], v[92:95]\n\t"
    "s_waitcnt lgkmcnt(4)\n\t"
    "v_mfma_f32_16x16x32_bf16 v[80:83], v[152:155], v[120:123], v[80:83]\n\t"
    "ds_read_b128 v[112:115], %[ea] offset:12288\n\t"
    "s_add_u32 m0, %[lb], 12288\n\t"
    "v_mfma_f32_16x16x32_bf16 v[84:87], v[156:159], v[120:123], v[84:87]\n\t"
    "ds_read_b128 v[116:119], %[ea] offset:14336\n\t"
    "global_load_lds_dwordx4 %[av6], %[s0] sc1\n\t"
    "v_mfma_f32_16x16x32_bf16 v[88:91], v[152:155], v[124:127], v[88:91]\n\t"
    "v_mfma_f32_16x16x32_bf16 v[92:95], v[156:159], v[124:127], v[92:95]\n\t"
    "s_waitcnt lgkmcnt(4)\n\t"
    "v_mfma_f32_16x16x32_bf16 v[80:83], v[160:163], v[96:99], v[80:83]\n\t"
    "ds_read_b128 v[120:123], %[da] offset:12288\n\t"
    "s_add_u32 m0, %[lb], 16384\n\t"
    "v_mfma_f32_16x16x32_bf16 v[84:87], v[164:167], v[96:99], v[84:87]\n\t"
    "ds_read_b128 v[124:127], %[da] offset:14336\n\t"
    "global_load_lds_dwordx4 %[av0], %[s1] sc1\n\t"
    "v_mfma_f32_16x16x32_bf16 v[88:91], v[160:163], v[100:103], v[88:91]\n\t"
    "v_mfma_f32_16x16x32_bf16 v[92:95], v[164:167], v[100:103], v[92:95]\n\t"
    "s_waitcnt vmcnt(7)\n\t"
    "s_barrier\n\t"
    "s_waitcnt lgkmcnt(4)\n\t"
    "v_mfma_f32_16x16x32_bf16 v[80:83], v[168:171], v[104:107], v[80:83]\n\t"
    "ds_read_b128 v[96:99], %[ea] offset:16384\n\t"
    "s_add_u32 m0, %[lb], 20480\n\t"
    "v_mfma_f32_16x16x32_bf16 v[84:87], v[172:175], v[104:107], v[84:87]\n\t"
    "ds_read_b128 v[100:103], %[ea] offset:18432\n\t"
    "global_load_lds_dwordx4 %[av2], %[s1] sc1\n\t"
    "v_mfma_f32_16x16x32_bf16 v[88:91], v[168:171], v[108:111], v[88:91]\n\t"
    "v_mfma_f32_16x16x32_bf16 v[92:95], v[172:175], v[108:111], v[92:95]\n\t"
    "s_waitcnt lgkmcnt(4)\n\t"
    "v_mfma_f32_16x16x32_bf16 v[80:83], v[176:179], v[112:115], v[80:83]\n\t"
    "ds_read_b128 v[104:107], %[da] offset:16384\n\t"
    "s_add_u32 m0, %[lb], 24576\n\t"
    "v_mfma_f32_16x16x32_bf16 v[84:87], v[180:183], v[112:115], v[84:87]\n\t"
    "ds_read_b128 v[108:111], %[da] offset:18432\n\t"
    "global_load_lds_dwordx4 %[av4], %[s1] sc1\n\t"
    "v_mfma_f32_16x16x32_bf16 v[88:91], v[176:179], v[116:119], v[88:91]\n\t"
    "v_mfma_f32_16x16x32_bf16 v[92:95], v[180:183], v[116:119], v[92:95]\n\t"
    "s_waitcnt lgkmcnt(4)\n\t"
    "v_mfma_f32_16x16x32_bf16 v[80:83], v[184:187], v[120:123], v[80:83]\n\t"
    "ds_read_b128 v[112:115], %[ea] offset:20480\n\t"
    "s_add_u32 m0, %[lb], 28672\n\t"
    "v_mfma_f32_16x16x32_bf16 v[84:87], v[188:191], v[120:123], v[84:87]\n\t"
    "ds_read_b128 v[116:119], %[ea] offset:22528\n\t"
    "global_load_lds_dwordx4 %[av6], %[s1] sc1\n\t"
    "v_mfma_f32_16x16x32_bf16 v[88:91], v[184:187], v[124:127], v[88:91]\n\t"
    "v_mfma_f32_16x16x32_bf16 v[92:95], v[188:191], v[124:127], v[92:95]\n\t"
    "s_waitcnt lgkmcnt(4)\n\t"
    "v_mfma_f32_16x16x32_bf16 v[80:83], v[192:195], v[96:99], v[80:83]\n\t"
    "ds_read_b128 v[120:123], %[da] offset:20480\n\t"
    "v_mfma_f32_16x16x32_bf16 v[84:87], v[196:199], v[96:99], v[84:87]\n\t"
    "ds_read_b128 v[124:127], %[da] offset:22528\n\t"
    "v_mfma_f32_16x16x32_bf16 v[88:91], v[192:195], v[100:103], v[88:91]\n\t"
    "v_mfma_f32_16x16x32_bf16 v[92:95], v[196:199], v[100:103], v[92:95]\n\t"
    "s_waitcnt vmcnt(8)\n\t"
    "s_barrier\n\t"
    "s_waitcnt lgkmcnt(4)\n\t"
    "v_mfma_f32_16x16x32_bf16 v[80:83], v[200:203], v[104:107], v[80:83]\n\t"
    "ds_read_b128 v[96:99], %[ea] offset:24576\n\t"
    "v_mfma_f32_16x16x32_bf16 v[84:87], v[204:207], v[104:107], v[84:87]\n\t"
    "ds_read_b128 v[100:103], %[ea] offset:26624\n\t"
    "v_mfma_f32_16x16x32_bf16 v[88:91], v[200:203], v[108:111], v[88:91]\n\t"
    "v_mfma_f32_16x16x32_bf16 v[92:95], v[204:207], v[108:111], v[92:95]\n\t"
    "s_waitcnt lgkmcnt(4)\n\t"
    "v_mfma_f32_16x16x32_bf16 v[80:83], v[208:211], v[112:115], v[80:83]\n\t"
    "ds_read_b128 v[104:107], %[da] offset:24576\n\t"
    "v_mfma_f32_16x16x32_bf16 v[84:87], v[212:215], v[112:115], v[84:87]\n\t"
    "ds_read_b128 v[108:111], %[da] offset:26624\n\t"
    "v_mfma_f32_16x16x32_bf16 v[88:91], v[208:211], v[116:119], v[88:91]\n\t"
    "v_mfma_f32_16x16x32_bf16 v[92:95], v[212:215], v[116:119], v[92:95]\n\t"
    "s_waitcnt lgkmcnt(4)\n\t"
    "v_mfma_f32_16x16x32_bf16 v[80:83], v[216:219], v[120:123], v[80:83]\n\t"
    "ds_read_b128 v[112:115], %[ea] offset:28672\n\t"
    "v_mfma_f32_16x16x32_bf16 v[84:87], v[220:223], v[120:123], v[84:87]\n\t"
    "ds_read_b128 v[116:119], %[ea] offset:30720\n\t"
    "v_mfma_f32_16x16x32_bf16 v[88:91], v[216:219], v[124:127], v[88:91]\n\t"
    "v_mfma_f32_16x16x32_bf16 v[92:95], v[220:223], v[124:127], v[92:95]\n\t"
    "s_waitcnt lgkmcnt(4)\n\t"
    "v_mfma_f32_16x16x32_bf16 v[80:83], v[224:227], v[96:99], v[80:83]\n\t"
    "ds_read_b128 v[120:123], %[da] offset:28672\n\t"
    "v_mfma_f32_16x16x32_bf16 v[84:87], v[228:231], v[96:99], v[84:87]\n\t"
    "ds_read_b128 v[124:127], %[da] offset:30720\n\t"
    "v_mfma_f32_16x16x32_bf16 v[88:91], v[224:227], v[100:103], v[88:91]\n\t"
    "v_mfma_f32_16x16x32_bf16 v[92:95], v[228:231], v[100:103], v[92:95]\n\t"
    "s_waitcnt lgkmcnt(4)\n\t"
    "v_mfma_f32_16x16x32_bf16 v[80:83], v[232:235], v[104:107], v[80:83]\n\t"
    "v_mfma_f32_16x16x32_bf16 v[84:87], v[236:239], v[104:107], v[84:87]\n\t"
    "v_mfma_f32_16x16x32_bf16 v[88:91], v[232:235], v[108:111], v[88:91]\n\t"
    "v_mfma_f32_16x16x32_bf16 v[92:95], v[236:239], v[108:111], v[92:95]\n\t"
    "s_waitcnt lgkmcnt(2)\n\t"
    "v_mfma_f32_16x16x32_bf16 v[80:83], v[240:243], v[112:115], v[80:83]\n\t"
    "v_mfma_f32_16x16x32_bf16 v[84:87], v[244:247], v[112:115], v[84:87]\n\t"
    "v_mfma_f32_16x16x32_bf16 v[88:91], v[240:243], v[116:119], v[88:91]\n\t"
    "v_mfma_f32_16x16x32_bf16 v[92:95], v[244:247], v[116:119], v[92:95]\n\t"
    "s_waitcnt lgkmcnt(0)\n\t"
    "v_mfma_f32_16x16x32_bf16 v[80:83], v[248:251], v[120:123], v[80:83]\n\t"
    "v_mfma_f32_16x16x32_bf16 v[84:87], v[252:255], v[120:123], v[84:87]\n\t"
    "v_mfma_f32_16x16x32_bf16 v[88:91], v[248:251], v[124:127], v[88:91]\n\t"
    "v_mfma_f32_16x16x32_bf16 v[92:95], v[252:255], v[124:127], v[92:95]\n\t"
    "s_nop 6\n\t"
    "s_branch 9f\n\t"
    "3:\n\t"
    "9:"
    : "=&{v[80:95]}"(pd)
    : [ea] "v"(ea), [da] "v"(da), [x] "s"(x), "{v[128:159]}"(W[0]), "{v[160:191]}"(W[1]), "{v[192:223]}"(W[2]), "{v[224:255]}"(W[3]), [lb] "s"(lb), [s0] "s"(s0), [s1] "s"(s1), [av0] "v"(av[0]), [av1] "v"(av[1]), [av2] "v"(av[2]), [av3] "v"(av[3]), [av4] "v"(av[4]), [av5] "v"(av[5]), [av6] "v"(av[6]), [av7] "v"(av[7])
    : "v96", "v97", "v98", "v99", "v100", "v101", "v102", "v103", "v104", "v105", "v106", "v107", "v108", "v109", "v110", "v111", "v112", "v113", "v114", "v115", "v116", "v117", "v118", "v119", "v120", "v121", "v122", "v123", "v124", "v125", "v126", "v127", "vcc", "scc", "m0", "memory");
}
__device__ __forceinline__ void lean_st0(const u32x32 (&W)[4], u32x16& pd, unsigned ea, unsigned da, unsigned x, unsigned pv, unsigned cn, unsigned sl, unsigned dv, const char* db, const char* pg) {
  unsigned st;
  asm volatile("ds_read_b128 v[96:99], %[ea] offset:0\n\t"
    "ds_read_b128 v[100:103], %[ea] offset:2048\n\t"
    "ds_read_b128 v[104:107], %[da] offset:0\n\t"
    "ds_read_b128 v[108:111], %[da] offset:2048\n\t"
    "ds_read_b128 v[112:115], %[ea] offset:4096\n\t"
    "ds_read_b128 v[116:119], %[ea] offset:6144\n\t"
    "s_waitcnt lgkmcnt(4)\n\t"
    "v_mfma_f32_16x16x32_bf16 v[80:83], v[128:131], v[96:99], 0\n\t"
    "ds_read_b128 v[120:123], %[da] offset:4096\n\t"
    "v_mfma_f32_16x16x32_bf16 v[84:87], v[132:135], v[96:99], 0\n\t"
    "ds_read_b128 v[124:127], %[da] offset:6144\n\t"
    "v_mfma_f32_16x16x32_bf16 v[88:91], v[128:131], v[100:103], 0\n\t"
    "v_mfma_f32_16x16x32_bf16 v[92:95], v[132:135], v[100:103], 0\n\t"
    "s_barrier\n\t"
    "s_waitcnt lgkmcnt(4)\n\t"
    "v_mfma_f32_16x16x32_bf16 v[80:83], v[136:139], v[104:107], v[80:83]\n\t"
    "ds_read_b128 v[96:99], %[ea] offset:8192\n\t"
    "v_mfma_f32_16x16x32_bf16 v[84:87], v[140:143], v[104:107], v[84:87]\n\t"
    "ds_read_b128 v[100:103], %[ea] offset:10240\n\t"
    "v_mfma_f32_16x16x32_bf16 v[88:91], v[136:139], v[108:111], v[88:91]\n\t"
    "v_mfma_f32_16x16x32_bf16 v[92:95], v[140:143], v[108:111], v[92:95]\n\t"
    "s_waitcnt lgkmcnt(4)\n\t"
    "v_mfma_f32_16x16x32_bf16 v[80:83], v[144:147], v[112:115], v[80:83]\n\t"
    "ds_read_b128 v[104:107], %[da] offset:8192\n\t"
    "v_mfma_f32_16x16x32_bf16 v[84:87], v[148:151], v[112:115], v[84:87]\n\t"
    "ds_read_b128 v[108:111], %[da] offset:10240\n\t"
    "v_mfma_f32_16x16x32_bf16 v[88:91], v[144:147], v[116:119], v[88:91]\n\t"
    "v_mfma_f32_16x16x32_bf16 v[92:95], v[148:151], v[116:119], v[92:95]\n\t"
    "s_waitcnt lgkmcnt(4)\n\t"
    "v_mfma_f32_16x16x32_bf16 v[80:83], v[152:155], v[120:123], v[80:83]\n\t"
    "ds_read_b128 v[112:115], %[ea] offset:12288\n\t"
    "v_mfma_f32_16x16x32_bf16 v[84:87], v[156:159], v[120:123], v[84:87]\n\t"
    "ds_read_b128 v[116:119], %[ea] offset:14336\n\t"
    "v_mfma_f32_16x16x32_bf16 v[88:91], v[152:155], v[124:127], v[88:91]\n\t"
    "v_mfma_f32_16x16x32_bf16 v[92:95], v[156:159], v[124:127], v[92:95]\n\t"
    "s_bitcmp1_b32 %[x], 2\n\t"
    "s_cbranch_scc1 7f\n\t"
    "8:\n\t"
    "s_waitcnt lgkmcnt(4)\n\t"
    "v_mfma_f32_16x16x32_bf16 v[80:83], v[160:163], v[96:99], v[80:83]\n\t"
    "ds_read_b128 v[120:123], %[da] offset:12288\n\t"
    "v_mfma_f32_16x16x32_bf16 v[84:87], v[164:167], v[96:99], v[84:87]\n\t"
    "ds_read_b128 v[124:127], %[da] offset:14336\n\t"
    "v_mfma_f32_16x16x32_bf16 v[88:91], v[160:163], v[100:103], v[88:91]\n\t"
    "v_mfma_f32_16x16x32_bf16 v[92:95], v[164:167], v[100:103], v[92:95]\n\t"
    "s_barrier\n\t"
    "s_waitcnt lgkmcnt(4)\n\t"
    "v_mfma_f32_16x16x32_bf16 v[80:83], v[168:171], v[104:107], v[80:83]\n\t"
    "ds_read_b128 v[96:99], %[ea] offset:16384\n\t"
    "v_mfma_f32_16x16x32_bf16 v[84:87], v[172:175], v[104:107], v[84:87]\n\t"
    "ds_read_b128 v[100:103], %[ea] offset:18432\n\t"
    "v_mfma_f32_16x16x32_bf16 v[88:91], v[168:171], v[108:111], v[88:91]\n\t"
    "v_mfma_f32_16x16x32_bf16 v[92:95], v[172:175], v[108:111], v[92:95]\n\t"
    "s_waitcnt lgkmcnt(4)\n\t"
    "v_mfma_f32_16x16x32_bf16 v[80:83], v[176:179], v[112:115], v[80:83]\n\t"
    "ds_read_b128 v[104:107], %[da] offset:16384\n\t"
    "v_mfma_f32_16x16x32_bf16 v[84:87], v[180:183], v[112:115], v[84:87]\n\t"
    "ds_read_b128 v[108:111], %[da] offset:18432\n\t"
    "v_mfma_f32_16x16x32_bf16 v[88:91], v[176:179], v[116:119], v[88:91]\n\t"
    "v_mfma_f32_16x16x32_bf16 v[92:95], v[180:183], v[116:119], v[92:95]\n\t"
    "s_waitcnt lgkmcnt(4)\n\t"
    "v_mfma_f32_16x16x32_bf16 v[80:83], v[184:187], v[120:123], v[80:83]\n\t"
    "ds_read_b128 v[112:115], %[ea] offset:20480\n\t"
    "v_mfma_f32_16x16x32_bf16 v[84:87], v[188:191], v[120:123], v[84:87]\n\t"
    "ds_read_b128 v[116:119], %[ea] offset:22528\n\t"
    "v_mfma_f32_16x16x32_bf16 v[88:91], v[184:187], v[124:127], v[88:91]\n\t"
    "v_mfma_f32_16x16x32_bf16 v[92:95], v[188:191], v[124:127], v[92:95]\n\t"
    "s_waitcnt lgkmcnt(4)\n\t"
    "v_mfma_f32_16x16x32_bf16 v[80:83], v[192:195], v[96:99], v[80:83]\n\t"
    "ds_read_b128 v[120:123], %[da] offset:20480\n\t"
    "v_mfma_f32_16x16x32_bf16 v[84:87], v[196:199], v[96:99], v[84:87]\n\t"
    "ds_read_b128 v[124:127], %[da] offset:22528\n\t"
    "v_mfma_f32_16x16x32_bf16 v[88:91], v[192:195], v[100:103], v[88:91]\n\t"
    "v_mfma_f32_16x16x32_bf16 v[92:95], v[196:199], v[100:103], v[92:95]\n\t"
    "s_barrier\n\t"
    "s_and_b32 %[st], %[x], 3\n\t"
    "s_cbranch_scc1 3f\n\t"
    "4:\n\t"
    "s_waitcnt lgkmcnt(4)\n\t"
    "v_mfma_f32_16x16x32_bf16 v[80:83], v[200:203], v[104:107], v[80:83]\n\t"
    "ds_read_b128 v[96:99], %[ea] offset:24576\n\t"
    "v_mfma_f32_16x16x32_bf16 v[84:87], v[204:207], v[104:107], v[84:87]\n\t"
    "ds_read_b128 v[100:103], %[ea] offset:26624\n\t"
    "v_mfma_f32_16x16x32_bf16 v[88:91], v[200:203], v[108:111], v[88:91]\n\t"
    "v_mfma_f32_16x16x32_bf16 v[92:95], v[204:207], v[108:111], v[92:95]\n\t"
    "s_waitcnt lgkmcnt(4)\n\t"
    "v_mfma_f32_16x16x32_bf16 v[80:83], v[208:211], v[112:115], v[80:83]\n\t"
    "ds_read_b128 v[104:107], %[da] offset:24576\n\t"
    "v_mfma_f32_16x16x32_bf16 v[84:87], v[212:215], v[112:115], v[84:87]\n\t"
    "ds_read_b128 v[108:111], %[da] offset:26624\n\t"
    "v_mfma_f32_16x16x32_bf16 v[88:91], v[208:211], v[116:119], v[88:91]\n\t"
    "v_mfma_f32_16x16x32_bf16 v[92:95], v[212:215], v[116:119], v[92:95]\n\t"
    "s_waitcnt lgkmcnt(4)\n\t"
    "v_mfma_f32_16x16x32_bf16 v[80:83], v[216:219], v[120:123], v[80:83]\n\t"
    "ds_read_b128 v[112:115], %[ea] offset:28672\n\t"
    "v_mfma_f32_16x16x32_bf16 v[84:87], v[220:223], v[120:123], v[84:87]\n\t"
    "ds_read_b128 v[116:119], %[ea] offset:30720\n\t"
    "v_mfma_f32_16x16x32_bf16 v[88:91], v[216:219], v[124:127], v[88:91]\n\t"
    "v_mfma_f32_16x16x32_bf16 v[92:95], v[220:223], v[124:127], v[92:95]\n\t"
    "s_waitcnt lgkmcnt(4)\n\t"
    "v_mfma_f32_16x16x32_bf16 v[80:83], v[224:227], v[96:99], v[80:83]\n\t"
    "ds_read_b128 v[120:123], %[da] offset:28672\n\t"
    "v_mfma_f32_16x16x32_bf16 v[84:87], v[228:231], v[96:99], v[84:87]\n\t"
    "ds_read_b128 v[124:127], %[da] offset:30720\n\t"
    "v_mfma_f32_16x16x32_bf16 v[88:91], v[224:227], v[100:103], v[88:91]\n\t"
    "v_mfma_f32_16x16x32_bf16 v[92:95], v[228:231], v[100:103], v[92:95]\n\t"
    "s_waitcnt lgkmcnt(4)\n\t"
    "v_mfma_f32_16x16x32_bf16 v[80:83], v[232:235], v[104:107], v[80:83]\n\t"
    "v_mfma_f32_16x16x32_bf16 v[84:87], v[236:239], v[104:107], v[84:87]\n\t"
    "v_mfma_f32_16x16x32_bf16 v[88:91], v[232:235], v[108:111], v[88:91]\n\t"
    "v_mfma_f32_16x16x32_bf16 v[92:95], v[236:239], v[108:111], v[92:95]\n\t"
    "s_waitcnt lgkmcnt(2)\n\t"
    "v_mfma_f32_16x16x32_bf16 v[80:83], v[240:243], v[112:115], v[80:83]\n\t"
    "v_mfma_f32_16x16x32_bf16 v[84:87], v[244:247], v[112:115], v[84:87]\n\t"
    "v_mfma_f32_16x16x32_bf16 v[88:91], v[240:243], v[116:119], v[88:91]\n\t"
    "v_mfma_f32_16x16x32_bf16 v[92:95], v[244:247], v[116:119], v[92:95]\n\t"
    "s_waitcnt lgkmcnt(0)\n\t"
    "v_mfma_f32_16x16x32_bf16 v[80:83], v[248:251], v[120:123], v[80:83]\n\t"
    "v_mfma_f32_16x16x32_bf16 v[84:87], v[252:255], v[120:123], v[84:87]\n\t"
    "v_mfma_f32_16x16x32_bf16 v[88:91], v[248:251], v[124:127], v[88:91]\n\t"
    "v_mfma_f32_16x16x32_bf16 v[92:95], v[252:255], v[124:127], v[92:95]\n\t"
    "s_nop 6\n\t"
    "s_branch 9f\n\t"
    "3:\n\t"
    "s_bitcmp0_b32 %[x], 0\n\t"
    "s_cbranch_scc1 5f\n\t"
    "7:\n\t"
    "v_mov_b32 v76, %[cn]\n\t"
    "v_mov_b32 v77, 1\n\t"
    "v_mov_b32 v78, %[pv]\n\t"
    "v_mov_b32 v79, 0\n\t"
    "s_waitcnt vmcnt(0)\n\t"
    "s_mov_b64 exec, 1\n\t"
    "ds_add_rtn_u32 v77, v76, v77\n\t"
    "s_waitcnt lgkmcnt(0)\n\t"
    "v_readfirstlane_b32 %[st], v77\n\t"
    "s_and_b32 %[st], %[st], 3\n\t"
    "s_cmp_lg_u32 %[st], 3\n\t"
    "s_cbranch_scc1 6f\n\t"
    "global_store_dword v79, v78, %[pg] sc1\n\t"
    "6:\n\t"
    "s_mov_b64 exec, -1\n\t"
    "s_bitcmp1_b32 %[x], 2\n\t"
    "s_cbranch_scc1 8b\n\t"
    "5:\n\t"
    "s_bitcmp0_b32 %[x], 1\n\t"
    "s_cbranch_scc1 4b\n\t"
    "s_mov_b32 m0, %[sl]\n\t"
    "s_nop 0\n\t"
    "global_load_lds_dword %[dv], %[db] sc1\n\t"
    "s_branch 4b\n\t"
    "9:"
    : "=&{v[80:95]}"(pd), [st] "=&s"(st)
    : [ea] "v"(ea), [da] "v"(da), [x] "s"(x), "{v[128:159]}"(W[0]), "{v[160:191]}"(W[1]), "{v[192:223]}"(W[2]), "{v[224:255]}"(W[3]), [pv] "s"(pv), [cn] "s"(cn), [sl] "s"(sl), [dv] "v"(dv), [db] "s"(db), [pg] "s"(pg)
    : "v96", "v97", "v98", "v99", "v100", "v101", "v102", "v103", "v104", "v105", "v106", "v107", "v108", "v109", "v110", "v111", "v112", "v113", "v114", "v115", "v116", "v117", "v118", "v119", "v120", "v121", "v122", "v123", "v124", "v125", "v126", "v127", "v76", "v77", "v78", "v79", "vcc", "scc", "m0", "memory");
}
#define LEAN_QB 1
__device__ __forceinline__ void paced_ld0(const u32x32 (&W)[4], unsigned ea, unsigned da, int b, int ns, unsigned ab, unsigned rw, unsigned fa,
    const char* p0, const char* p1, unsigned lb, int& cap, int wm, unsigned rq, unsigned mu, unsigned ad, unsigned dv, const char* db, unsigned sl, unsigned sa, const unsigned (&av)[8]) {
  unsigned st;
  asm volatile("s_setprio 2\n\t"
    "v_mov_b32 v78, %[wm]\n\t"
    "s_cmp_lg_u32 %[rq], 0\n\t"
    "s_cbranch_scc1 3f\n\t"
    "2:\n\t"
    "v_cmp_ge_i32 vcc, %[b], v78\n\t"
    "s_cbranch_vccz 4f\n\t"
    "21:\n\t"
    "s_cmp_eq_u32 %[sl], 0\n\t"
    "s_cbranch_scc1 71f\n\t"
    "s_mov_b32 m0, %[sl]\n\t"
    "s_nop 0\n\t"
    "global_load_lds_dword %[dv], %[db] sc1\n\t"
    "71:\n\t"
    "s_sub_u32 %[cap], %[cap], 1\n\t"
    "s_max_i32 %[cap], %[cap], 0\n\t"
    "s_waitcnt vmcnt(0)\n\t"
    "ds_read_b32 v79, %[sa]\n\t"
    "s_waitcnt lgkmcnt(0)\n\t"
    "v_mad_u32_u24 v79, v79, %[mu], %[ad]\n\t"
    "v_max_i32 v78, v78, v79\n\t"
    "v_cmp_ge_i32 vcc, %[b], v78\n\t"
    "s_cbranch_vccz 4f\n\t"
    "s_sleep 1\n\t"
    "s_cmp_lg_u32 %[cap], 0\n\t"
    "s_cbranch_scc1 21b\n\t"
    "4:\n\t"
    "s_add_u32 m0, %[lb], 0\n\t"
    "s_nop 0\n\t"
    "global_load_lds_dwordx4 %[av0], s[36:37] sc1\n\t"
    "s_add_u32 m0, %[lb], 32768\n\t"
    "s_nop 0\n\t"
    "global_load_lds_dwordx4 %[av1], s[36:37] sc1\n\t"
    "s_add_u32 m0, %[lb], 4096\n\t"
    "s_nop 0\n\t"
    "global_load_lds_dwordx4 %[av2], s[36:37] sc1\n\t"
    "s_add_u32 m0, %[lb], 36864\n\t"
    "s_nop 0\n\t"
    "global_load_lds_dwordx4 %[av3], s[36:37] sc1\n\t"
    "s_add_u32 m0, %[lb], 8192\n\t"
    "s_nop 0\n\t"
    "global_load_lds_dwordx4 %[av4], s[36:37] sc1\n\t"
    "s_add_u32 m0, %[lb], 40960\n\t"
    "s_nop 0\n\t"
    "global_load_lds_dwordx4 %[av5], s[36:37] sc1\n\t"
    "s_add_u32 m0, %[lb], 12288\n\t"
    "s_nop 0\n\t"
    "global_load_lds_dwordx4 %[av6], s[36:37] sc1\n\t"
    "s_add_u32 m0, %[lb], 45056\n\t"
    "s_nop 0\n\t"
    "global_load_lds_dwordx4 %[av7], s[36:37] sc1\n\t"
    "s_add_u32 m0, %[lb], 16384\n\t"
    "s_nop 0\n\t"
    "global_load_lds_dwordx4 %[av0], s[38:39] sc1\n\t"
    "s_add_u32 m0, %[lb], 49152\n\t"
    "s_nop 0\n\t"
    "global_load_lds_dwordx4 %[av1], s[38:39] sc1\n\t"
    "s_add_u32 m0, %[lb], 20480\n\t"
    "s_nop 0\n\t"
    "global_load_lds_dwordx4 %[av2], s[38:39] sc1\n\t"
    "s_add_u32 m0, %[lb], 53248\n\t"
    "s_nop 0\n\t"
    "global_load_lds_dwordx4 %[av3], s[38:39] sc1\n\t"
    "s_add_u32 m0, %[lb], 24576\n\t"
    "s_nop 0\n\t"
    "global_load_lds_dwordx4 %[av4], s[38:39] sc1\n\t"
    "s_add_u32 m0, %[lb], 57344\n\t"
    "s_nop 0\n\t"
    "global_load_lds_dwordx4 %[av5], s[38:39] sc1\n\t"
    "s_add_u32 m0, %[lb], 28672\n\t"
    "s_nop 0\n\t"
    "global_load_lds_dwordx4 %[av6], s[38:39] sc1\n\t"
    "s_add_u32 m0, %[lb], 61440\n\t"
    "s_nop 0\n\t"
    "global_load_lds_dwordx4 %[av7], s[38:39] sc1\n\t"
    "s_add_u32 s36, s36, %[ab]\n\t"
    "s_addc_u32 s37, s37, 0\n\t"
    "s_add_u32 s38, s38, %[ab]\n\t"
    "s_addc_u32 s39, s39, 0\n\t"
    "s_xor_b32 %[lb], %[lb], 0x10000\n\t"
    "3:\n\t"
    "s_add_u32 %[st], %[b], 1\n\t"
    "v_min_i32 v78, %[ns], v78\n\t"
    "v_cmp_ge_i32 vcc, %[st], v78\n\t"
    "s_waitcnt vmcnt(12) lgkmcnt(0)\n\t"
    "s_barrier\n\t"
    "ds_read_b128 v[96:99], %[ea] offset:0\n\t"
    "ds_read_b128 v[100:103], %[ea] offset:2048\n\t"
    "ds_read_b128 v[104:107], %[da] offset:0\n\t"
    "ds_read_b128 v[108:111], %[da] offset:2048\n\t"
    "ds_read_b128 v[112:115], %[ea] offset:4096\n\t"
    "ds_read_b128 v[116:119], %[ea] offset:6144\n\t"
    "s_cbranch_vccz 5f\n\t"
    "s_waitcnt lgkmcnt(4)\n\t"
    "v_mfma_f32_16x16x32_bf16 v[80:83], v[128:131], v[96:99], 0\n\t"
    "ds_read_b128 v[120:123], %[da] offset:4096\n\t"
    "v_mfma_f32_16x16x32_bf16 v[84:87], v[132:135], v[96:99], 0\n\t"
    "ds_read_b128 v[124:127], %[da] offset:6144\n\t"
    "v_mfma_f32_16x16x32_bf16 v[88:91], v[128:131], v[100:103], 0\n\t"
    "v_mfma_f32_16x16x32_bf16 v[92:95], v[132:135], v[100:103], 0\n\t"
    "s_waitcnt vmcnt(8)\n\t"
    "s_barrier\n\t"
    "s_waitcnt lgkmcnt(4)\n\t"
    "v_mfma_f32_16x16x32_bf16 v[80:83], v[136:139], v[104:107], v[80:83]\n\t"
    "ds_read_b128 v[96:99], %[ea] offset:8192\n\t"
    "v_mfma_f32_16x16x32_bf16 v[84:87], v[140:143], v[104:107], v[84:87]\n\t"
    "ds_read_b128 v[100:103], %[ea] offset:10240\n\t"
    "v_mfma_f32_16x16x32_bf16 v[88:91], v[136:139], v[108:111], v[88:91]\n\t"
    "v_mfma_f32_16x16x32_bf16 v[92:95], v[140:143], v[108:111], v[92:95]\n\t"
    "s_waitcnt lgkmcnt(4)\n\t"
    "v_mfma_f32_16x16x32_bf16 v[80:83], v[144:147], v[112:115], v[80:83]\n\t"
    "ds_read_b128 v[104:107], %[da] offset:8192\n\t"
    "v_mfma_f32_16x16x32_bf16 v[84:87], v[148:151], v[112:115], v[84:87]\n\t"
    "ds_read_b128 v[108:111], %[da] offset:10240\n\t"
    "v_mfma_f32_16x16x32_bf16 v[88:91], v[144:147], v[116:119], v[88:91]\n\t"
    "v_mfma_f32_16x16x32_bf16 v[92:95], v[148:151], v[116:119], v[92:95]\n\t"
    "s_waitcnt lgkmcnt(4)\n\t"
    "v_mfma_f32_16x16x32_bf16 v[80:83], v[152:155], v[120:123], v[80:83]\n\t"
    "ds_read_b128 v[112:115], %[ea] offset:12288\n\t"
    "v_mfma_f32_16x16x32_bf16 v[84:87], v[156:159], v[120:123], v[84:87]\n\t"
    "ds_read_b128 v[116:119], %[ea] offset:14336\n\t"
    "v_mfma_f32_16x16x32_bf16 v[88:91], v[152:155], v[124:127], v[88:91]\n\t"
    "v_mfma_f32_16x16x32_bf16 v[92:95], v[156:159], v[124:127], v[92:95]\n\t"
    "s_waitcnt lgkmcnt(4)\n\t"
    "v_mfma_f32_16x16x32_bf16 v[80:83], v[160:163], v[96:99], v[80:83]\n\t"
    "ds_read_b128 v[120:123], %[da] offset:12288\n\t"
    "v_mfma_f32_16x16x32_bf16 v[84:87], v[164:167], v[96:99], v[84:87]\n\t"
    "ds_read_b128 v[124:127], %[da] offset:14336\n\t"
    "v_mfma_f32_16x16x32_bf16 v[88:91], v[160:163], v[100:103], v[88:91]\n\t"
    "v_mfma_f32_16x16x32_bf16 v[92:95], v[164:167], v[100:103], v[92:95]\n\t"
    "s_waitcnt vmcnt(4)\n\t"
    "s_barrier\n\t"
    "s_waitcnt lgkmcnt(4)\n\t"
    "v_mfma_f32_16x16x32_bf16 v[80:83], v[168:171], v[104:107], v[80:83]\n\t"
    "ds_read_b128 v[96:99], %[ea] offset:16384\n\t"
    "v_mfma_f32_16x16x32_bf16 v[84:87], v[172:175], v[104:107], v[84:87]\n\t"
    "ds_read_b128 v[100:103], %[ea] offset:18432\n\t"
    "v_mfma_f32_16x16x32_bf16 v[88:91], v[168:171], v[108:111], v[88:91]\n\t"
    "v_mfma_f32_16x16x32_bf16 v[92:95], v[172:175], v[108:111], v[92:95]\n\t"
    "s_waitcnt lgkmcnt(4)\n\t"
    "v_mfma_f32_16x16x32_bf16 v[80:83], v[176:179], v[112:115], v[80:83]\n\t"
    "ds_read_b128 v[104:107], %[da] offset:16384\n\t"
    "v_mfma_f32_16x16x32_bf16 v[84:87], v[180:183], v[112:115], v[84:87]\n\t"
    "ds_read_b128 v[108:111], %[da] offset:18432\n\t"
    "v_mfma_f32_16x16x32_bf16 v[88:91], v[176:179], v[116:119], v[88:91]\n\t"
    "v_mfma_f32_16x16x32_bf16 v[92:95], v[180:183], v[116:119], v[92:95]\n\t"
    "s_waitcnt lgkmcnt(4)\n\t"
    "v_mfma_f32_16x16x32_bf16 v[80:83], v[184:187], v[120:123], v[80:83]\n\t"
    "ds_read_b128 v[112:115], %[ea] offset:20480\n\t"
    "v_mfma_f32_16x16x32_bf16 v[84:87], v[188:191], v[120:123], v[84:87]\n\t"
    "ds_read_b128 v[116:119], %[ea] offset:22528\n\t"
    "v_mfma_f32_16x16x32_bf16 v[88:91], v[184:187], v[124:127], v[88:91]\n\t"
    "v_mfma_f32_16x16x32_bf16 v[92:95], v[188:191], v[124:127], v[92:95]\n\t"
    "s_waitcnt lgkmcnt(4)\n\t"
    "v_mfma_f32_16x16x32_bf16 v[80:83], v[192:195], v[96:99], v[80:83]\n\t"
    "ds_read_b128 v[120:123], %[da] offset:20480\n\t"
    "v_mfma_f32_16x16x32_bf16 v[84:87], v[196:199], v[96:99], v[84:87]\n\t"
    "ds_read_b128 v[124:127], %[da] offset:22528\n\t"
    "v_mfma_f32_16x16x32_bf16 v[88:91], v[192:195], v[100:103], v[88:91]\n\t"
    "v_mfma_f32_16x16x32_bf16 v[92:95], v[196:199], v[100:103], v[92:95]\n\t"
    "s_waitcnt vmcnt(0)\n\t"
    "s_barrier\n\t"
    "s_waitcnt lgkmcnt(4)\n\t"
    "v_mfma_f32_16x16x32_bf16 v[80:83], v[200:203], v[104:107], v[80:83]\n\t"
    "ds_read_b128 v[96:99], %[ea] offset:24576\n\t"
    "v_mfma_f32_16x16x32_bf16 v[84:87], v[204:207], v[104:107], v[84:87]\n\t"
    "ds_read_b128 v[100:103], %[ea] offset:26624\n\t"
    "v_mfma_f32_16x16x32_bf16 v[88:91], v[200:203], v[108:111], v[88:91]\n\t"
    "v_mfma_f32_16x16x32_bf16 v[92:95], v[204:207], v[108:111], v[92:95]\n\t"
    "s_waitcnt lgkmcnt(4)\n\t"
    "v_mfma_f32_16x16x32_bf16 v[80:83], v[208:211], v[112:115], v[80:83]\n\t"
    "ds_read_b128 v[104:107], %[da] offset:24576\n\t"
    "v_mfma_f32_16x16x32_bf16 v[84:87], v[212:215], v[112:115], v[84:87]\n\t"
    "ds_read_b128 v[108:111], %[da] offset:26624\n\t"
    "v_mfma_f32_16x16x32_bf16 v[88:91], v[208:211], v[116:119], v[88:91]\n\t"
    "v_mfma_f32_16x16x32_bf16 v[92:95], v[212:215], v[116:119], v[92:95]\n\t"
    "s_cmp_eq_u32 %[sl], 0\n\t"
    "s_cbranch_scc1 72f\n\t"
    "s_mov_b32 m0, %[sl]\n\t"
    "s_nop 0\n\t"
    "global_load_lds_dword %[dv], %[db] sc1\n\t"
    "72:\n\t"
    "s_waitcnt lgkmcnt(4)\n\t"
    "v_mfma_f32_16x16x32_bf16 v[80:83], v[216:219], v[120:123], v[80:83]\n\t"
    "ds_read_b128 v[112:115], %[ea] offset:28672\n\t"
    "v_mfma_f32_16x16x32_bf16 v[84:87], v[220:223], v[120:123], v[84:87]\n\t"
    "ds_read_b128 v[116:119], %[ea] offset:30720\n\t"
    "v_mfma_f32_16x16x32_bf16 v[88:91], v[216:219], v[124:127], v[88:91]\n\t"
    "v_mfma_f32_16x16x32_bf16 v[92:95], v[220:223], v[124:127], v[92:95]\n\t"
    "s_waitcnt lgkmcnt(4)\n\t"
    "v_mfma_f32_16x16x32_bf16 v[80:83], v[224:227], v[96:99], v[80:83]\n\t"
    "ds_read_b128 v[120:123], %[da] offset:28672\n\t"
    "v_mfma_f32_16x16x32_bf16 v[84:87], v[228:231], v[96:99], v[84:87]\n\t"
    "ds_read_b128 v[124:127], %[da] offset:30720\n\t"
    "v_mfma_f32_16x16x32_bf16 v[88:91], v[224:227], v[100:103], v[88:91]\n\t"
    "v_mfma_f32_16x16x32_bf16 v[92:95], v[228:231], v[100:103], v[92:95]\n\t"
    "s_waitcnt lgkmcnt(4)\n\t"
    "v_mfma_f32_16x16x32_bf16 v[80:83], v[232:235], v[104:107], v[80:83]\n\t"
    "v_mfma_f32_16x16x32_bf16 v[84:87], v[236:239], v[104:107], v[84:87]\n\t"
    "v_mfma_f32_16x16x32_bf16 v[88:91], v[232:235], v[108:111], v[88:91]\n\t"
    "v_xor_b32 %[ea], 0x10000, %[ea]\n\t"
    "v_mfma_f32_16x16x32_bf16 v[92:95], v[236:239], v[108:111], v[92:95]\n\t"
    "v_xor_b32 %[da], 0x10000, %[da]\n\t"
    "s_waitcnt lgkmcnt(2)\n\t"
    "v_mfma_f32_16x16x32_bf16 v[80:83], v[240:243], v[112:115], v[80:83]\n\t"
    "v_mfma_f32_16x16x32_bf16 v[84:87], v[244:247], v[112:115], v[84:87]\n\t"
    "v_mfma_f32_16x16x32_bf16 v[88:91], v[240:243], v[116:119], v[88:91]\n\t"
    "s_add_u32 %[b], %[b], 1\n\t"
    "v_mfma_f32_16x16x32_bf16 v[92:95], v[244:247], v[116:119], v[92:95]\n\t"
    "s_waitcnt lgkmcnt(0)\n\t"
    "v_mfma_f32_16x16x32_bf16 v[80:83], v[248:251], v[120:123], v[80:83]\n\t"
    "v_mfma_f32_16x16x32_bf16 v[84:87], v[252:255], v[120:123], v[84:87]\n\t"
    "v_mfma_f32_16x16x32_bf16 v[88:91], v[248:251], v[124:127], v[88:91]\n\t"
    "v_mfma_f32_16x16x32_bf16 v[92:95], v[252:255], v[124:127], v[92:95]\n\t"
    "s_nop 4\n\t"
    "ds_write_b128 %[rw], v[80:83] offset:0\n\t"
    "ds_write_b128 %[rw], v[84:87] offset:1024\n\t"
    "ds_write_b128 %[rw], v[88:91] offset:2048\n\t"
    "ds_write_b128 %[rw], v[92:95] offset:3072\n\t"
    "s_waitcnt vmcnt(0)\n\t"
    "ds_read_b32 v79, %[sa]\n\t"
    "s_waitcnt lgkmcnt(0)\n\t"
    "v_mad_u32_u24 v79, v79, %[mu], %[ad]\n\t"
    "v_max_i32 v78, v78, v79\n\t"
    "v_mov_b32 v77, %[b]\n\t"
    "v_cmp_ge_i32 vcc, %[b], v78\n\t"
    "s_cmp_ge_i32 %[b], %[ns]\n\t"
    "s_cbranch_scc1 6f\n\t"
    "s_cbranch_vccnz 6f\n\t"
    "v_or_b32 v77, 0x100, v77\n\t"
    "ds_write_b32 %[fa], v77\n\t"
    "s_branch 4b\n\t"
    "6:\n\t"
    "ds_write_b32 %[fa], v77\n\t"
    "s_cbranch_scc0 21b\n\t"
    "s_branch 9f\n\t"
    "5:\n\t"
    "s_waitcnt lgkmcnt(4)\n\t"
    "v_mfma_f32_16x16x32_bf16 v[80:83], v[128:131], v[96:99], 0\n\t"
    "ds_read_b128 v[120:123], %[da] offset:4096\n\t"
    "s_add_u32 m0, %[lb], 0\n\t"
    "v_mfma_f32_16x16x32_bf16 v[84:87], v[132:135], v[96:99], 0\n\t"
    "ds_read_b128 v[124:127], %[da] offset:6144\n\t"
    "global_load_lds_dwordx4 %[av0], s[36:37] sc1\n\t"
    "v_mfma_f32_16x16x32_bf16 v[88:91], v[128:131], v[100:103], 0\n\t"
    "s_add_u32 m0, %[lb], 32768\n\t"
    "v_mfma_f32_16x16x32_bf16 v[92:95], v[132:135], v[100:103], 0\n\t"
    "global_load_lds_dwordx4 %[av1], s[36:37] sc1\n\t"
    "s_waitcnt vmcnt(10)\n\t"
    "s_barrier\n\t"
    "s_waitcnt lgkmcnt(4)\n\t"
    "v_mfma_f32_16x16x32_bf16 v[80:83], v[136:139], v[104:107], v[80:83]\n\t"
    "ds_read_b128 v[96:99], %[ea] offset:8192\n\t"
    "s_add_u32 m0, %[lb], 4096\n\t"
    "v_mfma_f32_16x16x32_bf16 v[84:87], v[140:143], v[104:107], v[84:87]\n\t"
    "ds_read_b128 v[100:103], %[ea] offset:10240\n\t"
    "global_load_lds_dwordx4 %[av2], s[36:37] sc1\n\t"
    "v_mfma_f32_16x16x32_bf16 v[88:91], v[136:139], v[108:111], v[88:91]\n\t"
    "s_add_u32 m0, %[lb], 36864\n\t"
    "v_mfma_f32_16x16x32_bf16 v[92:95], v[140:143], v[108:111], v[92:95]\n\t"
    "global_load_lds_dwordx4 %[av3], s[36:37] sc1\n\t"
    "s_waitcnt lgkmcnt(4)\n\t"
    "v_mfma_f32_16x16x32_bf16 v[80:83], v[144:147], v[112:115], v[80:83]\n\t"
    "ds_read_b128 v[104:107], %[da] offset:8192\n\t"
    "s_add_u32 m0, %[lb], 8192\n\t"
    "v_mfma_f32_16x16x32_bf16 v[84:87], v[148:151], v[112:115], v[84:87]\n\t"
    "ds_read_b128 v[108:111], %[da] offset:10240\n\t"
    "global_load_lds_dwordx4 %[av4], s[36:37] sc1\n\t"
    "v_mfma_f32_16x16x32_bf16 v[88:91], v[144:147], v[116:119], v[88:91]\n\t"
    "s_add_u32 m0, %[lb], 40960\n\t"
    "v_mfma_f32_16x16x32_bf16 v[92:95], v[148:151], v[116:119], v[92:95]\n\t"
    "global_load_lds_dwordx4 %[av5], s[36:37] sc1\n\t"
    "s_waitcnt lgkmcnt(4)\n\t"
    "v_mfma_f32_16x16x32_bf16 v[80:83], v[152:155], v[120:123], v[80:83]\n\t"
    "ds_read_b128 v[112:115], %[ea] offset:12288\n\t"
    "s_add_u32 m0, %[lb], 12288\n\t"
    "v_mfma_f32_16x16x32_bf16 v[84:87], v[156:159], v[120:123], v[84:87]\n\t"
    "ds_read_b128 v[116:119], %[ea] offset:14336\n\t"
    "global_load_lds_dwordx4 %[av6], s[36:37] sc1\n\t"
    "v_mfma_f32_16x16x32_bf16 v[88:91], v[152:155], v[124:127], v[88:91]\n\t"
    "s_add_u32 m0, %[lb], 45056\n\t"
    "v_mfma_f32_16x16x32_bf16 v[92:95], v[156:159], v[124:127], v[92:95]\n\t"
    "global_load_lds_dwordx4 %[av7], s[36:37] sc1\n\t"
    "s_waitcnt lgkmcnt(4)\n\t"
    "v_mfma_f32_16x16x32_bf16 v[80:83], v[160:163], v[96:99], v[80:83]\n\t"
    "ds_read_b128 v[120:123], %[da] offset:12288\n\t"
    "s_add_u32 m0, %[lb], 16384\n\t"
    "v_mfma_f32_16x16x32_bf16 v[84:87], v[164:167], v[96:99], v[84:87]\n\t"
    "ds_read_b128 v[124:127], %[da] offset:14336\n\t"
    "global_load_lds_dwordx4 %[av0], s[38:39] sc1\n\t"
    "v_mfma_f32_16x16x32_bf16 v[88:91], v[160:163], v[100:103], v[88:91]\n\t"
    "s_add_u32 m0, %[lb], 49152\n\t"
    "v_mfma_f32_16x16x32_bf16 v[92:95], v[164:167], v[100:103], v[92:95]\n\t"
    "global_load_lds_dwordx4 %[av1], s[38:39] sc1\n\t"
    "s_waitcnt vmcnt(14)\n\t"
    "s_barrier\n\t"
    "s_waitcnt lgkmcnt(4)\n\t"
    "v_mfma_f32_16x16x32_bf16 v[80:83], v[168:171], v[104:107], v[80:83]\n\t"
    "ds_read_b128 v[96:99], %[ea] offset:16384\n\t"
    "s_add_u32 m0, %[lb], 20480\n\t"
    "v_mfma_f32_16x16x32_bf16 v[84:87], v[172:175], v[104:107], v[84:87]\n\t"
    "ds_read_b128 v[100:103], %[ea] offset:18432\n\t"
    "global_load_lds_dwordx4 %[av2], s[38:39] sc1\n\t"
    "v_mfma_f32_16x16x32_bf16 v[88:91], v[168:171], v[108:111], v[88:91]\n\t"
    "s_add_u32 m0, %[lb], 53248\n\t"
    "v_mfma_f32_16x16x32_bf16 v[92:95], v[172:175], v[108:111], v[92:95]\n\t"
    "global_load_lds_dwordx4 %[av3], s[38:39] sc1\n\t"
    "s_waitcnt lgkmcnt(4)\n\t"
    "v_mfma_f32_16x16x32_bf16 v[80:83], v[176:179], v[112:115], v[80:83]\n\t"
    "ds_read_b128 v[104:107], %[da] offset:16384\n\t"
    "s_add_u32 m0, %[lb], 24576\n\t"
    "v_mfma_f32_16x16x32_bf16 v[84:87], v[180:183], v[112:115], v[84:87]\n\t"
    "ds_read_b128 v[108:111], %[da] offset:18432\n\t"
    "global_load_lds_dwordx4 %[av4], s[38:39] sc1\n\t"
    "v_mfma_f32_16x16x32_bf16 v[88:91], v[176:179], v[116:119], v[88:91]\n\t"
    "s_add_u32 m0, %[lb], 57344\n\t"
    "v_mfma_f32_16x16x32_bf16 v[92:95], v[180:183], v[116:119], v[92:95]\n\t"
    "global_load_lds_dwordx4 %[av5], s[38:39] sc1\n\t"
    "s_waitcnt lgkmcnt(4)\n\t"
    "v_mfma_f32_16x16x32_bf16 v[80:83], v[184:187], v[120:123], v[80:83]\n\t"
    "ds_read_b128 v[112:115], %[ea] offset:20480\n\t"
    "s_add_u32 m0, %[lb], 28672\n\t"
    "v_mfma_f32_16x16x32_bf16 v[84:87], v[188:191], v[120:123], v[84:87]\n\t"
    "ds_read_b128 v[116:119], %[ea] offset:22528\n\t"
    "global_load_lds_dwordx4 %[av6], s[38:39] sc1\n\t"
    "v_mfma_f32_16x16x32_bf16 v[88:91], v[184:187], v[124:127], v[88:91]\n\t"
    "s_add_u32 m0, %[lb], 61440\n\t"
    "v_mfma_f32_16x16x32_bf16 v[92:95], v[188:191], v[124:127], v[92:95]\n\t"
    "global_load_lds_dwordx4 %[av7], s[38:39] sc1\n\t"
    "s_waitcnt lgkmcnt(4)\n\t"
    "v_mfma_f32_16x16x32_bf16 v[80:83], v[192:195], v[96:99], v[80:83]\n\t"
    "ds_read_b128 v[120:123], %[da] offset:20480\n\t"
    "v_mfma_f32_16x16x32_bf16 v[84:87], v[196:199], v[96:99], v[84:87]\n\t"
    "ds_read_b128 v[124:127], %[da] offset:22528\n\t"
    "v_mfma_f32_16x16x32_bf16 v[88:91], v[192:195], v[100:103], v[88:91]\n\t"
    "v_mfma_f32_16x16x32_bf16 v[92:95], v[196:199], v[100:103], v[92:95]\n\t"
    "s_add_u32 s36, s36, %[ab]\n\t"
    "s_addc_u32 s37, s37, 0\n\t"
    "s_add_u32 s38, s38, %[ab]\n\t"
    "s_addc_u32 s39, s39, 0\n\t"
    "s_xor_b32 %[lb], %[lb], 0x10000\n\t"
    "s_waitcnt vmcnt(16)\n\t"
    "s_barrier\n\t"
    "s_waitcnt lgkmcnt(4)\n\t"
    "v_mfma_f32_16x16x32_bf16 v[80:83], v[200:203], v[104:107], v[80:83]\n\t"
    "ds_read_b128 v[96:99], %[ea] offset:24576\n\t"
    "v_mfma_f32_16x16x32_bf16 v[84:87], v[204:207], v[104:107], v[84:87]\n\t"
    "ds_read_b128 v[100:103], %[ea] offset:26624\n\t"
    "v_mfma_f32_16x16x32_bf16 v[88:91], v[200:203], v[108:111], v[88:91]\n\t"
    "v_mfma_f32_16x16x32_bf16 v[92:95], v[204:207], v[108:111], v[92:95]\n\t"
    "s_waitcnt lgkmcnt(4)\n\t"
    "v_mfma_f32_16x16x32_bf16 v[80:83], v[208:211], v[112:115], v[80:83]\n\t"
    "ds_read_b128 v[104:107], %[da] offset:24576\n\t"
    "v_mfma_f32_16x16x32_bf16 v[84:87], v[212:215], v[112:115], v[84:87]\n\t"
    "ds_read_b128 v[108:111], %[da] offset:26624\n\t"
    "v_mfma_f32_16x16x32_bf16 v[88:91], v[208:211], v[116:119], v[88:91]\n\t"
    "v_mfma_f32_16x16x32_bf16 v[92:95], v[212:215], v[116:119], v[92:95]\n\t"
    "s_cmp_eq_u32 %[sl], 0\n\t"
    "s_cbranch_scc1 73f\n\t"
    "s_mov_b32 m0, %[sl]\n\t"
    "s_nop 0\n\t"
    "global_load_lds_dword %[dv], %[db] sc1\n\t"
    "73:\n\t"
    "s_waitcnt lgkmcnt(4)\n\t"
    "v_mfma_f32_16x16x32_bf16 v[80:83], v[216:219], v[120:123], v[80:83]\n\t"
    "ds_read_b128 v[112:115], %[ea] offset:28672\n\t"
    "v_mfma_f32_16x16x32_bf16 v[84:87], v[220:223], v[120:123], v[84:87]\n\t"
    "ds_read_b128 v[116:119], %[ea] offset:30720\n\t"
    "v_mfma_f32_16x16x32_bf16 v[88:91], v[216:219], v[124:127], v[88:91]\n\t"
    "v_mfma_f32_16x16x32_bf16 v[92:95], v[220:223], v[124:127], v[92:95]\n\t"
    "s_waitcnt lgkmcnt(4)\n\t"
    "v_mfma_f32_16x16x32_bf16 v[80:83], v[224:227], v[96:99], v[80:83]\n\t"
    "ds_read_b128 v[120:123], %[da] offset:28672\n\t"
    "v_mfma_f32_16x16x32_bf16 v[84:87], v[228:231], v[96:99], v[84:87]\n\t"
    "ds_read_b128 v[124:127], %[da] offset:30720\n\t"
    "v_mfma_f32_16x16x32_bf16 v[88:91], v[224:227], v[100:103], v[88:91]\n\t"
    "v_mfma_f32_16x16x32_bf16 v[92:95], v[228:231], v[100:103], v[92:95]\n\t"
    "s_waitcnt lgkmcnt(4)\n\t"
    "v_mfma_f32_16x16x32_bf16 v[80:83], v[232:235], v[104:107], v[80:83]\n\t"
    "v_mfma_f32_16x16x32_bf16 v[84:87], v[236:239], v[104:107], v[84:87]\n\t"
    "v_mfma_f32_16x16x32_bf16 v[88:91], v[232:235], v[108:111], v[88:91]\n\t"
    "v_xor_b32 %[ea], 0x10000, %[ea]\n\t"
    "v_mfma_f32_16x16x32_bf16 v[92:95], v[236:239], v[108:111], v[92:95]\n\t"
    "v_xor_b32 %[da], 0x10000, %[da]\n\t"
    "s_waitcnt lgkmcnt(2)\n\t"
    "v_mfma_f32_16x16x32_bf16 v[80:83], v[240:243], v[112:115], v[80:83]\n\t"
    "v_mfma_f32_16x16x32_bf16 v[84:87], v[244:247], v[112:115], v[84:87]\n\t"
    "v_mfma_f32_16x16x32_bf16 v[88:91], v[240:243], v[116:119], v[88:91]\n\t"
    "s_add_u32 %[b], %[b], 1\n\t"
    "v_mfma_f32_16x16x32_bf16 v[92:95], v[244:247], v[116:119], v[92:95]\n\t"
    "s_waitcnt lgkmcnt(0)\n\t"
    "v_mfma_f32_16x16x32_bf16 v[80:83], v[248:251], v[120:123], v[80:83]\n\t"
    "v_mfma_f32_16x16x32_bf16 v[84:87], v[252:255], v[120:123], v[84:87]\n\t"
    "v_mfma_f32_16x16x32_bf16 v[88:91], v[248:251], v[124:127], v[88:91]\n\t"
    "v_mfma_f32_16x16x32_bf16 v[92:95], v[252:255], v[124:127], v[92:95]\n\t"
    "v_mov_b32 v77, %[b]\n\t"
    "v_or_b32 v77, 0x100, v77\n\t"
    "s_nop 2\n\t"
    "ds_write_b128 %[rw], v[80:83] offset:0\n\t"
    "ds_write_b128 %[rw], v[84:87] offset:1024\n\t"
    "ds_write_b128 %[rw], v[88:91] offset:2048\n\t"
    "ds_write_b128 %[rw], v[92:95] offset:3072\n\t"
    "ds_write_b32 %[fa], v77\n\t"
    "s_waitcnt vmcnt(0)\n\t"
    "ds_read_b32 v79, %[sa]\n\t"
    "s_waitcnt lgkmcnt(0)\n\t"
    "v_mad_u32_u24 v79, v79, %[mu], %[ad]\n\t"
    "v_max_i32 v78, v78, v79\n\t"
    "s_branch 3b\n\t"
    "9:\n\t"
    "s_waitcnt lgkmcnt(0)"
    : [ea] "+&v"(ea), [da] "+&v"(da), [b] "+&s"(b), [st] "=&s"(st), "+{s[36:37]}"(p0), "+{s[38:39]}"(p1), [lb] "+&s"(lb), [cap] "+&s"(cap)
    : [ns] "s"(ns), [ab] "s"(ab), [rw] "v"(rw), [fa] "v"(fa), "{v[128:159]}"(W[0]), "{v[160:191]}"(W[1]), "{v[192:223]}"(W[2]), "{v[224:255]}"(W[3]), [wm] "s"(wm), [rq] "s"(rq), [mu] "s"(mu), [ad] "v"(ad), [dv] "v"(dv), [db] "s"(db), [sl] "s"(sl), [sa] "v"(sa), [av0] "v"(av[0]), [av1] "v"(av[1]), [av2] "v"(av[2]), [av3] "v"(av[3]), [av4] "v"(av[4]), [av5] "v"(av[5]), [av6] "v"(av[6]), [av7] "v"(av[7])
    : "v77", "v78", "v79", "v80", "v81", "v82", "v83", "v84", "v85", "v86", "v87", "v88", "v89", "v90", "v91", "v92", "v93", "v94", "v95", "v96", "v97", "v98", "v99", "v100", "v101", "v102", "v103", "v104", "v105", "v106", "v107", "v108", "v109", "v110", "v111", "v112", "v113", "v114", "v115", "v116", "v117", "v118", "v119", "v120", "v121", "v122", "v123", "v124", "v125", "v126", "v127", "vcc", "scc", "m0", "memory");
}
__device__ __forceinline__ void paced_ld2(const u32x32 (&W)[4], unsigned ea, unsigned da, int b, int ns, unsigned ab, unsigned rw, unsigned fa,
    const char* p0, const char* p1, unsigned lb, int& cap, int wm, unsigned rq, unsigned mu, unsigned ad, unsigned dv, const char* db, unsigned sl, unsigned sa, const unsigned (&av)[8]) {
  unsigned st;
  asm volatile("s_setprio 2\n\t"
    "v_mov_b32 v78, %[wm]\n\t"
    "s_cmp_lg_u32 %[rq], 0\n\t"
    "s_cbranch_scc1 3f\n\t"
    "2:\n\t"
    "v_cmp_ge_i32 vcc, %[b], v78\n\t"
    "s_cbranch_vccz 4f\n\t"
    "21:\n\t"
    "s_cmp_eq_u32 %[sl], 0\n\t"
    "s_cbranch_scc1 71f\n\t"
    "s_mov_b32 m0, %[sl]\n\t"
    "s_nop 0\n\t"
    "global_load_lds_dword %[dv], %[db] sc1\n\t"
    "71:\n\t"
    "s_sub_u32 %[cap], %[cap], 1\n\t"
    "s_max_i32 %[cap], %[cap], 0\n\t"
    "s_waitcnt vmcnt(0)\n\t"
    "ds_read_b32 v79, %[sa]\n\t"
    "s_waitcnt lgkmcnt(0)\n\t"
    "v_mad_u32_u24 v79, v79, %[mu], %[ad]\n\t"
    "v_max_i32 v78, v78, v79\n\t"
    "v_cmp_ge_i32 vcc, %[b], v78\n\t"
    "s_cbranch_vccz 4f\n\t"
    "s_sleep 1\n\t"
    "s_cmp_lg_u32 %[cap], 0\n\t"
    "s_cbranch_scc1 21b\n\t"
    "4:\n\t"
    "s_add_u32 m0, %[lb], 0\n\t"
    "s_nop 0\n\t"
    "global_load_lds_dwordx4 %[av0], s[36:37] sc1\n\t"
    "s_add_u32 m0, %[lb], 4096\n\t"
    "s_nop 0\n\t"
    "global_load_lds_dwordx4 %[av2], s[36:37] sc1\n\t"
    "s_add_u32 m0, %[lb], 8192\n\t"
    "s_nop 0\n\t"
    "global_load_lds_dwordx4 %[av4], s[36:37] sc1\n\t"
    "s_add_u32 m0, %[lb], 12288\n\t"
    "s_nop 0\n\t"
    "global_load_lds_dwordx4 %[av6], s[36:37] sc1\n\t"
    "s_add_u32 m0, %[lb], 16384\n\t"
    "s_nop 0\n\t"
    "global_load_lds_dwordx4 %[av0], s[38:39] sc1\n\t"
    "s_add_u32 m0, %[lb], 20480\n\t"
    "s_nop 0\n\t"
    "global_load_lds_dwordx4 %[av2], s[38:39] sc1\n\t"
    "s_add_u32 m0, %[lb], 24576\n\t"
    "s_nop 0\n\t"
    "global_load_lds_dwordx4 %[av4], s[38:39] sc1\n\t"
    "s_add_u32 m0, %[lb], 28672\n\t"
    "s_nop 0\n\t"
    "global_load_lds_dwordx4 %[av6], s[38:39] sc1\n\t"
    "s_add_u32 s36, s36, %[ab]\n\t"
    "s_addc_u32 s37, s37, 0\n\t"
    "s_add_u32 s38, s38, %[ab]\n\t"
    "s_addc_u32 s39, s39, 0\n\t"
    "s_xor_b32 %[lb], %[lb], 0x10000\n\t"
    "3:\n\t"
    "s_add_u32 %[st], %[b], 1\n\t"
    "v_min_i32 v78, %[ns], v78\n\t"
    "v_cmp_ge_i32 vcc, %[st], v78\n\t"
    "s_waitcnt vmcnt(6) lgkmcnt(0)\n\t"
    "s_barrier\n\t"
    "ds_read_b128 v[96:99], %[ea] offset:0\n\t"
    "ds_read_b128 v[100:103], %[ea] offset:2048\n\t"
    "ds_read_b128 v[104:107], %[da] offset:0\n\t"
    "ds_read_b128 v[108:111], %[da] offset:2048\n\t"
    "ds_read_b128 v[112:115], %[ea] offset:4096\n\t"
    "ds_read_b128 v[116:119], %[ea] offset:6144\n\t"
    "s_cbranch_vccz 5f\n\t"
    "s_waitcnt lgkmcnt(4)\n\t"
    "v_mfma_f32_16x16x32_bf16 v[80:83], v[128:131], v[96:99], 0\n\t"
    "ds_read_b128 v[120:123], %[da] offset:4096\n\t"
    "v_mfma_f32_16x16x32_bf16 v[84:87], v[132:135], v[96:99], 0\n\t"
    "ds_read_b128 v[124:127], %[da] offset:6144\n\t"
    "v_mfma_f32_16x16x32_bf16 v[88:91], v[128:131], v[100:103], 0\n\t"
    "v_mfma_f32_16x16x32_bf16 v[92:95], v[132:135], v[100:103], 0\n\t"
    "s_waitcnt vmcnt(4)\n\t"
    "s_barrier\n\t"
    "s_waitcnt lgkmcnt(4)\n\t"
    "v_mfma_f32_16x16x32_bf16 v[80:83], v[136:139], v[104:107], v[80:83]\n\t"
    "ds_read_b128 v[96:99], %[ea] offset:8192\n\t"
    "v_mfma_f32_16x16x32_bf16 v[84:87], v[140:143], v[104:107], v[84:87]\n\t"
    "ds_read_b128 v[100:103], %[ea] offset:10240\n\t"
    "v_mfma_f32_16x16x32_bf16 v[88:91], v[136:139], v[108:111], v[88:91]\n\t"
    "v_mfma_f32_16x16x32_bf16 v[92:95], v[140:143], v[108:111], v[92:95]\n\t"
    "s_waitcnt lgkmcnt(4)\n\t"
    "v_mfma_f32_16x16x32_bf16 v[80:83], v[144:147], v[112:115], v[80:83]\n\t"
    "ds_read_b128 v[104:107], %[da] offset:8192\n\t"
    "v_mfma_f32_16x16x32_bf16 v[84:87], v[148:151], v[112:115], v[84:87]\n\t"
    "ds_read_b128 v[108:111], %[da] offset:10240\n\t"
    "v_mfma_f32_16x16x32_bf16 v[88:91], v[144:147], v[116:119], v[88:91]\n\t"
    "v_mfma_f32_16x16x32_bf16 v[92:95], v[148:151], v[116:119], v[92:95]\n\t"
    "s_waitcnt lgkmcnt(4)\n\t"
    "v_mfma_f32_16x16x32_bf16 v[80:83], v[152:155], v[120:123], v[80:83]\n\t"
    "ds_read_b128 v[112:115], %[ea] offset:12288\n\t"
    "v_mfma_f32_16x16x32_bf16 v[84:87], v[156:159], v[120:123], v[84:87]\n\t"
    "ds_read_b128 v[116:119], %[ea] offset:14336\n\t"
    "v_mfma_f32_16x16x32_bf16 v[88:91], v[152:155], v[124:127], v[88:91]\n\t"
    "v_mfma_f32_16x16x32_bf16 v[92:95], v[156:159], v[124:127], v[92:95]\n\t"
    "s_waitcnt lgkmcnt(4)\n\t"
    "v_mfma_f32_16x16x32_bf16 v[80:83], v[160:163], v[96:99], v[80:83]\n\t"
    "ds_read_b128 v[120:123], %[da] offset:12288\n\t"
    "v_mfma_f32_16x16x32_bf16 v[84:87], v[164:167], v[96:99], v[84:87]\n\t"
    "ds_read_b128 v[124:127], %[da] offset:14336\n\t"
    "v_mfma_f32_16x16x32_bf16 v[88:91], v[160:163], v[100:103], v[88:91]\n\t"
    "v_mfma_f32_16x16x32_bf16 v[92:95], v[164:167], v[100:103], v[92:95]\n\t"
    "s_waitcnt vmcnt(2)\n\t"
    "s_barrier\n\t"
    "s_waitcnt lgkmcnt(4)\n\t"
    "v_mfma_f32_16x16x32_bf16 v[80:83], v[168:171], v[104:107], v[80:83]\n\t"
    "ds_read_b128 v[96:99], %[ea] offset:16384\n\t"
    "v_mfma_f32_16x16x32_bf16 v[84:87], v[172:175], v[104:107], v[84:87]\n\t"
    "ds_read_b128 v[100:103], %[ea] offset:18432\n\t"
    "v_mfma_f32_16x16x32_bf16 v[88:91], v[168:171], v[108:111], v[88:91]\n\t"
    "v_mfma_f32_16x16x32_bf16 v[92:95], v[172:175], v[108:111], v[92:95]\n\t"
    "s_waitcnt lgkmcnt(4)\n\t"
    "v_mfma_f32_16x16x32_bf16 v[80:83], v[176:179], v[112:115], v[80:83]\n\t"
    "ds_read_b128 v[104:107], %[da] offset:16384\n\t"
    "v_mfma_f32_16x16x32_bf16 v[84:87], v[180:183], v[112:115], v[84:87]\n\t"
    "ds_read_b128 v[108:111], %[da] offset:18432\n\t"
    "v_mfma_f32_16x16x32_bf16 v[88:91], v[176:179], v[116:119], v[88:91]\n\t"
    "v_mfma_f32_16x16x32_bf16 v[92:95], v[180:183], v[116:119], v[92:95]\n\t"
    "s_waitcnt lgkmcnt(4)\n\t"
    "v_mfma_f32_16x16x32_bf16 v[80:83], v[184:187], v[120:123], v[80:83]\n\t"
    "ds_read_b128 v[112:115], %[ea] offset:20480\n\t"
    "v_mfma_f32_16x16x32_bf16 v[84:87], v[188:191], v[120:123], v[84:87]\n\t"
    "ds_read_b128 v[116:119], %[ea] offset:22528\n\t"
    "v_mfma_f32_16x16x32_bf16 v[88:91], v[184:187], v[124:127], v[88:91]\n\t"
    "v_mfma_f32_16x16x32_bf16 v[92:95], v[188:191], v[124:127], v[92:95]\n\t"
    "s_waitcnt lgkmcnt(4)\n\t"
    "v_mfma_f32_16x16x32_bf16 v[80:83], v[192:195], v[96:99], v[80:83]\n\t"
    "ds_read_b128 v[120:123], %[da] offset:20480\n\t"
    "v_mfma_f32_16x16x32_bf16 v[84:87], v[196:199], v[96:99], v[84:87]\n\t"
    "ds_read_b128 v[124:127], %[da] offset:22528\n\t"
    "v_mfma_f32_16x16x32_bf16 v[88:91], v[192:195], v[100:103], v[88:91]\n\t"
    "v_mfma_f32_16x16x32_bf16 v[92:95], v[196:199], v[100:103], v[92:95]\n\t"
    "s_waitcnt vmcnt(0)\n\t"
    "s_barrier\n\t"
    "s_waitcnt lgkmcnt(4)\n\t"
    "v_mfma_f32_16x16x32_bf16 v[80:83], v[200:203], v[104:107], v[80:83]\n\t"
    "ds_read_b128 v[96:99], %[ea] offset:24576\n\t"
    "v_mfma_f32_16x16x32_bf16 v[84:87], v[204:207], v[104:107], v[84:87]\n\t"
    "ds_read_b128 v[100:103], %[ea] offset:26624\n\t"
    "v_mfma_f32_16x16x32_bf16 v[88:91], v[200:203], v[108:111], v[88:91]\n\t"
    "v_mfma_f32_16x16x32_bf16 v[92:95], v[204:207], v[108:111], v[92:95]\n\t"
    "s_waitcnt lgkmcnt(4)\n\t"
    "v_mfma_f32_16x16x32_bf16 v[80:83], v[208:211], v[112:115], v[80:83]\n\t"
    "ds_read_b128 v[104:107], %[da] offset:24576\n\t"
    "v_mfma_f32_16x16x32_bf16 v[84:87], v[212:215], v[112:115], v[84:87]\n\t"
    "ds_read_b128 v[108:111], %[da] offset:26624\n\t"
    "v_mfma_f32_16x16x32_bf16 v[88:91], v[208:211], v[116:119], v[88:91]\n\t"
    "v_mfma_f32_16x16x32_bf16 v[92:95], v[212:215], v[116:119], v[92:95]\n\t"
    "s_cmp_eq_u32 %[sl], 0\n\t"
    "s_cbranch_scc1 72f\n\t"
    "s_mov_b32 m0, %[sl]\n\t"
    "s_nop 0\n\t"
    "global_load_lds_dword %[dv], %[db] sc1\n\t"
    "72:\n\t"
    "s_waitcnt lgkmcnt(4)\n\t"
    "v_mfma_f32_16x16x32_bf16 v[80:83], v[216:219], v[120:123], v[80:83]\n\t"
    "ds_read_b128 v[112:115], %[ea] offset:28672\n\t"
    "v_mfma_f32_16x16x32_bf16 v[84:87], v[220:223], v[120:123], v[84:87]\n\t"
    "ds_read_b128 v[116:119], %[ea] offset:30720\n\t"
    "v_mfma_f32_16x16x32_bf16 v[88:91], v[216:219], v[124:127], v[88:91]\n\t"
    "v_mfma_f32_16x16x32_bf16 v[92:95], v[220:223], v[124:127], v[92:95]\n\t"
    "s_waitcnt lgkmcnt(4)\n\t"
    "v_mfma_f32_16x16x32_bf16 v[80:83], v[224:227], v[96:99], v[80:83]\n\t"
    "ds_read_b128 v[120:123], %[da] offset:28672\n\t"
    "v_mfma_f32_16x16x32_bf16 v[84:87], v[228:231], v[96:99], v[84:87]\n\t"
    "ds_read_b128 v[124:127], %[da] offset:30720\n\t"
    "v_mfma_f32_16x16x32_bf16 v[88:91], v[224:227], v[100:103], v[88:91]\n\t"
    "v_mfma_f32_16x16x32_bf16 v[92:95], v[228:231], v[100:103], v[92:95]\n\t"
    "s_waitcnt lgkmcnt(4)\n\t"
    "v_mfma_f32_16x16x32_bf16 v[80:83], v[232:235], v[104:107], v[80:83]\n\t"
    "v_mfma_f32_16x16x32_bf16 v[84:87], v[236:239], v[104:107], v[84:87]\n\t"
    "v_mfma_f32_16x16x32_bf16 v[88:91], v[232:235], v[108:111], v[88:91]\n\t"
    "v_xor_b32 %[ea], 0x10000, %[ea]\n\t"
    "v_mfma_f32_16x16x32_bf16 v[92:95], v[236:239], v[108:111], v[92:95]\n\t"
    "v_xor_b32 %[da], 0x10000, %[da]\n\t"
    "s_waitcnt lgkmcnt(2)\n\t"
    "v_mfma_f32_16x16x32_bf16 v[80:83], v[240:243], v[112:115], v[80:83]\n\t"
    "v_mfma_f32_16x16x32_bf16 v[84:87], v[244:247], v[112:115], v[84:87]\n\t"
    "v_mfma_f32_16x16x32_bf16 v[88:91], v[240:243], v[116:119], v[88:91]\n\t"
    "s_add_u32 %[b], %[b], 1\n\t"
    "v_mfma_f32_16x16x32_bf16 v[92:95], v[244:247], v[116:119], v[92:95]\n\t"
    "s_waitcnt lgkmcnt(0)\n\t"
    "v_mfma_f32_16x16x32_bf16 v[80:83], v[248:251], v[120:123], v[80:83]\n\t"
    "v_mfma_f32_16x16x32_bf16 v[84:87], v[252:255], v[120:123], v[84:87]\n\t"
    "v_mfma_f32_16x16x32_bf16 v[88:91], v[248:251], v[124:127], v[88:91]\n\t"
    "v_mfma_f32_16x16x32_bf16 v[92:95], v[252:255], v[124:127], v[92:95]\n\t"
    "s_nop 4\n\t"
    "ds_write_b128 %[rw], v[80:83] offset:0\n\t"
    "ds_write_b128 %[rw], v[84:87] offset:1024\n\t"
    "ds_write_b128 %[rw], v[88:91] offset:2048\n\t"
    "ds_write_b128 %[rw], v[92:95] offset:3072\n\t"
    "s_waitcnt vmcnt(0)\n\t"
    "ds_read_b32 v79, %[sa]\n\t"
    "s_waitcnt lgkmcnt(0)\n\t"
    "v_mad_u32_u24 v79, v79, %[mu], %[ad]\n\t"
    "v_max_i32 v78, v78, v79\n\t"
    "v_mov_b32 v77, %[b]\n\t"
    "v_cmp_ge_i32 vcc, %[b], v78\n\t"
    "s_cmp_ge_i32 %[b], %[ns]\n\t"
    "s_cbranch_scc1 6f\n\t"
    "s_cbranch_vccnz 6f\n\t"
    "v_or_b32 v77, 0x100, v77\n\t"
    "ds_write_b32 %[fa], v77\n\t"
    "s_branch 4b\n\t"
    "6:\n\t"
    "ds_write_b32 %[fa], v77\n\t"
    "s_cbranch_scc0 21b\n\t"
    "s_branch 9f\n\t"
    "5:\n\t"
    "s_waitcnt lgkmcnt(4)\n\t"
    "v_mfma_f32_16x16x32_bf16 v[80:83], v[128:131], v[96:99], 0\n\t"
    "ds_read_b128 v[120:123], %[da] offset:4096\n\t"
    "s_add_u32 m0, %[lb], 0\n\t"
    "v_mfma_f32_16x16x32_bf16 v[84:87], v[132:135], v[96:99], 0\n\t"
    "ds_read_b128 v[124:127], %[da] offset:6144\n\t"
    "global_load_lds_dwordx4 %[av0], s[36:37] sc1\n\t"
    "v_mfma_f32_16x16x32_bf16 v[88:91], v[128:131], v[100:103], 0\n\t"
    "v_mfma_f32_16x16x32_bf16 v[92:95], v[132:135], v[100:103], 0\n\t"
    "s_waitcnt vmcnt(5)\n\t"
    "s_barrier\n\t"
    "s_waitcnt lgkmcnt(4)\n\t"
    "v_mfma_f32_16x16x32_bf16 v[80:83], v[136:139], v[104:107], v[80:83]\n\t"
    "ds_read_b128 v[96:99], %[ea] offset:8192\n\t"
    "s_add_u32 m0, %[lb], 4096\n\t"
    "v_mfma_f32_16x16x32_bf16 v[84:87], v[140:143], v[104:107], v[84:87]\n\t"
    "ds_read_b128 v[100:103], %[ea] offset:10240\n\t"
    "global_load_lds_dwordx4 %[av2], s[36:37] sc1\n\t"
    "v_mfma_f32_16x16x32_bf16 v[88:91], v[136:139], v[108:111], v[88:91]\n\t"
    "v_mfma_f32_16x16x32_bf16 v[92:95], v[140:143], v[108:111], v[92:95]\n\t"
    "s_waitcnt lgkmcnt(4)\n\t"
    "v_mfma_f32_16x16x32_bf16 v[80:83], v[144:147], v[112:115], v[80:83]\n\t"
    "ds_read_b128 v[104:107], %[da] offset:8192\n\t"
    "s_add_u32 m0, %[lb], 8192\n\t"
    "v_mfma_f32_16x16x32_bf16 v[84:87], v[148:151], v[112:115], v[84:87]\n\t"
    "ds_read_b128 v[108:111], %[da] offset:10240\n\t"
    "global_load_lds_dwordx4 %[av4], s[36:37] sc1\n\t"
    "v_mfma_f32_16x16x32_bf16 v[88:91], v[144:147], v[116:119], v[88:91]\n\t"
    "v_mfma_f32_16x16x32_bf16 v[92:95], v[148:151], v[116:119], v[92:95]\n\t"
    "s_waitcnt lgkmcnt(4)\n\t"
    "v_mfma_f32_16x16x32_bf16 v[80:83], v[152:155], v[120:123], v[80:83]\n\t"
    "ds_read_b128 v[112:115], %[ea] offset:12288\n\t"
    "s_add_u32 m0, %[lb], 12288\n\t"
    "v_mfma_f32_16x16x32_bf16 v[84:87], v[156:159], v[120:123], v[84:87]\n\t"
    "ds_read_b128 v[116:119], %[ea] offset:14336\n\t"
    "global_load_lds_dwordx4 %[av6], s[36:37] sc1\n\t"
    "v_mfma_f32_16x16x32_bf16 v[88:91], v[152:155], v[124:127], v[88:91]\n\t"
    "v_mfma_f32_16x16x32_bf16 v[92:95], v[156:159], v[124:127], v[92:95]\n\t"
    "s_waitcnt lgkmcnt(4)\n\t"
    "v_mfma_f32_16x16x32_bf16 v[80:83], v[160:163], v[96:99], v[80:83]\n\t"
    "ds_read_b128 v[120:123], %[da] offset:12288\n\t"
    "s_add_u32 m0, %[lb], 16384\n\t"
    "v_mfma_f32_16x16x32_bf16 v[84:87], v[164:167], v[96:99], v[84:87]\n\t"
    "ds_read_b128 v[124:127], %[da] offset:14336\n\t"
    "global_load_lds_dwordx4 %[av0], s[38:39] sc1\n\t"
    "v_mfma_f32_16x16x32_bf16 v[88:91], v[160:163], v[100:103], v[88:91]\n\t"
    "v_mfma_f32_16x16x32_bf16 v[92:95], v[164:167], v[100:103], v[92:95]\n\t"
    "s_waitcnt vmcnt(7)\n\t"
    "s_barrier\n\t"
    "s_waitcnt lgkmcnt(4)\n\t"
    "v_mfma_f32_16x16x32_bf16 v[80:83], v[168:171], v[104:107], v[80:83]\n\t"
    "ds_read_b128 v[96:99], %[ea] offset:16384\n\t"
    "s_add_u32 m0, %[lb], 20480\n\t"
    "v_mfma_f32_16x16x32_bf16 v[84:87], v[172:175], v[104:107], v[84:87]\n\t"
    "ds_read_b128 v[100:103], %[ea] offset:18432\n\t"
    "global_load_lds_dwordx4 %[av2], s[38:39] sc1\n\t"
    "v_mfma_f32_16x16x32_bf16 v[88:91], v[168:171], v[108:111], v[88:91]\n\t"
    "v_mfma_f32_16x16x32_bf16 v[92:95], v[172:175], v[108:111], v[92:95]\n\t"
    "s_waitcnt lgkmcnt(4)\n\t"
    "v_mfma_f32_16x16x32_bf16 v[80:83], v[176:179], v[112:115], v[80:83]\n\t"
    "ds_read_b128 v[104:107], %[da] offset:16384\n\t"
    "s_add_u32 m0, %[lb], 24576\n\t"
    "v_mfma_f32_16x16x32_bf16 v[84:87], v[180:183], v[112:115], v[84:87]\n\t"
    "ds_read_b128 v[108:111], %[da] offset:18432\n\t"
    "global_load_lds_dwordx4 %[av4], s[38:39] sc1\n\t"
    "v_mfma_f32_16x16x32_bf16 v[88:91], v[176:179], v[116:119], v[88:91]\n\t"
    "v_mfma_f32_16x16x32_bf16 v[92:95], v[180:183], v[116:119], v[92:95]\n\t"
    "s_waitcnt lgkmcnt(4)\n\t"
    "v_mfma_f32_16x16x32_bf16 v[80:83], v[184:187], v[120:123], v[80:83]\n\t"
    "ds_read_b128 v[112:115], %[ea] offset:20480\n\t"
    "s_add_u32 m0, %[lb], 28672\n\t"
    "v_mfma_f32_16x16x32_bf16 v[84:87], v[188:191], v[120:123], v[84:87]\n\t"
    "ds_read_b128 v[116:119], %[ea] offset:22528\n\t"
    "global_load_lds_dwordx4 %[av6], s[38:39] sc1\n\t"
    "v_mfma_f32_16x16x32_bf16 v[88:91], v[184:187], v[124:127], v[88:91]\n\t"
    "v_mfma_f32_16x16x32_bf16 v[92:95], v[188:191], v[124:127], v[92:95]\n\t"
    "s_waitcnt lgkmcnt(4)\n\t"
    "v_mfma_f32_16x16x32_bf16 v[80:83], v[192:195], v[96:99], v[80:83]\n\t"
    "ds_read_b128 v[120:123], %[da] offset:20480\n\t"
    "v_mfma_f32_16x16x32_bf16 v[84:87], v[196:199], v[96:99], v[84:87]\n\t"
    "ds_read_b128 v[124:127], %[da] offset:22528\n\t"
    "v_mfma_f32_16x16x32_bf16 v[88:91], v[192:195], v[100:103], v[88:91]\n\t"
    "v_mfma_f32_16x16x32_bf16 v[92:95], v[196:199], v[100:103], v[92:95]\n\t"
    "s_add_u32 s36, s36, %[ab]\n\t"
    "s_addc_u32 s37, s37, 0\n\t"
    "s_add_u32 s38, s38, %[ab]\n\t"
    "s_addc_u32 s39, s39, 0\n\t"
    "s_xor_b32 %[lb], %[lb], 0x10000\n\t"
    "s_waitcnt vmcnt(8)\n\t"
    "s_barrier\n\t"
    "s_waitcnt lgkmcnt(4)\n\t"
    "v_mfma_f32_16x16x32_bf16 v[80:83], v[200:203], v[104:107], v[80:83]\n\t"
    "ds_read_b128 v[96:99], %[ea] offset:24576\n\t"
    "v_mfma_f32_16x16x32_bf16 v[84:87], v[204:207], v[104:107], v[84:87]\n\t"
    "ds_read_b128 v[100:103], %[ea] offset:26624\n\t"
    "v_mfma_f32_16x16x32_bf16 v[88:91], v[200:203], v[108:111], v[88:91]\n\t"
    "v_mfma_f32_16x16x32_bf16 v[92:95], v[204:207], v[108:111], v[92:95]\n\t"
    "s_waitcnt lgkmcnt(4)\n\t"
    "v_mfma_f32_16x16x32_bf16 v[80:83], v[208:211], v[112:115], v[80:83]\n\t"
    "ds_read_b128 v[104:107], %[da] offset:24576\n\t"
    "v_mfma_f32_16x16x32_bf16 v[84:87], v[212:215], v[112:115], v[84:87]\n\t"
    "ds_read_b128 v[108:111], %[da] offset:26624\n\t"
    "v_mfma_f32_16x16x32_bf16 v[88:91], v[208:211], v[116:119], v[88:91]\n\t"
    "v_mfma_f32_16x16x32_bf16 v[92:95], v[212:215], v[116:119], v[92:95]\n\t"
    "s_cmp_eq_u32 %[sl], 0\n\t"
    "s_cbranch_scc1 73f\n\t"
    "s_mov_b32 m0, %[sl]\n\t"
    "s_nop 0\n\t"
    "global_load_lds_dword %[dv], %[db] sc1\n\t"
    "73:\n\t"
    "s_waitcnt lgkmcnt(4)\n\t"
    "v_mfma_f32_16x16x32_bf16 v[80:83], v[216:219], v[120:123], v[80:83]\n\t"
    "ds_read_b128 v[112:115], %[ea] offset:28672\n\t"
    "v_mfma_f32_16x16x32_bf16 v[84:87], v[220:223], v[120:123], v[84:87]\n\t"
    "ds_read_b128 v[116:119], %[ea] offset:30720\n\t"
    "v_mfma_f32_16x16x32_bf16 v[88:91], v[216:219], v[124:127], v[88:91]\n\t"
    "v_mfma_f32_16x16x32_bf16 v[92:95], v[220:223], v[124:127], v[92:95]\n\t"
    "s_waitcnt lgkmcnt(4)\n\t"
    "v_mfma_f32_16x16x32_bf16 v[80:83], v[224:227], v[96:99], v[80:83]\n\t"
    "ds_read_b128 v[120:123], %[da] offset:28672\n\t"
    "v_mfma_f32_16x16x32_bf16 v[84:87], v[228:231], v[96:99], v[84:87]\n\t"
    "ds_read_b128 v[124:127], %[da] offset:30720\n\t"
    "v_mfma_f32_16x16x32_bf16 v[88:91], v[224:227], v[100:103], v[88:91]\n\t"
    "v_mfma_f32_16x16x32_bf16 v[92:95], v[228:231], v[100:103], v[92:95]\n\t"
    "s_waitcnt lgkmcnt(4)\n\t"
    "v_mfma_f32_16x16x32_bf16 v[80:83], v[232:235], v[104:107], v[80:83]\n\t"
    "v_mfma_f32_16x16x32_bf16 v[84:87], v[236:239], v[104:107], v[84:87]\n\t"
    "v_mfma_f32_16x16x32_bf16 v[88:91], v[232:235], v[108:111], v[88:91]\n\t"
    "v_xor_b32 %[ea], 0x10000, %[ea]\n\t"
    "v_mfma_f32_16x16x32_bf16 v[92:95], v[236:239], v[108:111], v[92:95]\n\t"
    "v_xor_b32 %[da], 0x10000, %[da]\n\t"
    "s_waitcnt lgkmcnt(2)\n\t"
    "v_mfma_f32_16x16x32_bf16 v[80:83], v[240:243], v[112:115], v[80:83]\n\t"
    "v_mfma_f32_16x16x32_bf16 v[84:87], v[244:247], v[112:115], v[84:87]\n\t"
    "v_mfma_f32_16x16x32_bf16 v[88:91], v[240:243], v[116:119], v[88:91]\n\t"
    "s_add_u32 %[b], %[b], 1\n\t"
    "v_mfma_f32_16x16x32_bf16 v[92:95], v[244:247], v[116:119], v[92:95]\n\t"
    "s_waitcnt lgkmcnt(0)\n\t"
    "v_mfma_f32_16x16x32_bf16 v[80:83], v[248:251], v[120:123], v[80:83]\n\t"
    "v_mfma_f32_16x16x32_bf16 v[84:87], v[252:255], v[120:123], v[84:87]\n\t"
    "v_mfma_f32_16x16x32_bf16 v[88:91], v[248:251], v[124:127], v[88:91]\n\t"
    "v_mfma_f32_16x16x32_bf16 v[92:95], v[252:255], v[124:127], v[92:95]\n\t"
    "v_mov_b32 v77, %[b]\n\t"
    "v_or_b32 v77, 0x100, v77\n\t"
    "s_nop 2\n\t"
    "ds_write_b128 %[rw], v[80:83] offset:0\n\t"
    "ds_write_b128 %[rw], v[84:87] offset:1024\n\t"
    "ds_write_b128 %[rw], v[88:91] offset:2048\n\t"
    "ds_write_b128 %[rw], v[92:95] offset:3072\n\t"
    "ds_write_b32 %[fa], v77\n\t"
    "s_waitcnt vmcnt(0)\n\t"
    "ds_read_b32 v79, %[sa]\n\t"
    "s_waitcnt lgkmcnt(0)\n\t"
    "v_mad_u32_u24 v79, v79, %[mu], %[ad]\n\t"
    "v_max_i32 v78, v78, v79\n\t"
    "s_branch 3b\n\t"
    "9:\n\t"
    "s_waitcnt lgkmcnt(0)"
    : [ea] "+&v"(ea), [da] "+&v"(da), [b] "+&s"(b), [st] "=&s"(st), "+{s[36:37]}"(p0), "+{s[38:39]}"(p1), [lb] "+&s"(lb), [cap] "+&s"(cap)
    : [ns] "s"(ns), [ab] "s"(ab), [rw] "v"(rw), [fa] "v"(fa), "{v[128:159]}"(W[0]), "{v[160:191]}"(W[1]), "{v[192:223]}"(W[2]), "{v[224:255]}"(W[3]), [wm] "s"(wm), [rq] "s"(rq), [mu] "s"(mu), [ad] "v"(ad), [dv] "v"(dv), [db] "s"(db), [sl] "s"(sl), [sa] "v"(sa), [av0] "v"(av[0]), [av1] "v"(av[1]), [av2] "v"(av[2]), [av3] "v"(av[3]), [av4] "v"(av[4]), [av5] "v"(av[5]), [av6] "v"(av[6]), [av7] "v"(av[7])
    : "v77", "v78", "v79", "v80", "v81", "v82", "v83", "v84", "v85", "v86", "v87", "v88", "v89", "v90", "v91", "v92", "v93", "v94", "v95", "v96", "v97", "v98", "v99", "v100", "v101", "v102", "v103", "v104", "v105", "v106", "v107", "v108", "v109", "v110", "v111", "v112", "v113", "v114", "v115", "v116", "v117", "v118", "v119", "v120", "v121", "v122", "v123", "v124", "v125", "v126", "v127", "vcc", "scc", "m0", "memory");
}
__device__ __forceinline__ void paced_st0(const u32x32 (&W)[4], unsigned ea, unsigned da, int b, int ns, unsigned ab, unsigned rw, unsigned fa,
    const char* p0, unsigned pp, int po, unsigned cn, const char* pg, unsigned co0, unsigned co1) {
  unsigned st;
  asm volatile("1:\n\t"
    "s_barrier\n\t"
    "ds_read_b128 v[96:99], %[ea] offset:0\n\t"
    "ds_read_b128 v[100:103], %[ea] offset:2048\n\t"
    "ds_read_b128 v[104:107], %[da] offset:0\n\t"
    "ds_read_b128 v[108:111], %[da] offset:2048\n\t"
    "ds_read_b128 v[112:115], %[ea] offset:4096\n\t"
    "ds_read_b128 v[116:119], %[ea] offset:6144\n\t"
    "s_waitcnt lgkmcnt(4)\n\t"
    "v_mfma_f32_16x16x32_bf16 v[80:83], v[128:131], v[96:99], 0\n\t"
    "ds_read_b128 v[120:123], %[da] offset:4096\n\t"
    "v_mfma_f32_16x16x32_bf16 v[84:87], v[132:135], v[96:99], 0\n\t"
    "ds_read_b128 v[124:127], %[da] offset:6144\n\t"
    "v_mfma_f32_16x16x32_bf16 v[88:91], v[128:131], v[100:103], 0\n\t"
    "v_mfma_f32_16x16x32_bf16 v[92:95], v[132:135], v[100:103], 0\n\t"
    "s_barrier\n\t"
    "s_waitcnt lgkmcnt(4)\n\t"
    "v_mfma_f32_16x16x32_bf16 v[80:83], v[136:139], v[104:107], v[80:83]\n\t"
    "ds_read_b128 v[96:99], %[ea] offset:8192\n\t"
    "v_mfma_f32_16x16x32_bf16 v[84:87], v[140:143], v[104:107], v[84:87]\n\t"
    "ds_read_b128 v[100:103], %[ea] offset:10240\n\t"
    "v_mfma_f32_16x16x32_bf16 v[88:91], v[136:139], v[108:111], v[88:91]\n\t"
    "v_mfma_f32_16x16x32_bf16 v[92:95], v[140:143], v[108:111], v[92:95]\n\t"
    "s_waitcnt lgkmcnt(4)\n\t"
    "v_mfma_f32_16x16x32_bf16 v[80:83], v[144:147], v[112:115], v[80:83]\n\t"
    "ds_read_b128 v[104:107], %[da] offset:8192\n\t"
    "v_mfma_f32_16x16x32_bf16 v[84:87], v[148:151], v[112:115], v[84:87]\n\t"
    "ds_read_b128 v[108:111], %[da] offset:10240\n\t"
    "v_mfma_f32_16x16x32_bf16 v[88:91], v[144:147], v[116:119], v[88:91]\n\t"
    "v_mfma_f32_16x16x32_bf16 v[92:95], v[148:151], v[116:119], v[92:95]\n\t"
    "s_waitcnt lgkmcnt(4)\n\t"
    "v_mfma_f32_16x16x32_bf16 v[80:83], v[152:155], v[120:123], v[80:83]\n\t"
    "ds_read_b128 v[112:115], %[ea] offset:12288\n\t"
    "v_mfma_f32_16x16x32_bf16 v[84:87], v[156:159], v[120:123], v[84:87]\n\t"
    "ds_read_b128 v[116:119], %[ea] offset:14336\n\t"
    "v_mfma_f32_16x16x32_bf16 v[88:91], v[152:155], v[124:127], v[88:91]\n\t"
    "v_mfma_f32_16x16x32_bf16 v[92:95], v[156:159], v[124:127], v[92:95]\n\t"
    "s_cmp_lg_u32 %[pp], 0\n\t"
    "s_cbranch_scc1 7f\n\t"
    "8:\n\t"
    "s_waitcnt lgkmcnt(4)\n\t"
    "v_mfma_f32_16x16x32_bf16 v[80:83], v[160:163], v[96:99], v[80:83]\n\t"
    "ds_read_b128 v[120:123], %[da] offset:12288\n\t"
    "v_mfma_f32_16x16x32_bf16 v[84:87], v[164:167], v[96:99], v[84:87]\n\t"
    "ds_read_b128 v[124:127], %[da] offset:14336\n\t"
    "v_mfma_f32_16x16x32_bf16 v[88:91], v[160:163], v[100:103], v[88:91]\n\t"
    "v_mfma_f32_16x16x32_bf16 v[92:95], v[164:167], v[100:103], v[92:95]\n\t"
    "s_barrier\n\t"
    "s_waitcnt lgkmcnt(4)\n\t"
    "v_mfma_f32_16x16x32_bf16 v[80:83], v[168:171], v[104:107], v[80:83]\n\t"
    "ds_read_b128 v[96:99], %[ea] offset:16384\n\t"
    "v_mfma_f32_16x16x32_bf16 v[84:87], v[172:175], v[104:107], v[84:87]\n\t"
    "ds_read_b128 v[100:103], %[ea] offset:18432\n\t"
    "v_mfma_f32_16x16x32_bf16 v[88:91], v[168:171], v[108:111], v[88:91]\n\t"
    "v_mfma_f32_16x16x32_bf16 v[92:95], v[172:175], v[108:111], v[92:95]\n\t"
    "s_waitcnt lgkmcnt(4)\n\t"
    "v_mfma_f32_16x16x32_bf16 v[80:83], v[176:179], v[112:115], v[80:83]\n\t"
    "ds_read_b128 v[104:107], %[da] offset:16384\n\t"
    "v_mfma_f32_16x16x32_bf16 v[84:87], v[180:183], v[112:115], v[84:87]\n\t"
    "ds_read_b128 v[108:111], %[da] offset:18432\n\t"
    "v_mfma_f32_16x16x32_bf16 v[88:91], v[176:179], v[116:119], v[88:91]\n\t"
    "v_mfma_f32_16x16x32_bf16 v[92:95], v[180:183], v[116:119], v[92:95]\n\t"
    "s_waitcnt lgkmcnt(4)\n\t"
    "v_mfma_f32_16x16x32_bf16 v[80:83], v[184:187], v[120:123], v[80:83]\n\t"
    "ds_read_b128 v[112:115], %[ea] offset:20480\n\t"
    "v_mfma_f32_16x16x32_bf16 v[84:87], v[188:191], v[120:123], v[84:87]\n\t"
    "ds_read_b128 v[116:119], %[ea] offset:22528\n\t"
    "v_mfma_f32_16x16x32_bf16 v[88:91], v[184:187], v[124:127], v[88:91]\n\t"
    "v_mfma_f32_16x16x32_bf16 v[92:95], v[188:191], v[124:127], v[92:95]\n\t"
    "s_waitcnt lgkmcnt(4)\n\t"
    "v_mfma_f32_16x16x32_bf16 v[80:83], v[192:195], v[96:99], v[80:83]\n\t"
    "ds_read_b128 v[120:123], %[da] offset:20480\n\t"
    "v_mfma_f32_16x16x32_bf16 v[84:87], v[196:199], v[96:99], v[84:87]\n\t"
    "ds_read_b128 v[124:127], %[da] offset:22528\n\t"
    "v_mfma_f32_16x16x32_bf16 v[88:91], v[192:195], v[100:103], v[88:91]\n\t"
    "v_mfma_f32_16x16x32_bf16 v[92:95], v[196:199], v[100:103], v[92:95]\n\t"
    "s_barrier\n\t"
    "s_waitcnt lgkmcnt(4)\n\t"
    "v_mfma_f32_16x16x32_bf16 v[80:83], v[200:203], v[104:107], v[80:83]\n\t"
    "ds_read_b128 v[96:99], %[ea] offset:24576\n\t"
    "v_mfma_f32_16x16x32_bf16 v[84:87], v[204:207], v[104:107], v[84:87]\n\t"
    "ds_read_b128 v[100:103], %[ea] offset:26624\n\t"
    "v_mfma_f32_16x16x32_bf16 v[88:91], v[200:203], v[108:111], v[88:91]\n\t"
    "v_mfma_f32_16x16x32_bf16 v[92:95], v[204:207], v[108:111], v[92:95]\n\t"
    "s_waitcnt lgkmcnt(4)\n\t"
    "v_mfma_f32_16x16x32_bf16 v[80:83], v[208:211], v[112:115], v[80:83]\n\t"
    "ds_read_b128 v[104:107], %[da] offset:24576\n\t"
    "v_mfma_f32_16x16x32_bf16 v[84:87], v[212:215], v[112:115], v[84:87]\n\t"
    "ds_read_b128 v[108:111], %[da] offset:26624\n\t"
    "v_mfma_f32_16x16x32_bf16 v[88:91], v[208:211], v[116:119], v[88:91]\n\t"
    "v_mfma_f32_16x16x32_bf16 v[92:95], v[212:215], v[116:119], v[92:95]\n\t"
    "s_waitcnt lgkmcnt(4)\n\t"
    "v_mfma_f32_16x16x32_bf16 v[80:83], v[216:219], v[120:123], v[80:83]\n\t"
    "ds_read_b128 v[112:115], %[ea] offset:28672\n\t"
    "v_mfma_f32_16x16x32_bf16 v[84:87], v[220:223], v[120:123], v[84:87]\n\t"
    "ds_read_b128 v[116:119], %[ea] offset:30720\n\t"
    "v_mfma_f32_16x16x32_bf16 v[88:91], v[216:219], v[124:127], v[88:91]\n\t"
    "v_mfma_f32_16x16x32_bf16 v[92:95], v[220:223], v[124:127], v[92:95]\n\t"
    "s_waitcnt lgkmcnt(4)\n\t"
    "v_mfma_f32_16x16x32_bf16 v[80:83], v[224:227], v[96:99], v[80:83]\n\t"
    "ds_read_b128 v[120:123], %[da] offset:28672\n\t"
    "v_mfma_f32_16x16x32_bf16 v[84:87], v[228:231], v[96:99], v[84:87]\n\t"
    "ds_read_b128 v[124:127], %[da] offset:30720\n\t"
    "v_mfma_f32_16x16x32_bf16 v[88:91], v[224:227], v[100:103], v[88:91]\n\t"
    "v_mfma_f32_16x16x32_bf16 v[92:95], v[228:231], v[100:103], v[92:95]\n\t"
    "s_waitcnt lgkmcnt(4)\n\t"
    "v_mfma_f32_16x16x32_bf16 v[80:83], v[232:235], v[104:107], v[80:83]\n\t"
    "v_mfma_f32_16x16x32_bf16 v[84:87], v[236:239], v[104:107], v[84:87]\n\t"
    "v_mfma_f32_16x16x32_bf16 v[88:91], v[232:235], v[108:111], v[88:91]\n\t"
    "v_xor_b32 %[ea], 0x10000, %[ea]\n\t"
    "v_mfma_f32_16x16x32_bf16 v[92:95], v[236:239], v[108:111], v[92:95]\n\t"
    "v_xor_b32 %[da], 0x10000, %[da]\n\t"
    "s_waitcnt lgkmcnt(2)\n\t"
    "v_mfma_f32_16x16x32_bf16 v[80:83], v[240:243], v[112:115], v[80:83]\n\t"
    "v_mfma_f32_16x16x32_bf16 v[84:87], v[244:247], v[112:115], v[84:87]\n\t"
    "v_mfma_f32_16x16x32_bf16 v[88:91], v[240:243], v[116:119], v[88:91]\n\t"
    "s_add_u32 %[b], %[b], 1\n\t"
    "v_mfma_f32_16x16x32_bf16 v[92:95], v[244:247], v[116:119], v[92:95]\n\t"
    "s_waitcnt lgkmcnt(0)\n\t"
    "v_mfma_f32_16x16x32_bf16 v[80:83], v[248:251], v[120:123], v[80:83]\n\t"
    "v_mfma_f32_16x16x32_bf16 v[84:87], v[252:255], v[120:123], v[84:87]\n\t"
    "v_mfma_f32_16x16x32_bf16 v[88:91], v[248:251], v[124:127], v[88:91]\n\t"
    "v_mfma_f32_16x16x32_bf16 v[92:95], v[252:255], v[124:127], v[92:95]\n\t"
    "2:\n\t"
    "ds_read_b32 v39, %[fa]\n\t"
    "ds_read_b128 v[48:51], %[rw] offset:0\n\t"
    "ds_read_b128 v[52:55], %[rw] offset:1024\n\t"
    "ds_read_b128 v[56:59], %[rw] offset:2048\n\t"
    "ds_read_b128 v[60:63], %[rw] offset:3072\n\t"
    "s_waitcnt lgkmcnt(2)\n\t"
    "v_readfirstlane_b32 %[pp], v39\n\t"
    "s_and_b32 %[st], %[pp], 0xff\n\t"
    "s_cmp_eq_u32 %[st], %[b]\n\t"
    "s_cbranch_scc1 3f\n\t"
    "s_waitcnt lgkmcnt(0)\n\t"
    "s_branch 2b\n\t"
    "3:\n\t"
    "v_add_f32 v48, v48, v80\n\t"
    "v_add_f32 v49, v49, v81\n\t"
    "v_add_f32 v50, v50, v82\n\t"
    "v_add_f32 v51, v51, v83\n\t"
    "v_add_f32 v52, v52, v84\n\t"
    "v_add_f32 v53, v53, v85\n\t"
    "v_add_f32 v54, v54, v86\n\t"
    "v_add_f32 v55, v55, v87\n\t"
    "v_cvt_pk_bf16_f32 v40, v48, v49\n\t"
    "v_cvt_pk_bf16_f32 v41, v50, v51\n\t"
    "v_cvt_pk_bf16_f32 v42, v52, v53\n\t"
    "v_cvt_pk_bf16_f32 v43, v54, v55\n\t"
    "global_store_dwordx4 %[co0], v[40:43], s[36:37] sc1\n\t"
    "s_waitcnt lgkmcnt(0)\n\t"
    "v_add_f32 v56, v56, v88\n\t"
    "v_add_f32 v57, v57, v89\n\t"
    "v_add_f32 v58, v58, v90\n\t"
    "v_add_f32 v59, v59, v91\n\t"
    "v_add_f32 v60, v60, v92\n\t"
    "v_add_f32 v61, v61, v93\n\t"
    "v_add_f32 v62, v62, v94\n\t"
    "v_add_f32 v63, v63, v95\n\t"
    "v_cvt_pk_bf16_f32 v44, v56, v57\n\t"
    "v_cvt_pk_bf16_f32 v45, v58, v59\n\t"
    "v_cvt_pk_bf16_f32 v46, v60, v61\n\t"
    "v_cvt_pk_bf16_f32 v47, v62, v63\n\t"
    "global_store_dwordx4 %[co1], v[44:47], s[36:37] sc1\n\t"
    "s_add_u32 s36, s36, %[ab]\n\t"
    "s_addc_u32 s37, s37, 0\n\t"
    "s_lshr_b32 %[pp], %[pp], 8\n\t"
    "s_cbranch_scc1 1b\n\t"
    "v_mov_b32 v72, %[cn]\n\t"
    "v_mov_b32 v73, 1\n\t"
    "s_add_u32 %[st], %[b], %[po]\n\t"
    "v_mov_b32 v74, %[st]\n\t"
    "v_mov_b32 v75, 0\n\t"
    "s_waitcnt vmcnt(0)\n\t"
    "s_mov_b64 exec, 1\n\t"
    "ds_add_rtn_u32 v73, v72, v73\n\t"
    "s_waitcnt lgkmcnt(0)\n\t"
    "v_readfirstlane_b32 %[st], v73\n\t"
    "s_and_b32 %[st], %[st], 3\n\t"
    "s_cmp_lg_u32 %[st], 3\n\t"
    "s_cbranch_scc1 61f\n\t"
    "global_store_dword v75, v74, %[pg] sc1\n\t"
    "61:\n\t"
    "s_mov_b64 exec, -1\n\t"
    "s_mov_b32 %[pp], 0\n\t"
    "s_cmp_lt_i32 %[b], %[ns]\n\t"
    "s_cbranch_scc1 1b\n\t"
    "s_branch 9f\n\t"
    "7:\n\t"
    "v_mov_b32 v72, %[cn]\n\t"
    "v_mov_b32 v73, 1\n\t"
    "s_add_u32 %[st], %[b], %[po]\n\t"
    "v_mov_b32 v74, %[st]\n\t"
    "v_mov_b32 v75, 0\n\t"
    "s_waitcnt vmcnt(0)\n\t"
    "s_mov_b64 exec, 1\n\t"
    "ds_add_rtn_u32 v73, v72, v73\n\t"
    "s_waitcnt lgkmcnt(0)\n\t"
    "v_readfirstlane_b32 %[st], v73\n\t"
    "s_and_b32 %[st], %[st], 3\n\t"
    "s_cmp_lg_u32 %[st], 3\n\t"
    "s_cbranch_scc1 61f\n\t"
    "global_store_dword v75, v74, %[pg] sc1\n\t"
    "61:\n\t"
    "s_mov_b64 exec, -1\n\t"
    "s_mov_b32 %[pp], 0\n\t"
    "s_branch 8b\n\t"
    "9:"
    : [ea] "+&v"(ea), [da] "+&v"(da), [b] "+&s"(b), [st] "=&s"(st), "+{s[36:37]}"(p0), [pp] "+&s"(pp)
    : [ns] "s"(ns), [ab] "s"(ab), [rw] "v"(rw), [fa] "v"(fa), "{v[128:159]}"(W[0]), "{v[160:191]}"(W[1]), "{v[192:223]}"(W[2]), "{v[224:255]}"(W[3]), [po] "s"(po), [cn] "s"(cn), [pg] "s"(pg), [co0] "v"(co0), [co1] "v"(co1)
    : "v39", "v40", "v41", "v42", "v43", "v44", "v45", "v46", "v47", "v48", "v49", "v50", "v51", "v52", "v53", "v54", "v55", "v56", "v57", "v58", "v59", "v60", "v61", "v62", "v63", "v72", "v73", "v74", "v75", "v80", "v81", "v82", "v83", "v84", "v85", "v86", "v87", "v88", "v89", "v90", "v91", "v92", "v93", "v94", "v95", "v96", "v97", "v98", "v99", "v100", "v101", "v102", "v103", "v104", "v105", "v106", "v107", "v108", "v109", "v110", "v111", "v112", "v113", "v114", "v115", "v116", "v117", "v118", "v119", "v120", "v121", "v122", "v123", "v124", "v125", "v126", "v127", "vcc", "scc", "m0", "memory");
}
__device__ __forceinline__ void paced_st1(const u32x32 (&W)[4], unsigned ea, unsigned da, int b, int ns, unsigned ab, unsigned rw, unsigned fa,
    const char* p0, unsigned pp, int po, unsigned cn, const char* pg, unsigned co0, unsigned co1) {
  unsigned st;
  asm volatile("1:\n\t"
    "s_barrier\n\t"
    "ds_read_b128 v[96:99], %[ea] offset:0\n\t"
    "ds_read_b128 v[100:103], %[ea] offset:2048\n\t"
    "ds_read_b128 v[104:107], %[da] offset:0\n\t"
    "ds_read_b128 v[108:111], %[da] offset:2048\n\t"
    "ds_read_b128 v[112:115], %[ea] offset:4096\n\t"
    "ds_read_b128 v[116:119], %[ea] offset:6144\n\t"
    "s_waitcnt lgkmcnt(4)\n\t"
    "v_mfma_f32_16x16x32_bf16 v[80:83], v[128:131], v[96:99], 0\n\t"
    "ds_read_b128 v[120:123], %[da] offset:4096\n\t"
    "v_mfma_f32_16x16x32_bf16 v[84:87], v[132:135], v[96:99], 0\n\t"
    "ds_read_b128 v[124:127], %[da] offset:6144\n\t"
    "v_mfma_f32_16x16x32_bf16 v[88:91], v[128:131], v[100:103], 0\n\t"
    "v_mfma_f32_16x16x32_bf16 v[92:95], v[132:135], v[100:103], 0\n\t"
    "s_barrier\n\t"
    "s_waitcnt lgkmcnt(4)\n\t"
    "v_mfma_f32_16x16x32_bf16 v[80:83], v[136:139], v[104:107], v[80:83]\n\t"
    "ds_read_b128 v[96:99], %[ea] offset:8192\n\t"
    "v_mfma_f32_16x16x32_bf16 v[84:87], v[140:143], v[104:107], v[84:87]\n\t"
    "ds_read_b128 v[100:103], %[ea] offset:10240\n\t"
    "v_mfma_f32_16x16x32_bf16 v[88:91], v[136:139], v[108:111], v[88:91]\n\t"
    "v_mfma_f32_16x16x32_bf16 v[92:95], v[140:143], v[108:111], v[92:95]\n\t"
    "s_waitcnt lgkmcnt(4)\n\t"
    "v_mfma_f32_16x16x32_bf16 v[80:83], v[144:147], v[112:115], v[80:83]\n\t"
    "ds_read_b128 v[104:107], %[da] offset:8192\n\t"
    "v_mfma_f32_16x16x32_bf16 v[84:87], v[148:151], v[112:115], v[84:87]\n\t"
    "ds_read_b128 v[108:111], %[da] offset:10240\n\t"
    "v_mfma_f32_16x16x32_bf16 v[88:91], v[144:147], v[116:119], v[88:91]\n\t"
    "v_mfma_f32_16x16x32_bf16 v[92:95], v[148:151], v[116:119], v[92:95]\n\t"
    "s_waitcnt lgkmcnt(4)\n\t"
    "v_mfma_f32_16x16x32_bf16 v[80:83], v[152:155], v[120:123], v[80:83]\n\t"
    "ds_read_b128 v[112:115], %[ea] offset:12288\n\t"
    "v_mfma_f32_16x16x32_bf16 v[84:87], v[156:159], v[120:123], v[84:87]\n\t"
    "ds_read_b128 v[116:119], %[ea] offset:14336\n\t"
    "v_mfma_f32_16x16x32_bf16 v[88:91], v[152:155], v[124:127], v[88:91]\n\t"
    "v_mfma_f32_16x16x32_bf16 v[92:95], v[156:159], v[124:127], v[92:95]\n\t"
    "s_cmp_lg_u32 %[pp], 0\n\t"
    "s_cbranch_scc1 7f\n\t"
    "8:\n\t"
    "s_waitcnt lgkmcnt(4)\n\t"
    "v_mfma_f32_16x16x32_bf16 v[80:83], v[160:163], v[96:99], v[80:83]\n\t"
    "ds_read_b128 v[120:123], %[da] offset:12288\n\t"
    "v_mfma_f32_16x16x32_bf16 v[84:87], v[164:167], v[96:99], v[84:87]\n\t"
    "ds_read_b128 v[124:127], %[da] offset:14336\n\t"
    "v_mfma_f32_16x16x32_bf16 v[88:91], v[160:163], v[100:103], v[88:91]\n\t"
    "v_mfma_f32_16x16x32_bf16 v[92:95], v[164:167], v[100:103], v[92:95]\n\t"
    "s_barrier\n\t"
    "s_waitcnt lgkmcnt(4)\n\t"
    "v_mfma_f32_16x16x32_bf16 v[80:83], v[168:171], v[104:107], v[80:83]\n\t"
    "ds_read_b128 v[96:99], %[ea] offset:16384\n\t"
    "v_mfma_f32_16x16x32_bf16 v[84:87], v[172:175], v[104:107], v[84:87]\n\t"
    "ds_read_b128 v[100:103], %[ea] offset:18432\n\t"
    "v_mfma_f32_16x16x32_bf16 v[88:91], v[168:171], v[108:111], v[88:91]\n\t"
    "v_mfma_f32_16x16x32_bf16 v[92:95], v[172:175], v[108:111], v[92:95]\n\t"
    "s_waitcnt lgkmcnt(4)\n\t"
    "v_mfma_f32_16x16x32_bf16 v[80:83], v[176:179], v[112:115], v[80:83]\n\t"
    "ds_read_b128 v[104:107], %[da] offset:16384\n\t"
    "v_mfma_f32_16x16x32_bf16 v[84:87], v[180:183], v[112:115], v[84:87]\n\t"
    "ds_read_b128 v[108:111], %[da] offset:18432\n\t"
    "v_mfma_f32_16x16x32_bf16 v[88:91], v[176:179], v[116:119], v[88:91]\n\t"
    "v_mfma_f32_16x16x32_bf16 v[92:95], v[180:183], v[116:119], v[92:95]\n\t"
    "s_waitcnt lgkmcnt(4)\n\t"
    "v_mfma_f32_16x16x32_bf16 v[80:83], v[184:187], v[120:123], v[80:83]\n\t"
    "ds_read_b128 v[112:115], %[ea] offset:20480\n\t"
    "v_mfma_f32_16x16x32_bf16 v[84:87], v[188:191], v[120:123], v[84:87]\n\t"
    "ds_read_b128 v[116:119], %[ea] offset:22528\n\t"
    "v_mfma_f32_16x16x32_bf16 v[88:91], v[184:187], v[124:127], v[88:91]\n\t"
    "v_mfma_f32_16x16x32_bf16 v[92:95], v[188:191], v[124:127], v[92:95]\n\t"
    "s_waitcnt lgkmcnt(4)\n\t"
    "v_mfma_f32_16x16x32_bf16 v[80:83], v[192:195], v[96:99], v[80:83]\n\t"
    "ds_read_b128 v[120:123], %[da] offset:20480\n\t"
    "v_mfma_f32_16x16x32_bf16 v[84:87], v[196:199], v[96:99], v[84:87]\n\t"
    "ds_read_b128 v[124:127], %[da] offset:22528\n\t"
    "v_mfma_f32_16x16x32_bf16 v[88:91], v[192:195], v[100:103], v[88:91]\n\t"
    "v_mfma_f32_16x16x32_bf16 v[92:95], v[196:199], v[100:103], v[92:95]\n\t"
    "s_barrier\n\t"
    "s_waitcnt lgkmcnt(4)\n\t"
    "v_mfma_f32_16x16x32_bf16 v[80:83], v[200:203], v[104:107], v[80:83]\n\t"
    "ds_read_b128 v[96:99], %[ea] offset:24576\n\t"
    "v_mfma_f32_16x16x32_bf16 v[84:87], v[204:207], v[104:107], v[84:87]\n\t"
    "ds_read_b128 v[100:103], %[ea] offset:26624\n\t"
    "v_mfma_f32_16x16x32_bf16 v[88:91], v[200:203], v[108:111], v[88:91]\n\t"
    "v_mfma_f32_16x16x32_bf16 v[92:95], v[204:207], v[108:111], v[92:95]\n\t"
    "s_waitcnt lgkmcnt(4)\n\t"
    "v_mfma_f32_16x16x32_bf16 v[80:83], v[208:211], v[112:115], v[80:83]\n\t"
    "ds_read_b128 v[104:107], %[da] offset:24576\n\t"
    "v_mfma_f32_16x16x32_bf16 v[84:87], v[212:215], v[112:115], v[84:87]\n\t"
    "ds_read_b128 v[108:111], %[da] offset:26624\n\t"
    "v_mfma_f32_16x16x32_bf16 v[88:91], v[208:211], v[116:119], v[88:91]\n\t"
    "v_mfma_f32_16x16x32_bf16 v[92:95], v[212:215], v[116:119], v[92:95]\n\t"
    "s_waitcnt lgkmcnt(4)\n\t"
    "v_mfma_f32_16x16x32_bf16 v[80:83], v[216:219], v[120:123], v[80:83]\n\t"
    "ds_read_b128 v[112:115], %[ea] offset:28672\n\t"
    "v_mfma_f32_16x16x32_bf16 v[84:87], v[220:223], v[120:123], v[84:87]\n\t"
    "ds_read_b128 v[116:119], %[ea] offset:30720\n\t"
    "v_mfma_f32_16x16x32_bf16 v[88:91], v[216:219], v[124:127], v[88:91]\n\t"
    "v_mfma_f32_16x16x32_bf16 v[92:95], v[220:223], v[124:127], v[92:95]\n\t"
    "s_waitcnt lgkmcnt(4)\n\t"
    "v_mfma_f32_16x16x32_bf16 v[80:83], v[224:227], v[96:99], v[80:83]\n\t"
    "ds_read_b128 v[120:123], %[da] offset:28672\n\t"
    "v_mfma_f32_16x16x32_bf16 v[84:87], v[228:231], v[96:99], v[84:87]\n\t"
    "ds_read_b128 v[124:127], %[da] offset:30720\n\t"
    "v_mfma_f32_16x16x32_bf16 v[88:91], v[224:227], v[100:103], v[88:91]\n\t"
    "v_mfma_f32_16x16x32_bf16 v[92:95], v[228:231], v[100:103], v[92:95]\n\t"
    "s_waitcnt lgkmcnt(4)\n\t"
    "v_mfma_f32_16x16x32_bf16 v[80:83], v[232:235], v[104:107], v[80:83]\n\t"
    "v_mfma_f32_16x16x32_bf16 v[84:87], v[236:239], v[104:107], v[84:87]\n\t"
    "v_mfma_f32_16x16x32_bf16 v[88:91], v[232:235], v[108:111], v[88:91]\n\t"
    "v_xor_b32 %[ea], 0x10000, %[ea]\n\t"
    "v_mfma_f32_16x16x32_bf16 v[92:95], v[236:239], v[108:111], v[92:95]\n\t"
    "v_xor_b32 %[da], 0x10000, %[da]\n\t"
    "s_waitcnt lgkmcnt(2)\n\t"
    "v_mfma_f32_16x16x32_bf16 v[80:83], v[240:243], v[112:115], v[80:83]\n\t"
    "v_mfma_f32_16x16x32_bf16 v[84:87], v[244:247], v[112:115], v[84:87]\n\t"
    "v_mfma_f32_16x16x32_bf16 v[88:91], v[240:243], v[116:119], v[88:91]\n\t"
    "s_add_u32 %[b], %[b], 1\n\t"
    "v_mfma_f32_16x16x32_bf16 v[92:95], v[244:247], v[116:119], v[92:95]\n\t"
    "s_waitcnt lgkmcnt(0)\n\t"
    "v_mfma_f32_16x16x32_bf16 v[80:83], v[248:251], v[120:123], v[80:83]\n\t"
    "v_mfma_f32_16x16x32_bf16 v[84:87], v[252:255], v[120:123], v[84:87]\n\t"
    "v_mfma_f32_16x16x32_bf16 v[88:91], v[248:251], v[124:127], v[88:91]\n\t"
    "v_mfma_f32_16x16x32_bf16 v[92:95], v[252:255], v[124:127], v[92:95]\n\t"
    "2:\n\t"
    "ds_read_b32 v39, %[fa]\n\t"
    "ds_read_b128 v[48:51], %[rw] offset:0\n\t"
    "ds_read_b128 v[52:55], %[rw] offset:1024\n\t"
    "ds_read_b128 v[56:59], %[rw] offset:2048\n\t"
    "ds_read_b128 v[60:63], %[rw] offset:3072\n\t"
    "s_waitcnt lgkmcnt(2)\n\t"
    "v_readfirstlane_b32 %[pp], v39\n\t"
    "s_and_b32 %[st], %[pp], 0xff\n\t"
    "s_cmp_eq_u32 %[st], %[b]\n\t"
    "s_cbranch_scc1 3f\n\t"
    "s_waitcnt lgkmcnt(0)\n\t"
    "s_branch 2b\n\t"
    "3:\n\t"
    "v_add_f32 v48, v48, v80\n\t"
    "v_add_f32 v49, v49, v81\n\t"
    "v_add_f32 v50, v50, v82\n\t"
    "v_add_f32 v51, v51, v83\n\t"
    "v_add_f32 v52, v52, v84\n\t"
    "v_add_f32 v53, v53, v85\n\t"
    "v_add_f32 v54, v54, v86\n\t"
    "v_add_f32 v55, v55, v87\n\t"
    "s_waitcnt lgkmcnt(0)\n\t"
    "v_add_f32 v56, v56, v88\n\t"
    "v_add_f32 v57, v57, v89\n\t"
    "v_add_f32 v58, v58, v90\n\t"
    "v_add_f32 v59, v59, v91\n\t"
    "v_add_f32 v60, v60, v92\n\t"
    "v_add_f32 v61, v61, v93\n\t"
    "v_add_f32 v62, v62, v94\n\t"
    "v_add_f32 v63, v63, v95\n\t"
    "v_mul_f32 v40, %[nl], v48\n\t"
    "v_mul_f32 v41, %[nl], v49\n\t"
    "v_mul_f32 v42, %[nl], v50\n\t"
    "v_mul_f32 v43, %[nl], v51\n\t"
    "v_mul_f32 v44, %[nl], v56\n\t"
    "v_mul_f32 v45, %[nl], v57\n\t"
    "v_mul_f32 v46, %[nl], v58\n\t"
    "v_mul_f32 v47, %[nl], v59\n\t"
    "v_exp_f32 v40, v40\n\t"
    "v_exp_f32 v41, v41\n\t"
    "v_exp_f32 v42, v42\n\t"
    "v_exp_f32 v43, v43\n\t"
    "v_exp_f32 v44, v44\n\t"
    "v_exp_f32 v45, v45\n\t"
    "v_exp_f32 v46, v46\n\t"
    "v_exp_f32 v47, v47\n\t"
    "v_add_f32 v40, 1.0, v40\n\t"
    "v_add_f32 v41, 1.0, v41\n\t"
    "v_add_f32 v42, 1.0, v42\n\t"
    "v_add_f32 v43, 1.0, v43\n\t"
    "v_add_f32 v44, 1.0, v44\n\t"
    "v_add_f32 v45, 1.0, v45\n\t"
    "v_add_f32 v46, 1.0, v46\n\t"
    "v_add_f32 v47, 1.0, v47\n\t"
    "v_rcp_f32 v40, v40\n\t"
    "v_rcp_f32 v41, v41\n\t"
    "v_rcp_f32 v42, v42\n\t"
    "v_rcp_f32 v43, v43\n\t"
    "v_rcp_f32 v44, v44\n\t"
    "v_rcp_f32 v45, v45\n\t"
    "v_rcp_f32 v46, v46\n\t"
    "v_rcp_f32 v47, v47\n\t"
    "v_mul_f32 v40, v48, v40\n\t"
    "v_mul_f32 v41, v49, v41\n\t"
    "v_mul_f32 v42, v50, v42\n\t"
    "v_mul_f32 v43, v51, v43\n\t"
    "v_mul_f32 v44, v56, v44\n\t"
    "v_mul_f32 v45, v57, v45\n\t"
    "v_mul_f32 v46, v58, v46\n\t"
    "v_mul_f32 v47, v59, v47\n\t"
    "v_mul_f32 v40, v40, v52\n\t"
    "v_mul_f32 v41, v41, v53\n\t"
    "v_mul_f32 v42, v42, v54\n\t"
    "v_mul_f32 v43, v43, v55\n\t"
    "v_mul_f32 v44, v44, v60\n\t"
    "v_mul_f32 v45, v45, v61\n\t"
    "v_mul_f32 v46, v46, v62\n\t"
    "v_mul_f32 v47, v47, v63\n\t"
    "v_cvt_pk_bf16_f32 v48, v40, v41\n\t"
    "v_cvt_pk_bf16_f32 v49, v42, v43\n\t"
    "v_cvt_pk_bf16_f32 v50, v44, v45\n\t"
    "v_cvt_pk_bf16_f32 v51, v46, v47\n\t"
    "s_nop 1\n\t"
    "v_permlane16_swap_b32 v48, v50\n\t"
    "v_permlane16_swap_b32 v49, v51\n\t"
    "global_store_dwordx4 %[co0], v[48:51], s[36:37] sc1\n\t"
    "s_add_u32 s36, s36, %[ab]\n\t"
    "s_addc_u32 s37, s37, 0\n\t"
    "s_lshr_b32 %[pp], %[pp], 8\n\t"
    "s_cbranch_scc1 1b\n\t"
    "v_mov_b32 v72, %[cn]\n\t"
    "v_mov_b32 v73, 1\n\t"
    "s_add_u32 %[st], %[b], %[po]\n\t"
    "v_mov_b32 v74, %[st]\n\t"
    "v_mov_b32 v75, 0\n\t"
    "s_waitcnt vmcnt(0)\n\t"
    "s_mov_b64 exec, 1\n\t"
    "ds_add_rtn_u32 v73, v72, v73\n\t"
    "s_waitcnt lgkmcnt(0)\n\t"
    "v_readfirstlane_b32 %[st], v73\n\t"
    "s_and_b32 %[st], %[st], 3\n\t"
    "s_cmp_lg_u32 %[st], 3\n\t"
    "s_cbranch_scc1 61f\n\t"
    "global_store_dword v75, v74, %[pg] sc1\n\t"
    "61:\n\t"
    "s_mov_b64 exec, -1\n\t"
    "s_mov_b32 %[pp], 0\n\t"
    "s_cmp_lt_i32 %[b], %[ns]\n\t"
    "s_cbranch_scc1 1b\n\t"
    "s_branch 9f\n\t"
    "7:\n\t"
    "v_mov_b32 v72, %[cn]\n\t"
    "v_mov_b32 v73, 1\n\t"
    "s_add_u32 %[st], %[b], %[po]\n\t"
    "v_mov_b32 v74, %[st]\n\t"
    "v_mov_b32 v75, 0\n\t"
    "s_waitcnt vmcnt(0)\n\t"
    "s_mov_b64 exec, 1\n\t"
    "ds_add_rtn_u32 v73, v72, v73\n\t"
    "s_waitcnt lgkmcnt(0)\n\t"
    "v_readfirstlane_b32 %[st], v73\n\t"
    "s_and_b32 %[st], %[st], 3\n\t"
    "s_cmp_lg_u32 %[st], 3\n\t"
    "s_cbranch_scc1 61f\n\t"
    "global_store_dword v75, v74, %[pg] sc1\n\t"
    "61:\n\t"
    "s_mov_b64 exec, -1\n\t"
    "s_mov_b32 %[pp], 0\n\t"
    "s_branch 8b\n\t"
    "9:"
    : [ea] "+&v"(ea), [da] "+&v"(da), [b] "+&s"(b), [st] "=&s"(st), "+{s[36:37]}"(p0), [pp] "+&s"(pp)
    : [ns] "s"(ns), [ab] "s"(ab), [rw] "v"(rw), [fa] "v"(fa), "{v[128:159]}"(W[0]), "{v[160:191]}"(W[1]), "{v[192:223]}"(W[2]), "{v[224:255]}"(W[3]), [po] "s"(po), [cn] "s"(cn), [pg] "s"(pg), [co0] "v"(co0), [nl] "s"(-LOG2E)
    : "v39", "v40", "v41", "v42", "v43", "v44", "v45", "v46", "v47", "v48", "v49", "v50", "v51", "v52", "v53", "v54", "v55", "v56", "v57", "v58", "v59", "v60", "v61", "v62", "v63", "v72", "v73", "v74", "v75", "v80", "v81", "v82", "v83", "v84", "v85", "v86", "v87", "v88", "v89", "v90", "v91", "v92", "v93", "v94", "v95", "v96", "v97", "v98", "v99", "v100", "v101", "v102", "v103", "v104", "v105", "v106", "v107", "v108", "v109", "v110", "v111", "v112", "v113", "v114", "v115", "v116", "v117", "v118", "v119", "v120", "v121", "v122", "v123", "v124", "v125", "v126", "v127", "vcc", "scc", "m0", "memory");
}
__device__ __forceinline__ void paced_st2(const u32x32 (&W)[4], unsigned ea, unsigned da, int b, int ns, unsigned ab, unsigned rw, unsigned fa,
    const char* p0, unsigned pp, int po, unsigned cn, const char* pg, unsigned co0, unsigned co1) {
  unsigned st;
  asm volatile("1:\n\t"
    "s_barrier\n\t"
    "ds_read_b128 v[96:99], %[ea] offset:0\n\t"
    "ds_read_b128 v[100:103], %[ea] offset:2048\n\t"
    "ds_read_b128 v[104:107], %[da] offset:0\n\t"
    "ds_read_b128 v[108:111], %[da] offset:2048\n\t"
    "ds_read_b128 v[112:115], %[ea] offset:4096\n\t"
    "ds_read_b128 v[116:119], %[ea] offset:6144\n\t"
    "s_waitcnt lgkmcnt(4)\n\t"
    "v_mfma_f32_16x16x32_bf16 v[80:83], v[128:131], v[96:99], 0\n\t"
    "ds_read_b128 v[120:123], %[da] offset:4096\n\t"
    "v_mfma_f32_16x16x32_bf16 v[84:87], v[132:135], v[96:99], 0\n\t"
    "ds_read_b128 v[124:127], %[da] offset:6144\n\t"
    "v_mfma_f32_16x16x32_bf16 v[88:91], v[128:131], v[100:103], 0\n\t"
    "v_mfma_f32_16x16x32_bf16 v[92:95], v[132:135], v[100:103], 0\n\t"
    "s_barrier\n\t"
    "s_waitcnt lgkmcnt(4)\n\t"
    "v_mfma_f32_16x16x32_bf16 v[80:83], v[136:139], v[104:107], v[80:83]\n\t"
    "ds_read_b128 v[96:99], %[ea] offset:8192\n\t"
    "v_mfma_f32_16x16x32_bf16 v[84:87], v[140:143], v[104:107], v[84:87]\n\t"
    "ds_read_b128 v[100:103], %[ea] offset:10240\n\t"
    "v_mfma_f32_16x16x32_bf16 v[88:91], v[136:139], v[108:111], v[88:91]\n\t"
    "v_mfma_f32_16x16x32_bf16 v[92:95], v[140:143], v[108:111], v[92:95]\n\t"
    "s_waitcnt lgkmcnt(4)\n\t"
    "v_mfma_f32_16x16x32_bf16 v[80:83], v[144:147], v[112:115], v[80:83]\n\t"
    "ds_read_b128 v[104:107], %[da] offset:8192\n\t"
    "v_mfma_f32_16x16x32_bf16 v[84:87], v[148:151], v[112:115], v[84:87]\n\t"
    "ds_read_b128 v[108:111], %[da] offset:10240\n\t"
    "v_mfma_f32_16x16x32_bf16 v[88:91], v[144:147], v[116:119], v[88:91]\n\t"
    "v_mfma_f32_16x16x32_bf16 v[92:95], v[148:151], v[116:119], v[92:95]\n\t"
    "s_waitcnt lgkmcnt(4)\n\t"
    "v_mfma_f32_16x16x32_bf16 v[80:83], v[152:155], v[120:123], v[80:83]\n\t"
    "ds_read_b128 v[112:115], %[ea] offset:12288\n\t"
    "v_mfma_f32_16x16x32_bf16 v[84:87], v[156:159], v[120:123], v[84:87]\n\t"
    "ds_read_b128 v[116:119], %[ea] offset:14336\n\t"
    "v_mfma_f32_16x16x32_bf16 v[88:91], v[152:155], v[124:127], v[88:91]\n\t"
    "v_mfma_f32_16x16x32_bf16 v[92:95], v[156:159], v[124:127], v[92:95]\n\t"
    "s_cmp_lg_u32 %[pp], 0\n\t"
    "s_cbranch_scc1 7f\n\t"
    "8:\n\t"
    "s_waitcnt lgkmcnt(4)\n\t"
    "v_mfma_f32_16x16x32_bf16 v[80:83], v[160:163], v[96:99], v[80:83]\n\t"
    "ds_read_b128 v[120:123], %[da] offset:12288\n\t"
    "v_mfma_f32_16x16x32_bf16 v[84:87], v[164:167], v[96:99], v[84:87]\n\t"
    "ds_read_b128 v[124:127], %[da] offset:14336\n\t"
    "v_mfma_f32_16x16x32_bf16 v[88:91], v[160:163], v[100:103], v[88:91]\n\t"
    "v_mfma_f32_16x16x32_bf16 v[92:95], v[164:167], v[100:103], v[92:95]\n\t"
    "s_barrier\n\t"
    "s_waitcnt lgkmcnt(4)\n\t"
    "v_mfma_f32_16x16x32_bf16 v[80:83], v[168:171], v[104:107], v[80:83]\n\t"
    "ds_read_b128 v[96:99], %[ea] offset:16384\n\t"
    "v_mfma_f32_16x16x32_bf16 v[84:87], v[172:175], v[104:107], v[84:87]\n\t"
    "ds_read_b128 v[100:103], %[ea] offset:18432\n\t"
    "v_mfma_f32_16x16x32_bf16 v[88:91], v[168:171], v[108:111], v[88:91]\n\t"
    "v_mfma_f32_16x16x32_bf16 v[92:95], v[172:175], v[108:111], v[92:95]\n\t"
    "s_waitcnt lgkmcnt(4)\n\t"
    "v_mfma_f32_16x16x32_bf16 v[80:83], v[176:179], v[112:115], v[80:83]\n\t"
    "ds_read_b128 v[104:107], %[da] offset:16384\n\t"
    "v_mfma_f32_16x16x32_bf16 v[84:87], v[180:183], v[112:115], v[84:87]\n\t"
    "ds_read_b128 v[108:111], %[da] offset:18432\n\t"
    "v_mfma_f32_16x16x32_bf16 v[88:91], v[176:179], v[116:119], v[88:91]\n\t"
    "v_mfma_f32_16x16x32_bf16 v[92:95], v[180:183], v[116:119], v[92:95]\n\t"
    "s_waitcnt lgkmcnt(4)\n\t"
    "v_mfma_f32_16x16x32_bf16 v[80:83], v[184:187], v[120:123], v[80:83]\n\t"
    "ds_read_b128 v[112:115], %[ea] offset:20480\n\t"
    "v_mfma_f32_16x16x32_bf16 v[84:87], v[188:191], v[120:123], v[84:87]\n\t"
    "ds_read_b128 v[116:119], %[ea] offset:22528\n\t"
    "v_mfma_f32_16x16x32_bf16 v[88:91], v[184:187], v[124:127], v[88:91]\n\t"
    "v_mfma_f32_16x16x32_bf16 v[92:95], v[188:191], v[124:127], v[92:95]\n\t"
    "s_waitcnt lgkmcnt(4)\n\t"
    "v_mfma_f32_16x16x32_bf16 v[80:83], v[192:195], v[96:99], v[80:83]\n\t"
    "ds_read_b128 v[120:123], %[da] offset:20480\n\t"
    "v_mfma_f32_16x16x32_bf16 v[84:87], v[196:199], v[96:99], v[84:87]\n\t"
    "ds_read_b128 v[124:127], %[da] offset:22528\n\t"
    "v_mfma_f32_16x16x32_bf16 v[88:91], v[192:195], v[100:103], v[88:91]\n\t"
    "v_mfma_f32_16x16x32_bf16 v[92:95], v[196:199], v[100:103], v[92:95]\n\t"
    "s_barrier\n\t"
    "s_waitcnt lgkmcnt(4)\n\t"
    "v_mfma_f32_16x16x32_bf16 v[80:83], v[200:203], v[104:107], v[80:83]\n\t"
    "ds_read_b128 v[96:99], %[ea] offset:24576\n\t"
    "v_mfma_f32_16x16x32_bf16 v[84:87], v[204:207], v[104:107], v[84:87]\n\t"
    "ds_read_b128 v[100:103], %[ea] offset:26624\n\t"
    "v_mfma_f32_16x16x32_bf16 v[88:91], v[200:203], v[108:111], v[88:91]\n\t"
    "v_mfma_f32_16x16x32_bf16 v[92:95], v[204:207], v[108:111], v[92:95]\n\t"
    "s_waitcnt lgkmcnt(4)\n\t"
    "v_mfma_f32_16x16x32_bf16 v[80:83], v[208:211], v[112:115], v[80:83]\n\t"
    "ds_read_b128 v[104:107], %[da] offset:24576\n\t"
    "v_mfma_f32_16x16x32_bf16 v[84:87], v[212:215], v[112:115], v[84:87]\n\t"
    "ds_read_b128 v[108:111], %[da] offset:26624\n\t"
    "v_mfma_f32_16x16x32_bf16 v[88:91], v[208:211], v[116:119], v[88:91]\n\t"
    "v_mfma_f32_16x16x32_bf16 v[92:95], v[212:215], v[116:119], v[92:95]\n\t"
    "s_waitcnt lgkmcnt(4)\n\t"
    "v_mfma_f32_16x16x32_bf16 v[80:83], v[216:219], v[120:123], v[80:83]\n\t"
    "ds_read_b128 v[112:115], %[ea] offset:28672\n\t"
    "v_mfma_f32_16x16x32_bf16 v[84:87], v[220:223], v[120:123], v[84:87]\n\t"
    "ds_read_b128 v[116:119], %[ea] offset:30720\n\t"
    "v_mfma_f32_16x16x32_bf16 v[88:91], v[216:219], v[124:127], v[88:91]\n\t"
    "v_mfma_f32_16x16x32_bf16 v[92:95], v[220:223], v[124:127], v[92:95]\n\t"
    "s_waitcnt lgkmcnt(4)\n\t"
    "v_mfma_f32_16x16x32_bf16 v[80:83], v[224:227], v[96:99], v[80:83]\n\t"
    "ds_read_b128 v[120:123], %[da] offset:28672\n\t"
    "v_mfma_f32_16x16x32_bf16 v[84:87], v[228:231], v[96:99], v[84:87]\n\t"
    "ds_read_b128 v[124:127], %[da] offset:30720\n\t"
    "v_mfma_f32_16x16x32_bf16 v[88:91], v[224:227], v[100:103], v[88:91]\n\t"
    "v_mfma_f32_16x16x32_bf16 v[92:95], v[228:231], v[100:103], v[92:95]\n\t"
    "s_waitcnt lgkmcnt(4)\n\t"
    "v_mfma_f32_16x16x32_bf16 v[80:83], v[232:235], v[104:107], v[80:83]\n\t"
    "v_mfma_f32_16x16x32_bf16 v[84:87], v[236:239], v[104:107], v[84:87]\n\t"
    "v_mfma_f32_16x16x32_bf16 v[88:91], v[232:235], v[108:111], v[88:91]\n\t"
    "v_xor_b32 %[ea], 0x10000, %[ea]\n\t"
    "v_mfma_f32_16x16x32_bf16 v[92:95], v[236:239], v[108:111], v[92:95]\n\t"
    "v_xor_b32 %[da], 0x10000, %[da]\n\t"
    "s_waitcnt lgkmcnt(2)\n\t"
    "v_mfma_f32_16x16x32_bf16 v[80:83], v[240:243], v[112:115], v[80:83]\n\t"
    "v_mfma_f32_16x16x32_bf16 v[84:87], v[244:247], v[112:115], v[84:87]\n\t"
    "v_mfma_f32_16x16x32_bf16 v[88:91], v[240:243], v[116:119], v[88:91]\n\t"
    "s_add_u32 %[b], %[b], 1\n\t"
    "v_mfma_f32_16x16x32_bf16 v[92:95], v[244:247], v[116:119], v[92:95]\n\t"
    "s_waitcnt lgkmcnt(0)\n\t"
    "v_mfma_f32_16x16x32_bf16 v[80:83], v[248:251], v[120:123], v[80:83]\n\t"
    "v_mfma_f32_16x16x32_bf16 v[84:87], v[252:255], v[120:123], v[84:87]\n\t"
    "v_mfma_f32_16x16x32_bf16 v[88:91], v[248:251], v[124:127], v[88:91]\n\t"
    "v_mfma_f32_16x16x32_bf16 v[92:95], v[252:255], v[124:127], v[92:95]\n\t"
    "s_nop 6\n\t"
    "v_cvt_pk_bf16_f32 v40, v80, v81\n\t"
    "v_cvt_pk_bf16_f32 v41, v82, v83\n\t"
    "v_cvt_pk_bf16_f32 v42, v84, v85\n\t"
    "v_cvt_pk_bf16_f32 v43, v86, v87\n\t"
    "global_store_dwordx4 %[co0], v[40:43], s[36:37] sc1\n\t"
    "v_cvt_pk_bf16_f32 v44, v88, v89\n\t"
    "v_cvt_pk_bf16_f32 v45, v90, v91\n\t"
    "v_cvt_pk_bf16_f32 v46, v92, v93\n\t"
    "v_cvt_pk_bf16_f32 v47, v94, v95\n\t"
    "global_store_dwordx4 %[co1], v[44:47], s[36:37] sc1\n\t"
    "2:\n\t"
    "ds_read_b32 v39, %[fa]\n\t"
    "ds_read_b128 v[48:51], %[rw] offset:0\n\t"
    "ds_read_b128 v[52:55], %[rw] offset:1024\n\t"
    "ds_read_b128 v[56:59], %[rw] offset:2048\n\t"
    "ds_read_b128 v[60:63], %[rw] offset:3072\n\t"
    "s_waitcnt lgkmcnt(2)\n\t"
    "v_readfirstlane_b32 %[pp], v39\n\t"
    "s_and_b32 %[st], %[pp], 0xff\n\t"
    "s_cmp_eq_u32 %[st], %[b]\n\t"
    "s_cbranch_scc1 3f\n\t"
    "s_waitcnt lgkmcnt(0)\n\t"
    "s_branch 2b\n\t"
    "3:\n\t"
    "v_cvt_pk_bf16_f32 v48, v48, v49\n\t"
    "v_cvt_pk_bf16_f32 v49, v50, v51\n\t"
    "v_cvt_pk_bf16_f32 v50, v52, v53\n\t"
    "v_cvt_pk_bf16_f32 v51, v54, v55\n\t"
    "global_store_dwordx4 %[co0], v[48:51], s[36:37] offset:2048 sc1\n\t"
    "s_waitcnt lgkmcnt(0)\n\t"
    "v_cvt_pk_bf16_f32 v56, v56, v57\n\t"
    "v_cvt_pk_bf16_f32 v57, v58, v59\n\t"
    "v_cvt_pk_bf16_f32 v58, v60, v61\n\t"
    "v_cvt_pk_bf16_f32 v59, v62, v63\n\t"
    "global_store_dwordx4 %[co1], v[56:59], s[36:37] offset:2048 sc1\n\t"
    "s_add_u32 s36, s36, %[ab]\n\t"
    "s_addc_u32 s37, s37, 0\n\t"
    "s_lshr_b32 %[pp], %[pp], 8\n\t"
    "s_cbranch_scc1 1b\n\t"
    "v_mov_b32 v72, %[cn]\n\t"
    "v_mov_b32 v73, 1\n\t"
    "s_add_u32 %[st], %[b], %[po]\n\t"
    "v_mov_b32 v74, %[st]\n\t"
    "v_mov_b32 v75, 0\n\t"
    "s_waitcnt vmcnt(0)\n\t"
    "s_mov_b64 exec, 1\n\t"
    "ds_add_rtn_u32 v73, v72, v73\n\t"
    "s_waitcnt lgkmcnt(0)\n\t"
    "v_readfirstlane_b32 %[st], v73\n\t"
    "s_and_b32 %[st], %[st], 3\n\t"
    "s_cmp_lg_u32 %[st], 3\n\t"
    "s_cbranch_scc1 61f\n\t"
    "global_store_dword v75, v74, %[pg] sc1\n\t"
    "61:\n\t"
    "s_mov_b64 exec, -1\n\t"
    "s_mov_b32 %[pp], 0\n\t"
    "s_cmp_lt_i32 %[b], %[ns]\n\t"
    "s_cbranch_scc1 1b\n\t"
    "s_branch 9f\n\t"
    "7:\n\t"
    "v_mov_b32 v72, %[cn]\n\t"
    "v_mov_b32 v73, 1\n\t"
    "s_add_u32 %[st], %[b], %[po]\n\t"
    "v_mov_b32 v74, %[st]\n\t"
    "v_mov_b32 v75, 0\n\t"
    "s_waitcnt vmcnt(0)\n\t"
    "s_mov_b64 exec, 1\n\t"
    "ds_add_rtn_u32 v73, v72, v73\n\t"
    "s_waitcnt lgkmcnt(0)\n\t"
    "v_readfirstlane_b32 %[st], v73\n\t"
    "s_and_b32 %[st], %[st], 3\n\t"
    "s_cmp_lg_u32 %[st], 3\n\t"
    "s_cbranch_scc1 61f\n\t"
    "global_store_dword v75, v74, %[pg] sc1\n\t"
    "61:\n\t"
    "s_mov_b64 exec, -1\n\t"
    "s_mov_b32 %[pp], 0\n\t"
    "s_branch 8b\n\t"
    "9:"
    : [ea] "+&v"(ea), [da] "+&v"(da), [b] "+&s"(b), [st] "=&s"(st), "+{s[36:37]}"(p0), [pp] "+&s"(pp)
    : [ns] "s"(ns), [ab] "s"(ab), [rw] "v"(rw), [fa] "v"(fa), "{v[128:159]}"(W[0]), "{v[160:191]}"(W[1]), "{v[192:223]}"(W[2]), "{v[224:255]}"(W[3]), [po] "s"(po), [cn] "s"(cn), [pg] "s"(pg), [co0] "v"(co0), [co1] "v"(co1)
    : "v39", "v40", "v41", "v42", "v43", "v44", "v45", "v46", "v47", "v48", "v49", "v50", "v51", "v52", "v53", "v54", "v55", "v56", "v57", "v58", "v59", "v60", "v61", "v62", "v63", "v72", "v73", "v74", "v75", "v80", "v81", "v82", "v83", "v84", "v85", "v86", "v87", "v88", "v89", "v90", "v91", "v92", "v93", "v94", "v95", "v96", "v97", "v98", "v99", "v100", "v101", "v102", "v103", "v104", "v105", "v106", "v107", "v108", "v109", "v110", "v111", "v112", "v113", "v114", "v115", "v116", "v117", "v118", "v119", "v120", "v121", "v122", "v123", "v124", "v125", "v126", "v127", "vcc", "scc", "m0", "memory");
}
__device__ __forceinline__ void fin_ld(const u32x16& pd, unsigned rw, unsigned fa, unsigned fv) {
  asm volatile("ds_write_b128 %[rw], v[80:83] offset:0\n\t"
    "ds_write_b128 %[rw], v[84:87] offset:1024\n\t"
    "ds_write_b128 %[rw], v[88:91] offset:2048\n\t"
    "ds_write_b128 %[rw], v[92:95] offset:3072\n\t"
    "ds_write_b32 %[fa], %[fv]"
    :
    : [rw] "v"(rw), "{v[80:95]}"(pd), [fa] "v"(fa), [fv] "v"(fv)
    : "memory");
}
__device__ __forceinline__ void fin_st0(const u32x16& pd, unsigned rw, unsigned fa, unsigned fv, const char* cb, unsigned co0, unsigned co1) {
  asm volatile("1:\n\t"
    "ds_read_b32 v39, %[fa]\n\t"
    "ds_read_b128 v[48:51], %[rw] offset:0\n\t"
    "ds_read_b128 v[52:55], %[rw] offset:1024\n\t"
    "ds_read_b128 v[56:59], %[rw] offset:2048\n\t"
    "ds_read_b128 v[60:63], %[rw] offset:3072\n\t"
    "s_waitcnt lgkmcnt(2)\n\t"
    "v_cmp_ne_u32 vcc, v39, %[fv]\n\t"
    "s_cbranch_vccz 2f\n\t"
    "s_waitcnt lgkmcnt(0)\n\t"
    "s_branch 1b\n\t"
    "2:\n\t"
    "v_add_f32 v48, v48, v80\n\t"
    "v_add_f32 v49, v49, v81\n\t"
    "v_add_f32 v50, v50, v82\n\t"
    "v_add_f32 v51, v51, v83\n\t"
    "v_add_f32 v52, v52, v84\n\t"
    "v_add_f32 v53, v53, v85\n\t"
    "v_add_f32 v54, v54, v86\n\t"
    "v_add_f32 v55, v55, v87\n\t"
    "v_cvt_pk_bf16_f32 v40, v48, v49\n\t"
    "v_cvt_pk_bf16_f32 v41, v50, v51\n\t"
    "v_cvt_pk_bf16_f32 v42, v52, v53\n\t"
    "v_cvt_pk_bf16_f32 v43, v54, v55\n\t"
    "global_store_dwordx4 %[co0], v[40:43], %[cb] sc1\n\t"
    "s_waitcnt lgkmcnt(0)\n\t"
    "v_add_f32 v56, v56, v88\n\t"
    "v_add_f32 v57, v57, v89\n\t"
    "v_add_f32 v58, v58, v90\n\t"
    "v_add_f32 v59, v59, v91\n\t"
    "v_add_f32 v60, v60, v92\n\t"
    "v_add_f32 v61, v61, v93\n\t"
    "v_add_f32 v62, v62, v94\n\t"
    "v_add_f32 v63, v63, v95\n\t"
    "v_cvt_pk_bf16_f32 v44, v56, v57\n\t"
    "v_cvt_pk_bf16_f32 v45, v58, v59\n\t"
    "v_cvt_pk_bf16_f32 v46, v60, v61\n\t"
    "v_cvt_pk_bf16_f32 v47, v62, v63\n\t"
    "global_store_dwordx4 %[co1], v[44:47], %[cb] sc1\n\t"
    "s_nop 1"
    :
    : [rw] "v"(rw), "{v[80:95]}"(pd), [fa] "v"(fa), [fv] "v"(fv), [cb] "s"(cb), [co0] "v"(co0), [co1] "v"(co1)
    : "v39", "v40", "v41", "v42", "v43", "v44", "v45", "v46", "v47", "v48", "v49", "v50", "v51", "v52", "v53", "v54", "v55", "v56", "v57", "v58", "v59", "v60", "v61", "v62", "v63", "vcc", "memory");
}
__device__ __forceinline__ void fin_st1(const u32x16& pd, unsigned rw, unsigned fa, unsigned fv, const char* cb, unsigned co0, unsigned co1) {
  asm volatile("1:\n\t"
    "ds_read_b32 v39, %[fa]\n\t"
    "ds_read_b128 v[48:51], %[rw] offset:0\n\t"
    "ds_read_b128 v[52:55], %[rw] offset:1024\n\t"
    "ds_read_b128 v[56:59], %[rw] offset:2048\n\t"
    "ds_read_b128 v[60:63], %[rw] offset:3072\n\t"
    "s_waitcnt lgkmcnt(2)\n\t"
    "v_cmp_ne_u32 vcc, v39, %[fv]\n\t"
    "s_cbranch_vccz 2f\n\t"
    "s_waitcnt lgkmcnt(0)\n\t"
    "s_branch 1b\n\t"
    "2:\n\t"
    "v_add_f32 v48, v48, v80\n\t"
    "v_add_f32 v49, v49, v81\n\t"
    "v_add_f32 v50, v50, v82\n\t"
    "v_add_f32 v51, v51, v83\n\t"
    "v_add_f32 v52, v52, v84\n\t"
    "v_add_f32 v53, v53, v85\n\t"
    "v_add_f32 v54, v54, v86\n\t"
    "v_add_f32 v55, v55, v87\n\t"
    "s_waitcnt lgkmcnt(0)\n\t"
    "v_add_f32 v56, v56, v88\n\t"
    "v_add_f32 v57, v57, v89\n\t"
    "v_add_f32 v58, v58, v90\n\t"
    "v_add_f32 v59, v59, v91\n\t"
    "v_add_f32 v60, v60, v92\n\t"
    "v_add_f32 v61, v61, v93\n\t"
    "v_add_f32 v62, v62, v94\n\t"
    "v_add_f32 v63, v63, v95\n\t"
    "v_mul_f32 v40, %[nl], v48\n\t"
    "v_mul_f32 v41, %[nl], v49\n\t"
    "v_mul_f32 v42, %[nl], v50\n\t"
    "v_mul_f32 v43, %[nl], v51\n\t"
    "v_mul_f32 v44, %[nl], v56\n\t"
    "v_mul_f32 v45, %[nl], v57\n\t"
    "v_mul_f32 v46, %[nl], v58\n\t"
    "v_mul_f32 v47, %[nl], v59\n\t"
    "v_exp_f32 v40, v40\n\t"
    "v_exp_f32 v41, v41\n\t"
    "v_exp_f32 v42, v42\n\t"
    "v_exp_f32 v43, v43\n\t"
    "v_exp_f32 v44, v44\n\t"
    "v_exp_f32 v45, v45\n\t"
    "v_exp_f32 v46, v46\n\t"
    "v_exp_f32 v47, v47\n\t"
    "v_add_f32 v40, 1.0, v40\n\t"
    "v_add_f32 v41, 1.0, v41\n\t"
    "v_add_f32 v42, 1.0, v42\n\t"
    "v_add_f32 v43, 1.0, v43\n\t"
    "v_add_f32 v44, 1.0, v44\n\t"
    "v_add_f32 v45, 1.0, v45\n\t"
    "v_add_f32 v46, 1.0, v46\n\t"
    "v_add_f32 v47, 1.0, v47\n\t"
    "v_rcp_f32 v40, v40\n\t"
    "v_rcp_f32 v41, v41\n\t"
    "v_rcp_f32 v42, v42\n\t"
    "v_rcp_f32 v43, v43\n\t"
    "v_rcp_f32 v44, v44\n\t"
    "v_rcp_f32 v45, v45\n\t"
    "v_rcp_f32 v46, v46\n\t"
    "v_rcp_f32 v47, v47\n\t"
    "v_mul_f32 v40, v48, v40\n\t"
    "v_mul_f32 v41, v49, v41\n\t"
    "v_mul_f32 v42, v50, v42\n\t"
    "v_mul_f32 v43, v51, v43\n\t"
    "v_mul_f32 v44, v56, v44\n\t"
    "v_mul_f32 v45, v57, v45\n\t"
    "v_mul_f32 v46, v58, v46\n\t"
    "v_mul_f32 v47, v59, v47\n\t"
    "v_mul_f32 v40, v40, v52\n\t"
    "v_mul_f32 v41, v41, v53\n\t"
    "v_mul_f32 v42, v42, v54\n\t"
    "v_mul_f32 v43, v43, v55\n\t"
    "v_mul_f32 v44, v44, v60\n\t"
    "v_mul_f32 v45, v45, v61\n\t"
    "v_mul_f32 v46, v46, v62\n\t"
    "v_mul_f32 v47, v47, v63\n\t"
    "v_cvt_pk_bf16_f32 v48, v40, v41\n\t"
    "v_cvt_pk_bf16_f32 v49, v42, v43\n\t"
    "v_cvt_pk_bf16_f32 v50, v44, v45\n\t"
    "v_cvt_pk_bf16_f32 v51, v46, v47\n\t"
    "s_nop 1\n\t"
    "v_permlane16_swap_b32 v48, v50\n\t"
    "v_permlane16_swap_b32 v49, v51\n\t"
    "global_store_dwordx4 %[co0], v[48:51], %[cb] sc1\n\t"
    "s_nop 1"
    :
    : [rw] "v"(rw), "{v[80:95]}"(pd), [fa] "v"(fa), [fv] "v"(fv), [cb] "s"(cb), [co0] "v"(co0), [nl] "s"(-LOG2E)
    : "v39", "v40", "v41", "v42", "v43", "v44", "v45", "v46", "v47", "v48", "v49", "v50", "v51", "v52", "v53", "v54", "v55", "v56", "v57", "v58", "v59", "v60", "v61", "v62", "v63", "vcc", "memory");
}
__device__ __forceinline__ void fin_st2(const u32x16& pd, unsigned rw, unsigned fa, unsigned fv, const char* cb, unsigned co0, unsigned co1) {
  asm volatile("s_nop 1\n\t"
    "v_cvt_pk_bf16_f32 v40, v80, v81\n\t"
    "v_cvt_pk_bf16_f32 v41, v82, v83\n\t"
    "v_cvt_pk_bf16_f32 v42, v84, v85\n\t"
    "v_cvt_pk_bf16_f32 v43, v86, v87\n\t"
    "global_store_dwordx4 %[co0], v[40:43], %[cb] sc1\n\t"
    "v_cvt_pk_bf16_f32 v44, v88, v89\n\t"
    "v_cvt_pk_bf16_f32 v45, v90, v91\n\t"
    "v_cvt_pk_bf16_f32 v46, v92, v93\n\t"
    "v_cvt_pk_bf16_f32 v47, v94, v95\n\t"
    "global_store_dwordx4 %[co1], v[44:47], %[cb] sc1\n\t"
    "1:\n\t"
    "ds_read_b32 v39, %[fa]\n\t"
    "ds_read_b128 v[48:51], %[rw] offset:0\n\t"
    "ds_read_b128 v[52:55], %[rw] offset:1024\n\t"
    "ds_read_b128 v[56:59], %[rw] offset:2048\n\t"
    "ds_read_b128 v[60:63], %[rw] offset:3072\n\t"
    "s_waitcnt lgkmcnt(2)\n\t"
    "v_cmp_ne_u32 vcc, v39, %[fv]\n\t"
    "s_cbranch_vccz 2f\n\t"
    "s_waitcnt lgkmcnt(0)\n\t"
    "s_branch 1b\n\t"
    "2:\n\t"
    "v_cvt_pk_bf16_f32 v48, v48, v49\n\t"
    "v_cvt_pk_bf16_f32 v49, v50, v51\n\t"
    "v_cvt_pk_bf16_f32 v50, v52, v53\n\t"
    "v_cvt_pk_bf16_f32 v51, v54, v55\n\t"
    "global_store_dwordx4 %[co0], v[48:51], %[cb] offset:2048 sc1\n\t"
    "s_waitcnt lgkmcnt(0)\n\t"
    "v_cvt_pk_bf16_f32 v56, v56, v57\n\t"
    "v_cvt_pk_bf16_f32 v57, v58, v59\n\t"
    "v_cvt_pk_bf16_f32 v58, v60, v61\n\t"
    "v_cvt_pk_bf16_f32 v59, v62, v63\n\t"
    "global_store_dwordx4 %[co1], v[56:59], %[cb] offset:2048 sc1\n\t"
    "s_nop 1"
    :
    : [rw] "v"(rw), "{v[80:95]}"(pd), [fa] "v"(fa), [fv] "v"(fv), [cb] "s"(cb), [co0] "v"(co0), [co1] "v"(co1)
    : "v39", "v40", "v41", "v42", "v43", "v44", "v45", "v46", "v47", "v48", "v49", "v50", "v51", "v52", "v53", "v54", "v55", "v56", "v57", "v58", "v59", "v60", "v61", "v62", "v63", "vcc", "memory");
}
template <int R> __device__ __forceinline__ void gw_load(u32x32& w, unsigned a0, unsigned a1);
template <> __device__ __forceinline__ void gw_load<0>(u32x32& w, unsigned a0, unsigned a1) {
  asm volatile("ds_read_b64_tr_b16 v[128:129], %1 offset:0\n\t"
    "ds_read_b64_tr_b16 v[130:131], %1 offset:512\n\t"
    "ds_read_b64_tr_b16 v[132:133], %2 offset:0\n\t"
    "ds_read_b64_tr_b16 v[134:135], %2 offset:512\n\t"
    "ds_read_b64_tr_b16 v[136:137], %1 offset:4096\n\t"
    "ds_read_b64_tr_b16 v[138:139], %1 offset:4608\n\t"
    "ds_read_b64_tr_b16 v[140:141], %2 offset:4096\n\t"
    "ds_read_b64_tr_b16 v[142:143], %2 offset:4608\n\t"
    "s_waitcnt lgkmcnt(0)\n\t"
    "ds_read_b64_tr_b16 v[144:145], %1 offset:16384\n\t"
    "ds_read_b64_tr_b16 v[146:147], %1 offset:16896\n\t"
    "ds_read_b64_tr_b16 v[148:149], %2 offset:16384\n\t"
    "ds_read_b64_tr_b16 v[150:151], %2 offset:16896\n\t"
    "ds_read_b64_tr_b16 v[152:153], %1 offset:20480\n\t"
    "ds_read_b64_tr_b16 v[154:155], %1 offset:20992\n\t"
    "ds_read_b64_tr_b16 v[156:157], %2 offset:20480\n\t"
    "ds_read_b64_tr_b16 v[158:159], %2 offset:20992\n\t"
    "s_waitcnt lgkmcnt(0)"
    : "=&{v[128:159]}"(w) : "v"(a0), "v"(a1) : "memory");
}
template <> __device__ __forceinline__ void gw_load<1>(u32x32& w, unsigned a0, unsigned a1) {
  asm volatile("ds_read_b64_tr_b16 v[160:161], %1 offset:0\n\t"
    "ds_read_b64_tr_b16 v[162:163], %1 offset:512\n\t"
    "ds_read_b64_tr_b16 v[164:165], %2 offset:0\n\t"
    "ds_read_b64_tr_b16 v[166:167], %2 offset:512\n\t"
    "ds_read_b64_tr_b16 v[168:169], %1 offset:4096\n\t"
    "ds_read_b64_tr_b16 v[170:171], %1 offset:4608\n\t"
    "ds_read_b64_tr_b16 v[172:173], %2 offset:4096\n\t"
    "ds_read_b64_tr_b16 v[174:175], %2 offset:4608\n\t"
    "s_waitcnt lgkmcnt(0)\n\t"
    "ds_read_b64_tr_b16 v[176:177], %1 offset:16384\n\t"
    "ds_read_b64_tr_b16 v[178:179], %1 offset:16896\n\t"
    "ds_read_b64_tr_b16 v[180:181], %2 offset:16384\n\t"
    "ds_read_b64_tr_b16 v[182:183], %2 offset:16896\n\t"
    "ds_read_b64_tr_b16 v[184:185], %1 offset:20480\n\t"
    "ds_read_b64_tr_b16 v[186:187], %1 offset:20992\n\t"
    "ds_read_b64_tr_b16 v[188:189], %2 offset:20480\n\t"
    "ds_read_b64_tr_b16 v[190:191], %2 offset:20992\n\t"
    "s_waitcnt lgkmcnt(0)"
    : "=&{v[160:191]}"(w) : "v"(a0), "v"(a1) : "memory");
}
template <> __device__ __forceinline__ void gw_load<2>(u32x32& w, unsigned a0, unsigned a1) {
  asm volatile("ds_read_b64_tr_b16 v[192:193], %1 offset:0\n\t"
    "ds_read_b64_tr_b16 v[194:195], %1 offset:512\n\t"
    "ds_read_b64_tr_b16 v[196:197], %2 offset:0\n\t"
    "ds_read_b64_tr_b16 v[198:199], %2 offset:512\n\t"
    "ds_read_b64_tr_b16 v[200:201], %1 offset:4096\n\t"
    "ds_read_b64_tr_b16 v[202:203], %1 offset:4608\n\t"
    "ds_read_b64_tr_b16 v[204:205], %2 offset:4096\n\t"
    "ds_read_b64_tr_b16 v[206:207], %2 offset:4608\n\t"
    "s_waitcnt lgkmcnt(0)\n\t"
    "ds_read_b64_tr_b16 v[208:209], %1 offset:16384\n\t"
    "ds_read_b64_tr_b16 v[210:211], %1 offset:16896\n\t"
    "ds_read_b64_tr_b16 v[212:213], %2 offset:16384\n\t"
    "ds_read_b64_tr_b16 v[214:215], %2 offset:16896\n\t"
    "ds_read_b64_tr_b16 v[216:217], %1 offset:20480\n\t"
    "ds_read_b64_tr_b16 v[218:219], %1 offset:20992\n\t"
    "ds_read_b64_tr_b16 v[220:221], %2 offset:20480\n\t"
    "ds_read_b64_tr_b16 v[222:223], %2 offset:20992\n\t"
    "s_waitcnt lgkmcnt(0)"
    : "=&{v[192:223]}"(w) : "v"(a0), "v"(a1) : "memory");
}
template <> __device__ __forceinline__ void gw_load<3>(u32x32& w, unsigned a0, unsigned a1) {
  asm volatile("ds_read_b64_tr_b16 v[224:225], %1 offset:0\n\t"
    "ds_read_b64_tr_b16 v[226:227], %1 offset:512\n\t"
    "ds_read_b64_tr_b16 v[228:229], %2 offset:0\n\t"
    "ds_read_b64_tr_b16 v[230:231], %2 offset:512\n\t"
    "ds_read_b64_tr_b16 v[232:233], %1 offset:4096\n\t"
    "ds_read_b64_tr_b16 v[234:235], %1 offset:4608\n\t"
    "ds_read_b64_tr_b16 v[236:237], %2 offset:4096\n\t"
    "ds_read_b64_tr_b16 v[238:239], %2 offset:4608\n\t"
    "s_waitcnt lgkmcnt(0)\n\t"
    "ds_read_b64_tr_b16 v[240:241], %1 offset:16384\n\t"
    "ds_read_b64_tr_b16 v[242:243], %1 offset:16896\n\t"
    "ds_read_b64_tr_b16 v[244:245], %2 offset:16384\n\t"
    "ds_read_b64_tr_b16 v[246:247], %2 offset:16896\n\t"
    "ds_read_b64_tr_b16 v[248:249], %1 offset:20480\n\t"
    "ds_read_b64_tr_b16 v[250:251], %1 offset:20992\n\t"
    "ds_read_b64_tr_b16 v[252:253], %2 offset:20480\n\t"
    "ds_read_b64_tr_b16 v[254:255], %2 offset:20992\n\t"
    "s_waitcnt lgkmcnt(0)"
    : "=&{v[224:255]}"(w) : "v"(a0), "v"(a1) : "memory");
}
template <int I, int E, class F> __device__ __forceinline__ void sfor(F&& f) { if constexpr (I < E) { f(std::integral_constant<int, I>{}); sfor<I + 1, E>(f); } }
template <int ROLE, int KQ, class F0, class F>
__device__ __forceinline__ void wload(char* smem, int wave_id_, const u16* W0, const u16* W1, int ldb, u32x32 (&bw)[4], int rot, F0&& hook0, F&& hook, unsigned long long* wts = nullptr) {
  constexpr int ABUF = 65536;
  const int tid = opaque_tid(), lane = tid & 63, wave = WAVE_ID;
  const int kq = KQ == 2 ? wave >> 2 : 0, nq = KQ == 2 ? wave & 3 : wave;
  const int g = lane >> 4, r16 = lane & 15, q4 = r16 >> 2, p4 = lane & 3;
  const unsigned ldsb = __builtin_amdgcn_readfirstlane((unsigned)(size_t)(LAS char*)smem);
  {
    const int srow = 8 * wave + (lane >> 3), ch = (lane & 7) ^ bswz(srow);
    const unsigned voff = (unsigned)(srow * ldb + ch * 8) * 2;
    const char* w0 = uni((const char*)W0); const char* w1 = uni((const char*)(ROLE == R_F1 ? W1 : W0 + 64));
    const size_t kb = (size_t)64 * ldb * 2;
    auto wsrc = [&](int r, int i) {
      const int kbi = KQ == 2 ? (ROLE == R_PART2 ? 0 : (i >> 2) * 8) + ((2 * r + ((i >> 1) & 1)) ^ rot) : 2 * r + (i >> 2);
      return KQ == 2 ? ((i & 1) ? w1 : w0) + kbi * kb + (ROLE == R_PART2 ? (i >> 2) * 256 : 0) : w0 + kbi * kb + (i & 3) * 128;
    };
    auto wstage = [&](int r) {
      const unsigned ld = ldsb + (r & 1) * ABUF + wave * 1024;
#pragma unroll
      for (int i = 0; i < 8; ++i) {
        unsigned keep;
        asm volatile("s_mov_b32 %0, m0\n\ts_mov_b32 m0, %3\n\ts_nop 0\n\tglobal_load_lds_dwordx4 %1, %2" WNT_ "\n\ts_mov_b32 m0, %0"
                     : "=&s"(keep) : "v"(voff), "s"(wsrc(r, i)), "s"(ld + i * 8192) : "memory");
      }
    };
    const int brow = 8 * g + q4, bs = bswz(brow);
    unsigned la[2];
#pragma unroll
    for (int j = 0; j < 2; ++j) {
      int blk, c16, sub;
      if (ROLE == R_F1) { blk = j; c16 = nq * 2 + (p4 >> 1); sub = (p4 & 1) * 8; }
      else { blk = nq >> 1; c16 = (nq & 1) * 4 + p4; sub = j * 8; }
      int set = kq;
      if (ROLE == R_PART2 && TLP) { set = nq >> 1; blk = nq & 1; c16 = kq * 4 + p4; }
      la[j] = (unsigned)((KQ == 2 ? set * 4 + blk : blk) * 8192 + brow * 128 + ((c16 ^ bs) << 4) + sub);
    }
    wstage(0); wstage(1);
    u32x4 raw[2][8];
#pragma unroll
    for (int r = 0; r < 2; ++r)
#pragma unroll
      for (int i = 0; i < 8; ++i) asm volatile("global_load_dwordx4 %0, %1, %2" WNT_ : "=&v"(raw[r][i]) : "v"(voff), "s"(wsrc(r + 2, i)) : "memory");
    sfor<0, 4>([&](auto r_c) __attribute__((always_inline)) {
      constexpr int r = decltype(r_c)::value;
      if (r == 0) WAIT_V(24); else if (r == 1) WAIT_V(16);
      if (r == 3) BAR(); else if (r < 2) { __builtin_amdgcn_s_barrier(); asm volatile("" ::: "memory"); }
      gw_load<r>(bw[r], ldsb + (r & 1) * ABUF + la[0], ldsb + (r & 1) * ABUF + la[1]);
      if (r == 1 || r == 2) {
        u32x4 (&q)[8] = raw[(r + 1) & 1];
        if (r == 1) hook0();
        if (r == 1) asm volatile("s_waitcnt vmcnt(8)" : "+v"(q[0]), "+v"(q[1]), "+v"(q[2]), "+v"(q[3]), "+v"(q[4]), "+v"(q[5]), "+v"(q[6]), "+v"(q[7]) :: "memory");
        else asm volatile("s_waitcnt vmcnt(0)" : "+v"(q[0]), "+v"(q[1]), "+v"(q[2]), "+v"(q[3]), "+v"(q[4]), "+v"(q[5]), "+v"(q[6]), "+v"(q[7]) :: "memory");
#pragma unroll
        for (int i = 0; i < 8; ++i) *(u32x4*)(smem + ((r + 1) & 1) * ABUF + wave * 1024 + i * 8192 + lane * 16) = q[i];
        if (r == 1) BAR();
      }
      if (r == 3) { hook(); BAR(); }
    });
  }
}
#define DSR2(d0, d1, a0_, a1_, off) asm volatile("ds_read_b128 %0, %2 offset:%4\n\tds_read_b128 %1, %3 offset:%4" : "=&v"(d0), "=&v"(d1) : "v"(a0_), "v"(a1_), "i"(off))
template <int N> __device__ __forceinline__ void waitl(u32x4& a, u32x4& b) { asm volatile("s_waitcnt lgkmcnt(%2)" : "+v"(a), "+v"(b) : "i"(N)); }
__device__ __forceinline__ void glds1(const char* sbase, unsigned voff, unsigned lds) {
  unsigned keep;
  asm volatile("s_mov_b32 %0, m0\n\ts_mov_b32 m0, %3\n\ts_nop 0\n\tglobal_load_lds_dwordx4 %1, %2 sc1\n\ts_mov_b32 m0, %0"
               : "=&s"(keep) : "v"(voff), "s"(sbase), "s"(lds) : "memory");
}
#define XS(i) do { } while (0)
#define XFL(b_) do { } while (0)
#define XA_ 0
#define XF_ 0
#define XE_ 0
#define XP_ 0
#define PUBK 9
#define LATE 0
#define NOWP 2
#define NEARM 15
#define PNEAR 2
#define LATEQ 0
#define DEFP 1
#define ROTM 0
#define ROTK 7
#define BPACE 40
#define W2 1
#define PDEEP 4
template <int ROLE>
__device__ __forceinline__ void gemm7(char* smem, int wave_id_, const u16* W0, const u16* W1, int ldb, const char* A_, size_t a_blk, Dep dep,
                                      char* C_, size_t c_blk, unsigned* myprog, unsigned* tmo, int slot, unsigned long long* mts,
                                      bool gl, int nimm = 0, int nseg = NSEG, int wm0 = 0, int pofs = 0, unsigned long long tag = 0, unsigned* par = nullptr) {
  volatile LAS unsigned* wsl = (volatile LAS unsigned*)(smem + L_SLOT + 512);
  if (opaque_tid() < 144) *(volatile LAS unsigned*)(smem + L_SLOT + opaque_tid() * 4) = 0u;
  const int wave = WAVE_ID;
  const int kq = wave >> 2, nq = wave & 3;
  const unsigned ldsb = __builtin_amdgcn_readfirstlane((unsigned)(size_t)(LAS char*)smem);
  const int rot = (ROTM >> ROLE) & 1 ? slot & ROTK : 0;
  const char* A = uni(A_) + nq * 1024;
  constexpr bool NEAR = (NEARM >> ROLE) & 1;
  constexpr int AH = NEAR ? 2 : 3, PD = NEAR ? PNEAR : PDEEP;
  constexpr bool P2C = ROLE == R_PART2;
  int pw = 0;
  int wm = wm0, nh = 0;
  auto lpoll = [&](int b, bool hs = false) {
    unsigned w;
    if (wave == 4) {
      for (int spins = 0;; ++spins) {
        unsigned v;
        if (hs) { WAIT_V(0); v = *(volatile LAS unsigned*)(smem + L_SLOT + 768 + lane_id() * 4); }
        else asm volatile("s_nop 4\n\tglobal_load_dword %0, %1, %2 sc1\n\ts_waitcnt vmcnt(0)" : "=&v"(v) : "v"(dep.voff), "s"(dep.base) : "memory");
        w = dep_eval(v, dep);
        if ((int)w > b) break;
        if (!hs) __builtin_amdgcn_s_sleep(2);
        hs = false;
        if (give_up(spins, tmo, 9)) { w = NSEG; break; }
      }
      w = w < 255u ? w : 255u;
      if (lane_id() == 0) wsl[0] = ((unsigned)(b + 1) << 8) | w;
    } else {
      for (;;) {
        w = __builtin_amdgcn_readfirstlane(wsl[0]);
        if ((w >> 8) == (unsigned)(b + 1)) break;
        __builtin_amdgcn_s_sleep(1);
      }
      w &= 255u;
    }
    return (int)w;
  };
  auto pubw = [&](int n) {
    WAIT_V(0);
    if (lane_id() == 0) {
      const unsigned o = __hip_atomic_fetch_add((LAS unsigned*)(smem + L_SLOT + 516), 1u, __ATOMIC_RELAXED, __HIP_MEMORY_SCOPE_WORKGROUP);
      if ((o & 3u) == 3u) {
        st4_sc1(myprog, (unsigned)(n + pofs));
      }
    }
    asm volatile("s_waitcnt lgkmcnt(0)" ::: "memory");
  };
  auto hsrc = [&](int hh) { return A + (size_t)(hh >> 1) * a_blk + ((hh ^ (rot >> 2)) & 1) * 16384; };
  auto hlds = [&](int hh) { return ldsb + (unsigned)(((hh >> 1) & 1) * 65536 + (hh & 1) * 16384 + nq * 1024); };
#define POFF(i_) (((i_) & 1) * 32768 + ((i_) >> 1) * 4096)
#define PIECE(s_, l_, i_) asm volatile("s_add_u32 m0, %2, %3\n\ts_nop 0\n\tglobal_load_lds_dwordx4 %0, %1 sc1" :: "v"(av[i_]), "s"(s_), "s"(l_), "i"(POFF(i_)) : "memory", "scc", "m0")
  u32x32 BW[4];
  KTS(1);
  wload<ROLE, 2>(smem, wave_id_, W0, W1, ldb, BW, rot, [&]() {
    if (ROLE == R_QKV && nimm > 0 && wave == 7) ini_request((const char*)tmo - OFF_TMO, ldsb + L_GB);
  }, [&]() {
    if (ROLE == R_QKV && nimm > 0) {
      *par = ini_resolve((const char*)tmo - OFF_TMO, smem + L_GB, tag);
      dep.base += (*par >> 2) << 13; myprog += (*par >> 2) << 11;
      if (kq) {
        wm = lpoll(0);
        const unsigned l16 = lane_id() * 16;
#pragma unroll
        for (int i = 0; i < 16; ++i) glds1(hsrc(i >> 3) + (i & 1) * 32768 + ((((i & 7) >> 1) ^ rot) & 3) * 4096, l16, hlds(i >> 3) + POFF(i & 7));
      }
      nh = 2;
    } else if (ROLE == R_QKV && wave == 4) wait_l1((const unsigned*)dep.base, 8u, tmo);
  }, ROLE == R_QKV ? (unsigned long long*)((char*)tmo - OFF_TMO + ((size_t)246 << 20)) + (128 + slot) * 8 : nullptr);
  KTS(2);
  if (ROLE == R_QKV && nimm > 0 && !kq) wm = (int)(__builtin_amdgcn_readfirstlane(wsl[0]) & 255u);
  if (ROLE == R_QKV && opaque_tid() == 0) STS((char*)tmo - OFF_TMO, 128 + slot, 0);
  auto body = [&](auto ldr_c) __attribute__((always_inline)) {
    constexpr bool LDR = decltype(ldr_c)::value;
    if (XP_ == 0 ? !LDR : (XP_ == 1 ? LDR : false)) __builtin_amdgcn_s_setprio(1);
    unsigned av[8];
    const int lane = lane_id();
#pragma unroll
    for (int i = 0; i < 8; ++i) av[i] = lane * 16 + (P2C ? 0 : (i & 1) * 32768) + (((i >> 1) ^ rot) & 3) * 4096;
    auto issue_all = [&](int hh) {
      if (LDR) {
        const char* s_ = hsrc(hh); const unsigned l_ = hlds(hh);
#pragma unroll
        for (int i = 0; i < 8; ++i) if (!(P2C && (i & 1))) PIECE(s_, l_, i);
      }
    };
    int b = 0;
    bool hs = false;
    while (b < nseg) {
      const int ln = lane_id(), g = ln >> 4, r16 = ln & 15;
      const unsigned e0 = ldsb + (unsigned)((P2C ? 0 : kq * 32768) + (b & 1) * 65536 + (gl ? (g >> 1) * 512 + r16 * 32 + (g & 1) * 16 : r16 * 128 + ((g ^ (r16 & 7)) << 4)));
      const unsigned d0 = e0 ^ (gl ? 1024u : 64u);
      const unsigned rw = ldsb + L_RED + nq * 4096 + ln * 16;
      const unsigned co0 = ROLE == R_F1 ? (unsigned)((g & 1) * 2048 + nq * 512 + r16 * 32 + (g >> 1) * 16)
                         : ((ROLE == R_QKV ? TLQ : TLP) ? (unsigned)(nq * (P2C ? 4096 : 2048) + r16 * 64 + g * 16)
                         : (ROLE == R_QKV ? (unsigned)(r16 * 256 + (((4 * nq + g) ^ r16) << 4)) : (unsigned)(r16 * PROW + (32 * nq + 8 * g) * 2)));
      const unsigned co1 = co0 + ((ROLE == R_QKV ? TLQ : TLP) ? 1024u : (ROLE == R_QKV ? 4096u : 16u * PROW));
      u32x16 pv_;
      bool smp = false, lt0 = false;
      if ((wm < nseg ? wm : nseg) >= b + PD && b >= nimm && nh <= 2 * b + AH) {
        __builtin_amdgcn_s_barrier(); asm volatile("" ::: "memory");
        for (; nh < 2 * b + AH; ++nh) issue_all(nh);
        int lf = (wm < nseg ? wm : nseg) - (AH - 1), cp = nseg - (AH - 1);
        if (!LDR) KTS(128 + b);
        const unsigned sa = ldsb + L_SLOT + ln * 4, ad = dep.add - (AH - 1);
        if constexpr (LDR) (NEAR ? (P2C ? near_ld2 : near_ld0) : (P2C ? fast_ld2 : fast_ld0))(BW, pv_, e0, d0, b, lf, cp, sa, dep.mul, ad, rw, (unsigned)a_blk, hsrc(2 * b + AH), hsrc(2 * b + AH + 1), hlds(2 * b + AH), hlds(2 * b + AH + 1), av);
        else {
          const char* c0_ = uni((const char*)tmo - OFF_TMO + OFF_DUMP); const char* c1_ = uni(C_ + (size_t)b * c_blk);
          const int sf = b + pofs - (pw > 0 ? 1 : 0);
          const char* pg = uni((const char*)myprog);
          (NEAR ? (ROLE == R_F1 ? near_st1 : (P2C ? near_st2 : near_st0)) : (ROLE == R_F1 ? fast_st1 : (P2C ? fast_st2 : fast_st0)))(BW, pv_, e0, d0, b, lf, cp, sa, dep.mul, ad, rw, (unsigned)c_blk, c0_, c1_, co0, co1, pofs, sf, (unsigned)wave,
                                                                   ldsb + L_SLOT, dep.voff, dep.base, pg);
        }
        if (!LDR) KTS(64 + b);
        wm = lf + (AH - 1) > wm ? lf + (AH - 1) : wm;
        nh = 2 * b + AH;
        --b;
        __builtin_amdgcn_s_barrier(); asm volatile("" ::: "memory");
      } else if (NEAR && b >= BPACE) {
        if constexpr (LDR) {
          int cap = wave == 4 ? SPIN_CAP : 8 * SPIN_CAP;
          if (hs) WAIT_V(0);
          (P2C ? paced_ld2 : paced_ld0)(BW, e0, d0, b, nseg, (unsigned)a_blk, rw, ldsb + L_SLOT + 528 + nq * 4, hsrc(nh), hsrc(nh + 1), hlds(nh), cap, wm, (unsigned)(nh - 2 * b),
                                        dep.mul, dep.add, dep.voff, dep.base, wave == 4 ? ldsb + L_SLOT : 0u, ldsb + L_SLOT + ln * 4, av);
          if (cap == 0 && ln == 0) st4_sc1(tmo, 15u);
        } else (ROLE == R_F1 ? paced_st1 : (P2C ? paced_st2 : paced_st0))(BW, e0, d0, b, nseg, (unsigned)c_blk, rw, ldsb + L_SLOT + 528 + nq * 4, uni(C_ + (size_t)b * c_blk),
                                                                          __builtin_amdgcn_readfirstlane(pw > 0 ? 1u : 0u), pofs, ldsb + L_SLOT + 516, uni((const char*)myprog), co0, co1);
        break;
      } else {
        const bool pl = wm <= b;
        XS(0);
        if (pl && LDR) wm = lpoll(b, hs);
        XS(1);
        const bool cold = nh < 2 * b + 2;
        { const int upto = nh == 2 * b + 3 ? 2 * b + 4 : 2 * b + 2; for (; nh < upto; ++nh) issue_all(nh); }
        smp = nh == 2 * b + 2 && b + 1 < nseg && wm0 < nseg;
        if (LDR && wave == 4 && smp)
          asm volatile("s_mov_b32 m0, %0\n\ts_nop 3\n\tglobal_load_lds_dword %1, %2 sc1" :: "s"(ldsb + L_SLOT + (b & 1) * 256), "v"(dep.voff), "s"(dep.base) : "memory", "m0");
        XS(2);
        if (LDR && LEAN_QB) { if (nh >= 2 * b + 4) { if (P2C) WAIT_V(14); else WAIT_V(28); } else { if (P2C) WAIT_V(6); else WAIT_V(12); } }
        else if (LDR) { if (nh >= 2 * b + 4) { if (P2C) WAIT_V(12); else WAIT_V(24); } else { if (P2C) WAIT_V(4); else WAIT_V(8); } }
        BAR();
        XS(3);
        if (pl && !LDR) wm = (int)(__builtin_amdgcn_readfirstlane(wsl[0]) & 255u);
        const bool dm = nh == 2 * b + 2 && wm > b + 1 && b + 1 < nseg;
        if (ROLE == R_QKV && b < 2 && opaque_tid() == 0) STS((char*)tmo - OFF_TMO, 128 + slot, 1 + b);
        lt0 = smp && !dm;
        if constexpr (LDR) {
          if (dm) (P2C ? lean_dm1 : lean_dm0)(BW, pv_, e0, d0, 0u, hlds(nh), hsrc(nh), hsrc(nh + 1), av);
          else (P2C ? lean_ld1 : lean_ld0)(BW, pv_, e0, d0, (unsigned)(nh - 2 * b - 2), hlds(nh), hsrc(nh), hsrc(nh + 1), av, ldsb + L_SLOT + (b & 1) * 256 + ln * 4, dep.mul, dep.add, LATEQ && lt0 ? b + 1 : 0x7FFFFFFF,
                                           __builtin_amdgcn_readfirstlane(lt0 && wave == 4 ? ldsb + L_SLOT + 768 : 0u), dep.voff, dep.base);
        }
        else lean_st0(BW, pv_, e0, d0, __builtin_amdgcn_readfirstlane(pw > 0 ? (cold ? 4u : 1u) : 0u), (unsigned)(pw + pofs), ldsb + L_SLOT + 516, ldsb + L_SLOT + 256,
                     dep.voff, dep.base, uni((const char*)myprog));
        if (dm) nh += 2;
        XS(5);
      }
      if constexpr (LDR) fin_ld(pv_, rw, ldsb + L_SLOT + 528 + nq * 4, (unsigned)(b + 1));
      else (ROLE == R_F1 ? fin_st1 : (P2C ? fin_st2 : fin_st0))(pv_, rw, ldsb + L_SLOT + 528 + nq * 4, (unsigned)(b + 1), uni(C_ + (size_t)b * c_blk), co0, co1);
      XS(6);
      if (smp) {
        const int w = (int)dep_eval(*(volatile LAS unsigned*)(smem + L_SLOT + (b & 1) * 256 + lane_id() * 4), dep);
        if (LATEQ && lt0 && w > b + 1) nh += 2;
        wm = w > wm ? w : wm;
      }
      bool now = (NOWP == 2 ? nh < 2 * b + 4 && wm < b + 1 + PD : wm <= b + 1) || b + 1 >= nseg || b < nimm;
      if (!LDR && now) {
        if (DEFP && b >= nimm && b + 1 < nseg && (wm > b + 1 || (__builtin_amdgcn_readfirstlane(wsl[0]) >> 8) == (unsigned)(b + 2))) now = false;
        else pubw(b + 1);
      }
      XS(7);
      XFL(b);
      pw = now ? 0 : b + 1;
      hs = W2 && lt0;
      ++b;
    }
    __builtin_amdgcn_s_setprio(0);
  };
  if (kq) body(std::true_type{}); else body(std::false_type{});
#undef PIECE
#undef POFF
  WAIT_V(0);
  BAR();
  KTS(3);
}
__device__ __forceinline__ void fin_role(char* smem, int wave_id_, int fidx, const float* mod, const char* P2, const u16* x1, u16* out, unsigned* cnt, unsigned* tmo,
                                         unsigned long long* mts) {
  const int tid = opaque_tid(), lane = tid & 63, wave = WAVE_ID;
  row_table(smem + L_GB, mod, cnt, tmo, tid);
  volatile LAS unsigned* wsl = (volatile LAS unsigned*)(smem + L_SLOT + 512);
  int wf = 0, par = 0;
  for (int nf = fidx >> 3; nf < NSEG; nf += 2) {
    if (wf <= nf) {
      if (wave == 0) {
        unsigned w;
        for (int spins = 0;; ++spins) {
          const unsigned v = rowmin16(ld4_sc1(cnt + (PF2 + ((lane & 31) < 28 ? (lane & 31) : 0)) * 32));
          w = min(rdl(v, 0), rdl(v, 16));
          if ((int)w > nf) break;
          __builtin_amdgcn_s_sleep(2);
          if (give_up(spins, tmo, 9)) { w = NSEG; break; }
        }
        if (lane == 0) wsl[par] = w;
      }
      BAR();
      wf = (int)__builtin_amdgcn_readfirstlane(wsl[par]);
      par ^= 1;
    }
    int ln = lane; asm volatile("" : "+v"(ln));
    if (wave == 0) WTS(256 + 8 + fidx, nf / 2, 0);
    fin_half(P2, x1, smem + L_GB + 12288, out, nf * 32 + 4 * (fidx & 7) + (wave >> 1), wave & 1, ln);
    if (wave == 0) WTS(256 + 8 + fidx, nf / 2, 2);
  }
}
constexpr int Q_MEM = 0;
constexpr int Q_QK  = 16384;
constexpr int Q_TM  = 22528;
constexpr int Q_PQ  = 24576;
constexpr int Q_K   = 32768;
constexpr int Q_RING = 32768;
constexpr int NCHAIN = 96;
#define NQQ 6
#define NQK 4
#define E_AG 7
#define RDYI -1
#define ROTW 2
#define DQU 6
#define DQM 18
#define DQD 1
#define ORSEL (xg >> 1)
#define D_Q 96
#define D_O 3
#define D_A 0
#define WOF 1
#define E_S 0
#define SPF 0
#define STE 0
#define RAE 0
#define SPUB 62
#define QDL 0
#define KFB 1
#define E_B1 0
#define ADQ 1
#define D_V 4
#define RAW 5
#define E_MIN 3
#define E_W 3
#define DOU 0
#define DOM 12
#define CL0 1
#define TSW 0
#define E_B 6
#define E_O 1900
#define KDEL 5
#define VPOS 0

typedef __bf16 bf16x16 __attribute__((ext_vector_type(16)));
typedef unsigned u32x8 __attribute__((ext_vector_type(8)));
__device__ __forceinline__ f32x4 SMF(u32x4 a, u32x8 b, f32x4 c, int idx) {
  return __builtin_amdgcn_smfmac_f32_16x16x64_bf16(__builtin_bit_cast(bf16x8, a), __builtin_bit_cast(bf16x16, b), c, idx, 0, 0);
}
__device__ __forceinline__ float fold32(float a, float b) {
  const auto p = __builtin_amdgcn_permlane32_swap(__float_as_uint(a), __float_as_uint(b), false, false);
  return __uint_as_float(p[0]) + __uint_as_float(p[1]);
}
__device__ __forceinline__ f32x2 rowsum(f32x4 x) { return f32x2{fold32(x[0], x[2]), fold32(x[1], x[3])}; }
__device__ __forceinline__ unsigned nosent1(unsigned v) { return v < 0xFFFFFFFEu ? v : 0xFFFFFFFEu; }
__device__ __forceinline__ void st4s(const char* sb, unsigned vo, unsigned v) { asm volatile("global_store_dword %0, %1, %2 sc1\n\ts_nop 0" :: "v"(vo), "v"(v), "s"(sb) : "memory"); }
__device__ __forceinline__ void st8s(const char* sb, unsigned vo, u32x2 v) { asm volatile("global_store_dwordx2 %0, %1, %2 sc1\n\ts_nop 1" :: "v"(vo), "v"(v), "s"(sb) : "memory"); }
__device__ __forceinline__ void gldsw(const char* sbase, unsigned voff, unsigned lds) {
  unsigned keep;
  asm volatile("s_mov_b32 %0, m0\n\ts_mov_b32 m0, %3\n\ts_nop 0\n\tglobal_load_lds_dwordx4 %1, %2" CNT_ "\n\ts_mov_b32 m0, %0"
               : "=&s"(keep) : "v"(voff), "s"(sbase), "s"(lds) : "memory");
}
#define PWC 1
#define PWL 0
__device__ __forceinline__ void wait_clk(unsigned t0, unsigned d) {
  while ((unsigned)__builtin_amdgcn_s_memtime() - t0 < d) __builtin_amdgcn_s_sleep(PWC);
}
__device__ __forceinline__ unsigned bell_time(const char* a, unsigned vo, unsigned tag, unsigned cap, unsigned v = 0) {
  for (unsigned spins = 0; !__all((v >> 24) >= tag); ++spins) {
    asm volatile("s_nop 4\n\tglobal_load_dword %0, %1, %2 sc1\n\ts_waitcnt vmcnt(0)" : "=&v"(v) : "v"(vo), "s"(a) : "memory");
    if (__all((v >> 24) >= tag) || spins >= cap) break;
  }
  const unsigned now = (unsigned)__builtin_amdgcn_s_memrealtime() + 250u;
  const unsigned age = rowmin16((now - v) & 0xFFFFFFu);
  unsigned r = __builtin_amdgcn_readfirstlane(now - min(min(min(rdl(age, 0), rdl(age, 16)), min(rdl(age, 32), rdl(age, 48))), 500u));
  asm volatile("" : "+s"(r));
  return r;
}
#define PWS 0
__device__ __forceinline__ void wait_rt(unsigned t0, int d) {
  for (;;) {
    const int r = d - (int)((unsigned)__builtin_amdgcn_s_memrealtime() - t0);
    if (r <= 0) break;
    if (PWS && r > 24) __builtin_amdgcn_s_sleep(6); else if (PWS && r > 9) __builtin_amdgcn_s_sleep(2);
  }
}
#define WBAR() do { __builtin_amdgcn_s_barrier(); asm volatile("" ::: "memory"); } while (0)
#define SB() __builtin_amdgcn_sched_barrier(0)
#define TN(k, v) do { } while (0)
template <bool HALF>
__device__ __forceinline__ void stage_proj(const char* src, unsigned lds, int tid, int wave) {
  const int row = tid >> 4, f = 2 * ((row & 1) | (((row >> 2) & 3) << 1));
#pragma unroll
  for (int j = 0; j < 4; ++j) {
    const int pc = (tid & 15) ^ f;
    gldsw(src + (size_t)j * 32 * HD * 2, (unsigned)(HALF ? (row + (pc >> 3) * 128) * HD * 2 + ((pc & 7) << 4) : row * HD * 2 + (pc << 4)), lds + (wave * 64 + 512 * j) * 16);
  }
}
__device__ __forceinline__ unsigned proj_base(int T, int lane) {
  const int g = lane >> 4, q4 = (lane & 15) >> 2, p4 = lane & 3, fs = 2 * ((q4 & 1) | ((g & 1) << 1) | ((q4 >> 1) << 2));
  return (unsigned)((16 * (g >> 1) + 4 * (g & 1) + (q4 & 1) + 8 * (q4 >> 1)) * 256 + (((2 * T) ^ fs) << 4) + p4 * 8);
}
__device__ __forceinline__ f32x4 zero4() { float z = 0.0f; asm volatile("" : "+v"(z)); return f32x4{z, z, z, z}; }
__device__ __forceinline__ unsigned lds_word(const char* p) { return __builtin_amdgcn_readfirstlane(*(volatile LAS unsigned*)p); }

#define MFMA16(a, b, c) __builtin_amdgcn_mfma_f32_16x16x16bf16_1k((a), (b), (c), 0, 0, 0)
#define UF(x) __uint_as_float(x)
__device__ __forceinline__ unsigned umax3(unsigned a, unsigned b, unsigned c) { return max(max(a, b), c); }
__device__ __forceinline__ f32x2 logit2(float a, float b) {
  return f32x2{a, b};
}
constexpr float LSC = LOG2E * SCALE;

#define FCS() do { } while (0)
struct QKW { u32x32 w0, w1, w2, w3, w4, w5; };
template <int W_> __device__ __forceinline__ void qk_step(const QKW& W, const char* ag, const char* vs, const char* v2, const char* db, const char* od, unsigned pb, unsigned sn, unsigned& cap, unsigned& sp,
    unsigned long long& tc, unsigned long long* ts, unsigned& d0, unsigned& d1);
template <> __device__ __forceinline__ void qk_step<0>(const QKW& W, const char* ag, const char* vs, const char* v2, const char* db, const char* od, unsigned pb, unsigned sn, unsigned& cap, unsigned& sp,
    unsigned long long& tc, unsigned long long* ts, unsigned& d0, unsigned& d1) {
  unsigned st, bg;
  asm volatile("v_mbcnt_lo_u32_b32 v60, -1, 0\n\t"
    "v_mbcnt_hi_u32_b32 v60, -1, v60\n\t"
    "v_lshlrev_b32 v5, 4, v60\n\t"
    "s_mov_b32 %[sp], 0\n\t"
    "s_setprio 2\n\t"
    "global_load_dwordx4 v[24:27], v5, %[ag] sc1\n\t"
    "global_load_dwordx4 v[28:31], v5, %[ag] offset:1024 sc1\n\t"
    "s_memtime %[tc]\n\t"
    "v_lshrrev_b32 v32, 4, v60\n\t"
    "v_and_b32 v33, 15, v60\n\t"
    "v_lshrrev_b32 v34, 2, v33\n\t"
    "v_and_b32 v35, 3, v60\n\t"
    "v_and_b32 v61, 1, v60\n\t"
    "v_lshlrev_b32 v61, 3, v61\n\t"
    "v_lshl_or_b32 v7, v34, 4, v61\n\t"
    "v_lshl_or_b32 v7, v32, 7, v7\n\t"
    "v_and_b32 v10, 2, v34\n\t"
    "v_xor_b32 v10, v10, v32\n\t"
    "v_lshl_or_b32 v10, v10, 4, %[pb]\n\t"
    "v_lshl_or_b32 v10, v33, 6, v10\n\t"
    "v_and_b32 v14, 32, v60\n\t"
    "v_lshl_or_b32 v14, v35, 3, v14\n\t"
    "v_lshrrev_b32 v34, 2, v60\n\t"
    "v_lshl_or_b32 v14, v34, 6, v14\n\t"
    "v_or_b32 v14, %[pb], v14\n\t"
    "v_and_b32 v61, 3, v60\n\t"
    "v_lshlrev_b32 v61, 6, v61\n\t"
    "v_lshl_or_b32 v4, v32, 4, v61\n\t"
    "v_and_b32 v61, 4, v60\n\t"
    "v_lshl_or_b32 v4, v61, 10, v4\n\t"
    "v_and_b32 v61, 8, v60\n\t"
    "v_lshl_or_b32 v4, v61, 14, v4\n\t"
    "v_or_b32 v4, 0, v4\n\t"
    "v_and_b32 v61, 2, v60\n\t"
    "v_mul_u32_u24 v61, 0x5555, v61\n\t"
    "v_or_b32 v6, %[c44], v61\n\t"
    "v_lshlrev_b32 v61, 2, v32\n\t"
    "v_lshl_or_b32 v15, v33, 4, v61\n\t"
    "3:\n\t"
    "v_mov_b32 v16, 0\n\t"
    "v_mov_b32 v17, 0\n\t"
    "v_mov_b32 v18, 0\n\t"
    "v_mov_b32 v19, 0\n\t"
    "v_mov_b32 v20, 0\n\t"
    "v_mov_b32 v21, 0\n\t"
    "v_mov_b32 v22, 0\n\t"
    "v_mov_b32 v23, 0\n\t"
    "s_waitcnt vmcnt(0) lgkmcnt(0)\n\t"
    "33:\n\t"
    "ds_write_b128 v5, v[24:27] offset:0\n\t"
    "ds_write_b128 v5, v[28:31] offset:1024\n\t"
    "v_smfmac_f32_16x16x64_bf16 v[16:19], v[24:27], v[64:71], v6\n\t"
    "v_max3_u32 v60, v24, v25, v26\n\t"
    "v_max3_u32 v61, v27, v28, v29\n\t"
    "v_smfmac_f32_16x16x64_bf16 v[20:23], v[24:27], v[72:79], v6\n\t"
    "v_max3_u32 v60, v60, v30, v31\n\t"
    "v_max_u32 v60, v60, v61\n\t"
    "v_cmp_eq_u32 vcc, -1, v60\n\t"
    "s_cbranch_vccnz 1f\n\t"
    "2:\n\t"
    "s_waitcnt lgkmcnt(0)\n\t"
    "s_barrier\n\t"
    "ds_read_b128 v[32:35], v5 offset:2048\n\t"
    "ds_read_b128 v[36:39], v5 offset:3072\n\t"
    "v_smfmac_f32_16x16x64_bf16 v[16:19], v[28:31], v[80:87], v6\n\t"
    "ds_read_b128 v[40:43], v5 offset:4096\n\t"
    "ds_read_b128 v[44:47], v5 offset:5120\n\t"
    "v_smfmac_f32_16x16x64_bf16 v[20:23], v[28:31], v[88:95], v6\n\t"
    "s_waitcnt lgkmcnt(3)\n\t"
    "v_smfmac_f32_16x16x64_bf16 v[16:19], v[32:35], v[96:103], v6\n\t"
    "ds_read_b128 v[48:51], v5 offset:6144\n\t"
    "v_smfmac_f32_16x16x64_bf16 v[20:23], v[32:35], v[104:111], v6\n\t"
    "ds_read_b128 v[52:55], v5 offset:7168\n\t"
    "s_waitcnt lgkmcnt(4)\n\t"
    "v_smfmac_f32_16x16x64_bf16 v[16:19], v[36:39], v[112:119], v6\n\t"
    "ds_read_b128 v[24:27], v5 offset:8192\n\t"
    "v_smfmac_f32_16x16x64_bf16 v[20:23], v[36:39], v[120:127], v6\n\t"
    "ds_read_b128 v[28:31], v5 offset:9216\n\t"
    "s_waitcnt lgkmcnt(5)\n\t"
    "v_smfmac_f32_16x16x64_bf16 v[16:19], v[40:43], v[128:135], v6\n\t"
    "ds_read_b128 v[56:59], v5 offset:10240\n\t"
    "v_smfmac_f32_16x16x64_bf16 v[20:23], v[40:43], v[136:143], v6\n\t"
    "ds_read_b128 v[60:63], v5 offset:11264\n\t"
    "ds_read_b128 v[36:39], v10 offset:35840\n\t"
    "s_waitcnt lgkmcnt(7)\n\t"
    "v_smfmac_f32_16x16x64_bf16 v[16:19], v[44:47], v[144:151], v6\n\t"
    "ds_read_b128 v[40:43], v10 offset:36864\n\t"
    "v_smfmac_f32_16x16x64_bf16 v[20:23], v[44:47], v[152:159], v6\n\t"
    "ds_read_b128 v[44:47], v10 offset:37888\n\t"
    "s_waitcnt lgkmcnt(8)\n\t"
    "v_smfmac_f32_16x16x64_bf16 v[16:19], v[48:51], v[160:167], v6\n\t"
    "v_smfmac_f32_16x16x64_bf16 v[20:23], v[48:51], v[168:175], v6\n\t"
    "ds_read_b128 v[48:51], v10 offset:38912\n\t"
    "s_waitcnt lgkmcnt(8)\n\t"
    "v_smfmac_f32_16x16x64_bf16 v[16:19], v[52:55], v[176:183], v6\n\t"
    "v_smfmac_f32_16x16x64_bf16 v[20:23], v[52:55], v[184:191], v6\n\t"
    "ds_read_b128 v[52:55], v10 offset:39936\n\t"
    "s_waitcnt lgkmcnt(8)\n\t"
    "v_smfmac_f32_16x16x64_bf16 v[16:19], v[24:27], v[192:199], v6\n\t"
    "v_smfmac_f32_16x16x64_bf16 v[20:23], v[24:27], v[200:207], v6\n\t"
    "ds_read_b128 v[24:27], v10 offset:32768\n\t"
    "s_waitcnt lgkmcnt(8)\n\t"
    "v_smfmac_f32_16x16x64_bf16 v[16:19], v[28:31], v[208:215], v6\n\t"
    "ds_read_b32 v8, v15 offset:28672\n\t"
    "ds_read_b128 v[32:35], v5 offset:24576\n\t"
    "s_setprio 3\n\t"
    "s_waitcnt lgkmcnt(9)\n\t"
    "v_smfmac_f32_16x16x64_bf16 v[16:19], v[56:59], v[224:231], v6\n\t"
    "s_waitcnt lgkmcnt(8)\n\t"
    "v_smfmac_f32_16x16x64_bf16 v[16:19], v[60:63], v[240:247], v6\n\t"
    "v_smfmac_f32_16x16x64_bf16 v[20:23], v[28:31], v[216:223], v6\n\t"
    "s_waitcnt lgkmcnt(0)\n\t"
    "21:\n\t"
    "v_cmp_ne_u32 vcc, %[sn], v8\n\t"
    "s_cbranch_vccnz 22f\n\t"
    "v_mul_f32 v32, %[lsc], v32\n\t"
    "v_mul_f32 v33, %[lsc], v33\n\t"
    "v_smfmac_f32_16x16x64_bf16 v[20:23], v[56:59], v[232:239], v6\n\t"
    "v_mul_f32 v34, %[lsc], v34\n\t"
    "v_mul_f32 v35, %[lsc], v35\n\t"
    "v_add_f32 v16, v16, v18\n\t"
    "v_add_f32 v17, v17, v19\n\t"
    "v_smfmac_f32_16x16x64_bf16 v[20:23], v[60:63], v[248:255], v6\n\t"
    "v_fma_f32 v16, v16, %[lsc], v32\n\t"
    "v_fma_f32 v17, v17, %[lsc], v33\n\t"
    "v_cvt_pk_bf16_f32 v16, v16, v17\n\t"
    "ds_write_b32 v15, v16 offset:16384\n\t"
    "s_mov_b32 %[bg], %[big]\n\t"
    "s_nop 2\n\t"
    "v_add_f32 v20, v20, v22\n\t"
    "v_add_f32 v21, v21, v23\n\t"
    "v_fma_f32 v20, v20, %[lsc], v34\n\t"
    "v_fma_f32 v21, v21, %[lsc], v35\n\t"
    "v_cvt_pk_bf16_f32 v20, v20, v21\n\t"
    "ds_write_b32 v15, v20 offset:16640\n\t"
    "ds_read_b128 v[28:31], v10 offset:33792\n\t"
    "ds_read_b128 v[32:35], v10 offset:34816\n\t"
    "s_waitcnt lgkmcnt(2)\n\t"
    "s_barrier\n\t"
    "ds_read_b64_tr_b16 v[16:17], v7 offset:16384\n\t"
    "ds_read_b64_tr_b16 v[18:19], v7 offset:16448\n\t"
    "ds_read_b64_tr_b16 v[20:21], v7 offset:16896\n\t"
    "ds_read_b64_tr_b16 v[22:23], v7 offset:16960\n\t"
    "ds_read_b64_tr_b16 v[56:57], v7 offset:17408\n\t"
    "ds_read_b64_tr_b16 v[58:59], v7 offset:17472\n\t"
    "ds_read_b64_tr_b16 v[60:61], v7 offset:17920\n\t"
    "ds_read_b64_tr_b16 v[62:63], v7 offset:17984\n\t"
    "global_load_dwordx4 v[6:9], v5, %[vs] sc1\n\t"
    "global_load_dwordx4 v[10:13], v5, %[vs] offset:1024 sc1\n\t"
    "s_waitcnt lgkmcnt(6)\n\t"
    "v_mfma_f32_16x16x32_bf16 v[24:27], v[24:27], v[16:19], 0\n\t"
    "v_mfma_f32_16x16x32_bf16 v[28:31], v[28:31], v[16:19], 0\n\t"
    "s_waitcnt lgkmcnt(4)\n\t"
    "v_mfma_f32_16x16x32_bf16 v[24:27], v[32:35], v[20:23], v[24:27]\n\t"
    "v_mfma_f32_16x16x32_bf16 v[28:31], v[36:39], v[20:23], v[28:31]\n\t"
    "v_xor_b32 v39, 32, v14\n\t"
    "ds_read_b64_tr_b16 v[32:33], v14 offset:40960\n\t"
    "ds_read_b64_tr_b16 v[34:35], v14 offset:41984\n\t"
    "ds_read_b64_tr_b16 v[36:37], v39 offset:40960\n\t"
    "ds_read_b64_tr_b16 v[38:39], v39 offset:41984\n\t"
    "s_waitcnt lgkmcnt(6)\n\t"
    "v_mfma_f32_16x16x32_bf16 v[24:27], v[40:43], v[56:59], v[24:27]\n\t"
    "v_mfma_f32_16x16x32_bf16 v[28:31], v[44:47], v[56:59], v[28:31]\n\t"
    "s_waitcnt lgkmcnt(4)\n\t"
    "v_mfma_f32_16x16x32_bf16 v[24:27], v[48:51], v[60:63], v[24:27]\n\t"
    "v_mfma_f32_16x16x32_bf16 v[28:31], v[52:55], v[60:63], v[28:31]\n\t"
    "v_mov_b32 v52, %[one]\n\t"
    "v_mov_b32 v53, %[one]\n\t"
    "v_mov_b32 v54, %[one]\n\t"
    "v_mov_b32 v55, %[one]\n\t"
    "v_mov_b32 v45, 0\n\t"
    "v_mov_b32 v47, 0\n\t"
    "v_mov_b32 v48, %[one]\n\t"
    "v_max3_f32 v14, |v24|, |v25|, |v26|\n\t"
    "v_max3_f32 v40, |v27|, |v28|, |v29|\n\t"
    "v_max3_f32 v14, v14, |v30|, |v31|\n\t"
    "v_max_f32 v14, v14, v40\n\t"
    "v_cmp_nge_f32 vcc, %[big], v14\n\t"
    "v_mov_b32 v14, 0\n\t"
    "s_cbranch_vccnz 12f\n\t"
    "13:\n\t"
    "v_exp_f32 v24, v24\n\t"
    "v_exp_f32 v25, v25\n\t"
    "v_exp_f32 v26, v26\n\t"
    "v_exp_f32 v27, v27\n\t"
    "v_exp_f32 v28, v28\n\t"
    "v_exp_f32 v29, v29\n\t"
    "v_exp_f32 v30, v30\n\t"
    "v_exp_f32 v31, v31\n\t"
    "v_cvt_pk_bf16_f32 v40, v24, v25\n\t"
    "v_cvt_pk_bf16_f32 v41, v26, v27\n\t"
    "v_cvt_pk_bf16_f32 v42, v28, v29\n\t"
    "v_cvt_pk_bf16_f32 v43, v30, v31\n\t"
    "v_mov_b32 v49, 0\n\t"
    "v_mov_b32 v51, 0\n\t"
    "s_waitcnt lgkmcnt(0)\n\t"
    "v_mfma_f32_16x16x32_bf16 v[52:55], v[52:55], v[40:43], 0\n\t"
    "v_mfma_f32_16x16x32_bf16 v[24:27], v[32:35], v[40:43], 0\n\t"
    "v_mfma_f32_16x16x32_bf16 v[28:31], v[36:39], v[40:43], 0\n\t"
    "s_waitcnt vmcnt(1)\n\t"
    "5:\n\t"
    "v_mfma_f32_16x16x32_bf16 v[32:35], v[6:9], v[16:19], 0\n\t"
    "v_mfma_f32_16x16x32_bf16 v[36:39], v[6:9], v[20:23], 0\n\t"
    "s_waitcnt vmcnt(0)\n\t"
    "v_mfma_f32_16x16x32_bf16 v[32:35], v[10:13], v[56:59], v[32:35]\n\t"
    "v_mfma_f32_16x16x32_bf16 v[36:39], v[10:13], v[60:63], v[36:39]\n\t"
    "ds_read_b32 v44, v15 offset:18432\n\t"
    "ds_read_b32 v46, v15 offset:18688\n\t"
    "v_max3_u32 v40, v6, v7, v8\n\t"
    "v_max3_u32 v41, v9, v10, v11\n\t"
    "v_max3_u32 v40, v40, v12, v13\n\t"
    "v_max_u32 v40, v40, v41\n\t"
    "v_cmp_eq_u32 vcc, -1, v40\n\t"
    "s_cbranch_vccnz 4f\n\t"
    "6:\n\t"
    "s_nop 0\n\t"
    "v_add_f32 v32, v32, v38\n\t"
    "v_add_f32 v33, v33, v39\n\t"
    "v_exp_f32 v34, v32\n\t"
    "v_max_f32 v40, v32, v33\n\t"
    "v_exp_f32 v35, v33\n\t"
    "v_cmp_nge_f32 vcc, %[bg], v40\n\t"
    "s_cbranch_vccnz 10f\n\t"
    "11:\n\t"
    "v_cvt_pk_bf16_f32 v50, v34, v35\n\t"
    "17:\n\t"
    "s_waitcnt vmcnt(0) lgkmcnt(0)\n\t"
    "v_max_u32 v40, v44, v46\n\t"
    "v_cmp_eq_u32 vcc, -1, v40\n\t"
    "s_cbranch_vccnz 18f\n\t"
    "19:\n\t"
    "v_mfma_f32_16x16x16_bf16 v[52:55], v[48:49], v[50:51], v[52:55]\n\t"
    "v_mfma_f32_16x16x16_bf16 v[24:27], v[44:45], v[50:51], v[24:27]\n\t"
    "v_mfma_f32_16x16x16_bf16 v[28:31], v[46:47], v[50:51], v[28:31]\n\t"
    "s_nop 5\n\t"
    "v_rcp_f32 v42, v52\n\t"
    "s_nop 0\n\t"
    "v_pk_mul_f32 v[24:25], v[24:25], v[42:43] op_sel_hi:[1,0]\n\t"
    "v_pk_mul_f32 v[26:27], v[26:27], v[42:43] op_sel_hi:[1,0]\n\t"
    "v_pk_mul_f32 v[28:29], v[28:29], v[42:43] op_sel_hi:[1,0]\n\t"
    "v_pk_mul_f32 v[30:31], v[30:31], v[42:43] op_sel_hi:[1,0]\n\t"
    "v_cvt_pk_bf16_f32 v36, v24, v25\n\t"
    "v_cvt_pk_bf16_f32 v37, v26, v27\n\t"
    "v_cvt_pk_bf16_f32 v38, v28, v29\n\t"
    "v_cvt_pk_bf16_f32 v39, v30, v31\n\t"
    "v_min_u32 v36, -2, v36\n\t"
    "v_min_u32 v37, -2, v37\n\t"
    "v_min_u32 v38, -2, v38\n\t"
    "v_min_u32 v39, -2, v39\n\t"
    "global_store_dwordx4 v4, v[36:39], %[od] sc1\n\t"
    "ds_write_b128 v5, v[6:9] offset:20480\n\t"
    "s_setprio 1\n\t"
    "s_waitcnt lgkmcnt(0)\n\t"
    "s_barrier\n\t"
    "s_waitcnt lgkmcnt(0)\n\t"
    "s_branch 9f\n\t"
    "1:\n\t"
    "s_add_u32 %[sp], %[sp], 1\n\t"
    "s_cmp_ge_u32 %[sp], %[cap]\n\t"
    "s_cbranch_scc1 7f\n\t"
    "global_load_dwordx4 v[24:27], v5, %[ag] sc1\n\t"
    "global_load_dwordx4 v[28:31], v5, %[ag] offset:1024 sc1\n\t"
    "s_waitcnt lgkmcnt(0)\n\t"
    "s_memtime %[tc]\n\t"
    "s_nop 7\n\t"
    "s_branch 3b\n\t"
    "7:\n\t"
    "s_mov_b32 %[cap], 0\n\t"
    "s_branch 2b\n\t"
    "4:\n\t"
    "s_add_u32 %[sp], %[sp], 0x10000\n\t"
    "s_lshr_b32 %[st], %[sp], 16\n\t"
    "s_cmp_ge_u32 %[st], %[cap]\n\t"
    "s_cbranch_scc1 8f\n\t"
    "global_load_dwordx4 v[6:9], v5, %[vs] sc1\n\t"
    "global_load_dwordx4 v[10:13], v5, %[vs] offset:1024 sc1\n\t"
    "s_waitcnt vmcnt(0) lgkmcnt(0)\n\t"
    "s_branch 5b\n\t"
    "8:\n\t"
    "s_mov_b32 %[cap], 0\n\t"
    "s_branch 6b\n\t"
    "18:\n\t"
    "s_add_u32 %[sp], %[sp], 0x10000\n\t"
    "s_lshr_b32 %[st], %[sp], 16\n\t"
    "s_cmp_ge_u32 %[st], %[cap]\n\t"
    "s_cbranch_scc1 20f\n\t"
    "ds_read_b32 v44, v15 offset:18432\n\t"
    "ds_read_b32 v46, v15 offset:18688\n\t"
    "s_waitcnt vmcnt(0) lgkmcnt(0)\n\t"
    "s_branch 17b\n\t"
    "20:\n\t"
    "s_mov_b32 %[cap], 0\n\t"
    "s_branch 19b\n\t"
    "22:\n\t"
    "ds_read_b32 v8, v15 offset:28672\n\t"
    "ds_read_b128 v[32:35], v5 offset:24576\n\t"
    "s_waitcnt lgkmcnt(0)\n\t"
    "s_branch 21b\n\t"
    "10:\n\t"
    "v_max_f32 v40, v40, v14\n\t"
    "v_mov_b32 v42, v40\n\t"
    "s_nop 1\n\t"
    "v_permlane16_swap_b32 v40, v42\n\t"
    "v_max_f32 v40, v40, v42\n\t"
    "v_mov_b32 v42, v40\n\t"
    "s_nop 1\n\t"
    "v_permlane32_swap_b32 v40, v42\n\t"
    "v_max_f32 v40, v40, v42\n\t"
    "v_sub_f32 v34, v32, v40\n\t"
    "v_sub_f32 v35, v33, v40\n\t"
    "v_sub_f32 v40, v14, v40\n\t"
    "v_exp_f32 v40, v40\n\t"
    "v_exp_f32 v34, v34\n\t"
    "v_exp_f32 v35, v35\n\t"
    "v_mul_f32 v24, v24, v40\n\t"
    "v_mul_f32 v25, v25, v40\n\t"
    "v_mul_f32 v26, v26, v40\n\t"
    "v_mul_f32 v27, v27, v40\n\t"
    "v_mul_f32 v28, v28, v40\n\t"
    "v_mul_f32 v29, v29, v40\n\t"
    "v_mul_f32 v30, v30, v40\n\t"
    "v_mul_f32 v31, v31, v40\n\t"
    "v_mul_f32 v52, v52, v40\n\t"
    "v_mul_f32 v53, v53, v40\n\t"
    "v_mul_f32 v54, v54, v40\n\t"
    "v_mul_f32 v55, v55, v40\n\t"
    "s_branch 11b\n\t"
    "12:\n\t"
    "v_max3_f32 v14, v24, v25, v26\n\t"
    "v_max3_f32 v40, v27, v28, v29\n\t"
    "v_max3_f32 v14, v14, v30, v31\n\t"
    "v_max3_f32 v14, v14, v40, %[neg]\n\t"
    "v_mov_b32 v40, v14\n\t"
    "s_nop 1\n\t"
    "v_permlane16_swap_b32 v14, v40\n\t"
    "v_max_f32 v14, v14, v40\n\t"
    "v_mov_b32 v40, v14\n\t"
    "s_nop 1\n\t"
    "v_permlane32_swap_b32 v14, v40\n\t"
    "v_max_f32 v14, v14, v40\n\t"
    "v_sub_f32 v24, v24, v14\n\t"
    "v_sub_f32 v25, v25, v14\n\t"
    "v_sub_f32 v26, v26, v14\n\t"
    "v_sub_f32 v27, v27, v14\n\t"
    "v_sub_f32 v28, v28, v14\n\t"
    "v_sub_f32 v29, v29, v14\n\t"
    "v_sub_f32 v30, v30, v14\n\t"
    "v_sub_f32 v31, v31, v14\n\t"
    "s_mov_b32 %[bg], 0x7fc00000\n\t"
    "s_branch 13b\n\t"
    "9:"
    : [tc] "=&s"(tc), [sp] "=&s"(sp), [cap] "+s"(cap), [st] "=&s"(st), [bg] "=&s"(bg)
    : [ag] "s"(ag), [c44] "s"(0x4444u), [lsc] "s"(LSC), [sn] "s"(sn), [pb] "s"(pb), [vs] "s"(vs), [od] "s"(od), [neg] "s"(-1.0e38f), [one] "s"(0x3F803F80u), [big] "s"(24.0f), "{v[64:95]}"(W.w0), "{v[96:127]}"(W.w1), "{v[128:159]}"(W.w2), "{v[160:191]}"(W.w3), "{v[192:223]}"(W.w4), "{v[224:255]}"(W.w5)
    : "v4", "v5", "v6", "v7", "v8", "v9", "v10", "v11", "v12", "v13", "v14", "v15", "v16", "v17", "v18", "v19", "v20", "v21", "v22", "v23", "v24", "v25", "v26", "v27", "v28", "v29", "v30", "v31", "v32", "v33", "v34", "v35", "v36", "v37", "v38", "v39", "v40", "v41", "v42", "v43", "v44", "v45", "v46", "v47", "v48", "v49", "v50", "v51", "v52", "v53", "v54", "v55", "v56", "v57", "v58", "v59", "v60", "v61", "v62", "v63", "vcc", "scc", "m0", "memory");
}
template <> __device__ __forceinline__ void qk_step<1>(const QKW& W, const char* ag, const char* vs, const char* v2, const char* db, const char* od, unsigned pb, unsigned sn, unsigned& cap, unsigned& sp,
    unsigned long long& tc, unsigned long long* ts, unsigned& d0, unsigned& d1) {
  unsigned st, bg;
  asm volatile("v_mbcnt_lo_u32_b32 v60, -1, 0\n\t"
    "v_mbcnt_hi_u32_b32 v60, -1, v60\n\t"
    "v_lshlrev_b32 v5, 4, v60\n\t"
    "s_mov_b32 %[sp], 0\n\t"
    "s_setprio 2\n\t"
    "global_load_dwordx4 v[24:27], v5, %[ag] sc1\n\t"
    "global_load_dwordx4 v[28:31], v5, %[ag] offset:1024 sc1\n\t"
    "s_memtime %[tc]\n\t"
    "v_lshrrev_b32 v32, 4, v60\n\t"
    "v_and_b32 v33, 15, v60\n\t"
    "v_lshrrev_b32 v34, 2, v33\n\t"
    "v_and_b32 v35, 3, v60\n\t"
    "v_and_b32 v61, 1, v60\n\t"
    "v_lshlrev_b32 v61, 3, v61\n\t"
    "v_lshl_or_b32 v7, v34, 4, v61\n\t"
    "v_lshl_or_b32 v7, v32, 7, v7\n\t"
    "v_and_b32 v10, 2, v34\n\t"
    "v_xor_b32 v10, v10, v32\n\t"
    "v_lshl_or_b32 v10, v10, 4, %[pb]\n\t"
    "v_lshl_or_b32 v10, v33, 6, v10\n\t"
    "v_and_b32 v14, 32, v60\n\t"
    "v_lshl_or_b32 v14, v35, 3, v14\n\t"
    "v_lshrrev_b32 v34, 2, v60\n\t"
    "v_lshl_or_b32 v14, v34, 6, v14\n\t"
    "v_or_b32 v14, %[pb], v14\n\t"
    "v_and_b32 v61, 3, v60\n\t"
    "v_lshlrev_b32 v61, 6, v61\n\t"
    "v_lshl_or_b32 v4, v32, 4, v61\n\t"
    "v_and_b32 v61, 4, v60\n\t"
    "v_lshl_or_b32 v4, v61, 10, v4\n\t"
    "v_and_b32 v61, 8, v60\n\t"
    "v_lshl_or_b32 v4, v61, 14, v4\n\t"
    "v_or_b32 v4, 256, v4\n\t"
    "v_and_b32 v61, 2, v60\n\t"
    "v_mul_u32_u24 v61, 0x5555, v61\n\t"
    "v_or_b32 v6, %[c44], v61\n\t"
    "v_lshlrev_b32 v61, 2, v32\n\t"
    "v_lshl_or_b32 v15, v33, 4, v61\n\t"
    "3:\n\t"
    "v_mov_b32 v16, 0\n\t"
    "v_mov_b32 v17, 0\n\t"
    "v_mov_b32 v18, 0\n\t"
    "v_mov_b32 v19, 0\n\t"
    "v_mov_b32 v20, 0\n\t"
    "v_mov_b32 v21, 0\n\t"
    "v_mov_b32 v22, 0\n\t"
    "v_mov_b32 v23, 0\n\t"
    "s_waitcnt vmcnt(0) lgkmcnt(0)\n\t"
    "33:\n\t"
    "ds_write_b128 v5, v[24:27] offset:2048\n\t"
    "ds_write_b128 v5, v[28:31] offset:3072\n\t"
    "v_smfmac_f32_16x16x64_bf16 v[16:19], v[24:27], v[96:103], v6\n\t"
    "v_max3_u32 v60, v24, v25, v26\n\t"
    "v_max3_u32 v61, v27, v28, v29\n\t"
    "v_smfmac_f32_16x16x64_bf16 v[20:23], v[24:27], v[104:111], v6\n\t"
    "v_max3_u32 v60, v60, v30, v31\n\t"
    "v_max_u32 v60, v60, v61\n\t"
    "v_cmp_eq_u32 vcc, -1, v60\n\t"
    "s_cbranch_vccnz 1f\n\t"
    "2:\n\t"
    "s_waitcnt lgkmcnt(0)\n\t"
    "s_barrier\n\t"
    "ds_read_b128 v[32:35], v5 offset:0\n\t"
    "ds_read_b128 v[36:39], v5 offset:1024\n\t"
    "v_smfmac_f32_16x16x64_bf16 v[16:19], v[28:31], v[112:119], v6\n\t"
    "ds_read_b128 v[40:43], v5 offset:4096\n\t"
    "ds_read_b128 v[44:47], v5 offset:5120\n\t"
    "v_smfmac_f32_16x16x64_bf16 v[20:23], v[28:31], v[120:127], v6\n\t"
    "s_waitcnt lgkmcnt(3)\n\t"
    "v_smfmac_f32_16x16x64_bf16 v[16:19], v[32:35], v[64:71], v6\n\t"
    "ds_read_b128 v[48:51], v5 offset:6144\n\t"
    "v_smfmac_f32_16x16x64_bf16 v[20:23], v[32:35], v[72:79], v6\n\t"
    "ds_read_b128 v[52:55], v5 offset:7168\n\t"
    "s_waitcnt lgkmcnt(4)\n\t"
    "v_smfmac_f32_16x16x64_bf16 v[16:19], v[36:39], v[80:87], v6\n\t"
    "ds_read_b128 v[24:27], v5 offset:8192\n\t"
    "v_smfmac_f32_16x16x64_bf16 v[20:23], v[36:39], v[88:95], v6\n\t"
    "ds_read_b128 v[28:31], v5 offset:9216\n\t"
    "s_waitcnt lgkmcnt(5)\n\t"
    "v_smfmac_f32_16x16x64_bf16 v[16:19], v[40:43], v[128:135], v6\n\t"
    "ds_read_b128 v[56:59], v5 offset:10240\n\t"
    "v_smfmac_f32_16x16x64_bf16 v[20:23], v[40:43], v[136:143], v6\n\t"
    "ds_read_b128 v[60:63], v5 offset:11264\n\t"
    "ds_read_b128 v[36:39], v10 offset:35840\n\t"
    "s_waitcnt lgkmcnt(7)\n\t"
    "v_smfmac_f32_16x16x64_bf16 v[16:19], v[44:47], v[144:151], v6\n\t"
    "ds_read_b128 v[40:43], v10 offset:36864\n\t"
    "v_smfmac_f32_16x16x64_bf16 v[20:23], v[44:47], v[152:159], v6\n\t"
    "ds_read_b128 v[44:47], v10 offset:37888\n\t"
    "s_waitcnt lgkmcnt(8)\n\t"
    "v_smfmac_f32_16x16x64_bf16 v[16:19], v[48:51], v[160:167], v6\n\t"
    "v_smfmac_f32_16x16x64_bf16 v[20:23], v[48:51], v[168:175], v6\n\t"
    "ds_read_b128 v[48:51], v10 offset:38912\n\t"
    "s_waitcnt lgkmcnt(8)\n\t"
    "v_smfmac_f32_16x16x64_bf16 v[16:19], v[52:55], v[176:183], v6\n\t"
    "v_smfmac_f32_16x16x64_bf16 v[20:23], v[52:55], v[184:191], v6\n\t"
    "ds_read_b128 v[52:55], v10 offset:39936\n\t"
    "s_waitcnt lgkmcnt(8)\n\t"
    "v_smfmac_f32_16x16x64_bf16 v[16:19], v[24:27], v[192:199], v6\n\t"
    "v_smfmac_f32_16x16x64_bf16 v[20:23], v[24:27], v[200:207], v6\n\t"
    "ds_read_b128 v[24:27], v10 offset:32768\n\t"
    "s_waitcnt lgkmcnt(8)\n\t"
    "v_smfmac_f32_16x16x64_bf16 v[16:19], v[28:31], v[208:215], v6\n\t"
    "ds_read_b32 v8, v15 offset:28928\n\t"
    "ds_read_b128 v[32:35], v5 offset:25600\n\t"
    "s_setprio 3\n\t"
    "s_waitcnt lgkmcnt(9)\n\t"
    "v_smfmac_f32_16x16x64_bf16 v[16:19], v[56:59], v[224:231], v6\n\t"
    "s_waitcnt lgkmcnt(8)\n\t"
    "v_smfmac_f32_16x16x64_bf16 v[16:19], v[60:63], v[240:247], v6\n\t"
    "v_smfmac_f32_16x16x64_bf16 v[20:23], v[28:31], v[216:223], v6\n\t"
    "s_waitcnt lgkmcnt(0)\n\t"
    "21:\n\t"
    "v_cmp_ne_u32 vcc, %[sn], v8\n\t"
    "s_cbranch_vccnz 22f\n\t"
    "v_mul_f32 v32, %[lsc], v32\n\t"
    "v_mul_f32 v33, %[lsc], v33\n\t"
    "v_smfmac_f32_16x16x64_bf16 v[20:23], v[56:59], v[232:239], v6\n\t"
    "v_mul_f32 v34, %[lsc], v34\n\t"
    "v_mul_f32 v35, %[lsc], v35\n\t"
    "v_add_f32 v16, v16, v18\n\t"
    "v_add_f32 v17, v17, v19\n\t"
    "v_smfmac_f32_16x16x64_bf16 v[20:23], v[60:63], v[248:255], v6\n\t"
    "v_fma_f32 v16, v16, %[lsc], v32\n\t"
    "v_fma_f32 v17, v17, %[lsc], v33\n\t"
    "v_cvt_pk_bf16_f32 v16, v16, v17\n\t"
    "ds_write_b32 v15, v16 offset:16896\n\t"
    "s_mov_b32 %[bg], %[big]\n\t"
    "s_nop 2\n\t"
    "v_add_f32 v20, v20, v22\n\t"
    "v_add_f32 v21, v21, v23\n\t"
    "v_fma_f32 v20, v20, %[lsc], v34\n\t"
    "v_fma_f32 v21, v21, %[lsc], v35\n\t"
    "v_cvt_pk_bf16_f32 v20, v20, v21\n\t"
    "ds_write_b32 v15, v20 offset:17152\n\t"
    "ds_read_b128 v[28:31], v10 offset:33792\n\t"
    "ds_read_b128 v[32:35], v10 offset:34816\n\t"
    "s_waitcnt lgkmcnt(2)\n\t"
    "s_barrier\n\t"
    "ds_read_b64_tr_b16 v[16:17], v7 offset:16384\n\t"
    "ds_read_b64_tr_b16 v[18:19], v7 offset:16448\n\t"
    "ds_read_b64_tr_b16 v[20:21], v7 offset:16896\n\t"
    "ds_read_b64_tr_b16 v[22:23], v7 offset:16960\n\t"
    "ds_read_b64_tr_b16 v[56:57], v7 offset:17408\n\t"
    "ds_read_b64_tr_b16 v[58:59], v7 offset:17472\n\t"
    "ds_read_b64_tr_b16 v[60:61], v7 offset:17920\n\t"
    "ds_read_b64_tr_b16 v[62:63], v7 offset:17984\n\t"
    "global_load_dwordx4 v[6:9], v5, %[vs] sc1\n\t"
    "global_load_dwordx4 v[10:13], v5, %[vs] offset:1024 sc1\n\t"
    "s_waitcnt lgkmcnt(6)\n\t"
    "v_mfma_f32_16x16x32_bf16 v[24:27], v[24:27], v[16:19], 0\n\t"
    "v_mfma_f32_16x16x32_bf16 v[28:31], v[28:31], v[16:19], 0\n\t"
    "s_waitcnt lgkmcnt(4)\n\t"
    "v_mfma_f32_16x16x32_bf16 v[24:27], v[32:35], v[20:23], v[24:27]\n\t"
    "v_mfma_f32_16x16x32_bf16 v[28:31], v[36:39], v[20:23], v[28:31]\n\t"
    "v_xor_b32 v39, 32, v14\n\t"
    "ds_read_b64_tr_b16 v[32:33], v14 offset:43008\n\t"
    "ds_read_b64_tr_b16 v[34:35], v14 offset:44032\n\t"
    "ds_read_b64_tr_b16 v[36:37], v39 offset:43008\n\t"
    "ds_read_b64_tr_b16 v[38:39], v39 offset:44032\n\t"
    "s_waitcnt lgkmcnt(6)\n\t"
    "v_mfma_f32_16x16x32_bf16 v[24:27], v[40:43], v[56:59], v[24:27]\n\t"
    "v_mfma_f32_16x16x32_bf16 v[28:31], v[44:47], v[56:59], v[28:31]\n\t"
    "s_waitcnt lgkmcnt(4)\n\t"
    "v_mfma_f32_16x16x32_bf16 v[24:27], v[48:51], v[60:63], v[24:27]\n\t"
    "v_mfma_f32_16x16x32_bf16 v[28:31], v[52:55], v[60:63], v[28:31]\n\t"
    "v_mov_b32 v52, %[one]\n\t"
    "v_mov_b32 v53, %[one]\n\t"
    "v_mov_b32 v54, %[one]\n\t"
    "v_mov_b32 v55, %[one]\n\t"
    "v_mov_b32 v45, 0\n\t"
    "v_mov_b32 v47, 0\n\t"
    "v_mov_b32 v48, %[one]\n\t"
    "v_max3_f32 v14, |v24|, |v25|, |v26|\n\t"
    "v_max3_f32 v40, |v27|, |v28|, |v29|\n\t"
    "v_max3_f32 v14, v14, |v30|, |v31|\n\t"
    "v_max_f32 v14, v14, v40\n\t"
    "v_cmp_nge_f32 vcc, %[big], v14\n\t"
    "v_mov_b32 v14, 0\n\t"
    "s_cbranch_vccnz 12f\n\t"
    "13:\n\t"
    "v_exp_f32 v24, v24\n\t"
    "v_exp_f32 v25, v25\n\t"
    "v_exp_f32 v26, v26\n\t"
    "v_exp_f32 v27, v27\n\t"
    "v_exp_f32 v28, v28\n\t"
    "v_exp_f32 v29, v29\n\t"
    "v_exp_f32 v30, v30\n\t"
    "v_exp_f32 v31, v31\n\t"
    "v_cvt_pk_bf16_f32 v40, v24, v25\n\t"
    "v_cvt_pk_bf16_f32 v41, v26, v27\n\t"
    "v_cvt_pk_bf16_f32 v42, v28, v29\n\t"
    "v_cvt_pk_bf16_f32 v43, v30, v31\n\t"
    "v_mov_b32 v49, 0\n\t"
    "v_mov_b32 v51, 0\n\t"
    "s_waitcnt lgkmcnt(0)\n\t"
    "v_mfma_f32_16x16x32_bf16 v[52:55], v[52:55], v[40:43], 0\n\t"
    "v_mfma_f32_16x16x32_bf16 v[24:27], v[32:35], v[40:43], 0\n\t"
    "v_mfma_f32_16x16x32_bf16 v[28:31], v[36:39], v[40:43], 0\n\t"
    "s_waitcnt vmcnt(1)\n\t"
    "5:\n\t"
    "v_mfma_f32_16x16x32_bf16 v[32:35], v[6:9], v[16:19], 0\n\t"
    "v_mfma_f32_16x16x32_bf16 v[36:39], v[6:9], v[20:23], 0\n\t"
    "s_waitcnt vmcnt(0)\n\t"
    "v_mfma_f32_16x16x32_bf16 v[32:35], v[10:13], v[56:59], v[32:35]\n\t"
    "v_mfma_f32_16x16x32_bf16 v[36:39], v[10:13], v[60:63], v[36:39]\n\t"
    "ds_read_b32 v44, v15 offset:18944\n\t"
    "ds_read_b32 v46, v15 offset:19200\n\t"
    "v_max3_u32 v40, v6, v7, v8\n\t"
    "v_max3_u32 v41, v9, v10, v11\n\t"
    "v_max3_u32 v40, v40, v12, v13\n\t"
    "v_max_u32 v40, v40, v41\n\t"
    "v_cmp_eq_u32 vcc, -1, v40\n\t"
    "s_cbranch_vccnz 4f\n\t"
    "6:\n\t"
    "s_nop 0\n\t"
    "v_add_f32 v32, v32, v38\n\t"
    "v_add_f32 v33, v33, v39\n\t"
    "v_exp_f32 v34, v32\n\t"
    "v_max_f32 v40, v32, v33\n\t"
    "v_exp_f32 v35, v33\n\t"
    "v_cmp_nge_f32 vcc, %[bg], v40\n\t"
    "s_cbranch_vccnz 10f\n\t"
    "11:\n\t"
    "v_cvt_pk_bf16_f32 v50, v34, v35\n\t"
    "17:\n\t"
    "s_waitcnt vmcnt(0) lgkmcnt(0)\n\t"
    "v_max_u32 v40, v44, v46\n\t"
    "v_cmp_eq_u32 vcc, -1, v40\n\t"
    "s_cbranch_vccnz 18f\n\t"
    "19:\n\t"
    "v_mfma_f32_16x16x16_bf16 v[52:55], v[48:49], v[50:51], v[52:55]\n\t"
    "v_mfma_f32_16x16x16_bf16 v[24:27], v[44:45], v[50:51], v[24:27]\n\t"
    "v_mfma_f32_16x16x16_bf16 v[28:31], v[46:47], v[50:51], v[28:31]\n\t"
    "s_nop 5\n\t"
    "v_rcp_f32 v42, v52\n\t"
    "s_nop 0\n\t"
    "v_pk_mul_f32 v[24:25], v[24:25], v[42:43] op_sel_hi:[1,0]\n\t"
    "v_pk_mul_f32 v[26:27], v[26:27], v[42:43] op_sel_hi:[1,0]\n\t"
    "v_pk_mul_f32 v[28:29], v[28:29], v[42:43] op_sel_hi:[1,0]\n\t"
    "v_pk_mul_f32 v[30:31], v[30:31], v[42:43] op_sel_hi:[1,0]\n\t"
    "v_cvt_pk_bf16_f32 v36, v24, v25\n\t"
    "v_cvt_pk_bf16_f32 v37, v26, v27\n\t"
    "v_cvt_pk_bf16_f32 v38, v28, v29\n\t"
    "v_cvt_pk_bf16_f32 v39, v30, v31\n\t"
    "v_min_u32 v36, -2, v36\n\t"
    "v_min_u32 v37, -2, v37\n\t"
    "v_min_u32 v38, -2, v38\n\t"
    "v_min_u32 v39, -2, v39\n\t"
    "global_store_dwordx4 v4, v[36:39], %[od] sc1\n\t"
    "ds_write_b128 v5, v[10:13] offset:21504\n\t"
    "s_setprio 1\n\t"
    "s_waitcnt lgkmcnt(0)\n\t"
    "s_barrier\n\t"
    "s_waitcnt lgkmcnt(0)\n\t"
    "s_branch 9f\n\t"
    "1:\n\t"
    "s_add_u32 %[sp], %[sp], 1\n\t"
    "s_cmp_ge_u32 %[sp], %[cap]\n\t"
    "s_cbranch_scc1 7f\n\t"
    "global_load_dwordx4 v[24:27], v5, %[ag] sc1\n\t"
    "global_load_dwordx4 v[28:31], v5, %[ag] offset:1024 sc1\n\t"
    "s_waitcnt lgkmcnt(0)\n\t"
    "s_memtime %[tc]\n\t"
    "s_nop 7\n\t"
    "s_branch 3b\n\t"
    "7:\n\t"
    "s_mov_b32 %[cap], 0\n\t"
    "s_branch 2b\n\t"
    "4:\n\t"
    "s_add_u32 %[sp], %[sp], 0x10000\n\t"
    "s_lshr_b32 %[st], %[sp], 16\n\t"
    "s_cmp_ge_u32 %[st], %[cap]\n\t"
    "s_cbranch_scc1 8f\n\t"
    "global_load_dwordx4 v[6:9], v5, %[vs] sc1\n\t"
    "global_load_dwordx4 v[10:13], v5, %[vs] offset:1024 sc1\n\t"
    "s_waitcnt vmcnt(0) lgkmcnt(0)\n\t"
    "s_branch 5b\n\t"
    "8:\n\t"
    "s_mov_b32 %[cap], 0\n\t"
    "s_branch 6b\n\t"
    "18:\n\t"
    "s_add_u32 %[sp], %[sp], 0x10000\n\t"
    "s_lshr_b32 %[st], %[sp], 16\n\t"
    "s_cmp_ge_u32 %[st], %[cap]\n\t"
    "s_cbranch_scc1 20f\n\t"
    "ds_read_b32 v44, v15 offset:18944\n\t"
    "ds_read_b32 v46, v15 offset:19200\n\t"
    "s_waitcnt vmcnt(0) lgkmcnt(0)\n\t"
    "s_branch 17b\n\t"
    "20:\n\t"
    "s_mov_b32 %[cap], 0\n\t"
    "s_branch 19b\n\t"
    "22:\n\t"
    "ds_read_b32 v8, v15 offset:28928\n\t"
    "ds_read_b128 v[32:35], v5 offset:25600\n\t"
    "s_waitcnt lgkmcnt(0)\n\t"
    "s_branch 21b\n\t"
    "10:\n\t"
    "v_max_f32 v40, v40, v14\n\t"
    "v_mov_b32 v42, v40\n\t"
    "s_nop 1\n\t"
    "v_permlane16_swap_b32 v40, v42\n\t"
    "v_max_f32 v40, v40, v42\n\t"
    "v_mov_b32 v42, v40\n\t"
    "s_nop 1\n\t"
    "v_permlane32_swap_b32 v40, v42\n\t"
    "v_max_f32 v40, v40, v42\n\t"
    "v_sub_f32 v34, v32, v40\n\t"
    "v_sub_f32 v35, v33, v40\n\t"
    "v_sub_f32 v40, v14, v40\n\t"
    "v_exp_f32 v40, v40\n\t"
    "v_exp_f32 v34, v34\n\t"
    "v_exp_f32 v35, v35\n\t"
    "v_mul_f32 v24, v24, v40\n\t"
    "v_mul_f32 v25, v25, v40\n\t"
    "v_mul_f32 v26, v26, v40\n\t"
    "v_mul_f32 v27, v27, v40\n\t"
    "v_mul_f32 v28, v28, v40\n\t"
    "v_mul_f32 v29, v29, v40\n\t"
    "v_mul_f32 v30, v30, v40\n\t"
    "v_mul_f32 v31, v31, v40\n\t"
    "v_mul_f32 v52, v52, v40\n\t"
    "v_mul_f32 v53, v53, v40\n\t"
    "v_mul_f32 v54, v54, v40\n\t"
    "v_mul_f32 v55, v55, v40\n\t"
    "s_branch 11b\n\t"
    "12:\n\t"
    "v_max3_f32 v14, v24, v25, v26\n\t"
    "v_max3_f32 v40, v27, v28, v29\n\t"
    "v_max3_f32 v14, v14, v30, v31\n\t"
    "v_max3_f32 v14, v14, v40, %[neg]\n\t"
    "v_mov_b32 v40, v14\n\t"
    "s_nop 1\n\t"
    "v_permlane16_swap_b32 v14, v40\n\t"
    "v_max_f32 v14, v14, v40\n\t"
    "v_mov_b32 v40, v14\n\t"
    "s_nop 1\n\t"
    "v_permlane32_swap_b32 v14, v40\n\t"
    "v_max_f32 v14, v14, v40\n\t"
    "v_sub_f32 v24, v24, v14\n\t"
    "v_sub_f32 v25, v25, v14\n\t"
    "v_sub_f32 v26, v26, v14\n\t"
    "v_sub_f32 v27, v27, v14\n\t"
    "v_sub_f32 v28, v28, v14\n\t"
    "v_sub_f32 v29, v29, v14\n\t"
    "v_sub_f32 v30, v30, v14\n\t"
    "v_sub_f32 v31, v31, v14\n\t"
    "s_mov_b32 %[bg], 0x7fc00000\n\t"
    "s_branch 13b\n\t"
    "9:"
    : [tc] "=&s"(tc), [sp] "=&s"(sp), [cap] "+s"(cap), [st] "=&s"(st), [bg] "=&s"(bg)
    : [ag] "s"(ag), [c44] "s"(0x4444u), [lsc] "s"(LSC), [sn] "s"(sn), [pb] "s"(pb), [vs] "s"(vs), [od] "s"(od), [neg] "s"(-1.0e38f), [one] "s"(0x3F803F80u), [big] "s"(24.0f), "{v[64:95]}"(W.w0), "{v[96:127]}"(W.w1), "{v[128:159]}"(W.w2), "{v[160:191]}"(W.w3), "{v[192:223]}"(W.w4), "{v[224:255]}"(W.w5)
    : "v4", "v5", "v6", "v7", "v8", "v9", "v10", "v11", "v12", "v13", "v14", "v15", "v16", "v17", "v18", "v19", "v20", "v21", "v22", "v23", "v24", "v25", "v26", "v27", "v28", "v29", "v30", "v31", "v32", "v33", "v34", "v35", "v36", "v37", "v38", "v39", "v40", "v41", "v42", "v43", "v44", "v45", "v46", "v47", "v48", "v49", "v50", "v51", "v52", "v53", "v54", "v55", "v56", "v57", "v58", "v59", "v60", "v61", "v62", "v63", "vcc", "scc", "m0", "memory");
}
template <> __device__ __forceinline__ void qk_step<2>(const QKW& W, const char* ag, const char* vs, const char* v2, const char* db, const char* od, unsigned pb, unsigned sn, unsigned& cap, unsigned& sp,
    unsigned long long& tc, unsigned long long* ts, unsigned& d0, unsigned& d1) {
  unsigned st, bg;
  asm volatile("v_mbcnt_lo_u32_b32 v60, -1, 0\n\t"
    "v_mbcnt_hi_u32_b32 v60, -1, v60\n\t"
    "v_lshlrev_b32 v5, 4, v60\n\t"
    "s_mov_b32 %[sp], 0\n\t"
    "s_setprio 2\n\t"
    "global_load_dwordx4 v[24:27], v5, %[ag] sc1\n\t"
    "global_load_dwordx4 v[28:31], v5, %[ag] offset:1024 sc1\n\t"
    "s_memtime %[tc]\n\t"
    "v_lshrrev_b32 v32, 4, v60\n\t"
    "v_and_b32 v33, 15, v60\n\t"
    "v_lshrrev_b32 v34, 2, v33\n\t"
    "v_and_b32 v35, 3, v60\n\t"
    "v_and_b32 v61, 1, v60\n\t"
    "v_lshlrev_b32 v61, 3, v61\n\t"
    "v_lshl_or_b32 v7, v34, 4, v61\n\t"
    "v_lshl_or_b32 v7, v32, 7, v7\n\t"
    "v_and_b32 v10, 2, v34\n\t"
    "v_xor_b32 v10, v10, v32\n\t"
    "v_lshl_or_b32 v10, v10, 4, %[pb]\n\t"
    "v_lshl_or_b32 v10, v33, 6, v10\n\t"
    "v_and_b32 v14, 32, v60\n\t"
    "v_lshl_or_b32 v14, v35, 3, v14\n\t"
    "v_lshrrev_b32 v34, 2, v60\n\t"
    "v_lshl_or_b32 v14, v34, 6, v14\n\t"
    "v_or_b32 v14, %[pb], v14\n\t"
    "v_and_b32 v61, 3, v60\n\t"
    "v_lshlrev_b32 v61, 6, v61\n\t"
    "v_lshl_or_b32 v4, v32, 4, v61\n\t"
    "v_and_b32 v61, 4, v60\n\t"
    "v_lshl_or_b32 v4, v61, 10, v4\n\t"
    "v_and_b32 v61, 8, v60\n\t"
    "v_lshl_or_b32 v4, v61, 14, v4\n\t"
    "v_or_b32 v4, 512, v4\n\t"
    "v_and_b32 v61, 2, v60\n\t"
    "v_mul_u32_u24 v61, 0x5555, v61\n\t"
    "v_or_b32 v6, %[c44], v61\n\t"
    "v_lshlrev_b32 v61, 2, v32\n\t"
    "v_lshl_or_b32 v15, v33, 4, v61\n\t"
    "3:\n\t"
    "v_mov_b32 v16, 0\n\t"
    "v_mov_b32 v17, 0\n\t"
    "v_mov_b32 v18, 0\n\t"
    "v_mov_b32 v19, 0\n\t"
    "v_mov_b32 v20, 0\n\t"
    "v_mov_b32 v21, 0\n\t"
    "v_mov_b32 v22, 0\n\t"
    "v_mov_b32 v23, 0\n\t"
    "s_waitcnt vmcnt(0) lgkmcnt(0)\n\t"
    "33:\n\t"
    "ds_write_b128 v5, v[24:27] offset:4096\n\t"
    "ds_write_b128 v5, v[28:31] offset:5120\n\t"
    "v_smfmac_f32_16x16x64_bf16 v[16:19], v[24:27], v[128:135], v6\n\t"
    "v_max3_u32 v60, v24, v25, v26\n\t"
    "v_max3_u32 v61, v27, v28, v29\n\t"
    "v_smfmac_f32_16x16x64_bf16 v[20:23], v[24:27], v[136:143], v6\n\t"
    "v_max3_u32 v60, v60, v30, v31\n\t"
    "v_max_u32 v60, v60, v61\n\t"
    "v_cmp_eq_u32 vcc, -1, v60\n\t"
    "s_cbranch_vccnz 1f\n\t"
    "2:\n\t"
    "s_waitcnt lgkmcnt(0)\n\t"
    "s_barrier\n\t"
    "ds_read_b128 v[32:35], v5 offset:0\n\t"
    "ds_read_b128 v[36:39], v5 offset:1024\n\t"
    "v_smfmac_f32_16x16x64_bf16 v[16:19], v[28:31], v[144:151], v6\n\t"
    "ds_read_b128 v[40:43], v5 offset:2048\n\t"
    "ds_read_b128 v[44:47], v5 offset:3072\n\t"
    "v_smfmac_f32_16x16x64_bf16 v[20:23], v[28:31], v[152:159], v6\n\t"
    "s_waitcnt lgkmcnt(3)\n\t"
    "v_smfmac_f32_16x16x64_bf16 v[16:19], v[32:35], v[64:71], v6\n\t"
    "ds_read_b128 v[48:51], v5 offset:6144\n\t"
    "v_smfmac_f32_16x16x64_bf16 v[20:23], v[32:35], v[72:79], v6\n\t"
    "ds_read_b128 v[52:55], v5 offset:7168\n\t"
    "s_waitcnt lgkmcnt(4)\n\t"
    "v_smfmac_f32_16x16x64_bf16 v[16:19], v[36:39], v[80:87], v6\n\t"
    "ds_read_b128 v[24:27], v5 offset:8192\n\t"
    "v_smfmac_f32_16x16x64_bf16 v[20:23], v[36:39], v[88:95], v6\n\t"
    "ds_read_b128 v[28:31], v5 offset:9216\n\t"
    "s_waitcnt lgkmcnt(5)\n\t"
    "v_smfmac_f32_16x16x64_bf16 v[16:19], v[40:43], v[96:103], v6\n\t"
    "ds_read_b128 v[56:59], v5 offset:10240\n\t"
    "v_smfmac_f32_16x16x64_bf16 v[20:23], v[40:43], v[104:111], v6\n\t"
    "ds_read_b128 v[60:63], v5 offset:11264\n\t"
    "ds_read_b128 v[36:39], v10 offset:35840\n\t"
    "s_waitcnt lgkmcnt(7)\n\t"
    "v_smfmac_f32_16x16x64_bf16 v[16:19], v[44:47], v[112:119], v6\n\t"
    "ds_read_b128 v[40:43], v10 offset:36864\n\t"
    "v_smfmac_f32_16x16x64_bf16 v[20:23], v[44:47], v[120:127], v6\n\t"
    "ds_read_b128 v[44:47], v10 offset:37888\n\t"
    "s_waitcnt lgkmcnt(8)\n\t"
    "v_smfmac_f32_16x16x64_bf16 v[16:19], v[48:51], v[160:167], v6\n\t"
    "v_smfmac_f32_16x16x64_bf16 v[20:23], v[48:51], v[168:175], v6\n\t"
    "ds_read_b128 v[48:51], v10 offset:38912\n\t"
    "s_waitcnt lgkmcnt(8)\n\t"
    "v_smfmac_f32_16x16x64_bf16 v[16:19], v[52:55], v[176:183], v6\n\t"
    "v_smfmac_f32_16x16x64_bf16 v[20:23], v[52:55], v[184:191], v6\n\t"
    "ds_read_b128 v[52:55], v10 offset:39936\n\t"
    "s_waitcnt lgkmcnt(8)\n\t"
    "v_smfmac_f32_16x16x64_bf16 v[16:19], v[24:27], v[192:199], v6\n\t"
    "v_smfmac_f32_16x16x64_bf16 v[20:23], v[24:27], v[200:207], v6\n\t"
    "ds_read_b128 v[24:27], v10 offset:32768\n\t"
    "s_waitcnt lgkmcnt(8)\n\t"
    "v_smfmac_f32_16x16x64_bf16 v[16:19], v[28:31], v[208:215], v6\n\t"
    "ds_read_b32 v8, v15 offset:29184\n\t"
    "ds_read_b128 v[32:35], v5 offset:26624\n\t"
    "s_setprio 3\n\t"
    "s_waitcnt lgkmcnt(9)\n\t"
    "v_smfmac_f32_16x16x64_bf16 v[16:19], v[56:59], v[224:231], v6\n\t"
    "s_waitcnt lgkmcnt(8)\n\t"
    "v_smfmac_f32_16x16x64_bf16 v[16:19], v[60:63], v[240:247], v6\n\t"
    "v_smfmac_f32_16x16x64_bf16 v[20:23], v[28:31], v[216:223], v6\n\t"
    "s_waitcnt lgkmcnt(0)\n\t"
    "21:\n\t"
    "v_cmp_ne_u32 vcc, %[sn], v8\n\t"
    "s_cbranch_vccnz 22f\n\t"
    "v_mul_f32 v32, %[lsc], v32\n\t"
    "v_mul_f32 v33, %[lsc], v33\n\t"
    "v_smfmac_f32_16x16x64_bf16 v[20:23], v[56:59], v[232:239], v6\n\t"
    "v_mul_f32 v34, %[lsc], v34\n\t"
    "v_mul_f32 v35, %[lsc], v35\n\t"
    "v_add_f32 v16, v16, v18\n\t"
    "v_add_f32 v17, v17, v19\n\t"
    "v_smfmac_f32_16x16x64_bf16 v[20:23], v[60:63], v[248:255], v6\n\t"
    "v_fma_f32 v16, v16, %[lsc], v32\n\t"
    "v_fma_f32 v17, v17, %[lsc], v33\n\t"
    "v_cvt_pk_bf16_f32 v16, v16, v17\n\t"
    "ds_write_b32 v15, v16 offset:17408\n\t"
    "s_mov_b32 %[bg], %[big]\n\t"
    "s_nop 2\n\t"
    "v_add_f32 v20, v20, v22\n\t"
    "v_add_f32 v21, v21, v23\n\t"
    "v_fma_f32 v20, v20, %[lsc], v34\n\t"
    "v_fma_f32 v21, v21, %[lsc], v35\n\t"
    "v_cvt_pk_bf16_f32 v20, v20, v21\n\t"
    "ds_write_b32 v15, v20 offset:17664\n\t"
    "ds_read_b128 v[28:31], v10 offset:33792\n\t"
    "ds_read_b128 v[32:35], v10 offset:34816\n\t"
    "s_waitcnt lgkmcnt(2)\n\t"
    "s_barrier\n\t"
    "ds_read_b64_tr_b16 v[16:17], v7 offset:16384\n\t"
    "ds_read_b64_tr_b16 v[18:19], v7 offset:16448\n\t"
    "ds_read_b64_tr_b16 v[20:21], v7 offset:16896\n\t"
    "ds_read_b64_tr_b16 v[22:23], v7 offset:16960\n\t"
    "ds_read_b64_tr_b16 v[56:57], v7 offset:17408\n\t"
    "ds_read_b64_tr_b16 v[58:59], v7 offset:17472\n\t"
    "ds_read_b64_tr_b16 v[60:61], v7 offset:17920\n\t"
    "ds_read_b64_tr_b16 v[62:63], v7 offset:17984\n\t"
    "global_load_dwordx4 v[6:9], v5, %[vs] sc1\n\t"
    "global_load_dwordx4 v[10:13], v5, %[vs] offset:1024 sc1\n\t"
    "s_waitcnt lgkmcnt(6)\n\t"
    "v_mfma_f32_16x16x32_bf16 v[24:27], v[24:27], v[16:19], 0\n\t"
    "v_mfma_f32_16x16x32_bf16 v[28:31], v[28:31], v[16:19], 0\n\t"
    "s_waitcnt lgkmcnt(4)\n\t"
    "v_mfma_f32_16x16x32_bf16 v[24:27], v[32:35], v[20:23], v[24:27]\n\t"
    "v_mfma_f32_16x16x32_bf16 v[28:31], v[36:39], v[20:23], v[28:31]\n\t"
    "v_xor_b32 v39, 32, v14\n\t"
    "ds_read_b64_tr_b16 v[32:33], v14 offset:45056\n\t"
    "ds_read_b64_tr_b16 v[34:35], v14 offset:46080\n\t"
    "ds_read_b64_tr_b16 v[36:37], v39 offset:45056\n\t"
    "ds_read_b64_tr_b16 v[38:39], v39 offset:46080\n\t"
    "s_waitcnt lgkmcnt(6)\n\t"
    "v_mfma_f32_16x16x32_bf16 v[24:27], v[40:43], v[56:59], v[24:27]\n\t"
    "v_mfma_f32_16x16x32_bf16 v[28:31], v[44:47], v[56:59], v[28:31]\n\t"
    "s_waitcnt lgkmcnt(4)\n\t"
    "v_mfma_f32_16x16x32_bf16 v[24:27], v[48:51], v[60:63], v[24:27]\n\t"
    "v_mfma_f32_16x16x32_bf16 v[28:31], v[52:55], v[60:63], v[28:31]\n\t"
    "v_mov_b32 v52, %[one]\n\t"
    "v_mov_b32 v53, %[one]\n\t"
    "v_mov_b32 v54, %[one]\n\t"
    "v_mov_b32 v55, %[one]\n\t"
    "v_mov_b32 v45, 0\n\t"
    "v_mov_b32 v47, 0\n\t"
    "v_mov_b32 v48, %[one]\n\t"
    "v_max3_f32 v14, |v24|, |v25|, |v26|\n\t"
    "v_max3_f32 v40, |v27|, |v28|, |v29|\n\t"
    "v_max3_f32 v14, v14, |v30|, |v31|\n\t"
    "v_max_f32 v14, v14, v40\n\t"
    "v_cmp_nge_f32 vcc, %[big], v14\n\t"
    "v_mov_b32 v14, 0\n\t"
    "s_cbranch_vccnz 12f\n\t"
    "13:\n\t"
    "v_exp_f32 v24, v24\n\t"
    "v_exp_f32 v25, v25\n\t"
    "v_exp_f32 v26, v26\n\t"
    "v_exp_f32 v27, v27\n\t"
    "v_exp_f32 v28, v28\n\t"
    "v_exp_f32 v29, v29\n\t"
    "v_exp_f32 v30, v30\n\t"
    "v_exp_f32 v31, v31\n\t"
    "v_lshrrev_b32 v46, 2, v5\n\t"
    "global_load_dword v44, v46, %[v2] sc1\n\t"
    "global_load_dword v46, v46, %[v2] offset:256 sc1\n\t"
    "v_cvt_pk_bf16_f32 v40, v24, v25\n\t"
    "v_cvt_pk_bf16_f32 v41, v26, v27\n\t"
    "v_cvt_pk_bf16_f32 v42, v28, v29\n\t"
    "v_cvt_pk_bf16_f32 v43, v30, v31\n\t"
    "v_mov_b32 v49, 0\n\t"
    "v_mov_b32 v51, 0\n\t"
    "s_waitcnt lgkmcnt(0)\n\t"
    "v_mfma_f32_16x16x32_bf16 v[52:55], v[52:55], v[40:43], 0\n\t"
    "v_mfma_f32_16x16x32_bf16 v[24:27], v[32:35], v[40:43], 0\n\t"
    "v_mfma_f32_16x16x32_bf16 v[28:31], v[36:39], v[40:43], 0\n\t"
    "s_waitcnt vmcnt(3)\n\t"
    "5:\n\t"
    "v_mfma_f32_16x16x32_bf16 v[32:35], v[6:9], v[16:19], 0\n\t"
    "v_mfma_f32_16x16x32_bf16 v[36:39], v[6:9], v[20:23], 0\n\t"
    "s_waitcnt vmcnt(2)\n\t"
    "v_mfma_f32_16x16x32_bf16 v[32:35], v[10:13], v[56:59], v[32:35]\n\t"
    "v_mfma_f32_16x16x32_bf16 v[36:39], v[10:13], v[60:63], v[36:39]\n\t"
    "v_max3_u32 v40, v6, v7, v8\n\t"
    "v_max3_u32 v41, v9, v10, v11\n\t"
    "v_max3_u32 v40, v40, v12, v13\n\t"
    "v_max_u32 v40, v40, v41\n\t"
    "v_cmp_eq_u32 vcc, -1, v40\n\t"
    "s_cbranch_vccnz 4f\n\t"
    "6:\n\t"
    "s_nop 0\n\t"
    "s_nop 0\n\t"
    "v_add_f32 v32, v32, v38\n\t"
    "v_add_f32 v33, v33, v39\n\t"
    "v_exp_f32 v34, v32\n\t"
    "v_max_f32 v40, v32, v33\n\t"
    "v_exp_f32 v35, v33\n\t"
    "v_cmp_nge_f32 vcc, %[bg], v40\n\t"
    "s_cbranch_vccnz 10f\n\t"
    "11:\n\t"
    "v_cvt_pk_bf16_f32 v50, v34, v35\n\t"
    "17:\n\t"
    "s_waitcnt vmcnt(0) lgkmcnt(0)\n\t"
    "v_max_u32 v40, v44, v46\n\t"
    "v_cmp_eq_u32 vcc, -1, v40\n\t"
    "s_cbranch_vccnz 18f\n\t"
    "19:\n\t"
    "s_memrealtime s[36:37]\n\t"
    "v_mfma_f32_16x16x16_bf16 v[52:55], v[48:49], v[50:51], v[52:55]\n\t"
    "v_mfma_f32_16x16x16_bf16 v[24:27], v[44:45], v[50:51], v[24:27]\n\t"
    "v_mfma_f32_16x16x16_bf16 v[28:31], v[46:47], v[50:51], v[28:31]\n\t"
    "s_nop 5\n\t"
    "v_rcp_f32 v42, v52\n\t"
    "s_nop 0\n\t"
    "v_pk_mul_f32 v[24:25], v[24:25], v[42:43] op_sel_hi:[1,0]\n\t"
    "v_pk_mul_f32 v[26:27], v[26:27], v[42:43] op_sel_hi:[1,0]\n\t"
    "v_pk_mul_f32 v[28:29], v[28:29], v[42:43] op_sel_hi:[1,0]\n\t"
    "v_pk_mul_f32 v[30:31], v[30:31], v[42:43] op_sel_hi:[1,0]\n\t"
    "v_cvt_pk_bf16_f32 v36, v24, v25\n\t"
    "v_cvt_pk_bf16_f32 v37, v26, v27\n\t"
    "v_cvt_pk_bf16_f32 v38, v28, v29\n\t"
    "v_cvt_pk_bf16_f32 v39, v30, v31\n\t"
    "v_min_u32 v36, -2, v36\n\t"
    "v_min_u32 v37, -2, v37\n\t"
    "v_min_u32 v38, -2, v38\n\t"
    "v_min_u32 v39, -2, v39\n\t"
    "global_store_dwordx4 v4, v[36:39], %[od] sc1\n\t"
    "v_lshlrev_b32 v40, 10, v5\n\t"
    "s_waitcnt lgkmcnt(0)\n\t"
    "s_and_b32 s36, s36, 0xffffff\n\t"
    "s_or_b32 s36, s36, %[sn]\n\t"
    "v_mov_b32 v41, s36\n\t"
    "s_mov_b64 exec, 7\n\t"
    "global_store_dword v40, v41, %[db] offset:0 sc1\n\t"
    "s_mov_b64 exec, -1\n\t"
    "ds_write_b32 v15, v44 offset:19456\n\t"
    "ds_write_b32 v15, v46 offset:19712\n\t"
    "s_setprio 1\n\t"
    "s_waitcnt lgkmcnt(0)\n\t"
    "s_barrier\n\t"
    "s_waitcnt lgkmcnt(0)\n\t"
    "s_branch 9f\n\t"
    "1:\n\t"
    "s_add_u32 %[sp], %[sp], 1\n\t"
    "s_cmp_ge_u32 %[sp], %[cap]\n\t"
    "s_cbranch_scc1 7f\n\t"
    "global_load_dwordx4 v[24:27], v5, %[ag] sc1\n\t"
    "global_load_dwordx4 v[28:31], v5, %[ag] offset:1024 sc1\n\t"
    "s_waitcnt lgkmcnt(0)\n\t"
    "s_memtime %[tc]\n\t"
    "s_nop 7\n\t"
    "s_branch 3b\n\t"
    "7:\n\t"
    "s_mov_b32 %[cap], 0\n\t"
    "s_branch 2b\n\t"
    "4:\n\t"
    "s_add_u32 %[sp], %[sp], 0x10000\n\t"
    "s_lshr_b32 %[st], %[sp], 16\n\t"
    "s_cmp_ge_u32 %[st], %[cap]\n\t"
    "s_cbranch_scc1 8f\n\t"
    "global_load_dwordx4 v[6:9], v5, %[vs] sc1\n\t"
    "global_load_dwordx4 v[10:13], v5, %[vs] offset:1024 sc1\n\t"
    "s_waitcnt vmcnt(0) lgkmcnt(0)\n\t"
    "s_branch 5b\n\t"
    "8:\n\t"
    "s_mov_b32 %[cap], 0\n\t"
    "s_branch 6b\n\t"
    "18:\n\t"
    "s_add_u32 %[sp], %[sp], 0x10000\n\t"
    "s_lshr_b32 %[st], %[sp], 16\n\t"
    "s_cmp_ge_u32 %[st], %[cap]\n\t"
    "s_cbranch_scc1 20f\n\t"
    "v_lshrrev_b32 v46, 2, v5\n\t"
    "global_load_dword v44, v46, %[v2] sc1\n\t"
    "global_load_dword v46, v46, %[v2] offset:256 sc1\n\t"
    "s_waitcnt vmcnt(0) lgkmcnt(0)\n\t"
    "s_branch 17b\n\t"
    "20:\n\t"
    "s_mov_b32 %[cap], 0\n\t"
    "s_branch 19b\n\t"
    "22:\n\t"
    "ds_read_b32 v8, v15 offset:29184\n\t"
    "ds_read_b128 v[32:35], v5 offset:26624\n\t"
    "s_waitcnt lgkmcnt(0)\n\t"
    "s_branch 21b\n\t"
    "10:\n\t"
    "v_max_f32 v40, v40, v14\n\t"
    "v_mov_b32 v42, v40\n\t"
    "s_nop 1\n\t"
    "v_permlane16_swap_b32 v40, v42\n\t"
    "v_max_f32 v40, v40, v42\n\t"
    "v_mov_b32 v42, v40\n\t"
    "s_nop 1\n\t"
    "v_permlane32_swap_b32 v40, v42\n\t"
    "v_max_f32 v40, v40, v42\n\t"
    "v_sub_f32 v34, v32, v40\n\t"
    "v_sub_f32 v35, v33, v40\n\t"
    "v_sub_f32 v40, v14, v40\n\t"
    "v_exp_f32 v40, v40\n\t"
    "v_exp_f32 v34, v34\n\t"
    "v_exp_f32 v35, v35\n\t"
    "v_mul_f32 v24, v24, v40\n\t"
    "v_mul_f32 v25, v25, v40\n\t"
    "v_mul_f32 v26, v26, v40\n\t"
    "v_mul_f32 v27, v27, v40\n\t"
    "v_mul_f32 v28, v28, v40\n\t"
    "v_mul_f32 v29, v29, v40\n\t"
    "v_mul_f32 v30, v30, v40\n\t"
    "v_mul_f32 v31, v31, v40\n\t"
    "v_mul_f32 v52, v52, v40\n\t"
    "v_mul_f32 v53, v53, v40\n\t"
    "v_mul_f32 v54, v54, v40\n\t"
    "v_mul_f32 v55, v55, v40\n\t"
    "s_branch 11b\n\t"
    "12:\n\t"
    "v_max3_f32 v14, v24, v25, v26\n\t"
    "v_max3_f32 v40, v27, v28, v29\n\t"
    "v_max3_f32 v14, v14, v30, v31\n\t"
    "v_max3_f32 v14, v14, v40, %[neg]\n\t"
    "v_mov_b32 v40, v14\n\t"
    "s_nop 1\n\t"
    "v_permlane16_swap_b32 v14, v40\n\t"
    "v_max_f32 v14, v14, v40\n\t"
    "v_mov_b32 v40, v14\n\t"
    "s_nop 1\n\t"
    "v_permlane32_swap_b32 v14, v40\n\t"
    "v_max_f32 v14, v14, v40\n\t"
    "v_sub_f32 v24, v24, v14\n\t"
    "v_sub_f32 v25, v25, v14\n\t"
    "v_sub_f32 v26, v26, v14\n\t"
    "v_sub_f32 v27, v27, v14\n\t"
    "v_sub_f32 v28, v28, v14\n\t"
    "v_sub_f32 v29, v29, v14\n\t"
    "v_sub_f32 v30, v30, v14\n\t"
    "v_sub_f32 v31, v31, v14\n\t"
    "s_mov_b32 %[bg], 0x7fc00000\n\t"
    "s_branch 13b\n\t"
    "9:"
    : [tc] "=&s"(tc), [sp] "=&s"(sp), [cap] "+s"(cap), [st] "=&s"(st), [bg] "=&s"(bg)
    : [ag] "s"(ag), [c44] "s"(0x4444u), [lsc] "s"(LSC), [sn] "s"(sn), [pb] "s"(pb), [vs] "s"(vs), [od] "s"(od), [neg] "s"(-1.0e38f), [one] "s"(0x3F803F80u), [big] "s"(24.0f), [v2] "s"(v2), [db] "s"(db), "{v[64:95]}"(W.w0), "{v[96:127]}"(W.w1), "{v[128:159]}"(W.w2), "{v[160:191]}"(W.w3), "{v[192:223]}"(W.w4), "{v[224:255]}"(W.w5)
    : "v4", "v5", "v6", "v7", "v8", "v9", "v10", "v11", "v12", "v13", "v14", "v15", "v16", "v17", "v18", "v19", "v20", "v21", "v22", "v23", "v24", "v25", "v26", "v27", "v28", "v29", "v30", "v31", "v32", "v33", "v34", "v35", "v36", "v37", "v38", "v39", "v40", "v41", "v42", "v43", "v44", "v45", "v46", "v47", "v48", "v49", "v50", "v51", "v52", "v53", "v54", "v55", "v56", "v57", "v58", "v59", "v60", "v61", "v62", "v63", "vcc", "scc", "m0", "memory", "s36", "s37", "s38");
}
template <> __device__ __forceinline__ void qk_step<3>(const QKW& W, const char* ag, const char* vs, const char* v2, const char* db, const char* od, unsigned pb, unsigned sn, unsigned& cap, unsigned& sp,
    unsigned long long& tc, unsigned long long* ts, unsigned& d0, unsigned& d1) {
  unsigned st, bg;
  asm volatile("v_mbcnt_lo_u32_b32 v60, -1, 0\n\t"
    "v_mbcnt_hi_u32_b32 v60, -1, v60\n\t"
    "v_lshlrev_b32 v5, 4, v60\n\t"
    "s_mov_b32 %[sp], 0\n\t"
    "s_setprio 2\n\t"
    "global_load_dwordx4 v[24:27], v5, %[ag] sc1\n\t"
    "global_load_dwordx4 v[28:31], v5, %[ag] offset:1024 sc1\n\t"
    "s_memtime %[tc]\n\t"
    "v_lshrrev_b32 v32, 4, v60\n\t"
    "v_and_b32 v33, 15, v60\n\t"
    "v_lshrrev_b32 v34, 2, v33\n\t"
    "v_and_b32 v35, 3, v60\n\t"
    "v_and_b32 v61, 1, v60\n\t"
    "v_lshlrev_b32 v61, 3, v61\n\t"
    "v_lshl_or_b32 v7, v34, 4, v61\n\t"
    "v_lshl_or_b32 v7, v32, 7, v7\n\t"
    "v_and_b32 v10, 2, v34\n\t"
    "v_xor_b32 v10, v10, v32\n\t"
    "v_lshl_or_b32 v10, v10, 4, %[pb]\n\t"
    "v_lshl_or_b32 v10, v33, 6, v10\n\t"
    "v_and_b32 v14, 32, v60\n\t"
    "v_lshl_or_b32 v14, v35, 3, v14\n\t"
    "v_lshrrev_b32 v34, 2, v60\n\t"
    "v_lshl_or_b32 v14, v34, 6, v14\n\t"
    "v_or_b32 v14, %[pb], v14\n\t"
    "v_and_b32 v61, 3, v60\n\t"
    "v_lshlrev_b32 v61, 6, v61\n\t"
    "v_lshl_or_b32 v4, v32, 4, v61\n\t"
    "v_and_b32 v61, 4, v60\n\t"
    "v_lshl_or_b32 v4, v61, 10, v4\n\t"
    "v_and_b32 v61, 8, v60\n\t"
    "v_lshl_or_b32 v4, v61, 14, v4\n\t"
    "v_or_b32 v4, 768, v4\n\t"
    "v_and_b32 v61, 2, v60\n\t"
    "v_mul_u32_u24 v61, 0x5555, v61\n\t"
    "v_or_b32 v6, %[c44], v61\n\t"
    "v_lshlrev_b32 v61, 2, v32\n\t"
    "v_lshl_or_b32 v15, v33, 4, v61\n\t"
    "3:\n\t"
    "v_mov_b32 v16, 0\n\t"
    "v_mov_b32 v17, 0\n\t"
    "v_mov_b32 v18, 0\n\t"
    "v_mov_b32 v19, 0\n\t"
    "v_mov_b32 v20, 0\n\t"
    "v_mov_b32 v21, 0\n\t"
    "v_mov_b32 v22, 0\n\t"
    "v_mov_b32 v23, 0\n\t"
    "s_waitcnt vmcnt(0) lgkmcnt(0)\n\t"
    "33:\n\t"
    "ds_write_b128 v5, v[24:27] offset:6144\n\t"
    "ds_write_b128 v5, v[28:31] offset:7168\n\t"
    "v_smfmac_f32_16x16x64_bf16 v[16:19], v[24:27], v[160:167], v6\n\t"
    "v_max3_u32 v60, v24, v25, v26\n\t"
    "v_max3_u32 v61, v27, v28, v29\n\t"
    "v_smfmac_f32_16x16x64_bf16 v[20:23], v[24:27], v[168:175], v6\n\t"
    "v_max3_u32 v60, v60, v30, v31\n\t"
    "v_max_u32 v60, v60, v61\n\t"
    "v_cmp_eq_u32 vcc, -1, v60\n\t"
    "s_cbranch_vccnz 1f\n\t"
    "2:\n\t"
    "s_waitcnt lgkmcnt(0)\n\t"
    "s_barrier\n\t"
    "ds_read_b128 v[32:35], v5 offset:0\n\t"
    "ds_read_b128 v[36:39], v5 offset:1024\n\t"
    "v_smfmac_f32_16x16x64_bf16 v[16:19], v[28:31], v[176:183], v6\n\t"
    "ds_read_b128 v[40:43], v5 offset:2048\n\t"
    "ds_read_b128 v[44:47], v5 offset:3072\n\t"
    "v_smfmac_f32_16x16x64_bf16 v[20:23], v[28:31], v[184:191], v6\n\t"
    "s_waitcnt lgkmcnt(3)\n\t"
    "v_smfmac_f32_16x16x64_bf16 v[16:19], v[32:35], v[64:71], v6\n\t"
    "ds_read_b128 v[48:51], v5 offset:4096\n\t"
    "v_smfmac_f32_16x16x64_bf16 v[20:23], v[32:35], v[72:79], v6\n\t"
    "ds_read_b128 v[52:55], v5 offset:5120\n\t"
    "s_waitcnt lgkmcnt(4)\n\t"
    "v_smfmac_f32_16x16x64_bf16 v[16:19], v[36:39], v[80:87], v6\n\t"
    "ds_read_b128 v[24:27], v5 offset:8192\n\t"
    "v_smfmac_f32_16x16x64_bf16 v[20:23], v[36:39], v[88:95], v6\n\t"
    "ds_read_b128 v[28:31], v5 offset:9216\n\t"
    "s_waitcnt lgkmcnt(5)\n\t"
    "v_smfmac_f32_16x16x64_bf16 v[16:19], v[40:43], v[96:103], v6\n\t"
    "ds_read_b128 v[56:59], v5 offset:10240\n\t"
    "v_smfmac_f32_16x16x64_bf16 v[20:23], v[40:43], v[104:111], v6\n\t"
    "ds_read_b128 v[60:63], v5 offset:11264\n\t"
    "ds_read_b128 v[36:39], v10 offset:35840\n\t"
    "s_waitcnt lgkmcnt(7)\n\t"
    "v_smfmac_f32_16x16x64_bf16 v[16:19], v[44:47], v[112:119], v6\n\t"
    "ds_read_b128 v[40:43], v10 offset:36864\n\t"
    "v_smfmac_f32_16x16x64_bf16 v[20:23], v[44:47], v[120:127], v6\n\t"
    "ds_read_b128 v[44:47], v10 offset:37888\n\t"
    "s_waitcnt lgkmcnt(8)\n\t"
    "v_smfmac_f32_16x16x64_bf16 v[16:19], v[48:51], v[128:135], v6\n\t"
    "v_smfmac_f32_16x16x64_bf16 v[20:23], v[48:51], v[136:143], v6\n\t"
    "ds_read_b128 v[48:51], v10 offset:38912\n\t"
    "s_waitcnt lgkmcnt(8)\n\t"
    "v_smfmac_f32_16x16x64_bf16 v[16:19], v[52:55], v[144:151], v6\n\t"
    "v_smfmac_f32_16x16x64_bf16 v[20:23], v[52:55], v[152:159], v6\n\t"
    "ds_read_b128 v[52:55], v10 offset:39936\n\t"
    "s_waitcnt lgkmcnt(8)\n\t"
    "v_smfmac_f32_16x16x64_bf16 v[16:19], v[24:27], v[192:199], v6\n\t"
    "v_smfmac_f32_16x16x64_bf16 v[20:23], v[24:27], v[200:207], v6\n\t"
    "ds_read_b128 v[24:27], v10 offset:32768\n\t"
    "s_waitcnt lgkmcnt(8)\n\t"
    "v_smfmac_f32_16x16x64_bf16 v[16:19], v[28:31], v[208:215], v6\n\t"
    "ds_read_b32 v8, v15 offset:29440\n\t"
    "ds_read_b128 v[32:35], v5 offset:27648\n\t"
    "s_setprio 3\n\t"
    "s_waitcnt lgkmcnt(9)\n\t"
    "v_smfmac_f32_16x16x64_bf16 v[16:19], v[56:59], v[224:231], v6\n\t"
    "s_waitcnt lgkmcnt(8)\n\t"
    "v_smfmac_f32_16x16x64_bf16 v[16:19], v[60:63], v[240:247], v6\n\t"
    "v_smfmac_f32_16x16x64_bf16 v[20:23], v[28:31], v[216:223], v6\n\t"
    "s_waitcnt lgkmcnt(0)\n\t"
    "21:\n\t"
    "v_cmp_ne_u32 vcc, %[sn], v8\n\t"
    "s_cbranch_vccnz 22f\n\t"
    "v_mul_f32 v32, %[lsc], v32\n\t"
    "v_mul_f32 v33, %[lsc], v33\n\t"
    "v_smfmac_f32_16x16x64_bf16 v[20:23], v[56:59], v[232:239], v6\n\t"
    "v_mul_f32 v34, %[lsc], v34\n\t"
    "v_mul_f32 v35, %[lsc], v35\n\t"
    "v_add_f32 v16, v16, v18\n\t"
    "v_add_f32 v17, v17, v19\n\t"
    "v_smfmac_f32_16x16x64_bf16 v[20:23], v[60:63], v[248:255], v6\n\t"
    "v_fma_f32 v16, v16, %[lsc], v32\n\t"
    "v_fma_f32 v17, v17, %[lsc], v33\n\t"
    "v_cvt_pk_bf16_f32 v16, v16, v17\n\t"
    "ds_write_b32 v15, v16 offset:17920\n\t"
    "s_mov_b32 %[bg], %[big]\n\t"
    "s_nop 2\n\t"
    "v_add_f32 v20, v20, v22\n\t"
    "v_add_f32 v21, v21, v23\n\t"
    "v_fma_f32 v20, v20, %[lsc], v34\n\t"
    "v_fma_f32 v21, v21, %[lsc], v35\n\t"
    "v_cvt_pk_bf16_f32 v20, v20, v21\n\t"
    "ds_write_b32 v15, v20 offset:18176\n\t"
    "ds_read_b128 v[28:31], v10 offset:33792\n\t"
    "ds_read_b128 v[32:35], v10 offset:34816\n\t"
    "s_waitcnt lgkmcnt(2)\n\t"
    "s_barrier\n\t"
    "ds_read_b64_tr_b16 v[16:17], v7 offset:16384\n\t"
    "ds_read_b64_tr_b16 v[18:19], v7 offset:16448\n\t"
    "ds_read_b64_tr_b16 v[20:21], v7 offset:16896\n\t"
    "ds_read_b64_tr_b16 v[22:23], v7 offset:16960\n\t"
    "ds_read_b64_tr_b16 v[56:57], v7 offset:17408\n\t"
    "ds_read_b64_tr_b16 v[58:59], v7 offset:17472\n\t"
    "ds_read_b64_tr_b16 v[60:61], v7 offset:17920\n\t"
    "ds_read_b64_tr_b16 v[62:63], v7 offset:17984\n\t"
    "global_load_dwordx4 v[6:9], v5, %[vs] sc1\n\t"
    "global_load_dwordx4 v[10:13], v5, %[vs] offset:1024 sc1\n\t"
    "s_waitcnt lgkmcnt(6)\n\t"
    "v_mfma_f32_16x16x32_bf16 v[24:27], v[24:27], v[16:19], 0\n\t"
    "v_mfma_f32_16x16x32_bf16 v[28:31], v[28:31], v[16:19], 0\n\t"
    "s_waitcnt lgkmcnt(4)\n\t"
    "v_mfma_f32_16x16x32_bf16 v[24:27], v[32:35], v[20:23], v[24:27]\n\t"
    "v_mfma_f32_16x16x32_bf16 v[28:31], v[36:39], v[20:23], v[28:31]\n\t"
    "v_xor_b32 v39, 32, v14\n\t"
    "ds_read_b64_tr_b16 v[32:33], v14 offset:47104\n\t"
    "ds_read_b64_tr_b16 v[34:35], v14 offset:48128\n\t"
    "ds_read_b64_tr_b16 v[36:37], v39 offset:47104\n\t"
    "ds_read_b64_tr_b16 v[38:39], v39 offset:48128\n\t"
    "s_waitcnt lgkmcnt(6)\n\t"
    "v_mfma_f32_16x16x32_bf16 v[24:27], v[40:43], v[56:59], v[24:27]\n\t"
    "v_mfma_f32_16x16x32_bf16 v[28:31], v[44:47], v[56:59], v[28:31]\n\t"
    "s_waitcnt lgkmcnt(4)\n\t"
    "v_mfma_f32_16x16x32_bf16 v[24:27], v[48:51], v[60:63], v[24:27]\n\t"
    "v_mfma_f32_16x16x32_bf16 v[28:31], v[52:55], v[60:63], v[28:31]\n\t"
    "v_mov_b32 v52, %[one]\n\t"
    "v_mov_b32 v53, %[one]\n\t"
    "v_mov_b32 v54, %[one]\n\t"
    "v_mov_b32 v55, %[one]\n\t"
    "v_mov_b32 v45, 0\n\t"
    "v_mov_b32 v47, 0\n\t"
    "v_mov_b32 v48, %[one]\n\t"
    "v_max3_f32 v14, |v24|, |v25|, |v26|\n\t"
    "v_max3_f32 v40, |v27|, |v28|, |v29|\n\t"
    "v_max3_f32 v14, v14, |v30|, |v31|\n\t"
    "v_max_f32 v14, v14, v40\n\t"
    "v_cmp_nge_f32 vcc, %[big], v14\n\t"
    "v_mov_b32 v14, 0\n\t"
    "s_cbranch_vccnz 12f\n\t"
    "13:\n\t"
    "v_exp_f32 v24, v24\n\t"
    "v_exp_f32 v25, v25\n\t"
    "v_exp_f32 v26, v26\n\t"
    "v_exp_f32 v27, v27\n\t"
    "v_exp_f32 v28, v28\n\t"
    "v_exp_f32 v29, v29\n\t"
    "v_exp_f32 v30, v30\n\t"
    "v_exp_f32 v31, v31\n\t"
    "v_lshrrev_b32 v46, 2, v5\n\t"
    "global_load_dword v44, v46, %[v2] sc1\n\t"
    "global_load_dword v46, v46, %[v2] offset:256 sc1\n\t"
    "v_cvt_pk_bf16_f32 v40, v24, v25\n\t"
    "v_cvt_pk_bf16_f32 v41, v26, v27\n\t"
    "v_cvt_pk_bf16_f32 v42, v28, v29\n\t"
    "v_cvt_pk_bf16_f32 v43, v30, v31\n\t"
    "v_mov_b32 v49, 0\n\t"
    "v_mov_b32 v51, 0\n\t"
    "s_waitcnt lgkmcnt(0)\n\t"
    "v_mfma_f32_16x16x32_bf16 v[52:55], v[52:55], v[40:43], 0\n\t"
    "v_mfma_f32_16x16x32_bf16 v[24:27], v[32:35], v[40:43], 0\n\t"
    "v_mfma_f32_16x16x32_bf16 v[28:31], v[36:39], v[40:43], 0\n\t"
    "s_waitcnt vmcnt(3)\n\t"
    "5:\n\t"
    "v_mfma_f32_16x16x32_bf16 v[32:35], v[6:9], v[16:19], 0\n\t"
    "v_mfma_f32_16x16x32_bf16 v[36:39], v[6:9], v[20:23], 0\n\t"
    "s_waitcnt vmcnt(2)\n\t"
    "v_mfma_f32_16x16x32_bf16 v[32:35], v[10:13], v[56:59], v[32:35]\n\t"
    "v_mfma_f32_16x16x32_bf16 v[36:39], v[10:13], v[60:63], v[36:39]\n\t"
    "v_max3_u32 v40, v6, v7, v8\n\t"
    "v_max3_u32 v41, v9, v10, v11\n\t"
    "v_max3_u32 v40, v40, v12, v13\n\t"
    "v_max_u32 v40, v40, v41\n\t"
    "v_cmp_eq_u32 vcc, -1, v40\n\t"
    "s_cbranch_vccnz 4f\n\t"
    "6:\n\t"
    "s_nop 0\n\t"
    "s_nop 0\n\t"
    "v_add_f32 v32, v32, v38\n\t"
    "v_add_f32 v33, v33, v39\n\t"
    "v_exp_f32 v34, v32\n\t"
    "v_max_f32 v40, v32, v33\n\t"
    "v_exp_f32 v35, v33\n\t"
    "v_cmp_nge_f32 vcc, %[bg], v40\n\t"
    "s_cbranch_vccnz 10f\n\t"
    "11:\n\t"
    "v_cvt_pk_bf16_f32 v50, v34, v35\n\t"
    "17:\n\t"
    "s_waitcnt vmcnt(0) lgkmcnt(0)\n\t"
    "v_max_u32 v40, v44, v46\n\t"
    "v_cmp_eq_u32 vcc, -1, v40\n\t"
    "s_cbranch_vccnz 18f\n\t"
    "19:\n\t"
    "s_memrealtime s[36:37]\n\t"
    "v_mfma_f32_16x16x16_bf16 v[52:55], v[48:49], v[50:51], v[52:55]\n\t"
    "v_mfma_f32_16x16x16_bf16 v[24:27], v[44:45], v[50:51], v[24:27]\n\t"
    "v_mfma_f32_16x16x16_bf16 v[28:31], v[46:47], v[50:51], v[28:31]\n\t"
    "s_nop 5\n\t"
    "v_rcp_f32 v42, v52\n\t"
    "s_nop 0\n\t"
    "v_pk_mul_f32 v[24:25], v[24:25], v[42:43] op_sel_hi:[1,0]\n\t"
    "v_pk_mul_f32 v[26:27], v[26:27], v[42:43] op_sel_hi:[1,0]\n\t"
    "v_pk_mul_f32 v[28:29], v[28:29], v[42:43] op_sel_hi:[1,0]\n\t"
    "v_pk_mul_f32 v[30:31], v[30:31], v[42:43] op_sel_hi:[1,0]\n\t"
    "v_cvt_pk_bf16_f32 v36, v24, v25\n\t"
    "v_cvt_pk_bf16_f32 v37, v26, v27\n\t"
    "v_cvt_pk_bf16_f32 v38, v28, v29\n\t"
    "v_cvt_pk_bf16_f32 v39, v30, v31\n\t"
    "v_min_u32 v36, -2, v36\n\t"
    "v_min_u32 v37, -2, v37\n\t"
    "v_min_u32 v38, -2, v38\n\t"
    "v_min_u32 v39, -2, v39\n\t"
    "global_store_dwordx4 v4, v[36:39], %[od] sc1\n\t"
    "v_lshlrev_b32 v40, 10, v5\n\t"
    "s_waitcnt lgkmcnt(0)\n\t"
    "s_and_b32 s36, s36, 0xffffff\n\t"
    "s_or_b32 s36, s36, %[sn]\n\t"
    "v_mov_b32 v41, s36\n\t"
    "s_mov_b64 exec, 7\n\t"
    "global_store_dword v40, v41, %[db] offset:8 sc1\n\t"
    "s_mov_b64 exec, -1\n\t"
    "ds_write_b32 v15, v44 offset:19968\n\t"
    "ds_write_b32 v15, v46 offset:20224\n\t"
    "s_setprio 1\n\t"
    "s_waitcnt lgkmcnt(0)\n\t"
    "s_barrier\n\t"
    "s_waitcnt lgkmcnt(0)\n\t"
    "s_branch 9f\n\t"
    "1:\n\t"
    "s_add_u32 %[sp], %[sp], 1\n\t"
    "s_cmp_ge_u32 %[sp], %[cap]\n\t"
    "s_cbranch_scc1 7f\n\t"
    "global_load_dwordx4 v[24:27], v5, %[ag] sc1\n\t"
    "global_load_dwordx4 v[28:31], v5, %[ag] offset:1024 sc1\n\t"
    "s_waitcnt lgkmcnt(0)\n\t"
    "s_memtime %[tc]\n\t"
    "s_nop 7\n\t"
    "s_branch 3b\n\t"
    "7:\n\t"
    "s_mov_b32 %[cap], 0\n\t"
    "s_branch 2b\n\t"
    "4:\n\t"
    "s_add_u32 %[sp], %[sp], 0x10000\n\t"
    "s_lshr_b32 %[st], %[sp], 16\n\t"
    "s_cmp_ge_u32 %[st], %[cap]\n\t"
    "s_cbranch_scc1 8f\n\t"
    "global_load_dwordx4 v[6:9], v5, %[vs] sc1\n\t"
    "global_load_dwordx4 v[10:13], v5, %[vs] offset:1024 sc1\n\t"
    "s_waitcnt vmcnt(0) lgkmcnt(0)\n\t"
    "s_branch 5b\n\t"
    "8:\n\t"
    "s_mov_b32 %[cap], 0\n\t"
    "s_branch 6b\n\t"
    "18:\n\t"
    "s_add_u32 %[sp], %[sp], 0x10000\n\t"
    "s_lshr_b32 %[st], %[sp], 16\n\t"
    "s_cmp_ge_u32 %[st], %[cap]\n\t"
    "s_cbranch_scc1 20f\n\t"
    "v_lshrrev_b32 v46, 2, v5\n\t"
    "global_load_dword v44, v46, %[v2] sc1\n\t"
    "global_load_dword v46, v46, %[v2] offset:256 sc1\n\t"
    "s_waitcnt vmcnt(0) lgkmcnt(0)\n\t"
    "s_branch 17b\n\t"
    "20:\n\t"
    "s_mov_b32 %[cap], 0\n\t"
    "s_branch 19b\n\t"
    "22:\n\t"
    "ds_read_b32 v8, v15 offset:29440\n\t"
    "ds_read_b128 v[32:35], v5 offset:27648\n\t"
    "s_waitcnt lgkmcnt(0)\n\t"
    "s_branch 21b\n\t"
    "10:\n\t"
    "v_max_f32 v40, v40, v14\n\t"
    "v_mov_b32 v42, v40\n\t"
    "s_nop 1\n\t"
    "v_permlane16_swap_b32 v40, v42\n\t"
    "v_max_f32 v40, v40, v42\n\t"
    "v_mov_b32 v42, v40\n\t"
    "s_nop 1\n\t"
    "v_permlane32_swap_b32 v40, v42\n\t"
    "v_max_f32 v40, v40, v42\n\t"
    "v_sub_f32 v34, v32, v40\n\t"
    "v_sub_f32 v35, v33, v40\n\t"
    "v_sub_f32 v40, v14, v40\n\t"
    "v_exp_f32 v40, v40\n\t"
    "v_exp_f32 v34, v34\n\t"
    "v_exp_f32 v35, v35\n\t"
    "v_mul_f32 v24, v24, v40\n\t"
    "v_mul_f32 v25, v25, v40\n\t"
    "v_mul_f32 v26, v26, v40\n\t"
    "v_mul_f32 v27, v27, v40\n\t"
    "v_mul_f32 v28, v28, v40\n\t"
    "v_mul_f32 v29, v29, v40\n\t"
    "v_mul_f32 v30, v30, v40\n\t"
    "v_mul_f32 v31, v31, v40\n\t"
    "v_mul_f32 v52, v52, v40\n\t"
    "v_mul_f32 v53, v53, v40\n\t"
    "v_mul_f32 v54, v54, v40\n\t"
    "v_mul_f32 v55, v55, v40\n\t"
    "s_branch 11b\n\t"
    "12:\n\t"
    "v_max3_f32 v14, v24, v25, v26\n\t"
    "v_max3_f32 v40, v27, v28, v29\n\t"
    "v_max3_f32 v14, v14, v30, v31\n\t"
    "v_max3_f32 v14, v14, v40, %[neg]\n\t"
    "v_mov_b32 v40, v14\n\t"
    "s_nop 1\n\t"
    "v_permlane16_swap_b32 v14, v40\n\t"
    "v_max_f32 v14, v14, v40\n\t"
    "v_mov_b32 v40, v14\n\t"
    "s_nop 1\n\t"
    "v_permlane32_swap_b32 v14, v40\n\t"
    "v_max_f32 v14, v14, v40\n\t"
    "v_sub_f32 v24, v24, v14\n\t"
    "v_sub_f32 v25, v25, v14\n\t"
    "v_sub_f32 v26, v26, v14\n\t"
    "v_sub_f32 v27, v27, v14\n\t"
    "v_sub_f32 v28, v28, v14\n\t"
    "v_sub_f32 v29, v29, v14\n\t"
    "v_sub_f32 v30, v30, v14\n\t"
    "v_sub_f32 v31, v31, v14\n\t"
    "s_mov_b32 %[bg], 0x7fc00000\n\t"
    "s_branch 13b\n\t"
    "9:"
    : [tc] "=&s"(tc), [sp] "=&s"(sp), [cap] "+s"(cap), [st] "=&s"(st), [bg] "=&s"(bg)
    : [ag] "s"(ag), [c44] "s"(0x4444u), [lsc] "s"(LSC), [sn] "s"(sn), [pb] "s"(pb), [vs] "s"(vs), [od] "s"(od), [neg] "s"(-1.0e38f), [one] "s"(0x3F803F80u), [big] "s"(24.0f), [v2] "s"(v2), [db] "s"(db), "{v[64:95]}"(W.w0), "{v[96:127]}"(W.w1), "{v[128:159]}"(W.w2), "{v[160:191]}"(W.w3), "{v[192:223]}"(W.w4), "{v[224:255]}"(W.w5)
    : "v4", "v5", "v6", "v7", "v8", "v9", "v10", "v11", "v12", "v13", "v14", "v15", "v16", "v17", "v18", "v19", "v20", "v21", "v22", "v23", "v24", "v25", "v26", "v27", "v28", "v29", "v30", "v31", "v32", "v33", "v34", "v35", "v36", "v37", "v38", "v39", "v40", "v41", "v42", "v43", "v44", "v45", "v46", "v47", "v48", "v49", "v50", "v51", "v52", "v53", "v54", "v55", "v56", "v57", "v58", "v59", "v60", "v61", "v62", "v63", "vcc", "scc", "m0", "memory", "s36", "s37", "s38");
}
template <> __device__ __forceinline__ void qk_step<4>(const QKW& W, const char* ag, const char* vs, const char* v2, const char* db, const char* od, unsigned pb, unsigned sn, unsigned& cap, unsigned& sp,
    unsigned long long& tc, unsigned long long* ts, unsigned& d0, unsigned& d1) {
  unsigned st, bg;
  asm volatile("v_mbcnt_lo_u32_b32 v60, -1, 0\n\t"
    "v_mbcnt_hi_u32_b32 v60, -1, v60\n\t"
    "v_lshlrev_b32 v6, 4, v60\n\t"
    "s_mov_b32 %[sp], 0\n\t"
    "s_setprio 3\n\t"
    "global_load_dwordx4 v[24:27], v6, %[ag] sc1\n\t"
    "global_load_dwordx4 v[28:31], v6, %[ag] offset:-4096 sc1\n\t"
    "s_memtime %[tc]\n\t"
    "v_lshrrev_b32 v56, 4, v60\n\t"
    "v_and_b32 v57, 15, v60\n\t"
    "v_mov_b32 v20, 0\n\t"
    "v_mov_b32 v21, 0\n\t"
    "v_mov_b32 v22, 0\n\t"
    "v_mov_b32 v23, 0\n\t"
    "v_mov_b32 v11, %[sn]\n\t"
    "v_mov_b32 v10, -1\n\t"
    "v_and_b32 v61, 2, v60\n\t"
    "v_mul_u32_u24 v61, 0x5555, v61\n\t"
    "v_or_b32 v8, %[c44], v61\n\t"
    "v_lshlrev_b32 v61, 2, v56\n\t"
    "v_lshl_or_b32 v9, v57, 4, v61\n\t"
    "3:\n\t"
    "v_mov_b32 v12, 0\n\t"
    "v_mov_b32 v13, 0\n\t"
    "v_mov_b32 v14, 0\n\t"
    "v_mov_b32 v15, 0\n\t"
    "v_mov_b32 v16, 0\n\t"
    "v_mov_b32 v17, 0\n\t"
    "v_mov_b32 v18, 0\n\t"
    "v_mov_b32 v19, 0\n\t"
    "s_waitcnt vmcnt(0) lgkmcnt(0)\n\t"
    "33:\n\t"
    "ds_write_b128 v6, v[24:27] offset:12288\n\t"
    "ds_write_b128 v6, v[28:31] offset:8192\n\t"
    "v_smfmac_f32_16x16x64_bf16 v[12:15], v[24:27], v[64:71], v8\n\t"
    "v_max3_u32 v60, v24, v25, v26\n\t"
    "v_max3_u32 v61, v27, v28, v29\n\t"
    "v_smfmac_f32_16x16x64_bf16 v[16:19], v[24:27], v[72:79], v8\n\t"
    "v_max3_u32 v60, v60, v30, v31\n\t"
    "v_max_u32 v60, v60, v61\n\t"
    "v_cmp_eq_u32 vcc, -1, v60\n\t"
    "s_cbranch_vccnz 1f\n\t"
    "2:\n\t"
    "s_waitcnt lgkmcnt(0)\n\t"
    "s_barrier\n\t"
    "ds_read_b128 v[32:35], v6 offset:13312\n\t"
    "ds_read_b128 v[36:39], v6 offset:14336\n\t"
    "ds_read_b128 v[40:43], v6 offset:15360\n\t"
    "ds_write_b32 v9, v10 offset:18432\n\t"
    "s_waitcnt lgkmcnt(3)\n\t"
    "v_smfmac_f32_16x16x64_bf16 v[12:15], v[32:35], v[80:87], v8\n\t"
    "v_smfmac_f32_16x16x64_bf16 v[16:19], v[32:35], v[88:95], v8\n\t"
    "s_waitcnt lgkmcnt(2)\n\t"
    "v_smfmac_f32_16x16x64_bf16 v[12:15], v[36:39], v[96:103], v8\n\t"
    "v_smfmac_f32_16x16x64_bf16 v[16:19], v[36:39], v[104:111], v8\n\t"
    "s_waitcnt lgkmcnt(1)\n\t"
    "v_smfmac_f32_16x16x64_bf16 v[12:15], v[40:43], v[112:119], v8\n\t"
    "v_smfmac_f32_16x16x64_bf16 v[16:19], v[40:43], v[120:127], v8\n\t"
    "s_setprio 0\n\t"
    "s_sleep 1\n\t"
    "s_nop 6\n\t"
    "v_add_f32 v12, v12, v14\n\t"
    "v_add_f32 v13, v13, v15\n\t"
    "v_add_f32 v14, v16, v18\n\t"
    "v_add_f32 v15, v17, v19\n\t"
    "ds_write_b128 v6, v[12:15] offset:24576\n\t"
    "ds_write_b32 v9, v11 offset:28672\n\t"
    "s_setprio 0\n\t"
    "s_memrealtime s[36:37]\n\t"
    "s_waitcnt lgkmcnt(0)\n\t"
    "s_mov_b32 s38, s36\n\t"
    "s_min_u32 s37, %[d0], %[d1]\n\t"
    "s_add_u32 s36, s36, s37\n\t"
    "s_and_b32 s36, s36, 0xffffff\n\t"
    "s_or_b32 s36, s36, %[sn]\n\t"
    "v_lshlrev_b32 v10, 10, v6\n\t"
    "v_mov_b32 v5, s36\n\t"
    "s_mov_b64 exec, 15\n\t"
    "global_store_dword v10, v5, %[vs] offset:-4096 sc1\n\t"
    "s_mov_b64 exec, -1\n\t"
    "ds_read_b128 v[44:47], v6 offset:0\n\t"
    "ds_read_b128 v[48:51], v6 offset:1024\n\t"
    "ds_read_b128 v[52:55], v6 offset:2048\n\t"
    "ds_read_b128 v[56:59], v6 offset:3072\n\t"
    "ds_read_b128 v[60:63], v6 offset:4096\n\t"
    "s_sleep 1\n\t"
    "v_smfmac_f32_16x16x64_bf16 v[20:23], v[28:31], v[192:199], v8\n\t"
    "ds_read_b128 v[28:31], v6 offset:5120\n\t"
    "v_smfmac_f32_16x16x64_bf16 v[20:23], v[24:27], v[224:231], v8\n\t"
    "ds_read_b128 v[24:27], v6 offset:6144\n\t"
    "v_smfmac_f32_16x16x64_bf16 v[20:23], v[32:35], v[240:247], v8\n\t"
    "ds_read_b128 v[32:35], v6 offset:7168\n\t"
    "v_smfmac_f32_16x16x64_bf16 v[20:23], v[36:39], v[232:239], v8\n\t"
    "ds_read_b128 v[36:39], v6 offset:9216\n\t"
    "v_smfmac_f32_16x16x64_bf16 v[20:23], v[40:43], v[248:255], v8\n\t"
    "ds_read_b128 v[40:43], v6 offset:10240\n\t"
    "s_waitcnt lgkmcnt(9)\n\t"
    "v_smfmac_f32_16x16x64_bf16 v[20:23], v[44:47], v[128:135], v8\n\t"
    "ds_read_b128 v[44:47], v6 offset:11264\n\t"
    "s_barrier\n\t"
    "s_setprio 0\n\t"
    "s_waitcnt lgkmcnt(9)\n\t"
    "v_smfmac_f32_16x16x64_bf16 v[20:23], v[48:51], v[144:151], v8\n\t"
    "s_waitcnt lgkmcnt(8)\n\t"
    "v_smfmac_f32_16x16x64_bf16 v[20:23], v[52:55], v[136:143], v8\n\t"
    "s_waitcnt lgkmcnt(7)\n\t"
    "v_smfmac_f32_16x16x64_bf16 v[20:23], v[56:59], v[152:159], v8\n\t"
    "s_waitcnt lgkmcnt(6)\n\t"
    "v_smfmac_f32_16x16x64_bf16 v[20:23], v[60:63], v[160:167], v8\n\t"
    "s_waitcnt lgkmcnt(5)\n\t"
    "v_smfmac_f32_16x16x64_bf16 v[20:23], v[28:31], v[176:183], v8\n\t"
    "s_waitcnt lgkmcnt(4)\n\t"
    "v_smfmac_f32_16x16x64_bf16 v[20:23], v[24:27], v[168:175], v8\n\t"
    "s_waitcnt lgkmcnt(3)\n\t"
    "v_smfmac_f32_16x16x64_bf16 v[20:23], v[32:35], v[184:191], v8\n\t"
    "s_waitcnt lgkmcnt(2)\n\t"
    "v_smfmac_f32_16x16x64_bf16 v[20:23], v[36:39], v[208:215], v8\n\t"
    "s_waitcnt lgkmcnt(1)\n\t"
    "v_smfmac_f32_16x16x64_bf16 v[20:23], v[40:43], v[200:207], v8\n\t"
    "s_waitcnt lgkmcnt(0)\n\t"
    "v_smfmac_f32_16x16x64_bf16 v[20:23], v[44:47], v[216:223], v8\n\t"
    "s_nop 7\n\t"
    "v_add_f32 v20, v20, v22\n\t"
    "v_add_f32 v21, v21, v23\n\t"
    "v_cvt_pk_bf16_f32 v20, v20, v21\n\t"
    "v_min_u32 v20, -2, v20\n\t"
    "ds_write_b32 v9, v20 offset:18432\n\t"
    "s_waitcnt lgkmcnt(0)\n\t"
    "s_setprio 3\n\t"
    "s_barrier\n\t"
    "s_memrealtime s[36:37]\n\t"
    "s_mov_b32 %[d1], %[d0]\n\t"
    "s_waitcnt lgkmcnt(0)\n\t"
    "s_sub_u32 %[d0], s36, s38\n\t"
    "s_min_u32 %[d0], %[d0], 200\n\t"
    "s_waitcnt lgkmcnt(0)\n\t"
    "s_branch 9f\n\t"
    "1:\n\t"
    "s_add_u32 %[sp], %[sp], 1\n\t"
    "s_cmp_ge_u32 %[sp], %[cap]\n\t"
    "s_cbranch_scc1 7f\n\t"
    "global_load_dwordx4 v[24:27], v6, %[ag] sc1\n\t"
    "global_load_dwordx4 v[28:31], v6, %[ag] offset:-4096 sc1\n\t"
    "s_waitcnt lgkmcnt(0)\n\t"
    "s_memtime %[tc]\n\t"
    "s_nop 7\n\t"
    "s_branch 3b\n\t"
    "7:\n\t"
    "s_mov_b32 %[cap], 0\n\t"
    "s_branch 2b\n\t"
    "9:"
    : [tc] "=&s"(tc), [sp] "=&s"(sp), [cap] "+s"(cap), [st] "=&s"(st), [d0] "+s"(d0), [d1] "+s"(d1)
    : [ag] "s"(ag), [c44] "s"(0x4444u), [lsc] "s"(LSC), [sn] "s"(sn), [vs] "s"(vs), "{v[64:95]}"(W.w0), "{v[96:127]}"(W.w1), "{v[128:159]}"(W.w2), "{v[160:191]}"(W.w3), "{v[192:223]}"(W.w4), "{v[224:255]}"(W.w5)
    : "v5", "v6", "v7", "v8", "v9", "v10", "v11", "v12", "v13", "v14", "v15", "v16", "v17", "v18", "v19", "v20", "v21", "v22", "v23", "v24", "v25", "v26", "v27", "v28", "v29", "v30", "v31", "v32", "v33", "v34", "v35", "v36", "v37", "v38", "v39", "v40", "v41", "v42", "v43", "v44", "v45", "v46", "v47", "v48", "v49", "v50", "v51", "v52", "v53", "v54", "v55", "v56", "v57", "v58", "v59", "v60", "v61", "v62", "v63", "vcc", "scc", "m0", "memory", "s36", "s37", "s38");
}
template <> __device__ __forceinline__ void qk_step<5>(const QKW& W, const char* ag, const char* vs, const char* v2, const char* db, const char* od, unsigned pb, unsigned sn, unsigned& cap, unsigned& sp,
    unsigned long long& tc, unsigned long long* ts, unsigned& d0, unsigned& d1) {
  unsigned st, bg;
  asm volatile("v_mbcnt_lo_u32_b32 v60, -1, 0\n\t"
    "v_mbcnt_hi_u32_b32 v60, -1, v60\n\t"
    "v_lshlrev_b32 v6, 4, v60\n\t"
    "s_mov_b32 %[sp], 0\n\t"
    "s_setprio 3\n\t"
    "global_load_dwordx4 v[24:27], v6, %[ag] sc1\n\t"
    "global_load_dwordx4 v[28:31], v6, %[ag] offset:-4096 sc1\n\t"
    "s_memtime %[tc]\n\t"
    "v_lshrrev_b32 v56, 4, v60\n\t"
    "v_and_b32 v57, 15, v60\n\t"
    "v_mov_b32 v20, 0\n\t"
    "v_mov_b32 v21, 0\n\t"
    "v_mov_b32 v22, 0\n\t"
    "v_mov_b32 v23, 0\n\t"
    "v_mov_b32 v11, %[sn]\n\t"
    "v_mov_b32 v10, -1\n\t"
    "v_and_b32 v61, 2, v60\n\t"
    "v_mul_u32_u24 v61, 0x5555, v61\n\t"
    "v_or_b32 v8, %[c44], v61\n\t"
    "v_lshlrev_b32 v61, 2, v56\n\t"
    "v_lshl_or_b32 v9, v57, 4, v61\n\t"
    "3:\n\t"
    "v_mov_b32 v12, 0\n\t"
    "v_mov_b32 v13, 0\n\t"
    "v_mov_b32 v14, 0\n\t"
    "v_mov_b32 v15, 0\n\t"
    "v_mov_b32 v16, 0\n\t"
    "v_mov_b32 v17, 0\n\t"
    "v_mov_b32 v18, 0\n\t"
    "v_mov_b32 v19, 0\n\t"
    "s_waitcnt vmcnt(0) lgkmcnt(0)\n\t"
    "33:\n\t"
    "ds_write_b128 v6, v[24:27] offset:13312\n\t"
    "ds_write_b128 v6, v[28:31] offset:9216\n\t"
    "v_smfmac_f32_16x16x64_bf16 v[12:15], v[24:27], v[80:87], v8\n\t"
    "v_max3_u32 v60, v24, v25, v26\n\t"
    "v_max3_u32 v61, v27, v28, v29\n\t"
    "v_smfmac_f32_16x16x64_bf16 v[16:19], v[24:27], v[88:95], v8\n\t"
    "v_max3_u32 v60, v60, v30, v31\n\t"
    "v_max_u32 v60, v60, v61\n\t"
    "v_cmp_eq_u32 vcc, -1, v60\n\t"
    "s_cbranch_vccnz 1f\n\t"
    "2:\n\t"
    "s_waitcnt lgkmcnt(0)\n\t"
    "s_barrier\n\t"
    "ds_read_b128 v[32:35], v6 offset:12288\n\t"
    "ds_read_b128 v[36:39], v6 offset:14336\n\t"
    "ds_read_b128 v[40:43], v6 offset:15360\n\t"
    "ds_write_b32 v9, v10 offset:18688\n\t"
    "s_waitcnt lgkmcnt(3)\n\t"
    "v_smfmac_f32_16x16x64_bf16 v[12:15], v[32:35], v[64:71], v8\n\t"
    "v_smfmac_f32_16x16x64_bf16 v[16:19], v[32:35], v[72:79], v8\n\t"
    "s_waitcnt lgkmcnt(2)\n\t"
    "v_smfmac_f32_16x16x64_bf16 v[12:15], v[36:39], v[96:103], v8\n\t"
    "v_smfmac_f32_16x16x64_bf16 v[16:19], v[36:39], v[104:111], v8\n\t"
    "s_waitcnt lgkmcnt(1)\n\t"
    "v_smfmac_f32_16x16x64_bf16 v[12:15], v[40:43], v[112:119], v8\n\t"
    "v_smfmac_f32_16x16x64_bf16 v[16:19], v[40:43], v[120:127], v8\n\t"
    "s_setprio 0\n\t"
    "s_sleep 1\n\t"
    "s_nop 6\n\t"
    "v_add_f32 v12, v12, v14\n\t"
    "v_add_f32 v13, v13, v15\n\t"
    "v_add_f32 v14, v16, v18\n\t"
    "v_add_f32 v15, v17, v19\n\t"
    "ds_write_b128 v6, v[12:15] offset:25600\n\t"
    "ds_write_b32 v9, v11 offset:28928\n\t"
    "s_setprio 0\n\t"
    "ds_read_b128 v[44:47], v6 offset:0\n\t"
    "ds_read_b128 v[48:51], v6 offset:1024\n\t"
    "ds_read_b128 v[52:55], v6 offset:2048\n\t"
    "ds_read_b128 v[56:59], v6 offset:3072\n\t"
    "ds_read_b128 v[60:63], v6 offset:4096\n\t"
    "s_sleep 1\n\t"
    "v_smfmac_f32_16x16x64_bf16 v[20:23], v[28:31], v[208:215], v8\n\t"
    "ds_read_b128 v[28:31], v6 offset:5120\n\t"
    "v_smfmac_f32_16x16x64_bf16 v[20:23], v[24:27], v[240:247], v8\n\t"
    "ds_read_b128 v[24:27], v6 offset:6144\n\t"
    "v_smfmac_f32_16x16x64_bf16 v[20:23], v[32:35], v[224:231], v8\n\t"
    "ds_read_b128 v[32:35], v6 offset:7168\n\t"
    "v_smfmac_f32_16x16x64_bf16 v[20:23], v[36:39], v[232:239], v8\n\t"
    "ds_read_b128 v[36:39], v6 offset:8192\n\t"
    "v_smfmac_f32_16x16x64_bf16 v[20:23], v[40:43], v[248:255], v8\n\t"
    "ds_read_b128 v[40:43], v6 offset:10240\n\t"
    "s_waitcnt lgkmcnt(9)\n\t"
    "v_smfmac_f32_16x16x64_bf16 v[20:23], v[44:47], v[128:135], v8\n\t"
    "ds_read_b128 v[44:47], v6 offset:11264\n\t"
    "s_barrier\n\t"
    "s_setprio 0\n\t"
    "s_waitcnt lgkmcnt(9)\n\t"
    "v_smfmac_f32_16x16x64_bf16 v[20:23], v[48:51], v[144:151], v8\n\t"
    "s_waitcnt lgkmcnt(8)\n\t"
    "v_smfmac_f32_16x16x64_bf16 v[20:23], v[52:55], v[136:143], v8\n\t"
    "s_waitcnt lgkmcnt(7)\n\t"
    "v_smfmac_f32_16x16x64_bf16 v[20:23], v[56:59], v[152:159], v8\n\t"
    "s_waitcnt lgkmcnt(6)\n\t"
    "v_smfmac_f32_16x16x64_bf16 v[20:23], v[60:63], v[160:167], v8\n\t"
    "s_waitcnt lgkmcnt(5)\n\t"
    "v_smfmac_f32_16x16x64_bf16 v[20:23], v[28:31], v[176:183], v8\n\t"
    "s_waitcnt lgkmcnt(4)\n\t"
    "v_smfmac_f32_16x16x64_bf16 v[20:23], v[24:27], v[168:175], v8\n\t"
    "s_waitcnt lgkmcnt(3)\n\t"
    "v_smfmac_f32_16x16x64_bf16 v[20:23], v[32:35], v[184:191], v8\n\t"
    "s_waitcnt lgkmcnt(2)\n\t"
    "v_smfmac_f32_16x16x64_bf16 v[20:23], v[36:39], v[192:199], v8\n\t"
    "s_waitcnt lgkmcnt(1)\n\t"
    "v_smfmac_f32_16x16x64_bf16 v[20:23], v[40:43], v[200:207], v8\n\t"
    "s_waitcnt lgkmcnt(0)\n\t"
    "v_smfmac_f32_16x16x64_bf16 v[20:23], v[44:47], v[216:223], v8\n\t"
    "s_nop 7\n\t"
    "v_add_f32 v20, v20, v22\n\t"
    "v_add_f32 v21, v21, v23\n\t"
    "v_cvt_pk_bf16_f32 v20, v20, v21\n\t"
    "v_min_u32 v20, -2, v20\n\t"
    "ds_write_b32 v9, v20 offset:18688\n\t"
    "s_waitcnt lgkmcnt(0)\n\t"
    "s_setprio 0\n\t"
    "s_setprio 0\n\t"
    "s_waitcnt lgkmcnt(0)\n\t"
    "s_branch 9f\n\t"
    "1:\n\t"
    "s_add_u32 %[sp], %[sp], 1\n\t"
    "s_cmp_ge_u32 %[sp], %[cap]\n\t"
    "s_cbranch_scc1 7f\n\t"
    "global_load_dwordx4 v[24:27], v6, %[ag] sc1\n\t"
    "global_load_dwordx4 v[28:31], v6, %[ag] offset:-4096 sc1\n\t"
    "s_waitcnt lgkmcnt(0)\n\t"
    "s_memtime %[tc]\n\t"
    "s_nop 7\n\t"
    "s_branch 3b\n\t"
    "7:\n\t"
    "s_mov_b32 %[cap], 0\n\t"
    "s_branch 2b\n\t"
    "9:"
    : [tc] "=&s"(tc), [sp] "=&s"(sp), [cap] "+s"(cap), [st] "=&s"(st)
    : [ag] "s"(ag), [c44] "s"(0x4444u), [lsc] "s"(LSC), [sn] "s"(sn), "{v[64:95]}"(W.w0), "{v[96:127]}"(W.w1), "{v[128:159]}"(W.w2), "{v[160:191]}"(W.w3), "{v[192:223]}"(W.w4), "{v[224:255]}"(W.w5)
    : "v6", "v7", "v8", "v9", "v10", "v11", "v12", "v13", "v14", "v15", "v16", "v17", "v18", "v19", "v20", "v21", "v22", "v23", "v24", "v25", "v26", "v27", "v28", "v29", "v30", "v31", "v32", "v33", "v34", "v35", "v36", "v37", "v38", "v39", "v40", "v41", "v42", "v43", "v44", "v45", "v46", "v47", "v48", "v49", "v50", "v51", "v52", "v53", "v54", "v55", "v56", "v57", "v58", "v59", "v60", "v61", "v62", "v63", "vcc", "scc", "m0", "memory");
}
template <> __device__ __forceinline__ void qk_step<6>(const QKW& W, const char* ag, const char* vs, const char* v2, const char* db, const char* od, unsigned pb, unsigned sn, unsigned& cap, unsigned& sp,
    unsigned long long& tc, unsigned long long* ts, unsigned& d0, unsigned& d1) {
  unsigned st, bg;
  asm volatile("v_mbcnt_lo_u32_b32 v60, -1, 0\n\t"
    "v_mbcnt_hi_u32_b32 v60, -1, v60\n\t"
    "v_lshlrev_b32 v6, 4, v60\n\t"
    "s_mov_b32 %[sp], 0\n\t"
    "s_setprio 3\n\t"
    "global_load_dwordx4 v[24:27], v6, %[ag] sc1\n\t"
    "global_load_dwordx4 v[28:31], v6, %[ag] offset:-4096 sc1\n\t"
    "s_memtime %[tc]\n\t"
    "v_lshrrev_b32 v56, 4, v60\n\t"
    "v_and_b32 v57, 15, v60\n\t"
    "v_mov_b32 v20, 0\n\t"
    "v_mov_b32 v21, 0\n\t"
    "v_mov_b32 v22, 0\n\t"
    "v_mov_b32 v23, 0\n\t"
    "v_mov_b32 v11, %[sn]\n\t"
    "v_mov_b32 v10, -1\n\t"
    "v_and_b32 v61, 2, v60\n\t"
    "v_mul_u32_u24 v61, 0x5555, v61\n\t"
    "v_or_b32 v8, %[c44], v61\n\t"
    "v_lshlrev_b32 v61, 2, v56\n\t"
    "v_lshl_or_b32 v9, v57, 4, v61\n\t"
    "s_setprio 0\n\t"
    "s_setprio 3\n\t"
    "3:\n\t"
    "v_mov_b32 v12, 0\n\t"
    "v_mov_b32 v13, 0\n\t"
    "v_mov_b32 v14, 0\n\t"
    "v_mov_b32 v15, 0\n\t"
    "v_mov_b32 v16, 0\n\t"
    "v_mov_b32 v17, 0\n\t"
    "v_mov_b32 v18, 0\n\t"
    "v_mov_b32 v19, 0\n\t"
    "s_waitcnt vmcnt(0) lgkmcnt(0)\n\t"
    "33:\n\t"
    "ds_write_b128 v6, v[24:27] offset:14336\n\t"
    "ds_write_b128 v6, v[28:31] offset:10240\n\t"
    "v_smfmac_f32_16x16x64_bf16 v[12:15], v[24:27], v[96:103], v8\n\t"
    "v_max3_u32 v60, v24, v25, v26\n\t"
    "v_max3_u32 v61, v27, v28, v29\n\t"
    "v_smfmac_f32_16x16x64_bf16 v[16:19], v[24:27], v[104:111], v8\n\t"
    "v_max3_u32 v60, v60, v30, v31\n\t"
    "v_max_u32 v60, v60, v61\n\t"
    "v_cmp_eq_u32 vcc, -1, v60\n\t"
    "s_cbranch_vccnz 1f\n\t"
    "2:\n\t"
    "s_waitcnt lgkmcnt(0)\n\t"
    "s_barrier\n\t"
    "ds_read_b128 v[32:35], v6 offset:12288\n\t"
    "ds_read_b128 v[36:39], v6 offset:13312\n\t"
    "ds_read_b128 v[40:43], v6 offset:15360\n\t"
    "ds_write_b32 v9, v10 offset:18944\n\t"
    "s_waitcnt lgkmcnt(3)\n\t"
    "v_smfmac_f32_16x16x64_bf16 v[12:15], v[32:35], v[64:71], v8\n\t"
    "v_smfmac_f32_16x16x64_bf16 v[16:19], v[32:35], v[72:79], v8\n\t"
    "s_waitcnt lgkmcnt(2)\n\t"
    "v_smfmac_f32_16x16x64_bf16 v[12:15], v[36:39], v[80:87], v8\n\t"
    "v_smfmac_f32_16x16x64_bf16 v[16:19], v[36:39], v[88:95], v8\n\t"
    "s_waitcnt lgkmcnt(1)\n\t"
    "v_smfmac_f32_16x16x64_bf16 v[12:15], v[40:43], v[112:119], v8\n\t"
    "v_smfmac_f32_16x16x64_bf16 v[16:19], v[40:43], v[120:127], v8\n\t"
    "s_setprio 0\n\t"
    "s_sleep 1\n\t"
    "s_nop 6\n\t"
    "v_add_f32 v12, v12, v14\n\t"
    "v_add_f32 v13, v13, v15\n\t"
    "v_add_f32 v14, v16, v18\n\t"
    "v_add_f32 v15, v17, v19\n\t"
    "ds_write_b128 v6, v[12:15] offset:26624\n\t"
    "ds_write_b32 v9, v11 offset:29184\n\t"
    "s_setprio 0\n\t"
    "ds_read_b128 v[44:47], v6 offset:0\n\t"
    "ds_read_b128 v[48:51], v6 offset:1024\n\t"
    "ds_read_b128 v[52:55], v6 offset:2048\n\t"
    "ds_read_b128 v[56:59], v6 offset:3072\n\t"
    "ds_read_b128 v[60:63], v6 offset:4096\n\t"
    "s_sleep 1\n\t"
    "v_smfmac_f32_16x16x64_bf16 v[20:23], v[28:31], v[200:207], v8\n\t"
    "ds_read_b128 v[28:31], v6 offset:5120\n\t"
    "v_smfmac_f32_16x16x64_bf16 v[20:23], v[24:27], v[232:239], v8\n\t"
    "ds_read_b128 v[24:27], v6 offset:6144\n\t"
    "v_smfmac_f32_16x16x64_bf16 v[20:23], v[32:35], v[224:231], v8\n\t"
    "ds_read_b128 v[32:35], v6 offset:7168\n\t"
    "v_smfmac_f32_16x16x64_bf16 v[20:23], v[36:39], v[240:247], v8\n\t"
    "ds_read_b128 v[36:39], v6 offset:8192\n\t"
    "v_smfmac_f32_16x16x64_bf16 v[20:23], v[40:43], v[248:255], v8\n\t"
    "ds_read_b128 v[40:43], v6 offset:9216\n\t"
    "s_waitcnt lgkmcnt(9)\n\t"
    "v_smfmac_f32_16x16x64_bf16 v[20:23], v[44:47], v[128:135], v8\n\t"
    "ds_read_b128 v[44:47], v6 offset:11264\n\t"
    "s_barrier\n\t"
    "s_setprio 0\n\t"
    "s_waitcnt lgkmcnt(9)\n\t"
    "v_smfmac_f32_16x16x64_bf16 v[20:23], v[48:51], v[144:151], v8\n\t"
    "s_waitcnt lgkmcnt(8)\n\t"
    "v_smfmac_f32_16x16x64_bf16 v[20:23], v[52:55], v[136:143], v8\n\t"
    "s_waitcnt lgkmcnt(7)\n\t"
    "v_smfmac_f32_16x16x64_bf16 v[20:23], v[56:59], v[152:159], v8\n\t"
    "s_waitcnt lgkmcnt(6)\n\t"
    "v_smfmac_f32_16x16x64_bf16 v[20:23], v[60:63], v[160:167], v8\n\t"
    "s_waitcnt lgkmcnt(5)\n\t"
    "v_smfmac_f32_16x16x64_bf16 v[20:23], v[28:31], v[176:183], v8\n\t"
    "s_waitcnt lgkmcnt(4)\n\t"
    "v_smfmac_f32_16x16x64_bf16 v[20:23], v[24:27], v[168:175], v8\n\t"
    "s_waitcnt lgkmcnt(3)\n\t"
    "v_smfmac_f32_16x16x64_bf16 v[20:23], v[32:35], v[184:191], v8\n\t"
    "s_waitcnt lgkmcnt(2)\n\t"
    "v_smfmac_f32_16x16x64_bf16 v[20:23], v[36:39], v[192:199], v8\n\t"
    "s_waitcnt lgkmcnt(1)\n\t"
    "v_smfmac_f32_16x16x64_bf16 v[20:23], v[40:43], v[208:215], v8\n\t"
    "s_waitcnt lgkmcnt(0)\n\t"
    "v_smfmac_f32_16x16x64_bf16 v[20:23], v[44:47], v[216:223], v8\n\t"
    "s_nop 7\n\t"
    "v_add_f32 v20, v20, v22\n\t"
    "v_add_f32 v21, v21, v23\n\t"
    "v_cvt_pk_bf16_f32 v20, v20, v21\n\t"
    "v_min_u32 v20, -2, v20\n\t"
    "ds_write_b32 v9, v20 offset:18944\n\t"
    "s_waitcnt lgkmcnt(0)\n\t"
    "s_setprio 0\n\t"
    "s_setprio 0\n\t"
    "s_waitcnt lgkmcnt(0)\n\t"
    "s_branch 9f\n\t"
    "1:\n\t"
    "s_add_u32 %[sp], %[sp], 1\n\t"
    "s_cmp_ge_u32 %[sp], %[cap]\n\t"
    "s_cbranch_scc1 7f\n\t"
    "global_load_dwordx4 v[24:27], v6, %[ag] sc1\n\t"
    "global_load_dwordx4 v[28:31], v6, %[ag] offset:-4096 sc1\n\t"
    "s_waitcnt lgkmcnt(0)\n\t"
    "s_memtime %[tc]\n\t"
    "s_nop 7\n\t"
    "s_branch 3b\n\t"
    "7:\n\t"
    "s_mov_b32 %[cap], 0\n\t"
    "s_branch 2b\n\t"
    "9:"
    : [tc] "=&s"(tc), [sp] "=&s"(sp), [cap] "+s"(cap), [st] "=&s"(st)
    : [ag] "s"(ag), [c44] "s"(0x4444u), [lsc] "s"(LSC), [sn] "s"(sn), "{v[64:95]}"(W.w0), "{v[96:127]}"(W.w1), "{v[128:159]}"(W.w2), "{v[160:191]}"(W.w3), "{v[192:223]}"(W.w4), "{v[224:255]}"(W.w5)
    : "v6", "v7", "v8", "v9", "v10", "v11", "v12", "v13", "v14", "v15", "v16", "v17", "v18", "v19", "v20", "v21", "v22", "v23", "v24", "v25", "v26", "v27", "v28", "v29", "v30", "v31", "v32", "v33", "v34", "v35", "v36", "v37", "v38", "v39", "v40", "v41", "v42", "v43", "v44", "v45", "v46", "v47", "v48", "v49", "v50", "v51", "v52", "v53", "v54", "v55", "v56", "v57", "v58", "v59", "v60", "v61", "v62", "v63", "vcc", "scc", "m0", "memory");
}
template <> __device__ __forceinline__ void qk_step<7>(const QKW& W, const char* ag, const char* vs, const char* v2, const char* db, const char* od, unsigned pb, unsigned sn, unsigned& cap, unsigned& sp,
    unsigned long long& tc, unsigned long long* ts, unsigned& d0, unsigned& d1) {
  unsigned st, bg;
  asm volatile("v_mbcnt_lo_u32_b32 v60, -1, 0\n\t"
    "v_mbcnt_hi_u32_b32 v60, -1, v60\n\t"
    "v_lshlrev_b32 v6, 4, v60\n\t"
    "s_mov_b32 %[sp], 0\n\t"
    "s_setprio 3\n\t"
    "global_load_dwordx4 v[24:27], v6, %[ag] sc1\n\t"
    "global_load_dwordx4 v[28:31], v6, %[ag] offset:-4096 sc1\n\t"
    "s_memtime %[tc]\n\t"
    "v_lshrrev_b32 v56, 4, v60\n\t"
    "v_and_b32 v57, 15, v60\n\t"
    "v_mov_b32 v20, 0\n\t"
    "v_mov_b32 v21, 0\n\t"
    "v_mov_b32 v22, 0\n\t"
    "v_mov_b32 v23, 0\n\t"
    "v_mov_b32 v11, %[sn]\n\t"
    "v_mov_b32 v10, -1\n\t"
    "v_and_b32 v61, 2, v60\n\t"
    "v_mul_u32_u24 v61, 0x5555, v61\n\t"
    "v_or_b32 v8, %[c44], v61\n\t"
    "v_lshlrev_b32 v61, 2, v56\n\t"
    "v_lshl_or_b32 v9, v57, 4, v61\n\t"
    "s_setprio 0\n\t"
    "s_setprio 3\n\t"
    "3:\n\t"
    "v_mov_b32 v12, 0\n\t"
    "v_mov_b32 v13, 0\n\t"
    "v_mov_b32 v14, 0\n\t"
    "v_mov_b32 v15, 0\n\t"
    "v_mov_b32 v16, 0\n\t"
    "v_mov_b32 v17, 0\n\t"
    "v_mov_b32 v18, 0\n\t"
    "v_mov_b32 v19, 0\n\t"
    "s_waitcnt vmcnt(0) lgkmcnt(0)\n\t"
    "33:\n\t"
    "ds_write_b128 v6, v[24:27] offset:15360\n\t"
    "ds_write_b128 v6, v[28:31] offset:11264\n\t"
    "v_smfmac_f32_16x16x64_bf16 v[12:15], v[24:27], v[112:119], v8\n\t"
    "v_max3_u32 v60, v24, v25, v26\n\t"
    "v_max3_u32 v61, v27, v28, v29\n\t"
    "v_smfmac_f32_16x16x64_bf16 v[16:19], v[24:27], v[120:127], v8\n\t"
    "v_max3_u32 v60, v60, v30, v31\n\t"
    "v_max_u32 v60, v60, v61\n\t"
    "v_cmp_eq_u32 vcc, -1, v60\n\t"
    "s_cbranch_vccnz 1f\n\t"
    "2:\n\t"
    "s_waitcnt lgkmcnt(0)\n\t"
    "s_barrier\n\t"
    "ds_read_b128 v[32:35], v6 offset:12288\n\t"
    "ds_read_b128 v[36:39], v6 offset:13312\n\t"
    "ds_read_b128 v[40:43], v6 offset:14336\n\t"
    "ds_write_b32 v9, v10 offset:19200\n\t"
    "s_waitcnt lgkmcnt(3)\n\t"
    "v_smfmac_f32_16x16x64_bf16 v[12:15], v[32:35], v[64:71], v8\n\t"
    "v_smfmac_f32_16x16x64_bf16 v[16:19], v[32:35], v[72:79], v8\n\t"
    "s_waitcnt lgkmcnt(2)\n\t"
    "v_smfmac_f32_16x16x64_bf16 v[12:15], v[36:39], v[80:87], v8\n\t"
    "v_smfmac_f32_16x16x64_bf16 v[16:19], v[36:39], v[88:95], v8\n\t"
    "s_waitcnt lgkmcnt(1)\n\t"
    "v_smfmac_f32_16x16x64_bf16 v[12:15], v[40:43], v[96:103], v8\n\t"
    "v_smfmac_f32_16x16x64_bf16 v[16:19], v[40:43], v[104:111], v8\n\t"
    "s_setprio 0\n\t"
    "s_sleep 1\n\t"
    "s_nop 6\n\t"
    "v_add_f32 v12, v12, v14\n\t"
    "v_add_f32 v13, v13, v15\n\t"
    "v_add_f32 v14, v16, v18\n\t"
    "v_add_f32 v15, v17, v19\n\t"
    "ds_write_b128 v6, v[12:15] offset:27648\n\t"
    "ds_write_b32 v9, v11 offset:29440\n\t"
    "s_setprio 0\n\t"
    "v_lshrrev_b32 v10, 24, v11\n\t"
    "v_add_u32 v10, -1, v10\n\t"
    "s_mov_b64 exec, 1\n\t"
    "global_store_dword v6, v10, %[vs] sc1\n\t"
    "s_mov_b64 exec, -1\n\t"
    "ds_read_b128 v[44:47], v6 offset:0\n\t"
    "ds_read_b128 v[48:51], v6 offset:1024\n\t"
    "ds_read_b128 v[52:55], v6 offset:2048\n\t"
    "ds_read_b128 v[56:59], v6 offset:3072\n\t"
    "ds_read_b128 v[60:63], v6 offset:4096\n\t"
    "s_sleep 1\n\t"
    "v_smfmac_f32_16x16x64_bf16 v[20:23], v[28:31], v[216:223], v8\n\t"
    "ds_read_b128 v[28:31], v6 offset:5120\n\t"
    "v_smfmac_f32_16x16x64_bf16 v[20:23], v[24:27], v[248:255], v8\n\t"
    "ds_read_b128 v[24:27], v6 offset:6144\n\t"
    "v_smfmac_f32_16x16x64_bf16 v[20:23], v[32:35], v[224:231], v8\n\t"
    "ds_read_b128 v[32:35], v6 offset:7168\n\t"
    "v_smfmac_f32_16x16x64_bf16 v[20:23], v[36:39], v[240:247], v8\n\t"
    "ds_read_b128 v[36:39], v6 offset:8192\n\t"
    "v_smfmac_f32_16x16x64_bf16 v[20:23], v[40:43], v[232:239], v8\n\t"
    "ds_read_b128 v[40:43], v6 offset:9216\n\t"
    "s_waitcnt lgkmcnt(9)\n\t"
    "v_smfmac_f32_16x16x64_bf16 v[20:23], v[44:47], v[128:135], v8\n\t"
    "ds_read_b128 v[44:47], v6 offset:10240\n\t"
    "s_barrier\n\t"
    "s_setprio 0\n\t"
    "s_waitcnt lgkmcnt(9)\n\t"
    "v_smfmac_f32_16x16x64_bf16 v[20:23], v[48:51], v[144:151], v8\n\t"
    "s_waitcnt lgkmcnt(8)\n\t"
    "v_smfmac_f32_16x16x64_bf16 v[20:23], v[52:55], v[136:143], v8\n\t"
    "s_waitcnt lgkmcnt(7)\n\t"
    "v_smfmac_f32_16x16x64_bf16 v[20:23], v[56:59], v[152:159], v8\n\t"
    "s_waitcnt lgkmcnt(6)\n\t"
    "v_smfmac_f32_16x16x64_bf16 v[20:23], v[60:63], v[160:167], v8\n\t"
    "s_waitcnt lgkmcnt(5)\n\t"
    "v_smfmac_f32_16x16x64_bf16 v[20:23], v[28:31], v[176:183], v8\n\t"
    "s_waitcnt lgkmcnt(4)\n\t"
    "v_smfmac_f32_16x16x64_bf16 v[20:23], v[24:27], v[168:175], v8\n\t"
    "s_waitcnt lgkmcnt(3)\n\t"
    "v_smfmac_f32_16x16x64_bf16 v[20:23], v[32:35], v[184:191], v8\n\t"
    "s_waitcnt lgkmcnt(2)\n\t"
    "v_smfmac_f32_16x16x64_bf16 v[20:23], v[36:39], v[192:199], v8\n\t"
    "s_waitcnt lgkmcnt(1)\n\t"
    "v_smfmac_f32_16x16x64_bf16 v[20:23], v[40:43], v[208:215], v8\n\t"
    "s_waitcnt lgkmcnt(0)\n\t"
    "v_smfmac_f32_16x16x64_bf16 v[20:23], v[44:47], v[200:207], v8\n\t"
    "s_nop 7\n\t"
    "v_add_f32 v20, v20, v22\n\t"
    "v_add_f32 v21, v21, v23\n\t"
    "v_cvt_pk_bf16_f32 v20, v20, v21\n\t"
    "v_min_u32 v20, -2, v20\n\t"
    "ds_write_b32 v9, v20 offset:19200\n\t"
    "s_waitcnt lgkmcnt(0)\n\t"
    "s_setprio 0\n\t"
    "s_waitcnt lgkmcnt(0)\n\t"
    "s_branch 9f\n\t"
    "1:\n\t"
    "s_add_u32 %[sp], %[sp], 1\n\t"
    "s_cmp_ge_u32 %[sp], %[cap]\n\t"
    "s_cbranch_scc1 7f\n\t"
    "global_load_dwordx4 v[24:27], v6, %[ag] sc1\n\t"
    "global_load_dwordx4 v[28:31], v6, %[ag] offset:-4096 sc1\n\t"
    "s_waitcnt lgkmcnt(0)\n\t"
    "s_memtime %[tc]\n\t"
    "s_nop 7\n\t"
    "s_branch 3b\n\t"
    "7:\n\t"
    "s_mov_b32 %[cap], 0\n\t"
    "s_branch 2b\n\t"
    "9:"
    : [tc] "=&s"(tc), [sp] "=&s"(sp), [cap] "+s"(cap), [st] "=&s"(st)
    : [ag] "s"(ag), [c44] "s"(0x4444u), [lsc] "s"(LSC), [sn] "s"(sn), [vs] "s"(vs), "{v[64:95]}"(W.w0), "{v[96:127]}"(W.w1), "{v[128:159]}"(W.w2), "{v[160:191]}"(W.w3), "{v[192:223]}"(W.w4), "{v[224:255]}"(W.w5)
    : "v6", "v7", "v8", "v9", "v10", "v11", "v12", "v13", "v14", "v15", "v16", "v17", "v18", "v19", "v20", "v21", "v22", "v23", "v24", "v25", "v26", "v27", "v28", "v29", "v30", "v31", "v32", "v33", "v34", "v35", "v36", "v37", "v38", "v39", "v40", "v41", "v42", "v43", "v44", "v45", "v46", "v47", "v48", "v49", "v50", "v51", "v52", "v53", "v54", "v55", "v56", "v57", "v58", "v59", "v60", "v61", "v62", "v63", "vcc", "scc", "m0", "memory");
}
template <int W_> __device__ __forceinline__ void seg_attn(const QKW& W, unsigned pb, const char* od, unsigned long long* ts) { }
template <> __device__ __forceinline__ void seg_attn<0>(const QKW& W, unsigned pb, const char* od, unsigned long long* ts) {
  asm volatile("v_mbcnt_lo_u32_b32 v32, -1, 0\n\t"
    "v_mbcnt_hi_u32_b32 v32, -1, v32\n\t"
    "v_lshlrev_b32 v35, 4, v32\n\t"
    "ds_read_b128 v[24:27], v35 offset:20480\n\t"
    "ds_read_b128 v[28:31], v35 offset:21504\n\t"
    "v_lshrrev_b32 v33, 4, v32\n\t"
    "v_and_b32 v34, 15, v32\n\t"
    "v_lshrrev_b32 v4, 2, v34\n\t"
    "v_and_b32 v4, 2, v4\n\t"
    "v_xor_b32 v4, v33, v4\n\t"
    "v_lshl_or_b32 v4, v4, 4, %[pb]\n\t"
    "v_lshl_or_b32 v4, v34, 6, v4\n\t"
    "ds_read_b128 v[8:11], v4 offset:49152\n\t"
    "ds_read_b128 v[12:15], v4 offset:51200\n\t"
    "ds_read_b128 v[16:19], v4 offset:53248\n\t"
    "ds_read_b128 v[20:23], v4 offset:55296\n\t"
    "ds_read_b128 v[40:43], v4 offset:32768\n\t"
    "ds_read_b128 v[44:47], v4 offset:34816\n\t"
    "ds_read_b128 v[48:51], v4 offset:36864\n\t"
    "ds_read_b128 v[52:55], v4 offset:38912\n\t"
    "v_lshlrev_b32 v35, 2, v33\n\t"
    "v_sub_u32 v35, v34, v35\n\t"
    "v_ashrrev_i32 v60, 31, v35\n\t"
    "v_and_b32 v60, %[neg], v60\n\t"
    "v_add_u32 v61, -1, v35\n\t"
    "v_ashrrev_i32 v61, 31, v61\n\t"
    "v_and_b32 v61, %[neg], v61\n\t"
    "v_add_u32 v62, -2, v35\n\t"
    "v_ashrrev_i32 v62, 31, v62\n\t"
    "v_and_b32 v62, %[neg], v62\n\t"
    "v_add_u32 v63, -3, v35\n\t"
    "v_ashrrev_i32 v63, 31, v63\n\t"
    "v_and_b32 v63, %[neg], v63\n\t"
    "s_waitcnt lgkmcnt(6)\n\t"
    "v_mfma_f32_16x16x32_bf16 v[32:35], v[24:27], v[8:11], 0\n\t"
    "v_mfma_f32_16x16x32_bf16 v[36:39], v[24:27], v[12:15], 0\n\t"
    "s_waitcnt lgkmcnt(4)\n\t"
    "v_mfma_f32_16x16x32_bf16 v[32:35], v[28:31], v[16:19], v[32:35]\n\t"
    "v_mfma_f32_16x16x32_bf16 v[36:39], v[28:31], v[20:23], v[36:39]\n\t"
    "s_waitcnt lgkmcnt(3)\n\t"
    "v_mfma_f32_16x16x32_bf16 v[60:63], v[40:43], v[8:11], v[60:63]\n\t"
    "s_waitcnt lgkmcnt(2)\n\t"
    "v_mfma_f32_16x16x32_bf16 v[60:63], v[44:47], v[12:15], v[60:63]\n\t"
    "v_mbcnt_lo_u32_b32 v7, -1, 0\n\t"
    "v_mbcnt_hi_u32_b32 v7, -1, v7\n\t"
    "s_waitcnt lgkmcnt(1)\n\t"
    "v_mfma_f32_16x16x32_bf16 v[60:63], v[48:51], v[16:19], v[60:63]\n\t"
    "v_lshrrev_b32 v5, 4, v7\n\t"
    "v_and_b32 v5, 2, v5\n\t"
    "s_waitcnt lgkmcnt(0)\n\t"
    "v_mfma_f32_16x16x32_bf16 v[60:63], v[52:55], v[20:23], v[60:63]\n\t"
    "v_xor_b32 v5, v5, v7\n\t"
    "v_lshl_or_b32 v5, v5, 4, %[pb]\n\t"
    "ds_read_b64_tr_b16 v[48:49], v5 offset:40960\n\t"
    "ds_read_b64_tr_b16 v[52:53], v5 offset:40968\n\t"
    "v_add_f32 v32, v32, v38\n\t"
    "v_add_f32 v33, v33, v39\n\t"
    "v_and_b32 v6, 12, v7\n\t"
    "v_and_b32 v4, 3, v7\n\t"
    "v_lshl_add_u32 v4, v6, 1, v4\n\t"
    "v_lshrrev_b32 v6, 4, v7\n\t"
    "v_lshl_add_u32 v4, v4, 2, v6\n\t"
    "v_lshlrev_b32 v4, 2, v4\n\t"
    "v_add_u32 v4, 18432, v4\n\t"
    "ds_read_b64_tr_b16 v[40:41], v5 offset:43008\n\t"
    "ds_read_b64_tr_b16 v[44:45], v5 offset:43016\n\t"
    "ds_read2_b32 v[36:37], v4 offset0:0 offset1:16\n\t"
    "ds_read2_b32 v[38:39], v4 offset0:128 offset1:144\n\t"
    "v_and_b32 v4, 7, v7\n\t"
    "v_xor_b32 v6, v6, v4\n\t"
    "v_max3_f32 v34, v32, v33, %[lo]\n\t"
    "v_max3_f32 v34, v34, v60, v61\n\t"
    "v_max3_f32 v34, v34, v62, v63\n\t"
    "v_mov_b32 v35, v34\n\t"
    "v_lshlrev_b32 v6, 4, v6\n\t"
    "v_and_b32 v4, 15, v7\n\t"
    "v_permlane16_swap_b32 v34, v35\n\t"
    "v_max_f32 v34, v34, v35\n\t"
    "v_mov_b32 v35, v34\n\t"
    "v_lshl_or_b32 v6, v4, 7, v6\n\t"
    "v_xor_b32 v4, 64, v6\n\t"
    "v_permlane32_swap_b32 v34, v35\n\t"
    "v_max_f32 v34, v34, v35\n\t"
    "v_mul_f32 v34, %[nl], v34\n\t"
    "v_fma_f32 v32, v32, %[lsc], v34\n\t"
    "v_fma_f32 v33, v33, %[lsc], v34\n\t"
    "v_fma_f32 v60, v60, %[lsc], v34\n\t"
    "v_fma_f32 v61, v61, %[lsc], v34\n\t"
    "v_fma_f32 v62, v62, %[lsc], v34\n\t"
    "v_fma_f32 v63, v63, %[lsc], v34\n\t"
    "v_exp_f32 v60, v60\n\t"
    "v_exp_f32 v61, v61\n\t"
    "v_exp_f32 v62, v62\n\t"
    "v_exp_f32 v63, v63\n\t"
    "v_exp_f32 v32, v32\n\t"
    "v_exp_f32 v33, v33\n\t"
    "v_cvt_pk_bf16_f32 v24, v60, v61\n\t"
    "v_cvt_pk_bf16_f32 v25, v62, v63\n\t"
    "v_cvt_pk_bf16_f32 v28, v32, v33\n\t"
    "s_waitcnt lgkmcnt(0)\n\t"
    "v_mfma_f32_16x16x16_bf16 v[8:11], v[48:49], v[24:25], 0\n\t"
    "v_mov_b32 v56, %[one]\n\t"
    "v_mov_b32 v57, %[one]\n\t"
    "v_mfma_f32_16x16x16_bf16 v[12:15], v[52:53], v[24:25], 0\n\t"
    "v_mov_b32 v58, %[one]\n\t"
    "v_mov_b32 v59, %[one]\n\t"
    "v_mfma_f32_16x16x16_bf16 v[16:19], v[40:41], v[24:25], 0\n\t"
    "v_mov_b32 v29, 0\n\t"
    "v_mov_b32 v30, 0\n\t"
    "v_mfma_f32_16x16x16_bf16 v[60:63], v[56:57], v[24:25], 0\n\t"
    "v_mov_b32 v31, v28\n\t"
    "v_mfma_f32_16x16x16_bf16 v[20:23], v[44:45], v[24:25], 0\n\t"
    "v_mfma_f32_16x16x16_bf16 v[8:11], v[36:37], v[28:29], v[8:11]\n\t"
    "v_mfma_f32_16x16x16_bf16 v[12:15], v[36:37], v[30:31], v[12:15]\n\t"
    "s_nop 1\n\t"
    "v_mfma_f32_16x16x16_bf16 v[16:19], v[38:39], v[28:29], v[16:19]\n\t"
    "v_mfma_f32_16x16x16_bf16 v[60:63], v[56:57], v[28:29], v[60:63]\n\t"
    "s_nop 1\n\t"
    "v_mfma_f32_16x16x16_bf16 v[20:23], v[38:39], v[30:31], v[20:23]\n\t"
    "s_nop 4\n\t"
    "v_rcp_f32 v60, v60\n\t"
    "s_nop 0\n\t"
    "v_pk_mul_f32 v[8:9], v[8:9], v[60:61] op_sel_hi:[1,0]\n\t"
    "v_pk_mul_f32 v[10:11], v[10:11], v[60:61] op_sel_hi:[1,0]\n\t"
    "v_pk_mul_f32 v[12:13], v[12:13], v[60:61] op_sel_hi:[1,0]\n\t"
    "v_pk_mul_f32 v[14:15], v[14:15], v[60:61] op_sel_hi:[1,0]\n\t"
    "v_cvt_pk_bf16_f32 v24, v8, v9\n\t"
    "v_cvt_pk_bf16_f32 v25, v10, v11\n\t"
    "v_cvt_pk_bf16_f32 v26, v12, v13\n\t"
    "v_cvt_pk_bf16_f32 v27, v14, v15\n\t"
    "global_store_dwordx4 v6, v[24:27], %[od] offset:0 sc1\n\t"
    "v_pk_mul_f32 v[16:17], v[16:17], v[60:61] op_sel_hi:[1,0]\n\t"
    "v_pk_mul_f32 v[18:19], v[18:19], v[60:61] op_sel_hi:[1,0]\n\t"
    "v_pk_mul_f32 v[20:21], v[20:21], v[60:61] op_sel_hi:[1,0]\n\t"
    "v_pk_mul_f32 v[22:23], v[22:23], v[60:61] op_sel_hi:[1,0]\n\t"
    "v_cvt_pk_bf16_f32 v28, v16, v17\n\t"
    "v_cvt_pk_bf16_f32 v29, v18, v19\n\t"
    "v_cvt_pk_bf16_f32 v30, v20, v21\n\t"
    "v_cvt_pk_bf16_f32 v31, v22, v23\n\t"
    "global_store_dwordx4 v4, v[28:31], %[od] offset:0 sc1\n\t"
    "s_waitcnt lgkmcnt(0)\n\t"
    "s_nop 0"
    :
    : [pb] "s"(pb), [od] "s"(od), [neg] "s"(-3.0e38f), [lo] "s"(-1.0e38f), [one] "s"(0x3F803F80u), [lsc] "s"(LSC), [nl] "s"(-LSC), "{v[64:95]}"(W.w0), "{v[96:127]}"(W.w1), "{v[128:159]}"(W.w2), "{v[160:191]}"(W.w3), "{v[192:223]}"(W.w4), "{v[224:255]}"(W.w5)
    : "v4", "v5", "v6", "v7", "v8", "v9", "v10", "v11", "v12", "v13", "v14", "v15", "v16", "v17", "v18", "v19", "v20", "v21", "v22", "v23", "v24", "v25", "v26", "v27", "v28", "v29", "v30", "v31", "v32", "v33", "v34", "v35", "v36", "v37", "v38", "v39", "v40", "v41", "v42", "v43", "v44", "v45", "v46", "v47", "v48", "v49", "v50", "v51", "v52", "v53", "v54", "v55", "v56", "v57", "v58", "v59", "v60", "v61", "v62", "v63", "memory");
}
template <> __device__ __forceinline__ void seg_attn<1>(const QKW& W, unsigned pb, const char* od, unsigned long long* ts) {
  asm volatile("v_mbcnt_lo_u32_b32 v32, -1, 0\n\t"
    "v_mbcnt_hi_u32_b32 v32, -1, v32\n\t"
    "v_lshlrev_b32 v35, 4, v32\n\t"
    "ds_read_b128 v[24:27], v35 offset:20480\n\t"
    "ds_read_b128 v[28:31], v35 offset:21504\n\t"
    "v_lshrrev_b32 v33, 4, v32\n\t"
    "v_and_b32 v34, 15, v32\n\t"
    "v_lshrrev_b32 v4, 2, v34\n\t"
    "v_and_b32 v4, 2, v4\n\t"
    "v_xor_b32 v4, v33, v4\n\t"
    "v_lshl_or_b32 v4, v4, 4, %[pb]\n\t"
    "v_lshl_or_b32 v4, v34, 6, v4\n\t"
    "ds_read_b128 v[8:11], v4 offset:50176\n\t"
    "ds_read_b128 v[12:15], v4 offset:52224\n\t"
    "ds_read_b128 v[16:19], v4 offset:54272\n\t"
    "ds_read_b128 v[20:23], v4 offset:56320\n\t"
    "ds_read_b128 v[40:43], v4 offset:32768\n\t"
    "ds_read_b128 v[44:47], v4 offset:34816\n\t"
    "ds_read_b128 v[48:51], v4 offset:36864\n\t"
    "ds_read_b128 v[52:55], v4 offset:38912\n\t"
    "v_lshlrev_b32 v35, 2, v33\n\t"
    "v_sub_u32 v35, v34, v35\n\t"
    "v_ashrrev_i32 v60, 31, v35\n\t"
    "v_and_b32 v60, %[neg], v60\n\t"
    "v_add_u32 v61, -1, v35\n\t"
    "v_ashrrev_i32 v61, 31, v61\n\t"
    "v_and_b32 v61, %[neg], v61\n\t"
    "v_add_u32 v62, -2, v35\n\t"
    "v_ashrrev_i32 v62, 31, v62\n\t"
    "v_and_b32 v62, %[neg], v62\n\t"
    "v_add_u32 v63, -3, v35\n\t"
    "v_ashrrev_i32 v63, 31, v63\n\t"
    "v_and_b32 v63, %[neg], v63\n\t"
    "s_waitcnt lgkmcnt(6)\n\t"
    "v_mfma_f32_16x16x32_bf16 v[32:35], v[24:27], v[8:11], 0\n\t"
    "v_mfma_f32_16x16x32_bf16 v[36:39], v[24:27], v[12:15], 0\n\t"
    "s_waitcnt lgkmcnt(4)\n\t"
    "v_mfma_f32_16x16x32_bf16 v[32:35], v[28:31], v[16:19], v[32:35]\n\t"
    "v_mfma_f32_16x16x32_bf16 v[36:39], v[28:31], v[20:23], v[36:39]\n\t"
    "ds_read_b128 v[24:27], v4 offset:33792\n\t"
    "ds_read_b128 v[28:31], v4 offset:35840\n\t"
    "s_waitcnt lgkmcnt(5)\n\t"
    "v_mfma_f32_16x16x32_bf16 v[56:59], v[40:43], v[8:11], 0\n\t"
    "s_waitcnt lgkmcnt(4)\n\t"
    "v_mfma_f32_16x16x32_bf16 v[56:59], v[44:47], v[12:15], v[56:59]\n\t"
    "ds_read_b128 v[40:43], v4 offset:37888\n\t"
    "ds_read_b128 v[44:47], v4 offset:39936\n\t"
    "v_mbcnt_lo_u32_b32 v7, -1, 0\n\t"
    "v_mbcnt_hi_u32_b32 v7, -1, v7\n\t"
    "s_waitcnt lgkmcnt(5)\n\t"
    "v_mfma_f32_16x16x32_bf16 v[56:59], v[48:51], v[16:19], v[56:59]\n\t"
    "v_lshrrev_b32 v5, 4, v7\n\t"
    "v_and_b32 v5, 2, v5\n\t"
    "s_waitcnt lgkmcnt(4)\n\t"
    "v_mfma_f32_16x16x32_bf16 v[56:59], v[52:55], v[20:23], v[56:59]\n\t"
    "v_xor_b32 v5, v5, v7\n\t"
    "v_lshl_or_b32 v5, v5, 4, %[pb]\n\t"
    "ds_read_b64_tr_b16 v[48:49], v5 offset:40960\n\t"
    "ds_read_b64_tr_b16 v[50:51], v5 offset:41984\n\t"
    "ds_read_b64_tr_b16 v[52:53], v5 offset:40968\n\t"
    "ds_read_b64_tr_b16 v[54:55], v5 offset:41992\n\t"
    "v_add_f32 v32, v32, v38\n\t"
    "v_add_f32 v33, v33, v39\n\t"
    "s_waitcnt lgkmcnt(7)\n\t"
    "v_mfma_f32_16x16x32_bf16 v[60:63], v[24:27], v[8:11], v[60:63]\n\t"
    "v_and_b32 v6, 12, v7\n\t"
    "v_and_b32 v4, 3, v7\n\t"
    "s_waitcnt lgkmcnt(6)\n\t"
    "v_mfma_f32_16x16x32_bf16 v[60:63], v[28:31], v[12:15], v[60:63]\n\t"
    "v_lshl_add_u32 v4, v6, 1, v4\n\t"
    "v_lshrrev_b32 v6, 4, v7\n\t"
    "s_waitcnt lgkmcnt(5)\n\t"
    "v_mfma_f32_16x16x32_bf16 v[60:63], v[40:43], v[16:19], v[60:63]\n\t"
    "v_lshl_add_u32 v4, v4, 2, v6\n\t"
    "v_lshlrev_b32 v4, 2, v4\n\t"
    "s_waitcnt lgkmcnt(4)\n\t"
    "v_mfma_f32_16x16x32_bf16 v[60:63], v[44:47], v[20:23], v[60:63]\n\t"
    "v_add_u32 v4, 18432, v4\n\t"
    "ds_read_b64_tr_b16 v[40:41], v5 offset:43008\n\t"
    "ds_read_b64_tr_b16 v[42:43], v5 offset:44032\n\t"
    "ds_read_b64_tr_b16 v[44:45], v5 offset:43016\n\t"
    "ds_read_b64_tr_b16 v[46:47], v5 offset:44040\n\t"
    "ds_read2_b32 v[36:37], v4 offset0:0 offset1:16\n\t"
    "ds_read2_b32 v[38:39], v4 offset0:128 offset1:144\n\t"
    "v_and_b32 v4, 7, v7\n\t"
    "v_xor_b32 v6, v6, v4\n\t"
    "v_max3_f32 v34, v32, v33, %[lo]\n\t"
    "v_max3_f32 v34, v34, v56, v57\n\t"
    "v_max3_f32 v34, v34, v58, v59\n\t"
    "v_max3_f32 v34, v34, v60, v61\n\t"
    "v_max3_f32 v34, v34, v62, v63\n\t"
    "v_mov_b32 v35, v34\n\t"
    "v_lshlrev_b32 v6, 4, v6\n\t"
    "v_and_b32 v4, 15, v7\n\t"
    "v_permlane16_swap_b32 v34, v35\n\t"
    "v_max_f32 v34, v34, v35\n\t"
    "v_mov_b32 v35, v34\n\t"
    "v_lshl_or_b32 v6, v4, 7, v6\n\t"
    "v_xor_b32 v4, 64, v6\n\t"
    "v_permlane32_swap_b32 v34, v35\n\t"
    "v_max_f32 v34, v34, v35\n\t"
    "v_mul_f32 v34, %[nl], v34\n\t"
    "v_fma_f32 v32, v32, %[lsc], v34\n\t"
    "v_fma_f32 v33, v33, %[lsc], v34\n\t"
    "v_fma_f32 v56, v56, %[lsc], v34\n\t"
    "v_fma_f32 v57, v57, %[lsc], v34\n\t"
    "v_fma_f32 v58, v58, %[lsc], v34\n\t"
    "v_fma_f32 v59, v59, %[lsc], v34\n\t"
    "v_fma_f32 v60, v60, %[lsc], v34\n\t"
    "v_fma_f32 v61, v61, %[lsc], v34\n\t"
    "v_fma_f32 v62, v62, %[lsc], v34\n\t"
    "v_fma_f32 v63, v63, %[lsc], v34\n\t"
    "v_exp_f32 v56, v56\n\t"
    "v_exp_f32 v57, v57\n\t"
    "v_exp_f32 v58, v58\n\t"
    "v_exp_f32 v59, v59\n\t"
    "v_exp_f32 v60, v60\n\t"
    "v_exp_f32 v61, v61\n\t"
    "v_exp_f32 v62, v62\n\t"
    "v_exp_f32 v63, v63\n\t"
    "v_exp_f32 v32, v32\n\t"
    "v_exp_f32 v33, v33\n\t"
    "v_cvt_pk_bf16_f32 v24, v56, v57\n\t"
    "v_cvt_pk_bf16_f32 v25, v58, v59\n\t"
    "v_cvt_pk_bf16_f32 v26, v60, v61\n\t"
    "v_cvt_pk_bf16_f32 v27, v62, v63\n\t"
    "v_cvt_pk_bf16_f32 v28, v32, v33\n\t"
    "s_waitcnt lgkmcnt(0)\n\t"
    "v_mfma_f32_16x16x32_bf16 v[8:11], v[48:51], v[24:27], 0\n\t"
    "v_mov_b32 v56, %[one]\n\t"
    "v_mov_b32 v57, %[one]\n\t"
    "v_mfma_f32_16x16x32_bf16 v[12:15], v[52:55], v[24:27], 0\n\t"
    "v_mov_b32 v58, %[one]\n\t"
    "v_mov_b32 v59, %[one]\n\t"
    "v_mfma_f32_16x16x32_bf16 v[16:19], v[40:43], v[24:27], 0\n\t"
    "v_mov_b32 v29, 0\n\t"
    "v_mov_b32 v30, 0\n\t"
    "v_mfma_f32_16x16x32_bf16 v[60:63], v[56:59], v[24:27], 0\n\t"
    "v_mov_b32 v31, v28\n\t"
    "v_mfma_f32_16x16x32_bf16 v[20:23], v[44:47], v[24:27], 0\n\t"
    "v_mfma_f32_16x16x16_bf16 v[8:11], v[36:37], v[28:29], v[8:11]\n\t"
    "v_mfma_f32_16x16x16_bf16 v[12:15], v[36:37], v[30:31], v[12:15]\n\t"
    "s_nop 1\n\t"
    "v_mfma_f32_16x16x16_bf16 v[16:19], v[38:39], v[28:29], v[16:19]\n\t"
    "v_mfma_f32_16x16x16_bf16 v[60:63], v[56:57], v[28:29], v[60:63]\n\t"
    "s_nop 1\n\t"
    "v_mfma_f32_16x16x16_bf16 v[20:23], v[38:39], v[30:31], v[20:23]\n\t"
    "s_nop 4\n\t"
    "v_rcp_f32 v60, v60\n\t"
    "s_nop 0\n\t"
    "v_pk_mul_f32 v[8:9], v[8:9], v[60:61] op_sel_hi:[1,0]\n\t"
    "v_pk_mul_f32 v[10:11], v[10:11], v[60:61] op_sel_hi:[1,0]\n\t"
    "v_pk_mul_f32 v[12:13], v[12:13], v[60:61] op_sel_hi:[1,0]\n\t"
    "v_pk_mul_f32 v[14:15], v[14:15], v[60:61] op_sel_hi:[1,0]\n\t"
    "v_cvt_pk_bf16_f32 v24, v8, v9\n\t"
    "v_cvt_pk_bf16_f32 v25, v10, v11\n\t"
    "v_cvt_pk_bf16_f32 v26, v12, v13\n\t"
    "v_cvt_pk_bf16_f32 v27, v14, v15\n\t"
    "global_store_dwordx4 v6, v[24:27], %[od] offset:2048 sc1\n\t"
    "v_pk_mul_f32 v[16:17], v[16:17], v[60:61] op_sel_hi:[1,0]\n\t"
    "v_pk_mul_f32 v[18:19], v[18:19], v[60:61] op_sel_hi:[1,0]\n\t"
    "v_pk_mul_f32 v[20:21], v[20:21], v[60:61] op_sel_hi:[1,0]\n\t"
    "v_pk_mul_f32 v[22:23], v[22:23], v[60:61] op_sel_hi:[1,0]\n\t"
    "v_cvt_pk_bf16_f32 v28, v16, v17\n\t"
    "v_cvt_pk_bf16_f32 v29, v18, v19\n\t"
    "v_cvt_pk_bf16_f32 v30, v20, v21\n\t"
    "v_cvt_pk_bf16_f32 v31, v22, v23\n\t"
    "global_store_dwordx4 v4, v[28:31], %[od] offset:2048 sc1\n\t"
    "s_waitcnt lgkmcnt(0)\n\t"
    "s_nop 0"
    :
    : [pb] "s"(pb), [od] "s"(od), [neg] "s"(-3.0e38f), [lo] "s"(-1.0e38f), [one] "s"(0x3F803F80u), [lsc] "s"(LSC), [nl] "s"(-LSC), "{v[64:95]}"(W.w0), "{v[96:127]}"(W.w1), "{v[128:159]}"(W.w2), "{v[160:191]}"(W.w3), "{v[192:223]}"(W.w4), "{v[224:255]}"(W.w5)
    : "v4", "v5", "v6", "v7", "v8", "v9", "v10", "v11", "v12", "v13", "v14", "v15", "v16", "v17", "v18", "v19", "v20", "v21", "v22", "v23", "v24", "v25", "v26", "v27", "v28", "v29", "v30", "v31", "v32", "v33", "v34", "v35", "v36", "v37", "v38", "v39", "v40", "v41", "v42", "v43", "v44", "v45", "v46", "v47", "v48", "v49", "v50", "v51", "v52", "v53", "v54", "v55", "v56", "v57", "v58", "v59", "v60", "v61", "v62", "v63", "memory");
}
template <> __device__ __forceinline__ void seg_attn<2>(const QKW& W, unsigned pb, const char* od, unsigned long long* ts) {
  asm volatile("v_mbcnt_lo_u32_b32 v32, -1, 0\n\t"
    "v_mbcnt_hi_u32_b32 v32, -1, v32\n\t"
    "v_lshlrev_b32 v35, 4, v32\n\t"
    "ds_read_b128 v[24:27], v35 offset:20480\n\t"
    "ds_read_b128 v[28:31], v35 offset:21504\n\t"
    "v_lshrrev_b32 v33, 4, v32\n\t"
    "v_and_b32 v34, 15, v32\n\t"
    "v_lshrrev_b32 v4, 2, v34\n\t"
    "v_and_b32 v4, 2, v4\n\t"
    "v_xor_b32 v4, v33, v4\n\t"
    "v_lshl_or_b32 v4, v4, 4, %[pb]\n\t"
    "v_lshl_or_b32 v4, v34, 6, v4\n\t"
    "ds_read_b128 v[8:11], v4 offset:49152\n\t"
    "ds_read_b128 v[12:15], v4 offset:51200\n\t"
    "ds_read_b128 v[16:19], v4 offset:53248\n\t"
    "ds_read_b128 v[20:23], v4 offset:55296\n\t"
    "ds_read_b128 v[40:43], v4 offset:32768\n\t"
    "ds_read_b128 v[44:47], v4 offset:34816\n\t"
    "ds_read_b128 v[48:51], v4 offset:36864\n\t"
    "ds_read_b128 v[52:55], v4 offset:38912\n\t"
    "v_lshlrev_b32 v35, 2, v33\n\t"
    "v_sub_u32 v35, v34, v35\n\t"
    "v_ashrrev_i32 v60, 31, v35\n\t"
    "v_and_b32 v60, %[neg], v60\n\t"
    "v_add_u32 v61, -1, v35\n\t"
    "v_ashrrev_i32 v61, 31, v61\n\t"
    "v_and_b32 v61, %[neg], v61\n\t"
    "v_add_u32 v62, -2, v35\n\t"
    "v_ashrrev_i32 v62, 31, v62\n\t"
    "v_and_b32 v62, %[neg], v62\n\t"
    "v_add_u32 v63, -3, v35\n\t"
    "v_ashrrev_i32 v63, 31, v63\n\t"
    "v_and_b32 v63, %[neg], v63\n\t"
    "s_waitcnt lgkmcnt(6)\n\t"
    "v_mfma_f32_16x16x32_bf16 v[32:35], v[24:27], v[8:11], 0\n\t"
    "v_mfma_f32_16x16x32_bf16 v[36:39], v[24:27], v[12:15], 0\n\t"
    "s_waitcnt lgkmcnt(4)\n\t"
    "v_mfma_f32_16x16x32_bf16 v[32:35], v[28:31], v[16:19], v[32:35]\n\t"
    "v_mfma_f32_16x16x32_bf16 v[36:39], v[28:31], v[20:23], v[36:39]\n\t"
    "s_waitcnt lgkmcnt(3)\n\t"
    "v_mfma_f32_16x16x32_bf16 v[60:63], v[40:43], v[8:11], v[60:63]\n\t"
    "s_waitcnt lgkmcnt(2)\n\t"
    "v_mfma_f32_16x16x32_bf16 v[60:63], v[44:47], v[12:15], v[60:63]\n\t"
    "v_mbcnt_lo_u32_b32 v7, -1, 0\n\t"
    "v_mbcnt_hi_u32_b32 v7, -1, v7\n\t"
    "s_waitcnt lgkmcnt(1)\n\t"
    "v_mfma_f32_16x16x32_bf16 v[60:63], v[48:51], v[16:19], v[60:63]\n\t"
    "v_lshrrev_b32 v5, 4, v7\n\t"
    "v_and_b32 v5, 2, v5\n\t"
    "s_waitcnt lgkmcnt(0)\n\t"
    "v_mfma_f32_16x16x32_bf16 v[60:63], v[52:55], v[20:23], v[60:63]\n\t"
    "v_xor_b32 v5, v5, v7\n\t"
    "v_lshl_or_b32 v5, v5, 4, %[pb]\n\t"
    "ds_read_b64_tr_b16 v[48:49], v5 offset:45056\n\t"
    "ds_read_b64_tr_b16 v[52:53], v5 offset:45064\n\t"
    "v_add_f32 v32, v32, v38\n\t"
    "v_add_f32 v33, v33, v39\n\t"
    "v_and_b32 v6, 12, v7\n\t"
    "v_and_b32 v4, 3, v7\n\t"
    "v_lshl_add_u32 v4, v6, 1, v4\n\t"
    "v_lshrrev_b32 v6, 4, v7\n\t"
    "v_lshl_add_u32 v4, v4, 2, v6\n\t"
    "v_lshlrev_b32 v4, 2, v4\n\t"
    "v_add_u32 v4, 19456, v4\n\t"
    "ds_read_b64_tr_b16 v[40:41], v5 offset:47104\n\t"
    "ds_read_b64_tr_b16 v[44:45], v5 offset:47112\n\t"
    "ds_read2_b32 v[36:37], v4 offset0:0 offset1:16\n\t"
    "ds_read2_b32 v[38:39], v4 offset0:128 offset1:144\n\t"
    "v_and_b32 v4, 7, v7\n\t"
    "v_xor_b32 v6, v6, v4\n\t"
    "v_max3_f32 v34, v32, v33, %[lo]\n\t"
    "v_max3_f32 v34, v34, v60, v61\n\t"
    "v_max3_f32 v34, v34, v62, v63\n\t"
    "v_mov_b32 v35, v34\n\t"
    "v_lshlrev_b32 v6, 4, v6\n\t"
    "v_and_b32 v4, 15, v7\n\t"
    "v_permlane16_swap_b32 v34, v35\n\t"
    "v_max_f32 v34, v34, v35\n\t"
    "v_mov_b32 v35, v34\n\t"
    "v_lshl_or_b32 v6, v4, 7, v6\n\t"
    "v_xor_b32 v4, 64, v6\n\t"
    "v_permlane32_swap_b32 v34, v35\n\t"
    "v_max_f32 v34, v34, v35\n\t"
    "v_mul_f32 v34, %[nl], v34\n\t"
    "v_fma_f32 v32, v32, %[lsc], v34\n\t"
    "v_fma_f32 v33, v33, %[lsc], v34\n\t"
    "v_fma_f32 v60, v60, %[lsc], v34\n\t"
    "v_fma_f32 v61, v61, %[lsc], v34\n\t"
    "v_fma_f32 v62, v62, %[lsc], v34\n\t"
    "v_fma_f32 v63, v63, %[lsc], v34\n\t"
    "v_exp_f32 v60, v60\n\t"
    "v_exp_f32 v61, v61\n\t"
    "v_exp_f32 v62, v62\n\t"
    "v_exp_f32 v63, v63\n\t"
    "v_exp_f32 v32, v32\n\t"
    "v_exp_f32 v33, v33\n\t"
    "v_cvt_pk_bf16_f32 v24, v60, v61\n\t"
    "v_cvt_pk_bf16_f32 v25, v62, v63\n\t"
    "v_cvt_pk_bf16_f32 v28, v32, v33\n\t"
    "s_waitcnt lgkmcnt(0)\n\t"
    "v_mfma_f32_16x16x16_bf16 v[8:11], v[48:49], v[24:25], 0\n\t"
    "v_mov_b32 v56, %[one]\n\t"
    "v_mov_b32 v57, %[one]\n\t"
    "v_mfma_f32_16x16x16_bf16 v[12:15], v[52:53], v[24:25], 0\n\t"
    "v_mov_b32 v58, %[one]\n\t"
    "v_mov_b32 v59, %[one]\n\t"
    "v_mfma_f32_16x16x16_bf16 v[16:19], v[40:41], v[24:25], 0\n\t"
    "v_mov_b32 v29, 0\n\t"
    "v_mov_b32 v30, 0\n\t"
    "v_mfma_f32_16x16x16_bf16 v[60:63], v[56:57], v[24:25], 0\n\t"
    "v_mov_b32 v31, v28\n\t"
    "v_mfma_f32_16x16x16_bf16 v[20:23], v[44:45], v[24:25], 0\n\t"
    "v_mfma_f32_16x16x16_bf16 v[8:11], v[36:37], v[28:29], v[8:11]\n\t"
    "v_mfma_f32_16x16x16_bf16 v[12:15], v[36:37], v[30:31], v[12:15]\n\t"
    "s_nop 1\n\t"
    "v_mfma_f32_16x16x16_bf16 v[16:19], v[38:39], v[28:29], v[16:19]\n\t"
    "v_mfma_f32_16x16x16_bf16 v[60:63], v[56:57], v[28:29], v[60:63]\n\t"
    "s_nop 1\n\t"
    "v_mfma_f32_16x16x16_bf16 v[20:23], v[38:39], v[30:31], v[20:23]\n\t"
    "s_nop 4\n\t"
    "v_rcp_f32 v60, v60\n\t"
    "s_nop 0\n\t"
    "v_pk_mul_f32 v[8:9], v[8:9], v[60:61] op_sel_hi:[1,0]\n\t"
    "v_pk_mul_f32 v[10:11], v[10:11], v[60:61] op_sel_hi:[1,0]\n\t"
    "v_pk_mul_f32 v[12:13], v[12:13], v[60:61] op_sel_hi:[1,0]\n\t"
    "v_pk_mul_f32 v[14:15], v[14:15], v[60:61] op_sel_hi:[1,0]\n\t"
    "v_cvt_pk_bf16_f32 v24, v8, v9\n\t"
    "v_cvt_pk_bf16_f32 v25, v10, v11\n\t"
    "v_cvt_pk_bf16_f32 v26, v12, v13\n\t"
    "v_cvt_pk_bf16_f32 v27, v14, v15\n\t"
    "global_store_dwordx4 v6, v[24:27], %[od] offset:0 sc1\n\t"
    "v_pk_mul_f32 v[16:17], v[16:17], v[60:61] op_sel_hi:[1,0]\n\t"
    "v_pk_mul_f32 v[18:19], v[18:19], v[60:61] op_sel_hi:[1,0]\n\t"
    "v_pk_mul_f32 v[20:21], v[20:21], v[60:61] op_sel_hi:[1,0]\n\t"
    "v_pk_mul_f32 v[22:23], v[22:23], v[60:61] op_sel_hi:[1,0]\n\t"
    "v_cvt_pk_bf16_f32 v28, v16, v17\n\t"
    "v_cvt_pk_bf16_f32 v29, v18, v19\n\t"
    "v_cvt_pk_bf16_f32 v30, v20, v21\n\t"
    "v_cvt_pk_bf16_f32 v31, v22, v23\n\t"
    "global_store_dwordx4 v4, v[28:31], %[od] offset:0 sc1\n\t"
    "s_waitcnt lgkmcnt(0)\n\t"
    "s_nop 0"
    :
    : [pb] "s"(pb), [od] "s"(od), [neg] "s"(-3.0e38f), [lo] "s"(-1.0e38f), [one] "s"(0x3F803F80u), [lsc] "s"(LSC), [nl] "s"(-LSC), "{v[64:95]}"(W.w0), "{v[96:127]}"(W.w1), "{v[128:159]}"(W.w2), "{v[160:191]}"(W.w3), "{v[192:223]}"(W.w4), "{v[224:255]}"(W.w5)
    : "v4", "v5", "v6", "v7", "v8", "v9", "v10", "v11", "v12", "v13", "v14", "v15", "v16", "v17", "v18", "v19", "v20", "v21", "v22", "v23", "v24", "v25", "v26", "v27", "v28", "v29", "v30", "v31", "v32", "v33", "v34", "v35", "v36", "v37", "v38", "v39", "v40", "v41", "v42", "v43", "v44", "v45", "v46", "v47", "v48", "v49", "v50", "v51", "v52", "v53", "v54", "v55", "v56", "v57", "v58", "v59", "v60", "v61", "v62", "v63", "memory");
}
template <> __device__ __forceinline__ void seg_attn<3>(const QKW& W, unsigned pb, const char* od, unsigned long long* ts) {
  asm volatile("v_mbcnt_lo_u32_b32 v32, -1, 0\n\t"
    "v_mbcnt_hi_u32_b32 v32, -1, v32\n\t"
    "v_lshlrev_b32 v35, 4, v32\n\t"
    "ds_read_b128 v[24:27], v35 offset:20480\n\t"
    "ds_read_b128 v[28:31], v35 offset:21504\n\t"
    "v_lshrrev_b32 v33, 4, v32\n\t"
    "v_and_b32 v34, 15, v32\n\t"
    "v_lshrrev_b32 v4, 2, v34\n\t"
    "v_and_b32 v4, 2, v4\n\t"
    "v_xor_b32 v4, v33, v4\n\t"
    "v_lshl_or_b32 v4, v4, 4, %[pb]\n\t"
    "v_lshl_or_b32 v4, v34, 6, v4\n\t"
    "ds_read_b128 v[8:11], v4 offset:50176\n\t"
    "ds_read_b128 v[12:15], v4 offset:52224\n\t"
    "ds_read_b128 v[16:19], v4 offset:54272\n\t"
    "ds_read_b128 v[20:23], v4 offset:56320\n\t"
    "ds_read_b128 v[40:43], v4 offset:32768\n\t"
    "ds_read_b128 v[44:47], v4 offset:34816\n\t"
    "ds_read_b128 v[48:51], v4 offset:36864\n\t"
    "ds_read_b128 v[52:55], v4 offset:38912\n\t"
    "v_lshlrev_b32 v35, 2, v33\n\t"
    "v_sub_u32 v35, v34, v35\n\t"
    "v_ashrrev_i32 v60, 31, v35\n\t"
    "v_and_b32 v60, %[neg], v60\n\t"
    "v_add_u32 v61, -1, v35\n\t"
    "v_ashrrev_i32 v61, 31, v61\n\t"
    "v_and_b32 v61, %[neg], v61\n\t"
    "v_add_u32 v62, -2, v35\n\t"
    "v_ashrrev_i32 v62, 31, v62\n\t"
    "v_and_b32 v62, %[neg], v62\n\t"
    "v_add_u32 v63, -3, v35\n\t"
    "v_ashrrev_i32 v63, 31, v63\n\t"
    "v_and_b32 v63, %[neg], v63\n\t"
    "s_waitcnt lgkmcnt(6)\n\t"
    "v_mfma_f32_16x16x32_bf16 v[32:35], v[24:27], v[8:11], 0\n\t"
    "v_mfma_f32_16x16x32_bf16 v[36:39], v[24:27], v[12:15], 0\n\t"
    "s_waitcnt lgkmcnt(4)\n\t"
    "v_mfma_f32_16x16x32_bf16 v[32:35], v[28:31], v[16:19], v[32:35]\n\t"
    "v_mfma_f32_16x16x32_bf16 v[36:39], v[28:31], v[20:23], v[36:39]\n\t"
    "ds_read_b128 v[24:27], v4 offset:33792\n\t"
    "ds_read_b128 v[28:31], v4 offset:35840\n\t"
    "s_waitcnt lgkmcnt(5)\n\t"
    "v_mfma_f32_16x16x32_bf16 v[56:59], v[40:43], v[8:11], 0\n\t"
    "s_waitcnt lgkmcnt(4)\n\t"
    "v_mfma_f32_16x16x32_bf16 v[56:59], v[44:47], v[12:15], v[56:59]\n\t"
    "ds_read_b128 v[40:43], v4 offset:37888\n\t"
    "ds_read_b128 v[44:47], v4 offset:39936\n\t"
    "v_mbcnt_lo_u32_b32 v7, -1, 0\n\t"
    "v_mbcnt_hi_u32_b32 v7, -1, v7\n\t"
    "s_waitcnt lgkmcnt(5)\n\t"
    "v_mfma_f32_16x16x32_bf16 v[56:59], v[48:51], v[16:19], v[56:59]\n\t"
    "v_lshrrev_b32 v5, 4, v7\n\t"
    "v_and_b32 v5, 2, v5\n\t"
    "s_waitcnt lgkmcnt(4)\n\t"
    "v_mfma_f32_16x16x32_bf16 v[56:59], v[52:55], v[20:23], v[56:59]\n\t"
    "v_xor_b32 v5, v5, v7\n\t"
    "v_lshl_or_b32 v5, v5, 4, %[pb]\n\t"
    "ds_read_b64_tr_b16 v[48:49], v5 offset:45056\n\t"
    "ds_read_b64_tr_b16 v[50:51], v5 offset:46080\n\t"
    "ds_read_b64_tr_b16 v[52:53], v5 offset:45064\n\t"
    "ds_read_b64_tr_b16 v[54:55], v5 offset:46088\n\t"
    "v_add_f32 v32, v32, v38\n\t"
    "v_add_f32 v33, v33, v39\n\t"
    "s_waitcnt lgkmcnt(7)\n\t"
    "v_mfma_f32_16x16x32_bf16 v[60:63], v[24:27], v[8:11], v[60:63]\n\t"
    "v_and_b32 v6, 12, v7\n\t"
    "v_and_b32 v4, 3, v7\n\t"
    "s_waitcnt lgkmcnt(6)\n\t"
    "v_mfma_f32_16x16x32_bf16 v[60:63], v[28:31], v[12:15], v[60:63]\n\t"
    "v_lshl_add_u32 v4, v6, 1, v4\n\t"
    "v_lshrrev_b32 v6, 4, v7\n\t"
    "s_waitcnt lgkmcnt(5)\n\t"
    "v_mfma_f32_16x16x32_bf16 v[60:63], v[40:43], v[16:19], v[60:63]\n\t"
    "v_lshl_add_u32 v4, v4, 2, v6\n\t"
    "v_lshlrev_b32 v4, 2, v4\n\t"
    "s_waitcnt lgkmcnt(4)\n\t"
    "v_mfma_f32_16x16x32_bf16 v[60:63], v[44:47], v[20:23], v[60:63]\n\t"
    "v_add_u32 v4, 19456, v4\n\t"
    "ds_read_b64_tr_b16 v[40:41], v5 offset:47104\n\t"
    "ds_read_b64_tr_b16 v[42:43], v5 offset:48128\n\t"
    "ds_read_b64_tr_b16 v[44:45], v5 offset:47112\n\t"
    "ds_read_b64_tr_b16 v[46:47], v5 offset:48136\n\t"
    "ds_read2_b32 v[36:37], v4 offset0:0 offset1:16\n\t"
    "ds_read2_b32 v[38:39], v4 offset0:128 offset1:144\n\t"
    "v_and_b32 v4, 7, v7\n\t"
    "v_xor_b32 v6, v6, v4\n\t"
    "v_max3_f32 v34, v32, v33, %[lo]\n\t"
    "v_max3_f32 v34, v34, v56, v57\n\t"
    "v_max3_f32 v34, v34, v58, v59\n\t"
    "v_max3_f32 v34, v34, v60, v61\n\t"
    "v_max3_f32 v34, v34, v62, v63\n\t"
    "v_mov_b32 v35, v34\n\t"
    "v_lshlrev_b32 v6, 4, v6\n\t"
    "v_and_b32 v4, 15, v7\n\t"
    "v_permlane16_swap_b32 v34, v35\n\t"
    "v_max_f32 v34, v34, v35\n\t"
    "v_mov_b32 v35, v34\n\t"
    "v_lshl_or_b32 v6, v4, 7, v6\n\t"
    "v_xor_b32 v4, 64, v6\n\t"
    "v_permlane32_swap_b32 v34, v35\n\t"
    "v_max_f32 v34, v34, v35\n\t"
    "v_mul_f32 v34, %[nl], v34\n\t"
    "v_fma_f32 v32, v32, %[lsc], v34\n\t"
    "v_fma_f32 v33, v33, %[lsc], v34\n\t"
    "v_fma_f32 v56, v56, %[lsc], v34\n\t"
    "v_fma_f32 v57, v57, %[lsc], v34\n\t"
    "v_fma_f32 v58, v58, %[lsc], v34\n\t"
    "v_fma_f32 v59, v59, %[lsc], v34\n\t"
    "v_fma_f32 v60, v60, %[lsc], v34\n\t"
    "v_fma_f32 v61, v61, %[lsc], v34\n\t"
    "v_fma_f32 v62, v62, %[lsc], v34\n\t"
    "v_fma_f32 v63, v63, %[lsc], v34\n\t"
    "v_exp_f32 v56, v56\n\t"
    "v_exp_f32 v57, v57\n\t"
    "v_exp_f32 v58, v58\n\t"
    "v_exp_f32 v59, v59\n\t"
    "v_exp_f32 v60, v60\n\t"
    "v_exp_f32 v61, v61\n\t"
    "v_exp_f32 v62, v62\n\t"
    "v_exp_f32 v63, v63\n\t"
    "v_exp_f32 v32, v32\n\t"
    "v_exp_f32 v33, v33\n\t"
    "v_cvt_pk_bf16_f32 v24, v56, v57\n\t"
    "v_cvt_pk_bf16_f32 v25, v58, v59\n\t"
    "v_cvt_pk_bf16_f32 v26, v60, v61\n\t"
    "v_cvt_pk_bf16_f32 v27, v62, v63\n\t"
    "v_cvt_pk_bf16_f32 v28, v32, v33\n\t"
    "s_waitcnt lgkmcnt(0)\n\t"
    "v_mfma_f32_16x16x32_bf16 v[8:11], v[48:51], v[24:27], 0\n\t"
    "v_mov_b32 v56, %[one]\n\t"
    "v_mov_b32 v57, %[one]\n\t"
    "v_mfma_f32_16x16x32_bf16 v[12:15], v[52:55], v[24:27], 0\n\t"
    "v_mov_b32 v58, %[one]\n\t"
    "v_mov_b32 v59, %[one]\n\t"
    "v_mfma_f32_16x16x32_bf16 v[16:19], v[40:43], v[24:27], 0\n\t"
    "v_mov_b32 v29, 0\n\t"
    "v_mov_b32 v30, 0\n\t"
    "v_mfma_f32_16x16x32_bf16 v[60:63], v[56:59], v[24:27], 0\n\t"
    "v_mov_b32 v31, v28\n\t"
    "v_mfma_f32_16x16x32_bf16 v[20:23], v[44:47], v[24:27], 0\n\t"
    "v_mfma_f32_16x16x16_bf16 v[8:11], v[36:37], v[28:29], v[8:11]\n\t"
    "v_mfma_f32_16x16x16_bf16 v[12:15], v[36:37], v[30:31], v[12:15]\n\t"
    "s_nop 1\n\t"
    "v_mfma_f32_16x16x16_bf16 v[16:19], v[38:39], v[28:29], v[16:19]\n\t"
    "v_mfma_f32_16x16x16_bf16 v[60:63], v[56:57], v[28:29], v[60:63]\n\t"
    "s_nop 1\n\t"
    "v_mfma_f32_16x16x16_bf16 v[20:23], v[38:39], v[30:31], v[20:23]\n\t"
    "s_nop 4\n\t"
    "v_rcp_f32 v60, v60\n\t"
    "s_nop 0\n\t"
    "v_pk_mul_f32 v[8:9], v[8:9], v[60:61] op_sel_hi:[1,0]\n\t"
    "v_pk_mul_f32 v[10:11], v[10:11], v[60:61] op_sel_hi:[1,0]\n\t"
    "v_pk_mul_f32 v[12:13], v[12:13], v[60:61] op_sel_hi:[1,0]\n\t"
    "v_pk_mul_f32 v[14:15], v[14:15], v[60:61] op_sel_hi:[1,0]\n\t"
    "v_cvt_pk_bf16_f32 v24, v8, v9\n\t"
    "v_cvt_pk_bf16_f32 v25, v10, v11\n\t"
    "v_cvt_pk_bf16_f32 v26, v12, v13\n\t"
    "v_cvt_pk_bf16_f32 v27, v14, v15\n\t"
    "global_store_dwordx4 v6, v[24:27], %[od] offset:2048 sc1\n\t"
    "v_pk_mul_f32 v[16:17], v[16:17], v[60:61] op_sel_hi:[1,0]\n\t"
    "v_pk_mul_f32 v[18:19], v[18:19], v[60:61] op_sel_hi:[1,0]\n\t"
    "v_pk_mul_f32 v[20:21], v[20:21], v[60:61] op_sel_hi:[1,0]\n\t"
    "v_pk_mul_f32 v[22:23], v[22:23], v[60:61] op_sel_hi:[1,0]\n\t"
    "v_cvt_pk_bf16_f32 v28, v16, v17\n\t"
    "v_cvt_pk_bf16_f32 v29, v18, v19\n\t"
    "v_cvt_pk_bf16_f32 v30, v20, v21\n\t"
    "v_cvt_pk_bf16_f32 v31, v22, v23\n\t"
    "global_store_dwordx4 v4, v[28:31], %[od] offset:2048 sc1\n\t"
    "s_waitcnt lgkmcnt(0)\n\t"
    "s_nop 0"
    :
    : [pb] "s"(pb), [od] "s"(od), [neg] "s"(-3.0e38f), [lo] "s"(-1.0e38f), [one] "s"(0x3F803F80u), [lsc] "s"(LSC), [nl] "s"(-LSC), "{v[64:95]}"(W.w0), "{v[96:127]}"(W.w1), "{v[128:159]}"(W.w2), "{v[160:191]}"(W.w3), "{v[192:223]}"(W.w4), "{v[224:255]}"(W.w5)
    : "v4", "v5", "v6", "v7", "v8", "v9", "v10", "v11", "v12", "v13", "v14", "v15", "v16", "v17", "v18", "v19", "v20", "v21", "v22", "v23", "v24", "v25", "v26", "v27", "v28", "v29", "v30", "v31", "v32", "v33", "v34", "v35", "v36", "v37", "v38", "v39", "v40", "v41", "v42", "v43", "v44", "v45", "v46", "v47", "v48", "v49", "v50", "v51", "v52", "v53", "v54", "v55", "v56", "v57", "v58", "v59", "v60", "v61", "v62", "v63", "memory");
}
template <int I> __device__ __forceinline__ void qk_wload(u32x32& w, unsigned a0, unsigned a1);
template <> __device__ __forceinline__ void qk_wload<0>(u32x32& w, unsigned a0, unsigned a1) {
  asm volatile("ds_read_b64_tr_b16 v[64:65], %1 offset:0\n\t"
    "ds_read_b64_tr_b16 v[66:67], %1 offset:512\n\t"
    "ds_read_b64_tr_b16 v[68:69], %1 offset:8192\n\t"
    "ds_read_b64_tr_b16 v[70:71], %1 offset:8704\n\t"
    "ds_read_b64_tr_b16 v[72:73], %2 offset:0\n\t"
    "ds_read_b64_tr_b16 v[74:75], %2 offset:512\n\t"
    "ds_read_b64_tr_b16 v[76:77], %2 offset:8192\n\t"
    "ds_read_b64_tr_b16 v[78:79], %2 offset:8704\n\t"
    "s_waitcnt lgkmcnt(0)\n\t"
    "ds_read_b64_tr_b16 v[80:81], %1 offset:16384\n\t"
    "ds_read_b64_tr_b16 v[82:83], %1 offset:16896\n\t"
    "ds_read_b64_tr_b16 v[84:85], %1 offset:24576\n\t"
    "ds_read_b64_tr_b16 v[86:87], %1 offset:25088\n\t"
    "ds_read_b64_tr_b16 v[88:89], %2 offset:16384\n\t"
    "ds_read_b64_tr_b16 v[90:91], %2 offset:16896\n\t"
    "ds_read_b64_tr_b16 v[92:93], %2 offset:24576\n\t"
    "ds_read_b64_tr_b16 v[94:95], %2 offset:25088\n\t"
    "s_waitcnt lgkmcnt(0)"
    : "=&{v[64:95]}"(w) : "v"(a0), "v"(a1) : "memory");
}
template <> __device__ __forceinline__ void qk_wload<1>(u32x32& w, unsigned a0, unsigned a1) {
  asm volatile("ds_read_b64_tr_b16 v[96:97], %1 offset:0\n\t"
    "ds_read_b64_tr_b16 v[98:99], %1 offset:512\n\t"
    "ds_read_b64_tr_b16 v[100:101], %1 offset:8192\n\t"
    "ds_read_b64_tr_b16 v[102:103], %1 offset:8704\n\t"
    "ds_read_b64_tr_b16 v[104:105], %2 offset:0\n\t"
    "ds_read_b64_tr_b16 v[106:107], %2 offset:512\n\t"
    "ds_read_b64_tr_b16 v[108:109], %2 offset:8192\n\t"
    "ds_read_b64_tr_b16 v[110:111], %2 offset:8704\n\t"
    "s_waitcnt lgkmcnt(0)\n\t"
    "ds_read_b64_tr_b16 v[112:113], %1 offset:16384\n\t"
    "ds_read_b64_tr_b16 v[114:115], %1 offset:16896\n\t"
    "ds_read_b64_tr_b16 v[116:117], %1 offset:24576\n\t"
    "ds_read_b64_tr_b16 v[118:119], %1 offset:25088\n\t"
    "ds_read_b64_tr_b16 v[120:121], %2 offset:16384\n\t"
    "ds_read_b64_tr_b16 v[122:123], %2 offset:16896\n\t"
    "ds_read_b64_tr_b16 v[124:125], %2 offset:24576\n\t"
    "ds_read_b64_tr_b16 v[126:127], %2 offset:25088\n\t"
    "s_waitcnt lgkmcnt(0)"
    : "=&{v[96:127]}"(w) : "v"(a0), "v"(a1) : "memory");
}
template <> __device__ __forceinline__ void qk_wload<2>(u32x32& w, unsigned a0, unsigned a1) {
  asm volatile("ds_read_b64_tr_b16 v[128:129], %1 offset:0\n\t"
    "ds_read_b64_tr_b16 v[130:131], %1 offset:512\n\t"
    "ds_read_b64_tr_b16 v[132:133], %1 offset:8192\n\t"
    "ds_read_b64_tr_b16 v[134:135], %1 offset:8704\n\t"
    "ds_read_b64_tr_b16 v[136:137], %2 offset:0\n\t"
    "ds_read_b64_tr_b16 v[138:139], %2 offset:512\n\t"
    "ds_read_b64_tr_b16 v[140:141], %2 offset:8192\n\t"
    "ds_read_b64_tr_b16 v[142:143], %2 offset:8704\n\t"
    "s_waitcnt lgkmcnt(0)\n\t"
    "ds_read_b64_tr_b16 v[144:145], %1 offset:16384\n\t"
    "ds_read_b64_tr_b16 v[146:147], %1 offset:16896\n\t"
    "ds_read_b64_tr_b16 v[148:149], %1 offset:24576\n\t"
    "ds_read_b64_tr_b16 v[150:151], %1 offset:25088\n\t"
    "ds_read_b64_tr_b16 v[152:153], %2 offset:16384\n\t"
    "ds_read_b64_tr_b16 v[154:155], %2 offset:16896\n\t"
    "ds_read_b64_tr_b16 v[156:157], %2 offset:24576\n\t"
    "ds_read_b64_tr_b16 v[158:159], %2 offset:25088\n\t"
    "s_waitcnt lgkmcnt(0)"
    : "=&{v[128:159]}"(w) : "v"(a0), "v"(a1) : "memory");
}
template <> __device__ __forceinline__ void qk_wload<3>(u32x32& w, unsigned a0, unsigned a1) {
  asm volatile("ds_read_b64_tr_b16 v[160:161], %1 offset:0\n\t"
    "ds_read_b64_tr_b16 v[162:163], %1 offset:512\n\t"
    "ds_read_b64_tr_b16 v[164:165], %1 offset:8192\n\t"
    "ds_read_b64_tr_b16 v[166:167], %1 offset:8704\n\t"
    "ds_read_b64_tr_b16 v[168:169], %2 offset:0\n\t"
    "ds_read_b64_tr_b16 v[170:171], %2 offset:512\n\t"
    "ds_read_b64_tr_b16 v[172:173], %2 offset:8192\n\t"
    "ds_read_b64_tr_b16 v[174:175], %2 offset:8704\n\t"
    "s_waitcnt lgkmcnt(0)\n\t"
    "ds_read_b64_tr_b16 v[176:177], %1 offset:16384\n\t"
    "ds_read_b64_tr_b16 v[178:179], %1 offset:16896\n\t"
    "ds_read_b64_tr_b16 v[180:181], %1 offset:24576\n\t"
    "ds_read_b64_tr_b16 v[182:183], %1 offset:25088\n\t"
    "ds_read_b64_tr_b16 v[184:185], %2 offset:16384\n\t"
    "ds_read_b64_tr_b16 v[186:187], %2 offset:16896\n\t"
    "ds_read_b64_tr_b16 v[188:189], %2 offset:24576\n\t"
    "ds_read_b64_tr_b16 v[190:191], %2 offset:25088\n\t"
    "s_waitcnt lgkmcnt(0)"
    : "=&{v[160:191]}"(w) : "v"(a0), "v"(a1) : "memory");
}
template <> __device__ __forceinline__ void qk_wload<4>(u32x32& w, unsigned a0, unsigned a1) {
  asm volatile("ds_read_b64_tr_b16 v[192:193], %1 offset:0\n\t"
    "ds_read_b64_tr_b16 v[194:195], %1 offset:512\n\t"
    "ds_read_b64_tr_b16 v[196:197], %1 offset:8192\n\t"
    "ds_read_b64_tr_b16 v[198:199], %1 offset:8704\n\t"
    "ds_read_b64_tr_b16 v[200:201], %2 offset:0\n\t"
    "ds_read_b64_tr_b16 v[202:203], %2 offset:512\n\t"
    "ds_read_b64_tr_b16 v[204:205], %2 offset:8192\n\t"
    "ds_read_b64_tr_b16 v[206:207], %2 offset:8704\n\t"
    "s_waitcnt lgkmcnt(0)\n\t"
    "ds_read_b64_tr_b16 v[208:209], %1 offset:16384\n\t"
    "ds_read_b64_tr_b16 v[210:211], %1 offset:16896\n\t"
    "ds_read_b64_tr_b16 v[212:213], %1 offset:24576\n\t"
    "ds_read_b64_tr_b16 v[214:215], %1 offset:25088\n\t"
    "ds_read_b64_tr_b16 v[216:217], %2 offset:16384\n\t"
    "ds_read_b64_tr_b16 v[218:219], %2 offset:16896\n\t"
    "ds_read_b64_tr_b16 v[220:221], %2 offset:24576\n\t"
    "ds_read_b64_tr_b16 v[222:223], %2 offset:25088\n\t"
    "s_waitcnt lgkmcnt(0)"
    : "=&{v[192:223]}"(w) : "v"(a0), "v"(a1) : "memory");
}
template <> __device__ __forceinline__ void qk_wload<5>(u32x32& w, unsigned a0, unsigned a1) {
  asm volatile("ds_read_b64_tr_b16 v[224:225], %1 offset:0\n\t"
    "ds_read_b64_tr_b16 v[226:227], %1 offset:512\n\t"
    "ds_read_b64_tr_b16 v[228:229], %1 offset:8192\n\t"
    "ds_read_b64_tr_b16 v[230:231], %1 offset:8704\n\t"
    "ds_read_b64_tr_b16 v[232:233], %2 offset:0\n\t"
    "ds_read_b64_tr_b16 v[234:235], %2 offset:512\n\t"
    "ds_read_b64_tr_b16 v[236:237], %2 offset:8192\n\t"
    "ds_read_b64_tr_b16 v[238:239], %2 offset:8704\n\t"
    "s_waitcnt lgkmcnt(0)\n\t"
    "ds_read_b64_tr_b16 v[240:241], %1 offset:16384\n\t"
    "ds_read_b64_tr_b16 v[242:243], %1 offset:16896\n\t"
    "ds_read_b64_tr_b16 v[244:245], %1 offset:24576\n\t"
    "ds_read_b64_tr_b16 v[246:247], %1 offset:25088\n\t"
    "ds_read_b64_tr_b16 v[248:249], %2 offset:16384\n\t"
    "ds_read_b64_tr_b16 v[250:251], %2 offset:16896\n\t"
    "ds_read_b64_tr_b16 v[252:253], %2 offset:24576\n\t"
    "ds_read_b64_tr_b16 v[254:255], %2 offset:25088\n\t"
    "s_waitcnt lgkmcnt(0)"
    : "=&{v[224:255]}"(w) : "v"(a0), "v"(a1) : "memory");
}
#define OREP 2
#define AREP 2
#define BELL1 0
#define RINGA 1
constexpr int V_P1 = 131072, V_PV = 149632, V_P2 = 0;
struct VRW { u32x32 k0, k1, o0, o1, o2, o3; };
__device__ __forceinline__ void vr_v(const VRW& W, const char* ag, const char* kd, const char* vd, unsigned wl, unsigned wa, unsigned wb, unsigned ra, unsigned rb, unsigned& cap, unsigned& sp, unsigned long long& tc, unsigned long long* ts) {
  asm volatile("s_mov_b32 %[sp], 0\n\t"
    "v_mbcnt_lo_u32_b32 v20, -1, 0\n\t"
    "v_mbcnt_hi_u32_b32 v20, -1, v20\n\t"
    "v_lshlrev_b32 v21, 4, v20\n\t"
    "s_nop 0\n\t"
    "global_load_dwordx4 v[24:27], v21, %[ag] sc1\n\t"
    "global_load_dwordx4 v[28:31], v21, %[ag] offset:1024 sc1\n\t"
    "s_memtime %[tc]\n\t"
    "v_lshl_add_u32 v6, v20, 4, %[wl]\n\t"
    "ds_read_b128 v[32:35], v6 offset:0\n\t"
    "ds_read_b128 v[36:39], v6 offset:1024\n\t"
    "ds_read_b128 v[40:43], v6 offset:2048\n\t"
    "ds_read_b128 v[44:47], v6 offset:3072\n\t"
    "ds_read_b128 v[48:51], v6 offset:4096\n\t"
    "ds_read_b128 v[52:55], v6 offset:5120\n\t"
    "ds_read_b128 v[56:59], v6 offset:6144\n\t"
    "ds_read_b128 v[60:63], v6 offset:7168\n\t"
    "s_setprio 3\n\t"
    "v_and_b32 v6, 2, v20\n\t"
    "v_mul_u32_u24 v6, 0x5555, v6\n\t"
    "v_or_b32 v4, %[c44], v6\n\t"
    "v_and_b32 v6, 15, v20\n\t"
    "v_lshl_add_u32 v5, v6, 2, %[wa]\n\t"
    "v_and_b32 v6, 48, v20\n\t"
    "v_mad_u32_u24 v5, v6, 36, v5\n\t"
    "v_and_b32 v6, 32, v20\n\t"
    "v_mul_u32_u24 v7, 18, v6\n\t"
    "v_and_b32 v6, 4, v20\n\t"
    "v_mul_u32_u24 v6, 72, v6\n\t"
    "v_add_u32 v7, v7, v6\n\t"
    "v_and_b32 v6, 16, v20\n\t"
    "v_lshl_add_u32 v7, v6, 3, v7\n\t"
    "v_and_b32 v6, 3, v20\n\t"
    "v_lshl_add_u32 v7, v6, 3, v7\n\t"
    "v_and_b32 v6, 8, v20\n\t"
    "v_mul_u32_u24 v6, 1160, v6\n\t"
    "v_add3_u32 v7, v7, v6, %[ra]\n\t"
    "3:\n\t"
    "v_mov_b32 v8, 0\n\t"
    "v_mov_b32 v9, 0\n\t"
    "v_mov_b32 v10, 0\n\t"
    "v_mov_b32 v11, 0\n\t"
    "v_mov_b32 v12, 0\n\t"
    "v_mov_b32 v13, 0\n\t"
    "v_mov_b32 v14, 0\n\t"
    "v_mov_b32 v15, 0\n\t"
    "v_mov_b32 v16, 0\n\t"
    "v_mov_b32 v17, 0\n\t"
    "v_mov_b32 v18, 0\n\t"
    "v_mov_b32 v19, 0\n\t"
    "v_mov_b32 v20, 0\n\t"
    "v_mov_b32 v21, 0\n\t"
    "v_mov_b32 v22, 0\n\t"
    "v_mov_b32 v23, 0\n\t"
    "s_waitcnt vmcnt(0) lgkmcnt(0)\n\t"
    "v_smfmac_f32_16x16x64_bf16 v[8:11], v[24:27], v[64:71], v4\n\t"
    "v_smfmac_f32_16x16x64_bf16 v[8:11], v[28:31], v[72:79], v4\n\t"
    "v_max3_u32 v6, v24, v25, v26\n\t"
    "v_max3_u32 v6, v6, v27, v28\n\t"
    "v_smfmac_f32_16x16x64_bf16 v[12:15], v[24:27], v[80:87], v4\n\t"
    "v_max3_u32 v6, v6, v29, v30\n\t"
    "v_max_u32 v6, v6, v31\n\t"
    "v_smfmac_f32_16x16x64_bf16 v[12:15], v[28:31], v[88:95], v4\n\t"
    "v_cmp_eq_u32 vcc, -1, v6\n\t"
    "s_cbranch_vccnz 1f\n\t"
    "2:\n\t"
    "v_smfmac_f32_16x16x64_bf16 v[16:19], v[24:27], v[96:103], v4\n\t"
    "v_smfmac_f32_16x16x64_bf16 v[16:19], v[28:31], v[104:111], v4\n\t"
    "v_add_f32 v8, v8, v10\n\t"
    "v_add_f32 v9, v9, v11\n\t"
    "ds_write2_b32 v5, v8, v9 offset0:0 offset1:72\n\t"
    "v_smfmac_f32_16x16x64_bf16 v[20:23], v[24:27], v[112:119], v4\n\t"
    "v_smfmac_f32_16x16x64_bf16 v[20:23], v[28:31], v[120:127], v4\n\t"
    "v_add_f32 v12, v12, v14\n\t"
    "v_add_f32 v13, v13, v15\n\t"
    "ds_write2_b32 v5, v12, v13 offset0:16 offset1:88\n\t"
    "v_add_f32 v16, v16, v18\n\t"
    "v_add_f32 v17, v17, v19\n\t"
    "ds_write2_b32 v5, v16, v17 offset0:32 offset1:104\n\t"
    "s_nop 1\n\t"
    "v_add_f32 v20, v20, v22\n\t"
    "v_add_f32 v21, v21, v23\n\t"
    "ds_write2_b32 v5, v20, v21 offset0:48 offset1:120\n\t"
    "v_mov_b32 v8, 0\n\t"
    "v_mov_b32 v9, 0\n\t"
    "v_mov_b32 v10, 0\n\t"
    "v_mov_b32 v11, 0\n\t"
    "v_mov_b32 v12, 0\n\t"
    "v_mov_b32 v13, 0\n\t"
    "v_mov_b32 v14, 0\n\t"
    "v_mov_b32 v15, 0\n\t"
    "v_mbcnt_lo_u32_b32 v5, -1, 0\n\t"
    "v_mbcnt_hi_u32_b32 v5, -1, v5\n\t"
    "s_waitcnt lgkmcnt(0)\n\t"
    "s_barrier\n\t"
    "ds_read_b64 v[16:17], v7 offset:0\n\t"
    "ds_read_b64 v[18:19], v7 offset:2304\n\t"
    "ds_read_b64 v[20:21], v7 offset:4608\n\t"
    "ds_read_b64 v[22:23], v7 offset:6912\n\t"
    "v_smfmac_f32_16x16x64_bf16 v[8:11], v[24:27], v[32:39], v4\n\t"
    "v_smfmac_f32_16x16x64_bf16 v[8:11], v[28:31], v[40:47], v4\n\t"
    "v_smfmac_f32_16x16x64_bf16 v[12:15], v[24:27], v[48:55], v4\n\t"
    "v_smfmac_f32_16x16x64_bf16 v[12:15], v[28:31], v[56:63], v4\n\t"
    "v_and_b32 v6, 48, v5\n\t"
    "v_and_b32 v7, 7, v5\n\t"
    "v_lshlrev_b32 v7, 2, v7\n\t"
    "v_lshl_or_b32 v7, v6, 1, v7\n\t"
    "s_waitcnt lgkmcnt(2)\n\t"
    "v_pk_add_f32 v[16:17], v[16:17], v[18:19]\n\t"
    "s_waitcnt lgkmcnt(0)\n\t"
    "v_pk_add_f32 v[20:21], v[20:21], v[22:23]\n\t"
    "v_pk_add_f32 v[16:17], v[16:17], v[20:21]\n\t"
    "s_nop 1\n\t"
    "v_add_f32_dpp v16, v16, v16 row_ror:8 row_mask:0xf bank_mask:0xf\n\t"
    "v_add_f32_dpp v17, v17, v17 row_ror:8 row_mask:0xf bank_mask:0xf\n\t"
    "v_cvt_pk_bf16_f32 v6, v16, v17\n\t"
    "v_min_u32 v6, -2, v6\n\t"
    "s_mov_b64 exec, %[em]\n\t"
    "global_store_dword v7, v6, %[kd] sc1\n\t"
    "s_mov_b64 exec, -1\n\t"
    "v_lshl_add_u32 v16, v5, 3, %[wb]\n\t"
    "v_add_f32 v8, v8, v10\n\t"
    "v_add_f32 v9, v9, v11\n\t"
    "v_add_f32 v12, v12, v14\n\t"
    "v_add_f32 v13, v13, v15\n\t"
    "ds_write2st64_b64 v16, v[8:9], v[12:13] offset1:1\n\t"
    "v_and_b32 v6, 8, v5\n\t"
    "v_mul_u32_u24 v6, 528, v6\n\t"
    "v_lshl_add_u32 v5, v7, 1, v6\n\t"
    "v_add_u32 v5, %[rb], v5\n\t"
    "s_cmp_ge_u32 %[wb], 153728\n\t"
    "s_waitcnt lgkmcnt(0)\n\t"
    "s_barrier\n\t"
    "s_cbranch_scc1 8f\n\t"
    "ds_read_b64 v[16:17], v5 offset:0\n\t"
    "ds_read_b64 v[18:19], v5 offset:1024\n\t"
    "ds_read_b64 v[20:21], v5 offset:2048\n\t"
    "ds_read_b64 v[22:23], v5 offset:3072\n\t"
    "s_waitcnt lgkmcnt(2)\n\t"
    "v_pk_add_f32 v[16:17], v[16:17], v[18:19]\n\t"
    "s_waitcnt lgkmcnt(0)\n\t"
    "v_pk_add_f32 v[20:21], v[20:21], v[22:23]\n\t"
    "v_pk_add_f32 v[16:17], v[16:17], v[20:21]\n\t"
    "s_nop 1\n\t"
    "v_add_f32_dpp v16, v16, v16 row_ror:8 row_mask:0xf bank_mask:0xf\n\t"
    "v_add_f32_dpp v17, v17, v17 row_ror:8 row_mask:0xf bank_mask:0xf\n\t"
    "v_cvt_pk_bf16_f32 v6, v16, v17\n\t"
    "v_min_u32 v6, -2, v6\n\t"
    "s_mov_b64 exec, %[em]\n\t"
    "global_store_dword v7, v6, %[vd] sc1\n\t"
    "s_mov_b64 exec, -1\n\t"
    "8:\n\t"
    "s_setprio 2\n\t"
    "s_waitcnt lgkmcnt(0)\n\t"
    "s_branch 9f\n\t"
    "1:\n\t"
    "s_add_u32 %[sp], %[sp], 1\n\t"
    "s_cmp_ge_u32 %[sp], %[cap]\n\t"
    "s_cbranch_scc1 7f\n\t"
    "v_mbcnt_lo_u32_b32 v20, -1, 0\n\t"
    "v_mbcnt_hi_u32_b32 v20, -1, v20\n\t"
    "v_lshlrev_b32 v21, 4, v20\n\t"
    "s_nop 0\n\t"
    "global_load_dwordx4 v[24:27], v21, %[ag] sc1\n\t"
    "global_load_dwordx4 v[28:31], v21, %[ag] offset:1024 sc1\n\t"
    "s_waitcnt lgkmcnt(0)\n\t"
    "s_memtime %[tc]\n\t"
    "s_nop 7\n\t"
    "s_branch 3b\n\t"
    "7:\n\t"
    "s_mov_b32 %[cap], 0\n\t"
    "s_branch 2b\n\t"
    "9:"
    : [tc] "=&s"(tc), [sp] "=&s"(sp), [cap] "+s"(cap)
    : [ag] "s"(ag), [kd] "s"(kd), [vd] "s"(vd), [wl] "s"(wl), [wa] "s"(wa), [wb] "s"(wb), [ra] "s"(ra), [rb] "s"(rb), [c44] "s"(0x4444u), [em] "s"(0x00FF00FF00FF00FFull), "{v[64:95]}"(W.k0), "{v[96:127]}"(W.k1), "{v[128:159]}"(W.o0), "{v[160:191]}"(W.o1), "{v[192:223]}"(W.o2), "{v[224:255]}"(W.o3)
    : "v4", "v5", "v6", "v7", "v8", "v9", "v10", "v11", "v12", "v13", "v14", "v15", "v16", "v17", "v18", "v19", "v20", "v21", "v22", "v23", "v24", "v25", "v26", "v27", "v28", "v29", "v30", "v31", "v32", "v33", "v34", "v35", "v36", "v37", "v38", "v39", "v40", "v41", "v42", "v43", "v44", "v45", "v46", "v47", "v48", "v49", "v50", "v51", "v52", "v53", "v54", "v55", "v56", "v57", "v58", "v59", "v60", "v61", "v62", "v63", "vcc", "scc", "memory");
}
__device__ __forceinline__ void vr_r(const VRW& W, const char* os, const char* ad, unsigned wa, unsigned ra, unsigned& cap, unsigned& sp, unsigned long long* ts) {
  asm volatile("v_mbcnt_lo_u32_b32 v4, -1, 0\n\t"
    "v_mbcnt_hi_u32_b32 v4, -1, v4\n\t"
    "v_lshlrev_b32 v5, 4, v4\n\t"
    "global_load_dwordx4 v[12:15], v5, %[os] offset:0 sc1\n\t"
    "global_load_dwordx4 v[16:19], v5, %[os] offset:1024 sc1\n\t"
    "global_load_dwordx4 v[20:23], v5, %[os] offset:2048 sc1\n\t"
    "global_load_dwordx4 v[24:27], v5, %[os] offset:3072 sc1\n\t"
    "s_mov_b32 %[sp], 0\n\t"
    "v_and_b32 v10, 1, v4\n\t"
    "v_mul_u32_u24 v10, 0xaaaa, v10\n\t"
    "v_or_b32 v6, %[c44], v10\n\t"
    "v_and_b32 v10, 15, v4\n\t"
    "v_lshl_add_u32 v7, v10, 2, %[wa]\n\t"
    "v_and_b32 v10, 48, v4\n\t"
    "v_mad_u32_u24 v7, v10, 12, v7\n\t"
    "v_and_b32 v10, 32, v4\n\t"
    "v_mul_u32_u24 v8, 12, v10\n\t"
    "v_and_b32 v10, 4, v4\n\t"
    "v_mad_u32_u24 v8, v10, 48, v8\n\t"
    "v_and_b32 v10, 16, v4\n\t"
    "v_lshl_add_u32 v8, v10, 1, v8\n\t"
    "v_and_b32 v10, 3, v4\n\t"
    "v_lshl_add_u32 v8, v10, 3, v8\n\t"
    "v_and_b32 v10, 8, v4\n\t"
    "v_lshlrev_b32 v9, 12, v10\n\t"
    "v_mul_u32_u24 v10, 400, v10\n\t"
    "v_add3_u32 v8, v8, v10, %[ra]\n\t"
    "v_and_b32 v10, 48, v4\n\t"
    "v_lshl_or_b32 v9, v10, 1, v9\n\t"
    "v_and_b32 v10, 7, v4\n\t"
    "v_lshl_or_b32 v9, v10, 2, v9\n\t"
    "3:\n\t"
    "v_mov_b32 v28, 0\n\t"
    "v_mov_b32 v29, 0\n\t"
    "v_mov_b32 v30, 0\n\t"
    "v_mov_b32 v31, 0\n\t"
    "v_mov_b32 v32, 0\n\t"
    "v_mov_b32 v33, 0\n\t"
    "v_mov_b32 v34, 0\n\t"
    "v_mov_b32 v35, 0\n\t"
    "v_mov_b32 v36, 0\n\t"
    "v_mov_b32 v37, 0\n\t"
    "v_mov_b32 v38, 0\n\t"
    "v_mov_b32 v39, 0\n\t"
    "v_mov_b32 v40, 0\n\t"
    "v_mov_b32 v41, 0\n\t"
    "v_mov_b32 v42, 0\n\t"
    "v_mov_b32 v43, 0\n\t"
    "v_mov_b32 v10, 0\n\t"
    "s_waitcnt vmcnt(3)\n\t"
    "v_smfmac_f32_16x16x64_bf16 v[28:31], v[12:15], v[128:135], v6\n\t"
    "v_max3_u32 v10, v10, v12, v13\n\t"
    "v_smfmac_f32_16x16x64_bf16 v[32:35], v[12:15], v[136:143], v6\n\t"
    "v_max3_u32 v10, v10, v14, v15\n\t"
    "v_smfmac_f32_16x16x64_bf16 v[36:39], v[12:15], v[144:151], v6\n\t"
    "v_smfmac_f32_16x16x64_bf16 v[40:43], v[12:15], v[152:159], v6\n\t"
    "s_waitcnt vmcnt(2)\n\t"
    "v_smfmac_f32_16x16x64_bf16 v[28:31], v[16:19], v[160:167], v6\n\t"
    "v_max3_u32 v10, v10, v16, v17\n\t"
    "v_smfmac_f32_16x16x64_bf16 v[32:35], v[16:19], v[168:175], v6\n\t"
    "v_max3_u32 v10, v10, v18, v19\n\t"
    "v_smfmac_f32_16x16x64_bf16 v[36:39], v[16:19], v[176:183], v6\n\t"
    "v_smfmac_f32_16x16x64_bf16 v[40:43], v[16:19], v[184:191], v6\n\t"
    "s_waitcnt vmcnt(1)\n\t"
    "v_smfmac_f32_16x16x64_bf16 v[28:31], v[20:23], v[192:199], v6\n\t"
    "v_max3_u32 v10, v10, v20, v21\n\t"
    "v_smfmac_f32_16x16x64_bf16 v[32:35], v[20:23], v[200:207], v6\n\t"
    "v_max3_u32 v10, v10, v22, v23\n\t"
    "v_smfmac_f32_16x16x64_bf16 v[36:39], v[20:23], v[208:215], v6\n\t"
    "v_smfmac_f32_16x16x64_bf16 v[40:43], v[20:23], v[216:223], v6\n\t"
    "s_waitcnt vmcnt(0)\n\t"
    "v_smfmac_f32_16x16x64_bf16 v[28:31], v[24:27], v[224:231], v6\n\t"
    "v_max3_u32 v10, v10, v24, v25\n\t"
    "v_smfmac_f32_16x16x64_bf16 v[32:35], v[24:27], v[232:239], v6\n\t"
    "v_max3_u32 v10, v10, v26, v27\n\t"
    "v_smfmac_f32_16x16x64_bf16 v[36:39], v[24:27], v[240:247], v6\n\t"
    "v_cmp_eq_u32 vcc, -1, v10\n\t"
    "v_smfmac_f32_16x16x64_bf16 v[40:43], v[24:27], v[248:255], v6\n\t"
    "s_cbranch_vccnz 4f\n\t"
    "5:\n\t"
    "s_cmp_ge_u32 %[wa], 1536\n\t"
    "s_nop 1\n\t"
    "v_add_f32 v28, v28, v29\n\t"
    "v_add_f32 v32, v32, v33\n\t"
    "v_add_f32 v36, v38, v39\n\t"
    "v_add_f32 v28, v28, v36\n\t"
    "v_add_f32 v40, v42, v43\n\t"
    "v_add_f32 v32, v32, v40\n\t"
    "ds_write2_b32 v7, v28, v32 offset1:16\n\t"
    "s_waitcnt lgkmcnt(0)\n\t"
    "s_barrier\n\t"
    "s_cbranch_scc1 9f\n\t"
    "ds_read_b64 v[44:45], v8 offset:0\n\t"
    "ds_read_b64 v[46:47], v8 offset:768\n\t"
    "ds_read_b64 v[48:49], v8 offset:1536\n\t"
    "ds_read_b64 v[50:51], v8 offset:2304\n\t"
    "s_waitcnt lgkmcnt(2)\n\t"
    "v_pk_add_f32 v[44:45], v[44:45], v[46:47]\n\t"
    "s_waitcnt lgkmcnt(0)\n\t"
    "v_pk_add_f32 v[48:49], v[48:49], v[50:51]\n\t"
    "v_pk_add_f32 v[44:45], v[44:45], v[48:49]\n\t"
    "s_nop 1\n\t"
    "v_add_f32_dpp v44, v44, v44 row_ror:8 row_mask:0xf bank_mask:0xf\n\t"
    "v_add_f32_dpp v45, v45, v45 row_ror:8 row_mask:0xf bank_mask:0xf\n\t"
    "v_cvt_pk_bf16_f32 v10, v44, v45\n\t"
    "v_min_u32 v10, -2, v10\n\t"
    "global_store_dword v9, v10, %[ad] sc1\n\t"
    "s_waitcnt lgkmcnt(0)\n\t"
    "s_branch 9f\n\t"
    "4:\n\t"
    "s_add_u32 %[sp], %[sp], 1\n\t"
    "s_cmp_ge_u32 %[sp], %[cap]\n\t"
    "s_cbranch_scc1 7f\n\t"
    "global_load_dwordx4 v[12:15], v5, %[os] offset:0 sc1\n\t"
    "global_load_dwordx4 v[16:19], v5, %[os] offset:1024 sc1\n\t"
    "global_load_dwordx4 v[20:23], v5, %[os] offset:2048 sc1\n\t"
    "global_load_dwordx4 v[24:27], v5, %[os] offset:3072 sc1\n\t"
    "s_nop 3\n\t"
    "s_branch 3b\n\t"
    "7:\n\t"
    "s_mov_b32 %[cap], 0\n\t"
    "s_branch 5b\n\t"
    "9:"
    : [sp] "=&s"(sp), [cap] "+s"(cap)
    : [os] "s"(os), [ad] "s"(ad), [wa] "s"(wa), [ra] "s"(ra), [c44] "s"(0x4444u), "{v[64:95]}"(W.k0), "{v[96:127]}"(W.k1), "{v[128:159]}"(W.o0), "{v[160:191]}"(W.o1), "{v[192:223]}"(W.o2), "{v[224:255]}"(W.o3)
    : "v4", "v5", "v6", "v7", "v8", "v9", "v10", "v11", "v12", "v13", "v14", "v15", "v16", "v17", "v18", "v19", "v20", "v21", "v22", "v23", "v24", "v25", "v26", "v27", "v28", "v29", "v30", "v31", "v32", "v33", "v34", "v35", "v36", "v37", "v38", "v39", "v40", "v41", "v42", "v43", "v44", "v45", "v46", "v47", "v48", "v49", "v50", "v51", "v52", "v53", "v54", "v55", "v56", "v57", "v58", "v59", "vcc", "scc", "memory");
}
__device__ __forceinline__ void kr_load_k0(VRW& W, unsigned a) {
  asm volatile("ds_read_b64_tr_b16 v[64:65], %[a] offset:0\n\t"
    "ds_read_b64_tr_b16 v[66:67], %[a] offset:256\n\t"
    "ds_read_b64_tr_b16 v[68:69], %[a] offset:4096\n\t"
    "ds_read_b64_tr_b16 v[70:71], %[a] offset:4352\n\t"
    "ds_read_b64_tr_b16 v[80:81], %[a] offset:32\n\t"
    "ds_read_b64_tr_b16 v[82:83], %[a] offset:288\n\t"
    "ds_read_b64_tr_b16 v[84:85], %[a] offset:4128\n\t"
    "ds_read_b64_tr_b16 v[86:87], %[a] offset:4384\n\t"
    "s_waitcnt lgkmcnt(0)\n\t"
    "ds_read_b64_tr_b16 v[96:97], %[a] offset:64\n\t"
    "ds_read_b64_tr_b16 v[98:99], %[a] offset:320\n\t"
    "ds_read_b64_tr_b16 v[100:101], %[a] offset:4160\n\t"
    "ds_read_b64_tr_b16 v[102:103], %[a] offset:4416\n\t"
    "ds_read_b64_tr_b16 v[112:113], %[a] offset:96\n\t"
    "ds_read_b64_tr_b16 v[114:115], %[a] offset:352\n\t"
    "ds_read_b64_tr_b16 v[116:117], %[a] offset:4192\n\t"
    "ds_read_b64_tr_b16 v[118:119], %[a] offset:4448\n\t"
    "s_waitcnt lgkmcnt(0)"
    : "=&{v[64:95]}"(W.k0), "=&{v[96:127]}"(W.k1) : [a] "v"(a) : "memory");
}
__device__ __forceinline__ void kr_load_k1(VRW& W, unsigned a) {
  asm volatile("ds_read_b64_tr_b16 v[72:73], %[a] offset:0\n\t"
    "ds_read_b64_tr_b16 v[74:75], %[a] offset:256\n\t"
    "ds_read_b64_tr_b16 v[76:77], %[a] offset:4096\n\t"
    "ds_read_b64_tr_b16 v[78:79], %[a] offset:4352\n\t"
    "ds_read_b64_tr_b16 v[88:89], %[a] offset:32\n\t"
    "ds_read_b64_tr_b16 v[90:91], %[a] offset:288\n\t"
    "ds_read_b64_tr_b16 v[92:93], %[a] offset:4128\n\t"
    "ds_read_b64_tr_b16 v[94:95], %[a] offset:4384\n\t"
    "s_waitcnt lgkmcnt(0)\n\t"
    "ds_read_b64_tr_b16 v[104:105], %[a] offset:64\n\t"
    "ds_read_b64_tr_b16 v[106:107], %[a] offset:320\n\t"
    "ds_read_b64_tr_b16 v[108:109], %[a] offset:4160\n\t"
    "ds_read_b64_tr_b16 v[110:111], %[a] offset:4416\n\t"
    "ds_read_b64_tr_b16 v[120:121], %[a] offset:96\n\t"
    "ds_read_b64_tr_b16 v[122:123], %[a] offset:352\n\t"
    "ds_read_b64_tr_b16 v[124:125], %[a] offset:4192\n\t"
    "ds_read_b64_tr_b16 v[126:127], %[a] offset:4448\n\t"
    "s_waitcnt lgkmcnt(0)"
    : "+&{v[64:95]}"(W.k0), "+&{v[96:127]}"(W.k1) : [a] "v"(a) : "memory");
}
__device__ __forceinline__ void kr_load_o0(VRW& W, unsigned a) {
  asm volatile("ds_read_b64_tr_b16 v[128:129], %[a] offset:0\n\t"
    "ds_read_b64_tr_b16 v[130:131], %[a] offset:128\n\t"
    "ds_read_b64_tr_b16 v[132:133], %[a] offset:1024\n\t"
    "ds_read_b64_tr_b16 v[134:135], %[a] offset:1152\n\t"
    "ds_read_b64_tr_b16 v[136:137], %[a] offset:32\n\t"
    "ds_read_b64_tr_b16 v[138:139], %[a] offset:160\n\t"
    "ds_read_b64_tr_b16 v[140:141], %[a] offset:1056\n\t"
    "ds_read_b64_tr_b16 v[142:143], %[a] offset:1184\n\t"
    "s_waitcnt lgkmcnt(0)\n\t"
    "ds_read_b64_tr_b16 v[144:145], %[a] offset:4096\n\t"
    "ds_read_b64_tr_b16 v[146:147], %[a] offset:4224\n\t"
    "ds_read_b64_tr_b16 v[148:149], %[a] offset:5120\n\t"
    "ds_read_b64_tr_b16 v[150:151], %[a] offset:5248\n\t"
    "ds_read_b64_tr_b16 v[152:153], %[a] offset:4128\n\t"
    "ds_read_b64_tr_b16 v[154:155], %[a] offset:4256\n\t"
    "ds_read_b64_tr_b16 v[156:157], %[a] offset:5152\n\t"
    "ds_read_b64_tr_b16 v[158:159], %[a] offset:5280\n\t"
    "s_waitcnt lgkmcnt(0)"
    : "=&{v[128:159]}"(W.o0) : [a] "v"(a) : "memory");
}
__device__ __forceinline__ void kr_load_o1(VRW& W, unsigned a) {
  asm volatile("ds_read_b64_tr_b16 v[160:161], %[a] offset:0\n\t"
    "ds_read_b64_tr_b16 v[162:163], %[a] offset:128\n\t"
    "ds_read_b64_tr_b16 v[164:165], %[a] offset:1024\n\t"
    "ds_read_b64_tr_b16 v[166:167], %[a] offset:1152\n\t"
    "ds_read_b64_tr_b16 v[168:169], %[a] offset:32\n\t"
    "ds_read_b64_tr_b16 v[170:171], %[a] offset:160\n\t"
    "ds_read_b64_tr_b16 v[172:173], %[a] offset:1056\n\t"
    "ds_read_b64_tr_b16 v[174:175], %[a] offset:1184\n\t"
    "s_waitcnt lgkmcnt(0)\n\t"
    "ds_read_b64_tr_b16 v[176:177], %[a] offset:4096\n\t"
    "ds_read_b64_tr_b16 v[178:179], %[a] offset:4224\n\t"
    "ds_read_b64_tr_b16 v[180:181], %[a] offset:5120\n\t"
    "ds_read_b64_tr_b16 v[182:183], %[a] offset:5248\n\t"
    "ds_read_b64_tr_b16 v[184:185], %[a] offset:4128\n\t"
    "ds_read_b64_tr_b16 v[186:187], %[a] offset:4256\n\t"
    "ds_read_b64_tr_b16 v[188:189], %[a] offset:5152\n\t"
    "ds_read_b64_tr_b16 v[190:191], %[a] offset:5280\n\t"
    "s_waitcnt lgkmcnt(0)"
    : "=&{v[160:191]}"(W.o1) : [a] "v"(a) : "memory");
}
__device__ __forceinline__ void kr_load_o2(VRW& W, unsigned a) {
  asm volatile("ds_read_b64_tr_b16 v[192:193], %[a] offset:0\n\t"
    "ds_read_b64_tr_b16 v[194:195], %[a] offset:128\n\t"
    "ds_read_b64_tr_b16 v[196:197], %[a] offset:1024\n\t"
    "ds_read_b64_tr_b16 v[198:199], %[a] offset:1152\n\t"
    "ds_read_b64_tr_b16 v[200:201], %[a] offset:32\n\t"
    "ds_read_b64_tr_b16 v[202:203], %[a] offset:160\n\t"
    "ds_read_b64_tr_b16 v[204:205], %[a] offset:1056\n\t"
    "ds_read_b64_tr_b16 v[206:207], %[a] offset:1184\n\t"
    "s_waitcnt lgkmcnt(0)\n\t"
    "ds_read_b64_tr_b16 v[208:209], %[a] offset:4096\n\t"
    "ds_read_b64_tr_b16 v[210:211], %[a] offset:4224\n\t"
    "ds_read_b64_tr_b16 v[212:213], %[a] offset:5120\n\t"
    "ds_read_b64_tr_b16 v[214:215], %[a] offset:5248\n\t"
    "ds_read_b64_tr_b16 v[216:217], %[a] offset:4128\n\t"
    "ds_read_b64_tr_b16 v[218:219], %[a] offset:4256\n\t"
    "ds_read_b64_tr_b16 v[220:221], %[a] offset:5152\n\t"
    "ds_read_b64_tr_b16 v[222:223], %[a] offset:5280\n\t"
    "s_waitcnt lgkmcnt(0)"
    : "=&{v[192:223]}"(W.o2) : [a] "v"(a) : "memory");
}
__device__ __forceinline__ void kr_load_o3(VRW& W, unsigned a) {
  asm volatile("ds_read_b64_tr_b16 v[224:225], %[a] offset:0\n\t"
    "ds_read_b64_tr_b16 v[226:227], %[a] offset:128\n\t"
    "ds_read_b64_tr_b16 v[228:229], %[a] offset:1024\n\t"
    "ds_read_b64_tr_b16 v[230:231], %[a] offset:1152\n\t"
    "ds_read_b64_tr_b16 v[232:233], %[a] offset:32\n\t"
    "ds_read_b64_tr_b16 v[234:235], %[a] offset:160\n\t"
    "ds_read_b64_tr_b16 v[236:237], %[a] offset:1056\n\t"
    "ds_read_b64_tr_b16 v[238:239], %[a] offset:1184\n\t"
    "s_waitcnt lgkmcnt(0)\n\t"
    "ds_read_b64_tr_b16 v[240:241], %[a] offset:4096\n\t"
    "ds_read_b64_tr_b16 v[242:243], %[a] offset:4224\n\t"
    "ds_read_b64_tr_b16 v[244:245], %[a] offset:5120\n\t"
    "ds_read_b64_tr_b16 v[246:247], %[a] offset:5248\n\t"
    "ds_read_b64_tr_b16 v[248:249], %[a] offset:4128\n\t"
    "ds_read_b64_tr_b16 v[250:251], %[a] offset:4256\n\t"
    "ds_read_b64_tr_b16 v[252:253], %[a] offset:5152\n\t"
    "ds_read_b64_tr_b16 v[254:255], %[a] offset:5280\n\t"
    "s_waitcnt lgkmcnt(0)"
    : "=&{v[224:255]}"(W.o3) : [a] "v"(a) : "memory");
}
#define XPTRS(l) const unsigned par_ = ini_resolve(ws, l, tag); char* xch_ = xch_base(ws); \
  const char* xb = uni(xch_ + ((size_t)3 << 21) + (((par_ >> 1) & 1) << 13)); const char* ob = uni(xch_ + ((par_ & 1) << 13)); \
  unsigned* cnt = (unsigned*)(xch_ + ((size_t)4 << 21) + ((par_ >> 2) << 13))
__device__ __forceinline__ void qk_body(char* smem, int wave_id_, int xg, int h, int wg, const u16* __restrict__ Wq, const u16* __restrict__ Wv,
                                        char* ws, unsigned* tmo, unsigned long long tag) {
  const int wave = WAVE_ID;
  const int ty = wave >> 2, w4 = wave & 3;
  const char* wq = uni((const char*)(Wq + h * DH)); const char* wv = uni((const char*)(Wv + h * DH));
  auto stage2 = [&](int i) {
    const int tid = wave * 64 + lane_id();
    if (i < 6) {
      stage_proj<false>(wq + (size_t)i * 128 * HD * 2, Q_RING + ((2 * i) & 3) * 32768, tid, wave);
      if (i < 2) stage_proj<false>(wq + (size_t)(i + 6) * 128 * HD * 2, Q_RING + ((2 * i + 1) & 3) * 32768, tid, wave);
      else stage_proj<true>(wv + (size_t)(i - 2) * 256 * HD * 2, Q_RING + ((2 * i + 1) & 3) * 32768, tid, wave);
    }
  };
  QKW W;
  delay64<QDL>();
  stage2(0); stage2(1);
#define WLD(i, w) do { if ((i) < 5) WAIT_V(8); else WAIT_V(0); \
    WBAR(); { const int l_ = lane_id(); const unsigned o_ = Q_RING + ty * 32768 + ((i) & 1) * 65536; const bool v_ = ty && (i) >= 2; \
      qk_wload<i>(w, proj_base(v_ ? w4 : 2 * w4, l_) + o_, proj_base(v_ ? w4 + 4 : 2 * w4 + 1, l_) + o_); } \
    WBAR(); stage2((i) + 2); if (wave == 0) STS(ws, wg, 1 + (i)); if ((i) == 4 && wave == 7) ini_request(ws, 30720); } while (0)
  WLD(0, W.w0); WLD(1, W.w1); WLD(2, W.w2); WLD(3, W.w3); WLD(4, W.w4); WLD(5, W.w5);
#undef WLD
  XPTRS(smem + 30720);
  if (opaque_tid() == 64) st4_sc1(cnt + (PQKV + h) * 32 + 3, 1u);
  const char* oh = ob + (size_t)(h >> 2) * 16384 + (h & 3) * 1024;
  auto rearm = [&](int ps) {
    unsigned z = SENT; asm volatile("" : "+v"(z));
    const unsigned l16 = lane_id() * 16;
#pragma unroll
    for (int r = 0; r < 2 * OREP; ++r) st16s(oh + (size_t)(ps * 4 + (r >> 1)) * 131072, (r & 1) * 4096u + l16, u32x4{z, z, z, z});
  };
  const char* dbw = xb + (size_t)48 * 16384 + 4096 + h * 128;
  if (wave == 4) *(volatile LAS u32x4*)(smem + Q_TM) = u32x4{0u, 0u, 0u, (unsigned)__builtin_amdgcn_s_memrealtime()};
  if (wave == 6) *(u32x4*)(smem + Q_PQ + 4096 + lane_id() * 16) = u32x4{0u, 0u, 0u, 0u};
  BAR();
  if (wave == 0) STS(ws, wg, 0);
  unsigned avail = 0;
  auto kvq = [&](int s) { return ws + (wave == 5 ? OFF_K : (wave == 6 ? OFF_V : OFF_Q)) + ((size_t)s * NH + h) * 8192; };
  auto stage_kv = [&](int s, int buf) {
    if (avail <= (unsigned)s) {
      for (int spins = 0;; ++spins) {
        avail = qkv_avail<0>(cnt + (PQKV + h) * 32);
        if (avail > (unsigned)s) break;
        __builtin_amdgcn_s_sleep(8);
        if (give_up(spins, tmo, 5)) break;
      }
    }
    const unsigned lane = lane_id(), v0 = (lane ^ ((lane >> 4) & 2)) * 16;
    const char* src = uni(kvq(s));
#pragma unroll
    for (int i = 0; i < 4; ++i) glds16x2s<0>(src + i * 2048, v0, Q_K + buf * 32768 + (wave - 5) * 8192 + i * 2048);
  };
  auto loop = [&](auto w_c) __attribute__((always_inline)) {
    constexpr int WV = decltype(w_c)::value;
    if (WV >= 5) stage_kv(0, 0);
    unsigned t0 = 0, cap = 60000, d0 = 105, d1 = 200;
    int dq = 0, e0 = D_Q, e1 = D_Q;
    unsigned tp = 0;
    int sl = 0;
    unsigned fc = 0;
    for (int s = 0; s < NSEG; ++s) {
      TSDECL;
      const char* ag = xb + (size_t)((sl * 4 + (AREP == 4 ? xg : xg >> 1)) * 2 + (WV >> 2)) * 16384 + (WV < 4 ? WV * 2048 : WV * 1024);
      const int nl = sl == 2 ? 0 : sl + 1;
      const unsigned pb = sl * 32768;
      const char* vs = WV == 4 ? dbw : WV == 7 ? (const char*)(cnt + (PROG + h) * 32) : xb + (size_t)(24 + sl * 8 + (h >> 2)) * 16384 + (h & 3) * 2048;
      const char* v2 = xb + (size_t)(52 + sl * 4 + (h >> 3)) * 16384 + (h & 7) * 1024 + (WV & 1) * 512;
      const char* od = oh + (size_t)sl * 524288;
      const unsigned sn = (unsigned)(s + 1) << 24;
      if (s > 0) wait_rt(t0, dq);
      TS(0);
      unsigned long long tc; unsigned sp;
      qk_step<WV>(W, ag, vs, v2, dbw, od, pb, sn, cap, sp, tc, nullptr, d0, d1);
      dq = (sp & 0xFFFFu) ? min(dq + DQU, DQM) : max(dq - DQD, 0);
      if (WV >= 5) {
        if (WV == 7) WAIT_V(0);
        if (RAE && WV == RAW && s > 0 && s < NSEG - 1) rearm(sl == 0 ? 2 : sl - 1);
        WBAR();
        delay64<E_S>();
        if (s + 1 < NSEG) stage_kv(s + 1, nl);
      }
      if (!RAE && WV == RAW && s > 0 && s < NSEG - 1) rearm(sl == 0 ? 2 : sl - 1);
      unsigned long long* tsp = nullptr;
      seg_attn<WV>(W, pb, ws + OFF_O + ((size_t)s * 64 + 2 * h + (WV >> 1)) * 4096, tsp);
      if (WV == 1) TSFLUSH(0x8000);
      if (WV < 4 && s >= SPUB) {
        WAIT_V(0);
        if (lane_id() == 0 && (__hip_atomic_fetch_add((LAS unsigned*)(smem + Q_TM + 8), 1u, __ATOMIC_RELAXED, __HIP_MEMORY_SCOPE_WORKGROUP) & 3u) == 3u)
          st4_sc1(cnt + (PROG + h) * 32, (unsigned)(s + 1));
      }
      if (s < NSEG - 1) {
        if (WV == 4) {
          delay64<E_B>();
          u32x2 v;
          for (unsigned spins = 0;; ++spins) {
            const int lane = lane_id();
            asm volatile("s_nop 4\n\tglobal_load_dwordx2 %0, %1, %2 sc1\n\ts_waitcnt vmcnt(0)" : "=&v"(v) : "v"((lane & 31) * 128 + (lane >> 5) * 8), "s"(xb + (size_t)(48 + xg) * 16384 + 4096) : "memory");
            if (__all((v.x >> 24) > (unsigned)s) || spins >= cap) break;
          }
          const unsigned now = (unsigned)__builtin_amdgcn_s_memrealtime();
          const unsigned age = rowmin16((now - v.x) & 0xFFFFFFu);
          const unsigned tb = __builtin_amdgcn_readfirstlane(now - min(min(min(rdl(age, 0), rdl(age, 16)), min(rdl(age, 32), rdl(age, 48))), 500u));
          t0 = tb + min(e0, e1);
          *(volatile LAS u32x2*)(smem + Q_TM) = u32x2{(unsigned)(s + 1), t0};
          if (ADQ && s > 1 && __all((v.y >> 24) == (unsigned)s)) {
            const unsigned ag2 = rowmin16((now - v.y) & 0xFFFFFFu);
            e1 = e0;
            e0 = __builtin_amdgcn_readfirstlane(min(max((int)(now - min(min(rdl(ag2, 0), rdl(ag2, 16)), min(rdl(ag2, 32), rdl(ag2, 48))) - tp) + D_V, D_Q - DQM), D_Q + DQM));
          }
          tp = tb;
          asm volatile("" : "+s"(t0), "+s"(e0), "+s"(e1), "+s"(tp));
          TS(14);
          TSFLUSH(0x4000);
        } else {
          for (;;) {
            const u32x2 v = *(volatile LAS u32x2*)(smem + Q_TM);
            t0 = __builtin_amdgcn_readfirstlane(v.y);
            if (__builtin_amdgcn_readfirstlane(v.x) == (unsigned)(s + 1)) break;
            __builtin_amdgcn_s_sleep(1);
          }
        }
      }
      sl = sl == 2 ? 0 : sl + 1;
    }
    if (cap == 0 && lane_id() == 0) st4_sc1(tmo, 1u);
    FCS();
  };
  switch (wave) {
    case 0: loop(std::integral_constant<int, 0>{}); break;
    case 1: loop(std::integral_constant<int, 1>{}); break;
    case 2: loop(std::integral_constant<int, 2>{}); break;
    case 3: loop(std::integral_constant<int, 3>{}); break;
    case 4: loop(std::integral_constant<int, 4>{}); break;
    case 5: loop(std::integral_constant<int, 5>{}); break;
    case 6: loop(std::integral_constant<int, 6>{}); break;
    default: loop(std::integral_constant<int, 7>{}); break;
  }
  __builtin_amdgcn_s_setprio(0);
}

__device__ __forceinline__ void vr_body(char* smem, int wave_id_, int xg, int h, int c, int wg, const u16* __restrict__ Wk, const u16* __restrict__ Wv,
                                        const u16* __restrict__ Wo, char* ws, unsigned* tmo, unsigned long long tag) {
  const int wave = WAVE_ID;
  const unsigned po = (unsigned)(h >> 4) * 16384u + ((h >> 1) & 7) * 1024 + (h & 1) * 512 + c * 128;
  const unsigned ko_ = (unsigned)(24 + (h >> 2)) * 16384u + (h & 3) * 2048 + c * 1024;
  const unsigned vo_ = (unsigned)(52 + (h >> 3)) * 16384u + (h & 7) * 1024 + c * 512;
  const int hg = (wave + ROTW * ((2 * h + c) & 3)) & 7;
  VRW W;
  {
    const int lane = lane_id();
    const int g = lane >> 4, q4 = (lane & 15) >> 2, p4 = lane & 3, fr = 4 * g + (q4 & 1) + 32 * (q4 >> 1);
    const unsigned lw = wave * 16384;
    const char* sk = uni((const char*)(Wk + (size_t)(128 * wave) * HD + h * DH + 64 * c));
    const char* sv_ = uni((const char*)(Wv + (size_t)(128 * wave) * HD + h * DH + 64 + 32 * c));
    const char* so = uni((const char*)(Wo + (size_t)(512 * hg) * DM + 32 * h));
    const int kp = lane >> 2, sr = 32 * (kp >> 3) + 16 * ((kp >> 2) & 1) + (kp & 3);
    auto issue = [&](int p) {
#pragma unroll
      for (int i = 0; i < 8; ++i) {
        const unsigned d = lw + (p & 1) * 8192 + i * 1024;
        if (p < 2) gldsw(sk + (size_t)(8 * p + i) * 8 * HD * 2, (unsigned)((lane >> 3) * HD * 2 + (lane & 7) * 16), d);
        else if (p < 6 && (NTM & 8)) gldsw(so + (size_t)(128 * (p - 2) + 64 * (i & 1) + 4 * (i >> 1)) * DM * 2, (unsigned)(sr * DM * 2 + p4 * 16), d);
        else if (p < 6) glds16s<0>(so + (size_t)(128 * (p - 2) + 64 * (i & 1) + 4 * (i >> 1)) * DM * 2, (unsigned)(sr * DM * 2 + p4 * 16), d);
        else if (NTM & 16) gldsw(sv_ + (size_t)i * 16 * HD * 2, (unsigned)(kp * HD * 2 + p4 * 16), d);
        else glds16s<0>(sv_ + (size_t)i * 16 * HD * 2, (unsigned)(kp * HD * 2 + p4 * 16), d);
      }
    };
#define VTS(p) do { } while (0)
    const int fm = 16 * (g >> 1) + 4 * (g & 1) + (q4 & 1) + 8 * (q4 >> 1);
    const unsigned ak = lw + fm * 128 + p4 * 8, ao = lw + fr * 64 + p4 * 8;
#define KB() do { if (KFB) WBAR(); } while (0)
    issue(2); KB(); issue(3);
    WAIT_V(8); VTS(1); kr_load_o0(W, ao); KB(); issue(4);
    WAIT_V(8); VTS(2); kr_load_o1(W, ao + 8192); KB(); issue(5);
    WAIT_V(8); VTS(3); kr_load_o2(W, ao); KB(); issue(0);
    WAIT_V(8); VTS(4); kr_load_o3(W, ao + 8192); KB(); issue(1);
    WAIT_V(8); VTS(5); kr_load_k0(W, ak); KB(); issue(6);
    WAIT_V(8); VTS(6); kr_load_k1(W, ak + 8192);
    if (wave == 0) ini_request(ws, V_PV);
    WAIT_V(0);
    u32x8 x[4];
#pragma unroll
    for (int j = 0; j < 4; ++j) {
      const unsigned a = lw + fm * 64 + p4 * 8 + (j & 1) * 4096 + (j >> 1) * 32;
      const u32x2 t0 = trread(smem, a), t1 = trread(smem, a + 128), t2 = trread(smem, a + 2048), t3 = trread(smem, a + 2176);
      x[j] = u32x8{t0.x, t0.y, t1.x, t1.y, t2.x, t2.y, t3.x, t3.y};
    }
    char* d = smem + lw + 8192 + lane * 16;
#pragma unroll
    for (int j = 0; j < 4; ++j) { *(u32x4*)(d + j * 2048) = u32x4{x[j][0], x[j][1], x[j][2], x[j][3]}; *(u32x4*)(d + j * 2048 + 1024) = u32x4{x[j][4], x[j][5], x[j][6], x[j][7]}; }
    VTS(7);
  }
  BAR();
  XPTRS(smem + V_PV);
  (void)cnt;
  auto rearm = [&](int ps) {
    const int lane = lane_id();
    unsigned z = SENT; asm volatile("" : "+v"(z));
    const u32x4 sv = u32x4{z, z, z, z};
    static_assert(AREP == 2, "");
    st16s(xb, lane < 32 ? (unsigned)(ps * 8 + (lane >> 4) * 2) * 16384u + po + ((lane >> 3) & 1) * 256 + (lane & 7) * 16 : (unsigned)ps * 65536u + vo_ + (lane & 31) * 16, sv);
    st16s(xb, (unsigned)ps * 131072u + ko_ + lane * 16, sv);
  };
  if (wave == 0) STS(ws, wg, 0);
  __builtin_amdgcn_s_setprio(2);
  {
    unsigned cap = 60000, tr = 0, fc = 0, tm = 0;
    int da = D_A, dd = D_O;
    int sl = 0;
    for (int s = 0; s < NSEG; ++s) {
      TSDECL;
      const int sn = sl == 2 ? 0 : sl + 1;
      const char* ag = xb + (size_t)((sl * 4 + (AREP == 4 ? xg : xg >> 1)) * 2 + (wave >> 2)) * 16384 + (wave & 3) * 2048;
      const char* kd = xb + (size_t)sl * 131072 + ko_ + wave * 128;
      const char* vd = xb + (size_t)sl * 65536 + vo_ + (wave & 3) * 128;
      const char* os = ob + (size_t)((sl * 4 + ORSEL) * 8 + hg) * 16384 + c * 4096;
      const char* ad = xb + (size_t)(sn * 8) * 16384 + po + (wave & 1) * 256;
      if (s > 0) { if (wave < AREP) delay64<E_AG - E_W>(); else delay64<E_AG>(); }
      TS(0);
      unsigned long long tc; unsigned sp;
      unsigned long long* tsp = nullptr;
      vr_v(W, ag, kd, vd, wave * 16384 + 8192, V_P1 + wave * 2304 + (wave >> 2) * 64, V_PV + wave * 1024 + (wave >> 2) * 128, V_P1 + (wave >> 1) * 32 + (wave & 1) * 1152,
           V_PV + (wave & 3) * 256, cap, sp, tc, tsp);
      TN(12, sp + 1);
      if (s > 3 && sp) fc += 1u;
      da = sp ? min(da + DQU, D_A + DQM) : max(da - DQD, D_A);
      if (s == NSEG - 1) break;
      if (ADQ && wave == 0 && s > 0) { const int l_ = lane_id(); if (l_ < 3) st4s(xb, (unsigned)(48 + l_) * 16384u + 4096 + h * 128 + c * 8 + 4, (tm & 0xFFFFFFu) | (unsigned)s << 24); }
      if (wave == 7 && s > 0) rearm(sl == 0 ? 2 : sl - 1);
      wait_clk((unsigned)tc, E_O);
      TS(3);
      const unsigned tb = bell_time(xb + (size_t)(48 + xg) * 16384 + hg * 512, (lane_id() & 3) * 128, (unsigned)(s + 1), cap);
      tr = tb;
      wait_rt(tb, dd);
      TS(4);
      vr_r(W, os, ad, V_P2 + wave * 768 + (wave >> 2) * 128, V_P2 + (wave & 1) * 64, cap, sp, tsp);
      if (ADQ && wave == 0) tm = (unsigned)__builtin_amdgcn_s_memrealtime();
      if (s > 3 && sp) fc += 0x10000u;
      dd = sp ? min(dd + DOU, D_O + DOM) : max(dd - DQD, D_O);
      TN(14, (sp & 0xFFFF) + 1);
      if (wave == 1) TSFLUSH(0x6078);
      sl = sn;
    }
    if (cap == 0 && lane_id() == 0) st4_sc1(tmo, 2u);
    FCS();
  }
  __builtin_amdgcn_s_setprio(0);
}
#define NCS 6
__device__ __forceinline__ int calibrate(char* ws, int i) {
  const int t = lane_id(), ci = (i & 15) % 3, w = (i >> 4) * 4 + (i & 15) / 3, chunk = ci == 0 ? 0 : ci + 2;
  char* base = xch_base(ws) + ((size_t)chunk << 21) + 127 * 16384 + w * 512;
  const char* sb[2] = {uni(base + (w & 1) * 8192), uni(base + (~w & 1) * 8192)};
  if ((t & 31) < 24) st16_sc1(base + (t >> 5) * 8192 + (t & 31) * 16, u32x4{SENT, SENT, SENT, SENT});
  unsigned v, best[2] = {1u << 30, 1u << 30};
  asm volatile("s_load_dword %0, %1, 384 glc\n\ts_waitcnt vmcnt(0) lgkmcnt(0)" : "=&s"(v) : "s"(sb[0]) : "memory");
#pragma unroll
  for (int r = 0; r < NCS; ++r) {
    unsigned long long t0, t1;
    asm volatile("s_memtime %0\n\ts_waitcnt lgkmcnt(0)\n\ts_load_dword %2, %3, %4 glc\n\ts_waitcnt lgkmcnt(0)\n\ts_memtime %1\n\ts_waitcnt lgkmcnt(0)"
                 : "=&s"(t0), "=&s"(t1), "=&s"(v) : "s"(sb[r & 1]), "i"((r >> 1) * 128) : "memory");
    best[r & 1] = min(best[r & 1], (unsigned)(t1 - t0));
  }
  return (w & 1) ? (int)best[0] - (int)best[1] : (int)best[1] - (int)best[0];
}
__device__ __forceinline__ void init_sums(char* smem, int t, int i, const u16* __restrict__ cvec, const u16* __restrict__ wmod) {
  const int cg = t & 15, r0 = t >> 4, k0 = (i >> 4) * 256 + r0 * 16;
  u32x4 w[16];
#pragma unroll
  for (int j = 0; j < 16; ++j) w[j] = ldnt<1>(wmod + (size_t)(k0 + j) * (6 * DM) + (i & 15) * 128 + cg * 8);
  const u32x4 c0 = *(const u32x4*)(cvec + k0), c1 = *(const u32x4*)(cvec + k0 + 8);
  f32x2 acc[4] = {f32x2{0, 0}, f32x2{0, 0}, f32x2{0, 0}, f32x2{0, 0}};
#pragma unroll
  for (int j = 0; j < 16; ++j) {
    const unsigned cw = j < 8 ? c0[(j >> 1) & 3] : c1[(j >> 1) & 3];
    const float c = (j & 1) ? bfhi(cw) : bflo(cw), sv = bfr(c / (1.0f + __expf(-c)));
#pragma unroll
    for (int e = 0; e < 4; ++e) acc[e] = pkfma(f32x2{sv, sv}, f32x2{bflo(w[j][e]), bfhi(w[j][e])}, acc[e]);
  }
  float* red = (float*)smem;
  *(f32x4*)(red + r0 * 128 + cg * 8) = f32x4{acc[0][0], acc[0][1], acc[1][0], acc[1][1]};
  *(f32x4*)(red + r0 * 128 + cg * 8 + 4) = f32x4{acc[2][0], acc[2][1], acc[3][0], acc[3][1]};
}
__device__ __forceinline__ void init_role(char* smem, int wave_id_, int i, const u16* __restrict__ mem0, char* ws, unsigned long long tag, unsigned vote) {
  const int t = opaque_tid();
  char* xch = xch_base(ws);
  if (t < 128) {
    const float* red = (const float*)smem;
    float s = 0;
#pragma unroll
    for (int r = 0; r < 16; ++r) s += red[r * 128 + t];
    st4_sc1(ws + OFF_MOD + ((i >> 4) * (2 * DM) + (i & 15) * 128 + t) * 4, __builtin_bit_cast(unsigned, s));
  }
  static_assert(OREP == 2 && AREP == 2, "");
  for (int b = i; b < 105; b += 64) {
    if (b < 101) {
      char* p = b < 48 ? xch + (size_t)((b >> 4) * 32 + (b & 15)) * 16384
              : (b < 56 ? xch + ((size_t)3 << 21) + (size_t)(8 + ((b - 48) >> 2) * 8 + (b & 3)) * 16384
              : (b < 96 ? xch + ((size_t)3 << 21) + (size_t)(b - 56 + 24) * 16384 : xch + ((size_t)4 << 21) + (size_t)(b - 96) * 16384));
      const unsigned v = b < 80 || (b >= 84 && b < 96) ? SENT : 0u;
#pragma unroll
      for (int j = 0; j < 2; ++j) st16_sc1(p + j * 8192 + t * 16, u32x4{v, v, v, v});
    } else {
      const int rep = (b - 101) & 1, par = (b - 101) >> 1;
      if (b == 101 && t == 0) st4_sc1(ws + OFF_TMO, 0u);
#pragma unroll
      for (int j = 0; j < 2; ++j) {
        const int u = t + 512 * j, lane = u & 63, g = lane >> 4, hf = (lane >> 1) & 1, row = ((lane >> 1) & 6) + (lane & 1);
        u32x4 v = *(const u32x4*)(mem0 + row * DM + 64 * (u >> 6) + 16 * g + 8 * hf);
        v.x = nosent(v.x); v.y = nosent(v.y); v.z = nosent(v.z); v.w = nosent(v.w);
        st16_sc1(xch + ((size_t)3 << 21) + (size_t)(rep * 2 + j) * 16384 + par * 8192 + t * 16, v);
      }
    }
  }
  if (wave_id_ == 7) IS(4);
  WAIT_V(0);
  BAR();
  if (wave_id_ == 7) IS(5);
  if (t == 0) st8_sc1(ws + OFF_INI + i * 128, u32x2{(unsigned)tag, ((unsigned)(tag >> 48) << 16) | vote});
}
#define GEMMX gemm7
#define CPF 128
#define PFD 2
#define PFW 0
#define NIMM 3
enum { T_NONE, T_QV, T_KR, T_WO, T_F1, T_F2, T_F2C, T_FIN, T_LN, T_SA };
__device__ __attribute__((aligned(16))) const unsigned ROLES[256][6] = {
  {0x1, 0x0, 0x0, 0x0, 0x0, 0x0},
  {0x11, 0x0, 0x0, 0x0, 0x0, 0x0},
  {0x21, 0x0, 0x0, 0x0, 0x0, 0x0},
  {0x31, 0x0, 0x0, 0x0, 0x0, 0x0},
  {0x41, 0x0, 0x0, 0x0, 0x0, 0x0},
  {0x51, 0x0, 0x0, 0x0, 0x0, 0x0},
  {0x61, 0x0, 0x0, 0x0, 0x0, 0x0},
  {0x71, 0x0, 0x0, 0x0, 0x0, 0x0},
  {0x81, 0x0, 0x0, 0x0, 0x0, 0x0},
  {0x91, 0x0, 0x0, 0x0, 0x0, 0x0},
  {0xa1, 0x0, 0x0, 0x0, 0x0, 0x0},
  {0xb1, 0x0, 0x0, 0x0, 0x0, 0x0},
  {0x2, 0x0, 0x0, 0x0, 0x0, 0x0},
  {0x12, 0x0, 0x0, 0x0, 0x0, 0x0},
  {0x22, 0x0, 0x0, 0x0, 0x0, 0x0},
  {0x32, 0x0, 0x0, 0x0, 0x0, 0x0},
  {0x42, 0x0, 0x0, 0x0, 0x0, 0x0},
  {0x52, 0x0, 0x0, 0x0, 0x0, 0x0},
  {0x62, 0x0, 0x0, 0x0, 0x0, 0x0},
  {0x72, 0x0, 0x0, 0x0, 0x0, 0x0},
  {0x82, 0x0, 0x0, 0x0, 0x0, 0x0},
  {0x92, 0x0, 0x0, 0x0, 0x0, 0x0},
  {0xa2, 0x0, 0x0, 0x0, 0x0, 0x0},
  {0xb2, 0x0, 0x0, 0x0, 0x0, 0x0},
  {0xc2, 0x0, 0x0, 0x0, 0x0, 0x0},
  {0xd2, 0x0, 0x0, 0x0, 0x0, 0x0},
  {0xe2, 0x0, 0x0, 0x0, 0x0, 0x0},
  {0xf2, 0x0, 0x0, 0x0, 0x0, 0x0},
  {0x102, 0x0, 0x0, 0x0, 0x0, 0x0},
  {0x112, 0x0, 0x0, 0x0, 0x0, 0x0},
  {0x122, 0x0, 0x0, 0x0, 0x0, 0x0},
  {0x132, 0x0, 0x0, 0x0, 0x0, 0x0},
  {0xc1, 0x0, 0x0, 0x0, 0x0, 0x0},
  {0xd1, 0x0, 0x0, 0x0, 0x0, 0x0},
  {0xe1, 0x0, 0x0, 0x0, 0x0, 0x0},
  {0xf1, 0x0, 0x0, 0x0, 0x0, 0x0},
  {0x101, 0x0, 0x0, 0x0, 0x0, 0x0},
  {0x111, 0x0, 0x0, 0x0, 0x0, 0x0},
  {0x121, 0x0, 0x0, 0x0, 0x0, 0x0},
  {0x131, 0x0, 0x0, 0x0, 0x0, 0x0},
  {0x142, 0x0, 0x0, 0x0, 0x0, 0x0},
  {0x152, 0x0, 0x0, 0x0, 0x0, 0x0},
  {0x162, 0x0, 0x0, 0x0, 0x0, 0x0},
  {0x172, 0x0, 0x0, 0x0, 0x0, 0x0},
  {0x182, 0x0, 0x0, 0x0, 0x0, 0x0},
  {0x192, 0x0, 0x0, 0x0, 0x0, 0x0},
  {0x1a2, 0x0, 0x0, 0x0, 0x0, 0x0},
  {0x1b2, 0x0, 0x0, 0x0, 0x0, 0x0},
  {0x1c2, 0x0, 0x0, 0x0, 0x0, 0x0},
  {0x1d2, 0x0, 0x0, 0x0, 0x0, 0x0},
  {0x1e2, 0x0, 0x0, 0x0, 0x0, 0x0},
  {0x1f2, 0x0, 0x0, 0x0, 0x0, 0x0},
  {0x202, 0x0, 0x0, 0x0, 0x0, 0x0},
  {0x212, 0x0, 0x0, 0x0, 0x0, 0x0},
  {0x222, 0x0, 0x0, 0x0, 0x0, 0x0},
  {0x232, 0x0, 0x0, 0x0, 0x0, 0x0},
  {0x242, 0x0, 0x0, 0x0, 0x0, 0x0},
  {0x252, 0x0, 0x0, 0x0, 0x0, 0x0},
  {0x262, 0x0, 0x0, 0x0, 0x0, 0x0},
  {0x272, 0x0, 0x0, 0x0, 0x0, 0x0},
  {0x282, 0x0, 0x0, 0x0, 0x0, 0x0},
  {0x292, 0x0, 0x0, 0x0, 0x0, 0x0},
  {0x2a2, 0x0, 0x0, 0x0, 0x0, 0x0},
  {0x2b2, 0x0, 0x0, 0x0, 0x0, 0x0},
  {0x141, 0x0, 0x0, 0x0, 0x0, 0x0},
  {0x151, 0x0, 0x0, 0x0, 0x0, 0x0},
  {0x161, 0x0, 0x0, 0x0, 0x0, 0x0},
  {0x171, 0x0, 0x0, 0x0, 0x0, 0x0},
  {0x181, 0x0, 0x0, 0x0, 0x0, 0x0},
  {0x191, 0x0, 0x0, 0x0, 0x0, 0x0},
  {0x1a1, 0x0, 0x0, 0x0, 0x0, 0x0},
  {0x1b1, 0x0, 0x0, 0x0, 0x0, 0x0},
  {0x1c1, 0x0, 0x0, 0x0, 0x0, 0x0},
  {0x1d1, 0x0, 0x0, 0x0, 0x0, 0x0},
  {0x1e1, 0x0, 0x0, 0x0, 0x0, 0x0},
  {0x1f1, 0x0, 0x0, 0x0, 0x0, 0x0},
  {0x2c2, 0x0, 0x0, 0x0, 0x0, 0x0},
  {0x2d2, 0x0, 0x0, 0x0, 0x0, 0x0},
  {0x2e2, 0x0, 0x0, 0x0, 0x0, 0x0},
  {0x2f2, 0x0, 0x0, 0x0, 0x0, 0x0},
  {0x302, 0x0, 0x0, 0x0, 0x0, 0x0},
  {0x312, 0x0, 0x0, 0x0, 0x0, 0x0},
  {0x322, 0x0, 0x0, 0x0, 0x0, 0x0},
  {0x332, 0x0, 0x0, 0x0, 0x0, 0x0},
  {0x342, 0x0, 0x0, 0x0, 0x0, 0x0},
  {0x352, 0x0, 0x0, 0x0, 0x0, 0x0},
  {0x362, 0x0, 0x0, 0x0, 0x0, 0x0},
  {0x372, 0x0, 0x0, 0x0, 0x0, 0x0},
  {0x382, 0x0, 0x0, 0x0, 0x0, 0x0},
  {0x392, 0x0, 0x0, 0x0, 0x0, 0x0},
  {0x3a2, 0x0, 0x0, 0x0, 0x0, 0x0},
  {0x3b2, 0x0, 0x0, 0x0, 0x0, 0x0},
  {0x3c2, 0x0, 0x0, 0x0, 0x0, 0x0},
  {0x3d2, 0x0, 0x0, 0x0, 0x0, 0x0},
  {0x3e2, 0x0, 0x0, 0x0, 0x0, 0x0},
  {0x3f2, 0x0, 0x0, 0x0, 0x0, 0x0},
  {0x100005, 0x300000, 0x0, 0x0, 0x0, 0x0},
  {0x100015, 0x300004, 0x0, 0x0, 0x0, 0x0},
  {0x100025, 0x300008, 0x0, 0x0, 0x0, 0x0},
  {0x100035, 0x30000c, 0x0, 0x0, 0x0, 0x0},
  {0x100045, 0x300010, 0x0, 0x0, 0x0, 0x0},
  {0x100055, 0x300014, 0x0, 0x0, 0x0, 0x0},
  {0x100065, 0x300018, 0x0, 0x0, 0x0, 0x0},
  {0x200037, 0x30001c, 0x1402a1e, 0x0, 0x0, 0x0},
  {0x100075, 0x300020, 0x0, 0x0, 0x0, 0x0},
  {0x100085, 0x300024, 0x0, 0x0, 0x0, 0x0},
  {0x100095, 0x300028, 0x0, 0x0, 0x0, 0x0},
  {0x1000a5, 0x30002c, 0x0, 0x0, 0x0, 0x0},
  {0x1000b5, 0x300030, 0x0, 0x0, 0x0, 0x0},
  {0x1000c5, 0x300034, 0x0, 0x0, 0x0, 0x0},
  {0x1000d5, 0x300038, 0x0, 0x0, 0x0, 0x0},
  {0x200077, 0x30003c, 0x1402a3e, 0x0, 0x0, 0x0},
  {0x1000e5, 0x300040, 0x0, 0x0, 0x0, 0x0},
  {0x1000f5, 0x300044, 0x0, 0x0, 0x0, 0x0},
  {0x100105, 0x300048, 0x0, 0x0, 0x0, 0x0},
  {0x100115, 0x30004c, 0x0, 0x0, 0x0, 0x0},
  {0x100125, 0x300050, 0x0, 0x0, 0x0, 0x0},
  {0x100135, 0x300054, 0x0, 0x0, 0x0, 0x0},
  {0x100145, 0x300058, 0x0, 0x0, 0x0, 0x0},
  {0x2000b7, 0x30005c, 0x1402a5e, 0x0, 0x0, 0x0},
  {0x100155, 0x300060, 0x0, 0x0, 0x0, 0x0},
  {0x100165, 0x300064, 0x0, 0x0, 0x0, 0x0},
  {0x100175, 0x300068, 0x0, 0x0, 0x0, 0x0},
  {0x100006, 0x30006c, 0x0, 0x0, 0x0, 0x0},
  {0x100016, 0x300070, 0x0, 0x0, 0x0, 0x0},
  {0x100026, 0x300074, 0x0, 0x0, 0x0, 0x0},
  {0x100036, 0x300078, 0x0, 0x0, 0x0, 0x0},
  {0x2000f7, 0x30007c, 0x1402a7e, 0x0, 0x0, 0x0},
  {0x100034, 0x5402a02, 0x0, 0x0, 0x0, 0x0},
  {0x1000b4, 0x5402a06, 0x0, 0x0, 0x0, 0x0},
  {0x100004, 0x5402a0a, 0x0, 0x0, 0x0, 0x0},
  {0x100014, 0x5402a0e, 0x0, 0x0, 0x0, 0x0},
  {0x100003, 0x5403000, 0x0, 0x0, 0x0, 0x0},
  {0x100013, 0x5403004, 0x0, 0x0, 0x0, 0x0},
  {0x100023, 0x5403008, 0x0, 0x0, 0x0, 0x0},
  {0x100033, 0x540300c, 0x0, 0x0, 0x0, 0x0},
  {0x200007, 0x360001, 0x1402a12, 0x0, 0x0, 0x0},
  {0x200017, 0x360005, 0x1402a16, 0x0, 0x0, 0x0},
  {0x200027, 0x360009, 0x1402a19, 0x0, 0x0, 0x0},
  {0x200000, 0x36000d, 0x1402a1a, 0x0, 0x0, 0x0},
  {0x200000, 0x360011, 0x1402a1d, 0x0, 0x0, 0x0},
  {0x200000, 0x400015, 0x1403018, 0x0, 0x0, 0x0},
  {0x100024, 0x2a0019, 0x0, 0x0, 0x0, 0x0},
  {0x100044, 0x2a001d, 0x0, 0x0, 0x0, 0x0},
  {0x100054, 0x2a0002, 0x0, 0x0, 0x0, 0x0},
  {0x100064, 0x2a0006, 0x0, 0x0, 0x0, 0x0},
  {0x100074, 0x2a000a, 0x0, 0x0, 0x0, 0x0},
  {0x100084, 0x2a000e, 0x0, 0x0, 0x0, 0x0},
  {0x100094, 0x2a0012, 0x0, 0x0, 0x0, 0x0},
  {0x1000a4, 0x2a0016, 0x0, 0x0, 0x0, 0x0},
  {0x1000c4, 0x2a001a, 0x0, 0x0, 0x0, 0x0},
  {0x1000d4, 0x2a001e, 0x0, 0x0, 0x0, 0x0},
  {0x101043, 0x1403601, 0x0, 0x0, 0x0, 0x0},
  {0x102053, 0x140360d, 0x0, 0x0, 0x0, 0x0},
  {0x103063, 0x1403605, 0x0, 0x0, 0x0, 0x0},
  {0x104073, 0x1403609, 0x0, 0x0, 0x0, 0x0},
  {0x105008, 0x1403010, 0x0, 0x0, 0x0, 0x0},
  {0x106088, 0x1403611, 0x0, 0x0, 0x0, 0x0},
  {0x107018, 0x1403014, 0x0, 0x0, 0x0, 0x0},
  {0x108098, 0x140301c, 0x0, 0x0, 0x0, 0x0},
  {0x100134, 0x5402a22, 0x0, 0x0, 0x0, 0x0},
  {0x1001b4, 0x5402a26, 0x0, 0x0, 0x0, 0x0},
  {0x1000e4, 0x5402a2a, 0x0, 0x0, 0x0, 0x0},
  {0x1000f4, 0x5402a2e, 0x0, 0x0, 0x0, 0x0},
  {0x100083, 0x5403020, 0x0, 0x0, 0x0, 0x0},
  {0x100093, 0x5403024, 0x0, 0x0, 0x0, 0x0},
  {0x1000a3, 0x5403028, 0x0, 0x0, 0x0, 0x0},
  {0x1000b3, 0x540302c, 0x0, 0x0, 0x0, 0x0},
  {0x200047, 0x360021, 0x1402a32, 0x0, 0x0, 0x0},
  {0x200057, 0x360025, 0x1402a36, 0x0, 0x0, 0x0},
  {0x200067, 0x360029, 0x1402a39, 0x0, 0x0, 0x0},
  {0x200000, 0x36002d, 0x1402a3a, 0x0, 0x0, 0x0},
  {0x200000, 0x360031, 0x1402a3d, 0x0, 0x0, 0x0},
  {0x200000, 0x400035, 0x1403038, 0x0, 0x0, 0x0},
  {0x100104, 0x2a0039, 0x0, 0x0, 0x0, 0x0},
  {0x100114, 0x2a003d, 0x0, 0x0, 0x0, 0x0},
  {0x100124, 0x2a0022, 0x0, 0x0, 0x0, 0x0},
  {0x100144, 0x2a0026, 0x0, 0x0, 0x0, 0x0},
  {0x100154, 0x2a002a, 0x0, 0x0, 0x0, 0x0},
  {0x100164, 0x2a002e, 0x0, 0x0, 0x0, 0x0},
  {0x100174, 0x2a0032, 0x0, 0x0, 0x0, 0x0},
  {0x100184, 0x2a0036, 0x0, 0x0, 0x0, 0x0},
  {0x100194, 0x2a003a, 0x0, 0x0, 0x0, 0x0},
  {0x1001a4, 0x2a003e, 0x0, 0x0, 0x0, 0x0},
  {0x1090c3, 0x1403621, 0x0, 0x0, 0x0, 0x0},
  {0x10a0d3, 0x140362d, 0x0, 0x0, 0x0, 0x0},
  {0x10b0e3, 0x1403625, 0x0, 0x0, 0x0, 0x0},
  {0x10c0f3, 0x1403629, 0x0, 0x0, 0x0, 0x0},
  {0x10d028, 0x1403030, 0x0, 0x0, 0x0, 0x0},
  {0x10e0a8, 0x1403631, 0x0, 0x0, 0x0, 0x0},
  {0x10f038, 0x1403034, 0x0, 0x0, 0x0, 0x0},
  {0x1100b8, 0x140303c, 0x0, 0x0, 0x0, 0x0},
  {0x100234, 0x5402a42, 0x0, 0x0, 0x0, 0x0},
  {0x1001c4, 0x5402a46, 0x0, 0x0, 0x0, 0x0},
  {0x1001d4, 0x5402a4a, 0x0, 0x0, 0x0, 0x0},
  {0x1001e4, 0x5402a4e, 0x0, 0x0, 0x0, 0x0},
  {0x100103, 0x5403040, 0x0, 0x0, 0x0, 0x0},
  {0x100113, 0x5403044, 0x0, 0x0, 0x0, 0x0},
  {0x100123, 0x5403048, 0x0, 0x0, 0x0, 0x0},
  {0x100133, 0x540304c, 0x0, 0x0, 0x0, 0x0},
  {0x200087, 0x360041, 0x1402a52, 0x0, 0x0, 0x0},
  {0x200097, 0x360045, 0x1402a56, 0x0, 0x0, 0x0},
  {0x2000a7, 0x360049, 0x1402a59, 0x0, 0x0, 0x0},
  {0x200000, 0x36004d, 0x1402a5a, 0x0, 0x0, 0x0},
  {0x200000, 0x360051, 0x1402a5d, 0x0, 0x0, 0x0},
  {0x200000, 0x400055, 0x1403058, 0x0, 0x0, 0x0},
  {0x1001f4, 0x2a0059, 0x0, 0x0, 0x0, 0x0},
  {0x100204, 0x2a005d, 0x0, 0x0, 0x0, 0x0},
  {0x100214, 0x2a0042, 0x0, 0x0, 0x0, 0x0},
  {0x100224, 0x2a0046, 0x0, 0x0, 0x0, 0x0},
  {0x100244, 0x2a004a, 0x0, 0x0, 0x0, 0x0},
  {0x100254, 0x2a004e, 0x0, 0x0, 0x0, 0x0},
  {0x100264, 0x2a0052, 0x0, 0x0, 0x0, 0x0},
  {0x100274, 0x2a0056, 0x0, 0x0, 0x0, 0x0},
  {0x100284, 0x2a005a, 0x0, 0x0, 0x0, 0x0},
  {0x100294, 0x2a005e, 0x0, 0x0, 0x0, 0x0},
  {0x111143, 0x1403641, 0x0, 0x0, 0x0, 0x0},
  {0x112153, 0x140364d, 0x0, 0x0, 0x0, 0x0},
  {0x113163, 0x1403645, 0x0, 0x0, 0x0, 0x0},
  {0x114173, 0x1403649, 0x0, 0x0, 0x0, 0x0},
  {0x115048, 0x1403050, 0x0, 0x0, 0x0, 0x0},
  {0x1160c8, 0x1403651, 0x0, 0x0, 0x0, 0x0},
  {0x117058, 0x1403054, 0x0, 0x0, 0x0, 0x0},
  {0x1180d8, 0x140305c, 0x0, 0x0, 0x0, 0x0},
  {0x1002b4, 0x5402a62, 0x0, 0x0, 0x0, 0x0},
  {0x100334, 0x5402a66, 0x0, 0x0, 0x0, 0x0},
  {0x1002a4, 0x5402a6a, 0x0, 0x0, 0x0, 0x0},
  {0x1002c4, 0x5402a6e, 0x0, 0x0, 0x0, 0x0},
  {0x100183, 0x5403060, 0x0, 0x0, 0x0, 0x0},
  {0x100193, 0x5403064, 0x0, 0x0, 0x0, 0x0},
  {0x1001a3, 0x5403068, 0x0, 0x0, 0x0, 0x0},
  {0x1001b3, 0x540306c, 0x0, 0x0, 0x0, 0x0},
  {0x2000c7, 0x360061, 0x1402a72, 0x0, 0x0, 0x0},
  {0x2000d7, 0x360065, 0x1402a76, 0x0, 0x0, 0x0},
  {0x2000e7, 0x360069, 0x1402a79, 0x0, 0x0, 0x0},
  {0x200000, 0x36006d, 0x1402a7a, 0x0, 0x0, 0x0},
  {0x200000, 0x360071, 0x1402a7d, 0x0, 0x0, 0x0},
  {0x200000, 0x400075, 0x1403078, 0x0, 0x0, 0x0},
  {0x1002d4, 0x2a0079, 0x0, 0x0, 0x0, 0x0},
  {0x1002e4, 0x2a007d, 0x0, 0x0, 0x0, 0x0},
  {0x1002f4, 0x2a0062, 0x0, 0x0, 0x0, 0x0},
  {0x100304, 0x2a0066, 0x0, 0x0, 0x0, 0x0},
  {0x100314, 0x2a006a, 0x0, 0x0, 0x0, 0x0},
  {0x100324, 0x2a006e, 0x0, 0x0, 0x0, 0x0},
  {0x100344, 0x2a0072, 0x0, 0x0, 0x0, 0x0},
  {0x100354, 0x2a0076, 0x0, 0x0, 0x0, 0x0},
  {0x100364, 0x2a007a, 0x0, 0x0, 0x0, 0x0},
  {0x100374, 0x2a007e, 0x0, 0x0, 0x0, 0x0},
  {0x1191c3, 0x1403661, 0x0, 0x0, 0x0, 0x0},
  {0x11a1d3, 0x140366d, 0x0, 0x0, 0x0, 0x0},
  {0x11b1e3, 0x1403665, 0x0, 0x0, 0x0, 0x0},
  {0x11c1f3, 0x1403669, 0x0, 0x0, 0x0, 0x0},
  {0x11d068, 0x1403070, 0x0, 0x0, 0x0, 0x0},
  {0x11e0e8, 0x1403671, 0x0, 0x0, 0x0, 0x0},
  {0x11f078, 0x1403074, 0x0, 0x0, 0x0, 0x0},
  {0x1200f8, 0x140307c, 0x0, 0x0, 0x0, 0x0},
};
constexpr unsigned NQV = 0xc080c;
__device__ __attribute__((aligned(16))) const unsigned PFT[8][128] = {
  {0x40000f, 0x40000e, 0x0, 0x0, 0x41000f, 0x41000e, 0x0, 0x0, 0x42000f, 0x42000e, 0x0, 0x0, 0x43000f, 0x43000e, 0x0, 0x0, 0x44000f, 0x40002d, 0x0, 0x0, 0x45000f, 0x41002d, 0x0, 0x0, 0x46000f, 0x42002d, 0x0, 0x0, 0x47000f, 0x43002d, 0x0, 0x0, 0x48000f, 0x40002e, 0x0, 0x0, 0x49000f, 0x41002e, 0x0, 0x0, 0x4a000f, 0x42002e, 0x0, 0x0, 0x4b000f, 0x43002e, 0x0, 0x0, 0x4c000f, 0x40004d, 0x0, 0x0, 0x4d000f, 0x41004d, 0x0, 0x0, 0x4e000f, 0x42004d, 0x0, 0x0, 0x4f000f, 0x43004d, 0x0, 0x0, 0x40000c, 0x40004e, 0x0, 0x0, 0x41000c, 0x41004e, 0x0, 0x0, 0x42000c, 0x42004e, 0x0, 0x0, 0x43000c, 0x43004e, 0x0, 0x0, 0x40002c, 0x400003, 0x0, 0x0, 0x41002c, 0x400007, 0x0, 0x0, 0x42002c, 0x40000b, 0x0, 0x0, 0x43002c, 0x400013, 0x0, 0x0, 0x40004c, 0x420003, 0x0, 0x0, 0x41004c, 0x420007, 0x0, 0x0, 0x42004c, 0x42000b, 0x0, 0x0, 0x43004c, 0x420013, 0x0, 0x0, 0x40000d, 0x440003, 0x0, 0x0, 0x41000d, 0x440007, 0x0, 0x0, 0x42000d, 0x44000b, 0x0, 0x0, 0x43000d, 0x440013, 0x0, 0x0},
  {0x40006c, 0x400017, 0x0, 0x0, 0x41006c, 0x40001b, 0x0, 0x0, 0x42006c, 0x40001f, 0x0, 0x0, 0x43006c, 0x400023, 0x0, 0x0, 0x40008c, 0x400027, 0x0, 0x0, 0x41008c, 0x40002b, 0x0, 0x0, 0x42008c, 0x420017, 0x0, 0x0, 0x43008c, 0x42001b, 0x0, 0x0, 0x40006d, 0x42001f, 0x0, 0x0, 0x41006d, 0x420023, 0x0, 0x0, 0x42006d, 0x420027, 0x0, 0x0, 0x43006d, 0x42002b, 0x0, 0x0, 0x40006e, 0x440017, 0x0, 0x0, 0x41006e, 0x44001b, 0x0, 0x0, 0x42006e, 0x44001f, 0x0, 0x0, 0x43006e, 0x440023, 0x0, 0x0, 0x40008d, 0x440027, 0x0, 0x0, 0x41008d, 0x44002b, 0x0, 0x0, 0x42008d, 0x460017, 0x0, 0x0, 0x43008d, 0x46001b, 0x0, 0x0, 0x40008e, 0x46001f, 0x0, 0x0, 0x41008e, 0x460023, 0x0, 0x0, 0x42008e, 0x460027, 0x0, 0x0, 0x43008e, 0x46002b, 0x0, 0x0, 0x4000ad, 0x480017, 0x0, 0x0, 0x4100ad, 0x48001b, 0x0, 0x0, 0x4200ad, 0x48001f, 0x0, 0x0, 0x4300ad, 0x480023, 0x0, 0x0, 0x4000ae, 0x480027, 0x0, 0x0, 0x4100ae, 0x48002b, 0x0, 0x0, 0x4200ae, 0x4a0017, 0x0, 0x0, 0x4300ae, 0x4a001b, 0x0, 0x0},
  {0x40002f, 0x4000ce, 0x0, 0x0, 0x41002f, 0x4100ce, 0x0, 0x0, 0x42002f, 0x4200ce, 0x0, 0x0, 0x43002f, 0x4300ce, 0x0, 0x0, 0x44002f, 0x4000ed, 0x0, 0x0, 0x45002f, 0x4100ed, 0x0, 0x0, 0x46002f, 0x4200ed, 0x0, 0x0, 0x47002f, 0x4300ed, 0x0, 0x0, 0x48002f, 0x4000ee, 0x0, 0x0, 0x49002f, 0x4100ee, 0x0, 0x0, 0x4a002f, 0x4200ee, 0x0, 0x0, 0x4b002f, 0x4300ee, 0x0, 0x0, 0x4c002f, 0x4000fe, 0x0, 0x0, 0x4d002f, 0x4100fe, 0x0, 0x0, 0x4e002f, 0x4200fe, 0x0, 0x0, 0x4f002f, 0x4300fe, 0x0, 0x0, 0x4000ac, 0x4000fd, 0x0, 0x0, 0x4100ac, 0x4100fd, 0x0, 0x0, 0x4200ac, 0x4200fd, 0x0, 0x0, 0x4300ac, 0x4300fd, 0x0, 0x0, 0x4000cc, 0x40003f, 0x0, 0x0, 0x4100cc, 0x41003f, 0x0, 0x0, 0x4200cc, 0x42003f, 0x0, 0x0, 0x4300cc, 0x43003f, 0x0, 0x0, 0x4000ec, 0x44003f, 0x0, 0x0, 0x4100ec, 0x45003f, 0x0, 0x0, 0x4200ec, 0x46003f, 0x0, 0x0, 0x4300ec, 0x47003f, 0x0, 0x0, 0x4000cd, 0x48003f, 0x0, 0x0, 0x4100cd, 0x49003f, 0x0, 0x0, 0x4200cd, 0x4a003f, 0x0, 0x0, 0x4300cd, 0x4b003f, 0x0, 0x0},
  {0x40000c, 0x0, 0x0, 0x0, 0x41000c, 0x0, 0x0, 0x0, 0x42000c, 0x0, 0x0, 0x0, 0x43000c, 0x0, 0x0, 0x0, 0x40002c, 0x0, 0x0, 0x0, 0x41002c, 0x0, 0x0, 0x0, 0x42002c, 0x0, 0x0, 0x0, 0x43002c, 0x0, 0x0, 0x0, 0x40004c, 0x0, 0x0, 0x0, 0x41004c, 0x0, 0x0, 0x0, 0x42004c, 0x0, 0x0, 0x0, 0x43004c, 0x0, 0x0, 0x0, 0x40006c, 0x0, 0x0, 0x0, 0x41006c, 0x0, 0x0, 0x0, 0x42006c, 0x0, 0x0, 0x0, 0x43006c, 0x0, 0x0, 0x0, 0x40008c, 0x0, 0x0, 0x0, 0x41008c, 0x0, 0x0, 0x0, 0x42008c, 0x0, 0x0, 0x0, 0x43008c, 0x0, 0x0, 0x0, 0x4000ac, 0x0, 0x0, 0x0, 0x4100ac, 0x0, 0x0, 0x0, 0x4200ac, 0x0, 0x0, 0x0, 0x4300ac, 0x0, 0x0, 0x0, 0x4000cc, 0x0, 0x0, 0x0, 0x4100cc, 0x0, 0x0, 0x0, 0x4200cc, 0x0, 0x0, 0x0, 0x4300cc, 0x0, 0x0, 0x0, 0x4000ec, 0x0, 0x0, 0x0, 0x4100ec, 0x0, 0x0, 0x0, 0x4200ec, 0x0, 0x0, 0x0, 0x4300ec, 0x0, 0x0, 0x0},
  {0x0, 0x0, 0x0, 0x0, 0x0, 0x0, 0x0, 0x0, 0x0, 0x0, 0x0, 0x0, 0x0, 0x0, 0x0, 0x0, 0x0, 0x0, 0x0, 0x0, 0x0, 0x0, 0x0, 0x0, 0x0, 0x0, 0x0, 0x0, 0x0, 0x0, 0x0, 0x0, 0x40000d, 0x0, 0x0, 0x0, 0x41000d, 0x0, 0x0, 0x0, 0x42000d, 0x0, 0x0, 0x0, 0x43000d, 0x0, 0x0, 0x0, 0x40002d, 0x0, 0x0, 0x0, 0x41002d, 0x0, 0x0, 0x0, 0x42002d, 0x0, 0x0, 0x0, 0x43002d, 0x0, 0x0, 0x0, 0x40000e, 0x0, 0x0, 0x0, 0x41000e, 0x0, 0x0, 0x0, 0x42000e, 0x0, 0x0, 0x0, 0x43000e, 0x0, 0x0, 0x0, 0x40002e, 0x0, 0x0, 0x0, 0x41002e, 0x0, 0x0, 0x0, 0x42002e, 0x0, 0x0, 0x0, 0x43002e, 0x0, 0x0, 0x0, 0x0, 0x0, 0x0, 0x0, 0x0, 0x0, 0x0, 0x0, 0x0, 0x0, 0x0, 0x0, 0x0, 0x0, 0x0, 0x0, 0x0, 0x0, 0x0, 0x0, 0x0, 0x0, 0x0, 0x0, 0x0, 0x0, 0x0, 0x0, 0x0, 0x0, 0x0, 0x0},
  {0x0, 0x0, 0x0, 0x0, 0x0, 0x0, 0x0, 0x0, 0x0, 0x0, 0x0, 0x0, 0x0, 0x0, 0x0, 0x0, 0x0, 0x0, 0x0, 0x0, 0x0, 0x0, 0x0, 0x0, 0x0, 0x0, 0x0, 0x0, 0x0, 0x0, 0x0, 0x0, 0x40004d, 0x0, 0x0, 0x0, 0x41004d, 0x0, 0x0, 0x0, 0x42004d, 0x0, 0x0, 0x0, 0x43004d, 0x0, 0x0, 0x0, 0x40006d, 0x0, 0x0, 0x0, 0x41006d, 0x0, 0x0, 0x0, 0x42006d, 0x0, 0x0, 0x0, 0x43006d, 0x0, 0x0, 0x0, 0x40004e, 0x0, 0x0, 0x0, 0x41004e, 0x0, 0x0, 0x0, 0x42004e, 0x0, 0x0, 0x0, 0x43004e, 0x0, 0x0, 0x0, 0x40006e, 0x0, 0x0, 0x0, 0x41006e, 0x0, 0x0, 0x0, 0x42006e, 0x0, 0x0, 0x0, 0x43006e, 0x0, 0x0, 0x0, 0x0, 0x0, 0x0, 0x0, 0x0, 0x0, 0x0, 0x0, 0x0, 0x0, 0x0, 0x0, 0x0, 0x0, 0x0, 0x0, 0x0, 0x0, 0x0, 0x0, 0x0, 0x0, 0x0, 0x0, 0x0, 0x0, 0x0, 0x0, 0x0, 0x0, 0x0, 0x0},
  {0x0, 0x0, 0x0, 0x0, 0x0, 0x0, 0x0, 0x0, 0x0, 0x0, 0x0, 0x0, 0x0, 0x0, 0x0, 0x0, 0x0, 0x0, 0x0, 0x0, 0x0, 0x0, 0x0, 0x0, 0x0, 0x0, 0x0, 0x0, 0x0, 0x0, 0x0, 0x0, 0x40008d, 0x0, 0x0, 0x0, 0x41008d, 0x0, 0x0, 0x0, 0x42008d, 0x0, 0x0, 0x0, 0x43008d, 0x0, 0x0, 0x0, 0x4000ad, 0x0, 0x0, 0x0, 0x4100ad, 0x0, 0x0, 0x0, 0x4200ad, 0x0, 0x0, 0x0, 0x4300ad, 0x0, 0x0, 0x0, 0x40008e, 0x0, 0x0, 0x0, 0x41008e, 0x0, 0x0, 0x0, 0x42008e, 0x0, 0x0, 0x0, 0x43008e, 0x0, 0x0, 0x0, 0x4000ae, 0x0, 0x0, 0x0, 0x4100ae, 0x0, 0x0, 0x0, 0x4200ae, 0x0, 0x0, 0x0, 0x4300ae, 0x0, 0x0, 0x0, 0x0, 0x0, 0x0, 0x0, 0x0, 0x0, 0x0, 0x0, 0x0, 0x0, 0x0, 0x0, 0x0, 0x0, 0x0, 0x0, 0x0, 0x0, 0x0, 0x0, 0x0, 0x0, 0x0, 0x0, 0x0, 0x0, 0x0, 0x0, 0x0, 0x0, 0x0, 0x0},
  {0x0, 0x0, 0x0, 0x0, 0x0, 0x0, 0x0, 0x0, 0x0, 0x0, 0x0, 0x0, 0x0, 0x0, 0x0, 0x0, 0x0, 0x0, 0x0, 0x0, 0x0, 0x0, 0x0, 0x0, 0x0, 0x0, 0x0, 0x0, 0x0, 0x0, 0x0, 0x0, 0x4000cd, 0x0, 0x0, 0x0, 0x4100cd, 0x0, 0x0, 0x0, 0x4200cd, 0x0, 0x0, 0x0, 0x4300cd, 0x0, 0x0, 0x0, 0x4000ed, 0x0, 0x0, 0x0, 0x4100ed, 0x0, 0x0, 0x0, 0x4200ed, 0x0, 0x0, 0x0, 0x4300ed, 0x0, 0x0, 0x0, 0x4000ce, 0x0, 0x0, 0x0, 0x4100ce, 0x0, 0x0, 0x0, 0x4200ce, 0x0, 0x0, 0x0, 0x4300ce, 0x0, 0x0, 0x0, 0x4000ee, 0x0, 0x0, 0x0, 0x4100ee, 0x0, 0x0, 0x0, 0x4200ee, 0x0, 0x0, 0x0, 0x4300ee, 0x0, 0x0, 0x0, 0x0, 0x0, 0x0, 0x0, 0x0, 0x0, 0x0, 0x0, 0x0, 0x0, 0x0, 0x0, 0x0, 0x0, 0x0, 0x0, 0x0, 0x0, 0x0, 0x0, 0x0, 0x0, 0x0, 0x0, 0x0, 0x0, 0x0, 0x0, 0x0, 0x0, 0x0, 0x0},
};
__global__ void __launch_bounds__(512, 2) k_mega(const u16* __restrict__ Wq, const u16* __restrict__ Wk, const u16* __restrict__ Wv,
                                                 const u16* __restrict__ Wo, const u16* __restrict__ w1, const u16* __restrict__ w3,
                                                 const u16* __restrict__ w2, const u16* __restrict__ x, const u16* __restrict__ cvec,
                                                 const u16* __restrict__ wmod, const u16* __restrict__ mem0, char* ws, u16* out) {
  extern __shared__ __attribute__((aligned(16))) char smem[];
  int wave_id_ = __builtin_amdgcn_readfirstlane((int)threadIdx.x >> 6);
  asm volatile("" : "+s"(wave_id_));
  unsigned* tmo = (unsigned*)(ws + OFF_TMO);
  unsigned long long* mts = nullptr;
  const float* mod = (const float*)(ws + OFF_MOD);
  volatile LAS unsigned* tslot = (volatile LAS unsigned*)(smem + L_SLOT + 768);
  unsigned xcc; asm volatile("s_getreg_b32 %0, hwreg(HW_REG_XCC_ID)" : "=s"(xcc)); xcc &= 7;
  auto ini_of = [](int sl) { return sl >= 128 && ((sl + 8) & 31) < 16 ? ((sl >> 5) - 4) * 16 + (sl & 15) : -1; };
  const int guess = xcc * 32 + ((blockIdx.x >> 3) & 31);
  if (wave_id_ == 3) {
    const char* pc; asm volatile("s_getpc_b64 %0" : "=s"(pc));
    const unsigned o = (unsigned)(lane_id() * 32 + ((blockIdx.x >> 3) & 31)) * 128;
    if (o < CPF * 1024) { const unsigned v = *(const unsigned*)(pc + o); asm volatile("" :: "v"(v)); }
  }
  if (wave_id_ >= 4 && ini_of(guess) >= 0) { init_sums(smem, opaque_tid() - 256, ini_of(guess), cvec, wmod); if (wave_id_ == 7) IS(3); }
  u32x2 rt_; u32x4 pfe;
  unsigned long long tag;
  asm volatile("s_load_dwordx2 %0, %3, %6\n\ts_load_dwordx2 %1, %4, 0\n\ts_load_dwordx4 %2, %5, 0\n\ts_waitcnt lgkmcnt(0)" : "=&s"(tag), "=&s"(rt_), "=&s"(pfe)
               : "s"(ws), "s"(&ROLES[guess][0]), "s"(&PFT[xcc][((blockIdx.x >> 3) & 31) * 4]), "i"((int)OFF_EP) : "memory");
  tag = (tag ^ (tag >> 31) ^ 0x9E3779B97F4A7C15ull) * 0xBF58476D1CE4E5B9ull; tag = (tag ^ (tag >> 29)) * 0x94D049BB133111EBull; tag ^= tag >> 32;
  if (wave_id_ == 0) {
    if (lane_id() == 0) {
      const unsigned long long g = tag; IS(0);
      unsigned sl = 0xFFFFu;
      for (int i = 0; i < 256; ++i)
        if (__hip_atomic_exchange((unsigned long long*)(ws + OFF_CLM) + ((guess + i) & 255), g, __ATOMIC_RELAXED, __HIP_MEMORY_SCOPE_AGENT) != g) { sl = (guess + i) & 255; break; }
      IS(1);
      tslot[0] = sl;
    }
  } else if (wave_id_ == 1) {
    const int ig = ini_of(guess);
    tslot[1] = ig >= 0 && (ig & 15) < 12 ? (unsigned)calibrate(ws, ig) : 0u;
    IS(2);
  } else if (ini_of(guess) >= 0) {
    if (wave_id_ < 4) { const unsigned v = *(const unsigned*)(mem0 + (opaque_tid() - 128) * 64); asm volatile("" :: "v"(v)); }
  } else if (wave_id_ >= 4) {
    delay64<PFD>();
#pragma unroll
    for (int i = 0; i < 4; ++i) {
      const unsigned e = pfe[i];
      const int t = opaque_tid() - 256, m = e & 3, wide = (e >> 21) & 1, row = ((e >> 8) & 8191) + (wide ? t >> 4 : t), cl = ((e >> 2) & 63) + (wide ? t & 15 : 0);
      const u16* p = (m == 0 ? Wq : (m == 1 ? Wk : (m == 2 ? Wv : Wo))) + (size_t)row * (m == 3 ? DM : HD) + cl * 64;
      if (e) asm volatile("s_mov_b32 m0, %1\n\ts_nop 0\n\tglobal_load_lds_dword %0, off" :: "v"(p), "s"((rt_.x & 15) == T_QV ? 31232 : L_GB + 8192) : "memory", "m0");
    }
    if (PFW) WAIT_V(0);
  }
  BAR();
  const int slot = (int)__builtin_amdgcn_readfirstlane(tslot[0]);
  const int cd = (int)__builtin_amdgcn_readfirstlane(tslot[1]);
  if (slot != guess) { rt_ = *(const u32x2*)ROLES[slot & 255]; WAIT_V(0); }
  BAR();
  asm volatile("" : "+s"(wave_id_));
  if (slot >= 256) return;
  const int xg = slot >> 5;
  const unsigned rw = rt_[0];
  const int role = rw & 15, idx = (rw >> 4) & 255;
  if (role == T_QV || role == T_KR) {
    const int wg = role == T_QV ? (idx >> 3) * 32 + (idx & 7) : (idx >> 4) * 32 + 8 + (idx & 15);
    if (role == T_QV) qk_body(smem, wave_id_, xg, idx, wg, Wq, Wv, ws, tmo, tag);
    else vr_body(smem, wave_id_, xg, idx >> 1, idx & 1, wg, Wk, Wv, Wo, ws, tmo, tag);
    return;
  }
  unsigned* cnt = (unsigned*)(xch_base(ws) + ((size_t)4 << 21));
  const int ini = ini_of(slot);
  if (ini >= 0) {
    if (slot != guess) { if (wave_id_ < 4) init_sums(smem, opaque_tid(), ini, cvec, wmod); BAR(); }
    init_role(smem, wave_id_, ini, mem0, ws, tag, slot != guess || cd == 0 ? 0u : ((cd > 0) == !(xcc & 4) ? 1u : 2u));
    if ((rw >> 12) & 255) l1_role(smem, wave_id_, ((rw >> 12) & 255) - 1, x, mod, cvec, wmod, ws + OFF_H1, cnt, tmo, mts, tag);
    else cnt += (ini_resolve1(smem, wave_id_, ws, tag) >> 2) << 11;
  }
  const int ntask = (rw >> 20) & 15;
  for (int it = 0; it < ntask; ++it) {
    const unsigned tk = it == 0 ? rt_[1] : ROLES[slot][1 + it];
    const int m = tk & 3, h = (tk >> 2) & 31, bo = (tk >> 8) & 255, ns = ((tk >> 16) & 255) - bo, part = (tk >> 24) & 3;
    const int lane = lane_id();
    const Dep d{uni((const char*)cnt), (unsigned)(PL1 + (lane & 31)) * 128, 8u, (unsigned)-bo};
    unsigned par = 0;
    if (wave_id_ == 0) {
      if ((tk >> 26) & 1) wait_k0(cnt, h, tmo);
    }
    BAR();
    gemm7<R_QKV>(smem, wave_id_, (m == 0 ? Wq : (m == 1 ? Wk : Wv)) + h * DH, nullptr, HD, ws + OFF_H1 + (size_t)bo * 65536, 65536, d,
                    ws + (m == 0 ? OFF_Q : (m == 1 ? OFF_K : OFF_V)) + h * 8192 + (size_t)bo * 32 * 8192, 32 * 8192,
                    cnt + (PQKV + h) * 32 + 4 * part + m, tmo, part ? 319 : (h >> 3) * 32 + m * 8 + (h & 7), mts, false,
                    bo ? 0 : NIMM, ns, bo ? ns : 0, bo * 257, tag, &par);
    cnt += (par >> 2) << 11;
  }
  __syncthreads();
  const char* cb = uni((const char*)cnt);
  if (role == T_WO || role == T_F2) {
    const bool wo = role == T_WO;
    const int ks = idx >> 3, cn = idx & 7;
    const int lane = lane_id();
    const Dep d{cb, (unsigned)(wo ? PROG + 8 * ks + (lane & 7) : PF1 + 16 * ks + (lane & 15)) * 128, 1u, 0u};
    gemm7<R_PART>(smem, wave_id_, (wo ? Wo : w2) + (size_t)ks * 1024 * DM + cn * 128, nullptr, DM, ws + (wo ? OFF_O : OFF_G) + ks * 16 * 4096, (wo ? 64 : 56) * 4096, d,
                  ws + (wo ? OFF_P : OFF_P2) + (ks * DM + cn * 128) * (TLP ? 64 : 2), (size_t)32 * PROW, cnt + ((wo ? PWO : PF2) + idx) * 32, tmo,
                  wo ? 128 + ks * 32 + cn : 128 + (idx / 7) * 32 + 22 + idx % 7, mts, !wo);
  } else if (role == T_F2C) {
    const int lane = lane_id();
    const Dep d{cb, (unsigned)(PF1 + 48 + (lane & 7)) * 128, 1u, 0u};
    gemm7<R_PART2>(smem, wave_id_, w2 + (size_t)3072 * DM + idx * 256, nullptr, DM, ws + OFF_G + 48 * 4096, 56 * 4096, d,
                   ws + OFF_P2 + (3 * DM + idx * 256) * (TLP ? 64 : 2), (size_t)32 * PROW, cnt + (PF2 + 24 + idx) * 32, tmo, 128 + 96 + 25 + idx, mts, true);
  } else if (role == T_F1) {
    const int lane = lane_id();
    const Dep d{cb, (unsigned)(PLN + (lane & 15)) * 128, 2u, (unsigned)((lane >> 3) & 1)};
    gemm7<R_F1>(smem, wave_id_, w1 + 64 * idx, w3 + 64 * idx, FF, ws + OFF_H2, 65536, d, ws + OFF_G + idx * 4096, 56 * 4096, cnt + (PF1 + idx) * 32, tmo,
                128 + (idx / 14) * 32 + 8 + idx % 14, mts, false);
  } else if (role == T_FIN) {
    fin_role(smem, wave_id_, idx, mod, ws + OFF_P2, (const u16*)(ws + OFF_X1), out, cnt, tmo, mts);
    if (idx == 15 && opaque_tid() == 0) st8_sc1(ws + OFF_EP, u32x2{(unsigned)tag, (unsigned)(tag >> 32)});
  } else if (role == T_LN) {
    row_role(smem, wave_id_, idx & 7, idx >> 3, mod, ws + OFF_P, x, (u16*)(ws + OFF_X1), ws + OFF_H2, cnt, tmo, 256 + ((idx >> 1) & 3), mts);
  }
}

extern "C" void kernel_launch(void* const* d_in, const int* in_sizes, int n_in, void* d_out, int out_size, void* d_ws, size_t ws_size,
                              hipStream_t stream) {
  const u16* x    = (const u16*)d_in[0];
  const u16* mem0 = (const u16*)d_in[1];
  const u16* Wq   = (const u16*)d_in[2];
  const u16* Wk   = (const u16*)d_in[3];
  const u16* Wv   = (const u16*)d_in[4];
  const u16* Wo   = (const u16*)d_in[5];
  const u16* w1   = (const u16*)d_in[8];
  const u16* w3   = (const u16*)d_in[9];
  const u16* w2   = (const u16*)d_in[10];
  const u16* c    = (const u16*)d_in[11];
  const u16* wmod = (const u16*)d_in[12];
#define WSHIFT 0
  char* ws = (char*)d_ws + ((size_t)WSHIFT << 20);
  hipFuncSetAttribute((const void*)k_mega, hipFuncAttributeMaxDynamicSharedMemorySize, MEGA_LDS);
  k_mega<<<256, 512, MEGA_LDS, stream>>>(Wq, Wk, Wv, Wo, w1, w3, w2, x, c, wmod, mem0, ws, (u16*)d_out);
}
```

```cpp
#include <hip/hip_runtime.h>
#include <hip/hip_bf16.h>
#include <stdint.h>
#include <type_traits>
#pragma clang diagnostic ignored "-Winline-asm"
#pragma clang diagnostic ignored "-Wunused-value"

typedef unsigned short u16;
typedef short s16x4 __attribute__((ext_vector_type(4)));
typedef __bf16 bf16x8 __attribute__((ext_vector_type(8)));
typedef float f32x4 __attribute__((ext_vector_type(4)));
typedef float f32x2 __attribute__((ext_vector_type(2)));
typedef unsigned u32x4 __attribute__((ext_vector_type(4)));
typedef unsigned u32x2 __attribute__((ext_vector_type(2)));
#define LAS __attribute__((address_space(3)))

constexpr int SEQ = 2048, DM = 1024, NH = 32, DH = 128, HD = 4096, NMEM = 8, SEG = 32, NSEG = 64, FF = 3584;
constexpr unsigned SENT = 0xFFFFFFFFu;

__device__ __forceinline__ int lane_id() { int l; asm volatile("v_mbcnt_lo_u32_b32 %0, -1, 0\n\tv_mbcnt_hi_u32_b32 %0, -1, %0" : "=v"(l)); return l; }
#define WAVE_ID wave_id_
#define opaque_tid() (WAVE_ID * 64 + lane_id())
__device__ __forceinline__ float bf2f(u16 v) { return __builtin_bit_cast(float, (unsigned)v << 16); }
__device__ __forceinline__ float bflo(unsigned v) { return __builtin_bit_cast(float, v << 16); }
__device__ __forceinline__ float bfhi(unsigned v) { return __builtin_bit_cast(float, v & 0xffff0000u); }
__device__ __forceinline__ float bfr(float x) { return (float)(__bf16)x; }
__device__ __forceinline__ unsigned pack2(float a, float b) {
  typedef __bf16 bf16x2 __attribute__((ext_vector_type(2)));
  bf16x2 v; v[0] = (__bf16)a; v[1] = (__bf16)b; return __builtin_bit_cast(unsigned, v);
}
__device__ __forceinline__ unsigned nosent(unsigned v) { return v == SENT ? 0x7FC07FC0u : v; }
__device__ __forceinline__ bf16x8 asbf8(u32x4 v) { return __builtin_bit_cast(bf16x8, v); }
__device__ __forceinline__ s16x4 ass4(u32x2 v) { return __builtin_bit_cast(s16x4, v); }
__device__ __forceinline__ u32x2 trread(const char* lds_base, unsigned off) {
  s16x4 v = __builtin_amdgcn_ds_read_tr16_b64_v4i16((LAS s16x4*)(lds_base + off));
  return __builtin_bit_cast(u32x2, v);
}
__device__ __forceinline__ void glds16(const void* gsrc, unsigned lds_dst) {
  unsigned keep;
  asm volatile("s_mov_b32 %0, m0\n\ts_mov_b32 m0, %2\n\ts_nop 0\n\tglobal_load_lds_dwordx4 %1, off\n\ts_mov_b32 m0, %0"
               : "=&s"(keep) : "v"(gsrc), "s"(lds_dst) : "memory");
}
#define CNT_ " nt"
#define WNT_ " nt"
__device__ __forceinline__ void glds16_w(const void* gsrc, unsigned lds_dst) {
  unsigned keep;
  asm volatile("s_mov_b32 %0, m0\n\ts_mov_b32 m0, %2\n\ts_nop 0\n\tglobal_load_lds_dwordx4 %1, off" CNT_ "\n\ts_mov_b32 m0, %0"
               : "=&s"(keep) : "v"(gsrc), "s"(lds_dst) : "memory");
}
__device__ __forceinline__ void glds16_sc1(const void* gsrc, unsigned lds_dst) {
  unsigned keep;
  asm volatile("s_mov_b32 %0, m0\n\ts_mov_b32 m0, %2\n\ts_nop 0\n\tglobal_load_lds_dwordx4 %1, off sc1\n\ts_mov_b32 m0, %0"
               : "=&s"(keep) : "v"(gsrc), "s"(lds_dst) : "memory");
}
#define NTM 1
template <int B> __device__ __forceinline__ u32x4 ldnt(const void* p) { if (NTM & B) return __builtin_nontemporal_load((const u32x4*)p); else return *(const u32x4*)p; }
#define MFMA32(a, b, c) __builtin_amdgcn_mfma_f32_16x16x32_bf16((a), (b), (c), 0, 0, 0)

__device__ __forceinline__ void st16_sc1(void* p, u32x4 v) { asm volatile("global_store_dwordx4 %0, %1, off sc1\n\ts_nop 1" :: "v"(p), "v"(v) : "memory"); }
__device__ __forceinline__ void st8_sc1(void* p, u32x2 v) { asm volatile("global_store_dwordx2 %0, %1, off sc1\n\ts_nop 1" :: "v"(p), "v"(v) : "memory"); }
template <bool WT> __device__ __forceinline__ void st8(void* p, u32x2 v) { if (WT) st8_sc1(p, v); else *(u32x2*)p = v; }
template <bool WT> __device__ __forceinline__ void st16(void* p, u32x4 v) { if (WT) st16_sc1(p, v); else *(u32x4*)p = v; }
__device__ __forceinline__ u32x4 ld16_sc1(const void* p) {
  u32x4 r; asm volatile("global_load_dwordx4 %0, %1, off sc1\n\ts_waitcnt vmcnt(0)" : "=&v"(r) : "v"(p) : "memory"); return r;
}
__device__ __forceinline__ void ld16x4_sc1(const void* p, u32x4& a, u32x4& b, u32x4& c, u32x4& d) {
  asm volatile("global_load_dwordx4 %0, %4, off sc1\n\tglobal_load_dwordx4 %1, %4, off offset:1024 sc1\n\t"
               "global_load_dwordx4 %2, %4, off offset:2048 sc1\n\tglobal_load_dwordx4 %3, %4, off offset:3072 sc1\n\ts_waitcnt vmcnt(0)"
               : "=&v"(a), "=&v"(b), "=&v"(c), "=&v"(d) : "v"(p) : "memory");
}
typedef __amdgpu_buffer_rsrc_t rsrc_t;
__device__ __forceinline__ rsrc_t mkrs(const void* p) { return __builtin_amdgcn_make_buffer_rsrc((void*)p, 0, 0x7FFFFFFF, 0x00020000); }
__device__ __forceinline__ u32x4 bld16(rsrc_t r, unsigned off) { return __builtin_amdgcn_raw_buffer_load_b128(r, off, 0, 16); }
__device__ __forceinline__ u32x2 bld8(rsrc_t r, unsigned off) { return __builtin_amdgcn_raw_buffer_load_b64(r, off, 0, 16); }
__device__ __forceinline__ bool ok4(u32x4 v) { return v.x != SENT && v.y != SENT && v.z != SENT && v.w != SENT; }
constexpr int SPIN_CAP = 2000000;
__device__ __forceinline__ bool give_up(int spins, unsigned* tmo, unsigned code) {
  if ((spins & 4095) != 4095) return false;
  if (spins > SPIN_CAP) { __hip_atomic_store(tmo, code, __ATOMIC_RELAXED, __HIP_MEMORY_SCOPE_AGENT); return true; }
  return __hip_atomic_load(tmo, __ATOMIC_RELAXED, __HIP_MEMORY_SCOPE_AGENT) != 0u;
}
template <bool ACQ = true>
__device__ __forceinline__ void wait_cnt(unsigned* c, unsigned n, unsigned* tmo) {
  for (int spins = 0;; ++spins) {
    const unsigned v = __hip_atomic_load(c, __ATOMIC_RELAXED, __HIP_MEMORY_SCOPE_AGENT);
    if (v >= n) break;
    __builtin_amdgcn_s_sleep(8);
    if (give_up(spins, tmo, 5)) break;
  }
  if (ACQ) {
    __builtin_amdgcn_fence(__ATOMIC_ACQUIRE, "agent");
    asm volatile("s_waitcnt vmcnt(0)" ::: "memory");
  }
}

__device__ __forceinline__ const char* uni(const char* p) {
  const unsigned long long v = (unsigned long long)p;
  const unsigned lo = __builtin_amdgcn_readfirstlane((unsigned)v), hi = __builtin_amdgcn_readfirstlane((unsigned)(v >> 32));
  return (const char*)(((unsigned long long)hi << 32) | lo);
}
#define TSCLK "s_memrealtime"
#define TC(k) do { } while (0)
#define TS(k) do { } while (0)
#define TSDECL do { } while (0)
#define TSFLUSH(mask) do { } while (0)

constexpr size_t OFF_MOD  = 0;
constexpr size_t OFF_MOD2 = 65536;
constexpr size_t OFF_TMO  = 98304;
constexpr size_t OFF_EP   = 98304 + 256;
constexpr size_t OFF_CLM  = 102400;
constexpr size_t OFF_INI  = 131072;
constexpr size_t OFF_H1   = 262144;
constexpr size_t OFF_Q    = OFF_H1 + (size_t)SEQ * DM * 2;
constexpr size_t OFF_K    = OFF_Q + (size_t)SEQ * HD * 2;
constexpr size_t OFF_V    = OFF_K + (size_t)SEQ * HD * 2;
constexpr size_t OFF_O    = OFF_V + (size_t)SEQ * HD * 2;
constexpr size_t OFF_X1   = OFF_O + (size_t)SEQ * HD * 2;
constexpr size_t OFF_H2   = OFF_X1 + (size_t)SEQ * DM * 2;
constexpr size_t OFF_G    = OFF_H2 + (size_t)SEQ * DM * 2;
constexpr size_t OFF_XCH  = (size_t)100 << 20;
constexpr size_t OFF_P    = (size_t)112 << 20;
constexpr size_t OFF_P2   = (size_t)128 << 20;
constexpr size_t OFF_DUMP = (size_t)144 << 20;
constexpr int PROW = 4 * DM * 2;
#define TLQ 1
#define TLP 1
static_assert(OFF_G + (size_t)SEQ * FF * 2 <= OFF_XCH, "layout");
__device__ __forceinline__ char* xch_base(char* ws) { return (char*)(((size_t)(ws + OFF_XCH) + ((2u << 20) - 1)) & ~(size_t)((2u << 20) - 1)); }
constexpr int PROG = 16;
constexpr int PQKV = 64;
constexpr int PL1 = 96;
constexpr int PARM = 128;
constexpr int PWO = 192;
constexpr int PLN = 224;
constexpr int PF2 = 320;
constexpr int PF1 = 448;
#define BEARLY 58
constexpr int L_RED = 131072;
constexpr int L_SLOT = L_RED + 16384;
constexpr int L_GB = L_SLOT + 1024;
constexpr int MEGA_LDS = 163840;
static_assert(BEARLY % 2 == 0, "");
static_assert(L_GB + 12288 + 2048 <= MEGA_LDS, "lds");
__device__ __forceinline__ int bswz(int row) { return (((row >> 1) & 1) | (((row >> 3) & 1) << 1)) << 1; }
template <int OFF, bool SC1 = false>
__device__ __forceinline__ void glds16s(const char* sbase, unsigned voff, unsigned lds_base) {
  unsigned keep;
  if (SC1)
    asm volatile("s_mov_b32 %0, m0\n\ts_add_u32 m0, %3, %4\n\ts_nop 0\n\tglobal_load_lds_dwordx4 %1, %2 sc1\n\ts_mov_b32 m0, %0"
                 : "=&s"(keep) : "v"(voff), "s"(sbase), "s"(lds_base), "i"(OFF) : "memory", "scc");
  else
    asm volatile("s_mov_b32 %0, m0\n\ts_add_u32 m0, %3, %4\n\ts_nop 0\n\tglobal_load_lds_dwordx4 %1, %2\n\ts_mov_b32 m0, %0"
                 : "=&s"(keep) : "v"(voff), "s"(sbase), "s"(lds_base), "i"(OFF) : "memory", "scc");
}
template <int OFF>
__device__ __forceinline__ void glds16x2s(const char* sbase, unsigned voff, unsigned lds_base) {
  unsigned keep;
  asm volatile("s_mov_b32 %0, m0\n\ts_add_u32 m0, %3, %4\n\ts_nop 0\n\tglobal_load_lds_dwordx4 %1, %2 sc1\n\tglobal_load_lds_dwordx4 %1, %2 offset:1024 sc1\n\ts_mov_b32 m0, %0"
               : "=&s"(keep) : "v"(voff), "s"(sbase), "s"(lds_base), "i"(OFF) : "memory", "scc");
}
__device__ __forceinline__ void st16s(const char* sb, unsigned vo, u32x4 v) { asm volatile("global_store_dwordx4 %0, %1, %2 sc1\n\ts_nop 1" :: "v"(vo), "v"(v), "s"(sb) : "memory"); }
__device__ __forceinline__ void head_to_lds(const char* src, int lane, unsigned lds) {
  if (TLQ) {
    const int c = (lane & 15) ^ (lane >> 4);
    const unsigned v0 = (unsigned)((c >> 2) * 2048 + (lane >> 4) * 64 + (c & 3) * 16);
#pragma unroll
    for (int i = 0; i < 8; ++i) glds16s<0, true>(src + i * 256, v0 ^ ((i & 3) * 2048), lds + i * 1024);
  } else {
#pragma unroll
    for (int i = 0; i < 4; ++i) glds16x2s<0>(src + i * 2048, (unsigned)lane * 16, lds + i * 2048);
  }
}
#define WAIT_V(n) asm volatile("s_waitcnt vmcnt(" #n ")" ::: "memory")
#define BAR() do { asm volatile("s_waitcnt lgkmcnt(0)" ::: "memory"); __builtin_amdgcn_s_barrier(); asm volatile("" ::: "memory"); } while (0)
__device__ __forceinline__ void st4_sc1(void* p, unsigned v) { asm volatile("global_store_dword %0, %1, off sc1" :: "v"(p), "v"(v) : "memory"); }
__device__ __forceinline__ unsigned ld4_sc1(const void* p) {
  unsigned r; asm volatile("global_load_dword %0, %1, off sc1\n\ts_waitcnt vmcnt(0)" : "=&v"(r) : "v"(p) : "memory"); return r;
}
template <int CTRL> __device__ __forceinline__ unsigned dppu(unsigned x) { return (unsigned)__builtin_amdgcn_update_dpp(0, (int)x, CTRL, 0xf, 0xf, false); }
__device__ __forceinline__ unsigned rowmin16(unsigned v) {
  v = min(v, dppu<0x128>(v)); v = min(v, dppu<0x124>(v)); v = min(v, dppu<0x122>(v)); v = min(v, dppu<0x121>(v));
  return v;
}
__device__ __forceinline__ unsigned rdl(unsigned v, int l) { return (unsigned)__builtin_amdgcn_readlane((int)v, l); }
constexpr float SCALE = 0.08838834764831845f;
constexpr float LOG2E = 1.4426950408889634f;
template <int N> __device__ __forceinline__ void delay64() {
  if (N >= 16) { __builtin_amdgcn_s_sleep(15); delay64<(N >= 16 ? N - 15 : 0)>(); } else if (N > 0) __builtin_amdgcn_s_sleep(N);
}
__device__ __forceinline__ void sw16(float x, float& a, float& b) {
  a = x; b = x; asm volatile("s_nop 1\n\tv_permlane16_swap_b32 %0, %1" : "+v"(a), "+v"(b));
}
__device__ __forceinline__ void sw32(float x, float& a, float& b) {
  a = x; b = x; asm volatile("s_nop 1\n\tv_permlane32_swap_b32 %0, %1" : "+v"(a), "+v"(b));
}
__device__ __forceinline__ float red16_32_max(float x) {
  float a, b; sw16(x, a, b); x = fmaxf(a, b); sw32(x, a, b); return fmaxf(a, b);
}
__device__ __forceinline__ float red16_32_add(float x) {
  float a, b; sw16(x, a, b); x = a + b; sw32(x, a, b); return a + b;
}
__device__ __forceinline__ unsigned cvtpk(float a, float b) { unsigned r; asm("v_cvt_pk_bf16_f32 %0, %1, %2" : "=v"(r) : "v"(a), "v"(b)); return r; }
__device__ __forceinline__ float max3(float a, float b, float c) { float r; asm("v_max3_f32 %0, %1, %2, %3" : "=v"(r) : "v"(a), "v"(b), "v"(c)); return r; }
__device__ __forceinline__ float max2(float a, float b) { float r; asm("v_max_f32 %0, %1, %2" : "=v"(r) : "v"(a), "v"(b)); return r; }
__device__ __forceinline__ unsigned ror8u(unsigned x) { return (unsigned)__builtin_amdgcn_update_dpp(0, (int)x, 0x128, 0xf, 0xf, false); }
__device__ __forceinline__ float ror8(float x) { return __builtin_bit_cast(float, ror8u(__builtin_bit_cast(unsigned, x))); }
__device__ __forceinline__ float add_ror8(float x) {
  return x + __builtin_bit_cast(float, __builtin_amdgcn_update_dpp(0, __builtin_bit_cast(int, x), 0x128, 0xf, 0xf, false));
}

#define RSL 4
__device__ __forceinline__ void ini_request(const char* ws, unsigned lds) {
  const char* p = uni(ws + OFF_INI);
  asm volatile("s_mov_b32 m0, %2\n\ts_nop 3\n\tglobal_load_lds_dword %0, %1 sc1\n\ts_add_u32 m0, %2, 252\n\ts_nop 0\n\tglobal_load_lds_dword %0, %1 offset:4 sc1"
               :: "v"(lane_id() * 128), "s"(p), "s"(lds) : "memory", "m0", "scc");
}
__device__ __forceinline__ unsigned ini_resolve(const char* ws, const char* l, unsigned long long tag) {
  const int lane = lane_id();
  u32x2 v = u32x2{~(unsigned)tag, 0u};
  if (l) v = u32x2{*(const volatile LAS unsigned*)(l + lane * 4), *(const volatile LAS unsigned*)(l + 256 + lane * 4)};
  for (int spins = 0; !__all(v.x == (unsigned)tag && (v.y >> 16) == (unsigned)(tag >> 48)) && spins < SPIN_CAP; ++spins) {
    if (spins) __builtin_amdgcn_s_sleep(RSL);
    asm volatile("global_load_dwordx2 %0, %1, off sc1\n\ts_waitcnt vmcnt(0)" : "=&v"(v) : "v"(ws + OFF_INI + lane * 128) : "memory");
  }
  const unsigned long long up = __ballot((v.y & 3u) == 1u), dn = __ballot((v.y & 3u) == 2u);
  unsigned par = 0;
#pragma unroll
  for (int c = 0; c < 3; ++c) { const unsigned long long m = (0x249ull << c) * 0x0001000100010001ull; par |= (__popcll(up & m) > __popcll(dn & m) ? 0u : 1u) << c; }
  par = __builtin_amdgcn_readfirstlane(par);
  asm volatile("" : "+s"(par));
  return par;
}
__device__ __forceinline__ unsigned ini_resolve1(char* smem, int wave_id_, const char* ws, unsigned long long tag) {
  volatile LAS unsigned* w = (volatile LAS unsigned*)(smem + L_SLOT + 800);
  if (WAVE_ID == 1) *w = ini_resolve(ws, nullptr, tag);
  BAR();
  return __builtin_amdgcn_readfirstlane(*w);
}
template <int M0> __device__ __forceinline__ unsigned qkv_avail(const unsigned* line) {
  const unsigned v = ld4_sc1(line + (lane_id() & 15));
  unsigned r = NSEG;
#pragma unroll
  for (int m = M0; m < 3; ++m) {
    unsigned a = rdl(v, m);
#pragma unroll
    for (int p = 1; p < 3; ++p) { const unsigned w = rdl(v, 4 * p + m); if (a >= (w >> 8)) a = max(a, w & 255u); }
    r = min(r, a);
  }
  return r;
}
__device__ __forceinline__ void wait_l1(const unsigned* cnt, unsigned n, unsigned* tmo) {
  const int lane = lane_id();
  for (int spins = 0;; ++spins) {
    const unsigned v = ld4_sc1(cnt + (PL1 + (lane & 31)) * 32);
    if (__all(v >= n)) break;
    __builtin_amdgcn_s_sleep(8);
    if (give_up(spins, tmo, 12)) break;
  }
}
constexpr int NARM = 8;
__device__ __forceinline__ void wait_arm(const unsigned* cnt, unsigned* tmo) {
  for (int spins = 0;; ++spins) {
    if (__all(ld4_sc1(cnt + (PARM + min(lane_id(), NARM - 1)) * 32) != 0u)) break;
    __builtin_amdgcn_s_sleep(8);
    if (give_up(spins, tmo, 12)) break;
  }
}
#define GKV 1
__device__ __forceinline__ void wait_k0(const unsigned* cnt, int h, unsigned* tmo, bool kv = true) {
  for (int spins = 0;; ++spins) {
    const int l = lane_id();
    const unsigned v = ld4_sc1(cnt + (PQKV + h) * 32 + (l == 0 || !kv ? 3 : 1 + (l & 1)));
    if (__all(v >= (l == 0 ? 1u : (unsigned)GKV))) break;
    __builtin_amdgcn_s_sleep(32);
    if (give_up(spins, tmo, 13)) break;
  }
}
#define IS(k) do { } while (0)
#define KTON 0
#define WCLK(slot, b) do { } while (0)
#define WTM(slot, b, k) do { } while (0)
#define WTS(slot, b, k) do { } while (0)
#define KTS(k_) do { } while (0)
#define STS(ws_, slot_, k_) do { } while (0)
typedef __bf16 bf16x2 __attribute__((ext_vector_type(2)));
__device__ __forceinline__ f32x2 pkfma(f32x2 a, f32x2 b, f32x2 c) { return __builtin_elementwise_fma(a, b, c); }
__device__ __forceinline__ float wavesum(float v) {
  v += __builtin_bit_cast(float, dppu<0x128>(__builtin_bit_cast(unsigned, v))); v += __builtin_bit_cast(float, dppu<0x124>(__builtin_bit_cast(unsigned, v)));
  v += __builtin_bit_cast(float, dppu<0x122>(__builtin_bit_cast(unsigned, v))); v += __builtin_bit_cast(float, dppu<0x121>(__builtin_bit_cast(unsigned, v)));
  return red16_32_add(v);
}
__device__ __forceinline__ void bst16(rsrc_t r, unsigned off, unsigned soff, u32x4 v) { __builtin_amdgcn_raw_buffer_store_b128(v, r, off + soff, 0, 16); }
__device__ __forceinline__ float adds(unsigned v, unsigned sel, float c) { return __builtin_amdgcn_fdot2_f32_bf16(__builtin_bit_cast(bf16x2, v), __builtin_bit_cast(bf16x2, sel), c, false); }
__device__ __forceinline__ void sum4(const u32x4 (&p)[4], f32x2 (&a)[4]) {
  unsigned slo = 0x00003F80u, shi = 0x3F800000u;
  asm("" : "+s"(slo), "+s"(shi));
#pragma unroll
  for (int e = 0; e < 4; ++e) {
    float lo = bflo(p[0][e]), hi = bfhi(p[0][e]);
#pragma unroll
    for (int q = 1; q < 4; ++q) { lo = adds(p[q][e], slo, lo); hi = adds(p[q][e], shi, hi); }
    a[e] = f32x2{lo, hi};
  }
}
__device__ __forceinline__ u32x4 norm8(const f32x2 (&v)[4], const char* mt, float mb, float rden) {
  const f32x4 h0 = *(const f32x4*)(mt + 4096), h1 = *(const f32x4*)(mt + 4096 + 1024), c0 = *(const f32x4*)(mt + 8192), c1 = *(const f32x4*)(mt + 8192 + 1024);
  const f32x2 m2 = f32x2{mb, mb}, d2 = f32x2{rden, rden};
  const f32x2 r0 = pkfma(v[0] - m2, f32x2{c0[0], c0[1]} * d2, f32x2{h0[0], h0[1]}), r1 = pkfma(v[1] - m2, f32x2{c0[2], c0[3]} * d2, f32x2{h0[2], h0[3]});
  const f32x2 r2 = pkfma(v[2] - m2, f32x2{c1[0], c1[1]} * d2, f32x2{h1[0], h1[1]}), r3 = pkfma(v[3] - m2, f32x2{c1[2], c1[3]} * d2, f32x2{h1[2], h1[3]});
  return u32x4{pack2(r0[0], r0[1]), pack2(r1[0], r1[1]), pack2(r2[0], r2[1]), pack2(r3[0], r3[1])};
}
__device__ __forceinline__ bool fin_half(const char* P, const u16* res, const char* gbt, u16* __restrict__ o1, int row, int hf, int lane, bool chk) {
  const rsrc_t rp = mkrs(P), rr = mkrs(res);
  const unsigned po = (unsigned)((row >> 5) * 32 * PROW + (hf * 16 + (lane >> 2)) * 2048 + (row & 31) * 64 + (lane & 3) * 16), ro = (unsigned)(row * DM * 2 + hf * 1024 + lane * 16);
  u32x4 p[4];
#pragma unroll
  for (int q = 0; q < 4; ++q) p[q] = bld16(rp, po + q * 65536);
  const u32x4 rv = bld16(rr, ro);
  if (chk && !__all(ok4(p[0]) && ok4(p[1]) && ok4(p[2]) && ok4(p[3]) && ok4(rv))) return false;
  f32x2 a[4];
  sum4(p, a);
  const u32x4 gv = *(const u32x4*)(gbt + hf * 1024 + lane * 16);
  u32x4 o;
#pragma unroll
  for (int e = 0; e < 4; ++e) { const f32x2 s = pkfma(f32x2{bflo(gv[e]), bfhi(gv[e])}, a[e], f32x2{bflo(rv[e]), bfhi(rv[e])}); o[e] = pack2(s[0], s[1]); }
  st16_sc1(o1 + (size_t)row * DM + hf * 512 + lane * 8, o);
  return true;
}
__device__ __forceinline__ void ln_half(const u32x4 (&p)[4], u32x4 rv, const f32x4 (&tb)[6], volatile LAS float* xs, const char* x1, unsigned xo, const char* h2, unsigned ho,
                                        int wave, int lane) {
  f32x2 a[4], v[4];
  sum4(p, a);
  u32x4 o;
#pragma unroll
  for (int e = 0; e < 4; ++e) {
    const f32x2 s = pkfma(f32x2{tb[e >> 1][2 * (e & 1)], tb[e >> 1][2 * (e & 1) + 1]}, a[e], f32x2{bflo(rv[e]), bfhi(rv[e])});
    o[e] = pack2(s[0], s[1]);
    v[e] = f32x2{bflo(o[e]), bfhi(o[e])};
  }
  const float c = __builtin_bit_cast(float, __builtin_amdgcn_readfirstlane(__builtin_bit_cast(int, v[0][0])));
  f32x2 s2 = f32x2{0, 0}, q2 = s2;
#pragma unroll
  for (int i = 0; i < 4; ++i) { const f32x2 d = v[i] - f32x2{c, c}; s2 += d; q2 = pkfma(d, d, q2); }
  float s = s2[0] + s2[1], q = q2[0] + q2[1];
#define DA(x, k) x += __builtin_bit_cast(float, dppu<k>(__builtin_bit_cast(unsigned, x)))
  DA(s, 0x128); DA(q, 0x128); DA(s, 0x124); DA(q, 0x124); DA(s, 0x122); DA(q, 0x122); DA(s, 0x121); DA(q, 0x121);
#undef DA
  {
    asm volatile("s_nop 1\n\tv_permlane16_swap_b32 %0, %1" : "+v"(s), "+v"(q));
    s += q; q = s;
    asm volatile("s_nop 1\n\tv_permlane32_swap_b32 %0, %1" : "+v"(s), "+v"(q));
    s += q;
  }
  if (lane < 48 && (lane & 15) == 0) xs[wave * 4 + (lane >> 4)] = lane == 32 ? c : s;
  st16s(x1, xo, o);
  BAR();
  const f32x4 u0 = *(const volatile LAS f32x4*)(xs + (wave & 6) * 4), u1 = *(const volatile LAS f32x4*)(xs + (wave | 1) * 4);
  const float m0 = u0[0] * (2.0f / DM), m1 = u1[0] * (2.0f / DM);
  const float dm = (u0[2] - u1[2]) + (m0 - m1);
  const float mean = ((u0[2] + m0) + (u1[2] + m1)) * 0.5f;
  const float qq = ((u0[1] - u0[0] * m0) + (u1[1] - u1[0] * m1)) + dm * dm * (DM / 4);
  const float mb = bfr(mean), vb = bfr(fmaxf(qq, 0.0f) * (1.0f / DM));
  const float rden = __builtin_amdgcn_rcpf(bfr(__builtin_amdgcn_sqrtf(bfr(vb + 1e-5f))));
  const f32x2 m2 = f32x2{mb, mb}, d2 = f32x2{rden, rden};
  u32x4 r;
#pragma unroll
  for (int e = 0; e < 4; ++e) {
    const f32x2 t = pkfma(v[e] - m2, f32x2{tb[4 + (e >> 1)][2 * (e & 1)], tb[4 + (e >> 1)][2 * (e & 1) + 1]} * d2, f32x2{tb[2 + (e >> 1)][2 * (e & 1)], tb[2 + (e >> 1)][2 * (e & 1) + 1]});
    r[e] = pack2(t[0], t[1]);
  }
  st16s(h2, ho, r);
}
__device__ __forceinline__ void row_table(char* tab, const float* mod, const unsigned* cnt, unsigned* tmo, int tid) {
  wait_l1(cnt, 9u, tmo);
  const rsrc_t rm = mkrs((const char*)mod + OFF_MOD2);
#pragma unroll
  for (int i = 0; i < 2; ++i) {
    const int u = tid + 512 * i, a = u >> 8, c = u & 255;
    f32x4 v = __builtin_bit_cast(f32x4, bld16(rm, (unsigned)u * 16));
    v = f32x4{bfr(v[0]), bfr(v[1]), bfr(v[2]), bfr(v[3])};
    if (a == 2) v = f32x4{bfr(1.0f + v[0]), bfr(1.0f + v[1]), bfr(1.0f + v[2]), bfr(1.0f + v[3])};
    if (a < 3) *(f32x4*)(tab + a * 4096 + ((c >> 7) * 2 + (c & 1)) * 1024 + ((c >> 1) & 63) * 16) = v;
    else *(u32x2*)(tab + 12288 + c * 8) = u32x2{pack2(v[0], v[1]), pack2(v[2], v[3])};
  }
  __syncthreads();
}
#define LS(i) do { } while (0)
#define LNSL 2
__device__ __forceinline__ void row_role(char* smem, int wave_id_, int grp, int set, const float* mod, const char* P, const u16* x, u16* x1, char* h2c,
                                         unsigned* cnt, unsigned* tmo, int slot, unsigned long long* mts) {
  const int tid = opaque_tid(), lane = tid & 63, wave = WAVE_ID;
  if (tid < 2) *(volatile LAS unsigned*)(smem + 16384 + 256 + tid * 4) = 0u;
  row_table(smem, mod, cnt, tmo, tid);
  static_assert(TLP, "");
  const int hf = wave & 1, r4 = 4 * grp + (wave >> 1);
  const unsigned pw2 = (unsigned)(PWO + ((lane >> 2) & 3) * 8 + hf * 4 + (lane & 3)) * 128 + ((lane >> 4) & 1) * 4;
  auto rdy = [&](unsigned v, int bb, bool early) { const unsigned long long m = __ballot(v > (unsigned)bb); return ((early ? m | m >> 16 : m) & 0xFFFFull) == 0xFFFFull; };
  unsigned* myp = cnt + (PLN + 8 * set + grp) * 32;
  unsigned pv = 0;
  u32x4 p[4], rv, rn;
  bool req = false;
  const char* pb = uni(P); const char* cb = uni((const char*)cnt); const char* xs_ = uni((const char*)x);
  auto publish = [&](int n, int e = 0) {
    if (lane == 0 && (__hip_atomic_fetch_add((LAS unsigned*)(smem + 16384 + 256 + 4 * e), 1u, __ATOMIC_RELAXED, __HIP_MEMORY_SCOPE_WORKGROUP) & 7u) == 7u) {
      if (e) st8_sc1(myp, u32x2{(unsigned)n - 1u, (unsigned)n}); else st4_sc1(myp, (unsigned)n);
    }
  };
  f32x4 tb[6];
#pragma unroll
  for (int i = 0; i < 6; ++i) tb[i] = *(const f32x4*)(smem + (i >> 1) * 4096 + hf * 2048 + (i & 1) * 1024 + lane * 16);
  const unsigned po = (unsigned)((hf * 16 + (lane >> 2)) * 2048 + r4 * 64 + (lane & 3) * 16), xo = (unsigned)(r4 * DM * 2 + hf * 1024 + lane * 16);
  const unsigned ho = (unsigned)(r4 * 128 + (((lane & 7) ^ (r4 & 7)) << 4) + (lane >> 3) * 4096 + hf * 32768);
  rn = *(const u32x4*)(xs_ + set * 65536 + xo);
  for (int bb = set; bb < NSEG; bb += 2) {
    for (bool early = true;; early = false) {
      for (int spins = 0; !rdy(pv, bb, early); ++spins) {
        pv = ld4_sc1((const char*)cnt + pw2);
        if (rdy(pv, bb, early)) break;
        __builtin_amdgcn_s_sleep(LNSL);
        if (give_up(spins, tmo, 11)) break;
      }
      WTM(slot, bb, 0);
      LS(0);
      asm volatile("s_nop 4\n\tglobal_load_dwordx4 %0, %5, %9 sc1\n\tglobal_load_dwordx4 %1, %6, %9 sc1\n\tglobal_load_dwordx4 %2, %7, %9 sc1\n\tglobal_load_dwordx4 %3, %8, %9 sc1\n\t"
                   "global_load_dword %4, %10, %11 sc1\n\ts_waitcnt vmcnt(0)"
                   : "=&v"(p[0]), "=&v"(p[1]), "=&v"(p[2]), "=&v"(p[3]), "=&v"(pv)
                   : "v"(po), "v"(po + 65536), "v"(po + 131072), "v"(po + 196608), "s"(pb + (size_t)bb * 32 * PROW), "v"(pw2), "s"(cb) : "memory");
      if (bb < BEARLY || !early || __all(ok4(p[0]) && ok4(p[1]) && ok4(p[2]) && ok4(p[3]))) break;
    }
    asm volatile("v_mov_b32 %0, %4\n\tv_mov_b32 %1, %5\n\tv_mov_b32 %2, %6\n\tv_mov_b32 %3, %7" : "=&v"(rv.x), "=&v"(rv.y), "=&v"(rv.z), "=&v"(rv.w) : "v"(rn.x), "v"(rn.y), "v"(rn.z), "v"(rn.w) : "memory");
    if (bb + 2 < NSEG) rn = *(const u32x4*)(xs_ + (size_t)(bb + 2) * 65536 + xo);
    LS(1);
    if (req && bb < BEARLY) publish(bb >> 1);
    ln_half(p, rv, tb, (volatile LAS float*)(smem + 16384 + ((bb >> 1) & 1) * 128), uni((const char*)x1) + (size_t)bb * 65536, xo, uni(h2c) + (size_t)bb * 65536, ho, wave, lane);
    LS(2);
    if (bb >= BEARLY) publish((bb >> 1) + 1, 1);
    req = bb + 2 < NSEG && (bb >= BEARLY || rdy(pv, bb + 2, true));
    if (!req) {
      if (bb + 2 < NSEG) pv = ld4_sc1((const char*)cnt + pw2);
      if (bb >= BEARLY) BAR();
      WAIT_V(0);
      LS(3);
      publish((bb >> 1) + 1);
    }
    WTM(slot, bb, 2);
  }
}
#define L1G 0
__device__ __forceinline__ void l1_role(char* smem, int wave_id_, int c, const u16* __restrict__ x, const float* __restrict__ mod, const u16* __restrict__ cvec,
                                        const u16* __restrict__ wmod, char* __restrict__ h1c, unsigned*& cnt, unsigned* tmo, unsigned long long* mts, unsigned long long tag) {
  const int tid = opaque_tid(), lane = tid & 63, wave = WAVE_ID;
  u32x4 xr[8][2];
  const u16* src = x + (size_t)(8 * c + wave) * DM + lane * 8;
  xr[0][0] = ldnt<2>(src); xr[0][1] = ldnt<2>(src + 512);
  {
    const float sva = bf2f(cvec[tid]), svb = bf2f(cvec[tid + 512]);
    cnt += (ini_resolve1(smem, wave_id_, (const char*)mod - OFF_MOD, tag) >> 2) << 11;
    if (wave == 7) IS(6);
    f32x4 m[4];
    const rsrc_t rm = mkrs(mod);
#pragma unroll
    for (int k = 0; k < 4; ++k) m[k] = __builtin_bit_cast(f32x4, bld16(rm, (unsigned)(k * (2 * DM) + tid * 4) * 4));
    f32x4 v = (m[0] + m[1]) + (m[2] + m[3]);
    v = f32x4{bfr(v[0]), bfr(v[1]), bfr(v[2]), bfr(v[3])};
    const int a = tid >> 8, cc = tid & 255;
    if (a) v = f32x4{bfr(1.0f + v[0]), bfr(1.0f + v[1]), bfr(1.0f + v[2]), bfr(1.0f + v[3])};
    *(f32x4*)(smem + 4096 + a * 4096 + ((cc >> 7) * 2 + (cc & 1)) * 1024 + ((cc >> 1) & 63) * 16) = v;
    float* sv = (float*)(smem + 12288);
    sv[tid] = bfr(sva / (1.0f + __expf(-sva))); sv[tid + 512] = bfr(svb / (1.0f + __expf(-svb)));
  }
  __syncthreads();
  if (wave == 7) IS(7);
#pragma unroll
  for (int p = 0; p < 8; ++p) {
    f32x2 v[2][4];
#pragma unroll
    for (int i = 0; i < 2; ++i)
#pragma unroll
      for (int e = 0; e < 4; ++e) v[i][e] = f32x2{bflo(xr[p][i][e]), bfhi(xr[p][i][e])};
    const f32x2 s2 = ((v[0][0] + v[0][1]) + (v[0][2] + v[0][3])) + ((v[1][0] + v[1][1]) + (v[1][2] + v[1][3]));
    const float mean = wavesum(s2[0] + s2[1]) * (1.0f / DM);
    f32x2 q2 = f32x2{0, 0};
#pragma unroll
    for (int i = 0; i < 8; ++i) { const f32x2 d = v[i >> 2][i & 3] - f32x2{mean, mean}; q2 = pkfma(d, d, q2); }
    const float q = wavesum(q2[0] + q2[1]);
    if (p == 0 && wave == 0) STS((char*)tmo - OFF_TMO, 128 + (c >> 3) * 32 + 24 + (c & 7), 0);
    const float mb = bfr(mean), vb = bfr(q * (1.0f / DM));
    const float rden = 1.0f / bfr(sqrtf(bfr(vb + 1e-5f)));
    const int row = 256 * p + 8 * c + wave, rb = row & 31;
    char* dst = h1c + (size_t)(row >> 5) * 65536 + (lane >> 3) * 4096 + rb * 128 + (((lane & 7) ^ (rb & 7)) << 4);
    const char* ml = smem + lane * 16;
#pragma unroll
    for (int i = 0; i < 2; ++i) st16_sc1(dst + i * 32768, norm8(v[i], ml + i * 2048, mb, rden));
    WAIT_V(0);
    __builtin_amdgcn_s_barrier();
    if (tid == 0) st4_sc1(cnt + (PL1 + c) * 32, (unsigned)(p + 1));
    if (p == 0) {
      if (L1G) wait_k0(cnt, c, tmo, L1G == 1);
#pragma unroll
      for (int pp = 1; pp < 8; ++pp) { xr[pp][0] = ldnt<2>(src + (size_t)pp * 256 * DM); xr[pp][1] = ldnt<2>(src + (size_t)pp * 256 * DM + 512); }
    }
    if (wave == 0) WTS(c, p, 0);
    if (p == 0 && wave == 0) STS((char*)tmo - OFF_TMO, 128 + (c >> 3) * 32 + 24 + (c & 7), 1);
  }
  wait_k0(cnt, c, tmo);
  {
    const int cg = tid & 15, r0 = tid >> 4;
    const u16* wp = wmod + (size_t)r0 * (6 * DM) + 2 * DM + 128 * c + cg * 8;
    const float* sv = (const float*)(smem + 12288) + r0;
    f32x2 acc[4] = {f32x2{0, 0}, f32x2{0, 0}, f32x2{0, 0}, f32x2{0, 0}};
#pragma unroll 1
    for (int o = 0; o < 4; ++o) {
      u32x4 w[8];
#pragma unroll
      for (int i = 0; i < 8; ++i) w[i] = ldnt<1>(wp + (size_t)(o * 8 + i) * 32 * (6 * DM));
#pragma unroll
      for (int i = 0; i < 8; ++i) {
        const float s = sv[(o * 8 + i) * 32];
#pragma unroll
        for (int e = 0; e < 4; ++e) acc[e] = pkfma(f32x2{s, s}, f32x2{bflo(w[i][e]), bfhi(w[i][e])}, acc[e]);
      }
    }
    float* red = (float*)(smem + 16384);
    *(f32x4*)(red + r0 * 128 + cg * 8) = f32x4{acc[0][0], acc[0][1], acc[1][0], acc[1][1]};
    *(f32x4*)(red + r0 * 128 + cg * 8 + 4) = f32x4{acc[2][0], acc[2][1], acc[3][0], acc[3][1]};
    __syncthreads();
    if (tid < 128) {
      float s = 0;
#pragma unroll
      for (int r = 0; r < 32; ++r) s += red[r * 128 + tid];
      st4_sc1((char*)mod + OFF_MOD2 + (128 * c + tid) * 4, __builtin_bit_cast(unsigned, s));
    }
    WAIT_V(0);
    __builtin_amdgcn_s_barrier();
    if (tid == 0) st4_sc1(cnt + (PL1 + c) * 32, 9u);
  }
}
enum { R_QKV = 0, R_PART = 1, R_F1 = 2, R_PART2 = 3 };
struct Dep {
  const char* base; unsigned voff; unsigned mul, add;
};
__device__ __forceinline__ unsigned dep_eval(unsigned v, const Dep& d) {
  v = rowmin16(v * d.mul + d.add);
  return min(min(rdl(v, 0), rdl(v, 16)), min(rdl(v, 32), rdl(v, 48)));
}
typedef unsigned u32x32 __attribute__((ext_vector_type(32)));
typedef unsigned u32x16 __attribute__((ext_vector_type(16)));
__device__ __forceinline__ void fast_ld0(const u32x32 (&W)[4], u32x16& pd, unsigned ea, unsigned da, int& b, int& lf, int cp, unsigned sa, unsigned mu, unsigned ad,
    unsigned rw, unsigned ab, const char* p1, const char* p0, unsigned l1, unsigned l0, const unsigned (&av)[8]) {
  asm volatile("s_setprio 0\n\t"
    "s_waitcnt vmcnt(12)\n\t"
    "s_barrier\n\t"
    "ds_read_b128 v[96:99], %[ea] offset:0\n\t"
    "ds_read_b128 v[100:103], %[ea] offset:2048\n\t"
    "ds_read_b128 v[104:107], %[da] offset:0\n\t"
    "ds_read_b128 v[108:111], %[da] offset:2048\n\t"
    "ds_read_b128 v[112:115], %[ea] offset:4096\n\t"
    "ds_read_b128 v[116:119], %[ea] offset:6144\n\t"
    "s_branch 2f\n\t"
    "1:\n\t"
    "s_waitcnt vmcnt(12)\n\t"
    "s_nop 1\n\t"
    "s_barrier\n\t"
    "2:\n\t"
    "s_waitcnt lgkmcnt(4)\n\t"
    "v_mfma_f32_16x16x32_bf16 v[64:67], v[128:131], v[96:99], 0\n\t"
    "ds_read_b128 v[120:123], %[da] offset:4096\n\t"
    "s_add_u32 m0, %[l1], 0\n\t"
    "ds_write_b128 %[rw], v[80:83] offset:0\n\t"
    "v_mfma_f32_16x16x32_bf16 v[68:71], v[132:135], v[96:99], 0\n\t"
    "ds_read_b128 v[124:127], %[da] offset:6144\n\t"
    "global_load_lds_dwordx4 %[av0], s[36:37] sc1\n\t"
    "ds_write_b128 %[rw], v[84:87] offset:1024\n\t"
    "v_mfma_f32_16x16x32_bf16 v[72:75], v[128:131], v[100:103], 0\n\t"
    "s_add_u32 m0, %[l1], 32768\n\t"
    "ds_write_b128 %[rw], v[88:91] offset:2048\n\t"
    "v_mfma_f32_16x16x32_bf16 v[76:79], v[132:135], v[100:103], 0\n\t"
    "global_load_lds_dwordx4 %[av1], s[36:37] sc1\n\t"
    "ds_write_b128 %[rw], v[92:95] offset:3072\n\t"
    "s_waitcnt lgkmcnt(8)\n\t"
    "v_mfma_f32_16x16x32_bf16 v[64:67], v[136:139], v[104:107], v[64:67]\n\t"
    "ds_read_b128 v[96:99], %[ea] offset:8192\n\t"
    "s_add_u32 m0, %[l1], 4096\n\t"
    "v_mfma_f32_16x16x32_bf16 v[68:71], v[140:143], v[104:107], v[68:71]\n\t"
    "ds_read_b128 v[100:103], %[ea] offset:10240\n\t"
    "global_load_lds_dwordx4 %[av2], s[36:37] sc1\n\t"
    "v_mfma_f32_16x16x32_bf16 v[72:75], v[136:139], v[108:111], v[72:75]\n\t"
    "s_add_u32 m0, %[l1], 36864\n\t"
    "v_mfma_f32_16x16x32_bf16 v[76:79], v[140:143], v[108:111], v[76:79]\n\t"
    "global_load_lds_dwordx4 %[av3], s[36:37] sc1\n\t"
    "s_waitcnt lgkmcnt(8)\n\t"
    "v_mfma_f32_16x16x32_bf16 v[64:67], v[144:147], v[112:115], v[64:67]\n\t"
    "ds_read_b128 v[104:107], %[da] offset:8192\n\t"
    "s_add_u32 m0, %[l1], 8192\n\t"
    "v_mfma_f32_16x16x32_bf16 v[68:71], v[148:151], v[112:115], v[68:71]\n\t"
    "ds_read_b128 v[108:111], %[da] offset:10240\n\t"
    "global_load_lds_dwordx4 %[av4], s[36:37] sc1\n\t"
    "v_mfma_f32_16x16x32_bf16 v[72:75], v[144:147], v[116:119], v[72:75]\n\t"
    "s_add_u32 m0, %[l1], 40960\n\t"
    "v_mfma_f32_16x16x32_bf16 v[76:79], v[148:151], v[116:119], v[76:79]\n\t"
    "global_load_lds_dwordx4 %[av5], s[36:37] sc1\n\t"
    "s_waitcnt lgkmcnt(7)\n\t"
    "v_mfma_f32_16x16x32_bf16 v[64:67], v[152:155], v[120:123], v[64:67]\n\t"
    "ds_read_b128 v[112:115], %[ea] offset:12288\n\t"
    "s_add_u32 m0, %[l1], 12288\n\t"
    "v_mfma_f32_16x16x32_bf16 v[68:71], v[156:159], v[120:123], v[68:71]\n\t"
    "ds_read_b128 v[116:119], %[ea] offset:14336\n\t"
    "global_load_lds_dwordx4 %[av6], s[36:37] sc1\n\t"
    "v_mfma_f32_16x16x32_bf16 v[72:75], v[152:155], v[124:127], v[72:75]\n\t"
    "s_add_u32 m0, %[l1], 45056\n\t"
    "v_mfma_f32_16x16x32_bf16 v[76:79], v[156:159], v[124:127], v[76:79]\n\t"
    "global_load_lds_dwordx4 %[av7], s[36:37] sc1\n\t"
    "s_waitcnt lgkmcnt(4)\n\t"
    "v_mfma_f32_16x16x32_bf16 v[64:67], v[160:163], v[96:99], v[64:67]\n\t"
    "ds_read_b128 v[120:123], %[da] offset:12288\n\t"
    "v_mfma_f32_16x16x32_bf16 v[68:71], v[164:167], v[96:99], v[68:71]\n\t"
    "ds_read_b128 v[124:127], %[da] offset:14336\n\t"
    "v_mfma_f32_16x16x32_bf16 v[72:75], v[160:163], v[100:103], v[72:75]\n\t"
    "v_mfma_f32_16x16x32_bf16 v[76:79], v[164:167], v[100:103], v[76:79]\n\t"
    "s_waitcnt lgkmcnt(4)\n\t"
    "v_mfma_f32_16x16x32_bf16 v[64:67], v[168:171], v[104:107], v[64:67]\n\t"
    "ds_read_b128 v[96:99], %[ea] offset:16384\n\t"
    "v_mfma_f32_16x16x32_bf16 v[68:71], v[172:175], v[104:107], v[68:71]\n\t"
    "ds_read_b128 v[100:103], %[ea] offset:18432\n\t"
    "v_mfma_f32_16x16x32_bf16 v[72:75], v[168:171], v[108:111], v[72:75]\n\t"
    "v_mfma_f32_16x16x32_bf16 v[76:79], v[172:175], v[108:111], v[76:79]\n\t"
    "s_waitcnt lgkmcnt(4)\n\t"
    "v_mfma_f32_16x16x32_bf16 v[64:67], v[176:179], v[112:115], v[64:67]\n\t"
    "ds_read_b128 v[104:107], %[da] offset:16384\n\t"
    "v_mfma_f32_16x16x32_bf16 v[68:71], v[180:183], v[112:115], v[68:71]\n\t"
    "ds_read_b128 v[108:111], %[da] offset:18432\n\t"
    "v_mfma_f32_16x16x32_bf16 v[72:75], v[176:179], v[116:119], v[72:75]\n\t"
    "v_mfma_f32_16x16x32_bf16 v[76:79], v[180:183], v[116:119], v[76:79]\n\t"
    "s_waitcnt lgkmcnt(4)\n\t"
    "v_mfma_f32_16x16x32_bf16 v[64:67], v[184:187], v[120:123], v[64:67]\n\t"
    "ds_read_b128 v[112:115], %[ea] offset:20480\n\t"
    "v_mfma_f32_16x16x32_bf16 v[68:71], v[188:191], v[120:123], v[68:71]\n\t"
    "ds_read_b128 v[116:119], %[ea] offset:22528\n\t"
    "v_mfma_f32_16x16x32_bf16 v[72:75], v[184:187], v[124:127], v[72:75]\n\t"
    "v_mfma_f32_16x16x32_bf16 v[76:79], v[188:191], v[124:127], v[76:79]\n\t"
    "s_waitcnt vmcnt(12)\n\t"
    "s_barrier\n\t"
    "s_waitcnt lgkmcnt(4)\n\t"
    "v_mfma_f32_16x16x32_bf16 v[64:67], v[192:195], v[96:99], v[64:67]\n\t"
    "ds_read_b128 v[120:123], %[da] offset:20480\n\t"
    "s_add_u32 m0, %[l0], 0\n\t"
    "v_mfma_f32_16x16x32_bf16 v[68:71], v[196:199], v[96:99], v[68:71]\n\t"
    "ds_read_b128 v[124:127], %[da] offset:22528\n\t"
    "global_load_lds_dwordx4 %[av0], s[38:39] sc1\n\t"
    "v_mfma_f32_16x16x32_bf16 v[72:75], v[192:195], v[100:103], v[72:75]\n\t"
    "s_add_u32 m0, %[l0], 32768\n\t"
    "v_mfma_f32_16x16x32_bf16 v[76:79], v[196:199], v[100:103], v[76:79]\n\t"
    "global_load_lds_dwordx4 %[av1], s[38:39] sc1\n\t"
    "s_waitcnt lgkmcnt(4)\n\t"
    "v_mfma_f32_16x16x32_bf16 v[64:67], v[200:203], v[104:107], v[64:67]\n\t"
    "ds_read_b128 v[96:99], %[ea] offset:24576\n\t"
    "s_add_u32 m0, %[l0], 4096\n\t"
    "v_mfma_f32_16x16x32_bf16 v[68:71], v[204:207], v[104:107], v[68:71]\n\t"
    "ds_read_b128 v[100:103], %[ea] offset:26624\n\t"
    "global_load_lds_dwordx4 %[av2], s[38:39] sc1\n\t"
    "v_mfma_f32_16x16x32_bf16 v[72:75], v[200:203], v[108:111], v[72:75]\n\t"
    "s_add_u32 m0, %[l0], 36864\n\t"
    "v_mfma_f32_16x16x32_bf16 v[76:79], v[204:207], v[108:111], v[76:79]\n\t"
    "global_load_lds_dwordx4 %[av3], s[38:39] sc1\n\t"
    "s_add_u32 s36, s36, %[ab]\n\t"
    "s_addc_u32 s37, s37, 0\n\t"
    "s_waitcnt lgkmcnt(4)\n\t"
    "v_mfma_f32_16x16x32_bf16 v[64:67], v[208:211], v[112:115], v[64:67]\n\t"
    "ds_read_b128 v[104:107], %[da] offset:24576\n\t"
    "s_add_u32 m0, %[l0], 8192\n\t"
    "v_mfma_f32_16x16x32_bf16 v[68:71], v[212:215], v[112:115], v[68:71]\n\t"
    "ds_read_b128 v[108:111], %[da] offset:26624\n\t"
    "global_load_lds_dwordx4 %[av4], s[38:39] sc1\n\t"
    "v_mfma_f32_16x16x32_bf16 v[72:75], v[208:211], v[116:119], v[72:75]\n\t"
    "ds_read_b32 v39, %[sa]\n\t"
    "s_add_u32 m0, %[l0], 40960\n\t"
    "v_mfma_f32_16x16x32_bf16 v[76:79], v[212:215], v[116:119], v[76:79]\n\t"
    "v_xor_b32 %[sa], 0x100, %[sa]\n\t"
    "global_load_lds_dwordx4 %[av5], s[38:39] sc1\n\t"
    "s_xor_b32 %[l1], %[l1], 0x10000\n\t"
    "s_waitcnt lgkmcnt(5)\n\t"
    "v_mfma_f32_16x16x32_bf16 v[64:67], v[216:219], v[120:123], v[64:67]\n\t"
    "ds_read_b128 v[112:115], %[ea] offset:28672\n\t"
    "s_add_u32 m0, %[l0], 12288\n\t"
    "v_mfma_f32_16x16x32_bf16 v[68:71], v[220:223], v[120:123], v[68:71]\n\t"
    "ds_read_b128 v[116:119], %[ea] offset:30720\n\t"
    "global_load_lds_dwordx4 %[av6], s[38:39] sc1\n\t"
    "v_mfma_f32_16x16x32_bf16 v[72:75], v[216:219], v[124:127], v[72:75]\n\t"
    "v_xor_b32 %[ea], 0x10000, %[ea]\n\t"
    "s_add_u32 m0, %[l0], 45056\n\t"
    "v_mfma_f32_16x16x32_bf16 v[76:79], v[220:223], v[124:127], v[76:79]\n\t"
    "global_load_lds_dwordx4 %[av7], s[38:39] sc1\n\t"
    "s_waitcnt lgkmcnt(5)\n\t"
    "v_mfma_f32_16x16x32_bf16 v[64:67], v[224:227], v[96:99], v[64:67]\n\t"
    "ds_read_b128 v[120:123], %[da] offset:28672\n\t"
    "v_mfma_f32_16x16x32_bf16 v[68:71], v[228:231], v[96:99], v[68:71]\n\t"
    "ds_read_b128 v[124:127], %[da] offset:30720\n\t"
    "v_mfma_f32_16x16x32_bf16 v[72:75], v[224:227], v[100:103], v[72:75]\n\t"
    "v_xor_b32 %[da], 0x10000, %[da]\n\t"
    "v_mfma_f32_16x16x32_bf16 v[76:79], v[228:231], v[100:103], v[76:79]\n\t"
    "s_waitcnt lgkmcnt(5)\n\t"
    "v_mfma_f32_16x16x32_bf16 v[64:67], v[232:235], v[104:107], v[64:67]\n\t"
    "ds_read_b128 v[96:99], %[ea] offset:0\n\t"
    "v_mfma_f32_16x16x32_bf16 v[68:71], v[236:239], v[104:107], v[68:71]\n\t"
    "ds_read_b128 v[100:103], %[ea] offset:2048\n\t"
    "v_mfma_f32_16x16x32_bf16 v[72:75], v[232:235], v[108:111], v[72:75]\n\t"
    "v_mfma_f32_16x16x32_bf16 v[76:79], v[236:239], v[108:111], v[76:79]\n\t"
    "s_waitcnt lgkmcnt(4)\n\t"
    "v_mfma_f32_16x16x32_bf16 v[64:67], v[240:243], v[112:115], v[64:67]\n\t"
    "ds_read_b128 v[104:107], %[da] offset:0\n\t"
    "v_mad_u32_u24 v39, v39, %[mu], %[ad]\n\t"
    "v_mfma_f32_16x16x32_bf16 v[68:71], v[244:247], v[112:115], v[68:71]\n\t"
    "ds_read_b128 v[108:111], %[da] offset:2048\n\t"
    "v_cmp_ge_i32 vcc, %[lf], v39\n\t"
    "s_cmp_eq_u64 vcc, 0\n\t"
    "s_addc_u32 %[lf], %[lf], 0\n\t"
    "v_mfma_f32_16x16x32_bf16 v[72:75], v[240:243], v[116:119], v[72:75]\n\t"
    "v_mfma_f32_16x16x32_bf16 v[76:79], v[244:247], v[116:119], v[76:79]\n\t"
    "v_cmp_ge_i32 vcc, %[lf], v39\n\t"
    "s_cmp_eq_u64 vcc, 0\n\t"
    "s_addc_u32 %[lf], %[lf], 0\n\t"
    "s_waitcnt lgkmcnt(4)\n\t"
    "v_mfma_f32_16x16x32_bf16 v[80:83], v[248:251], v[120:123], v[64:67]\n\t"
    "ds_read_b128 v[112:115], %[ea] offset:4096\n\t"
    "s_min_i32 %[lf], %[lf], %[cp]\n\t"
    "s_add_u32 %[b], %[b], 1\n\t"
    "v_mfma_f32_16x16x32_bf16 v[84:87], v[252:255], v[120:123], v[68:71]\n\t"
    "ds_read_b128 v[116:119], %[ea] offset:6144\n\t"
    "v_mfma_f32_16x16x32_bf16 v[88:91], v[248:251], v[124:127], v[72:75]\n\t"
    "v_mfma_f32_16x16x32_bf16 v[92:95], v[252:255], v[124:127], v[76:79]\n\t"
    "s_add_u32 s38, s38, %[ab]\n\t"
    "s_addc_u32 s39, s39, 0\n\t"
    "s_xor_b32 %[l0], %[l0], 0x10000\n\t"
    "s_cmp_lt_i32 %[b], %[lf]\n\t"
    "s_cbranch_scc1 1b\n\t"
    "s_waitcnt lgkmcnt(0)\n\t"
    "s_nop 4"
    : "=&{v[80:95]}"(pd), [ea] "+&v"(ea), [da] "+&v"(da), [b] "+&s"(b), [lf] "+&s"(lf), "+{s[36:37]}"(p1), "+{s[38:39]}"(p0), [sa] "+&v"(sa), [l1] "+&s"(l1), [l0] "+&s"(l0)
    : [rw] "v"(rw), [ab] "s"(ab), [cp] "s"(cp), [mu] "s"(mu), [ad] "v"(ad), "{v[128:159]}"(W[0]), "{v[160:191]}"(W[1]), "{v[192:223]}"(W[2]), "{v[224:255]}"(W[3]), [av0] "v"(av[0]), [av1] "v"(av[1]), [av2] "v"(av[2]), [av3] "v"(av[3]), [av4] "v"(av[4]), [av5] "v"(av[5]), [av6] "v"(av[6]), [av7] "v"(av[7])
    : "v39", "v64", "v65", "v66", "v67", "v68", "v69", "v70", "v71", "v72", "v73", "v74", "v75", "v76", "v77", "v78", "v79", "v96", "v97", "v98", "v99", "v100", "v101", "v102", "v103", "v104", "v105", "v106", "v107", "v108", "v109", "v110", "v111", "v112", "v113", "v114", "v115", "v116", "v117", "v118", "v119", "v120", "v121", "v122", "v123", "v124", "v125", "v126", "v127", "vcc", "scc", "m0", "memory");
}
__device__ __forceinline__ void fast_ld2(const u32x32 (&W)[4], u32x16& pd, unsigned ea, unsigned da, int& b, int& lf, int cp, unsigned sa, unsigned mu, unsigned ad,
    unsigned rw, unsigned ab, const char* p1, const char* p0, unsigned l1, unsigned l0, const unsigned (&av)[8]) {
  asm volatile("s_setprio 0\n\t"
    "s_waitcnt vmcnt(6)\n\t"
    "s_barrier\n\t"
    "ds_read_b128 v[96:99], %[ea] offset:0\n\t"
    "ds_read_b128 v[100:103], %[ea] offset:2048\n\t"
    "ds_read_b128 v[104:107], %[da] offset:0\n\t"
    "ds_read_b128 v[108:111], %[da] offset:2048\n\t"
    "ds_read_b128 v[112:115], %[ea] offset:4096\n\t"
    "ds_read_b128 v[116:119], %[ea] offset:6144\n\t"
    "s_branch 2f\n\t"
    "1:\n\t"
    "s_waitcnt vmcnt(6)\n\t"
    "s_nop 1\n\t"
    "s_barrier\n\t"
    "2:\n\t"
    "s_waitcnt lgkmcnt(4)\n\t"
    "v_mfma_f32_16x16x32_bf16 v[64:67], v[128:131], v[96:99], 0\n\t"
    "ds_read_b128 v[120:123], %[da] offset:4096\n\t"
    "s_add_u32 m0, %[l1], 0\n\t"
    "ds_write_b128 %[rw], v[80:83] offset:0\n\t"
    "v_mfma_f32_16x16x32_bf16 v[68:71], v[132:135], v[96:99], 0\n\t"
    "ds_read_b128 v[124:127], %[da] offset:6144\n\t"
    "global_load_lds_dwordx4 %[av0], s[36:37] sc1\n\t"
    "ds_write_b128 %[rw], v[84:87] offset:1024\n\t"
    "v_mfma_f32_16x16x32_bf16 v[72:75], v[128:131], v[100:103], 0\n\t"
    "ds_write_b128 %[rw], v[88:91] offset:2048\n\t"
    "v_mfma_f32_16x16x32_bf16 v[76:79], v[132:135], v[100:103], 0\n\t"
    "ds_write_b128 %[rw], v[92:95] offset:3072\n\t"
    "s_waitcnt lgkmcnt(8)\n\t"
    "v_mfma_f32_16x16x32_bf16 v[64:67], v[136:139], v[104:107], v[64:67]\n\t"
    "ds_read_b128 v[96:99], %[ea] offset:8192\n\t"
    "s_add_u32 m0, %[l1], 4096\n\t"
    "v_mfma_f32_16x16x32_bf16 v[68:71], v[140:143], v[104:107], v[68:71]\n\t"
    "ds_read_b128 v[100:103], %[ea] offset:10240\n\t"
    "global_load_lds_dwordx4 %[av2], s[36:37] sc1\n\t"
    "v_mfma_f32_16x16x32_bf16 v[72:75], v[136:139], v[108:111], v[72:75]\n\t"
    "v_mfma_f32_16x16x32_bf16 v[76:79], v[140:143], v[108:111], v[76:79]\n\t"
    "s_waitcnt lgkmcnt(8)\n\t"
    "v_mfma_f32_16x16x32_bf16 v[64:67], v[144:147], v[112:115], v[64:67]\n\t"
    "ds_read_b128 v[104:107], %[da] offset:8192\n\t"
    "s_add_u32 m0, %[l1], 8192\n\t"
    "v_mfma_f32_16x16x32_bf16 v[68:71], v[148:151], v[112:115], v[68:71]\n\t"
    "ds_read_b128 v[108:111], %[da] offset:10240\n\t"
    "global_load_lds_dwordx4 %[av4], s[36:37] sc1\n\t"
    "v_mfma_f32_16x16x32_bf16 v[72:75], v[144:147], v[116:119], v[72:75]\n\t"
    "v_mfma_f32_16x16x32_bf16 v[76:79], v[148:151], v[116:119], v[76:79]\n\t"
    "s_waitcnt lgkmcnt(7)\n\t"
    "v_mfma_f32_16x16x32_bf16 v[64:67], v[152:155], v[120:123], v[64:67]\n\t"
    "ds_read_b128 v[112:115], %[ea] offset:12288\n\t"
    "s_add_u32 m0, %[l1], 12288\n\t"
    "v_mfma_f32_16x16x32_bf16 v[68:71], v[156:159], v[120:123], v[68:71]\n\t"
    "ds_read_b128 v[116:119], %[ea] offset:14336\n\t"
    "global_load_lds_dwordx4 %[av6], s[36:37] sc1\n\t"
    "v_mfma_f32_16x16x32_bf16 v[72:75], v[152:155], v[124:127], v[72:75]\n\t"
    "v_mfma_f32_16x16x32_bf16 v[76:79], v[156:159], v[124:127], v[76:79]\n\t"
    "s_waitcnt lgkmcnt(4)\n\t"
    "v_mfma_f32_16x16x32_bf16 v[64:67], v[160:163], v[96:99], v[64:67]\n\t"
    "ds_read_b128 v[120:123], %[da] offset:12288\n\t"
    "v_mfma_f32_16x16x32_bf16 v[68:71], v[164:167], v[96:99], v[68:71]\n\t"
    "ds_read_b128 v[124:127], %[da] offset:14336\n\t"
    "v_mfma_f32_16x16x32_bf16 v[72:75], v[160:163], v[100:103], v[72:75]\n\t"
    "v_mfma_f32_16x16x32_bf16 v[76:79], v[164:167], v[100:103], v[76:79]\n\t"
    "s_waitcnt lgkmcnt(4)\n\t"
    "v_mfma_f32_16x16x32_bf16 v[64:67], v[168:171], v[104:107], v[64:67]\n\t"
    "ds_read_b128 v[96:99], %[ea] offset:16384\n\t"
    "v_mfma_f32_16x16x32_bf16 v[68:71], v[172:175], v[104:107], v[68:71]\n\t"
    "ds_read_b128 v[100:103], %[ea] offset:18432\n\t"
    "v_mfma_f32_16x16x32_bf16 v[72:75], v[168:171], v[108:111], v[72:75]\n\t"
    "v_mfma_f32_16x16x32_bf16 v[76:79], v[172:175], v[108:111], v[76:79]\n\t"
    "s_waitcnt lgkmcnt(4)\n\t"
    "v_mfma_f32_16x16x32_bf16 v[64:67], v[176:179], v[112:115], v[64:67]\n\t"
    "ds_read_b128 v[104:107], %[da] offset:16384\n\t"
    "v_mfma_f32_16x16x32_bf16 v[68:71], v[180:183], v[112:115], v[68:71]\n\t"
    "ds_read_b128 v[108:111], %[da] offset:18432\n\t"
    "v_mfma_f32_16x16x32_bf16 v[72:75], v[176:179], v[116:119], v[72:75]\n\t"
    "v_mfma_f32_16x16x32_bf16 v[76:79], v[180:183], v[116:119], v[76:79]\n\t"
    "s_waitcnt lgkmcnt(4)\n\t"
    "v_mfma_f32_16x16x32_bf16 v[64:67], v[184:187], v[120:123], v[64:67]\n\t"
    "ds_read_b128 v[112:115], %[ea] offset:20480\n\t"
    "v_mfma_f32_16x16x32_bf16 v[68:71], v[188:191], v[120:123], v[68:71]\n\t"
    "ds_read_b128 v[116:119], %[ea] offset:22528\n\t"
    "v_mfma_f32_16x16x32_bf16 v[72:75], v[184:187], v[124:127], v[72:75]\n\t"
    "v_mfma_f32_16x16x32_bf16 v[76:79], v[188:191], v[124:127], v[76:79]\n\t"
    "s_waitcnt vmcnt(6)\n\t"
    "s_barrier\n\t"
    "s_waitcnt lgkmcnt(4)\n\t"
    "v_mfma_f32_16x16x32_bf16 v[64:67], v[192:195], v[96:99], v[64:67]\n\t"
    "ds_read_b128 v[120:123], %[da] offset:20480\n\t"
    "s_add_u32 m0, %[l0], 0\n\t"
    "v_mfma_f32_16x16x32_bf16 v[68:71], v[196:199], v[96:99], v[68:71]\n\t"
    "ds_read_b128 v[124:127], %[da] offset:22528\n\t"
    "global_load_lds_dwordx4 %[av0], s[38:39] sc1\n\t"
    "v_mfma_f32_16x16x32_bf16 v[72:75], v[192:195], v[100:103], v[72:75]\n\t"
    "v_mfma_f32_16x16x32_bf16 v[76:79], v[196:199], v[100:103], v[76:79]\n\t"
    "s_waitcnt lgkmcnt(4)\n\t"
    "v_mfma_f32_16x16x32_bf16 v[64:67], v[200:203], v[104:107], v[64:67]\n\t"
    "ds_read_b128 v[96:99], %[ea] offset:24576\n\t"
    "s_add_u32 m0, %[l0], 4096\n\t"
    "v_mfma_f32_16x16x32_bf16 v[68:71], v[204:207], v[104:107], v[68:71]\n\t"
    "ds_read_b128 v[100:103], %[ea] offset:26624\n\t"
    "global_load_lds_dwordx4 %[av2], s[38:39] sc1\n\t"
    "v_mfma_f32_16x16x32_bf16 v[72:75], v[200:203], v[108:111], v[72:75]\n\t"
    "v_mfma_f32_16x16x32_bf16 v[76:79], v[204:207], v[108:111], v[76:79]\n\t"
    "s_add_u32 s36, s36, %[ab]\n\t"
    "s_addc_u32 s37, s37, 0\n\t"
    "s_waitcnt lgkmcnt(4)\n\t"
    "v_mfma_f32_16x16x32_bf16 v[64:67], v[208:211], v[112:115], v[64:67]\n\t"
    "ds_read_b128 v[104:107], %[da] offset:24576\n\t"
    "s_add_u32 m0, %[l0], 8192\n\t"
    "v_mfma_f32_16x16x32_bf16 v[68:71], v[212:215], v[112:115], v[68:71]\n\t"
    "ds_read_b128 v[108:111], %[da] offset:26624\n\t"
    "global_load_lds_dwordx4 %[av4], s[38:39] sc1\n\t"
    "v_mfma_f32_16x16x32_bf16 v[72:75], v[208:211], v[116:119], v[72:75]\n\t"
    "ds_read_b32 v39, %[sa]\n\t"
    "v_mfma_f32_16x16x32_bf16 v[76:79], v[212:215], v[116:119], v[76:79]\n\t"
    "v_xor_b32 %[sa], 0x100, %[sa]\n\t"
    "s_xor_b32 %[l1], %[l1], 0x10000\n\t"
    "s_waitcnt lgkmcnt(5)\n\t"
    "v_mfma_f32_16x16x32_bf16 v[64:67], v[216:219], v[120:123], v[64:67]\n\t"
    "ds_read_b128 v[112:115], %[ea] offset:28672\n\t"
    "s_add_u32 m0, %[l0], 12288\n\t"
    "v_mfma_f32_16x16x32_bf16 v[68:71], v[220:223], v[120:123], v[68:71]\n\t"
    "ds_read_b128 v[116:119], %[ea] offset:30720\n\t"
    "global_load_lds_dwordx4 %[av6], s[38:39] sc1\n\t"
    "v_mfma_f32_16x16x32_bf16 v[72:75], v[216:219], v[124:127], v[72:75]\n\t"
    "v_xor_b32 %[ea], 0x10000, %[ea]\n\t"
    "v_mfma_f32_16x16x32_bf16 v[76:79], v[220:223], v[124:127], v[76:79]\n\t"
    "s_waitcnt lgkmcnt(5)\n\t"
    "v_mfma_f32_16x16x32_bf16 v[64:67], v[224:227], v[96:99], v[64:67]\n\t"
    "ds_read_b128 v[120:123], %[da] offset:28672\n\t"
    "v_mfma_f32_16x16x32_bf16 v[68:71], v[228:231], v[96:99], v[68:71]\n\t"
    "ds_read_b128 v[124:127], %[da] offset:30720\n\t"
    "v_mfma_f32_16x16x32_bf16 v[72:75], v[224:227], v[100:103], v[72:75]\n\t"
    "v_xor_b32 %[da], 0x10000, %[da]\n\t"
    "v_mfma_f32_16x16x32_bf16 v[76:79], v[228:231], v[100:103], v[76:79]\n\t"
    "s_waitcnt lgkmcnt(5)\n\t"
    "v_mfma_f32_16x16x32_bf16 v[64:67], v[232:235], v[104:107], v[64:67]\n\t"
    "ds_read_b128 v[96:99], %[ea] offset:0\n\t"
    "v_mfma_f32_16x16x32_bf16 v[68:71], v[236:239], v[104:107], v[68:71]\n\t"
    "ds_read_b128 v[100:103], %[ea] offset:2048\n\t"
    "v_mfma_f32_16x16x32_bf16 v[72:75], v[232:235], v[108:111], v[72:75]\n\t"
    "v_mfma_f32_16x16x32_bf16 v[76:79], v[236:239], v[108:111], v[76:79]\n\t"
    "s_waitcnt lgkmcnt(4)\n\t"
    "v_mfma_f32_16x16x32_bf16 v[64:67], v[240:243], v[112:115], v[64:67]\n\t"
    "ds_read_b128 v[104:107], %[da] offset:0\n\t"
    "v_mad_u32_u24 v39, v39, %[mu], %[ad]\n\t"
    "v_mfma_f32_16x16x32_bf16 v[68:71], v[244:247], v[112:115], v[68:71]\n\t"
    "ds_read_b128 v[108:111], %[da] offset:2048\n\t"
    "v_cmp_ge_i32 vcc, %[lf], v39\n\t"
    "s_cmp_eq_u64 vcc, 0\n\t"
    "s_addc_u32 %[lf], %[lf], 0\n\t"
    "v_mfma_f32_16x16x32_bf16 v[72:75], v[240:243], v[116:119], v[72:75]\n\t"
    "v_mfma_f32_16x16x32_bf16 v[76:79], v[244:247], v[116:119], v[76:79]\n\t"
    "v_cmp_ge_i32 vcc, %[lf], v39\n\t"
    "s_cmp_eq_u64 vcc, 0\n\t"
    "s_addc_u32 %[lf], %[lf], 0\n\t"
    "s_waitcnt lgkmcnt(4)\n\t"
    "v_mfma_f32_16x16x32_bf16 v[80:83], v[248:251], v[120:123], v[64:67]\n\t"
    "ds_read_b128 v[112:115], %[ea] offset:4096\n\t"
    "s_min_i32 %[lf], %[lf], %[cp]\n\t"
    "s_add_u32 %[b], %[b], 1\n\t"
    "v_mfma_f32_16x16x32_bf16 v[84:87], v[252:255], v[120:123], v[68:71]\n\t"
    "ds_read_b128 v[116:119], %[ea] offset:6144\n\t"
    "v_mfma_f32_16x16x32_bf16 v[88:91], v[248:251], v[124:127], v[72:75]\n\t"
    "v_mfma_f32_16x16x32_bf16 v[92:95], v[252:255], v[124:127], v[76:79]\n\t"
    "s_add_u32 s38, s38, %[ab]\n\t"
    "s_addc_u32 s39, s39, 0\n\t"
    "s_xor_b32 %[l0], %[l0], 0x10000\n\t"
    "s_cmp_lt_i32 %[b], %[lf]\n\t"
    "s_cbranch_scc1 1b\n\t"
    "s_waitcnt lgkmcnt(0)\n\t"
    "s_nop 4"
    : "=&{v[80:95]}"(pd), [ea] "+&v"(ea), [da] "+&v"(da), [b] "+&s"(b), [lf] "+&s"(lf), "+{s[36:37]}"(p1), "+{s[38:39]}"(p0), [sa] "+&v"(sa), [l1] "+&s"(l1), [l0] "+&s"(l0)
    : [rw] "v"(rw), [ab] "s"(ab), [cp] "s"(cp), [mu] "s"(mu), [ad] "v"(ad), "{v[128:159]}"(W[0]), "{v[160:191]}"(W[1]), "{v[192:223]}"(W[2]), "{v[224:255]}"(W[3]), [av0] "v"(av[0]), [av1] "v"(av[1]), [av2] "v"(av[2]), [av3] "v"(av[3]), [av4] "v"(av[4]), [av5] "v"(av[5]), [av6] "v"(av[6]), [av7] "v"(av[7])
    : "v39", "v64", "v65", "v66", "v67", "v68", "v69", "v70", "v71", "v72", "v73", "v74", "v75", "v76", "v77", "v78", "v79", "v96", "v97", "v98", "v99", "v100", "v101", "v102", "v103", "v104", "v105", "v106", "v107", "v108", "v109", "v110", "v111", "v112", "v113", "v114", "v115", "v116", "v117", "v118", "v119", "v120", "v121", "v122", "v123", "v124", "v125", "v126", "v127", "vcc", "scc", "m0", "memory");
}
__device__ __forceinline__ void fast_st0(const u32x32 (&W)[4], u32x16& pd, unsigned ea, unsigned da, int& b, int& lf, int cp, unsigned sa, unsigned mu, unsigned ad,
    unsigned rw, unsigned ab, const char* p1, const char* p0, unsigned co0, unsigned co1, int po, int pf, unsigned w0, unsigned sl, unsigned dv, const char* db, const char* pg) {
  unsigned st;
  asm volatile("s_barrier\n\t"
    "ds_read_b128 v[96:99], %[ea] offset:0\n\t"
    "ds_read_b128 v[100:103], %[ea] offset:2048\n\t"
    "ds_read_b128 v[104:107], %[da] offset:0\n\t"
    "ds_read_b128 v[108:111], %[da] offset:2048\n\t"
    "ds_read_b128 v[112:115], %[ea] offset:4096\n\t"
    "ds_read_b128 v[116:119], %[ea] offset:6144\n\t"
    "s_branch 2f\n\t"
    "1:\n\t"
    "s_barrier\n\t"
    "2:\n\t"
    "s_waitcnt lgkmcnt(4)\n\t"
    "v_mfma_f32_16x16x32_bf16 v[64:67], v[128:131], v[96:99], 0\n\t"
    "ds_read_b128 v[120:123], %[da] offset:4096\n\t"
    "v_mfma_f32_16x16x32_bf16 v[68:71], v[132:135], v[96:99], 0\n\t"
    "ds_read_b128 v[124:127], %[da] offset:6144\n\t"
    "v_mfma_f32_16x16x32_bf16 v[72:75], v[128:131], v[100:103], 0\n\t"
    "v_mfma_f32_16x16x32_bf16 v[76:79], v[132:135], v[100:103], 0\n\t"
    "s_waitcnt lgkmcnt(4)\n\t"
    "v_mfma_f32_16x16x32_bf16 v[64:67], v[136:139], v[104:107], v[64:67]\n\t"
    "ds_read_b128 v[96:99], %[ea] offset:8192\n\t"
    "v_mfma_f32_16x16x32_bf16 v[68:71], v[140:143], v[104:107], v[68:71]\n\t"
    "ds_read_b128 v[100:103], %[ea] offset:10240\n\t"
    "v_mfma_f32_16x16x32_bf16 v[72:75], v[136:139], v[108:111], v[72:75]\n\t"
    "v_mfma_f32_16x16x32_bf16 v[76:79], v[140:143], v[108:111], v[76:79]\n\t"
    "s_waitcnt lgkmcnt(4)\n\t"
    "v_mfma_f32_16x16x32_bf16 v[64:67], v[144:147], v[112:115], v[64:67]\n\t"
    "ds_read_b128 v[104:107], %[da] offset:8192\n\t"
    "v_mfma_f32_16x16x32_bf16 v[68:71], v[148:151], v[112:115], v[68:71]\n\t"
    "ds_read_b128 v[108:111], %[da] offset:10240\n\t"
    "v_mfma_f32_16x16x32_bf16 v[72:75], v[144:147], v[116:119], v[72:75]\n\t"
    "v_mfma_f32_16x16x32_bf16 v[76:79], v[148:151], v[116:119], v[76:79]\n\t"
    "s_waitcnt lgkmcnt(4)\n\t"
    "v_mfma_f32_16x16x32_bf16 v[64:67], v[152:155], v[120:123], v[64:67]\n\t"
    "ds_read_b128 v[112:115], %[ea] offset:12288\n\t"
    "v_mfma_f32_16x16x32_bf16 v[68:71], v[156:159], v[120:123], v[68:71]\n\t"
    "ds_read_b128 v[116:119], %[ea] offset:14336\n\t"
    "v_mfma_f32_16x16x32_bf16 v[72:75], v[152:155], v[124:127], v[72:75]\n\t"
    "v_mfma_f32_16x16x32_bf16 v[76:79], v[156:159], v[124:127], v[76:79]\n\t"
    "s_waitcnt lgkmcnt(4)\n\t"
    "v_mfma_f32_16x16x32_bf16 v[64:67], v[160:163], v[96:99], v[64:67]\n\t"
    "ds_read_b128 v[120:123], %[da] offset:12288\n\t"
    "v_mfma_f32_16x16x32_bf16 v[68:71], v[164:167], v[96:99], v[68:71]\n\t"
    "ds_read_b128 v[124:127], %[da] offset:14336\n\t"
    "v_mfma_f32_16x16x32_bf16 v[72:75], v[160:163], v[100:103], v[72:75]\n\t"
    "v_mfma_f32_16x16x32_bf16 v[76:79], v[164:167], v[100:103], v[76:79]\n\t"
    "s_waitcnt lgkmcnt(4)\n\t"
    "v_mfma_f32_16x16x32_bf16 v[64:67], v[168:171], v[104:107], v[64:67]\n\t"
    "ds_read_b128 v[96:99], %[ea] offset:16384\n\t"
    "v_mfma_f32_16x16x32_bf16 v[68:71], v[172:175], v[104:107], v[68:71]\n\t"
    "ds_read_b128 v[100:103], %[ea] offset:18432\n\t"
    "v_mfma_f32_16x16x32_bf16 v[72:75], v[168:171], v[108:111], v[72:75]\n\t"
    "v_mfma_f32_16x16x32_bf16 v[76:79], v[172:175], v[108:111], v[76:79]\n\t"
    "s_waitcnt lgkmcnt(4)\n\t"
    "v_mfma_f32_16x16x32_bf16 v[64:67], v[176:179], v[112:115], v[64:67]\n\t"
    "ds_read_b128 v[104:107], %[da] offset:16384\n\t"
    "v_mfma_f32_16x16x32_bf16 v[68:71], v[180:183], v[112:115], v[68:71]\n\t"
    "ds_read_b128 v[108:111], %[da] offset:18432\n\t"
    "v_mfma_f32_16x16x32_bf16 v[72:75], v[176:179], v[116:119], v[72:75]\n\t"
    "v_mfma_f32_16x16x32_bf16 v[76:79], v[180:183], v[116:119], v[76:79]\n\t"
    "s_waitcnt lgkmcnt(4)\n\t"
    "v_mfma_f32_16x16x32_bf16 v[64:67], v[184:187], v[120:123], v[64:67]\n\t"
    "ds_read_b128 v[112:115], %[ea] offset:20480\n\t"
    "v_mfma_f32_16x16x32_bf16 v[68:71], v[188:191], v[120:123], v[68:71]\n\t"
    "ds_read_b128 v[116:119], %[ea] offset:22528\n\t"
    "v_mfma_f32_16x16x32_bf16 v[72:75], v[184:187], v[124:127], v[72:75]\n\t"
    "v_mfma_f32_16x16x32_bf16 v[76:79], v[188:191], v[124:127], v[76:79]\n\t"
    "s_waitcnt vmcnt(0)\n\t"
    "s_barrier\n\t"
    "s_waitcnt lgkmcnt(4)\n\t"
    "v_mfma_f32_16x16x32_bf16 v[64:67], v[192:195], v[96:99], v[64:67]\n\t"
    "ds_read_b128 v[120:123], %[da] offset:20480\n\t"
    "v_mfma_f32_16x16x32_bf16 v[68:71], v[196:199], v[96:99], v[68:71]\n\t"
    "ds_read_b128 v[124:127], %[da] offset:22528\n\t"
    "v_mfma_f32_16x16x32_bf16 v[72:75], v[192:195], v[100:103], v[72:75]\n\t"
    "ds_read_b128 v[48:51], %[rw] offset:0\n\t"
    "v_mfma_f32_16x16x32_bf16 v[76:79], v[196:199], v[100:103], v[76:79]\n\t"
    "ds_read_b128 v[52:55], %[rw] offset:1024\n\t"
    "s_cmp_eq_u32 %[w0], 0\n\t"
    "s_cbranch_scc1 5f\n\t"
    "6:\n\t"
    "s_waitcnt lgkmcnt(6)\n\t"
    "v_mfma_f32_16x16x32_bf16 v[64:67], v[200:203], v[104:107], v[64:67]\n\t"
    "ds_read_b128 v[96:99], %[ea] offset:24576\n\t"
    "v_mfma_f32_16x16x32_bf16 v[68:71], v[204:207], v[104:107], v[68:71]\n\t"
    "ds_read_b128 v[100:103], %[ea] offset:26624\n\t"
    "v_mfma_f32_16x16x32_bf16 v[72:75], v[200:203], v[108:111], v[72:75]\n\t"
    "ds_read_b128 v[56:59], %[rw] offset:2048\n\t"
    "v_mfma_f32_16x16x32_bf16 v[76:79], v[204:207], v[108:111], v[76:79]\n\t"
    "ds_read_b128 v[60:63], %[rw] offset:3072\n\t"
    "s_waitcnt lgkmcnt(0)\n\t"
    "v_mfma_f32_16x16x32_bf16 v[64:67], v[208:211], v[112:115], v[64:67]\n\t"
    "ds_read_b128 v[104:107], %[da] offset:24576\n\t"
    "v_add_f32 v48, v48, v80\n\t"
    "v_add_f32 v49, v49, v81\n\t"
    "v_mfma_f32_16x16x32_bf16 v[68:71], v[212:215], v[112:115], v[68:71]\n\t"
    "ds_read_b128 v[108:111], %[da] offset:26624\n\t"
    "v_add_f32 v50, v50, v82\n\t"
    "v_mfma_f32_16x16x32_bf16 v[72:75], v[208:211], v[116:119], v[72:75]\n\t"
    "ds_read_b32 v39, %[sa]\n\t"
    "v_add_f32 v51, v51, v83\n\t"
    "v_mfma_f32_16x16x32_bf16 v[76:79], v[212:215], v[116:119], v[76:79]\n\t"
    "v_xor_b32 %[sa], 0x100, %[sa]\n\t"
    "v_add_f32 v52, v52, v84\n\t"
    "v_add_f32 v53, v53, v85\n\t"
    "v_mfma_f32_16x16x32_bf16 v[64:67], v[216:219], v[120:123], v[64:67]\n\t"
    "ds_read_b128 v[112:115], %[ea] offset:28672\n\t"
    "v_add_f32 v54, v54, v86\n\t"
    "v_mfma_f32_16x16x32_bf16 v[68:71], v[220:223], v[120:123], v[68:71]\n\t"
    "ds_read_b128 v[116:119], %[ea] offset:30720\n\t"
    "v_add_f32 v55, v55, v87\n\t"
    "v_mfma_f32_16x16x32_bf16 v[72:75], v[216:219], v[124:127], v[72:75]\n\t"
    "v_xor_b32 %[ea], 0x10000, %[ea]\n\t"
    "v_add_f32 v56, v56, v88\n\t"
    "v_add_f32 v57, v57, v89\n\t"
    "v_mfma_f32_16x16x32_bf16 v[76:79], v[220:223], v[124:127], v[76:79]\n\t"
    "v_add_f32 v58, v58, v90\n\t"
    "v_mfma_f32_16x16x32_bf16 v[64:67], v[224:227], v[96:99], v[64:67]\n\t"
    "ds_read_b128 v[120:123], %[da] offset:28672\n\t"
    "v_add_f32 v59, v59, v91\n\t"
    "v_mfma_f32_16x16x32_bf16 v[68:71], v[228:231], v[96:99], v[68:71]\n\t"
    "ds_read_b128 v[124:127], %[da] offset:30720\n\t"
    "v_add_f32 v60, v60, v92\n\t"
    "v_mfma_f32_16x16x32_bf16 v[72:75], v[224:227], v[100:103], v[72:75]\n\t"
    "v_xor_b32 %[da], 0x10000, %[da]\n\t"
    "v_add_f32 v61, v61, v93\n\t"
    "v_add_f32 v62, v62, v94\n\t"
    "v_mfma_f32_16x16x32_bf16 v[76:79], v[228:231], v[100:103], v[76:79]\n\t"
    "v_add_f32 v63, v63, v95\n\t"
    "s_waitcnt lgkmcnt(5)\n\t"
    "v_mfma_f32_16x16x32_bf16 v[64:67], v[232:235], v[104:107], v[64:67]\n\t"
    "ds_read_b128 v[96:99], %[ea] offset:0\n\t"
    "v_cvt_pk_bf16_f32 v40, v48, v49\n\t"
    "v_mfma_f32_16x16x32_bf16 v[68:71], v[236:239], v[104:107], v[68:71]\n\t"
    "ds_read_b128 v[100:103], %[ea] offset:2048\n\t"
    "v_cvt_pk_bf16_f32 v41, v50, v51\n\t"
    "v_cvt_pk_bf16_f32 v42, v52, v53\n\t"
    "v_mfma_f32_16x16x32_bf16 v[72:75], v[232:235], v[108:111], v[72:75]\n\t"
    "v_cvt_pk_bf16_f32 v43, v54, v55\n\t"
    "v_mfma_f32_16x16x32_bf16 v[76:79], v[236:239], v[108:111], v[76:79]\n\t"
    "global_store_dwordx4 %[co0], v[40:43], s[36:37] sc1\n\t"
    "s_waitcnt lgkmcnt(4)\n\t"
    "v_mfma_f32_16x16x32_bf16 v[64:67], v[240:243], v[112:115], v[64:67]\n\t"
    "ds_read_b128 v[104:107], %[da] offset:0\n\t"
    "v_mad_u32_u24 v39, v39, %[mu], %[ad]\n\t"
    "v_cvt_pk_bf16_f32 v44, v56, v57\n\t"
    "v_cvt_pk_bf16_f32 v45, v58, v59\n\t"
    "v_mfma_f32_16x16x32_bf16 v[68:71], v[244:247], v[112:115], v[68:71]\n\t"
    "ds_read_b128 v[108:111], %[da] offset:2048\n\t"
    "v_cmp_ge_i32 vcc, %[lf], v39\n\t"
    "s_cmp_eq_u64 vcc, 0\n\t"
    "s_addc_u32 %[lf], %[lf], 0\n\t"
    "v_cvt_pk_bf16_f32 v46, v60, v61\n\t"
    "v_mfma_f32_16x16x32_bf16 v[72:75], v[240:243], v[116:119], v[72:75]\n\t"
    "v_cvt_pk_bf16_f32 v47, v62, v63\n\t"
    "v_mfma_f32_16x16x32_bf16 v[76:79], v[244:247], v[116:119], v[76:79]\n\t"
    "v_cmp_ge_i32 vcc, %[lf], v39\n\t"
    "s_cmp_eq_u64 vcc, 0\n\t"
    "s_addc_u32 %[lf], %[lf], 0\n\t"
    "global_store_dwordx4 %[co1], v[44:47], s[36:37] sc1\n\t"
    "s_waitcnt lgkmcnt(4)\n\t"
    "v_mfma_f32_16x16x32_bf16 v[80:83], v[248:251], v[120:123], v[64:67]\n\t"
    "ds_read_b128 v[112:115], %[ea] offset:4096\n\t"
    "s_min_i32 %[lf], %[lf], %[cp]\n\t"
    "s_add_u32 %[b], %[b], 1\n\t"
    "v_mfma_f32_16x16x32_bf16 v[84:87], v[252:255], v[120:123], v[68:71]\n\t"
    "ds_read_b128 v[116:119], %[ea] offset:6144\n\t"
    "v_mfma_f32_16x16x32_bf16 v[88:91], v[248:251], v[124:127], v[72:75]\n\t"
    "v_mfma_f32_16x16x32_bf16 v[92:95], v[252:255], v[124:127], v[76:79]\n\t"
    "s_mov_b64 s[36:37], s[38:39]\n\t"
    "s_add_u32 s38, s38, %[ab]\n\t"
    "s_addc_u32 s39, s39, 0\n\t"
    "s_cmp_lt_i32 %[b], %[lf]\n\t"
    "s_cbranch_scc1 1b\n\t"
    "s_waitcnt lgkmcnt(0)\n\t"
    "s_nop 4\n\t"
    "s_branch 9f\n\t"
    "5:\n\t"
    "s_add_u32 %[st], %[b], %[po]\n\t"
    "s_max_i32 %[st], %[st], %[p0]\n\t"
    "v_mov_b32 v39, %[st]\n\t"
    "v_mov_b32 v38, 0\n\t"
    "s_mov_b64 exec, 1\n\t"
    "global_store_dword v38, v39, %[pg] sc1\n\t"
    "s_mov_b64 exec, -1\n\t"
    "s_mov_b32 %[p0], %[p1]\n\t"
    "s_xor_b32 %[sl], %[sl], 0x100\n\t"
    "s_cmp_ge_i32 %[lf], %[cp]\n\t"
    "s_cbranch_scc1 6b\n\t"
    "s_mov_b32 m0, %[sl]\n\t"
    "s_nop 0\n\t"
    "global_load_lds_dword %[dv], %[db] sc1\n\t"
    "s_branch 6b\n\t"
    "9:"
    : "=&{v[80:95]}"(pd), [ea] "+&v"(ea), [da] "+&v"(da), [b] "+&s"(b), [lf] "+&s"(lf), "+{s[36:37]}"(p1), "+{s[38:39]}"(p0), [sa] "+&v"(sa), [st] "=&s"(st), [sl] "+&s"(sl), [p0] "+&s"(pf)
    : [rw] "v"(rw), [ab] "s"(ab), [cp] "s"(cp), [mu] "s"(mu), [ad] "v"(ad), "{v[128:159]}"(W[0]), "{v[160:191]}"(W[1]), "{v[192:223]}"(W[2]), "{v[224:255]}"(W[3]), [co0] "v"(co0), [w0] "s"(w0), [dv] "v"(dv), [db] "s"(db), [pg] "s"(pg), [po] "s"(po - 1), [p1] "s"(b + po), [co1] "v"(co1)
    : "v38", "v39", "v40", "v41", "v42", "v43", "v44", "v45", "v46", "v47", "v48", "v49", "v50", "v51", "v52", "v53", "v54", "v55", "v56", "v57", "v58", "v59", "v60", "v61", "v62", "v63", "v64", "v65", "v66", "v67", "v68", "v69", "v70", "v71", "v72", "v73", "v74", "v75", "v76", "v77", "v78", "v79", "v96", "v97", "v98", "v99", "v100", "v101", "v102", "v103", "v104", "v105", "v106", "v107", "v108", "v109", "v110", "v111", "v112", "v113", "v114", "v115", "v116", "v117", "v118", "v119", "v120", "v121", "v122", "v123", "v124", "v125", "v126", "v127", "vcc", "scc", "m0", "memory");
}
__device__ __forceinline__ void fast_st1(const u32x32 (&W)[4], u32x16& pd, unsigned ea, unsigned da, int& b, int& lf, int cp, unsigned sa, unsigned mu, unsigned ad,
    unsigned rw, unsigned ab, const char* p1, const char* p0, unsigned co0, unsigned co1, int po, int pf, unsigned w0, unsigned sl, unsigned dv, const char* db, const char* pg) {
  unsigned st;
  asm volatile("s_barrier\n\t"
    "ds_read_b128 v[96:99], %[ea] offset:0\n\t"
    "ds_read_b128 v[100:103], %[ea] offset:2048\n\t"
    "ds_read_b128 v[104:107], %[da] offset:0\n\t"
    "ds_read_b128 v[108:111], %[da] offset:2048\n\t"
    "ds_read_b128 v[112:115], %[ea] offset:4096\n\t"
    "ds_read_b128 v[116:119], %[ea] offset:6144\n\t"
    "s_branch 2f\n\t"
    "1:\n\t"
    "s_barrier\n\t"
    "2:\n\t"
    "s_waitcnt lgkmcnt(4)\n\t"
    "v_mfma_f32_16x16x32_bf16 v[64:67], v[128:131], v[96:99], 0\n\t"
    "ds_read_b128 v[120:123], %[da] offset:4096\n\t"
    "v_mfma_f32_16x16x32_bf16 v[68:71], v[132:135], v[96:99], 0\n\t"
    "ds_read_b128 v[124:127], %[da] offset:6144\n\t"
    "v_mfma_f32_16x16x32_bf16 v[72:75], v[128:131], v[100:103], 0\n\t"
    "v_mfma_f32_16x16x32_bf16 v[76:79], v[132:135], v[100:103], 0\n\t"
    "s_waitcnt lgkmcnt(4)\n\t"
    "v_mfma_f32_16x16x32_bf16 v[64:67], v[136:139], v[104:107], v[64:67]\n\t"
    "ds_read_b128 v[96:99], %[ea] offset:8192\n\t"
    "v_mfma_f32_16x16x32_bf16 v[68:71], v[140:143], v[104:107], v[68:71]\n\t"
    "ds_read_b128 v[100:103], %[ea] offset:10240\n\t"
    "v_mfma_f32_16x16x32_bf16 v[72:75], v[136:139], v[108:111], v[72:75]\n\t"
    "v_mfma_f32_16x16x32_bf16 v[76:79], v[140:143], v[108:111], v[76:79]\n\t"
    "s_waitcnt lgkmcnt(4)\n\t"
    "v_mfma_f32_16x16x32_bf16 v[64:67], v[144:147], v[112:115], v[64:67]\n\t"
    "ds_read_b128 v[104:107], %[da] offset:8192\n\t"
    "v_mfma_f32_16x16x32_bf16 v[68:71], v[148:151], v[112:115], v[68:71]\n\t"
    "ds_read_b128 v[108:111], %[da] offset:10240\n\t"
    "v_mfma_f32_16x16x32_bf16 v[72:75], v[144:147], v[116:119], v[72:75]\n\t"
    "v_mfma_f32_16x16x32_bf16 v[76:79], v[148:151], v[116:119], v[76:79]\n\t"
    "s_waitcnt lgkmcnt(4)\n\t"
    "v_mfma_f32_16x16x32_bf16 v[64:67], v[152:155], v[120:123], v[64:67]\n\t"
    "ds_read_b128 v[112:115], %[ea] offset:12288\n\t"
    "v_mfma_f32_16x16x32_bf16 v[68:71], v[156:159], v[120:123], v[68:71]\n\t"
    "ds_read_b128 v[116:119], %[ea] offset:14336\n\t"
    "v_mfma_f32_16x16x32_bf16 v[72:75], v[152:155], v[124:127], v[72:75]\n\t"
    "v_mfma_f32_16x16x32_bf16 v[76:79], v[156:159], v[124:127], v[76:79]\n\t"
    "s_waitcnt lgkmcnt(4)\n\t"
    "v_mfma_f32_16x16x32_bf16 v[64:67], v[160:163], v[96:99], v[64:67]\n\t"
    "ds_read_b128 v[120:123], %[da] offset:12288\n\t"
    "v_mfma_f32_16x16x32_bf16 v[68:71], v[164:167], v[96:99], v[68:71]\n\t"
    "ds_read_b128 v[124:127], %[da] offset:14336\n\t"
    "v_mfma_f32_16x16x32_bf16 v[72:75], v[160:163], v[100:103], v[72:75]\n\t"
    "v_mfma_f32_16x16x32_bf16 v[76:79], v[164:167], v[100:103], v[76:79]\n\t"
    "s_waitcnt lgkmcnt(4)\n\t"
    "v_mfma_f32_16x16x32_bf16 v[64:67], v[168:171], v[104:107], v[64:67]\n\t"
    "ds_read_b128 v[96:99], %[ea] offset:16384\n\t"
    "v_mfma_f32_16x16x32_bf16 v[68:71], v[172:175], v[104:107], v[68:71]\n\t"
    "ds_read_b128 v[100:103], %[ea] offset:18432\n\t"
    "v_mfma_f32_16x16x32_bf16 v[72:75], v[168:171], v[108:111], v[72:75]\n\t"
    "v_mfma_f32_16x16x32_bf16 v[76:79], v[172:175], v[108:111], v[76:79]\n\t"
    "s_waitcnt lgkmcnt(4)\n\t"
    "v_mfma_f32_16x16x32_bf16 v[64:67], v[176:179], v[112:115], v[64:67]\n\t"
    "ds_read_b128 v[104:107], %[da] offset:16384\n\t"
    "v_mfma_f32_16x16x32_bf16 v[68:71], v[180:183], v[112:115], v[68:71]\n\t"
    "ds_read_b128 v[108:111], %[da] offset:18432\n\t"
    "v_mfma_f32_16x16x32_bf16 v[72:75], v[176:179], v[116:119], v[72:75]\n\t"
    "v_mfma_f32_16x16x32_bf16 v[76:79], v[180:183], v[116:119], v[76:79]\n\t"
    "s_waitcnt lgkmcnt(4)\n\t"
    "v_mfma_f32_16x16x32_bf16 v[64:67], v[184:187], v[120:123], v[64:67]\n\t"
    "ds_read_b128 v[112:115], %[ea] offset:20480\n\t"
    "v_mfma_f32_16x16x32_bf16 v[68:71], v[188:191], v[120:123], v[68:71]\n\t"
    "ds_read_b128 v[116:119], %[ea] offset:22528\n\t"
    "v_mfma_f32_16x16x32_bf16 v[72:75], v[184:187], v[124:127], v[72:75]\n\t"
    "v_mfma_f32_16x16x32_bf16 v[76:79], v[188:191], v[124:127], v[76:79]\n\t"
    "s_waitcnt vmcnt(0)\n\t"
    "s_barrier\n\t"
    "s_waitcnt lgkmcnt(4)\n\t"
    "v_mfma_f32_16x16x32_bf16 v[64:67], v[192:195], v[96:99], v[64:67]\n\t"
    "ds_read_b128 v[120:123], %[da] offset:20480\n\t"
    "v_mfma_f32_16x16x32_bf16 v[68:71], v[196:199], v[96:99], v[68:71]\n\t"
    "ds_read_b128 v[124:127], %[da] offset:22528\n\t"
    "v_mfma_f32_16x16x32_bf16 v[72:75], v[192:195], v[100:103], v[72:75]\n\t"
    "ds_read_b128 v[48:51], %[rw] offset:0\n\t"
    "v_mfma_f32_16x16x32_bf16 v[76:79], v[196:199], v[100:103], v[76:79]\n\t"
    "ds_read_b128 v[52:55], %[rw] offset:1024\n\t"
    "s_cmp_eq_u32 %[w0], 0\n\t"
    "s_cbranch_scc1 5f\n\t"
    "6:\n\t"
    "s_waitcnt lgkmcnt(6)\n\t"
    "v_mfma_f32_16x16x32_bf16 v[64:67], v[200:203], v[104:107], v[64:67]\n\t"
    "ds_read_b128 v[96:99], %[ea] offset:24576\n\t"
    "v_mfma_f32_16x16x32_bf16 v[68:71], v[204:207], v[104:107], v[68:71]\n\t"
    "ds_read_b128 v[100:103], %[ea] offset:26624\n\t"
    "v_mfma_f32_16x16x32_bf16 v[72:75], v[200:203], v[108:111], v[72:75]\n\t"
    "ds_read_b128 v[56:59], %[rw] offset:2048\n\t"
    "v_mfma_f32_16x16x32_bf16 v[76:79], v[204:207], v[108:111], v[76:79]\n\t"
    "ds_read_b128 v[60:63], %[rw] offset:3072\n\t"
    "s_waitcnt lgkmcnt(0)\n\t"
    "v_mfma_f32_16x16x32_bf16 v[64:67], v[208:211], v[112:115], v[64:67]\n\t"
    "ds_read_b128 v[104:107], %[da] offset:24576\n\t"
    "v_add_f32 v48, v48, v80\n\t"
    "v_add_f32 v49, v49, v81\n\t"
    "v_add_f32 v50, v50, v82\n\t"
    "v_mfma_f32_16x16x32_bf16 v[68:71], v[212:215], v[112:115], v[68:71]\n\t"
    "ds_read_b128 v[108:111], %[da] offset:26624\n\t"
    "v_add_f32 v51, v51, v83\n\t"
    "v_add_f32 v52, v52, v84\n\t"
    "v_add_f32 v53, v53, v85\n\t"
    "v_mfma_f32_16x16x32_bf16 v[72:75], v[208:211], v[116:119], v[72:75]\n\t"
    "ds_read_b32 v39, %[sa]\n\t"
    "v_add_f32 v54, v54, v86\n\t"
    "v_add_f32 v55, v55, v87\n\t"
    "v_add_f32 v56, v56, v88\n\t"
    "v_mfma_f32_16x16x32_bf16 v[76:79], v[212:215], v[116:119], v[76:79]\n\t"
    "v_xor_b32 %[sa], 0x100, %[sa]\n\t"
    "v_add_f32 v57, v57, v89\n\t"
    "v_add_f32 v58, v58, v90\n\t"
    "v_add_f32 v59, v59, v91\n\t"
    "v_mfma_f32_16x16x32_bf16 v[64:67], v[216:219], v[120:123], v[64:67]\n\t"
    "ds_read_b128 v[112:115], %[ea] offset:28672\n\t"
    "v_add_f32 v60, v60, v92\n\t"
    "v_add_f32 v61, v61, v93\n\t"
    "v_add_f32 v62, v62, v94\n\t"
    "v_mfma_f32_16x16x32_bf16 v[68:71], v[220:223], v[120:123], v[68:71]\n\t"
    "ds_read_b128 v[116:119], %[ea] offset:30720\n\t"
    "v_add_f32 v63, v63, v95\n\t"
    "v_mul_f32 v40, %[nl], v48\n\t"
    "v_mul_f32 v41, %[nl], v49\n\t"
    "v_mfma_f32_16x16x32_bf16 v[72:75], v[216:219], v[124:127], v[72:75]\n\t"
    "v_xor_b32 %[ea], 0x10000, %[ea]\n\t"
    "v_mul_f32 v42, %[nl], v50\n\t"
    "v_mul_f32 v43, %[nl], v51\n\t"
    "v_mul_f32 v44, %[nl], v56\n\t"
    "v_mfma_f32_16x16x32_bf16 v[76:79], v[220:223], v[124:127], v[76:79]\n\t"
    "v_mul_f32 v45, %[nl], v57\n\t"
    "v_mul_f32 v46, %[nl], v58\n\t"
    "v_mul_f32 v47, %[nl], v59\n\t"
    "v_mfma_f32_16x16x32_bf16 v[64:67], v[224:227], v[96:99], v[64:67]\n\t"
    "ds_read_b128 v[120:123], %[da] offset:28672\n\t"
    "v_exp_f32 v40, v40\n\t"
    "v_exp_f32 v41, v41\n\t"
    "v_exp_f32 v42, v42\n\t"
    "v_mfma_f32_16x16x32_bf16 v[68:71], v[228:231], v[96:99], v[68:71]\n\t"
    "ds_read_b128 v[124:127], %[da] offset:30720\n\t"
    "v_exp_f32 v43, v43\n\t"
    "v_exp_f32 v44, v44\n\t"
    "v_exp_f32 v45, v45\n\t"
    "v_mfma_f32_16x16x32_bf16 v[72:75], v[224:227], v[100:103], v[72:75]\n\t"
    "v_xor_b32 %[da], 0x10000, %[da]\n\t"
    "v_exp_f32 v46, v46\n\t"
    "v_exp_f32 v47, v47\n\t"
    "v_add_f32 v40, 1.0, v40\n\t"
    "v_mfma_f32_16x16x32_bf16 v[76:79], v[228:231], v[100:103], v[76:79]\n\t"
    "v_add_f32 v41, 1.0, v41\n\t"
    "v_add_f32 v42, 1.0, v42\n\t"
    "v_add_f32 v43, 1.0, v43\n\t"
    "s_waitcnt lgkmcnt(5)\n\t"
    "v_mfma_f32_16x16x32_bf16 v[64:67], v[232:235], v[104:107], v[64:67]\n\t"
    "ds_read_b128 v[96:99], %[ea] offset:0\n\t"
    "v_add_f32 v44, 1.0, v44\n\t"
    "v_add_f32 v45, 1.0, v45\n\t"
    "v_add_f32 v46, 1.0, v46\n\t"
    "v_mfma_f32_16x16x32_bf16 v[68:71], v[236:239], v[104:107], v[68:71]\n\t"
    "ds_read_b128 v[100:103], %[ea] offset:2048\n\t"
    "v_add_f32 v47, 1.0, v47\n\t"
    "v_rcp_f32 v40, v40\n\t"
    "v_rcp_f32 v41, v41\n\t"
    "v_mfma_f32_16x16x32_bf16 v[72:75], v[232:235], v[108:111], v[72:75]\n\t"
    "v_rcp_f32 v42, v42\n\t"
    "v_rcp_f32 v43, v43\n\t"
    "v_rcp_f32 v44, v44\n\t"
    "v_mfma_f32_16x16x32_bf16 v[76:79], v[236:239], v[108:111], v[76:79]\n\t"
    "v_rcp_f32 v45, v45\n\t"
    "v_rcp_f32 v46, v46\n\t"
    "v_rcp_f32 v47, v47\n\t"
    "s_waitcnt lgkmcnt(4)\n\t"
    "v_mfma_f32_16x16x32_bf16 v[64:67], v[240:243], v[112:115], v[64:67]\n\t"
    "ds_read_b128 v[104:107], %[da] offset:0\n\t"
    "v_mad_u32_u24 v39, v39, %[mu], %[ad]\n\t"
    "v_mul_f32 v40, v48, v40\n\t"
    "v_mul_f32 v41, v49, v41\n\t"
    "v_mul_f32 v42, v50, v42\n\t"
    "v_mfma_f32_16x16x32_bf16 v[68:71], v[244:247], v[112:115], v[68:71]\n\t"
    "ds_read_b128 v[108:111], %[da] offset:2048\n\t"
    "v_cmp_ge_i32 vcc, %[lf], v39\n\t"
    "s_cmp_eq_u64 vcc, 0\n\t"
    "s_addc_u32 %[lf], %[lf], 0\n\t"
    "v_mul_f32 v43, v51, v43\n\t"
    "v_mul_f32 v44, v56, v44\n\t"
    "v_mul_f32 v45, v57, v45\n\t"
    "v_mfma_f32_16x16x32_bf16 v[72:75], v[240:243], v[116:119], v[72:75]\n\t"
    "v_mul_f32 v46, v58, v46\n\t"
    "v_mul_f32 v47, v59, v47\n\t"
    "v_mul_f32 v40, v40, v52\n\t"
    "v_mfma_f32_16x16x32_bf16 v[76:79], v[244:247], v[116:119], v[76:79]\n\t"
    "v_cmp_ge_i32 vcc, %[lf], v39\n\t"
    "s_cmp_eq_u64 vcc, 0\n\t"
    "s_addc_u32 %[lf], %[lf], 0\n\t"
    "v_mul_f32 v41, v41, v53\n\t"
    "v_mul_f32 v42, v42, v54\n\t"
    "v_mul_f32 v43, v43, v55\n\t"
    "s_waitcnt lgkmcnt(4)\n\t"
    "v_mfma_f32_16x16x32_bf16 v[80:83], v[248:251], v[120:123], v[64:67]\n\t"
    "ds_read_b128 v[112:115], %[ea] offset:4096\n\t"
    "s_min_i32 %[lf], %[lf], %[cp]\n\t"
    "s_add_u32 %[b], %[b], 1\n\t"
    "v_mul_f32 v44, v44, v60\n\t"
    "v_mul_f32 v45, v45, v61\n\t"
    "v_mul_f32 v46, v46, v62\n\t"
    "v_mfma_f32_16x16x32_bf16 v[84:87], v[252:255], v[120:123], v[68:71]\n\t"
    "ds_read_b128 v[116:119], %[ea] offset:6144\n\t"
    "v_mul_f32 v47, v47, v63\n\t"
    "v_cvt_pk_bf16_f32 v48, v40, v41\n\t"
    "v_cvt_pk_bf16_f32 v49, v42, v43\n\t"
    "v_mfma_f32_16x16x32_bf16 v[88:91], v[248:251], v[124:127], v[72:75]\n\t"
    "v_cvt_pk_bf16_f32 v50, v44, v45\n\t"
    "v_cvt_pk_bf16_f32 v51, v46, v47\n\t"
    "s_nop 1\n\t"
    "v_mfma_f32_16x16x32_bf16 v[92:95], v[252:255], v[124:127], v[76:79]\n\t"
    "v_permlane16_swap_b32 v48, v50\n\t"
    "v_permlane16_swap_b32 v49, v51\n\t"
    "global_store_dwordx4 %[co0], v[48:51], s[36:37] sc1\n\t"
    "s_mov_b64 s[36:37], s[38:39]\n\t"
    "s_add_u32 s38, s38, %[ab]\n\t"
    "s_addc_u32 s39, s39, 0\n\t"
    "s_cmp_lt_i32 %[b], %[lf]\n\t"
    "s_cbranch_scc1 1b\n\t"
    "s_waitcnt lgkmcnt(0)\n\t"
    "s_nop 4\n\t"
    "s_branch 9f\n\t"
    "5:\n\t"
    "s_add_u32 %[st], %[b], %[po]\n\t"
    "s_max_i32 %[st], %[st], %[p0]\n\t"
    "v_mov_b32 v39, %[st]\n\t"
    "v_mov_b32 v38, 0\n\t"
    "s_mov_b64 exec, 1\n\t"
    "global_store_dword v38, v39, %[pg] sc1\n\t"
    "s_mov_b64 exec, -1\n\t"
    "s_mov_b32 %[p0], %[p1]\n\t"
    "s_xor_b32 %[sl], %[sl], 0x100\n\t"
    "s_cmp_ge_i32 %[lf], %[cp]\n\t"
    "s_cbranch_scc1 6b\n\t"
    "s_mov_b32 m0, %[sl]\n\t"
    "s_nop 0\n\t"
    "global_load_lds_dword %[dv], %[db] sc1\n\t"
    "s_branch 6b\n\t"
    "9:"
    : "=&{v[80:95]}"(pd), [ea] "+&v"(ea), [da] "+&v"(da), [b] "+&s"(b), [lf] "+&s"(lf), "+{s[36:37]}"(p1), "+{s[38:39]}"(p0), [sa] "+&v"(sa), [st] "=&s"(st), [sl] "+&s"(sl), [p0] "+&s"(pf)
    : [rw] "v"(rw), [ab] "s"(ab), [cp] "s"(cp), [mu] "s"(mu), [ad] "v"(ad), "{v[128:159]}"(W[0]), "{v[160:191]}"(W[1]), "{v[192:223]}"(W[2]), "{v[224:255]}"(W[3]), [co0] "v"(co0), [w0] "s"(w0), [dv] "v"(dv), [db] "s"(db), [pg] "s"(pg), [po] "s"(po - 1), [p1] "s"(b + po), [nl] "s"(-LOG2E)
    : "v38", "v39", "v40", "v41", "v42", "v43", "v44", "v45", "v46", "v47", "v48", "v49", "v50", "v51", "v52", "v53", "v54", "v55", "v56", "v57", "v58", "v59", "v60", "v61", "v62", "v63", "v64", "v65", "v66", "v67", "v68", "v69", "v70", "v71", "v72", "v73", "v74", "v75", "v76", "v77", "v78", "v79", "v96", "v97", "v98", "v99", "v100", "v101", "v102", "v103", "v104", "v105", "v106", "v107", "v108", "v109", "v110", "v111", "v112", "v113", "v114", "v115", "v116", "v117", "v118", "v119", "v120", "v121", "v122", "v123", "v124", "v125", "v126", "v127", "vcc", "scc", "m0", "memory");
}
__device__ __forceinline__ void fast_st2(const u32x32 (&W)[4], u32x16& pd, unsigned ea, unsigned da, int& b, int& lf, int cp, unsigned sa, unsigned mu, unsigned ad,
    unsigned rw, unsigned ab, const char* p1, const char* p0, unsigned co0, unsigned co1, int po, int pf, unsigned w0, unsigned sl, unsigned dv, const char* db, const char* pg) {
  unsigned st;
  asm volatile("s_barrier\n\t"
    "ds_read_b128 v[96:99], %[ea] offset:0\n\t"
    "ds_read_b128 v[100:103], %[ea] offset:2048\n\t"
    "ds_read_b128 v[104:107], %[da] offset:0\n\t"
    "ds_read_b128 v[108:111], %[da] offset:2048\n\t"
    "ds_read_b128 v[112:115], %[ea] offset:4096\n\t"
    "ds_read_b128 v[116:119], %[ea] offset:6144\n\t"
    "s_branch 2f\n\t"
    "1:\n\t"
    "s_barrier\n\t"
    "2:\n\t"
    "s_waitcnt lgkmcnt(4)\n\t"
    "v_mfma_f32_16x16x32_bf16 v[64:67], v[128:131], v[96:99], 0\n\t"
    "ds_read_b128 v[120:123], %[da] offset:4096\n\t"
    "v_mfma_f32_16x16x32_bf16 v[68:71], v[132:135], v[96:99], 0\n\t"
    "ds_read_b128 v[124:127], %[da] offset:6144\n\t"
    "v_mfma_f32_16x16x32_bf16 v[72:75], v[128:131], v[100:103], 0\n\t"
    "v_mfma_f32_16x16x32_bf16 v[76:79], v[132:135], v[100:103], 0\n\t"
    "s_waitcnt lgkmcnt(4)\n\t"
    "v_mfma_f32_16x16x32_bf16 v[64:67], v[136:139], v[104:107], v[64:67]\n\t"
    "ds_read_b128 v[96:99], %[ea] offset:8192\n\t"
    "v_mfma_f32_16x16x32_bf16 v[68:71], v[140:143], v[104:107], v[68:71]\n\t"
    "ds_read_b128 v[100:103], %[ea] offset:10240\n\t"
    "v_mfma_f32_16x16x32_bf16 v[72:75], v[136:139], v[108:111], v[72:75]\n\t"
    "v_mfma_f32_16x16x32_bf16 v[76:79], v[140:143], v[108:111], v[76:79]\n\t"
    "s_waitcnt lgkmcnt(4)\n\t"
    "v_mfma_f32_16x16x32_bf16 v[64:67], v[144:147], v[112:115], v[64:67]\n\t"
    "ds_read_b128 v[104:107], %[da] offset:8192\n\t"
    "v_mfma_f32_16x16x32_bf16 v[68:71], v[148:151], v[112:115], v[68:71]\n\t"
    "ds_read_b128 v[108:111], %[da] offset:10240\n\t"
    "v_mfma_f32_16x16x32_bf16 v[72:75], v[144:147], v[116:119], v[72:75]\n\t"
    "v_mfma_f32_16x16x32_bf16 v[76:79], v[148:151], v[116:119], v[76:79]\n\t"
    "s_waitcnt lgkmcnt(4)\n\t"
    "v_mfma_f32_16x16x32_bf16 v[64:67], v[152:155], v[120:123], v[64:67]\n\t"
    "ds_read_b128 v[112:115], %[ea] offset:12288\n\t"
    "v_mfma_f32_16x16x32_bf16 v[68:71], v[156:159], v[120:123], v[68:71]\n\t"
    "ds_read_b128 v[116:119], %[ea] offset:14336\n\t"
    "v_mfma_f32_16x16x32_bf16 v[72:75], v[152:155], v[124:127], v[72:75]\n\t"
    "v_mfma_f32_16x16x32_bf16 v[76:79], v[156:159], v[124:127], v[76:79]\n\t"
    "s_waitcnt lgkmcnt(4)\n\t"
    "v_mfma_f32_16x16x32_bf16 v[64:67], v[160:163], v[96:99], v[64:67]\n\t"
    "ds_read_b128 v[120:123], %[da] offset:12288\n\t"
    "v_mfma_f32_16x16x32_bf16 v[68:71], v[164:167], v[96:99], v[68:71]\n\t"
    "ds_read_b128 v[124:127], %[da] offset:14336\n\t"
    "v_mfma_f32_16x16x32_bf16 v[72:75], v[160:163], v[100:103], v[72:75]\n\t"
    "v_mfma_f32_16x16x32_bf16 v[76:79], v[164:167], v[100:103], v[76:79]\n\t"
    "s_waitcnt lgkmcnt(4)\n\t"
    "v_mfma_f32_16x16x32_bf16 v[64:67], v[168:171], v[104:107], v[64:67]\n\t"
    "ds_read_b128 v[96:99], %[ea] offset:16384\n\t"
    "v_mfma_f32_16x16x32_bf16 v[68:71], v[172:175], v[104:107], v[68:71]\n\t"
    "ds_read_b128 v[100:103], %[ea] offset:18432\n\t"
    "v_mfma_f32_16x16x32_bf16 v[72:75], v[168:171], v[108:111], v[72:75]\n\t"
    "v_mfma_f32_16x16x32_bf16 v[76:79], v[172:175], v[108:111], v[76:79]\n\t"
    "s_waitcnt lgkmcnt(4)\n\t"
    "v_mfma_f32_16x16x32_bf16 v[64:67], v[176:179], v[112:115], v[64:67]\n\t"
    "ds_read_b128 v[104:107], %[da] offset:16384\n\t"
    "v_mfma_f32_16x16x32_bf16 v[68:71], v[180:183], v[112:115], v[68:71]\n\t"
    "ds_read_b128 v[108:111], %[da] offset:18432\n\t"
    "v_mfma_f32_16x16x32_bf16 v[72:75], v[176:179], v[116:119], v[72:75]\n\t"
    "v_mfma_f32_16x16x32_bf16 v[76:79], v[180:183], v[116:119], v[76:79]\n\t"
    "s_waitcnt lgkmcnt(4)\n\t"
    "v_mfma_f32_16x16x32_bf16 v[64:67], v[184:187], v[120:123], v[64:67]\n\t"
    "ds_read_b128 v[112:115], %[ea] offset:20480\n\t"
    "v_mfma_f32_16x16x32_bf16 v[68:71], v[188:191], v[120:123], v[68:71]\n\t"
    "ds_read_b128 v[116:119], %[ea] offset:22528\n\t"
    "v_mfma_f32_16x16x32_bf16 v[72:75], v[184:187], v[124:127], v[72:75]\n\t"
    "v_mfma_f32_16x16x32_bf16 v[76:79], v[188:191], v[124:127], v[76:79]\n\t"
    "s_waitcnt vmcnt(0)\n\t"
    "s_barrier\n\t"
    "s_waitcnt lgkmcnt(4)\n\t"
    "v_mfma_f32_16x16x32_bf16 v[64:67], v[192:195], v[96:99], v[64:67]\n\t"
    "ds_read_b128 v[120:123], %[da] offset:20480\n\t"
    "v_mfma_f32_16x16x32_bf16 v[68:71], v[196:199], v[96:99], v[68:71]\n\t"
    "ds_read_b128 v[124:127], %[da] offset:22528\n\t"
    "v_mfma_f32_16x16x32_bf16 v[72:75], v[192:195], v[100:103], v[72:75]\n\t"
    "ds_read_b128 v[48:51], %[rw] offset:0\n\t"
    "v_mfma_f32_16x16x32_bf16 v[76:79], v[196:199], v[100:103], v[76:79]\n\t"
    "ds_read_b128 v[52:55], %[rw] offset:1024\n\t"
    "s_cmp_eq_u32 %[w0], 0\n\t"
    "s_cbranch_scc1 5f\n\t"
    "6:\n\t"
    "s_waitcnt lgkmcnt(6)\n\t"
    "v_mfma_f32_16x16x32_bf16 v[64:67], v[200:203], v[104:107], v[64:67]\n\t"
    "ds_read_b128 v[96:99], %[ea] offset:24576\n\t"
    "v_mfma_f32_16x16x32_bf16 v[68:71], v[204:207], v[104:107], v[68:71]\n\t"
    "ds_read_b128 v[100:103], %[ea] offset:26624\n\t"
    "v_mfma_f32_16x16x32_bf16 v[72:75], v[200:203], v[108:111], v[72:75]\n\t"
    "ds_read_b128 v[56:59], %[rw] offset:2048\n\t"
    "v_mfma_f32_16x16x32_bf16 v[76:79], v[204:207], v[108:111], v[76:79]\n\t"
    "ds_read_b128 v[60:63], %[rw] offset:3072\n\t"
    "s_waitcnt lgkmcnt(0)\n\t"
    "v_mfma_f32_16x16x32_bf16 v[64:67], v[208:211], v[112:115], v[64:67]\n\t"
    "ds_read_b128 v[104:107], %[da] offset:24576\n\t"
    "v_cvt_pk_bf16_f32 v40, v80, v81\n\t"
    "v_mfma_f32_16x16x32_bf16 v[68:71], v[212:215], v[112:115], v[68:71]\n\t"
    "ds_read_b128 v[108:111], %[da] offset:26624\n\t"
    "v_cvt_pk_bf16_f32 v41, v82, v83\n\t"
    "v_mfma_f32_16x16x32_bf16 v[72:75], v[208:211], v[116:119], v[72:75]\n\t"
    "ds_read_b32 v39, %[sa]\n\t"
    "v_cvt_pk_bf16_f32 v42, v84, v85\n\t"
    "v_mfma_f32_16x16x32_bf16 v[76:79], v[212:215], v[116:119], v[76:79]\n\t"
    "v_xor_b32 %[sa], 0x100, %[sa]\n\t"
    "v_cvt_pk_bf16_f32 v43, v86, v87\n\t"
    "v_mfma_f32_16x16x32_bf16 v[64:67], v[216:219], v[120:123], v[64:67]\n\t"
    "ds_read_b128 v[112:115], %[ea] offset:28672\n\t"
    "global_store_dwordx4 %[co0], v[40:43], s[36:37] sc1\n\t"
    "v_mfma_f32_16x16x32_bf16 v[68:71], v[220:223], v[120:123], v[68:71]\n\t"
    "ds_read_b128 v[116:119], %[ea] offset:30720\n\t"
    "v_cvt_pk_bf16_f32 v44, v88, v89\n\t"
    "v_mfma_f32_16x16x32_bf16 v[72:75], v[216:219], v[124:127], v[72:75]\n\t"
    "v_xor_b32 %[ea], 0x10000, %[ea]\n\t"
    "v_cvt_pk_bf16_f32 v45, v90, v91\n\t"
    "v_mfma_f32_16x16x32_bf16 v[76:79], v[220:223], v[124:127], v[76:79]\n\t"
    "v_cvt_pk_bf16_f32 v46, v92, v93\n\t"
    "v_mfma_f32_16x16x32_bf16 v[64:67], v[224:227], v[96:99], v[64:67]\n\t"
    "ds_read_b128 v[120:123], %[da] offset:28672\n\t"
    "v_cvt_pk_bf16_f32 v47, v94, v95\n\t"
    "v_mfma_f32_16x16x32_bf16 v[68:71], v[228:231], v[96:99], v[68:71]\n\t"
    "ds_read_b128 v[124:127], %[da] offset:30720\n\t"
    "global_store_dwordx4 %[co1], v[44:47], s[36:37] sc1\n\t"
    "v_mfma_f32_16x16x32_bf16 v[72:75], v[224:227], v[100:103], v[72:75]\n\t"
    "v_xor_b32 %[da], 0x10000, %[da]\n\t"
    "v_cvt_pk_bf16_f32 v48, v48, v49\n\t"
    "v_mfma_f32_16x16x32_bf16 v[76:79], v[228:231], v[100:103], v[76:79]\n\t"
    "v_cvt_pk_bf16_f32 v49, v50, v51\n\t"
    "s_waitcnt lgkmcnt(5)\n\t"
    "v_mfma_f32_16x16x32_bf16 v[64:67], v[232:235], v[104:107], v[64:67]\n\t"
    "ds_read_b128 v[96:99], %[ea] offset:0\n\t"
    "v_cvt_pk_bf16_f32 v50, v52, v53\n\t"
    "v_mfma_f32_16x16x32_bf16 v[68:71], v[236:239], v[104:107], v[68:71]\n\t"
    "ds_read_b128 v[100:103], %[ea] offset:2048\n\t"
    "v_cvt_pk_bf16_f32 v51, v54, v55\n\t"
    "v_mfma_f32_16x16x32_bf16 v[72:75], v[232:235], v[108:111], v[72:75]\n\t"
    "global_store_dwordx4 %[co0], v[48:51], s[36:37] offset:2048 sc1\n\t"
    "v_mfma_f32_16x16x32_bf16 v[76:79], v[236:239], v[108:111], v[76:79]\n\t"
    "v_cvt_pk_bf16_f32 v56, v56, v57\n\t"
    "s_waitcnt lgkmcnt(4)\n\t"
    "v_mfma_f32_16x16x32_bf16 v[64:67], v[240:243], v[112:115], v[64:67]\n\t"
    "ds_read_b128 v[104:107], %[da] offset:0\n\t"
    "v_mad_u32_u24 v39, v39, %[mu], %[ad]\n\t"
    "v_cvt_pk_bf16_f32 v57, v58, v59\n\t"
    "v_mfma_f32_16x16x32_bf16 v[68:71], v[244:247], v[112:115], v[68:71]\n\t"
    "ds_read_b128 v[108:111], %[da] offset:2048\n\t"
    "v_cmp_ge_i32 vcc, %[lf], v39\n\t"
    "s_cmp_eq_u64 vcc, 0\n\t"
    "s_addc_u32 %[lf], %[lf], 0\n\t"
    "v_cvt_pk_bf16_f32 v58, v60, v61\n\t"
    "v_mfma_f32_16x16x32_bf16 v[72:75], v[240:243], v[116:119], v[72:75]\n\t"
    "v_cvt_pk_bf16_f32 v59, v62, v63\n\t"
    "v_mfma_f32_16x16x32_bf16 v[76:79], v[244:247], v[116:119], v[76:79]\n\t"
    "v_cmp_ge_i32 vcc, %[lf], v39\n\t"
    "s_cmp_eq_u64 vcc, 0\n\t"
    "s_addc_u32 %[lf], %[lf], 0\n\t"
    "global_store_dwordx4 %[co1], v[56:59], s[36:37] offset:2048 sc1\n\t"
    "s_waitcnt lgkmcnt(4)\n\t"
    "v_mfma_f32_16x16x32_bf16 v[80:83], v[248:251], v[120:123], v[64:67]\n\t"
    "ds_read_b128 v[112:115], %[ea] offset:4096\n\t"
    "s_min_i32 %[lf], %[lf], %[cp]\n\t"
    "s_add_u32 %[b], %[b], 1\n\t"
    "v_mfma_f32_16x16x32_bf16 v[84:87], v[252:255], v[120:123], v[68:71]\n\t"
    "ds_read_b128 v[116:119], %[ea] offset:6144\n\t"
    "v_mfma_f32_16x16x32_bf16 v[88:91], v[248:251], v[124:127], v[72:75]\n\t"
    "v_mfma_f32_16x16x32_bf16 v[92:95], v[252:255], v[124:127], v[76:79]\n\t"
    "s_mov_b64 s[36:37], s[38:39]\n\t"
    "s_add_u32 s38, s38, %[ab]\n\t"
    "s_addc_u32 s39, s39, 0\n\t"
    "s_cmp_lt_i32 %[b], %[lf]\n\t"
    "s_cbranch_scc1 1b\n\t"
    "s_waitcnt lgkmcnt(0)\n\t"
    "s_nop 4\n\t"
    "s_branch 9f\n\t"
    "5:\n\t"
    "s_add_u32 %[st], %[b], %[po]\n\t"
    "s_max_i32 %[st], %[st], %[p0]\n\t"
    "v_mov_b32 v39, %[st]\n\t"
    "v_mov_b32 v38, 0\n\t"
    "s_mov_b64 exec, 1\n\t"
    "global_store_dword v38, v39, %[pg] sc1\n\t"
    "s_mov_b64 exec, -1\n\t"
    "s_mov_b32 %[p0], %[p1]\n\t"
    "s_xor_b32 %[sl], %[sl], 0x100\n\t"
    "s_cmp_ge_i32 %[lf], %[cp]\n\t"
    "s_cbranch_scc1 6b\n\t"
    "s_mov_b32 m0, %[sl]\n\t"
    "s_nop 0\n\t"
    "global_load_lds_dword %[dv], %[db] sc1\n\t"
    "s_branch 6b\n\t"
    "9:"
    : "=&{v[80:95]}"(pd), [ea] "+&v"(ea), [da] "+&v"(da), [b] "+&s"(b), [lf] "+&s"(lf), "+{s[36:37]}"(p1), "+{s[38:39]}"(p0), [sa] "+&v"(sa), [st] "=&s"(st), [sl] "+&s"(sl), [p0] "+&s"(pf)
    : [rw] "v"(rw), [ab] "s"(ab), [cp] "s"(cp), [mu] "s"(mu), [ad] "v"(ad), "{v[128:159]}"(W[0]), "{v[160:191]}"(W[1]), "{v[192:223]}"(W[2]), "{v[224:255]}"(W[3]), [co0] "v"(co0), [w0] "s"(w0), [dv] "v"(dv), [db] "s"(db), [pg] "s"(pg), [po] "s"(po - 1), [p1] "s"(b + po), [co1] "v"(co1)
    : "v38", "v39", "v40", "v41", "v42", "v43", "v44", "v45", "v46", "v47", "v48", "v49", "v50", "v51", "v52", "v53", "v54", "v55", "v56", "v57", "v58", "v59", "v60", "v61", "v62", "v63", "v64", "v65", "v66", "v67", "v68", "v69", "v70", "v71", "v72", "v73", "v74", "v75", "v76", "v77", "v78", "v79", "v96", "v97", "v98", "v99", "v100", "v101", "v102", "v103", "v104", "v105", "v106", "v107", "v108", "v109", "v110", "v111", "v112", "v113", "v114", "v115", "v116", "v117", "v118", "v119", "v120", "v121", "v122", "v123", "v124", "v125", "v126", "v127", "vcc", "scc", "m0", "memory");
}
__device__ __forceinline__ void near_ld0(const u32x32 (&W)[4], u32x16& pd, unsigned ea, unsigned da, int& b, int& lf, int cp, unsigned sa, unsigned mu, unsigned ad,
    unsigned rw, unsigned ab, const char* p1, const char* p0, unsigned l1, unsigned l0, const unsigned (&av)[8]) {
  asm volatile("s_setprio 0\n\t"
    "s_waitcnt vmcnt(4)\n\t"
    "s_barrier\n\t"
    "ds_read_b128 v[96:99], %[ea] offset:0\n\t"
    "ds_read_b128 v[100:103], %[ea] offset:2048\n\t"
    "ds_read_b128 v[104:107], %[da] offset:0\n\t"
    "ds_read_b128 v[108:111], %[da] offset:2048\n\t"
    "ds_read_b128 v[112:115], %[ea] offset:4096\n\t"
    "ds_read_b128 v[116:119], %[ea] offset:6144\n\t"
    "s_branch 2f\n\t"
    "1:\n\t"
    "s_waitcnt vmcnt(4)\n\t"
    "s_nop 1\n\t"
    "s_barrier\n\t"
    "2:\n\t"
    "s_waitcnt lgkmcnt(4)\n\t"
    "v_mfma_f32_16x16x32_bf16 v[64:67], v[128:131], v[96:99], 0\n\t"
    "ds_read_b128 v[120:123], %[da] offset:4096\n\t"
    "s_add_u32 m0, %[l1], 0\n\t"
    "ds_write_b128 %[rw], v[80:83] offset:0\n\t"
    "v_mfma_f32_16x16x32_bf16 v[68:71], v[132:135], v[96:99], 0\n\t"
    "ds_read_b128 v[124:127], %[da] offset:6144\n\t"
    "global_load_lds_dwordx4 %[av0], s[36:37] sc1\n\t"
    "ds_write_b128 %[rw], v[84:87] offset:1024\n\t"
    "v_mfma_f32_16x16x32_bf16 v[72:75], v[128:131], v[100:103], 0\n\t"
    "s_add_u32 m0, %[l1], 32768\n\t"
    "ds_write_b128 %[rw], v[88:91] offset:2048\n\t"
    "v_mfma_f32_16x16x32_bf16 v[76:79], v[132:135], v[100:103], 0\n\t"
    "global_load_lds_dwordx4 %[av1], s[36:37] sc1\n\t"
    "ds_write_b128 %[rw], v[92:95] offset:3072\n\t"
    "s_waitcnt lgkmcnt(8)\n\t"
    "v_mfma_f32_16x16x32_bf16 v[64:67], v[136:139], v[104:107], v[64:67]\n\t"
    "ds_read_b128 v[96:99], %[ea] offset:8192\n\t"
    "s_add_u32 m0, %[l1], 4096\n\t"
    "v_mfma_f32_16x16x32_bf16 v[68:71], v[140:143], v[104:107], v[68:71]\n\t"
    "ds_read_b128 v[100:103], %[ea] offset:10240\n\t"
    "global_load_lds_dwordx4 %[av2], s[36:37] sc1\n\t"
    "v_mfma_f32_16x16x32_bf16 v[72:75], v[136:139], v[108:111], v[72:75]\n\t"
    "s_add_u32 m0, %[l1], 36864\n\t"
    "v_mfma_f32_16x16x32_bf16 v[76:79], v[140:143], v[108:111], v[76:79]\n\t"
    "global_load_lds_dwordx4 %[av3], s[36:37] sc1\n\t"
    "s_waitcnt lgkmcnt(2)\n\t"
    "v_mfma_f32_16x16x32_bf16 v[64:67], v[144:147], v[112:115], v[64:67]\n\t"
    "ds_read_b128 v[104:107], %[da] offset:8192\n\t"
    "s_add_u32 m0, %[l1], 8192\n\t"
    "v_mfma_f32_16x16x32_bf16 v[68:71], v[148:151], v[112:115], v[68:71]\n\t"
    "ds_read_b128 v[108:111], %[da] offset:10240\n\t"
    "global_load_lds_dwordx4 %[av4], s[36:37] sc1\n\t"
    "v_mfma_f32_16x16x32_bf16 v[72:75], v[144:147], v[116:119], v[72:75]\n\t"
    "s_add_u32 m0, %[l1], 40960\n\t"
    "v_mfma_f32_16x16x32_bf16 v[76:79], v[148:151], v[116:119], v[76:79]\n\t"
    "global_load_lds_dwordx4 %[av5], s[36:37] sc1\n\t"
    "s_waitcnt vmcnt(6)\n\t"
    "s_barrier\n\t"
    "v_mfma_f32_16x16x32_bf16 v[64:67], v[152:155], v[120:123], v[64:67]\n\t"
    "ds_read_b128 v[112:115], %[ea] offset:12288\n\t"
    "s_add_u32 m0, %[l1], 12288\n\t"
    "v_mfma_f32_16x16x32_bf16 v[68:71], v[156:159], v[120:123], v[68:71]\n\t"
    "ds_read_b128 v[116:119], %[ea] offset:14336\n\t"
    "global_load_lds_dwordx4 %[av6], s[36:37] sc1\n\t"
    "v_mfma_f32_16x16x32_bf16 v[72:75], v[152:155], v[124:127], v[72:75]\n\t"
    "s_add_u32 m0, %[l1], 45056\n\t"
    "v_mfma_f32_16x16x32_bf16 v[76:79], v[156:159], v[124:127], v[76:79]\n\t"
    "global_load_lds_dwordx4 %[av7], s[36:37] sc1\n\t"
    "s_waitcnt lgkmcnt(4)\n\t"
    "v_mfma_f32_16x16x32_bf16 v[64:67], v[160:163], v[96:99], v[64:67]\n\t"
    "ds_read_b128 v[120:123], %[da] offset:12288\n\t"
    "s_add_u32 m0, %[l0], 0\n\t"
    "v_mfma_f32_16x16x32_bf16 v[68:71], v[164:167], v[96:99], v[68:71]\n\t"
    "ds_read_b128 v[124:127], %[da] offset:14336\n\t"
    "global_load_lds_dwordx4 %[av0], s[38:39] sc1\n\t"
    "v_mfma_f32_16x16x32_bf16 v[72:75], v[160:163], v[100:103], v[72:75]\n\t"
    "s_add_u32 m0, %[l0], 32768\n\t"
    "v_mfma_f32_16x16x32_bf16 v[76:79], v[164:167], v[100:103], v[76:79]\n\t"
    "global_load_lds_dwordx4 %[av1], s[38:39] sc1\n\t"
    "s_waitcnt lgkmcnt(4)\n\t"
    "v_mfma_f32_16x16x32_bf16 v[64:67], v[168:171], v[104:107], v[64:67]\n\t"
    "ds_read_b128 v[96:99], %[ea] offset:16384\n\t"
    "s_add_u32 m0, %[l0], 4096\n\t"
    "v_mfma_f32_16x16x32_bf16 v[68:71], v[172:175], v[104:107], v[68:71]\n\t"
    "ds_read_b128 v[100:103], %[ea] offset:18432\n\t"
    "global_load_lds_dwordx4 %[av2], s[38:39] sc1\n\t"
    "v_mfma_f32_16x16x32_bf16 v[72:75], v[168:171], v[108:111], v[72:75]\n\t"
    "s_add_u32 m0, %[l0], 36864\n\t"
    "v_mfma_f32_16x16x32_bf16 v[76:79], v[172:175], v[108:111], v[76:79]\n\t"
    "global_load_lds_dwordx4 %[av3], s[38:39] sc1\n\t"
    "s_waitcnt lgkmcnt(4)\n\t"
    "v_mfma_f32_16x16x32_bf16 v[64:67], v[176:179], v[112:115], v[64:67]\n\t"
    "ds_read_b128 v[104:107], %[da] offset:16384\n\t"
    "s_add_u32 m0, %[l0], 8192\n\t"
    "v_mfma_f32_16x16x32_bf16 v[68:71], v[180:183], v[112:115], v[68:71]\n\t"
    "ds_read_b128 v[108:111], %[da] offset:18432\n\t"
    "global_load_lds_dwordx4 %[av4], s[38:39] sc1\n\t"
    "v_mfma_f32_16x16x32_bf16 v[72:75], v[176:179], v[116:119], v[72:75]\n\t"
    "s_add_u32 m0, %[l0], 40960\n\t"
    "v_mfma_f32_16x16x32_bf16 v[76:79], v[180:183], v[116:119], v[76:79]\n\t"
    "global_load_lds_dwordx4 %[av5], s[38:39] sc1\n\t"
    "s_waitcnt lgkmcnt(4)\n\t"
    "v_mfma_f32_16x16x32_bf16 v[64:67], v[184:187], v[120:123], v[64:67]\n\t"
    "ds_read_b128 v[112:115], %[ea] offset:20480\n\t"
    "s_add_u32 m0, %[l0], 12288\n\t"
    "v_mfma_f32_16x16x32_bf16 v[68:71], v[188:191], v[120:123], v[68:71]\n\t"
    "ds_read_b128 v[116:119], %[ea] offset:22528\n\t"
    "global_load_lds_dwordx4 %[av6], s[38:39] sc1\n\t"
    "v_mfma_f32_16x16x32_bf16 v[72:75], v[184:187], v[124:127], v[72:75]\n\t"
    "s_add_u32 m0, %[l0], 45056\n\t"
    "v_mfma_f32_16x16x32_bf16 v[76:79], v[188:191], v[124:127], v[76:79]\n\t"
    "global_load_lds_dwordx4 %[av7], s[38:39] sc1\n\t"
    "s_waitcnt lgkmcnt(4)\n\t"
    "v_mfma_f32_16x16x32_bf16 v[64:67], v[192:195], v[96:99], v[64:67]\n\t"
    "ds_read_b128 v[120:123], %[da] offset:20480\n\t"
    "v_mfma_f32_16x16x32_bf16 v[68:71], v[196:199], v[96:99], v[68:71]\n\t"
    "ds_read_b128 v[124:127], %[da] offset:22528\n\t"
    "v_mfma_f32_16x16x32_bf16 v[72:75], v[192:195], v[100:103], v[72:75]\n\t"
    "v_mfma_f32_16x16x32_bf16 v[76:79], v[196:199], v[100:103], v[76:79]\n\t"
    "s_waitcnt lgkmcnt(4)\n\t"
    "v_mfma_f32_16x16x32_bf16 v[64:67], v[200:203], v[104:107], v[64:67]\n\t"
    "ds_read_b128 v[96:99], %[ea] offset:24576\n\t"
    "v_mfma_f32_16x16x32_bf16 v[68:71], v[204:207], v[104:107], v[68:71]\n\t"
    "ds_read_b128 v[100:103], %[ea] offset:26624\n\t"
    "v_mfma_f32_16x16x32_bf16 v[72:75], v[200:203], v[108:111], v[72:75]\n\t"
    "v_mfma_f32_16x16x32_bf16 v[76:79], v[204:207], v[108:111], v[76:79]\n\t"
    "s_add_u32 s36, s36, %[ab]\n\t"
    "s_addc_u32 s37, s37, 0\n\t"
    "s_waitcnt lgkmcnt(4)\n\t"
    "v_mfma_f32_16x16x32_bf16 v[64:67], v[208:211], v[112:115], v[64:67]\n\t"
    "ds_read_b128 v[104:107], %[da] offset:24576\n\t"
    "v_mfma_f32_16x16x32_bf16 v[68:71], v[212:215], v[112:115], v[68:71]\n\t"
    "ds_read_b128 v[108:111], %[da] offset:26624\n\t"
    "v_mfma_f32_16x16x32_bf16 v[72:75], v[208:211], v[116:119], v[72:75]\n\t"
    "v_mfma_f32_16x16x32_bf16 v[76:79], v[212:215], v[116:119], v[76:79]\n\t"
    "s_xor_b32 %[l1], %[l1], 0x10000\n\t"
    "s_waitcnt lgkmcnt(4)\n\t"
    "v_mfma_f32_16x16x32_bf16 v[64:67], v[216:219], v[120:123], v[64:67]\n\t"
    "ds_read_b128 v[112:115], %[ea] offset:28672\n\t"
    "v_mfma_f32_16x16x32_bf16 v[68:71], v[220:223], v[120:123], v[68:71]\n\t"
    "ds_read_b128 v[116:119], %[ea] offset:30720\n\t"
    "v_mfma_f32_16x16x32_bf16 v[72:75], v[216:219], v[124:127], v[72:75]\n\t"
    "v_xor_b32 %[ea], 0x10000, %[ea]\n\t"
    "v_mfma_f32_16x16x32_bf16 v[76:79], v[220:223], v[124:127], v[76:79]\n\t"
    "s_waitcnt vmcnt(12)\n\t"
    "s_barrier\n\t"
    "s_waitcnt lgkmcnt(4)\n\t"
    "v_mfma_f32_16x16x32_bf16 v[64:67], v[224:227], v[96:99], v[64:67]\n\t"
    "ds_read_b32 v39, %[sa]\n\t"
    "ds_read_b128 v[120:123], %[da] offset:28672\n\t"
    "v_mfma_f32_16x16x32_bf16 v[68:71], v[228:231], v[96:99], v[68:71]\n\t"
    "ds_read_b128 v[124:127], %[da] offset:30720\n\t"
    "v_mfma_f32_16x16x32_bf16 v[72:75], v[224:227], v[100:103], v[72:75]\n\t"
    "v_xor_b32 %[da], 0x10000, %[da]\n\t"
    "v_mfma_f32_16x16x32_bf16 v[76:79], v[228:231], v[100:103], v[76:79]\n\t"
    "s_waitcnt lgkmcnt(5)\n\t"
    "v_mfma_f32_16x16x32_bf16 v[64:67], v[232:235], v[104:107], v[64:67]\n\t"
    "ds_read_b128 v[96:99], %[ea] offset:0\n\t"
    "v_mfma_f32_16x16x32_bf16 v[68:71], v[236:239], v[104:107], v[68:71]\n\t"
    "ds_read_b128 v[100:103], %[ea] offset:2048\n\t"
    "v_mfma_f32_16x16x32_bf16 v[72:75], v[232:235], v[108:111], v[72:75]\n\t"
    "v_mfma_f32_16x16x32_bf16 v[76:79], v[236:239], v[108:111], v[76:79]\n\t"
    "s_waitcnt lgkmcnt(4)\n\t"
    "v_mfma_f32_16x16x32_bf16 v[64:67], v[240:243], v[112:115], v[64:67]\n\t"
    "ds_read_b128 v[104:107], %[da] offset:0\n\t"
    "v_mad_u32_u24 v39, v39, %[mu], %[ad]\n\t"
    "v_mfma_f32_16x16x32_bf16 v[68:71], v[244:247], v[112:115], v[68:71]\n\t"
    "ds_read_b128 v[108:111], %[da] offset:2048\n\t"
    "v_cmp_ge_i32 vcc, %[lf], v39\n\t"
    "s_cmp_eq_u64 vcc, 0\n\t"
    "s_addc_u32 %[lf], %[lf], 0\n\t"
    "v_mfma_f32_16x16x32_bf16 v[72:75], v[240:243], v[116:119], v[72:75]\n\t"
    "v_mfma_f32_16x16x32_bf16 v[76:79], v[244:247], v[116:119], v[76:79]\n\t"
    "v_cmp_ge_i32 vcc, %[lf], v39\n\t"
    "s_cmp_eq_u64 vcc, 0\n\t"
    "s_addc_u32 %[lf], %[lf], 0\n\t"
    "s_waitcnt lgkmcnt(4)\n\t"
    "v_mfma_f32_16x16x32_bf16 v[80:83], v[248:251], v[120:123], v[64:67]\n\t"
    "ds_read_b128 v[112:115], %[ea] offset:4096\n\t"
    "s_min_i32 %[lf], %[lf], %[cp]\n\t"
    "s_add_u32 %[b], %[b], 1\n\t"
    "v_mfma_f32_16x16x32_bf16 v[84:87], v[252:255], v[120:123], v[68:71]\n\t"
    "ds_read_b128 v[116:119], %[ea] offset:6144\n\t"
    "v_mfma_f32_16x16x32_bf16 v[88:91], v[248:251], v[124:127], v[72:75]\n\t"
    "v_mfma_f32_16x16x32_bf16 v[92:95], v[252:255], v[124:127], v[76:79]\n\t"
    "s_add_u32 s38, s38, %[ab]\n\t"
    "s_addc_u32 s39, s39, 0\n\t"
    "s_xor_b32 %[l0], %[l0], 0x10000\n\t"
    "s_cmp_lt_i32 %[b], %[lf]\n\t"
    "s_cbranch_scc1 1b\n\t"
    "s_waitcnt lgkmcnt(0)\n\t"
    "s_nop 4"
    : "=&{v[80:95]}"(pd), [ea] "+&v"(ea), [da] "+&v"(da), [b] "+&s"(b), [lf] "+&s"(lf), "+{s[36:37]}"(p1), "+{s[38:39]}"(p0), [sa] "+&v"(sa), [l1] "+&s"(l1), [l0] "+&s"(l0)
    : [rw] "v"(rw), [ab] "s"(ab), [cp] "s"(cp), [mu] "s"(mu), [ad] "v"(ad), "{v[128:159]}"(W[0]), "{v[160:191]}"(W[1]), "{v[192:223]}"(W[2]), "{v[224:255]}"(W[3]), [av0] "v"(av[0]), [av1] "v"(av[1]), [av2] "v"(av[2]), [av3] "v"(av[3]), [av4] "v"(av[4]), [av5] "v"(av[5]), [av6] "v"(av[6]), [av7] "v"(av[7])
    : "v39", "v64", "v65", "v66", "v67", "v68", "v69", "v70", "v71", "v72", "v73", "v74", "v75", "v76", "v77", "v78", "v79", "v96", "v97", "v98", "v99", "v100", "v101", "v102", "v103", "v104", "v105", "v106", "v107", "v108", "v109", "v110", "v111", "v112", "v113", "v114", "v115", "v116", "v117", "v118", "v119", "v120", "v121", "v122", "v123", "v124", "v125", "v126", "v127", "vcc", "scc", "m0", "memory");
}
__device__ __forceinline__ void near_ld2(const u32x32 (&W)[4], u32x16& pd, unsigned ea, unsigned da, int& b, int& lf, int cp, unsigned sa, unsigned mu, unsigned ad,
    unsigned rw, unsigned ab, const char* p1, const char* p0, unsigned l1, unsigned l0, const unsigned (&av)[8]) {
  asm volatile("s_setprio 0\n\t"
    "s_waitcnt vmcnt(2)\n\t"
    "s_barrier\n\t"
    "ds_read_b128 v[96:99], %[ea] offset:0\n\t"
    "ds_read_b128 v[100:103], %[ea] offset:2048\n\t"
    "ds_read_b128 v[104:107], %[da] offset:0\n\t"
    "ds_read_b128 v[108:111], %[da] offset:2048\n\t"
    "ds_read_b128 v[112:115], %[ea] offset:4096\n\t"
    "ds_read_b128 v[116:119], %[ea] offset:6144\n\t"
    "s_branch 2f\n\t"
    "1:\n\t"
    "s_waitcnt vmcnt(2)\n\t"
    "s_nop 1\n\t"
    "s_barrier\n\t"
    "2:\n\t"
    "s_waitcnt lgkmcnt(4)\n\t"
    "v_mfma_f32_16x16x32_bf16 v[64:67], v[128:131], v[96:99], 0\n\t"
    "ds_read_b128 v[120:123], %[da] offset:4096\n\t"
    "s_add_u32 m0, %[l1], 0\n\t"
    "ds_write_b128 %[rw], v[80:83] offset:0\n\t"
    "v_mfma_f32_16x16x32_bf16 v[68:71], v[132:135], v[96:99], 0\n\t"
    "ds_read_b128 v[124:127], %[da] offset:6144\n\t"
    "global_load_lds_dwordx4 %[av0], s[36:37] sc1\n\t"
    "ds_write_b128 %[rw], v[84:87] offset:1024\n\t"
    "v_mfma_f32_16x16x32_bf16 v[72:75], v[128:131], v[100:103], 0\n\t"
    "ds_write_b128 %[rw], v[88:91] offset:2048\n\t"
    "v_mfma_f32_16x16x32_bf16 v[76:79], v[132:135], v[100:103], 0\n\t"
    "ds_write_b128 %[rw], v[92:95] offset:3072\n\t"
    "s_waitcnt lgkmcnt(8)\n\t"
    "v_mfma_f32_16x16x32_bf16 v[64:67], v[136:139], v[104:107], v[64:67]\n\t"
    "ds_read_b128 v[96:99], %[ea] offset:8192\n\t"
    "s_add_u32 m0, %[l1], 4096\n\t"
    "v_mfma_f32_16x16x32_bf16 v[68:71], v[140:143], v[104:107], v[68:71]\n\t"
    "ds_read_b128 v[100:103], %[ea] offset:10240\n\t"
    "global_load_lds_dwordx4 %[av2], s[36:37] sc1\n\t"
    "v_mfma_f32_16x16x32_bf16 v[72:75], v[136:139], v[108:111], v[72:75]\n\t"
    "v_mfma_f32_16x16x32_bf16 v[76:79], v[140:143], v[108:111], v[76:79]\n\t"
    "s_waitcnt lgkmcnt(2)\n\t"
    "v_mfma_f32_16x16x32_bf16 v[64:67], v[144:147], v[112:115], v[64:67]\n\t"
    "ds_read_b128 v[104:107], %[da] offset:8192\n\t"
    "s_add_u32 m0, %[l1], 8192\n\t"
    "v_mfma_f32_16x16x32_bf16 v[68:71], v[148:151], v[112:115], v[68:71]\n\t"
    "ds_read_b128 v[108:111], %[da] offset:10240\n\t"
    "global_load_lds_dwordx4 %[av4], s[36:37] sc1\n\t"
    "v_mfma_f32_16x16x32_bf16 v[72:75], v[144:147], v[116:119], v[72:75]\n\t"
    "v_mfma_f32_16x16x32_bf16 v[76:79], v[148:151], v[116:119], v[76:79]\n\t"
    "s_waitcnt vmcnt(3)\n\t"
    "s_barrier\n\t"
    "v_mfma_f32_16x16x32_bf16 v[64:67], v[152:155], v[120:123], v[64:67]\n\t"
    "ds_read_b128 v[112:115], %[ea] offset:12288\n\t"
    "s_add_u32 m0, %[l1], 12288\n\t"
    "v_mfma_f32_16x16x32_bf16 v[68:71], v[156:159], v[120:123], v[68:71]\n\t"
    "ds_read_b128 v[116:119], %[ea] offset:14336\n\t"
    "global_load_lds_dwordx4 %[av6], s[36:37] sc1\n\t"
    "v_mfma_f32_16x16x32_bf16 v[72:75], v[152:155], v[124:127], v[72:75]\n\t"
    "v_mfma_f32_16x16x32_bf16 v[76:79], v[156:159], v[124:127], v[76:79]\n\t"
    "s_waitcnt lgkmcnt(4)\n\t"
    "v_mfma_f32_16x16x32_bf16 v[64:67], v[160:163], v[96:99], v[64:67]\n\t"
    "ds_read_b128 v[120:123], %[da] offset:12288\n\t"
    "s_add_u32 m0, %[l0], 0\n\t"
    "v_mfma_f32_16x16x32_bf16 v[68:71], v[164:167], v[96:99], v[68:71]\n\t"
    "ds_read_b128 v[124:127], %[da] offset:14336\n\t"
    "global_load_lds_dwordx4 %[av0], s[38:39] sc1\n\t"
    "v_mfma_f32_16x16x32_bf16 v[72:75], v[160:163], v[100:103], v[72:75]\n\t"
    "v_mfma_f32_16x16x32_bf16 v[76:79], v[164:167], v[100:103], v[76:79]\n\t"
    "s_waitcnt lgkmcnt(4)\n\t"
    "v_mfma_f32_16x16x32_bf16 v[64:67], v[168:171], v[104:107], v[64:67]\n\t"
    "ds_read_b128 v[96:99], %[ea] offset:16384\n\t"
    "s_add_u32 m0, %[l0], 4096\n\t"
    "v_mfma_f32_16x16x32_bf16 v[68:71], v[172:175], v[104:107], v[68:71]\n\t"
    "ds_read_b128 v[100:103], %[ea] offset:18432\n\t"
    "global_load_lds_dwordx4 %[av2], s[38:39] sc1\n\t"
    "v_mfma_f32_16x16x32_bf16 v[72:75], v[168:171], v[108:111], v[72:75]\n\t"
    "v_mfma_f32_16x16x32_bf16 v[76:79], v[172:175], v[108:111], v[76:79]\n\t"
    "s_waitcnt lgkmcnt(4)\n\t"
    "v_mfma_f32_16x16x32_bf16 v[64:67], v[176:179], v[112:115], v[64:67]\n\t"
    "ds_read_b128 v[104:107], %[da] offset:16384\n\t"
    "s_add_u32 m0, %[l0], 8192\n\t"
    "v_mfma_f32_16x16x32_bf16 v[68:71], v[180:183], v[112:115], v[68:71]\n\t"
    "ds_read_b128 v[108:111], %[da] offset:18432\n\t"
    "global_load_lds_dwordx4 %[av4], s[38:39] sc1\n\t"
    "v_mfma_f32_16x16x32_bf16 v[72:75], v[176:179], v[116:119], v[72:75]\n\t"
    "v_mfma_f32_16x16x32_bf16 v[76:79], v[180:183], v[116:119], v[76:79]\n\t"
    "s_waitcnt lgkmcnt(4)\n\t"
    "v_mfma_f32_16x16x32_bf16 v[64:67], v[184:187], v[120:123], v[64:67]\n\t"
    "ds_read_b128 v[112:115], %[ea] offset:20480\n\t"
    "s_add_u32 m0, %[l0], 12288\n\t"
    "v_mfma_f32_16x16x32_bf16 v[68:71], v[188:191], v[120:123], v[68:71]\n\t"
    "ds_read_b128 v[116:119], %[ea] offset:22528\n\t"
    "global_load_lds_dwordx4 %[av6], s[38:39] sc1\n\t"
    "v_mfma_f32_16x16x32_bf16 v[72:75], v[184:187], v[124:127], v[72:75]\n\t"
    "v_mfma_f32_16x16x32_bf16 v[76:79], v[188:191], v[124:127], v[76:79]\n\t"
    "s_waitcnt lgkmcnt(4)\n\t"
    "v_mfma_f32_16x16x32_bf16 v[64:67], v[192:195], v[96:99], v[64:67]\n\t"
    "ds_read_b128 v[120:123], %[da] offset:20480\n\t"
    "v_mfma_f32_16x16x32_bf16 v[68:71], v[196:199], v[96:99], v[68:71]\n\t"
    "ds_read_b128 v[124:127], %[da] offset:22528\n\t"
    "v_mfma_f32_16x16x32_bf16 v[72:75], v[192:195], v[100:103], v[72:75]\n\t"
    "v_mfma_f32_16x16x32_bf16 v[76:79], v[196:199], v[100:103], v[76:79]\n\t"
    "s_waitcnt lgkmcnt(4)\n\t"
    "v_mfma_f32_16x16x32_bf16 v[64:67], v[200:203], v[104:107], v[64:67]\n\t"
    "ds_read_b128 v[96:99], %[ea] offset:24576\n\t"
    "v_mfma_f32_16x16x32_bf16 v[68:71], v[204:207], v[104:107], v[68:71]\n\t"
    "ds_read_b128 v[100:103], %[ea] offset:26624\n\t"
    "v_mfma_f32_16x16x32_bf16 v[72:75], v[200:203], v[108:111], v[72:75]\n\t"
    "v_mfma_f32_16x16x32_bf16 v[76:79], v[204:207], v[108:111], v[76:79]\n\t"
    "s_add_u32 s36, s36, %[ab]\n\t"
    "s_addc_u32 s37, s37, 0\n\t"
    "s_waitcnt lgkmcnt(4)\n\t"
    "v_mfma_f32_16x16x32_bf16 v[64:67], v[208:211], v[112:115], v[64:67]\n\t"
    "ds_read_b128 v[104:107], %[da] offset:24576\n\t"
    "v_mfma_f32_16x16x32_bf16 v[68:71], v[212:215], v[112:115], v[68:71]\n\t"
    "ds_read_b128 v[108:111], %[da] offset:26624\n\t"
    "v_mfma_f32_16x16x32_bf16 v[72:75], v[208:211], v[116:119], v[72:75]\n\t"
    "v_mfma_f32_16x16x32_bf16 v[76:79], v[212:215], v[116:119], v[76:79]\n\t"
    "s_xor_b32 %[l1], %[l1], 0x10000\n\t"
    "s_waitcnt lgkmcnt(4)\n\t"
    "v_mfma_f32_16x16x32_bf16 v[64:67], v[216:219], v[120:123], v[64:67]\n\t"
    "ds_read_b128 v[112:115], %[ea] offset:28672\n\t"
    "v_mfma_f32_16x16x32_bf16 v[68:71], v[220:223], v[120:123], v[68:71]\n\t"
    "ds_read_b128 v[116:119], %[ea] offset:30720\n\t"
    "v_mfma_f32_16x16x32_bf16 v[72:75], v[216:219], v[124:127], v[72:75]\n\t"
    "v_xor_b32 %[ea], 0x10000, %[ea]\n\t"
    "v_mfma_f32_16x16x32_bf16 v[76:79], v[220:223], v[124:127], v[76:79]\n\t"
    "s_waitcnt vmcnt(6)\n\t"
    "s_barrier\n\t"
    "s_waitcnt lgkmcnt(4)\n\t"
    "v_mfma_f32_16x16x32_bf16 v[64:67], v[224:227], v[96:99], v[64:67]\n\t"
    "ds_read_b32 v39, %[sa]\n\t"
    "ds_read_b128 v[120:123], %[da] offset:28672\n\t"
    "v_mfma_f32_16x16x32_bf16 v[68:71], v[228:231], v[96:99], v[68:71]\n\t"
    "ds_read_b128 v[124:127], %[da] offset:30720\n\t"
    "v_mfma_f32_16x16x32_bf16 v[72:75], v[224:227], v[100:103], v[72:75]\n\t"
    "v_xor_b32 %[da], 0x10000, %[da]\n\t"
    "v_mfma_f32_16x16x32_bf16 v[76:79], v[228:231], v[100:103], v[76:79]\n\t"
    "s_waitcnt lgkmcnt(5)\n\t"
    "v_mfma_f32_16x16x32_bf16 v[64:67], v[232:235], v[104:107], v[64:67]\n\t"
    "ds_read_b128 v[96:99], %[ea] offset:0\n\t"
    "v_mfma_f32_16x16x32_bf16 v[68:71], v[236:239], v[104:107], v[68:71]\n\t"
    "ds_read_b128 v[100:103], %[ea] offset:2048\n\t"
    "v_mfma_f32_16x16x32_bf16 v[72:75], v[232:235], v[108:111], v[72:75]\n\t"
    "v_mfma_f32_16x16x32_bf16 v[76:79], v[236:239], v[108:111], v[76:79]\n\t"
    "s_waitcnt lgkmcnt(4)\n\t"
    "v_mfma_f32_16x16x32_bf16 v[64:67], v[240:243], v[112:115], v[64:67]\n\t"
    "ds_read_b128 v[104:107], %[da] offset:0\n\t"
    "v_mad_u32_u24 v39, v39, %[mu], %[ad]\n\t"
    "v_mfma_f32_16x16x32_bf16 v[68:71], v[244:247], v[112:115], v[68:71]\n\t"
    "ds_read_b128 v[108:111], %[da] offset:2048\n\t"
    "v_cmp_ge_i32 vcc, %[lf], v39\n\t"
    "s_cmp_eq_u64 vcc, 0\n\t"
    "s_addc_u32 %[lf], %[lf], 0\n\t"
    "v_mfma_f32_16x16x32_bf16 v[72:75], v[240:243], v[116:119], v[72:75]\n\t"
    "v_mfma_f32_16x16x32_bf16 v[76:79], v[244:247], v[116:119], v[76:79]\n\t"
    "v_cmp_ge_i32 vcc, %[lf], v39\n\t"
    "s_cmp_eq_u64 vcc, 0\n\t"
    "s_addc_u32 %[lf], %[lf], 0\n\t"
    "s_waitcnt lgkmcnt(4)\n\t"
    "v_mfma_f32_16x16x32_bf16 v[80:83], v[248:251], v[120:123], v[64:67]\n\t"
    "ds_read_b128 v[112:115], %[ea] offset:4096\n\t"
    "s_min_i32 %[lf], %[lf], %[cp]\n\t"
    "s_add_u32 %[b], %[b], 1\n\t"
    "v_mfma_f32_16x16x32_bf16 v[84:87], v[252:255], v[120:123], v[68:71]\n\t"
    "ds_read_b128 v[116:119], %[ea] offset:6144\n\t"
    "v_mfma_f32_16x16x32_bf16 v[88:91], v[248:251], v[124:127], v[72:75]\n\t"
    "v_mfma_f32_16x16x32_bf16 v[92:95], v[252:255], v[124:127], v[76:79]\n\t"
    "s_add_u32 s38, s38, %[ab]\n\t"
    "s_addc_u32 s39, s39, 0\n\t"
    "s_xor_b32 %[l0], %[l0], 0x10000\n\t"
    "s_cmp_lt_i32 %[b], %[lf]\n\t"
    "s_cbranch_scc1 1b\n\t"
    "s_waitcnt lgkmcnt(0)\n\t"
    "s_nop 4"
    : "=&{v[80:95]}"(pd), [ea] "+&v"(ea), [da] "+&v"(da), [b] "+&s"(b), [lf] "+&s"(lf), "+{s[36:37]}"(p1), "+{s[38:39]}"(p0), [sa] "+&v"(sa), [l1] "+&s"(l1), [l0] "+&s"(l0)
    : [rw] "v"(rw), [ab] "s"(ab), [cp] "s"(cp), [mu] "s"(mu), [ad] "v"(ad), "{v[128:159]}"(W[0]), "{v[160:191]}"(W[1]), "{v[192:223]}"(W[2]), "{v[224:255]}"(W[3]), [av0] "v"(av[0]), [av1] "v"(av[1]), [av2] "v"(av[2]), [av3] "v"(av[3]), [av4] "v"(av[4]), [av5] "v"(av[5]), [av6] "v"(av[6]), [av7] "v"(av[7])
    : "v39", "v64", "v65", "v66", "v67", "v68", "v69", "v70", "v71", "v72", "v73", "v74", "v75", "v76", "v77", "v78", "v79", "v96", "v97", "v98", "v99", "v100", "v101", "v102", "v103", "v104", "v105", "v106", "v107", "v108", "v109", "v110", "v111", "v112", "v113", "v114", "v115", "v116", "v117", "v118", "v119", "v120", "v121", "v122", "v123", "v124", "v125", "v126", "v127", "vcc", "scc", "m0", "memory");
}
__device__ __forceinline__ void near_st0(const u32x32 (&W)[4], u32x16& pd, unsigned ea, unsigned da, int& b, int& lf, int cp, unsigned sa, unsigned mu, unsigned ad,
    unsigned rw, unsigned ab, const char* p1, const char* p0, unsigned co0, unsigned co1, int po, int pf, unsigned w0, unsigned sl, unsigned dv, const char* db, const char* pg) {
  unsigned st;
  asm volatile("s_waitcnt vmcnt(0)\n\t"
    "s_barrier\n\t"
    "ds_read_b128 v[96:99], %[ea] offset:0\n\t"
    "ds_read_b128 v[100:103], %[ea] offset:2048\n\t"
    "ds_read_b128 v[104:107], %[da] offset:0\n\t"
    "ds_read_b128 v[108:111], %[da] offset:2048\n\t"
    "ds_read_b128 v[112:115], %[ea] offset:4096\n\t"
    "ds_read_b128 v[116:119], %[ea] offset:6144\n\t"
    "s_branch 2f\n\t"
    "1:\n\t"
    "s_waitcnt vmcnt(0)\n\t"
    "s_barrier\n\t"
    "2:\n\t"
    "s_waitcnt lgkmcnt(4)\n\t"
    "v_mfma_f32_16x16x32_bf16 v[64:67], v[128:131], v[96:99], 0\n\t"
    "ds_read_b128 v[120:123], %[da] offset:4096\n\t"
    "v_mfma_f32_16x16x32_bf16 v[68:71], v[132:135], v[96:99], 0\n\t"
    "ds_read_b128 v[124:127], %[da] offset:6144\n\t"
    "v_mfma_f32_16x16x32_bf16 v[72:75], v[128:131], v[100:103], 0\n\t"
    "v_mfma_f32_16x16x32_bf16 v[76:79], v[132:135], v[100:103], 0\n\t"
    "s_cmp_eq_u32 %[w0], 0\n\t"
    "s_cbranch_scc1 5f\n\t"
    "6:\n\t"
    "s_waitcnt lgkmcnt(4)\n\t"
    "v_mfma_f32_16x16x32_bf16 v[64:67], v[136:139], v[104:107], v[64:67]\n\t"
    "ds_read_b128 v[96:99], %[ea] offset:8192\n\t"
    "v_mfma_f32_16x16x32_bf16 v[68:71], v[140:143], v[104:107], v[68:71]\n\t"
    "ds_read_b128 v[100:103], %[ea] offset:10240\n\t"
    "v_mfma_f32_16x16x32_bf16 v[72:75], v[136:139], v[108:111], v[72:75]\n\t"
    "v_mfma_f32_16x16x32_bf16 v[76:79], v[140:143], v[108:111], v[76:79]\n\t"
    "s_waitcnt lgkmcnt(4)\n\t"
    "v_mfma_f32_16x16x32_bf16 v[64:67], v[144:147], v[112:115], v[64:67]\n\t"
    "ds_read_b128 v[104:107], %[da] offset:8192\n\t"
    "v_mfma_f32_16x16x32_bf16 v[68:71], v[148:151], v[112:115], v[68:71]\n\t"
    "ds_read_b128 v[108:111], %[da] offset:10240\n\t"
    "v_mfma_f32_16x16x32_bf16 v[72:75], v[144:147], v[116:119], v[72:75]\n\t"
    "v_mfma_f32_16x16x32_bf16 v[76:79], v[148:151], v[116:119], v[76:79]\n\t"
    "s_barrier\n\t"
    "s_waitcnt lgkmcnt(4)\n\t"
    "v_mfma_f32_16x16x32_bf16 v[64:67], v[152:155], v[120:123], v[64:67]\n\t"
    "ds_read_b128 v[112:115], %[ea] offset:12288\n\t"
    "v_mfma_f32_16x16x32_bf16 v[68:71], v[156:159], v[120:123], v[68:71]\n\t"
    "ds_read_b128 v[116:119], %[ea] offset:14336\n\t"
    "v_mfma_f32_16x16x32_bf16 v[72:75], v[152:155], v[124:127], v[72:75]\n\t"
    "ds_read_b128 v[48:51], %[rw] offset:0\n\t"
    "v_mfma_f32_16x16x32_bf16 v[76:79], v[156:159], v[124:127], v[76:79]\n\t"
    "ds_read_b128 v[52:55], %[rw] offset:1024\n\t"
    "s_waitcnt lgkmcnt(6)\n\t"
    "v_mfma_f32_16x16x32_bf16 v[64:67], v[160:163], v[96:99], v[64:67]\n\t"
    "ds_read_b128 v[120:123], %[da] offset:12288\n\t"
    "v_mfma_f32_16x16x32_bf16 v[68:71], v[164:167], v[96:99], v[68:71]\n\t"
    "ds_read_b128 v[124:127], %[da] offset:14336\n\t"
    "v_mfma_f32_16x16x32_bf16 v[72:75], v[160:163], v[100:103], v[72:75]\n\t"
    "ds_read_b128 v[56:59], %[rw] offset:2048\n\t"
    "v_mfma_f32_16x16x32_bf16 v[76:79], v[164:167], v[100:103], v[76:79]\n\t"
    "ds_read_b128 v[60:63], %[rw] offset:3072\n\t"
    "s_waitcnt lgkmcnt(0)\n\t"
    "v_mfma_f32_16x16x32_bf16 v[64:67], v[168:171], v[104:107], v[64:67]\n\t"
    "ds_read_b128 v[96:99], %[ea] offset:16384\n\t"
    "v_add_f32 v48, v48, v80\n\t"
    "v_add_f32 v49, v49, v81\n\t"
    "v_mfma_f32_16x16x32_bf16 v[68:71], v[172:175], v[104:107], v[68:71]\n\t"
    "ds_read_b128 v[100:103], %[ea] offset:18432\n\t"
    "v_add_f32 v50, v50, v82\n\t"
    "v_mfma_f32_16x16x32_bf16 v[72:75], v[168:171], v[108:111], v[72:75]\n\t"
    "v_add_f32 v51, v51, v83\n\t"
    "v_mfma_f32_16x16x32_bf16 v[76:79], v[172:175], v[108:111], v[76:79]\n\t"
    "v_add_f32 v52, v52, v84\n\t"
    "v_add_f32 v53, v53, v85\n\t"
    "v_mfma_f32_16x16x32_bf16 v[64:67], v[176:179], v[112:115], v[64:67]\n\t"
    "ds_read_b128 v[104:107], %[da] offset:16384\n\t"
    "v_add_f32 v54, v54, v86\n\t"
    "v_mfma_f32_16x16x32_bf16 v[68:71], v[180:183], v[112:115], v[68:71]\n\t"
    "ds_read_b128 v[108:111], %[da] offset:18432\n\t"
    "v_add_f32 v55, v55, v87\n\t"
    "v_mfma_f32_16x16x32_bf16 v[72:75], v[176:179], v[116:119], v[72:75]\n\t"
    "v_add_f32 v56, v56, v88\n\t"
    "v_add_f32 v57, v57, v89\n\t"
    "v_mfma_f32_16x16x32_bf16 v[76:79], v[180:183], v[116:119], v[76:79]\n\t"
    "v_add_f32 v58, v58, v90\n\t"
    "v_mfma_f32_16x16x32_bf16 v[64:67], v[184:187], v[120:123], v[64:67]\n\t"
    "ds_read_b128 v[112:115], %[ea] offset:20480\n\t"
    "v_add_f32 v59, v59, v91\n\t"
    "v_mfma_f32_16x16x32_bf16 v[68:71], v[188:191], v[120:123], v[68:71]\n\t"
    "ds_read_b128 v[116:119], %[ea] offset:22528\n\t"
    "v_add_f32 v60, v60, v92\n\t"
    "v_mfma_f32_16x16x32_bf16 v[72:75], v[184:187], v[124:127], v[72:75]\n\t"
    "v_add_f32 v61, v61, v93\n\t"
    "v_add_f32 v62, v62, v94\n\t"
    "v_mfma_f32_16x16x32_bf16 v[76:79], v[188:191], v[124:127], v[76:79]\n\t"
    "v_add_f32 v63, v63, v95\n\t"
    "s_waitcnt lgkmcnt(4)\n\t"
    "v_mfma_f32_16x16x32_bf16 v[64:67], v[192:195], v[96:99], v[64:67]\n\t"
    "ds_read_b128 v[120:123], %[da] offset:20480\n\t"
    "v_cvt_pk_bf16_f32 v40, v48, v49\n\t"
    "v_mfma_f32_16x16x32_bf16 v[68:71], v[196:199], v[96:99], v[68:71]\n\t"
    "ds_read_b128 v[124:127], %[da] offset:22528\n\t"
    "v_cvt_pk_bf16_f32 v41, v50, v51\n\t"
    "v_cvt_pk_bf16_f32 v42, v52, v53\n\t"
    "v_mfma_f32_16x16x32_bf16 v[72:75], v[192:195], v[100:103], v[72:75]\n\t"
    "v_cvt_pk_bf16_f32 v43, v54, v55\n\t"
    "v_mfma_f32_16x16x32_bf16 v[76:79], v[196:199], v[100:103], v[76:79]\n\t"
    "global_store_dwordx4 %[co0], v[40:43], s[36:37] sc1\n\t"
    "s_waitcnt lgkmcnt(4)\n\t"
    "v_mfma_f32_16x16x32_bf16 v[64:67], v[200:203], v[104:107], v[64:67]\n\t"
    "ds_read_b128 v[96:99], %[ea] offset:24576\n\t"
    "v_cvt_pk_bf16_f32 v44, v56, v57\n\t"
    "v_cvt_pk_bf16_f32 v45, v58, v59\n\t"
    "v_mfma_f32_16x16x32_bf16 v[68:71], v[204:207], v[104:107], v[68:71]\n\t"
    "ds_read_b128 v[100:103], %[ea] offset:26624\n\t"
    "v_cvt_pk_bf16_f32 v46, v60, v61\n\t"
    "v_mfma_f32_16x16x32_bf16 v[72:75], v[200:203], v[108:111], v[72:75]\n\t"
    "v_cvt_pk_bf16_f32 v47, v62, v63\n\t"
    "v_mfma_f32_16x16x32_bf16 v[76:79], v[204:207], v[108:111], v[76:79]\n\t"
    "global_store_dwordx4 %[co1], v[44:47], s[36:37] sc1\n\t"
    "s_waitcnt lgkmcnt(4)\n\t"
    "v_mfma_f32_16x16x32_bf16 v[64:67], v[208:211], v[112:115], v[64:67]\n\t"
    "ds_read_b128 v[104:107], %[da] offset:24576\n\t"
    "v_mfma_f32_16x16x32_bf16 v[68:71], v[212:215], v[112:115], v[68:71]\n\t"
    "ds_read_b128 v[108:111], %[da] offset:26624\n\t"
    "v_mfma_f32_16x16x32_bf16 v[72:75], v[208:211], v[116:119], v[72:75]\n\t"
    "v_mfma_f32_16x16x32_bf16 v[76:79], v[212:215], v[116:119], v[76:79]\n\t"
    "s_waitcnt lgkmcnt(4)\n\t"
    "v_mfma_f32_16x16x32_bf16 v[64:67], v[216:219], v[120:123], v[64:67]\n\t"
    "ds_read_b128 v[112:115], %[ea] offset:28672\n\t"
    "v_mfma_f32_16x16x32_bf16 v[68:71], v[220:223], v[120:123], v[68:71]\n\t"
    "ds_read_b128 v[116:119], %[ea] offset:30720\n\t"
    "v_mfma_f32_16x16x32_bf16 v[72:75], v[216:219], v[124:127], v[72:75]\n\t"
    "v_xor_b32 %[ea], 0x10000, %[ea]\n\t"
    "v_mfma_f32_16x16x32_bf16 v[76:79], v[220:223], v[124:127], v[76:79]\n\t"
    "s_waitcnt vmcnt(2)\n\t"
    "s_barrier\n\t"
    "s_waitcnt lgkmcnt(4)\n\t"
    "v_mfma_f32_16x16x32_bf16 v[64:67], v[224:227], v[96:99], v[64:67]\n\t"
    "ds_read_b32 v39, %[sa]\n\t"
    "ds_read_b128 v[120:123], %[da] offset:28672\n\t"
    "v_mfma_f32_16x16x32_bf16 v[68:71], v[228:231], v[96:99], v[68:71]\n\t"
    "ds_read_b128 v[124:127], %[da] offset:30720\n\t"
    "v_mfma_f32_16x16x32_bf16 v[72:75], v[224:227], v[100:103], v[72:75]\n\t"
    "v_xor_b32 %[da], 0x10000, %[da]\n\t"
    "v_mfma_f32_16x16x32_bf16 v[76:79], v[228:231], v[100:103], v[76:79]\n\t"
    "s_waitcnt lgkmcnt(5)\n\t"
    "v_mfma_f32_16x16x32_bf16 v[64:67], v[232:235], v[104:107], v[64:67]\n\t"
    "ds_read_b128 v[96:99], %[ea] offset:0\n\t"
    "v_mfma_f32_16x16x32_bf16 v[68:71], v[236:239], v[104:107], v[68:71]\n\t"
    "ds_read_b128 v[100:103], %[ea] offset:2048\n\t"
    "v_mfma_f32_16x16x32_bf16 v[72:75], v[232:235], v[108:111], v[72:75]\n\t"
    "v_mfma_f32_16x16x32_bf16 v[76:79], v[236:239], v[108:111], v[76:79]\n\t"
    "s_waitcnt lgkmcnt(4)\n\t"
    "v_mfma_f32_16x16x32_bf16 v[64:67], v[240:243], v[112:115], v[64:67]\n\t"
    "ds_read_b128 v[104:107], %[da] offset:0\n\t"
    "v_mad_u32_u24 v39, v39, %[mu], %[ad]\n\t"
    "v_mfma_f32_16x16x32_bf16 v[68:71], v[244:247], v[112:115], v[68:71]\n\t"
    "ds_read_b128 v[108:111], %[da] offset:2048\n\t"
    "v_cmp_ge_i32 vcc, %[lf], v39\n\t"
    "s_cmp_eq_u64 vcc, 0\n\t"
    "s_addc_u32 %[lf], %[lf], 0\n\t"
    "v_mfma_f32_16x16x32_bf16 v[72:75], v[240:243], v[116:119], v[72:75]\n\t"
    "v_mfma_f32_16x16x32_bf16 v[76:79], v[244:247], v[116:119], v[76:79]\n\t"
    "v_cmp_ge_i32 vcc, %[lf], v39\n\t"
    "s_cmp_eq_u64 vcc, 0\n\t"
    "s_addc_u32 %[lf], %[lf], 0\n\t"
    "s_waitcnt lgkmcnt(4)\n\t"
    "v_mfma_f32_16x16x32_bf16 v[80:83], v[248:251], v[120:123], v[64:67]\n\t"
    "ds_read_b128 v[112:115], %[ea] offset:4096\n\t"
    "s_min_i32 %[lf], %[lf], %[cp]\n\t"
    "s_add_u32 %[b], %[b], 1\n\t"
    "v_mfma_f32_16x16x32_bf16 v[84:87], v[252:255], v[120:123], v[68:71]\n\t"
    "ds_read_b128 v[116:119], %[ea] offset:6144\n\t"
    "v_mfma_f32_16x16x32_bf16 v[88:91], v[248:251], v[124:127], v[72:75]\n\t"
    "v_mfma_f32_16x16x32_bf16 v[92:95], v[252:255], v[124:127], v[76:79]\n\t"
    "s_mov_b64 s[36:37], s[38:39]\n\t"
    "s_add_u32 s38, s38, %[ab]\n\t"
    "s_addc_u32 s39, s39, 0\n\t"
    "s_cmp_lt_i32 %[b], %[lf]\n\t"
    "s_cbranch_scc1 1b\n\t"
    "s_waitcnt lgkmcnt(0)\n\t"
    "s_nop 4\n\t"
    "s_branch 9f\n\t"
    "5:\n\t"
    "s_add_u32 %[st], %[b], %[po]\n\t"
    "s_max_i32 %[st], %[st], %[p0]\n\t"
    "v_mov_b32 v39, %[st]\n\t"
    "v_mov_b32 v38, 0\n\t"
    "s_mov_b64 exec, 1\n\t"
    "global_store_dword v38, v39, %[pg] sc1\n\t"
    "s_mov_b64 exec, -1\n\t"
    "s_mov_b32 %[p0], %[p1]\n\t"
    "s_cmp_ge_i32 %[lf], %[cp]\n\t"
    "s_cbranch_scc1 6b\n\t"
    "s_mov_b32 m0, %[sl]\n\t"
    "s_nop 0\n\t"
    "global_load_lds_dword %[dv], %[db] sc1\n\t"
    "s_branch 6b\n\t"
    "9:"
    : "=&{v[80:95]}"(pd), [ea] "+&v"(ea), [da] "+&v"(da), [b] "+&s"(b), [lf] "+&s"(lf), "+{s[36:37]}"(p1), "+{s[38:39]}"(p0), [sa] "+&v"(sa), [st] "=&s"(st), [sl] "+&s"(sl), [p0] "+&s"(pf)
    : [rw] "v"(rw), [ab] "s"(ab), [cp] "s"(cp), [mu] "s"(mu), [ad] "v"(ad), "{v[128:159]}"(W[0]), "{v[160:191]}"(W[1]), "{v[192:223]}"(W[2]), "{v[224:255]}"(W[3]), [co0] "v"(co0), [w0] "s"(w0), [dv] "v"(dv), [db] "s"(db), [pg] "s"(pg), [po] "s"(po - 1), [p1] "s"(b + po), [co1] "v"(co1)
    : "v38", "v39", "v40", "v41", "v42", "v43", "v44", "v45", "v46", "v47", "v48", "v49", "v50", "v51", "v52", "v53", "v54", "v55", "v56", "v57", "v58", "v59", "v60", "v61", "v62", "v63", "v64", "v65", "v66", "v67", "v68", "v69", "v70", "v71", "v72", "v73", "v74", "v75", "v76", "v77", "v78", "v79", "v96", "v97", "v98", "v99", "v100", "v101", "v102", "v103", "v104", "v105", "v106", "v107", "v108", "v109", "v110", "v111", "v112", "v113", "v114", "v115", "v116", "v117", "v118", "v119", "v120", "v121", "v122", "v123", "v124", "v125", "v126", "v127", "vcc", "scc", "m0", "memory");
}
__device__ __forceinline__ void near_st1(const u32x32 (&W)[4], u32x16& pd, unsigned ea, unsigned da, int& b, int& lf, int cp, unsigned sa, unsigned mu, unsigned ad,
    unsigned rw, unsigned ab, const char* p1, const char* p0, unsigned co0, unsigned co1, int po, int pf, unsigned w0, unsigned sl, unsigned dv, const char* db, const char* pg) {
  unsigned st;
  asm volatile("s_waitcnt vmcnt(0)\n\t"
    "s_barrier\n\t"
    "ds_read_b128 v[96:99], %[ea] offset:0\n\t"
    "ds_read_b128 v[100:103], %[ea] offset:2048\n\t"
    "ds_read_b128 v[104:107], %[da] offset:0\n\t"
    "ds_read_b128 v[108:111], %[da] offset:2048\n\t"
    "ds_read_b128 v[112:115], %[ea] offset:4096\n\t"
    "ds_read_b128 v[116:119], %[ea] offset:6144\n\t"
    "s_branch 2f\n\t"
    "1:\n\t"
    "s_waitcnt vmcnt(0)\n\t"
    "s_barrier\n\t"
    "2:\n\t"
    "s_waitcnt lgkmcnt(4)\n\t"
    "v_mfma_f32_16x16x32_bf16 v[64:67], v[128:131], v[96:99], 0\n\t"
    "ds_read_b128 v[120:123], %[da] offset:4096\n\t"
    "v_mfma_f32_16x16x32_bf16 v[68:71], v[132:135], v[96:99], 0\n\t"
    "ds_read_b128 v[124:127], %[da] offset:6144\n\t"
    "v_mfma_f32_16x16x32_bf16 v[72:75], v[128:131], v[100:103], 0\n\t"
    "v_mfma_f32_16x16x32_bf16 v[76:79], v[132:135], v[100:103], 0\n\t"
    "s_cmp_eq_u32 %[w0], 0\n\t"
    "s_cbranch_scc1 5f\n\t"
    "6:\n\t"
    "s_waitcnt lgkmcnt(4)\n\t"
    "v_mfma_f32_16x16x32_bf16 v[64:67], v[136:139], v[104:107], v[64:67]\n\t"
    "ds_read_b128 v[96:99], %[ea] offset:8192\n\t"
    "v_mfma_f32_16x16x32_bf16 v[68:71], v[140:143], v[104:107], v[68:71]\n\t"
    "ds_read_b128 v[100:103], %[ea] offset:10240\n\t"
    "v_mfma_f32_16x16x32_bf16 v[72:75], v[136:139], v[108:111], v[72:75]\n\t"
    "v_mfma_f32_16x16x32_bf16 v[76:79], v[140:143], v[108:111], v[76:79]\n\t"
    "s_waitcnt lgkmcnt(4)\n\t"
    "v_mfma_f32_16x16x32_bf16 v[64:67], v[144:147], v[112:115], v[64:67]\n\t"
    "ds_read_b128 v[104:107], %[da] offset:8192\n\t"
    "v_mfma_f32_16x16x32_bf16 v[68:71], v[148:151], v[112:115], v[68:71]\n\t"
    "ds_read_b128 v[108:111], %[da] offset:10240\n\t"
    "v_mfma_f32_16x16x32_bf16 v[72:75], v[144:147], v[116:119], v[72:75]\n\t"
    "v_mfma_f32_16x16x32_bf16 v[76:79], v[148:151], v[116:119], v[76:79]\n\t"
    "s_barrier\n\t"
    "s_waitcnt lgkmcnt(4)\n\t"
    "v_mfma_f32_16x16x32_bf16 v[64:67], v[152:155], v[120:123], v[64:67]\n\t"
    "ds_read_b128 v[112:115], %[ea] offset:12288\n\t"
    "v_mfma_f32_16x16x32_bf16 v[68:71], v[156:159], v[120:123], v[68:71]\n\t"
    "ds_read_b128 v[116:119], %[ea] offset:14336\n\t"
    "v_mfma_f32_16x16x32_bf16 v[72:75], v[152:155], v[124:127], v[72:75]\n\t"
    "ds_read_b128 v[48:51], %[rw] offset:0\n\t"
    "v_mfma_f32_16x16x32_bf16 v[76:79], v[156:159], v[124:127], v[76:79]\n\t"
    "ds_read_b128 v[52:55], %[rw] offset:1024\n\t"
    "s_waitcnt lgkmcnt(6)\n\t"
    "v_mfma_f32_16x16x32_bf16 v[64:67], v[160:163], v[96:99], v[64:67]\n\t"
    "ds_read_b128 v[120:123], %[da] offset:12288\n\t"
    "v_mfma_f32_16x16x32_bf16 v[68:71], v[164:167], v[96:99], v[68:71]\n\t"
    "ds_read_b128 v[124:127], %[da] offset:14336\n\t"
    "v_mfma_f32_16x16x32_bf16 v[72:75], v[160:163], v[100:103], v[72:75]\n\t"
    "ds_read_b128 v[56:59], %[rw] offset:2048\n\t"
    "v_mfma_f32_16x16x32_bf16 v[76:79], v[164:167], v[100:103], v[76:79]\n\t"
    "ds_read_b128 v[60:63], %[rw] offset:3072\n\t"
    "s_waitcnt lgkmcnt(0)\n\t"
    "v_mfma_f32_16x16x32_bf16 v[64:67], v[168:171], v[104:107], v[64:67]\n\t"
    "ds_read_b128 v[96:99], %[ea] offset:16384\n\t"
    "v_add_f32 v48, v48, v80\n\t"
    "v_add_f32 v49, v49, v81\n\t"
    "v_add_f32 v50, v50, v82\n\t"
    "v_mfma_f32_16x16x32_bf16 v[68:71], v[172:175], v[104:107], v[68:71]\n\t"
    "ds_read_b128 v[100:103], %[ea] offset:18432\n\t"
    "v_add_f32 v51, v51, v83\n\t"
    "v_add_f32 v52, v52, v84\n\t"
    "v_add_f32 v53, v53, v85\n\t"
    "v_mfma_f32_16x16x32_bf16 v[72:75], v[168:171], v[108:111], v[72:75]\n\t"
    "v_add_f32 v54, v54, v86\n\t"
    "v_add_f32 v55, v55, v87\n\t"
    "v_add_f32 v56, v56, v88\n\t"
    "v_mfma_f32_16x16x32_bf16 v[76:79], v[172:175], v[108:111], v[76:79]\n\t"
    "v_add_f32 v57, v57, v89\n\t"
    "v_add_f32 v58, v58, v90\n\t"
    "v_add_f32 v59, v59, v91\n\t"
    "v_mfma_f32_16x16x32_bf16 v[64:67], v[176:179], v[112:115], v[64:67]\n\t"
    "ds_read_b128 v[104:107], %[da] offset:16384\n\t"
    "v_add_f32 v60, v60, v92\n\t"
    "v_add_f32 v61, v61, v93\n\t"
    "v_add_f32 v62, v62, v94\n\t"
    "v_mfma_f32_16x16x32_bf16 v[68:71], v[180:183], v[112:115], v[68:71]\n\t"
    "ds_read_b128 v[108:111], %[da] offset:18432\n\t"
    "v_add_f32 v63, v63, v95\n\t"
    "v_mul_f32 v40, %[nl], v48\n\t"
    "v_mul_f32 v41, %[nl], v49\n\t"
    "v_mfma_f32_16x16x32_bf16 v[72:75], v[176:179], v[116:119], v[72:75]\n\t"
    "v_mul_f32 v42, %[nl], v50\n\t"
    "v_mul_f32 v43, %[nl], v51\n\t"
    "v_mul_f32 v44, %[nl], v56\n\t"
    "v_mfma_f32_16x16x32_bf16 v[76:79], v[180:183], v[116:119], v[76:79]\n\t"
    "v_mul_f32 v45, %[nl], v57\n\t"
    "v_mul_f32 v46, %[nl], v58\n\t"
    "v_mul_f32 v47, %[nl], v59\n\t"
    "v_mfma_f32_16x16x32_bf16 v[64:67], v[184:187], v[120:123], v[64:67]\n\t"
    "ds_read_b128 v[112:115], %[ea] offset:20480\n\t"
    "v_exp_f32 v40, v40\n\t"
    "v_exp_f32 v41, v41\n\t"
    "v_exp_f32 v42, v42\n\t"
    "v_mfma_f32_16x16x32_bf16 v[68:71], v[188:191], v[120:123], v[68:71]\n\t"
    "ds_read_b128 v[116:119], %[ea] offset:22528\n\t"
    "v_exp_f32 v43, v43\n\t"
    "v_exp_f32 v44, v44\n\t"
    "v_exp_f32 v45, v45\n\t"
    "v_mfma_f32_16x16x32_bf16 v[72:75], v[184:187], v[124:127], v[72:75]\n\t"
    "v_exp_f32 v46, v46\n\t"
    "v_exp_f32 v47, v47\n\t"
    "v_add_f32 v40, 1.0, v40\n\t"
    "v_mfma_f32_16x16x32_bf16 v[76:79], v[188:191], v[124:127], v[76:79]\n\t"
    "v_add_f32 v41, 1.0, v41\n\t"
    "v_add_f32 v42, 1.0, v42\n\t"
    "v_add_f32 v43, 1.0, v43\n\t"
    "s_waitcnt lgkmcnt(4)\n\t"
    "v_mfma_f32_16x16x32_bf16 v[64:67], v[192:195], v[96:99], v[64:67]\n\t"
    "ds_read_b128 v[120:123], %[da] offset:20480\n\t"
    "v_add_f32 v44, 1.0, v44\n\t"
    "v_add_f32 v45, 1.0, v45\n\t"
    "v_add_f32 v46, 1.0, v46\n\t"
    "v_mfma_f32_16x16x32_bf16 v[68:71], v[196:199], v[96:99], v[68:71]\n\t"
    "ds_read_b128 v[124:127], %[da] offset:22528\n\t"
    "v_add_f32 v47, 1.0, v47\n\t"
    "v_rcp_f32 v40, v40\n\t"
    "v_rcp_f32 v41, v41\n\t"
    "v_mfma_f32_16x16x32_bf16 v[72:75], v[192:195], v[100:103], v[72:75]\n\t"
    "v_rcp_f32 v42, v42\n\t"
    "v_rcp_f32 v43, v43\n\t"
    "v_rcp_f32 v44, v44\n\t"
    "v_mfma_f32_16x16x32_bf16 v[76:79], v[196:199], v[100:103], v[76:79]\n\t"
    "v_rcp_f32 v45, v45\n\t"
    "v_rcp_f32 v46, v46\n\t"
    "v_rcp_f32 v47, v47\n\t"
    "s_waitcnt lgkmcnt(4)\n\t"
    "v_mfma_f32_16x16x32_bf16 v[64:67], v[200:203], v[104:107], v[64:67]\n\t"
    "ds_read_b128 v[96:99], %[ea] offset:24576\n\t"
    "v_mul_f32 v40, v48, v40\n\t"
    "v_mul_f32 v41, v49, v41\n\t"
    "v_mul_f32 v42, v50, v42\n\t"
    "v_mfma_f32_16x16x32_bf16 v[68:71], v[204:207], v[104:107], v[68:71]\n\t"
    "ds_read_b128 v[100:103], %[ea] offset:26624\n\t"
    "v_mul_f32 v43, v51, v43\n\t"
    "v_mul_f32 v44, v56, v44\n\t"
    "v_mul_f32 v45, v57, v45\n\t"
    "v_mfma_f32_16x16x32_bf16 v[72:75], v[200:203], v[108:111], v[72:75]\n\t"
    "v_mul_f32 v46, v58, v46\n\t"
    "v_mul_f32 v47, v59, v47\n\t"
    "v_mul_f32 v40, v40, v52\n\t"
    "v_mfma_f32_16x16x32_bf16 v[76:79], v[204:207], v[108:111], v[76:79]\n\t"
    "v_mul_f32 v41, v41, v53\n\t"
    "v_mul_f32 v42, v42, v54\n\t"
    "v_mul_f32 v43, v43, v55\n\t"
    "s_waitcnt lgkmcnt(4)\n\t"
    "v_mfma_f32_16x16x32_bf16 v[64:67], v[208:211], v[112:115], v[64:67]\n\t"
    "ds_read_b128 v[104:107], %[da] offset:24576\n\t"
    "v_mul_f32 v44, v44, v60\n\t"
    "v_mul_f32 v45, v45, v61\n\t"
    "v_mul_f32 v46, v46, v62\n\t"
    "v_mfma_f32_16x16x32_bf16 v[68:71], v[212:215], v[112:115], v[68:71]\n\t"
    "ds_read_b128 v[108:111], %[da] offset:26624\n\t"
    "v_mul_f32 v47, v47, v63\n\t"
    "v_cvt_pk_bf16_f32 v48, v40, v41\n\t"
    "v_cvt_pk_bf16_f32 v49, v42, v43\n\t"
    "v_mfma_f32_16x16x32_bf16 v[72:75], v[208:211], v[116:119], v[72:75]\n\t"
    "v_cvt_pk_bf16_f32 v50, v44, v45\n\t"
    "v_cvt_pk_bf16_f32 v51, v46, v47\n\t"
    "s_nop 1\n\t"
    "v_mfma_f32_16x16x32_bf16 v[76:79], v[212:215], v[116:119], v[76:79]\n\t"
    "v_permlane16_swap_b32 v48, v50\n\t"
    "v_permlane16_swap_b32 v49, v51\n\t"
    "global_store_dwordx4 %[co0], v[48:51], s[36:37] sc1\n\t"
    "s_waitcnt lgkmcnt(4)\n\t"
    "v_mfma_f32_16x16x32_bf16 v[64:67], v[216:219], v[120:123], v[64:67]\n\t"
    "ds_read_b128 v[112:115], %[ea] offset:28672\n\t"
    "v_mfma_f32_16x16x32_bf16 v[68:71], v[220:223], v[120:123], v[68:71]\n\t"
    "ds_read_b128 v[116:119], %[ea] offset:30720\n\t"
    "v_mfma_f32_16x16x32_bf16 v[72:75], v[216:219], v[124:127], v[72:75]\n\t"
    "v_xor_b32 %[ea], 0x10000, %[ea]\n\t"
    "v_mfma_f32_16x16x32_bf16 v[76:79], v[220:223], v[124:127], v[76:79]\n\t"
    "s_waitcnt vmcnt(1)\n\t"
    "s_barrier\n\t"
    "s_waitcnt lgkmcnt(4)\n\t"
    "v_mfma_f32_16x16x32_bf16 v[64:67], v[224:227], v[96:99], v[64:67]\n\t"
    "ds_read_b32 v39, %[sa]\n\t"
    "ds_read_b128 v[120:123], %[da] offset:28672\n\t"
    "v_mfma_f32_16x16x32_bf16 v[68:71], v[228:231], v[96:99], v[68:71]\n\t"
    "ds_read_b128 v[124:127], %[da] offset:30720\n\t"
    "v_mfma_f32_16x16x32_bf16 v[72:75], v[224:227], v[100:103], v[72:75]\n\t"
    "v_xor_b32 %[da], 0x10000, %[da]\n\t"
    "v_mfma_f32_16x16x32_bf16 v[76:79], v[228:231], v[100:103], v[76:79]\n\t"
    "s_waitcnt lgkmcnt(5)\n\t"
    "v_mfma_f32_16x16x32_bf16 v[64:67], v[232:235], v[104:107], v[64:67]\n\t"
    "ds_read_b128 v[96:99], %[ea] offset:0\n\t"
    "v_mfma_f32_16x16x32_bf16 v[68:71], v[236:239], v[104:107], v[68:71]\n\t"
    "ds_read_b128 v[100:103], %[ea] offset:2048\n\t"
    "v_mfma_f32_16x16x32_bf16 v[72:75], v[232:235], v[108:111], v[72:75]\n\t"
    "v_mfma_f32_16x16x32_bf16 v[76:79], v[236:239], v[108:111], v[76:79]\n\t"
    "s_waitcnt lgkmcnt(4)\n\t"
    "v_mfma_f32_16x16x32_bf16 v[64:67], v[240:243], v[112:115], v[64:67]\n\t"
    "ds_read_b128 v[104:107], %[da] offset:0\n\t"
    "v_mad_u32_u24 v39, v39, %[mu], %[ad]\n\t"
    "v_mfma_f32_16x16x32_bf16 v[68:71], v[244:247], v[112:115], v[68:71]\n\t"
    "ds_read_b128 v[108:111], %[da] offset:2048\n\t"
    "v_cmp_ge_i32 vcc, %[lf], v39\n\t"
    "s_cmp_eq_u64 vcc, 0\n\t"
    "s_addc_u32 %[lf], %[lf], 0\n\t"
    "v_mfma_f32_16x16x32_bf16 v[72:75], v[240:243], v[116:119], v[72:75]\n\t"
    "v_mfma_f32_16x16x32_bf16 v[76:79], v[244:247], v[116:119], v[76:79]\n\t"
    "v_cmp_ge_i32 vcc, %[lf], v39\n\t"
    "s_cmp_eq_u64 vcc, 0\n\t"
    "s_addc_u32 %[lf], %[lf], 0\n\t"
    "s_waitcnt lgkmcnt(4)\n\t"
    "v_mfma_f32_16x16x32_bf16 v[80:83], v[248:251], v[120:123], v[64:67]\n\t"
    "ds_read_b128 v[112:115], %[ea] offset:4096\n\t"
    "s_min_i32 %[lf], %[lf], %[cp]\n\t"
    "s_add_u32 %[b], %[b], 1\n\t"
    "v_mfma_f32_16x16x32_bf16 v[84:87], v[252:255], v[120:123], v[68:71]\n\t"
    "ds_read_b128 v[116:119], %[ea] offset:6144\n\t"
    "v_mfma_f32_16x16x32_bf16 v[88:91], v[248:251], v[124:127], v[72:75]\n\t"
    "v_mfma_f32_16x16x32_bf16 v[92:95], v[252:255], v[124:127], v[76:79]\n\t"
    "s_mov_b64 s[36:37], s[38:39]\n\t"
    "s_add_u32 s38, s38, %[ab]\n\t"
    "s_addc_u32 s39, s39, 0\n\t"
    "s_cmp_lt_i32 %[b], %[lf]\n\t"
    "s_cbranch_scc1 1b\n\t"
    "s_waitcnt lgkmcnt(0)\n\t"
    "s_nop 4\n\t"
    "s_branch 9f\n\t"
    "5:\n\t"
    "s_add_u32 %[st], %[b], %[po]\n\t"
    "s_max_i32 %[st], %[st], %[p0]\n\t"
    "v_mov_b32 v39, %[st]\n\t"
    "v_mov_b32 v38, 0\n\t"
    "s_mov_b64 exec, 1\n\t"
    "global_store_dword v38, v39, %[pg] sc1\n\t"
    "s_mov_b64 exec, -1\n\t"
    "s_mov_b32 %[p0], %[p1]\n\t"
    "s_cmp_ge_i32 %[lf], %[cp]\n\t"
    "s_cbranch_scc1 6b\n\t"
    "s_mov_b32 m0, %[sl]\n\t"
    "s_nop 0\n\t"
    "global_load_lds_dword %[dv], %[db] sc1\n\t"
    "s_branch 6b\n\t"
    "9:"
    : "=&{v[80:95]}"(pd), [ea] "+&v"(ea), [da] "+&v"(da), [b] "+&s"(b), [lf] "+&s"(lf), "+{s[36:37]}"(p1), "+{s[38:39]}"(p0), [sa] "+&v"(sa), [st] "=&s"(st), [sl] "+&s"(sl), [p0] "+&s"(pf)
    : [rw] "v"(rw), [ab] "s"(ab), [cp] "s"(cp), [mu] "s"(mu), [ad] "v"(ad), "{v[128:159]}"(W[0]), "{v[160:191]}"(W[1]), "{v[192:223]}"(W[2]), "{v[224:255]}"(W[3]), [co0] "v"(co0), [w0] "s"(w0), [dv] "v"(dv), [db] "s"(db), [pg] "s"(pg), [po] "s"(po - 1), [p1] "s"(b + po), [nl] "s"(-LOG2E)
    : "v38", "v39", "v40", "v41", "v42", "v43", "v44", "v45", "v46", "v47", "v48", "v49", "v50", "v51", "v52", "v53", "v54", "v55", "v56", "v57", "v58", "v59", "v60", "v61", "v62", "v63", "v64", "v65", "v66", "v67", "v68", "v69", "v70", "v71", "v72", "v73", "v74", "v75", "v76", "v77", "v78", "v79", "v96", "v97", "v98", "v99", "v100", "v101", "v102", "v103", "v104", "v105", "v106", "v107", "v108", "v109", "v110", "v111", "v112", "v113", "v114", "v115", "v116", "v117", "v118", "v119", "v120", "v121", "v122", "v123", "v124", "v125", "v126", "v127", "vcc", "scc", "m0", "memory");
}
__device__ __forceinline__ void near_st2(const u32x32 (&W)[4], u32x16& pd, unsigned ea, unsigned da, int& b, int& lf, int cp, unsigned sa, unsigned mu, unsigned ad,
    unsigned rw, unsigned ab, const char* p1, const char* p0, unsigned co0, unsigned co1, int po, int pf, unsigned w0, unsigned sl, unsigned dv, const char* db, const char* pg) {
  unsigned st;
  asm volatile("s_waitcnt vmcnt(0)\n\t"
    "s_barrier\n\t"
    "ds_read_b128 v[96:99], %[ea] offset:0\n\t"
    "ds_read_b128 v[100:103], %[ea] offset:2048\n\t"
    "ds_read_b128 v[104:107], %[da] offset:0\n\t"
    "ds_read_b128 v[108:111], %[da] offset:2048\n\t"
    "ds_read_b128 v[112:115], %[ea] offset:4096\n\t"
    "ds_read_b128 v[116:119], %[ea] offset:6144\n\t"
    "s_branch 2f\n\t"
    "1:\n\t"
    "s_waitcnt vmcnt(0)\n\t"
    "s_barrier\n\t"
    "2:\n\t"
    "s_waitcnt lgkmcnt(4)\n\t"
    "v_mfma_f32_16x16x32_bf16 v[64:67], v[128:131], v[96:99], 0\n\t"
    "ds_read_b128 v[120:123], %[da] offset:4096\n\t"
    "v_mfma_f32_16x16x32_bf16 v[68:71], v[132:135], v[96:99], 0\n\t"
    "ds_read_b128 v[124:127], %[da] offset:6144\n\t"
    "v_mfma_f32_16x16x32_bf16 v[72:75], v[128:131], v[100:103], 0\n\t"
    "v_mfma_f32_16x16x32_bf16 v[76:79], v[132:135], v[100:103], 0\n\t"
    "s_cmp_eq_u32 %[w0], 0\n\t"
    "s_cbranch_scc1 5f\n\t"
    "6:\n\t"
    "s_waitcnt lgkmcnt(4)\n\t"
    "v_mfma_f32_16x16x32_bf16 v[64:67], v[136:139], v[104:107], v[64:67]\n\t"
    "ds_read_b128 v[96:99], %[ea] offset:8192\n\t"
    "v_mfma_f32_16x16x32_bf16 v[68:71], v[140:143], v[104:107], v[68:71]\n\t"
    "ds_read_b128 v[100:103], %[ea] offset:10240\n\t"
    "v_mfma_f32_16x16x32_bf16 v[72:75], v[136:139], v[108:111], v[72:75]\n\t"
    "v_mfma_f32_16x16x32_bf16 v[76:79], v[140:143], v[108:111], v[76:79]\n\t"
    "s_waitcnt lgkmcnt(4)\n\t"
    "v_mfma_f32_16x16x32_bf16 v[64:67], v[144:147], v[112:115], v[64:67]\n\t"
    "ds_read_b128 v[104:107], %[da] offset:8192\n\t"
    "v_mfma_f32_16x16x32_bf16 v[68:71], v[148:151], v[112:115], v[68:71]\n\t"
    "ds_read_b128 v[108:111], %[da] offset:10240\n\t"
    "v_mfma_f32_16x16x32_bf16 v[72:75], v[144:147], v[116:119], v[72:75]\n\t"
    "v_mfma_f32_16x16x32_bf16 v[76:79], v[148:151], v[116:119], v[76:79]\n\t"
    "s_barrier\n\t"
    "s_waitcnt lgkmcnt(4)\n\t"
    "v_mfma_f32_16x16x32_bf16 v[64:67], v[152:155], v[120:123], v[64:67]\n\t"
    "ds_read_b128 v[112:115], %[ea] offset:12288\n\t"
    "v_mfma_f32_16x16x32_bf16 v[68:71], v[156:159], v[120:123], v[68:71]\n\t"
    "ds_read_b128 v[116:119], %[ea] offset:14336\n\t"
    "v_mfma_f32_16x16x32_bf16 v[72:75], v[152:155], v[124:127], v[72:75]\n\t"
    "ds_read_b128 v[48:51], %[rw] offset:0\n\t"
    "v_mfma_f32_16x16x32_bf16 v[76:79], v[156:159], v[124:127], v[76:79]\n\t"
    "ds_read_b128 v[52:55], %[rw] offset:1024\n\t"
    "s_waitcnt lgkmcnt(6)\n\t"
    "v_mfma_f32_16x16x32_bf16 v[64:67], v[160:163], v[96:99], v[64:67]\n\t"
    "ds_read_b128 v[120:123], %[da] offset:12288\n\t"
    "v_mfma_f32_16x16x32_bf16 v[68:71], v[164:167], v[96:99], v[68:71]\n\t"
    "ds_read_b128 v[124:127], %[da] offset:14336\n\t"
    "v_mfma_f32_16x16x32_bf16 v[72:75], v[160:163], v[100:103], v[72:75]\n\t"
    "ds_read_b128 v[56:59], %[rw] offset:2048\n\t"
    "v_mfma_f32_16x16x32_bf16 v[76:79], v[164:167], v[100:103], v[76:79]\n\t"
    "ds_read_b128 v[60:63], %[rw] offset:3072\n\t"
    "s_waitcnt lgkmcnt(0)\n\t"
    "v_mfma_f32_16x16x32_bf16 v[64:67], v[168:171], v[104:107], v[64:67]\n\t"
    "ds_read_b128 v[96:99], %[ea] offset:16384\n\t"
    "v_cvt_pk_bf16_f32 v40, v80, v81\n\t"
    "v_mfma_f32_16x16x32_bf16 v[68:71], v[172:175], v[104:107], v[68:71]\n\t"
    "ds_read_b128 v[100:103], %[ea] offset:18432\n\t"
    "v_cvt_pk_bf16_f32 v41, v82, v83\n\t"
    "v_mfma_f32_16x16x32_bf16 v[72:75], v[168:171], v[108:111], v[72:75]\n\t"
    "v_cvt_pk_bf16_f32 v42, v84, v85\n\t"
    "v_mfma_f32_16x16x32_bf16 v[76:79], v[172:175], v[108:111], v[76:79]\n\t"
    "v_cvt_pk_bf16_f32 v43, v86, v87\n\t"
    "v_mfma_f32_16x16x32_bf16 v[64:67], v[176:179], v[112:115], v[64:67]\n\t"
    "ds_read_b128 v[104:107], %[da] offset:16384\n\t"
    "global_store_dwordx4 %[co0], v[40:43], s[36:37] sc1\n\t"
    "v_mfma_f32_16x16x32_bf16 v[68:71], v[180:183], v[112:115], v[68:71]\n\t"
    "ds_read_b128 v[108:111], %[da] offset:18432\n\t"
    "v_cvt_pk_bf16_f32 v44, v88, v89\n\t"
    "v_mfma_f32_16x16x32_bf16 v[72:75], v[176:179], v[116:119], v[72:75]\n\t"
    "v_cvt_pk_bf16_f32 v45, v90, v91\n\t"
    "v_mfma_f32_16x16x32_bf16 v[76:79], v[180:183], v[116:119], v[76:79]\n\t"
    "v_cvt_pk_bf16_f32 v46, v92, v93\n\t"
    "v_mfma_f32_16x16x32_bf16 v[64:67], v[184:187], v[120:123], v[64:67]\n\t"
    "ds_read_b128 v[112:115], %[ea] offset:20480\n\t"
    "v_cvt_pk_bf16_f32 v47, v94, v95\n\t"
    "v_mfma_f32_16x16x32_bf16 v[68:71], v[188:191], v[120:123], v[68:71]\n\t"
    "ds_read_b128 v[116:119], %[ea] offset:22528\n\t"
    "global_store_dwordx4 %[co1], v[44:47], s[36:37] sc1\n\t"
    "v_mfma_f32_16x16x32_bf16 v[72:75], v[184:187], v[124:127], v[72:75]\n\t"
    "v_cvt_pk_bf16_f32 v48, v48, v49\n\t"
    "v_mfma_f32_16x16x32_bf16 v[76:79], v[188:191], v[124:127], v[76:79]\n\t"
    "v_cvt_pk_bf16_f32 v49, v50, v51\n\t"
    "s_waitcnt lgkmcnt(4)\n\t"
    "v_mfma_f32_16x16x32_bf16 v[64:67], v[192:195], v[96:99], v[64:67]\n\t"
    "ds_read_b128 v[120:123], %[da] offset:20480\n\t"
    "v_cvt_pk_bf16_f32 v50, v52, v53\n\t"
    "v_mfma_f32_16x16x32_bf16 v[68:71], v[196:199], v[96:99], v[68:71]\n\t"
    "ds_read_b128 v[124:127], %[da] offset:22528\n\t"
    "v_cvt_pk_bf16_f32 v51, v54, v55\n\t"
    "v_mfma_f32_16x16x32_bf16 v[72:75], v[192:195], v[100:103], v[72:75]\n\t"
    "global_store_dwordx4 %[co0], v[48:51], s[36:37] offset:2048 sc1\n\t"
    "v_mfma_f32_16x16x32_bf16 v[76:79], v[196:199], v[100:103], v[76:79]\n\t"
    "v_cvt_pk_bf16_f32 v56, v56, v57\n\t"
    "s_waitcnt lgkmcnt(4)\n\t"
    "v_mfma_f32_16x16x32_bf16 v[64:67], v[200:203], v[104:107], v[64:67]\n\t"
    "ds_read_b128 v[96:99], %[ea] offset:24576\n\t"
    "v_cvt_pk_bf16_f32 v57, v58, v59\n\t"
    "v_mfma_f32_16x16x32_bf16 v[68:71], v[204:207], v[104:107], v[68:71]\n\t"
    "ds_read_b128 v[100:103], %[ea] offset:26624\n\t"
    "v_cvt_pk_bf16_f32 v58, v60, v61\n\t"
    "v_mfma_f32_16x16x32_bf16 v[72:75], v[200:203], v[108:111], v[72:75]\n\t"
    "v_cvt_pk_bf16_f32 v59, v62, v63\n\t"
    "v_mfma_f32_16x16x32_bf16 v[76:79], v[204:207], v[108:111], v[76:79]\n\t"
    "global_store_dwordx4 %[co1], v[56:59], s[36:37] offset:2048 sc1\n\t"
    "s_waitcnt lgkmcnt(4)\n\t"
    "v_mfma_f32_16x16x32_bf16 v[64:67], v[208:211], v[112:115], v[64:67]\n\t"
    "ds_read_b128 v[104:107], %[da] offset:24576\n\t"
    "v_mfma_f32_16x16x32_bf16 v[68:71], v[212:215], v[112:115], v[68:71]\n\t"
    "ds_read_b128 v[108:111], %[da] offset:26624\n\t"
    "v_mfma_f32_16x16x32_bf16 v[72:75], v[208:211], v[116:119], v[72:75]\n\t"
    "v_mfma_f32_16x16x32_bf16 v[76:79], v[212:215], v[116:119], v[76:79]\n\t"
    "s_waitcnt lgkmcnt(4)\n\t"
    "v_mfma_f32_16x16x32_bf16 v[64:67], v[216:219], v[120:123], v[64:67]\n\t"
    "ds_read_b128 v[112:115], %[ea] offset:28672\n\t"
    "v_mfma_f32_16x16x32_bf16 v[68:71], v[220:223], v[120:123], v[68:71]\n\t"
    "ds_read_b128 v[116:119], %[ea] offset:30720\n\t"
    "v_mfma_f32_16x16x32_bf16 v[72:75], v[216:219], v[124:127], v[72:75]\n\t"
    "v_xor_b32 %[ea], 0x10000, %[ea]\n\t"
    "v_mfma_f32_16x16x32_bf16 v[76:79], v[220:223], v[124:127], v[76:79]\n\t"
    "s_waitcnt vmcnt(4)\n\t"
    "s_barrier\n\t"
    "s_waitcnt lgkmcnt(4)\n\t"
    "v_mfma_f32_16x16x32_bf16 v[64:67], v[224:227], v[96:99], v[64:67]\n\t"
    "ds_read_b32 v39, %[sa]\n\t"
    "ds_read_b128 v[120:123], %[da] offset:28672\n\t"
    "v_mfma_f32_16x16x32_bf16 v[68:71], v[228:231], v[96:99], v[68:71]\n\t"
    "ds_read_b128 v[124:127], %[da] offset:30720\n\t"
    "v_mfma_f32_16x16x32_bf16 v[72:75], v[224:227], v[100:103], v[72:75]\n\t"
    "v_xor_b32 %[da], 0x10000, %[da]\n\t"
    "v_mfma_f32_16x16x32_bf16 v[76:79], v[228:231], v[100:103], v[76:79]\n\t"
    "s_waitcnt lgkmcnt(5)\n\t"
    "v_mfma_f32_16x16x32_bf16 v[64:67], v[232:235], v[104:107], v[64:67]\n\t"
    "ds_read_b128 v[96:99], %[ea] offset:0\n\t"
    "v_mfma_f32_16x16x32_bf16 v[68:71], v[236:239], v[104:107], v[68:71]\n\t"
    "ds_read_b128 v[100:103], %[ea] offset:2048\n\t"
    "v_mfma_f32_16x16x32_bf16 v[72:75], v[232:235], v[108:111], v[72:75]\n\t"
    "v_mfma_f32_16x16x32_bf16 v[76:79], v[236:239], v[108:111], v[76:79]\n\t"
    "s_waitcnt lgkmcnt(4)\n\t"
    "v_mfma_f32_16x16x32_bf16 v[64:67], v[240:243], v[112:115], v[64:67]\n\t"
    "ds_read_b128 v[104:107], %[da] offset:0\n\t"
    "v_mad_u32_u24 v39, v39, %[mu], %[ad]\n\t"
    "v_mfma_f32_16x16x32_bf16 v[68:71], v[244:247], v[112:115], v[68:71]\n\t"
    "ds_read_b128 v[108:111], %[da] offset:2048\n\t"
    "v_cmp_ge_i32 vcc, %[lf], v39\n\t"
    "s_cmp_eq_u64 vcc, 0\n\t"
    "s_addc_u32 %[lf], %[lf], 0\n\t"
    "v_mfma_f32_16x16x32_bf16 v[72:75], v[240:243], v[116:119], v[72:75]\n\t"
    "v_mfma_f32_16x16x32_bf16 v[76:79], v[244:247], v[116:119], v[76:79]\n\t"
    "v_cmp_ge_i32 vcc, %[lf], v39\n\t"
    "s_cmp_eq_u64 vcc, 0\n\t"
    "s_addc_u32 %[lf], %[lf], 0\n\t"
    "s_waitcnt lgkmcnt(4)\n\t"
    "v_mfma_f32_16x16x32_bf16 v[80:83], v[248:251], v[120:123], v[64:67]\n\t"
    "ds_read_b128 v[112:115], %[ea] offset:4096\n\t"
    "s_min_i32 %[lf], %[lf], %[cp]\n\t"
    "s_add_u32 %[b], %[b], 1\n\t"
    "v_mfma_f32_16x16x32_bf16 v[84:87], v[252:255], v[120:123], v[68:71]\n\t"
    "ds_read_b128 v[116:119], %[ea] offset:6144\n\t"
    "v_mfma_f32_16x16x32_bf16 v[88:91], v[248:251], v[124:127], v[72:75]\n\t"
    "v_mfma_f32_16x16x32_bf16 v[92:95], v[252:255], v[124:127], v[76:79]\n\t"
    "s_mov_b64 s[36:37], s[38:39]\n\t"
    "s_add_u32 s38, s38, %[ab]\n\t"
    "s_addc_u32 s39, s39, 0\n\t"
    "s_cmp_lt_i32 %[b], %[lf]\n\t"
    "s_cbranch_scc1 1b\n\t"
    "s_waitcnt lgkmcnt(0)\n\t"
    "s_nop 4\n\t"
    "s_branch 9f\n\t"
    "5:\n\t"
    "s_add_u32 %[st], %[b], %[po]\n\t"
    "s_max_i32 %[st], %[st], %[p0]\n\t"
    "v_mov_b32 v39, %[st]\n\t"
    "v_mov_b32 v38, 0\n\t"
    "s_mov_b64 exec, 1\n\t"
    "global_store_dword v38, v39, %[pg] sc1\n\t"
    "s_mov_b64 exec, -1\n\t"
    "s_mov_b32 %[p0], %[p1]\n\t"
    "s_cmp_ge_i32 %[lf], %[cp]\n\t"
    "s_cbranch_scc1 6b\n\t"
    "s_mov_b32 m0, %[sl]\n\t"
    "s_nop 0\n\t"
    "global_load_lds_dword %[dv], %[db] sc1\n\t"
    "s_branch 6b\n\t"
    "9:"
    : "=&{v[80:95]}"(pd), [ea] "+&v"(ea), [da] "+&v"(da), [b] "+&s"(b), [lf] "+&s"(lf), "+{s[36:37]}"(p1), "+{s[38:39]}"(p0), [sa] "+&v"(sa), [st] "=&s"(st), [sl] "+&s"(sl), [p0] "+&s"(pf)
    : [rw] "v"(rw), [ab] "s"(ab), [cp] "s"(cp), [mu] "s"(mu), [ad] "v"(ad), "{v[128:159]}"(W[0]), "{v[160:191]}"(W[1]), "{v[192:223]}"(W[2]), "{v[224:255]}"(W[3]), [co0] "v"(co0), [w0] "s"(w0), [dv] "v"(dv), [db] "s"(db), [pg] "s"(pg), [po] "s"(po - 1), [p1] "s"(b + po), [co1] "v"(co1)
    : "v38", "v39", "v40", "v41", "v42", "v43", "v44", "v45", "v46", "v47", "v48", "v49", "v50", "v51", "v52", "v53", "v54", "v55", "v56", "v57", "v58", "v59", "v60", "v61", "v62", "v63", "v64", "v65", "v66", "v67", "v68", "v69", "v70", "v71", "v72", "v73", "v74", "v75", "v76", "v77", "v78", "v79", "v96", "v97", "v98", "v99", "v100", "v101", "v102", "v103", "v104", "v105", "v106", "v107", "v108", "v109", "v110", "v111", "v112", "v113", "v114", "v115", "v116", "v117", "v118", "v119", "v120", "v121", "v122", "v123", "v124", "v125", "v126", "v127", "vcc", "scc", "m0", "memory");
}
__device__ __forceinline__ void lean_ld0(const u32x32 (&W)[4], u32x16& pd, unsigned ea, unsigned da, unsigned x, unsigned lb, const char* s0, const char* s1, const unsigned (&av)[8], unsigned sa, unsigned mu, unsigned ad, int th, unsigned s2, unsigned dv, const char* db) {
  asm volatile("s_setprio 2\n\t"
    "ds_read_b128 v[96:99], %[ea] offset:0\n\t"
    "ds_read_b128 v[100:103], %[ea] offset:2048\n\t"
    "ds_read_b128 v[104:107], %[da] offset:0\n\t"
    "ds_read_b128 v[108:111], %[da] offset:2048\n\t"
    "ds_read_b128 v[112:115], %[ea] offset:4096\n\t"
    "ds_read_b128 v[116:119], %[ea] offset:6144\n\t"
    "s_waitcnt lgkmcnt(4)\n\t"
    "v_mfma_f32_16x16x32_bf16 v[80:83], v[128:131], v[96:99], 0\n\t"
    "ds_read_b128 v[120:123], %[da] offset:4096\n\t"
    "v_mfma_f32_16x16x32_bf16 v[84:87], v[132:135], v[96:99], 0\n\t"
    "ds_read_b128 v[124:127], %[da] offset:6144\n\t"
    "v_mfma_f32_16x16x32_bf16 v[88:91], v[128:131], v[100:103], 0\n\t"
    "v_mfma_f32_16x16x32_bf16 v[92:95], v[132:135], v[100:103], 0\n\t"
    "s_waitcnt vmcnt(24)\n\t"
    "s_cmp_lt_u32 %[x], 2\n\t"
    "s_cbranch_scc0 41f\n\t"
    "s_waitcnt vmcnt(8)\n\t"
    "41:\n\t"
    "s_barrier\n\t"
    "s_waitcnt lgkmcnt(4)\n\t"
    "v_mfma_f32_16x16x32_bf16 v[80:83], v[136:139], v[104:107], v[80:83]\n\t"
    "ds_read_b128 v[96:99], %[ea] offset:8192\n\t"
    "v_mfma_f32_16x16x32_bf16 v[84:87], v[140:143], v[104:107], v[84:87]\n\t"
    "ds_read_b128 v[100:103], %[ea] offset:10240\n\t"
    "v_mfma_f32_16x16x32_bf16 v[88:91], v[136:139], v[108:111], v[88:91]\n\t"
    "v_mfma_f32_16x16x32_bf16 v[92:95], v[140:143], v[108:111], v[92:95]\n\t"
    "s_waitcnt lgkmcnt(4)\n\t"
    "v_mfma_f32_16x16x32_bf16 v[80:83], v[144:147], v[112:115], v[80:83]\n\t"
    "ds_read_b128 v[104:107], %[da] offset:8192\n\t"
    "v_mfma_f32_16x16x32_bf16 v[84:87], v[148:151], v[112:115], v[84:87]\n\t"
    "ds_read_b128 v[108:111], %[da] offset:10240\n\t"
    "v_mfma_f32_16x16x32_bf16 v[88:91], v[144:147], v[116:119], v[88:91]\n\t"
    "v_mfma_f32_16x16x32_bf16 v[92:95], v[148:151], v[116:119], v[92:95]\n\t"
    "s_waitcnt lgkmcnt(4)\n\t"
    "v_mfma_f32_16x16x32_bf16 v[80:83], v[152:155], v[120:123], v[80:83]\n\t"
    "ds_read_b128 v[112:115], %[ea] offset:12288\n\t"
    "v_mfma_f32_16x16x32_bf16 v[84:87], v[156:159], v[120:123], v[84:87]\n\t"
    "ds_read_b128 v[116:119], %[ea] offset:14336\n\t"
    "v_mfma_f32_16x16x32_bf16 v[88:91], v[152:155], v[124:127], v[88:91]\n\t"
    "v_mfma_f32_16x16x32_bf16 v[92:95], v[156:159], v[124:127], v[92:95]\n\t"
    "s_waitcnt lgkmcnt(4)\n\t"
    "v_mfma_f32_16x16x32_bf16 v[80:83], v[160:163], v[96:99], v[80:83]\n\t"
    "ds_read_b128 v[120:123], %[da] offset:12288\n\t"
    "v_mfma_f32_16x16x32_bf16 v[84:87], v[164:167], v[96:99], v[84:87]\n\t"
    "ds_read_b128 v[124:127], %[da] offset:14336\n\t"
    "v_mfma_f32_16x16x32_bf16 v[88:91], v[160:163], v[100:103], v[88:91]\n\t"
    "v_mfma_f32_16x16x32_bf16 v[92:95], v[164:167], v[100:103], v[92:95]\n\t"
    "s_waitcnt vmcnt(20)\n\t"
    "s_cmp_lt_u32 %[x], 2\n\t"
    "s_cbranch_scc0 45f\n\t"
    "s_waitcnt vmcnt(4)\n\t"
    "45:\n\t"
    "s_barrier\n\t"
    "s_waitcnt lgkmcnt(4)\n\t"
    "v_mfma_f32_16x16x32_bf16 v[80:83], v[168:171], v[104:107], v[80:83]\n\t"
    "ds_read_b128 v[96:99], %[ea] offset:16384\n\t"
    "v_mfma_f32_16x16x32_bf16 v[84:87], v[172:175], v[104:107], v[84:87]\n\t"
    "ds_read_b128 v[100:103], %[ea] offset:18432\n\t"
    "v_mfma_f32_16x16x32_bf16 v[88:91], v[168:171], v[108:111], v[88:91]\n\t"
    "v_mfma_f32_16x16x32_bf16 v[92:95], v[172:175], v[108:111], v[92:95]\n\t"
    "s_waitcnt lgkmcnt(4)\n\t"
    "v_mfma_f32_16x16x32_bf16 v[80:83], v[176:179], v[112:115], v[80:83]\n\t"
    "ds_read_b128 v[104:107], %[da] offset:16384\n\t"
    "v_mfma_f32_16x16x32_bf16 v[84:87], v[180:183], v[112:115], v[84:87]\n\t"
    "ds_read_b128 v[108:111], %[da] offset:18432\n\t"
    "v_mfma_f32_16x16x32_bf16 v[88:91], v[176:179], v[116:119], v[88:91]\n\t"
    "v_mfma_f32_16x16x32_bf16 v[92:95], v[180:183], v[116:119], v[92:95]\n\t"
    "s_waitcnt lgkmcnt(4)\n\t"
    "v_mfma_f32_16x16x32_bf16 v[80:83], v[184:187], v[120:123], v[80:83]\n\t"
    "ds_read_b128 v[112:115], %[ea] offset:20480\n\t"
    "v_mfma_f32_16x16x32_bf16 v[84:87], v[188:191], v[120:123], v[84:87]\n\t"
    "ds_read_b128 v[116:119], %[ea] offset:22528\n\t"
    "v_mfma_f32_16x16x32_bf16 v[88:91], v[184:187], v[124:127], v[88:91]\n\t"
    "v_mfma_f32_16x16x32_bf16 v[92:95], v[188:191], v[124:127], v[92:95]\n\t"
    "s_waitcnt lgkmcnt(4)\n\t"
    "v_mfma_f32_16x16x32_bf16 v[80:83], v[192:195], v[96:99], v[80:83]\n\t"
    "ds_read_b128 v[120:123], %[da] offset:20480\n\t"
    "v_mfma_f32_16x16x32_bf16 v[84:87], v[196:199], v[96:99], v[84:87]\n\t"
    "ds_read_b128 v[124:127], %[da] offset:22528\n\t"
    "v_mfma_f32_16x16x32_bf16 v[88:91], v[192:195], v[100:103], v[88:91]\n\t"
    "v_mfma_f32_16x16x32_bf16 v[92:95], v[196:199], v[100:103], v[92:95]\n\t"
    "s_waitcnt vmcnt(16)\n\t"
    "s_cmp_lt_u32 %[x], 2\n\t"
    "s_cbranch_scc0 49f\n\t"
    "s_waitcnt vmcnt(0)\n\t"
    "49:\n\t"
    "s_barrier\n\t"
    "ds_read_b32 v79, %[sa]\n\t"
    "s_waitcnt lgkmcnt(5)\n\t"
    "v_mfma_f32_16x16x32_bf16 v[80:83], v[200:203], v[104:107], v[80:83]\n\t"
    "ds_read_b128 v[96:99], %[ea] offset:24576\n\t"
    "v_mfma_f32_16x16x32_bf16 v[84:87], v[204:207], v[104:107], v[84:87]\n\t"
    "ds_read_b128 v[100:103], %[ea] offset:26624\n\t"
    "v_mfma_f32_16x16x32_bf16 v[88:91], v[200:203], v[108:111], v[88:91]\n\t"
    "v_mfma_f32_16x16x32_bf16 v[92:95], v[204:207], v[108:111], v[92:95]\n\t"
    "s_waitcnt lgkmcnt(5)\n\t"
    "v_mfma_f32_16x16x32_bf16 v[80:83], v[208:211], v[112:115], v[80:83]\n\t"
    "ds_read_b128 v[104:107], %[da] offset:24576\n\t"
    "v_mfma_f32_16x16x32_bf16 v[84:87], v[212:215], v[112:115], v[84:87]\n\t"
    "ds_read_b128 v[108:111], %[da] offset:26624\n\t"
    "v_mfma_f32_16x16x32_bf16 v[88:91], v[208:211], v[116:119], v[88:91]\n\t"
    "v_mfma_f32_16x16x32_bf16 v[92:95], v[212:215], v[116:119], v[92:95]\n\t"
    "s_cmp_eq_u32 %[s2], 0\n\t"
    "s_cbranch_scc1 7f\n\t"
    "s_mov_b32 m0, %[s2]\n\t"
    "s_nop 0\n\t"
    "global_load_lds_dword %[dv], %[db] sc1\n\t"
    "7:\n\t"
    "s_waitcnt lgkmcnt(4)\n\t"
    "v_mfma_f32_16x16x32_bf16 v[80:83], v[216:219], v[120:123], v[80:83]\n\t"
    "ds_read_b128 v[112:115], %[ea] offset:28672\n\t"
    "v_mfma_f32_16x16x32_bf16 v[84:87], v[220:223], v[120:123], v[84:87]\n\t"
    "ds_read_b128 v[116:119], %[ea] offset:30720\n\t"
    "v_mfma_f32_16x16x32_bf16 v[88:91], v[216:219], v[124:127], v[88:91]\n\t"
    "v_mad_u32_u24 v79, v79, %[mu], %[ad]\n\t"
    "v_cmp_ge_i32 vcc, %[th], v79\n\t"
    "v_mfma_f32_16x16x32_bf16 v[92:95], v[220:223], v[124:127], v[92:95]\n\t"
    "s_cbranch_vccz 5f\n\t"
    "s_waitcnt lgkmcnt(4)\n\t"
    "v_mfma_f32_16x16x32_bf16 v[80:83], v[224:227], v[96:99], v[80:83]\n\t"
    "ds_read_b128 v[120:123], %[da] offset:28672\n\t"
    "v_mfma_f32_16x16x32_bf16 v[84:87], v[228:231], v[96:99], v[84:87]\n\t"
    "ds_read_b128 v[124:127], %[da] offset:30720\n\t"
    "v_mfma_f32_16x16x32_bf16 v[88:91], v[224:227], v[100:103], v[88:91]\n\t"
    "v_mfma_f32_16x16x32_bf16 v[92:95], v[228:231], v[100:103], v[92:95]\n\t"
    "s_waitcnt lgkmcnt(4)\n\t"
    "v_mfma_f32_16x16x32_bf16 v[80:83], v[232:235], v[104:107], v[80:83]\n\t"
    "v_mfma_f32_16x16x32_bf16 v[84:87], v[236:239], v[104:107], v[84:87]\n\t"
    "v_mfma_f32_16x16x32_bf16 v[88:91], v[232:235], v[108:111], v[88:91]\n\t"
    "v_mfma_f32_16x16x32_bf16 v[92:95], v[236:239], v[108:111], v[92:95]\n\t"
    "s_waitcnt lgkmcnt(2)\n\t"
    "v_mfma_f32_16x16x32_bf16 v[80:83], v[240:243], v[112:115], v[80:83]\n\t"
    "v_mfma_f32_16x16x32_bf16 v[84:87], v[244:247], v[112:115], v[84:87]\n\t"
    "v_mfma_f32_16x16x32_bf16 v[88:91], v[240:243], v[116:119], v[88:91]\n\t"
    "v_mfma_f32_16x16x32_bf16 v[92:95], v[244:247], v[116:119], v[92:95]\n\t"
    "s_waitcnt lgkmcnt(0)\n\t"
    "v_mfma_f32_16x16x32_bf16 v[80:83], v[248:251], v[120:123], v[80:83]\n\t"
    "v_mfma_f32_16x16x32_bf16 v[84:87], v[252:255], v[120:123], v[84:87]\n\t"
    "v_mfma_f32_16x16x32_bf16 v[88:91], v[248:251], v[124:127], v[88:91]\n\t"
    "v_mfma_f32_16x16x32_bf16 v[92:95], v[252:255], v[124:127], v[92:95]\n\t"
    "s_branch 6f\n\t"
    "5:\n\t"
    "s_waitcnt lgkmcnt(4)\n\t"
    "v_mfma_f32_16x16x32_bf16 v[80:83], v[224:227], v[96:99], v[80:83]\n\t"
    "ds_read_b128 v[120:123], %[da] offset:28672\n\t"
    "s_add_u32 m0, %[lb], 0\n\t"
    "s_nop 0\n\t"
    "global_load_lds_dwordx4 %[av0], %[s0] sc1\n\t"
    "v_mfma_f32_16x16x32_bf16 v[84:87], v[228:231], v[96:99], v[84:87]\n\t"
    "ds_read_b128 v[124:127], %[da] offset:30720\n\t"
    "s_add_u32 m0, %[lb], 32768\n\t"
    "s_nop 0\n\t"
    "global_load_lds_dwordx4 %[av1], %[s0] sc1\n\t"
    "v_mfma_f32_16x16x32_bf16 v[88:91], v[224:227], v[100:103], v[88:91]\n\t"
    "s_add_u32 m0, %[lb], 4096\n\t"
    "s_nop 0\n\t"
    "global_load_lds_dwordx4 %[av2], %[s0] sc1\n\t"
    "v_mfma_f32_16x16x32_bf16 v[92:95], v[228:231], v[100:103], v[92:95]\n\t"
    "s_add_u32 m0, %[lb], 36864\n\t"
    "s_nop 0\n\t"
    "global_load_lds_dwordx4 %[av3], %[s0] sc1\n\t"
    "s_waitcnt lgkmcnt(4)\n\t"
    "v_mfma_f32_16x16x32_bf16 v[80:83], v[232:235], v[104:107], v[80:83]\n\t"
    "s_add_u32 m0, %[lb], 8192\n\t"
    "s_nop 0\n\t"
    "global_load_lds_dwordx4 %[av4], %[s0] sc1\n\t"
    "v_mfma_f32_16x16x32_bf16 v[84:87], v[236:239], v[104:107], v[84:87]\n\t"
    "s_add_u32 m0, %[lb], 40960\n\t"
    "s_nop 0\n\t"
    "global_load_lds_dwordx4 %[av5], %[s0] sc1\n\t"
    "v_mfma_f32_16x16x32_bf16 v[88:91], v[232:235], v[108:111], v[88:91]\n\t"
    "s_add_u32 m0, %[lb], 12288\n\t"
    "s_nop 0\n\t"
    "global_load_lds_dwordx4 %[av6], %[s0] sc1\n\t"
    "v_mfma_f32_16x16x32_bf16 v[92:95], v[236:239], v[108:111], v[92:95]\n\t"
    "s_add_u32 m0, %[lb], 45056\n\t"
    "s_nop 0\n\t"
    "global_load_lds_dwordx4 %[av7], %[s0] sc1\n\t"
    "s_waitcnt lgkmcnt(2)\n\t"
    "v_mfma_f32_16x16x32_bf16 v[80:83], v[240:243], v[112:115], v[80:83]\n\t"
    "s_add_u32 m0, %[lb], 16384\n\t"
    "s_nop 0\n\t"
    "global_load_lds_dwordx4 %[av0], %[s1] sc1\n\t"
    "v_mfma_f32_16x16x32_bf16 v[84:87], v[244:247], v[112:115], v[84:87]\n\t"
    "s_add_u32 m0, %[lb], 49152\n\t"
    "s_nop 0\n\t"
    "global_load_lds_dwordx4 %[av1], %[s1] sc1\n\t"
    "v_mfma_f32_16x16x32_bf16 v[88:91], v[240:243], v[116:119], v[88:91]\n\t"
    "s_add_u32 m0, %[lb], 20480\n\t"
    "s_nop 0\n\t"
    "global_load_lds_dwordx4 %[av2], %[s1] sc1\n\t"
    "v_mfma_f32_16x16x32_bf16 v[92:95], v[244:247], v[116:119], v[92:95]\n\t"
    "s_add_u32 m0, %[lb], 53248\n\t"
    "s_nop 0\n\t"
    "global_load_lds_dwordx4 %[av3], %[s1] sc1\n\t"
    "s_waitcnt lgkmcnt(0)\n\t"
    "v_mfma_f32_16x16x32_bf16 v[80:83], v[248:251], v[120:123], v[80:83]\n\t"
    "s_add_u32 m0, %[lb], 24576\n\t"
    "s_nop 0\n\t"
    "global_load_lds_dwordx4 %[av4], %[s1] sc1\n\t"
    "v_mfma_f32_16x16x32_bf16 v[84:87], v[252:255], v[120:123], v[84:87]\n\t"
    "s_add_u32 m0, %[lb], 57344\n\t"
    "s_nop 0\n\t"
    "global_load_lds_dwordx4 %[av5], %[s1] sc1\n\t"
    "v_mfma_f32_16x16x32_bf16 v[88:91], v[248:251], v[124:127], v[88:91]\n\t"
    "s_add_u32 m0, %[lb], 28672\n\t"
    "s_nop 0\n\t"
    "global_load_lds_dwordx4 %[av6], %[s1] sc1\n\t"
    "v_mfma_f32_16x16x32_bf16 v[92:95], v[252:255], v[124:127], v[92:95]\n\t"
    "s_add_u32 m0, %[lb], 61440\n\t"
    "s_nop 0\n\t"
    "global_load_lds_dwordx4 %[av7], %[s1] sc1\n\t"
    "6:\n\t"
    "s_nop 6\n\t"
    "s_branch 9f\n\t"
    "3:\n\t"
    "9:"
    : "=&{v[80:95]}"(pd)
    : [ea] "v"(ea), [da] "v"(da), [x] "s"(x), "{v[128:159]}"(W[0]), "{v[160:191]}"(W[1]), "{v[192:223]}"(W[2]), "{v[224:255]}"(W[3]), [lb] "s"(lb), [s0] "s"(s0), [s1] "s"(s1), [av0] "v"(av[0]), [av1] "v"(av[1]), [av2] "v"(av[2]), [av3] "v"(av[3]), [av4] "v"(av[4]), [av5] "v"(av[5]), [av6] "v"(av[6]), [av7] "v"(av[7]), [sa] "v"(sa), [mu] "s"(mu), [ad] "v"(ad), [th] "s"(th), [s2] "s"(s2), [dv] "v"(dv), [db] "s"(db)
    : "v96", "v97", "v98", "v99", "v100", "v101", "v102", "v103", "v104", "v105", "v106", "v107", "v108", "v109", "v110", "v111", "v112", "v113", "v114", "v115", "v116", "v117", "v118", "v119", "v120", "v121", "v122", "v123", "v124", "v125", "v126", "v127", "v79", "vcc", "scc", "m0", "memory");
}
__device__ __forceinline__ void lean_dm0(const u32x32 (&W)[4], u32x16& pd, unsigned ea, unsigned da, unsigned x, unsigned lb, const char* s0, const char* s1, const unsigned (&av)[8]) {
  asm volatile("s_setprio 2\n\t"
    "ds_read_b128 v[96:99], %[ea] offset:0\n\t"
    "ds_read_b128 v[100:103], %[ea] offset:2048\n\t"
    "ds_read_b128 v[104:107], %[da] offset:0\n\t"
    "ds_read_b128 v[108:111], %[da] offset:2048\n\t"
    "ds_read_b128 v[112:115], %[ea] offset:4096\n\t"
    "ds_read_b128 v[116:119], %[ea] offset:6144\n\t"
    "s_waitcnt lgkmcnt(4)\n\t"
    "v_mfma_f32_16x16x32_bf16 v[80:83], v[128:131], v[96:99], 0\n\t"
    "ds_read_b128 v[120:123], %[da] offset:4096\n\t"
    "s_add_u32 m0, %[lb], 0\n\t"
    "v_mfma_f32_16x16x32_bf16 v[84:87], v[132:135], v[96:99], 0\n\t"
    "ds_read_b128 v[124:127], %[da] offset:6144\n\t"
    "global_load_lds_dwordx4 %[av0], %[s0] sc1\n\t"
    "v_mfma_f32_16x16x32_bf16 v[88:91], v[128:131], v[100:103], 0\n\t"
    "s_add_u32 m0, %[lb], 32768\n\t"
    "v_mfma_f32_16x16x32_bf16 v[92:95], v[132:135], v[100:103], 0\n\t"
    "global_load_lds_dwordx4 %[av1], %[s0] sc1\n\t"
    "s_waitcnt vmcnt(10)\n\t"
    "s_barrier\n\t"
    "s_waitcnt lgkmcnt(4)\n\t"
    "v_mfma_f32_16x16x32_bf16 v[80:83], v[136:139], v[104:107], v[80:83]\n\t"
    "ds_read_b128 v[96:99], %[ea] offset:8192\n\t"
    "s_add_u32 m0, %[lb], 4096\n\t"
    "v_mfma_f32_16x16x32_bf16 v[84:87], v[140:143], v[104:107], v[84:87]\n\t"
    "ds_read_b128 v[100:103], %[ea] offset:10240\n\t"
    "global_load_lds_dwordx4 %[av2], %[s0] sc1\n\t"
    "v_mfma_f32_16x16x32_bf16 v[88:91], v[136:139], v[108:111], v[88:91]\n\t"
    "s_add_u32 m0, %[lb], 36864\n\t"
    "v_mfma_f32_16x16x32_bf16 v[92:95], v[140:143], v[108:111], v[92:95]\n\t"
    "global_load_lds_dwordx4 %[av3], %[s0] sc1\n\t"
    "s_waitcnt lgkmcnt(4)\n\t"
    "v_mfma_f32_16x16x32_bf16 v[80:83], v[144:147], v[112:115], v[80:83]\n\t"
    "ds_read_b128 v[104:107], %[da] offset:8192\n\t"
    "s_add_u32 m0, %[lb], 8192\n\t"
    "v_mfma_f32_16x16x32_bf16 v[84:87], v[148:151], v[112:115], v[84:87]\n\t"
    "ds_read_b128 v[108:111], %[da] offset:10240\n\t"
    "global_load_lds_dwordx4 %[av4], %[s0] sc1\n\t"
    "v_mfma_f32_16x16x32_bf16 v[88:91], v[144:147], v[116:119], v[88:91]\n\t"
    "s_add_u32 m0, %[lb], 40960\n\t"
    "v_mfma_f32_16x16x32_bf16 v[92:95], v[148:151], v[116:119], v[92:95]\n\t"
    "global_load_lds_dwordx4 %[av5], %[s0] sc1\n\t"
    "s_waitcnt lgkmcnt(4)\n\t"
    "v_mfma_f32_16x16x32_bf16 v[80:83], v[152:155], v[120:123], v[80:83]\n\t"
    "ds_read_b128 v[112:115], %[ea] offset:12288\n\t"
    "s_add_u32 m0, %[lb], 12288\n\t"
    "v_mfma_f32_16x16x32_bf16 v[84:87], v[156:159], v[120:123], v[84:87]\n\t"
    "ds_read_b128 v[116:119], %[ea] offset:14336\n\t"
    "global_load_lds_dwordx4 %[av6], %[s0] sc1\n\t"
    "v_mfma_f32_16x16x32_bf16 v[88:91], v[152:155], v[124:127], v[88:91]\n\t"
    "s_add_u32 m0, %[lb], 45056\n\t"
    "v_mfma_f32_16x16x32_bf16 v[92:95], v[156:159], v[124:127], v[92:95]\n\t"
    "global_load_lds_dwordx4 %[av7], %[s0] sc1\n\t"
    "s_waitcnt lgkmcnt(4)\n\t"
    "v_mfma_f32_16x16x32_bf16 v[80:83], v[160:163], v[96:99], v[80:83]\n\t"
    "ds_read_b128 v[120:123], %[da] offset:12288\n\t"
    "s_add_u32 m0, %[lb], 16384\n\t"
    "v_mfma_f32_16x16x32_bf16 v[84:87], v[164:167], v[96:99], v[84:87]\n\t"
    "ds_read_b128 v[124:127], %[da] offset:14336\n\t"
    "global_load_lds_dwordx4 %[av0], %[s1] sc1\n\t"
    "v_mfma_f32_16x16x32_bf16 v[88:91], v[160:163], v[100:103], v[88:91]\n\t"
    "s_add_u32 m0, %[lb], 49152\n\t"
    "v_mfma_f32_16x16x32_bf16 v[92:95], v[164:167], v[100:103], v[92:95]\n\t"
    "global_load_lds_dwordx4 %[av1], %[s1] sc1\n\t"
    "s_waitcnt vmcnt(14)\n\t"
    "s_barrier\n\t"
    "s_waitcnt lgkmcnt(4)\n\t"
    "v_mfma_f32_16x16x32_bf16 v[80:83], v[168:171], v[104:107], v[80:83]\n\t"
    "ds_read_b128 v[96:99], %[ea] offset:16384\n\t"
    "s_add_u32 m0, %[lb], 20480\n\t"
    "v_mfma_f32_16x16x32_bf16 v[84:87], v[172:175], v[104:107], v[84:87]\n\t"
    "ds_read_b128 v[100:103], %[ea] offset:18432\n\t"
    "global_load_lds_dwordx4 %[av2], %[s1] sc1\n\t"
    "v_mfma_f32_16x16x32_bf16 v[88:91], v[168:171], v[108:111], v[88:91]\n\t"
    "s_add_u32 m0, %[lb], 53248\n\t"
    "v_mfma_f32_16x16x32_bf16 v[92:95], v[172:175], v[108:111], v[92:95]\n\t"
    "global_load_lds_dwordx4 %[av3], %[s1] sc1\n\t"
    "s_waitcnt lgkmcnt(4)\n\t"
    "v_mfma_f32_16x16x32_bf16 v[80:83], v[176:179], v[112:115], v[80:83]\n\t"
    "ds_read_b128 v[104:107], %[da] offset:16384\n\t"
    "s_add_u32 m0, %[lb], 24576\n\t"
    "v_mfma_f32_16x16x32_bf16 v[84:87], v[180:183], v[112:115], v[84:87]\n\t"
    "ds_read_b128 v[108:111], %[da] offset:18432\n\t"
    "global_load_lds_dwordx4 %[av4], %[s1] sc1\n\t"
    "v_mfma_f32_16x16x32_bf16 v[88:91], v[176:179], v[116:119], v[88:91]\n\t"
    "s_add_u32 m0, %[lb], 57344\n\t"
    "v_mfma_f32_16x16x32_bf16 v[92:95], v[180:183], v[116:119], v[92:95]\n\t"
    "global_load_lds_dwordx4 %[av5], %[s1] sc1\n\t"
    "s_waitcnt lgkmcnt(4)\n\t"
    "v_mfma_f32_16x16x32_bf16 v[80:83], v[184:187], v[120:123], v[80:83]\n\t"
    "ds_read_b128 v[112:115], %[ea] offset:20480\n\t"
    "s_add_u32 m0, %[lb], 28672\n\t"
    "v_mfma_f32_16x16x32_bf16 v[84:87], v[188:191], v[120:123], v[84:87]\n\t"
    "ds_read_b128 v[116:119], %[ea] offset:22528\n\t"
    "global_load_lds_dwordx4 %[av6], %[s1] sc1\n\t"
    "v_mfma_f32_16x16x32_bf16 v[88:91], v[184:187], v[124:127], v[88:91]\n\t"
    "s_add_u32 m0, %[lb], 61440\n\t"
    "v_mfma_f32_16x16x32_bf16 v[92:95], v[188:191], v[124:127], v[92:95]\n\t"
    "global_load_lds_dwordx4 %[av7], %[s1] sc1\n\t"
    "s_waitcnt lgkmcnt(4)\n\t"
    "v_mfma_f32_16x16x32_bf16 v[80:83], v[192:195], v[96:99], v[80:83]\n\t"
    "ds_read_b128 v[120:123], %[da] offset:20480\n\t"
    "v_mfma_f32_16x16x32_bf16 v[84:87], v[196:199], v[96:99], v[84:87]\n\t"
    "ds_read_b128 v[124:127], %[da] offset:22528\n\t"
    "v_mfma_f32_16x16x32_bf16 v[88:91], v[192:195], v[100:103], v[88:91]\n\t"
    "v_mfma_f32_16x16x32_bf16 v[92:95], v[196:199], v[100:103], v[92:95]\n\t"
    "s_waitcnt vmcnt(16)\n\t"
    "s_barrier\n\t"
    "s_waitcnt lgkmcnt(4)\n\t"
    "v_mfma_f32_16x16x32_bf16 v[80:83], v[200:203], v[104:107], v[80:83]\n\t"
    "ds_read_b128 v[96:99], %[ea] offset:24576\n\t"
    "v_mfma_f32_16x16x32_bf16 v[84:87], v[204:207], v[104:107], v[84:87]\n\t"
    "ds_read_b128 v[100:103], %[ea] offset:26624\n\t"
    "v_mfma_f32_16x16x32_bf16 v[88:91], v[200:203], v[108:111], v[88:91]\n\t"
    "v_mfma_f32_16x16x32_bf16 v[92:95], v[204:207], v[108:111], v[92:95]\n\t"
    "s_waitcnt lgkmcnt(4)\n\t"
    "v_mfma_f32_16x16x32_bf16 v[80:83], v[208:211], v[112:115], v[80:83]\n\t"
    "ds_read_b128 v[104:107], %[da] offset:24576\n\t"
    "v_mfma_f32_16x16x32_bf16 v[84:87], v[212:215], v[112:115], v[84:87]\n\t"
    "ds_read_b128 v[108:111], %[da] offset:26624\n\t"
    "v_mfma_f32_16x16x32_bf16 v[88:91], v[208:211], v[116:119], v[88:91]\n\t"
    "v_mfma_f32_16x16x32_bf16 v[92:95], v[212:215], v[116:119], v[92:95]\n\t"
    "s_waitcnt lgkmcnt(4)\n\t"
    "v_mfma_f32_16x16x32_bf16 v[80:83], v[216:219], v[120:123], v[80:83]\n\t"
    "ds_read_b128 v[112:115], %[ea] offset:28672\n\t"
    "v_mfma_f32_16x16x32_bf16 v[84:87], v[220:223], v[120:123], v[84:87]\n\t"
    "ds_read_b128 v[116:119], %[ea] offset:30720\n\t"
    "v_mfma_f32_16x16x32_bf16 v[88:91], v[216:219], v[124:127], v[88:91]\n\t"
    "v_mfma_f32_16x16x32_bf16 v[92:95], v[220:223], v[124:127], v[92:95]\n\t"
    "s_waitcnt lgkmcnt(4)\n\t"
    "v_mfma_f32_16x16x32_bf16 v[80:83], v[224:227], v[96:99], v[80:83]\n\t"
    "ds_read_b128 v[120:123], %[da] offset:28672\n\t"
    "v_mfma_f32_16x16x32_bf16 v[84:87], v[228:231], v[96:99], v[84:87]\n\t"
    "ds_read_b128 v[124:127], %[da] offset:30720\n\t"
    "v_mfma_f32_16x16x32_bf16 v[88:91], v[224:227], v[100:103], v[88:91]\n\t"
    "v_mfma_f32_16x16x32_bf16 v[92:95], v[228:231], v[100:103], v[92:95]\n\t"
    "s_waitcnt lgkmcnt(4)\n\t"
    "v_mfma_f32_16x16x32_bf16 v[80:83], v[232:235], v[104:107], v[80:83]\n\t"
    "v_mfma_f32_16x16x32_bf16 v[84:87], v[236:239], v[104:107], v[84:87]\n\t"
    "v_mfma_f32_16x16x32_bf16 v[88:91], v[232:235], v[108:111], v[88:91]\n\t"
    "v_mfma_f32_16x16x32_bf16 v[92:95], v[236:239], v[108:111], v[92:95]\n\t"
    "s_waitcnt lgkmcnt(2)\n\t"
    "v_mfma_f32_16x16x32_bf16 v[80:83], v[240:243], v[112:115], v[80:83]\n\t"
    "v_mfma_f32_16x16x32_bf16 v[84:87], v[244:247], v[112:115], v[84:87]\n\t"
    "v_mfma_f32_16x16x32_bf16 v[88:91], v[240:243], v[116:119], v[88:91]\n\t"
    "v_mfma_f32_16x16x32_bf16 v[92:95], v[244:247], v[116:119], v[92:95]\n\t"
    "s_waitcnt lgkmcnt(0)\n\t"
    "v_mfma_f32_16x16x32_bf16 v[80:83], v[248:251], v[120:123], v[80:83]\n\t"
    "v_mfma_f32_16x16x32_bf16 v[84:87], v[252:255], v[120:123], v[84:87]\n\t"
    "v_mfma_f32_16x16x32_bf16 v[88:91], v[248:251], v[124:127], v[88:91]\n\t"
    "v_mfma_f32_16x16x32_bf16 v[92:95], v[252:255], v[124:127], v[92:95]\n\t"
    "s_nop 6\n\t"
    "s_branch 9f\n\t"
    "3:\n\t"
    "9:"
    : "=&{v[80:95]}"(pd)
    : [ea] "v"(ea), [da] "v"(da), [x] "s"(x), "{v[128:159]}"(W[0]), "{v[160:191]}"(W[1]), "{v[192:223]}"(W[2]), "{v[224:255]}"(W[3]), [lb] "s"(lb), [s0] "s"(s0), [s1] "s"(s1), [av0] "v"(av[0]), [av1] "v"(av[1]), [av2] "v"(av[2]), [av3] "v"(av[3]), [av4] "v"(av[4]), [av5] "v"(av[5]), [av6] "v"(av[6]), [av7] "v"(av[7])
    : "v96", "v97", "v98", "v99", "v100", "v101", "v102", "v103", "v104", "v105", "v106", "v107", "v108", "v109", "v110", "v111", "v112", "v113", "v114", "v115", "v116", "v117", "v118", "v119", "v120", "v121", "v122", "v123", "v124", "v125", "v126", "v127", "vcc", "scc", "m0", "memory");
}
__device__ __forceinline__ void lean_ld1(const u32x32 (&W)[4], u32x16& pd, unsigned ea, unsigned da, unsigned x, unsigned lb, const char* s0, const char* s1, const unsigned (&av)[8], unsigned sa, unsigned mu, unsigned ad, int th, unsigned s2, unsigned dv, const char* db) {
  asm volatile("s_setprio 2\n\t"
    "ds_read_b128 v[96:99], %[ea] offset:0\n\t"
    "ds_read_b128 v[100:103], %[ea] offset:2048\n\t"
    "ds_read_b128 v[104:107], %[da] offset:0\n\t"
    "ds_read_b128 v[108:111], %[da] offset:2048\n\t"
    "ds_read_b128 v[112:115], %[ea] offset:4096\n\t"
    "ds_read_b128 v[116:119], %[ea] offset:6144\n\t"
    "s_waitcnt lgkmcnt(4)\n\t"
    "v_mfma_f32_16x16x32_bf16 v[80:83], v[128:131], v[96:99], 0\n\t"
    "ds_read_b128 v[120:123], %[da] offset:4096\n\t"
    "v_mfma_f32_16x16x32_bf16 v[84:87], v[132:135], v[96:99], 0\n\t"
    "ds_read_b128 v[124:127], %[da] offset:6144\n\t"
    "v_mfma_f32_16x16x32_bf16 v[88:91], v[128:131], v[100:103], 0\n\t"
    "v_mfma_f32_16x16x32_bf16 v[92:95], v[132:135], v[100:103], 0\n\t"
    "s_waitcnt vmcnt(12)\n\t"
    "s_cmp_lt_u32 %[x], 2\n\t"
    "s_cbranch_scc0 41f\n\t"
    "s_waitcnt vmcnt(4)\n\t"
    "41:\n\t"
    "s_barrier\n\t"
    "s_waitcnt lgkmcnt(4)\n\t"
    "v_mfma_f32_16x16x32_bf16 v[80:83], v[136:139], v[104:107], v[80:83]\n\t"
    "ds_read_b128 v[96:99], %[ea] offset:8192\n\t"
    "v_mfma_f32_16x16x32_bf16 v[84:87], v[140:143], v[104:107], v[84:87]\n\t"
    "ds_read_b128 v[100:103], %[ea] offset:10240\n\t"
    "v_mfma_f32_16x16x32_bf16 v[88:91], v[136:139], v[108:111], v[88:91]\n\t"
    "v_mfma_f32_16x16x32_bf16 v[92:95], v[140:143], v[108:111], v[92:95]\n\t"
    "s_waitcnt lgkmcnt(4)\n\t"
    "v_mfma_f32_16x16x32_bf16 v[80:83], v[144:147], v[112:115], v[80:83]\n\t"
    "ds_read_b128 v[104:107], %[da] offset:8192\n\t"
    "v_mfma_f32_16x16x32_bf16 v[84:87], v[148:151], v[112:115], v[84:87]\n\t"
    "ds_read_b128 v[108:111], %[da] offset:10240\n\t"
    "v_mfma_f32_16x16x32_bf16 v[88:91], v[144:147], v[116:119], v[88:91]\n\t"
    "v_mfma_f32_16x16x32_bf16 v[92:95], v[148:151], v[116:119], v[92:95]\n\t"
    "s_waitcnt lgkmcnt(4)\n\t"
    "v_mfma_f32_16x16x32_bf16 v[80:83], v[152:155], v[120:123], v[80:83]\n\t"
    "ds_read_b128 v[112:115], %[ea] offset:12288\n\t"
    "v_mfma_f32_16x16x32_bf16 v[84:87], v[156:159], v[120:123], v[84:87]\n\t"
    "ds_read_b128 v[116:119], %[ea] offset:14336\n\t"
    "v_mfma_f32_16x16x32_bf16 v[88:91], v[152:155], v[124:127], v[88:91]\n\t"
    "v_mfma_f32_16x16x32_bf16 v[92:95], v[156:159], v[124:127], v[92:95]\n\t"
    "s_waitcnt lgkmcnt(4)\n\t"
    "v_mfma_f32_16x16x32_bf16 v[80:83], v[160:163], v[96:99], v[80:83]\n\t"
    "ds_read_b128 v[120:123], %[da] offset:12288\n\t"
    "v_mfma_f32_16x16x32_bf16 v[84:87], v[164:167], v[96:99], v[84:87]\n\t"
    "ds_read_b128 v[124:127], %[da] offset:14336\n\t"
    "v_mfma_f32_16x16x32_bf16 v[88:91], v[160:163], v[100:103], v[88:91]\n\t"
    "v_mfma_f32_16x16x32_bf16 v[92:95], v[164:167], v[100:103], v[92:95]\n\t"
    "s_waitcnt vmcnt(10)\n\t"
    "s_cmp_lt_u32 %[x], 2\n\t"
    "s_cbranch_scc0 45f\n\t"
    "s_waitcnt vmcnt(2)\n\t"
    "45:\n\t"
    "s_barrier\n\t"
    "s_waitcnt lgkmcnt(4)\n\t"
    "v_mfma_f32_16x16x32_bf16 v[80:83], v[168:171], v[104:107], v[80:83]\n\t"
    "ds_read_b128 v[96:99], %[ea] offset:16384\n\t"
    "v_mfma_f32_16x16x32_bf16 v[84:87], v[172:175], v[104:107], v[84:87]\n\t"
    "ds_read_b128 v[100:103], %[ea] offset:18432\n\t"
    "v_mfma_f32_16x16x32_bf16 v[88:91], v[168:171], v[108:111], v[88:91]\n\t"
    "v_mfma_f32_16x16x32_bf16 v[92:95], v[172:175], v[108:111], v[92:95]\n\t"
    "s_waitcnt lgkmcnt(4)\n\t"
    "v_mfma_f32_16x16x32_bf16 v[80:83], v[176:179], v[112:115], v[80:83]\n\t"
    "ds_read_b128 v[104:107], %[da] offset:16384\n\t"
    "v_mfma_f32_16x16x32_bf16 v[84:87], v[180:183], v[112:115], v[84:87]\n\t"
    "ds_read_b128 v[108:111], %[da] offset:18432\n\t"
    "v_mfma_f32_16x16x32_bf16 v[88:91], v[176:179], v[116:119], v[88:91]\n\t"
    "v_mfma_f32_16x16x32_bf16 v[92:95], v[180:183], v[116:119], v[92:95]\n\t"
    "s_waitcnt lgkmcnt(4)\n\t"
    "v_mfma_f32_16x16x32_bf16 v[80:83], v[184:187], v[120:123], v[80:83]\n\t"
    "ds_read_b128 v[112:115], %[ea] offset:20480\n\t"
    "v_mfma_f32_16x16x32_bf16 v[84:87], v[188:191], v[120:123], v[84:87]\n\t"
    "ds_read_b128 v[116:119], %[ea] offset:22528\n\t"
    "v_mfma_f32_16x16x32_bf16 v[88:91], v[184:187], v[124:127], v[88:91]\n\t"
    "v_mfma_f32_16x16x32_bf16 v[92:95], v[188:191], v[124:127], v[92:95]\n\t"
    "s_waitcnt lgkmcnt(4)\n\t"
    "v_mfma_f32_16x16x32_bf16 v[80:83], v[192:195], v[96:99], v[80:83]\n\t"
    "ds_read_b128 v[120:123], %[da] offset:20480\n\t"
    "v_mfma_f32_16x16x32_bf16 v[84:87], v[196:199], v[96:99], v[84:87]\n\t"
    "ds_read_b128 v[124:127], %[da] offset:22528\n\t"
    "v_mfma_f32_16x16x32_bf16 v[88:91], v[192:195], v[100:103], v[88:91]\n\t"
    "v_mfma_f32_16x16x32_bf16 v[92:95], v[196:199], v[100:103], v[92:95]\n\t"
    "s_waitcnt vmcnt(8)\n\t"
    "s_cmp_lt_u32 %[x], 2\n\t"
    "s_cbranch_scc0 49f\n\t"
    "s_waitcnt vmcnt(0)\n\t"
    "49:\n\t"
    "s_barrier\n\t"
    "ds_read_b32 v79, %[sa]\n\t"
    "s_waitcnt lgkmcnt(5)\n\t"
    "v_mfma_f32_16x16x32_bf16 v[80:83], v[200:203], v[104:107], v[80:83]\n\t"
    "ds_read_b128 v[96:99], %[ea] offset:24576\n\t"
    "v_mfma_f32_16x16x32_bf16 v[84:87], v[204:207], v[104:107], v[84:87]\n\t"
    "ds_read_b128 v[100:103], %[ea] offset:26624\n\t"
    "v_mfma_f32_16x16x32_bf16 v[88:91], v[200:203], v[108:111], v[88:91]\n\t"
    "v_mfma_f32_16x16x32_bf16 v[92:95], v[204:207], v[108:111], v[92:95]\n\t"
    "s_waitcnt lgkmcnt(5)\n\t"
    "v_mfma_f32_16x16x32_bf16 v[80:83], v[208:211], v[112:115], v[80:83]\n\t"
    "ds_read_b128 v[104:107], %[da] offset:24576\n\t"
    "v_mfma_f32_16x16x32_bf16 v[84:87], v[212:215], v[112:115], v[84:87]\n\t"
    "ds_read_b128 v[108:111], %[da] offset:26624\n\t"
    "v_mfma_f32_16x16x32_bf16 v[88:91], v[208:211], v[116:119], v[88:91]\n\t"
    "v_mfma_f32_16x16x32_bf16 v[92:95], v[212:215], v[116:119], v[92:95]\n\t"
    "s_cmp_eq_u32 %[s2], 0\n\t"
    "s_cbranch_scc1 7f\n\t"
    "s_mov_b32 m0, %[s2]\n\t"
    "s_nop 0\n\t"
    "global_load_lds_dword %[dv], %[db] sc1\n\t"
    "7:\n\t"
    "s_waitcnt lgkmcnt(4)\n\t"
    "v_mfma_f32_16x16x32_bf16 v[80:83], v[216:219], v[120:123], v[80:83]\n\t"
    "ds_read_b128 v[112:115], %[ea] offset:28672\n\t"
    "v_mfma_f32_16x16x32_bf16 v[84:87], v[220:223], v[120:123], v[84:87]\n\t"
    "ds_read_b128 v[116:119], %[ea] offset:30720\n\t"
    "v_mfma_f32_16x16x32_bf16 v[88:91], v[216:219], v[124:127], v[88:91]\n\t"
    "v_mad_u32_u24 v79, v79, %[mu], %[ad]\n\t"
    "v_cmp_ge_i32 vcc, %[th], v79\n\t"
    "v_mfma_f32_16x16x32_bf16 v[92:95], v[220:223], v[124:127], v[92:95]\n\t"
    "s_cbranch_vccz 5f\n\t"
    "s_waitcnt lgkmcnt(4)\n\t"
    "v_mfma_f32_16x16x32_bf16 v[80:83], v[224:227], v[96:99], v[80:83]\n\t"
    "ds_read_b128 v[120:123], %[da] offset:28672\n\t"
    "v_mfma_f32_16x16x32_bf16 v[84:87], v[228:231], v[96:99], v[84:87]\n\t"
    "ds_read_b128 v[124:127], %[da] offset:30720\n\t"
    "v_mfma_f32_16x16x32_bf16 v[88:91], v[224:227], v[100:103], v[88:91]\n\t"
    "v_mfma_f32_16x16x32_bf16 v[92:95], v[228:231], v[100:103], v[92:95]\n\t"
    "s_waitcnt lgkmcnt(4)\n\t"
    "v_mfma_f32_16x16x32_bf16 v[80:83], v[232:235], v[104:107], v[80:83]\n\t"
    "v_mfma_f32_16x16x32_bf16 v[84:87], v[236:239], v[104:107], v[84:87]\n\t"
    "v_mfma_f32_16x16x32_bf16 v[88:91], v[232:235], v[108:111], v[88:91]\n\t"
    "v_mfma_f32_16x16x32_bf16 v[92:95], v[236:239], v[108:111], v[92:95]\n\t"
    "s_waitcnt lgkmcnt(2)\n\t"
    "v_mfma_f32_16x16x32_bf16 v[80:83], v[240:243], v[112:115], v[80:83]\n\t"
    "v_mfma_f32_16x16x32_bf16 v[84:87], v[244:247], v[112:115], v[84:87]\n\t"
    "v_mfma_f32_16x16x32_bf16 v[88:91], v[240:243], v[116:119], v[88:91]\n\t"
    "v_mfma_f32_16x16x32_bf16 v[92:95], v[244:247], v[116:119], v[92:95]\n\t"
    "s_waitcnt lgkmcnt(0)\n\t"
    "v_mfma_f32_16x16x32_bf16 v[80:83], v[248:251], v[120:123], v[80:83]\n\t"
    "v_mfma_f32_16x16x32_bf16 v[84:87], v[252:255], v[120:123], v[84:87]\n\t"
    "v_mfma_f32_16x16x32_bf16 v[88:91], v[248:251], v[124:127], v[88:91]\n\t"
    "v_mfma_f32_16x16x32_bf16 v[92:95], v[252:255], v[124:127], v[92:95]\n\t"
    "s_branch 6f\n\t"
    "5:\n\t"
    "s_waitcnt lgkmcnt(4)\n\t"
    "v_mfma_f32_16x16x32_bf16 v[80:83], v[224:227], v[96:99], v[80:83]\n\t"
    "ds_read_b128 v[120:123], %[da] offset:28672\n\t"
    "s_add_u32 m0, %[lb], 0\n\t"
    "s_nop 0\n\t"
    "global_load_lds_dwordx4 %[av0], %[s0] sc1\n\t"
    "v_mfma_f32_16x16x32_bf16 v[84:87], v[228:231], v[96:99], v[84:87]\n\t"
    "ds_read_b128 v[124:127], %[da] offset:30720\n\t"
    "v_mfma_f32_16x16x32_bf16 v[88:91], v[224:227], v[100:103], v[88:91]\n\t"
    "s_add_u32 m0, %[lb], 4096\n\t"
    "s_nop 0\n\t"
    "global_load_lds_dwordx4 %[av2], %[s0] sc1\n\t"
    "v_mfma_f32_16x16x32_bf16 v[92:95], v[228:231], v[100:103], v[92:95]\n\t"
    "s_waitcnt lgkmcnt(4)\n\t"
    "v_mfma_f32_16x16x32_bf16 v[80:83], v[232:235], v[104:107], v[80:83]\n\t"
    "s_add_u32 m0, %[lb], 8192\n\t"
    "s_nop 0\n\t"
    "global_load_lds_dwordx4 %[av4], %[s0] sc1\n\t"
    "v_mfma_f32_16x16x32_bf16 v[84:87], v[236:239], v[104:107], v[84:87]\n\t"
    "v_mfma_f32_16x16x32_bf16 v[88:91], v[232:235], v[108:111], v[88:91]\n\t"
    "s_add_u32 m0, %[lb], 12288\n\t"
    "s_nop 0\n\t"
    "global_load_lds_dwordx4 %[av6], %[s0] sc1\n\t"
    "v_mfma_f32_16x16x32_bf16 v[92:95], v[236:239], v[108:111], v[92:95]\n\t"
    "s_waitcnt lgkmcnt(2)\n\t"
    "v_mfma_f32_16x16x32_bf16 v[80:83], v[240:243], v[112:115], v[80:83]\n\t"
    "s_add_u32 m0, %[lb], 16384\n\t"
    "s_nop 0\n\t"
    "global_load_lds_dwordx4 %[av0], %[s1] sc1\n\t"
    "v_mfma_f32_16x16x32_bf16 v[84:87], v[244:247], v[112:115], v[84:87]\n\t"
    "v_mfma_f32_16x16x32_bf16 v[88:91], v[240:243], v[116:119], v[88:91]\n\t"
    "s_add_u32 m0, %[lb], 20480\n\t"
    "s_nop 0\n\t"
    "global_load_lds_dwordx4 %[av2], %[s1] sc1\n\t"
    "v_mfma_f32_16x16x32_bf16 v[92:95], v[244:247], v[116:119], v[92:95]\n\t"
    "s_waitcnt lgkmcnt(0)\n\t"
    "v_mfma_f32_16x16x32_bf16 v[80:83], v[248:251], v[120:123], v[80:83]\n\t"
    "s_add_u32 m0, %[lb], 24576\n\t"
    "s_nop 0\n\t"
    "global_load_lds_dwordx4 %[av4], %[s1] sc1\n\t"
    "v_mfma_f32_16x16x32_bf16 v[84:87], v[252:255], v[120:123], v[84:87]\n\t"
    "v_mfma_f32_16x16x32_bf16 v[88:91], v[248:251], v[124:127], v[88:91]\n\t"
    "s_add_u32 m0, %[lb], 28672\n\t"
    "s_nop 0\n\t"
    "global_load_lds_dwordx4 %[av6], %[s1] sc1\n\t"
    "v_mfma_f32_16x16x32_bf16 v[92:95], v[252:255], v[124:127], v[92:95]\n\t"
    "6:\n\t"
    "s_nop 6\n\t"
    "s_branch 9f\n\t"
    "3:\n\t"
    "9:"
    : "=&{v[80:95]}"(pd)
    : [ea] "v"(ea), [da] "v"(da), [x] "s"(x), "{v[128:159]}"(W[0]), "{v[160:191]}"(W[1]), "{v[192:223]}"(W[2]), "{v[224:255]}"(W[3]), [lb] "s"(lb), [s0] "s"(s0), [s1] "s"(s1), [av0] "v"(av[0]), [av1] "v"(av[1]), [av2] "v"(av[2]), [av3] "v"(av[3]), [av4] "v"(av[4]), [av5] "v"(av[5]), [av6] "v"(av[6]), [av7] "v"(av[7]), [sa] "v"(sa), [mu] "s"(mu), [ad] "v"(ad), [th] "s"(th), [s2] "s"(s2), [dv] "v"(dv), [db] "s"(db)
    : "v96", "v97", "v98", "v99", "v100", "v101", "v102", "v103", "v104", "v105", "v106", "v107", "v108", "v109", "v110", "v111", "v112", "v113", "v114", "v115", "v116", "v117", "v118", "v119", "v120", "v121", "v122", "v123", "v124", "v125", "v126", "v127", "v79", "vcc", "scc", "m0", "memory");
}
__device__ __forceinline__ void lean_dm1(const u32x32 (&W)[4], u32x16& pd, unsigned ea, unsigned da, unsigned x, unsigned lb, const char* s0, const char* s1, const unsigned (&av)[8]) {
  asm volatile("s_setprio 2\n\t"
    "ds_read_b128 v[96:99], %[ea] offset:0\n\t"
    "ds_read_b128 v[100:103], %[ea] offset:2048\n\t"
    "ds_read_b128 v[104:107], %[da] offset:0\n\t"
    "ds_read_b128 v[108:111], %[da] offset:2048\n\t"
    "ds_read_b128 v[112:115], %[ea] offset:4096\n\t"
    "ds_read_b128 v[116:119], %[ea] offset:6144\n\t"
    "s_waitcnt lgkmcnt(4)\n\t"
    "v_mfma_f32_16x16x32_bf16 v[80:83], v[128:131], v[96:99], 0\n\t"
    "ds_read_b128 v[120:123], %[da] offset:4096\n\t"
    "s_add_u32 m0, %[lb], 0\n\t"
    "v_mfma_f32_16x16x32_bf16 v[84:87], v[132:135], v[96:99], 0\n\t"
    "ds_read_b128 v[124:127], %[da] offset:6144\n\t"
    "global_load_lds_dwordx4 %[av0], %[s0] sc1\n\t"
    "v_mfma_f32_16x16x32_bf16 v[88:91], v[128:131], v[100:103], 0\n\t"
    "v_mfma_f32_16x16x32_bf16 v[92:95], v[132:135], v[100:103], 0\n\t"
    "s_waitcnt vmcnt(5)\n\t"
    "s_barrier\n\t"
    "s_waitcnt lgkmcnt(4)\n\t"
    "v_mfma_f32_16x16x32_bf16 v[80:83], v[136:139], v[104:107], v[80:83]\n\t"
    "ds_read_b128 v[96:99], %[ea] offset:8192\n\t"
    "s_add_u32 m0, %[lb], 4096\n\t"
    "v_mfma_f32_16x16x32_bf16 v[84:87], v[140:143], v[104:107], v[84:87]\n\t"
    "ds_read_b128 v[100:103], %[ea] offset:10240\n\t"
    "global_load_lds_dwordx4 %[av2], %[s0] sc1\n\t"
    "v_mfma_f32_16x16x32_bf16 v[88:91], v[136:139], v[108:111], v[88:91]\n\t"
    "v_mfma_f32_16x16x32_bf16 v[92:95], v[140:143], v[108:111], v[92:95]\n\t"
    "s_waitcnt lgkmcnt(4)\n\t"
    "v_mfma_f32_16x16x32_bf16 v[80:83], v[144:147], v[112:115], v[80:83]\n\t"
    "ds_read_b128 v[104:107], %[da] offset:8192\n\t"
    "s_add_u32 m0, %[lb], 8192\n\t"
    "v_mfma_f32_16x16x32_bf16 v[84:87], v[148:151], v[112:115], v[84:87]\n\t"
    "ds_read_b128 v[108:111], %[da] offset:10240\n\t"
    "global_load_lds_dwordx4 %[av4], %[s0] sc1\n\t"
    "v_mfma_f32_16x16x32_bf16 v[88:91], v[144:147], v[116:119], v[88:91]\n\t"
    "v_mfma_f32_16x16x32_bf16 v[92:95], v[148:151], v[116:119], v[92:95]\n\t"
    "s_waitcnt lgkmcnt(4)\n\t"
    "v_mfma_f32_16x16x32_bf16 v[80:83], v[152:155], v[120:123], v[80:83]\n\t"
    "ds_read_b128 v[112:115], %[ea] offset:12288\n\t"
    "s_add_u32 m0, %[lb], 12288\n\t"
    "v_mfma_f32_16x16x32_bf16 v[84:87], v[156:159], v[120:123], v[84:87]\n\t"
    "ds_read_b128 v[116:119], %[ea] offset:14336\n\t"
    "global_load_lds_dwordx4 %[av6], %[s0] sc1\n\t"
    "v_mfma_f32_16x16x32_bf16 v[88:91], v[152:155], v[124:127], v[88:91]\n\t"
    "v_mfma_f32_16x16x32_bf16 v[92:95], v[156:159], v[124:127], v[92:95]\n\t"
    "s_waitcnt lgkmcnt(4)\n\t"
    "v_mfma_f32_16x16x32_bf16 v[80:83], v[160:163], v[96:99], v[80:83]\n\t"
    "ds_read_b128 v[120:123], %[da] offset:12288\n\t"
    "s_add_u32 m0, %[lb], 16384\n\t"
    "v_mfma_f32_16x16x32_bf16 v[84:87], v[164:167], v[96:99], v[84:87]\n\t"
    "ds_read_b128 v[124:127], %[da] offset:14336\n\t"
    "global_load_lds_dwordx4 %[av0], %[s1] sc1\n\t"
    "v_mfma_f32_16x16x32_bf16 v[88:91], v[160:163], v[100:103], v[88:91]\n\t"
    "v_mfma_f32_16x16x32_bf16 v[92:95], v[164:167], v[100:103], v[92:95]\n\t"
    "s_waitcnt vmcnt(7)\n\t"
    "s_barrier\n\t"
    "s_waitcnt lgkmcnt(4)\n\t"
    "v_mfma_f32_16x16x32_bf16 v[80:83], v[168:171], v[104:107], v[80:83]\n\t"
    "ds_read_b128 v[96:99], %[ea] offset:16384\n\t"
    "s_add_u32 m0, %[lb], 20480\n\t"
    "v_mfma_f32_16x16x32_bf16 v[84:87], v[172:175], v[104:107], v[84:87]\n\t"
    "ds_read_b128 v[100:103], %[ea] offset:18432\n\t"
    "global_load_lds_dwordx4 %[av2], %[s1] sc1\n\t"
    "v_mfma_f32_16x16x32_bf16 v[88:91], v[168:171], v[108:111], v[88:91]\n\t"
    "v_mfma_f32_16x16x32_bf16 v[92:95], v[172:175], v[108:111], v[92:95]\n\t"
    "s_waitcnt lgkmcnt(4)\n\t"
    "v_mfma_f32_16x16x32_bf16 v[80:83], v[176:179], v[112:115], v[80:83]\n\t"
    "ds_read_b128 v[104:107], %[da] offset:16384\n\t"
    "s_add_u32 m0, %[lb], 24576\n\t"
    "v_mfma_f32_16x16x32_bf16 v[84:87], v[180:183], v[112:115], v[84:87]\n\t"
    "ds_read_b128 v[108:111], %[da] offset:18432\n\t"
    "global_load_lds_dwordx4 %[av4], %[s1] sc1\n\t"
    "v_mfma_f32_16x16x32_bf16 v[88:91], v[176:179], v[116:119], v[88:91]\n\t"
    "v_mfma_f32_16x16x32_bf16 v[92:95], v[180:183], v[116:119], v[92:95]\n\t"
    "s_waitcnt lgkmcnt(4)\n\t"
    "v_mfma_f32_16x16x32_bf16 v[80:83], v[184:187], v[120:123], v[80:83]\n\t"
    "ds_read_b128 v[112:115], %[ea] offset:20480\n\t"
    "s_add_u32 m0, %[lb], 28672\n\t"
    "v_mfma_f32_16x16x32_bf16 v[84:87], v[188:191], v[120:123], v[84:87]\n\t"
    "ds_read_b128 v[116:119], %[ea] offset:22528\n\t"
    "global_load_lds_dwordx4 %[av6], %[s1] sc1\n\t"
    "v_mfma_f32_16x16x32_bf16 v[88:91], v[184:187], v[124:127], v[88:91]\n\t"
    "v_mfma_f32_16x16x32_bf16 v[92:95], v[188:191], v[124:127], v[92:95]\n\t"
    "s_waitcnt lgkmcnt(4)\n\t"
    "v_mfma_f32_16x16x32_bf16 v[80:83], v[192:195], v[96:99], v[80:83]\n\t"
    "ds_read_b128 v[120:123], %[da] offset:20480\n\t"
    "v_mfma_f32_16x16x32_bf16 v[84:87], v[196:199], v[96:99], v[84:87]\n\t"
    "ds_read_b128 v[124:127], %[da] offset:22528\n\t"
    "v_mfma_f32_16x16x32_bf16 v[88:91], v[192:195], v[100:103], v[88:91]\n\t"
    "v_mfma_f32_16x16x32_bf16 v[92:95], v[196:199], v[100:103], v[92:95]\n\t"
    "s_waitcnt vmcnt(8)\n\t"
    "s_barrier\n\t"
    "s_waitcnt lgkmcnt(4)\n\t"
    "v_mfma_f32_16x16x32_bf16 v[80:83], v[200:203], v[104:107], v[80:83]\n\t"
    "ds_read_b128 v[96:99], %[ea] offset:24576\n\t"
    "v_mfma_f32_16x16x32_bf16 v[84:87], v[204:207], v[104:107], v[84:87]\n\t"
    "ds_read_b128 v[100:103], %[ea] offset:26624\n\t"
    "v_mfma_f32_16x16x32_bf16 v[88:91], v[200:203], v[108:111], v[88:91]\n\t"
    "v_mfma_f32_16x16x32_bf16 v[92:95], v[204:207], v[108:111], v[92:95]\n\t"
    "s_waitcnt lgkmcnt(4)\n\t"
    "v_mfma_f32_16x16x32_bf16 v[80:83], v[208:211], v[112:115], v[80:83]\n\t"
    "ds_read_b128 v[104:107], %[da] offset:24576\n\t"
    "v_mfma_f32_16x16x32_bf16 v[84:87], v[212:215], v[112:115], v[84:87]\n\t"
    "ds_read_b128 v[108:111], %[da] offset:26624\n\t"
    "v_mfma_f32_16x16x32_bf16 v[88:91], v[208:211], v[116:119], v[88:91]\n\t"
    "v_mfma_f32_16x16x32_bf16 v[92:95], v[212:215], v[116:119], v[92:95]\n\t"
    "s_waitcnt lgkmcnt(4)\n\t"
    "v_mfma_f32_16x16x32_bf16 v[80:83], v[216:219], v[120:123], v[80:83]\n\t"
    "ds_read_b128 v[112:115], %[ea] offset:28672\n\t"
    "v_mfma_f32_16x16x32_bf16 v[84:87], v[220:223], v[120:123], v[84:87]\n\t"
    "ds_read_b128 v[116:119], %[ea] offset:30720\n\t"
    "v_mfma_f32_16x16x32_bf16 v[88:91], v[216:219], v[124:127], v[88:91]\n\t"
    "v_mfma_f32_16x16x32_bf16 v[92:95], v[220:223], v[124:127], v[92:95]\n\t"
    "s_waitcnt lgkmcnt(4)\n\t"
    "v_mfma_f32_16x16x32_bf16 v[80:83], v[224:227], v[96:99], v[80:83]\n\t"
    "ds_read_b128 v[120:123], %[da] offset:28672\n\t"
    "v_mfma_f32_16x16x32_bf16 v[84:87], v[228:231], v[96:99], v[84:87]\n\t"
    "ds_read_b128 v[124:127], %[da] offset:30720\n\t"
    "v_mfma_f32_16x16x32_bf16 v[88:91], v[224:227], v[100:103], v[88:91]\n\t"
    "v_mfma_f32_16x16x32_bf16 v[92:95], v[228:231], v[100:103], v[92:95]\n\t"
    "s_waitcnt lgkmcnt(4)\n\t"
    "v_mfma_f32_16x16x32_bf16 v[80:83], v[232:235], v[104:107], v[80:83]\n\t"
    "v_mfma_f32_16x16x32_bf16 v[84:87], v[236:239], v[104:107], v[84:87]\n\t"
    "v_mfma_f32_16x16x32_bf16 v[88:91], v[232:235], v[108:111], v[88:91]\n\t"
    "v_mfma_f32_16x16x32_bf16 v[92:95], v[236:239], v[108:111], v[92:95]\n\t"
    "s_waitcnt lgkmcnt(2)\n\t"
    "v_mfma_f32_16x16x32_bf16 v[80:83], v[240:243], v[112:115], v[80:83]\n\t"
    "v_mfma_f32_16x16x32_bf16 v[84:87], v[244:247], v[112:115], v[84:87]\n\t"
    "v_mfma_f32_16x16x32_bf16 v[88:91], v[240:243], v[116:119], v[88:91]\n\t"
    "v_mfma_f32_16x16x32_bf16 v[92:95], v[244:247], v[116:119], v[92:95]\n\t"
    "s_waitcnt lgkmcnt(0)\n\t"
    "v_mfma_f32_16x16x32_bf16 v[80:83], v[248:251], v[120:123], v[80:83]\n\t"
    "v_mfma_f32_16x16x32_bf16 v[84:87], v[252:255], v[120:123], v[84:87]\n\t"
    "v_mfma_f32_16x16x32_bf16 v[88:91], v[248:251], v[124:127], v[88:91]\n\t"
    "v_mfma_f32_16x16x32_bf16 v[92:95], v[252:255], v[124:127], v[92:95]\n\t"
    "s_nop 6\n\t"
    "s_branch 9f\n\t"
    "3:\n\t"
    "9:"
    : "=&{v[80:95]}"(pd)
    : [ea] "v"(ea), [da] "v"(da), [x] "s"(x), "{v[128:159]}"(W[0]), "{v[160:191]}"(W[1]), "{v[192:223]}"(W[2]), "{v[224:255]}"(W[3]), [lb] "s"(lb), [s0] "s"(s0), [s1] "s"(s1), [av0] "v"(av[0]), [av1] "v"(av[1]), [av2] "v"(av[2]), [av3] "v"(av[3]), [av4] "v"(av[4]), [av5] "v"(av[5]), [av6] "v"(av[6]), [av7] "v"(av[7])
    : "v96", "v97", "v98", "v99", "v100", "v101", "v102", "v103", "v104", "v105", "v106", "v107", "v108", "v109", "v110", "v111", "v112", "v113", "v114", "v115", "v116", "v117", "v118", "v119", "v120", "v121", "v122", "v123", "v124", "v125", "v126", "v127", "vcc", "scc", "m0", "memory");
}
__device__ __forceinline__ void lean_st0(const u32x32 (&W)[4], u32x16& pd, unsigned ea, unsigned da, unsigned x, unsigned pv, unsigned cn, unsigned sl, unsigned dv, const char* db, const char* pg) {
  unsigned st;
  asm volatile("ds_read_b128 v[96:99], %[ea] offset:0\n\t"
    "ds_read_b128 v[100:103], %[ea] offset:2048\n\t"
    "ds_read_b128 v[104:107], %[da] offset:0\n\t"
    "ds_read_b128 v[108:111], %[da] offset:2048\n\t"
    "ds_read_b128 v[112:115], %[ea] offset:4096\n\t"
    "ds_read_b128 v[116:119], %[ea] offset:6144\n\t"
    "s_waitcnt lgkmcnt(4)\n\t"
    "v_mfma_f32_16x16x32_bf16 v[80:83], v[128:131], v[96:99], 0\n\t"
    "ds_read_b128 v[120:123], %[da] offset:4096\n\t"
    "v_mfma_f32_16x16x32_bf16 v[84:87], v[132:135], v[96:99], 0\n\t"
    "ds_read_b128 v[124:127], %[da] offset:6144\n\t"
    "v_mfma_f32_16x16x32_bf16 v[88:91], v[128:131], v[100:103], 0\n\t"
    "v_mfma_f32_16x16x32_bf16 v[92:95], v[132:135], v[100:103], 0\n\t"
    "s_barrier\n\t"
    "s_waitcnt lgkmcnt(4)\n\t"
    "v_mfma_f32_16x16x32_bf16 v[80:83], v[136:139], v[104:107], v[80:83]\n\t"
    "ds_read_b128 v[96:99], %[ea] offset:8192\n\t"
    "v_mfma_f32_16x16x32_bf16 v[84:87], v[140:143], v[104:107], v[84:87]\n\t"
    "ds_read_b128 v[100:103], %[ea] offset:10240\n\t"
    "v_mfma_f32_16x16x32_bf16 v[88:91], v[136:139], v[108:111], v[88:91]\n\t"
    "v_mfma_f32_16x16x32_bf16 v[92:95], v[140:143], v[108:111], v[92:95]\n\t"
    "s_waitcnt lgkmcnt(4)\n\t"
    "v_mfma_f32_16x16x32_bf16 v[80:83], v[144:147], v[112:115], v[80:83]\n\t"
    "ds_read_b128 v[104:107], %[da] offset:8192\n\t"
    "v_mfma_f32_16x16x32_bf16 v[84:87], v[148:151], v[112:115], v[84:87]\n\t"
    "ds_read_b128 v[108:111], %[da] offset:10240\n\t"
    "v_mfma_f32_16x16x32_bf16 v[88:91], v[144:147], v[116:119], v[88:91]\n\t"
    "v_mfma_f32_16x16x32_bf16 v[92:95], v[148:151], v[116:119], v[92:95]\n\t"
    "s_waitcnt lgkmcnt(4)\n\t"
    "v_mfma_f32_16x16x32_bf16 v[80:83], v[152:155], v[120:123], v[80:83]\n\t"
    "ds_read_b128 v[112:115], %[ea] offset:12288\n\t"
    "v_mfma_f32_16x16x32_bf16 v[84:87], v[156:159], v[120:123], v[84:87]\n\t"
    "ds_read_b128 v[116:119], %[ea] offset:14336\n\t"
    "v_mfma_f32_16x16x32_bf16 v[88:91], v[152:155], v[124:127], v[88:91]\n\t"
    "v_mfma_f32_16x16x32_bf16 v[92:95], v[156:159], v[124:127], v[92:95]\n\t"
    "s_bitcmp1_b32 %[x], 2\n\t"
    "s_cbranch_scc1 7f\n\t"
    "8:\n\t"
    "s_waitcnt lgkmcnt(4)\n\t"
    "v_mfma_f32_16x16x32_bf16 v[80:83], v[160:163], v[96:99], v[80:83]\n\t"
    "ds_read_b128 v[120:123], %[da] offset:12288\n\t"
    "v_mfma_f32_16x16x32_bf16 v[84:87], v[164:167], v[96:99], v[84:87]\n\t"
    "ds_read_b128 v[124:127], %[da] offset:14336\n\t"
    "v_mfma_f32_16x16x32_bf16 v[88:91], v[160:163], v[100:103], v[88:91]\n\t"
    "v_mfma_f32_16x16x32_bf16 v[92:95], v[164:167], v[100:103], v[92:95]\n\t"
    "s_barrier\n\t"
    "s_waitcnt lgkmcnt(4)\n\t"
    "v_mfma_f32_16x16x32_bf16 v[80:83], v[168:171], v[104:107], v[80:83]\n\t"
    "ds_read_b128 v[96:99], %[ea] offset:16384\n\t"
    "v_mfma_f32_16x16x32_bf16 v[84:87], v[172:175], v[104:107], v[84:87]\n\t"
    "ds_read_b128 v[100:103], %[ea] offset:18432\n\t"
    "v_mfma_f32_16x16x32_bf16 v[88:91], v[168:171], v[108:111], v[88:91]\n\t"
    "v_mfma_f32_16x16x32_bf16 v[92:95], v[172:175], v[108:111], v[92:95]\n\t"
    "s_waitcnt lgkmcnt(4)\n\t"
    "v_mfma_f32_16x16x32_bf16 v[80:83], v[176:179], v[112:115], v[80:83]\n\t"
    "ds_read_b128 v[104:107], %[da] offset:16384\n\t"
    "v_mfma_f32_16x16x32_bf16 v[84:87], v[180:183], v[112:115], v[84:87]\n\t"
    "ds_read_b128 v[108:111], %[da] offset:18432\n\t"
    "v_mfma_f32_16x16x32_bf16 v[88:91], v[176:179], v[116:119], v[88:91]\n\t"
    "v_mfma_f32_16x16x32_bf16 v[92:95], v[180:183], v[116:119], v[92:95]\n\t"
    "s_waitcnt lgkmcnt(4)\n\t"
    "v_mfma_f32_16x16x32_bf16 v[80:83], v[184:187], v[120:123], v[80:83]\n\t"
    "ds_read_b128 v[112:115], %[ea] offset:20480\n\t"
    "v_mfma_f32_16x16x32_bf16 v[84:87], v[188:191], v[120:123], v[84:87]\n\t"
    "ds_read_b128 v[116:119], %[ea] offset:22528\n\t"
    "v_mfma_f32_16x16x32_bf16 v[88:91], v[184:187], v[124:127], v[88:91]\n\t"
    "v_mfma_f32_16x16x32_bf16 v[92:95], v[188:191], v[124:127], v[92:95]\n\t"
    "s_waitcnt lgkmcnt(4)\n\t"
    "v_mfma_f32_16x16x32_bf16 v[80:83], v[192:195], v[96:99], v[80:83]\n\t"
    "ds_read_b128 v[120:123], %[da] offset:20480\n\t"
    "v_mfma_f32_16x16x32_bf16 v[84:87], v[196:199], v[96:99], v[84:87]\n\t"
    "ds_read_b128 v[124:127], %[da] offset:22528\n\t"
    "v_mfma_f32_16x16x32_bf16 v[88:91], v[192:195], v[100:103], v[88:91]\n\t"
    "v_mfma_f32_16x16x32_bf16 v[92:95], v[196:199], v[100:103], v[92:95]\n\t"
    "s_barrier\n\t"
    "s_and_b32 %[st], %[x], 3\n\t"
    "s_cbranch_scc1 3f\n\t"
    "4:\n\t"
    "s_waitcnt lgkmcnt(4)\n\t"
    "v_mfma_f32_16x16x32_bf16 v[80:83], v[200:203], v[104:107], v[80:83]\n\t"
    "ds_read_b128 v[96:99], %[ea] offset:24576\n\t"
    "v_mfma_f32_16x16x32_bf16 v[84:87], v[204:207], v[104:107], v[84:87]\n\t"
    "ds_read_b128 v[100:103], %[ea] offset:26624\n\t"
    "v_mfma_f32_16x16x32_bf16 v[88:91], v[200:203], v[108:111], v[88:91]\n\t"
    "v_mfma_f32_16x16x32_bf16 v[92:95], v[204:207], v[108:111], v[92:95]\n\t"
    "s_waitcnt lgkmcnt(4)\n\t"
    "v_mfma_f32_16x16x32_bf16 v[80:83], v[208:211], v[112:115], v[80:83]\n\t"
    "ds_read_b128 v[104:107], %[da] offset:24576\n\t"
    "v_mfma_f32_16x16x32_bf16 v[84:87], v[212:215], v[112:115], v[84:87]\n\t"
    "ds_read_b128 v[108:111], %[da] offset:26624\n\t"
    "v_mfma_f32_16x16x32_bf16 v[88:91], v[208:211], v[116:119], v[88:91]\n\t"
    "v_mfma_f32_16x16x32_bf16 v[92:95], v[212:215], v[116:119], v[92:95]\n\t"
    "s_waitcnt lgkmcnt(4)\n\t"
    "v_mfma_f32_16x16x32_bf16 v[80:83], v[216:219], v[120:123], v[80:83]\n\t"
    "ds_read_b128 v[112:115], %[ea] offset:28672\n\t"
    "v_mfma_f32_16x16x32_bf16 v[84:87], v[220:223], v[120:123], v[84:87]\n\t"
    "ds_read_b128 v[116:119], %[ea] offset:30720\n\t"
    "v_mfma_f32_16x16x32_bf16 v[88:91], v[216:219], v[124:127], v[88:91]\n\t"
    "v_mfma_f32_16x16x32_bf16 v[92:95], v[220:223], v[124:127], v[92:95]\n\t"
    "s_waitcnt lgkmcnt(4)\n\t"
    "v_mfma_f32_16x16x32_bf16 v[80:83], v[224:227], v[96:99], v[80:83]\n\t"
    "ds_read_b128 v[120:123], %[da] offset:28672\n\t"
    "v_mfma_f32_16x16x32_bf16 v[84:87], v[228:231], v[96:99], v[84:87]\n\t"
    "ds_read_b128 v[124:127], %[da] offset:30720\n\t"
    "v_mfma_f32_16x16x32_bf16 v[88:91], v[224:227], v[100:103], v[88:91]\n\t"
    "v_mfma_f32_16x16x32_bf16 v[92:95], v[228:231], v[100:103], v[92:95]\n\t"
    "s_waitcnt lgkmcnt(4)\n\t"
    "v_mfma_f32_16x16x32_bf16 v[80:83], v[232:235], v[104:107], v[80:83]\n\t"
    "v_mfma_f32_16x16x32_bf16 v[84:87], v[236:239], v[104:107], v[84:87]\n\t"
    "v_mfma_f32_16x16x32_bf16 v[88:91], v[232:235], v[108:111], v[88:91]\n\t"
    "v_mfma_f32_16x16x32_bf16 v[92:95], v[236:239], v[108:111], v[92:95]\n\t"
    "s_waitcnt lgkmcnt(2)\n\t"
    "v_mfma_f32_16x16x32_bf16 v[80:83], v[240:243], v[112:115], v[80:83]\n\t"
    "v_mfma_f32_16x16x32_bf16 v[84:87], v[244:247], v[112:115], v[84:87]\n\t"
    "v_mfma_f32_16x16x32_bf16 v[88:91], v[240:243], v[116:119], v[88:91]\n\t"
    "v_mfma_f32_16x16x32_bf16 v[92:95], v[244:247], v[116:119], v[92:95]\n\t"
    "s_waitcnt lgkmcnt(0)\n\t"
    "v_mfma_f32_16x16x32_bf16 v[80:83], v[248:251], v[120:123], v[80:83]\n\t"
    "v_mfma_f32_16x16x32_bf16 v[84:87], v[252:255], v[120:123], v[84:87]\n\t"
    "v_mfma_f32_16x16x32_bf16 v[88:91], v[248:251], v[124:127], v[88:91]\n\t"
    "v_mfma_f32_16x16x32_bf16 v[92:95], v[252:255], v[124:127], v[92:95]\n\t"
    "s_nop 6\n\t"
    "s_branch 9f\n\t"
    "3:\n\t"
    "s_bitcmp0_b32 %[x], 0\n\t"
    "s_cbranch_scc1 5f\n\t"
    "7:\n\t"
    "v_mov_b32 v76, %[cn]\n\t"
    "v_mov_b32 v77, 1\n\t"
    "v_mov_b32 v78, %[pv]\n\t"
    "v_mov_b32 v79, 0\n\t"
    "s_waitcnt vmcnt(0)\n\t"
    "s_mov_b64 exec, 1\n\t"
    "ds_add_rtn_u32 v77, v76, v77\n\t"
    "s_waitcnt lgkmcnt(0)\n\t"
    "v_readfirstlane_b32 %[st], v77\n\t"
    "s_and_b32 %[st], %[st], 3\n\t"
    "s_cmp_lg_u32 %[st], 3\n\t"
    "s_cbranch_scc1 6f\n\t"
    "global_store_dword v79, v78, %[pg] sc1\n\t"
    "6:\n\t"
    "s_mov_b64 exec, -1\n\t"
    "s_bitcmp1_b32 %[x], 2\n\t"
    "s_cbranch_scc1 8b\n\t"
    "5:\n\t"
    "s_bitcmp0_b32 %[x], 1\n\t"
    "s_cbranch_scc1 4b\n\t"
    "s_mov_b32 m0, %[sl]\n\t"
    "s_nop 0\n\t"
    "global_load_lds_dword %[dv], %[db] sc1\n\t"
    "s_branch 4b\n\t"
    "9:"
    : "=&{v[80:95]}"(pd), [st] "=&s"(st)
    : [ea] "v"(ea), [da] "v"(da), [x] "s"(x), "{v[128:159]}"(W[0]), "{v[160:191]}"(W[1]), "{v[192:223]}"(W[2]), "{v[224:255]}"(W[3]), [pv] "s"(pv), [cn] "s"(cn), [sl] "s"(sl), [dv] "v"(dv), [db] "s"(db), [pg] "s"(pg)
    : "v96", "v97", "v98", "v99", "v100", "v101", "v102", "v103", "v104", "v105", "v106", "v107", "v108", "v109", "v110", "v111", "v112", "v113", "v114", "v115", "v116", "v117", "v118", "v119", "v120", "v121", "v122", "v123", "v124", "v125", "v126", "v127", "v76", "v77", "v78", "v79", "vcc", "scc", "m0", "memory");
}
#define LEAN_QB 1
__device__ __forceinline__ void paced_ld0(const u32x32 (&W)[4], unsigned ea, unsigned da, int b, int ns, unsigned ab, unsigned rw, unsigned fa,
    const char* p0, const char* p1, unsigned lb, int& cap, int wm, unsigned rq, unsigned mu, unsigned ad, unsigned dv, const char* db, unsigned sl, unsigned sa, const unsigned (&av)[8]) {
  unsigned st;
  asm volatile("s_setprio 2\n\t"
    "v_mov_b32 v77, %[wm]\n\t"
    "s_cmp_lg_u32 %[rq], 0\n\t"
    "s_cbranch_scc1 3f\n\t"
    "2:\n\t"
    "v_cmp_ge_i32 vcc, %[b], v77\n\t"
    "s_cbranch_vccz 4f\n\t"
    "21:\n\t"
    "s_cmp_eq_u32 %[sl], 0\n\t"
    "s_cbranch_scc1 71f\n\t"
    "s_mov_b32 m0, %[sl]\n\t"
    "s_nop 0\n\t"
    "global_load_lds_dword %[dv], %[db] sc1\n\t"
    "71:\n\t"
    "s_sub_u32 %[cap], %[cap], 1\n\t"
    "s_max_i32 %[cap], %[cap], 0\n\t"
    "s_waitcnt vmcnt(0)\n\t"
    "ds_read2_b32 v[78:79], %[sa] offset1:32\n\t"
    "s_waitcnt lgkmcnt(0)\n\t"
    "v_max_u32 v78, v78, v79\n\t"
    "v_mad_u32_u24 v78, v78, %[mu], %[ad]\n\t"
    "v_max_i32 v77, v77, v78\n\t"
    "v_cmp_ge_i32 vcc, %[b], v77\n\t"
    "s_cbranch_vccz 4f\n\t"
    "s_sleep 1\n\t"
    "s_cmp_lg_u32 %[cap], 0\n\t"
    "s_cbranch_scc1 21b\n\t"
    "4:\n\t"
    "s_add_u32 m0, %[lb], 0\n\t"
    "s_nop 0\n\t"
    "global_load_lds_dwordx4 %[av0], s[36:37] sc1\n\t"
    "s_add_u32 m0, %[lb], 32768\n\t"
    "s_nop 0\n\t"
    "global_load_lds_dwordx4 %[av1], s[36:37] sc1\n\t"
    "s_add_u32 m0, %[lb], 4096\n\t"
    "s_nop 0\n\t"
    "global_load_lds_dwordx4 %[av2], s[36:37] sc1\n\t"
    "s_add_u32 m0, %[lb], 36864\n\t"
    "s_nop 0\n\t"
    "global_load_lds_dwordx4 %[av3], s[36:37] sc1\n\t"
    "s_add_u32 m0, %[lb], 8192\n\t"
    "s_nop 0\n\t"
    "global_load_lds_dwordx4 %[av4], s[36:37] sc1\n\t"
    "s_add_u32 m0, %[lb], 40960\n\t"
    "s_nop 0\n\t"
    "global_load_lds_dwordx4 %[av5], s[36:37] sc1\n\t"
    "s_add_u32 m0, %[lb], 12288\n\t"
    "s_nop 0\n\t"
    "global_load_lds_dwordx4 %[av6], s[36:37] sc1\n\t"
    "s_add_u32 m0, %[lb], 45056\n\t"
    "s_nop 0\n\t"
    "global_load_lds_dwordx4 %[av7], s[36:37] sc1\n\t"
    "s_add_u32 m0, %[lb], 16384\n\t"
    "s_nop 0\n\t"
    "global_load_lds_dwordx4 %[av0], s[38:39] sc1\n\t"
    "s_add_u32 m0, %[lb], 49152\n\t"
    "s_nop 0\n\t"
    "global_load_lds_dwordx4 %[av1], s[38:39] sc1\n\t"
    "s_add_u32 m0, %[lb], 20480\n\t"
    "s_nop 0\n\t"
    "global_load_lds_dwordx4 %[av2], s[38:39] sc1\n\t"
    "s_add_u32 m0, %[lb], 53248\n\t"
    "s_nop 0\n\t"
    "global_load_lds_dwordx4 %[av3], s[38:39] sc1\n\t"
    "s_add_u32 m0, %[lb], 24576\n\t"
    "s_nop 0\n\t"
    "global_load_lds_dwordx4 %[av4], s[38:39] sc1\n\t"
    "s_add_u32 m0, %[lb], 57344\n\t"
    "s_nop 0\n\t"
    "global_load_lds_dwordx4 %[av5], s[38:39] sc1\n\t"
    "s_add_u32 m0, %[lb], 28672\n\t"
    "s_nop 0\n\t"
    "global_load_lds_dwordx4 %[av6], s[38:39] sc1\n\t"
    "s_add_u32 m0, %[lb], 61440\n\t"
    "s_nop 0\n\t"
    "global_load_lds_dwordx4 %[av7], s[38:39] sc1\n\t"
    "s_add_u32 s36, s36, %[ab]\n\t"
    "s_addc_u32 s37, s37, 0\n\t"
    "s_add_u32 s38, s38, %[ab]\n\t"
    "s_addc_u32 s39, s39, 0\n\t"
    "s_xor_b32 %[lb], %[lb], 0x10000\n\t"
    "3:\n\t"
    "s_add_u32 %[st], %[b], 1\n\t"
    "v_min_i32 v77, %[ns], v77\n\t"
    "v_cmp_ge_i32 vcc, %[st], v77\n\t"
    "s_waitcnt vmcnt(12) lgkmcnt(0)\n\t"
    "s_barrier\n\t"
    "ds_read_b32 v75, %[fa] offset:16\n\t"
    "ds_read_b128 v[96:99], %[ea] offset:0\n\t"
    "ds_read_b128 v[100:103], %[ea] offset:2048\n\t"
    "ds_read_b128 v[104:107], %[da] offset:0\n\t"
    "ds_read_b128 v[108:111], %[da] offset:2048\n\t"
    "ds_read_b128 v[112:115], %[ea] offset:4096\n\t"
    "ds_read_b128 v[116:119], %[ea] offset:6144\n\t"
    "s_cbranch_vccz 5f\n\t"
    "s_waitcnt lgkmcnt(4)\n\t"
    "v_mfma_f32_16x16x32_bf16 v[80:83], v[128:131], v[96:99], 0\n\t"
    "ds_read_b128 v[120:123], %[da] offset:4096\n\t"
    "v_mfma_f32_16x16x32_bf16 v[84:87], v[132:135], v[96:99], 0\n\t"
    "ds_read_b128 v[124:127], %[da] offset:6144\n\t"
    "v_readfirstlane_b32 %[st], v75\n\t"
    "v_mfma_f32_16x16x32_bf16 v[88:91], v[128:131], v[100:103], 0\n\t"
    "v_mfma_f32_16x16x32_bf16 v[92:95], v[132:135], v[100:103], 0\n\t"
    "s_cmp_lg_u32 %[st], 0\n\t"
    "s_cbranch_scc1 9f\n\t"
    "s_waitcnt vmcnt(8)\n\t"
    "s_barrier\n\t"
    "s_waitcnt lgkmcnt(4)\n\t"
    "v_mfma_f32_16x16x32_bf16 v[80:83], v[136:139], v[104:107], v[80:83]\n\t"
    "ds_read_b128 v[96:99], %[ea] offset:8192\n\t"
    "v_mfma_f32_16x16x32_bf16 v[84:87], v[140:143], v[104:107], v[84:87]\n\t"
    "ds_read_b128 v[100:103], %[ea] offset:10240\n\t"
    "v_mfma_f32_16x16x32_bf16 v[88:91], v[136:139], v[108:111], v[88:91]\n\t"
    "v_mfma_f32_16x16x32_bf16 v[92:95], v[140:143], v[108:111], v[92:95]\n\t"
    "s_waitcnt lgkmcnt(4)\n\t"
    "v_mfma_f32_16x16x32_bf16 v[80:83], v[144:147], v[112:115], v[80:83]\n\t"
    "ds_read_b128 v[104:107], %[da] offset:8192\n\t"
    "v_mfma_f32_16x16x32_bf16 v[84:87], v[148:151], v[112:115], v[84:87]\n\t"
    "ds_read_b128 v[108:111], %[da] offset:10240\n\t"
    "v_mfma_f32_16x16x32_bf16 v[88:91], v[144:147], v[116:119], v[88:91]\n\t"
    "v_mfma_f32_16x16x32_bf16 v[92:95], v[148:151], v[116:119], v[92:95]\n\t"
    "s_waitcnt lgkmcnt(4)\n\t"
    "v_mfma_f32_16x16x32_bf16 v[80:83], v[152:155], v[120:123], v[80:83]\n\t"
    "ds_read_b128 v[112:115], %[ea] offset:12288\n\t"
    "v_mfma_f32_16x16x32_bf16 v[84:87], v[156:159], v[120:123], v[84:87]\n\t"
    "ds_read_b128 v[116:119], %[ea] offset:14336\n\t"
    "v_mfma_f32_16x16x32_bf16 v[88:91], v[152:155], v[124:127], v[88:91]\n\t"
    "v_mfma_f32_16x16x32_bf16 v[92:95], v[156:159], v[124:127], v[92:95]\n\t"
    "s_waitcnt lgkmcnt(4)\n\t"
    "v_mfma_f32_16x16x32_bf16 v[80:83], v[160:163], v[96:99], v[80:83]\n\t"
    "ds_read_b128 v[120:123], %[da] offset:12288\n\t"
    "v_mfma_f32_16x16x32_bf16 v[84:87], v[164:167], v[96:99], v[84:87]\n\t"
    "ds_read_b128 v[124:127], %[da] offset:14336\n\t"
    "v_mfma_f32_16x16x32_bf16 v[88:91], v[160:163], v[100:103], v[88:91]\n\t"
    "v_mfma_f32_16x16x32_bf16 v[92:95], v[164:167], v[100:103], v[92:95]\n\t"
    "s_waitcnt vmcnt(4)\n\t"
    "s_barrier\n\t"
    "s_waitcnt lgkmcnt(4)\n\t"
    "v_mfma_f32_16x16x32_bf16 v[80:83], v[168:171], v[104:107], v[80:83]\n\t"
    "ds_read_b128 v[96:99], %[ea] offset:16384\n\t"
    "v_mfma_f32_16x16x32_bf16 v[84:87], v[172:175], v[104:107], v[84:87]\n\t"
    "ds_read_b128 v[100:103], %[ea] offset:18432\n\t"
    "v_mfma_f32_16x16x32_bf16 v[88:91], v[168:171], v[108:111], v[88:91]\n\t"
    "v_mfma_f32_16x16x32_bf16 v[92:95], v[172:175], v[108:111], v[92:95]\n\t"
    "s_waitcnt lgkmcnt(4)\n\t"
    "v_mfma_f32_16x16x32_bf16 v[80:83], v[176:179], v[112:115], v[80:83]\n\t"
    "ds_read_b128 v[104:107], %[da] offset:16384\n\t"
    "v_mfma_f32_16x16x32_bf16 v[84:87], v[180:183], v[112:115], v[84:87]\n\t"
    "ds_read_b128 v[108:111], %[da] offset:18432\n\t"
    "v_mfma_f32_16x16x32_bf16 v[88:91], v[176:179], v[116:119], v[88:91]\n\t"
    "v_mfma_f32_16x16x32_bf16 v[92:95], v[180:183], v[116:119], v[92:95]\n\t"
    "s_waitcnt lgkmcnt(4)\n\t"
    "v_mfma_f32_16x16x32_bf16 v[80:83], v[184:187], v[120:123], v[80:83]\n\t"
    "ds_read_b128 v[112:115], %[ea] offset:20480\n\t"
    "v_mfma_f32_16x16x32_bf16 v[84:87], v[188:191], v[120:123], v[84:87]\n\t"
    "ds_read_b128 v[116:119], %[ea] offset:22528\n\t"
    "v_mfma_f32_16x16x32_bf16 v[88:91], v[184:187], v[124:127], v[88:91]\n\t"
    "v_mfma_f32_16x16x32_bf16 v[92:95], v[188:191], v[124:127], v[92:95]\n\t"
    "s_waitcnt lgkmcnt(4)\n\t"
    "v_mfma_f32_16x16x32_bf16 v[80:83], v[192:195], v[96:99], v[80:83]\n\t"
    "ds_read_b128 v[120:123], %[da] offset:20480\n\t"
    "v_mfma_f32_16x16x32_bf16 v[84:87], v[196:199], v[96:99], v[84:87]\n\t"
    "ds_read_b128 v[124:127], %[da] offset:22528\n\t"
    "v_mfma_f32_16x16x32_bf16 v[88:91], v[192:195], v[100:103], v[88:91]\n\t"
    "v_mfma_f32_16x16x32_bf16 v[92:95], v[196:199], v[100:103], v[92:95]\n\t"
    "s_waitcnt vmcnt(0)\n\t"
    "s_barrier\n\t"
    "s_waitcnt lgkmcnt(4)\n\t"
    "v_mfma_f32_16x16x32_bf16 v[80:83], v[200:203], v[104:107], v[80:83]\n\t"
    "ds_read_b128 v[96:99], %[ea] offset:24576\n\t"
    "v_mfma_f32_16x16x32_bf16 v[84:87], v[204:207], v[104:107], v[84:87]\n\t"
    "ds_read_b128 v[100:103], %[ea] offset:26624\n\t"
    "v_mfma_f32_16x16x32_bf16 v[88:91], v[200:203], v[108:111], v[88:91]\n\t"
    "v_mfma_f32_16x16x32_bf16 v[92:95], v[204:207], v[108:111], v[92:95]\n\t"
    "s_cmp_eq_u32 %[sl], 0\n\t"
    "s_cbranch_scc1 72f\n\t"
    "s_mov_b32 m0, %[sl]\n\t"
    "s_nop 0\n\t"
    "global_load_lds_dword %[dv], %[db] sc1\n\t"
    "72:\n\t"
    "s_waitcnt lgkmcnt(4)\n\t"
    "v_mfma_f32_16x16x32_bf16 v[80:83], v[208:211], v[112:115], v[80:83]\n\t"
    "ds_read_b128 v[104:107], %[da] offset:24576\n\t"
    "v_mfma_f32_16x16x32_bf16 v[84:87], v[212:215], v[112:115], v[84:87]\n\t"
    "ds_read_b128 v[108:111], %[da] offset:26624\n\t"
    "v_mfma_f32_16x16x32_bf16 v[88:91], v[208:211], v[116:119], v[88:91]\n\t"
    "v_mfma_f32_16x16x32_bf16 v[92:95], v[212:215], v[116:119], v[92:95]\n\t"
    "s_waitcnt lgkmcnt(4)\n\t"
    "v_mfma_f32_16x16x32_bf16 v[80:83], v[216:219], v[120:123], v[80:83]\n\t"
    "ds_read_b128 v[112:115], %[ea] offset:28672\n\t"
    "v_mfma_f32_16x16x32_bf16 v[84:87], v[220:223], v[120:123], v[84:87]\n\t"
    "ds_read_b128 v[116:119], %[ea] offset:30720\n\t"
    "v_mfma_f32_16x16x32_bf16 v[88:91], v[216:219], v[124:127], v[88:91]\n\t"
    "v_mfma_f32_16x16x32_bf16 v[92:95], v[220:223], v[124:127], v[92:95]\n\t"
    "s_waitcnt lgkmcnt(4)\n\t"
    "v_mfma_f32_16x16x32_bf16 v[80:83], v[224:227], v[96:99], v[80:83]\n\t"
    "ds_read_b128 v[120:123], %[da] offset:28672\n\t"
    "v_mfma_f32_16x16x32_bf16 v[84:87], v[228:231], v[96:99], v[84:87]\n\t"
    "ds_read_b128 v[124:127], %[da] offset:30720\n\t"
    "v_mfma_f32_16x16x32_bf16 v[88:91], v[224:227], v[100:103], v[88:91]\n\t"
    "v_mfma_f32_16x16x32_bf16 v[92:95], v[228:231], v[100:103], v[92:95]\n\t"
    "s_waitcnt lgkmcnt(4)\n\t"
    "v_mfma_f32_16x16x32_bf16 v[80:83], v[232:235], v[104:107], v[80:83]\n\t"
    "v_mfma_f32_16x16x32_bf16 v[84:87], v[236:239], v[104:107], v[84:87]\n\t"
    "v_mfma_f32_16x16x32_bf16 v[88:91], v[232:235], v[108:111], v[88:91]\n\t"
    "v_xor_b32 %[ea], 0x10000, %[ea]\n\t"
    "v_mfma_f32_16x16x32_bf16 v[92:95], v[236:239], v[108:111], v[92:95]\n\t"
    "v_xor_b32 %[da], 0x10000, %[da]\n\t"
    "s_waitcnt lgkmcnt(2)\n\t"
    "v_mfma_f32_16x16x32_bf16 v[80:83], v[240:243], v[112:115], v[80:83]\n\t"
    "v_mfma_f32_16x16x32_bf16 v[84:87], v[244:247], v[112:115], v[84:87]\n\t"
    "v_mfma_f32_16x16x32_bf16 v[88:91], v[240:243], v[116:119], v[88:91]\n\t"
    "s_add_u32 %[b], %[b], 1\n\t"
    "v_mfma_f32_16x16x32_bf16 v[92:95], v[244:247], v[116:119], v[92:95]\n\t"
    "s_waitcnt lgkmcnt(0)\n\t"
    "v_mfma_f32_16x16x32_bf16 v[80:83], v[248:251], v[120:123], v[80:83]\n\t"
    "v_mfma_f32_16x16x32_bf16 v[84:87], v[252:255], v[120:123], v[84:87]\n\t"
    "v_mfma_f32_16x16x32_bf16 v[88:91], v[248:251], v[124:127], v[88:91]\n\t"
    "v_mfma_f32_16x16x32_bf16 v[92:95], v[252:255], v[124:127], v[92:95]\n\t"
    "s_nop 4\n\t"
    "ds_write_b128 %[rw], v[80:83] offset:0\n\t"
    "ds_write_b128 %[rw], v[84:87] offset:1024\n\t"
    "ds_write_b128 %[rw], v[88:91] offset:2048\n\t"
    "ds_write_b128 %[rw], v[92:95] offset:3072\n\t"
    "s_waitcnt vmcnt(0)\n\t"
    "ds_read2_b32 v[78:79], %[sa] offset1:32\n\t"
    "s_waitcnt lgkmcnt(0)\n\t"
    "v_max_u32 v78, v78, v79\n\t"
    "v_mad_u32_u24 v78, v78, %[mu], %[ad]\n\t"
    "v_max_i32 v77, v77, v78\n\t"
    "v_mov_b32 v76, %[b]\n\t"
    "v_cmp_ge_i32 vcc, %[b], v77\n\t"
    "s_cmp_ge_i32 %[b], %[ns]\n\t"
    "s_cbranch_scc1 6f\n\t"
    "s_cbranch_vccnz 6f\n\t"
    "v_or_b32 v76, 0x100, v76\n\t"
    "ds_write_b32 %[fa], v76\n\t"
    "s_branch 4b\n\t"
    "6:\n\t"
    "ds_write_b32 %[fa], v76\n\t"
    "s_cbranch_scc0 21b\n\t"
    "s_waitcnt lgkmcnt(0)\n\t"
    "s_barrier\n\t"
    "s_branch 9f\n\t"
    "5:\n\t"
    "s_waitcnt lgkmcnt(4)\n\t"
    "v_mfma_f32_16x16x32_bf16 v[80:83], v[128:131], v[96:99], 0\n\t"
    "ds_read_b128 v[120:123], %[da] offset:4096\n\t"
    "s_add_u32 m0, %[lb], 0\n\t"
    "v_mfma_f32_16x16x32_bf16 v[84:87], v[132:135], v[96:99], 0\n\t"
    "ds_read_b128 v[124:127], %[da] offset:6144\n\t"
    "global_load_lds_dwordx4 %[av0], s[36:37] sc1\n\t"
    "v_readfirstlane_b32 %[st], v75\n\t"
    "v_mfma_f32_16x16x32_bf16 v[88:91], v[128:131], v[100:103], 0\n\t"
    "s_add_u32 m0, %[lb], 32768\n\t"
    "v_mfma_f32_16x16x32_bf16 v[92:95], v[132:135], v[100:103], 0\n\t"
    "global_load_lds_dwordx4 %[av1], s[36:37] sc1\n\t"
    "s_cmp_lg_u32 %[st], 0\n\t"
    "s_cbranch_scc1 9f\n\t"
    "s_waitcnt vmcnt(10)\n\t"
    "s_barrier\n\t"
    "s_waitcnt lgkmcnt(4)\n\t"
    "v_mfma_f32_16x16x32_bf16 v[80:83], v[136:139], v[104:107], v[80:83]\n\t"
    "ds_read_b128 v[96:99], %[ea] offset:8192\n\t"
    "s_add_u32 m0, %[lb], 4096\n\t"
    "v_mfma_f32_16x16x32_bf16 v[84:87], v[140:143], v[104:107], v[84:87]\n\t"
    "ds_read_b128 v[100:103], %[ea] offset:10240\n\t"
    "global_load_lds_dwordx4 %[av2], s[36:37] sc1\n\t"
    "v_mfma_f32_16x16x32_bf16 v[88:91], v[136:139], v[108:111], v[88:91]\n\t"
    "s_add_u32 m0, %[lb], 36864\n\t"
    "v_mfma_f32_16x16x32_bf16 v[92:95], v[140:143], v[108:111], v[92:95]\n\t"
    "global_load_lds_dwordx4 %[av3], s[36:37] sc1\n\t"
    "s_waitcnt lgkmcnt(4)\n\t"
    "v_mfma_f32_16x16x32_bf16 v[80:83], v[144:147], v[112:115], v[80:83]\n\t"
    "ds_read_b128 v[104:107], %[da] offset:8192\n\t"
    "s_add_u32 m0, %[lb], 8192\n\t"
    "v_mfma_f32_16x16x32_bf16 v[84:87], v[148:151], v[112:115], v[84:87]\n\t"
    "ds_read_b128 v[108:111], %[da] offset:10240\n\t"
    "global_load_lds_dwordx4 %[av4], s[36:37] sc1\n\t"
    "v_mfma_f32_16x16x32_bf16 v[88:91], v[144:147], v[116:119], v[88:91]\n\t"
    "s_add_u32 m0, %[lb], 40960\n\t"
    "v_mfma_f32_16x16x32_bf16 v[92:95], v[148:151], v[116:119], v[92:95]\n\t"
    "global_load_lds_dwordx4 %[av5], s[36:37] sc1\n\t"
    "s_waitcnt lgkmcnt(4)\n\t"
    "v_mfma_f32_16x16x32_bf16 v[80:83], v[152:155], v[120:123], v[80:83]\n\t"
    "ds_read_b128 v[112:115], %[ea] offset:12288\n\t"
    "s_add_u32 m0, %[lb], 12288\n\t"
    "v_mfma_f32_16x16x32_bf16 v[84:87], v[156:159], v[120:123], v[84:87]\n\t"
    "ds_read_b128 v[116:119], %[ea] offset:14336\n\t"
    "global_load_lds_dwordx4 %[av6], s[36:37] sc1\n\t"
    "v_mfma_f32_16x16x32_bf16 v[88:91], v[152:155], v[124:127], v[88:91]\n\t"
    "s_add_u32 m0, %[lb], 45056\n\t"
    "v_mfma_f32_16x16x32_bf16 v[92:95], v[156:159], v[124:127], v[92:95]\n\t"
    "global_load_lds_dwordx4 %[av7], s[36:37] sc1\n\t"
    "s_waitcnt lgkmcnt(4)\n\t"
    "v_mfma_f32_16x16x32_bf16 v[80:83], v[160:163], v[96:99], v[80:83]\n\t"
    "ds_read_b128 v[120:123], %[da] offset:12288\n\t"
    "s_add_u32 m0, %[lb], 16384\n\t"
    "v_mfma_f32_16x16x32_bf16 v[84:87], v[164:167], v[96:99], v[84:87]\n\t"
    "ds_read_b128 v[124:127], %[da] offset:14336\n\t"
    "global_load_lds_dwordx4 %[av0], s[38:39] sc1\n\t"
    "v_mfma_f32_16x16x32_bf16 v[88:91], v[160:163], v[100:103], v[88:91]\n\t"
    "s_add_u32 m0, %[lb], 49152\n\t"
    "v_mfma_f32_16x16x32_bf16 v[92:95], v[164:167], v[100:103], v[92:95]\n\t"
    "global_load_lds_dwordx4 %[av1], s[38:39] sc1\n\t"
    "s_waitcnt vmcnt(14)\n\t"
    "s_barrier\n\t"
    "s_waitcnt lgkmcnt(4)\n\t"
    "v_mfma_f32_16x16x32_bf16 v[80:83], v[168:171], v[104:107], v[80:83]\n\t"
    "ds_read_b128 v[96:99], %[ea] offset:16384\n\t"
    "s_add_u32 m0, %[lb], 20480\n\t"
    "v_mfma_f32_16x16x32_bf16 v[84:87], v[172:175], v[104:107], v[84:87]\n\t"
    "ds_read_b128 v[100:103], %[ea] offset:18432\n\t"
    "global_load_lds_dwordx4 %[av2], s[38:39] sc1\n\t"
    "v_mfma_f32_16x16x32_bf16 v[88:91], v[168:171], v[108:111], v[88:91]\n\t"
    "s_add_u32 m0, %[lb], 53248\n\t"
    "v_mfma_f32_16x16x32_bf16 v[92:95], v[172:175], v[108:111], v[92:95]\n\t"
    "global_load_lds_dwordx4 %[av3], s[38:39] sc1\n\t"
    "s_waitcnt lgkmcnt(4)\n\t"
    "v_mfma_f32_16x16x32_bf16 v[80:83], v[176:179], v[112:115], v[80:83]\n\t"
    "ds_read_b128 v[104:107], %[da] offset:16384\n\t"
    "s_add_u32 m0, %[lb], 24576\n\t"
    "v_mfma_f32_16x16x32_bf16 v[84:87], v[180:183], v[112:115], v[84:87]\n\t"
    "ds_read_b128 v[108:111], %[da] offset:18432\n\t"
    "global_load_lds_dwordx4 %[av4], s[38:39] sc1\n\t"
    "v_mfma_f32_16x16x32_bf16 v[88:91], v[176:179], v[116:119], v[88:91]\n\t"
    "s_add_u32 m0, %[lb], 57344\n\t"
    "v_mfma_f32_16x16x32_bf16 v[92:95], v[180:183], v[116:119], v[92:95]\n\t"
    "global_load_lds_dwordx4 %[av5], s[38:39] sc1\n\t"
    "s_waitcnt lgkmcnt(4)\n\t"
    "v_mfma_f32_16x16x32_bf16 v[80:83], v[184:187], v[120:123], v[80:83]\n\t"
    "ds_read_b128 v[112:115], %[ea] offset:20480\n\t"
    "s_add_u32 m0, %[lb], 28672\n\t"
    "v_mfma_f32_16x16x32_bf16 v[84:87], v[188:191], v[120:123], v[84:87]\n\t"
    "ds_read_b128 v[116:119], %[ea] offset:22528\n\t"
    "global_load_lds_dwordx4 %[av6], s[38:39] sc1\n\t"
    "v_mfma_f32_16x16x32_bf16 v[88:91], v[184:187], v[124:127], v[88:91]\n\t"
    "s_add_u32 m0, %[lb], 61440\n\t"
    "v_mfma_f32_16x16x32_bf16 v[92:95], v[188:191], v[124:127], v[92:95]\n\t"
    "global_load_lds_dwordx4 %[av7], s[38:39] sc1\n\t"
    "s_waitcnt lgkmcnt(4)\n\t"
    "v_mfma_f32_16x16x32_bf16 v[80:83], v[192:195], v[96:99], v[80:83]\n\t"
    "ds_read_b128 v[120:123], %[da] offset:20480\n\t"
    "v_mfma_f32_16x16x32_bf16 v[84:87], v[196:199], v[96:99], v[84:87]\n\t"
    "ds_read_b128 v[124:127], %[da] offset:22528\n\t"
    "v_mfma_f32_16x16x32_bf16 v[88:91], v[192:195], v[100:103], v[88:91]\n\t"
    "v_mfma_f32_16x16x32_bf16 v[92:95], v[196:199], v[100:103], v[92:95]\n\t"
    "s_add_u32 s36, s36, %[ab]\n\t"
    "s_addc_u32 s37, s37, 0\n\t"
    "s_add_u32 s38, s38, %[ab]\n\t"
    "s_addc_u32 s39, s39, 0\n\t"
    "s_xor_b32 %[lb], %[lb], 0x10000\n\t"
    "s_waitcnt vmcnt(16)\n\t"
    "s_barrier\n\t"
    "s_waitcnt lgkmcnt(4)\n\t"
    "v_mfma_f32_16x16x32_bf16 v[80:83], v[200:203], v[104:107], v[80:83]\n\t"
    "ds_read_b128 v[96:99], %[ea] offset:24576\n\t"
    "v_mfma_f32_16x16x32_bf16 v[84:87], v[204:207], v[104:107], v[84:87]\n\t"
    "ds_read_b128 v[100:103], %[ea] offset:26624\n\t"
    "v_mfma_f32_16x16x32_bf16 v[88:91], v[200:203], v[108:111], v[88:91]\n\t"
    "v_mfma_f32_16x16x32_bf16 v[92:95], v[204:207], v[108:111], v[92:95]\n\t"
    "s_cmp_eq_u32 %[sl], 0\n\t"
    "s_cbranch_scc1 73f\n\t"
    "s_mov_b32 m0, %[sl]\n\t"
    "s_nop 0\n\t"
    "global_load_lds_dword %[dv], %[db] sc1\n\t"
    "73:\n\t"
    "s_waitcnt lgkmcnt(4)\n\t"
    "v_mfma_f32_16x16x32_bf16 v[80:83], v[208:211], v[112:115], v[80:83]\n\t"
    "ds_read_b128 v[104:107], %[da] offset:24576\n\t"
    "v_mfma_f32_16x16x32_bf16 v[84:87], v[212:215], v[112:115], v[84:87]\n\t"
    "ds_read_b128 v[108:111], %[da] offset:26624\n\t"
    "v_mfma_f32_16x16x32_bf16 v[88:91], v[208:211], v[116:119], v[88:91]\n\t"
    "v_mfma_f32_16x16x32_bf16 v[92:95], v[212:215], v[116:119], v[92:95]\n\t"
    "s_waitcnt lgkmcnt(4)\n\t"
    "v_mfma_f32_16x16x32_bf16 v[80:83], v[216:219], v[120:123], v[80:83]\n\t"
    "ds_read_b128 v[112:115], %[ea] offset:28672\n\t"
    "v_mfma_f32_16x16x32_bf16 v[84:87], v[220:223], v[120:123], v[84:87]\n\t"
    "ds_read_b128 v[116:119], %[ea] offset:30720\n\t"
    "v_mfma_f32_16x16x32_bf16 v[88:91], v[216:219], v[124:127], v[88:91]\n\t"
    "v_mfma_f32_16x16x32_bf16 v[92:95], v[220:223], v[124:127], v[92:95]\n\t"
    "s_waitcnt lgkmcnt(4)\n\t"
    "v_mfma_f32_16x16x32_bf16 v[80:83], v[224:227], v[96:99], v[80:83]\n\t"
    "ds_read_b128 v[120:123], %[da] offset:28672\n\t"
    "v_mfma_f32_16x16x32_bf16 v[84:87], v[228:231], v[96:99], v[84:87]\n\t"
    "ds_read_b128 v[124:127], %[da] offset:30720\n\t"
    "v_mfma_f32_16x16x32_bf16 v[88:91], v[224:227], v[100:103], v[88:91]\n\t"
    "v_mfma_f32_16x16x32_bf16 v[92:95], v[228:231], v[100:103], v[92:95]\n\t"
    "s_waitcnt lgkmcnt(4)\n\t"
    "v_mfma_f32_16x16x32_bf16 v[80:83], v[232:235], v[104:107], v[80:83]\n\t"
    "v_mfma_f32_16x16x32_bf16 v[84:87], v[236:239], v[104:107], v[84:87]\n\t"
    "v_mfma_f32_16x16x32_bf16 v[88:91], v[232:235], v[108:111], v[88:91]\n\t"
    "v_xor_b32 %[ea], 0x10000, %[ea]\n\t"
    "v_mfma_f32_16x16x32_bf16 v[92:95], v[236:239], v[108:111], v[92:95]\n\t"
    "v_xor_b32 %[da], 0x10000, %[da]\n\t"
    "s_waitcnt lgkmcnt(2)\n\t"
    "v_mfma_f32_16x16x32_bf16 v[80:83], v[240:243], v[112:115], v[80:83]\n\t"
    "v_mfma_f32_16x16x32_bf16 v[84:87], v[244:247], v[112:115], v[84:87]\n\t"
    "v_mfma_f32_16x16x32_bf16 v[88:91], v[240:243], v[116:119], v[88:91]\n\t"
    "s_add_u32 %[b], %[b], 1\n\t"
    "v_mfma_f32_16x16x32_bf16 v[92:95], v[244:247], v[116:119], v[92:95]\n\t"
    "s_waitcnt lgkmcnt(0)\n\t"
    "v_mfma_f32_16x16x32_bf16 v[80:83], v[248:251], v[120:123], v[80:83]\n\t"
    "v_mfma_f32_16x16x32_bf16 v[84:87], v[252:255], v[120:123], v[84:87]\n\t"
    "v_mfma_f32_16x16x32_bf16 v[88:91], v[248:251], v[124:127], v[88:91]\n\t"
    "v_mfma_f32_16x16x32_bf16 v[92:95], v[252:255], v[124:127], v[92:95]\n\t"
    "v_mov_b32 v76, %[b]\n\t"
    "v_or_b32 v76, 0x100, v76\n\t"
    "s_nop 2\n\t"
    "ds_write_b128 %[rw], v[80:83] offset:0\n\t"
    "ds_write_b128 %[rw], v[84:87] offset:1024\n\t"
    "ds_write_b128 %[rw], v[88:91] offset:2048\n\t"
    "ds_write_b128 %[rw], v[92:95] offset:3072\n\t"
    "ds_write_b32 %[fa], v76\n\t"
    "s_waitcnt vmcnt(0)\n\t"
    "ds_read2_b32 v[78:79], %[sa] offset1:32\n\t"
    "s_waitcnt lgkmcnt(0)\n\t"
    "v_max_u32 v78, v78, v79\n\t"
    "v_mad_u32_u24 v78, v78, %[mu], %[ad]\n\t"
    "v_max_i32 v77, v77, v78\n\t"
    "s_branch 3b\n\t"
    "9:\n\t"
    "s_waitcnt lgkmcnt(0)\n\t"
    "s_nop 7"
    : [ea] "+&v"(ea), [da] "+&v"(da), [b] "+&s"(b), [st] "=&s"(st), "+{s[36:37]}"(p0), "+{s[38:39]}"(p1), [lb] "+&s"(lb), [cap] "+&s"(cap)
    : [ns] "s"(ns), [ab] "s"(ab), [rw] "v"(rw), [fa] "v"(fa), "{v[128:159]}"(W[0]), "{v[160:191]}"(W[1]), "{v[192:223]}"(W[2]), "{v[224:255]}"(W[3]), [wm] "s"(wm), [rq] "s"(rq), [mu] "s"(mu), [ad] "v"(ad), [dv] "v"(dv), [db] "s"(db), [sl] "s"(sl), [sa] "v"(sa), [av0] "v"(av[0]), [av1] "v"(av[1]), [av2] "v"(av[2]), [av3] "v"(av[3]), [av4] "v"(av[4]), [av5] "v"(av[5]), [av6] "v"(av[6]), [av7] "v"(av[7])
    : "v75", "v76", "v77", "v78", "v79", "v80", "v81", "v82", "v83", "v84", "v85", "v86", "v87", "v88", "v89", "v90", "v91", "v92", "v93", "v94", "v95", "v96", "v97", "v98", "v99", "v100", "v101", "v102", "v103", "v104", "v105", "v106", "v107", "v108", "v109", "v110", "v111", "v112", "v113", "v114", "v115", "v116", "v117", "v118", "v119", "v120", "v121", "v122", "v123", "v124", "v125", "v126", "v127", "vcc", "scc", "m0", "memory");
}
__device__ __forceinline__ void paced_ld2(const u32x32 (&W)[4], unsigned ea, unsigned da, int b, int ns, unsigned ab, unsigned rw, unsigned fa,
    const char* p0, const char* p1, unsigned lb, int& cap, int wm, unsigned rq, unsigned mu, unsigned ad, unsigned dv, const char* db, unsigned sl, unsigned sa, const unsigned (&av)[8]) {
  unsigned st;
  asm volatile("s_setprio 2\n\t"
    "v_mov_b32 v77, %[wm]\n\t"
    "s_cmp_lg_u32 %[rq], 0\n\t"
    "s_cbranch_scc1 3f\n\t"
    "2:\n\t"
    "v_cmp_ge_i32 vcc, %[b], v77\n\t"
    "s_cbranch_vccz 4f\n\t"
    "21:\n\t"
    "s_cmp_eq_u32 %[sl], 0\n\t"
    "s_cbranch_scc1 71f\n\t"
    "s_mov_b32 m0, %[sl]\n\t"
    "s_nop 0\n\t"
    "global_load_lds_dword %[dv], %[db] sc1\n\t"
    "71:\n\t"
    "s_sub_u32 %[cap], %[cap], 1\n\t"
    "s_max_i32 %[cap], %[cap], 0\n\t"
    "s_waitcnt vmcnt(0)\n\t"
    "ds_read2_b32 v[78:79], %[sa] offset1:32\n\t"
    "s_waitcnt lgkmcnt(0)\n\t"
    "v_max_u32 v78, v78, v79\n\t"
    "v_mad_u32_u24 v78, v78, %[mu], %[ad]\n\t"
    "v_max_i32 v77, v77, v78\n\t"
    "v_cmp_ge_i32 vcc, %[b], v77\n\t"
    "s_cbranch_vccz 4f\n\t"
    "s_sleep 1\n\t"
    "s_cmp_lg_u32 %[cap], 0\n\t"
    "s_cbranch_scc1 21b\n\t"
    "4:\n\t"
    "s_add_u32 m0, %[lb], 0\n\t"
    "s_nop 0\n\t"
    "global_load_lds_dwordx4 %[av0], s[36:37] sc1\n\t"
    "s_add_u32 m0, %[lb], 4096\n\t"
    "s_nop 0\n\t"
    "global_load_lds_dwordx4 %[av2], s[36:37] sc1\n\t"
    "s_add_u32 m0, %[lb], 8192\n\t"
    "s_nop 0\n\t"
    "global_load_lds_dwordx4 %[av4], s[36:37] sc1\n\t"
    "s_add_u32 m0, %[lb], 12288\n\t"
    "s_nop 0\n\t"
    "global_load_lds_dwordx4 %[av6], s[36:37] sc1\n\t"
    "s_add_u32 m0, %[lb], 16384\n\t"
    "s_nop 0\n\t"
    "global_load_lds_dwordx4 %[av0], s[38:39] sc1\n\t"
    "s_add_u32 m0, %[lb], 20480\n\t"
    "s_nop 0\n\t"
    "global_load_lds_dwordx4 %[av2], s[38:39] sc1\n\t"
    "s_add_u32 m0, %[lb], 24576\n\t"
    "s_nop 0\n\t"
    "global_load_lds_dwordx4 %[av4], s[38:39] sc1\n\t"
    "s_add_u32 m0, %[lb], 28672\n\t"
    "s_nop 0\n\t"
    "global_load_lds_dwordx4 %[av6], s[38:39] sc1\n\t"
    "s_add_u32 s36, s36, %[ab]\n\t"
    "s_addc_u32 s37, s37, 0\n\t"
    "s_add_u32 s38, s38, %[ab]\n\t"
    "s_addc_u32 s39, s39, 0\n\t"
    "s_xor_b32 %[lb], %[lb], 0x10000\n\t"
    "3:\n\t"
    "s_add_u32 %[st], %[b], 1\n\t"
    "v_min_i32 v77, %[ns], v77\n\t"
    "v_cmp_ge_i32 vcc, %[st], v77\n\t"
    "s_waitcnt vmcnt(6) lgkmcnt(0)\n\t"
    "s_barrier\n\t"
    "ds_read_b32 v75, %[fa] offset:16\n\t"
    "ds_read_b128 v[96:99], %[ea] offset:0\n\t"
    "ds_read_b128 v[100:103], %[ea] offset:2048\n\t"
    "ds_read_b128 v[104:107], %[da] offset:0\n\t"
    "ds_read_b128 v[108:111], %[da] offset:2048\n\t"
    "ds_read_b128 v[112:115], %[ea] offset:4096\n\t"
    "ds_read_b128 v[116:119], %[ea] offset:6144\n\t"
    "s_cbranch_vccz 5f\n\t"
    "s_waitcnt lgkmcnt(4)\n\t"
    "v_mfma_f32_16x16x32_bf16 v[80:83], v[128:131], v[96:99], 0\n\t"
    "ds_read_b128 v[120:123], %[da] offset:4096\n\t"
    "v_mfma_f32_16x16x32_bf16 v[84:87], v[132:135], v[96:99], 0\n\t"
    "ds_read_b128 v[124:127], %[da] offset:6144\n\t"
    "v_readfirstlane_b32 %[st], v75\n\t"
    "v_mfma_f32_16x16x32_bf16 v[88:91], v[128:131], v[100:103], 0\n\t"
    "v_mfma_f32_16x16x32_bf16 v[92:95], v[132:135], v[100:103], 0\n\t"
    "s_cmp_lg_u32 %[st], 0\n\t"
    "s_cbranch_scc1 9f\n\t"
    "s_waitcnt vmcnt(4)\n\t"
    "s_barrier\n\t"
    "s_waitcnt lgkmcnt(4)\n\t"
    "v_mfma_f32_16x16x32_bf16 v[80:83], v[136:139], v[104:107], v[80:83]\n\t"
    "ds_read_b128 v[96:99], %[ea] offset:8192\n\t"
    "v_mfma_f32_16x16x32_bf16 v[84:87], v[140:143], v[104:107], v[84:87]\n\t"
    "ds_read_b128 v[100:103], %[ea] offset:10240\n\t"
    "v_mfma_f32_16x16x32_bf16 v[88:91], v[136:139], v[108:111], v[88:91]\n\t"
    "v_mfma_f32_16x16x32_bf16 v[92:95], v[140:143], v[108:111], v[92:95]\n\t"
    "s_waitcnt lgkmcnt(4)\n\t"
    "v_mfma_f32_16x16x32_bf16 v[80:83], v[144:147], v[112:115], v[80:83]\n\t"
    "ds_read_b128 v[104:107], %[da] offset:8192\n\t"
    "v_mfma_f32_16x16x32_bf16 v[84:87], v[148:151], v[112:115], v[84:87]\n\t"
    "ds_read_b128 v[108:111], %[da] offset:10240\n\t"
    "v_mfma_f32_16x16x32_bf16 v[88:91], v[144:147], v[116:119], v[88:91]\n\t"
    "v_mfma_f32_16x16x32_bf16 v[92:95], v[148:151], v[116:119], v[92:95]\n\t"
    "s_waitcnt lgkmcnt(4)\n\t"
    "v_mfma_f32_16x16x32_bf16 v[80:83], v[152:155], v[120:123], v[80:83]\n\t"
    "ds_read_b128 v[112:115], %[ea] offset:12288\n\t"
    "v_mfma_f32_16x16x32_bf16 v[84:87], v[156:159], v[120:123], v[84:87]\n\t"
    "ds_read_b128 v[116:119], %[ea] offset:14336\n\t"
    "v_mfma_f32_16x16x32_bf16 v[88:91], v[152:155], v[124:127], v[88:91]\n\t"
    "v_mfma_f32_16x16x32_bf16 v[92:95], v[156:159], v[124:127], v[92:95]\n\t"
    "s_waitcnt lgkmcnt(4)\n\t"
    "v_mfma_f32_16x16x32_bf16 v[80:83], v[160:163], v[96:99], v[80:83]\n\t"
    "ds_read_b128 v[120:123], %[da] offset:12288\n\t"
    "v_mfma_f32_16x16x32_bf16 v[84:87], v[164:167], v[96:99], v[84:87]\n\t"
    "ds_read_b128 v[124:127], %[da] offset:14336\n\t"
    "v_mfma_f32_16x16x32_bf16 v[88:91], v[160:163], v[100:103], v[88:91]\n\t"
    "v_mfma_f32_16x16x32_bf16 v[92:95], v[164:167], v[100:103], v[92:95]\n\t"
    "s_waitcnt vmcnt(2)\n\t"
    "s_barrier\n\t"
    "s_waitcnt lgkmcnt(4)\n\t"
    "v_mfma_f32_16x16x32_bf16 v[80:83], v[168:171], v[104:107], v[80:83]\n\t"
    "ds_read_b128 v[96:99], %[ea] offset:16384\n\t"
    "v_mfma_f32_16x16x32_bf16 v[84:87], v[172:175], v[104:107], v[84:87]\n\t"
    "ds_read_b128 v[100:103], %[ea] offset:18432\n\t"
    "v_mfma_f32_16x16x32_bf16 v[88:91], v[168:171], v[108:111], v[88:91]\n\t"
    "v_mfma_f32_16x16x32_bf16 v[92:95], v[172:175], v[108:111], v[92:95]\n\t"
    "s_waitcnt lgkmcnt(4)\n\t"
    "v_mfma_f32_16x16x32_bf16 v[80:83], v[176:179], v[112:115], v[80:83]\n\t"
    "ds_read_b128 v[104:107], %[da] offset:16384\n\t"
    "v_mfma_f32_16x16x32_bf16 v[84:87], v[180:183], v[112:115], v[84:87]\n\t"
    "ds_read_b128 v[108:111], %[da] offset:18432\n\t"
    "v_mfma_f32_16x16x32_bf16 v[88:91], v[176:179], v[116:119], v[88:91]\n\t"
    "v_mfma_f32_16x16x32_bf16 v[92:95], v[180:183], v[116:119], v[92:95]\n\t"
    "s_waitcnt lgkmcnt(4)\n\t"
    "v_mfma_f32_16x16x32_bf16 v[80:83], v[184:187], v[120:123], v[80:83]\n\t"
    "ds_read_b128 v[112:115], %[ea] offset:20480\n\t"
    "v_mfma_f32_16x16x32_bf16 v[84:87], v[188:191], v[120:123], v[84:87]\n\t"
    "ds_read_b128 v[116:119], %[ea] offset:22528\n\t"
    "v_mfma_f32_16x16x32_bf16 v[88:91], v[184:187], v[124:127], v[88:91]\n\t"
    "v_mfma_f32_16x16x32_bf16 v[92:95], v[188:191], v[124:127], v[92:95]\n\t"
    "s_waitcnt lgkmcnt(4)\n\t"
    "v_mfma_f32_16x16x32_bf16 v[80:83], v[192:195], v[96:99], v[80:83]\n\t"
    "ds_read_b128 v[120:123], %[da] offset:20480\n\t"
    "v_mfma_f32_16x16x32_bf16 v[84:87], v[196:199], v[96:99], v[84:87]\n\t"
    "ds_read_b128 v[124:127], %[da] offset:22528\n\t"
    "v_mfma_f32_16x16x32_bf16 v[88:91], v[192:195], v[100:103], v[88:91]\n\t"
    "v_mfma_f32_16x16x32_bf16 v[92:95], v[196:199], v[100:103], v[92:95]\n\t"
    "s_waitcnt vmcnt(0)\n\t"
    "s_barrier\n\t"
    "s_waitcnt lgkmcnt(4)\n\t"
    "v_mfma_f32_16x16x32_bf16 v[80:83], v[200:203], v[104:107], v[80:83]\n\t"
    "ds_read_b128 v[96:99], %[ea] offset:24576\n\t"
    "v_mfma_f32_16x16x32_bf16 v[84:87], v[204:207], v[104:107], v[84:87]\n\t"
    "ds_read_b128 v[100:103], %[ea] offset:26624\n\t"
    "v_mfma_f32_16x16x32_bf16 v[88:91], v[200:203], v[108:111], v[88:91]\n\t"
    "v_mfma_f32_16x16x32_bf16 v[92:95], v[204:207], v[108:111], v[92:95]\n\t"
    "s_cmp_eq_u32 %[sl], 0\n\t"
    "s_cbranch_scc1 72f\n\t"
    "s_mov_b32 m0, %[sl]\n\t"
    "s_nop 0\n\t"
    "global_load_lds_dword %[dv], %[db] sc1\n\t"
    "72:\n\t"
    "s_waitcnt lgkmcnt(4)\n\t"
    "v_mfma_f32_16x16x32_bf16 v[80:83], v[208:211], v[112:115], v[80:83]\n\t"
    "ds_read_b128 v[104:107], %[da] offset:24576\n\t"
    "v_mfma_f32_16x16x32_bf16 v[84:87], v[212:215], v[112:115], v[84:87]\n\t"
    "ds_read_b128 v[108:111], %[da] offset:26624\n\t"
    "v_mfma_f32_16x16x32_bf16 v[88:91], v[208:211], v[116:119], v[88:91]\n\t"
    "v_mfma_f32_16x16x32_bf16 v[92:95], v[212:215], v[116:119], v[92:95]\n\t"
    "s_waitcnt lgkmcnt(4)\n\t"
    "v_mfma_f32_16x16x32_bf16 v[80:83], v[216:219], v[120:123], v[80:83]\n\t"
    "ds_read_b128 v[112:115], %[ea] offset:28672\n\t"
    "v_mfma_f32_16x16x32_bf16 v[84:87], v[220:223], v[120:123], v[84:87]\n\t"
    "ds_read_b128 v[116:119], %[ea] offset:30720\n\t"
    "v_mfma_f32_16x16x32_bf16 v[88:91], v[216:219], v[124:127], v[88:91]\n\t"
    "v_mfma_f32_16x16x32_bf16 v[92:95], v[220:223], v[124:127], v[92:95]\n\t"
    "s_waitcnt lgkmcnt(4)\n\t"
    "v_mfma_f32_16x16x32_bf16 v[80:83], v[224:227], v[96:99], v[80:83]\n\t"
    "ds_read_b128 v[120:123], %[da] offset:28672\n\t"
    "v_mfma_f32_16x16x32_bf16 v[84:87], v[228:231], v[96:99], v[84:87]\n\t"
    "ds_read_b128 v[124:127], %[da] offset:30720\n\t"
    "v_mfma_f32_16x16x32_bf16 v[88:91], v[224:227], v[100:103], v[88:91]\n\t"
    "v_mfma_f32_16x16x32_bf16 v[92:95], v[228:231], v[100:103], v[92:95]\n\t"
    "s_waitcnt lgkmcnt(4)\n\t"
    "v_mfma_f32_16x16x32_bf16 v[80:83], v[232:235], v[104:107], v[80:83]\n\t"
    "v_mfma_f32_16x16x32_bf16 v[84:87], v[236:239], v[104:107], v[84:87]\n\t"
    "v_mfma_f32_16x16x32_bf16 v[88:91], v[232:235], v[108:111], v[88:91]\n\t"
    "v_xor_b32 %[ea], 0x10000, %[ea]\n\t"
    "v_mfma_f32_16x16x32_bf16 v[92:95], v[236:239], v[108:111], v[92:95]\n\t"
    "v_xor_b32 %[da], 0x10000, %[da]\n\t"
    "s_waitcnt lgkmcnt(2)\n\t"
    "v_mfma_f32_16x16x32_bf16 v[80:83], v[240:243], v[112:115], v[80:83]\n\t"
    "v_mfma_f32_16x16x32_bf16 v[84:87], v[244:247], v[112:115], v[84:87]\n\t"
    "v_mfma_f32_16x16x32_bf16 v[88:91], v[240:243], v[116:119], v[88:91]\n\t"
    "s_add_u32 %[b], %[b], 1\n\t"
    "v_mfma_f32_16x16x32_bf16 v[92:95], v[244:247], v[116:119], v[92:95]\n\t"
    "s_waitcnt lgkmcnt(0)\n\t"
    "v_mfma_f32_16x16x32_bf16 v[80:83], v[248:251], v[120:123], v[80:83]\n\t"
    "v_mfma_f32_16x16x32_bf16 v[84:87], v[252:255], v[120:123], v[84:87]\n\t"
    "v_mfma_f32_16x16x32_bf16 v[88:91], v[248:251], v[124:127], v[88:91]\n\t"
    "v_mfma_f32_16x16x32_bf16 v[92:95], v[252:255], v[124:127], v[92:95]\n\t"
    "s_nop 4\n\t"
    "ds_write_b128 %[rw], v[80:83] offset:0\n\t"
    "ds_write_b128 %[rw], v[84:87] offset:1024\n\t"
    "ds_write_b128 %[rw], v[88:91] offset:2048\n\t"
    "ds_write_b128 %[rw], v[92:95] offset:3072\n\t"
    "s_waitcnt vmcnt(0)\n\t"
    "ds_read2_b32 v[78:79], %[sa] offset1:32\n\t"
    "s_waitcnt lgkmcnt(0)\n\t"
    "v_max_u32 v78, v78, v79\n\t"
    "v_mad_u32_u24 v78, v78, %[mu], %[ad]\n\t"
    "v_max_i32 v77, v77, v78\n\t"
    "v_mov_b32 v76, %[b]\n\t"
    "v_cmp_ge_i32 vcc, %[b], v77\n\t"
    "s_cmp_ge_i32 %[b], %[ns]\n\t"
    "s_cbranch_scc1 6f\n\t"
    "s_cbranch_vccnz 6f\n\t"
    "v_or_b32 v76, 0x100, v76\n\t"
    "ds_write_b32 %[fa], v76\n\t"
    "s_branch 4b\n\t"
    "6:\n\t"
    "ds_write_b32 %[fa], v76\n\t"
    "s_cbranch_scc0 21b\n\t"
    "s_waitcnt lgkmcnt(0)\n\t"
    "s_barrier\n\t"
    "s_branch 9f\n\t"
    "5:\n\t"
    "s_waitcnt lgkmcnt(4)\n\t"
    "v_mfma_f32_16x16x32_bf16 v[80:83], v[128:131], v[96:99], 0\n\t"
    "ds_read_b128 v[120:123], %[da] offset:4096\n\t"
    "s_add_u32 m0, %[lb], 0\n\t"
    "v_mfma_f32_16x16x32_bf16 v[84:87], v[132:135], v[96:99], 0\n\t"
    "ds_read_b128 v[124:127], %[da] offset:6144\n\t"
    "global_load_lds_dwordx4 %[av0], s[36:37] sc1\n\t"
    "v_readfirstlane_b32 %[st], v75\n\t"
    "v_mfma_f32_16x16x32_bf16 v[88:91], v[128:131], v[100:103], 0\n\t"
    "v_mfma_f32_16x16x32_bf16 v[92:95], v[132:135], v[100:103], 0\n\t"
    "s_cmp_lg_u32 %[st], 0\n\t"
    "s_cbranch_scc1 9f\n\t"
    "s_waitcnt vmcnt(5)\n\t"
    "s_barrier\n\t"
    "s_waitcnt lgkmcnt(4)\n\t"
    "v_mfma_f32_16x16x32_bf16 v[80:83], v[136:139], v[104:107], v[80:83]\n\t"
    "ds_read_b128 v[96:99], %[ea] offset:8192\n\t"
    "s_add_u32 m0, %[lb], 4096\n\t"
    "v_mfma_f32_16x16x32_bf16 v[84:87], v[140:143], v[104:107], v[84:87]\n\t"
    "ds_read_b128 v[100:103], %[ea] offset:10240\n\t"
    "global_load_lds_dwordx4 %[av2], s[36:37] sc1\n\t"
    "v_mfma_f32_16x16x32_bf16 v[88:91], v[136:139], v[108:111], v[88:91]\n\t"
    "v_mfma_f32_16x16x32_bf16 v[92:95], v[140:143], v[108:111], v[92:95]\n\t"
    "s_waitcnt lgkmcnt(4)\n\t"
    "v_mfma_f32_16x16x32_bf16 v[80:83], v[144:147], v[112:115], v[80:83]\n\t"
    "ds_read_b128 v[104:107], %[da] offset:8192\n\t"
    "s_add_u32 m0, %[lb], 8192\n\t"
    "v_mfma_f32_16x16x32_bf16 v[84:87], v[148:151], v[112:115], v[84:87]\n\t"
    "ds_read_b128 v[108:111], %[da] offset:10240\n\t"
    "global_load_lds_dwordx4 %[av4], s[36:37] sc1\n\t"
    "v_mfma_f32_16x16x32_bf16 v[88:91], v[144:147], v[116:119], v[88:91]\n\t"
    "v_mfma_f32_16x16x32_bf16 v[92:95], v[148:151], v[116:119], v[92:95]\n\t"
    "s_waitcnt lgkmcnt(4)\n\t"
    "v_mfma_f32_16x16x32_bf16 v[80:83], v[152:155], v[120:123], v[80:83]\n\t"
    "ds_read_b128 v[112:115], %[ea] offset:12288\n\t"
    "s_add_u32 m0, %[lb], 12288\n\t"
    "v_mfma_f32_16x16x32_bf16 v[84:87], v[156:159], v[120:123], v[84:87]\n\t"
    "ds_read_b128 v[116:119], %[ea] offset:14336\n\t"
    "global_load_lds_dwordx4 %[av6], s[36:37] sc1\n\t"
    "v_mfma_f32_16x16x32_bf16 v[88:91], v[152:155], v[124:127], v[88:91]\n\t"
    "v_mfma_f32_16x16x32_bf16 v[92:95], v[156:159], v[124:127], v[92:95]\n\t"
    "s_waitcnt lgkmcnt(4)\n\t"
    "v_mfma_f32_16x16x32_bf16 v[80:83], v[160:163], v[96:99], v[80:83]\n\t"
    "ds_read_b128 v[120:123], %[da] offset:12288\n\t"
    "s_add_u32 m0, %[lb], 16384\n\t"
    "v_mfma_f32_16x16x32_bf16 v[84:87], v[164:167], v[96:99], v[84:87]\n\t"
    "ds_read_b128 v[124:127], %[da] offset:14336\n\t"
    "global_load_lds_dwordx4 %[av0], s[38:39] sc1\n\t"
    "v_mfma_f32_16x16x32_bf16 v[88:91], v[160:163], v[100:103], v[88:91]\n\t"
    "v_mfma_f32_16x16x32_bf16 v[92:95], v[164:167], v[100:103], v[92:95]\n\t"
    "s_waitcnt vmcnt(7)\n\t"
    "s_barrier\n\t"
    "s_waitcnt lgkmcnt(4)\n\t"
    "v_mfma_f32_16x16x32_bf16 v[80:83], v[168:171], v[104:107], v[80:83]\n\t"
    "ds_read_b128 v[96:99], %[ea] offset:16384\n\t"
    "s_add_u32 m0, %[lb], 20480\n\t"
    "v_mfma_f32_16x16x32_bf16 v[84:87], v[172:175], v[104:107], v[84:87]\n\t"
    "ds_read_b128 v[100:103], %[ea] offset:18432\n\t"
    "global_load_lds_dwordx4 %[av2], s[38:39] sc1\n\t"
    "v_mfma_f32_16x16x32_bf16 v[88:91], v[168:171], v[108:111], v[88:91]\n\t"
    "v_mfma_f32_16x16x32_bf16 v[92:95], v[172:175], v[108:111], v[92:95]\n\t"
    "s_waitcnt lgkmcnt(4)\n\t"
    "v_mfma_f32_16x16x32_bf16 v[80:83], v[176:179], v[112:115], v[80:83]\n\t"
    "ds_read_b128 v[104:107], %[da] offset:16384\n\t"
    "s_add_u32 m0, %[lb], 24576\n\t"
    "v_mfma_f32_16x16x32_bf16 v[84:87], v[180:183], v[112:115], v[84:87]\n\t"
    "ds_read_b128 v[108:111], %[da] offset:18432\n\t"
    "global_load_lds_dwordx4 %[av4], s[38:39] sc1\n\t"
    "v_mfma_f32_16x16x32_bf16 v[88:91], v[176:179], v[116:119], v[88:91]\n\t"
    "v_mfma_f32_16x16x32_bf16 v[92:95], v[180:183], v[116:119], v[92:95]\n\t"
    "s_waitcnt lgkmcnt(4)\n\t"
    "v_mfma_f32_16x16x32_bf16 v[80:83], v[184:187], v[120:123], v[80:83]\n\t"
    "ds_read_b128 v[112:115], %[ea] offset:20480\n\t"
    "s_add_u32 m0, %[lb], 28672\n\t"
    "v_mfma_f32_16x16x32_bf16 v[84:87], v[188:191], v[120:123], v[84:87]\n\t"
    "ds_read_b128 v[116:119], %[ea] offset:22528\n\t"
    "global_load_lds_dwordx4 %[av6], s[38:39] sc1\n\t"
    "v_mfma_f32_16x16x32_bf16 v[88:91], v[184:187], v[124:127], v[88:91]\n\t"
    "v_mfma_f32_16x16x32_bf16 v[92:95], v[188:191], v[124:127], v[92:95]\n\t"
    "s_waitcnt lgkmcnt(4)\n\t"
    "v_mfma_f32_16x16x32_bf16 v[80:83], v[192:195], v[96:99], v[80:83]\n\t"
    "ds_read_b128 v[120:123], %[da] offset:20480\n\t"
    "v_mfma_f32_16x16x32_bf16 v[84:87], v[196:199], v[96:99], v[84:87]\n\t"
    "ds_read_b128 v[124:127], %[da] offset:22528\n\t"
    "v_mfma_f32_16x16x32_bf16 v[88:91], v[192:195], v[100:103], v[88:91]\n\t"
    "v_mfma_f32_16x16x32_bf16 v[92:95], v[196:199], v[100:103], v[92:95]\n\t"
    "s_add_u32 s36, s36, %[ab]\n\t"
    "s_addc_u32 s37, s37, 0\n\t"
    "s_add_u32 s38, s38, %[ab]\n\t"
    "s_addc_u32 s39, s39, 0\n\t"
    "s_xor_b32 %[lb], %[lb], 0x10000\n\t"
    "s_waitcnt vmcnt(8)\n\t"
    "s_barrier\n\t"
    "s_waitcnt lgkmcnt(4)\n\t"
    "v_mfma_f32_16x16x32_bf16 v[80:83], v[200:203], v[104:107], v[80:83]\n\t"
    "ds_read_b128 v[96:99], %[ea] offset:24576\n\t"
    "v_mfma_f32_16x16x32_bf16 v[84:87], v[204:207], v[104:107], v[84:87]\n\t"
    "ds_read_b128 v[100:103], %[ea] offset:26624\n\t"
    "v_mfma_f32_16x16x32_bf16 v[88:91], v[200:203], v[108:111], v[88:91]\n\t"
    "v_mfma_f32_16x16x32_bf16 v[92:95], v[204:207], v[108:111], v[92:95]\n\t"
    "s_cmp_eq_u32 %[sl], 0\n\t"
    "s_cbranch_scc1 73f\n\t"
    "s_mov_b32 m0, %[sl]\n\t"
    "s_nop 0\n\t"
    "global_load_lds_dword %[dv], %[db] sc1\n\t"
    "73:\n\t"
    "s_waitcnt lgkmcnt(4)\n\t"
    "v_mfma_f32_16x16x32_bf16 v[80:83], v[208:211], v[112:115], v[80:83]\n\t"
    "ds_read_b128 v[104:107], %[da] offset:24576\n\t"
    "v_mfma_f32_16x16x32_bf16 v[84:87], v[212:215], v[112:115], v[84:87]\n\t"
    "ds_read_b128 v[108:111], %[da] offset:26624\n\t"
    "v_mfma_f32_16x16x32_bf16 v[88:91], v[208:211], v[116:119], v[88:91]\n\t"
    "v_mfma_f32_16x16x32_bf16 v[92:95], v[212:215], v[116:119], v[92:95]\n\t"
    "s_waitcnt lgkmcnt(4)\n\t"
    "v_mfma_f32_16x16x32_bf16 v[80:83], v[216:219], v[120:123], v[80:83]\n\t"
    "ds_read_b128 v[112:115], %[ea] offset:28672\n\t"
    "v_mfma_f32_16x16x32_bf16 v[84:87], v[220:223], v[120:123], v[84:87]\n\t"
    "ds_read_b128 v[116:119], %[ea] offset:30720\n\t"
    "v_mfma_f32_16x16x32_bf16 v[88:91], v[216:219], v[124:127], v[88:91]\n\t"
    "v_mfma_f32_16x16x32_bf16 v[92:95], v[220:223], v[124:127], v[92:95]\n\t"
    "s_waitcnt lgkmcnt(4)\n\t"
    "v_mfma_f32_16x16x32_bf16 v[80:83], v[224:227], v[96:99], v[80:83]\n\t"
    "ds_read_b128 v[120:123], %[da] offset:28672\n\t"
    "v_mfma_f32_16x16x32_bf16 v[84:87], v[228:231], v[96:99], v[84:87]\n\t"
    "ds_read_b128 v[124:127], %[da] offset:30720\n\t"
    "v_mfma_f32_16x16x32_bf16 v[88:91], v[224:227], v[100:103], v[88:91]\n\t"
    "v_mfma_f32_16x16x32_bf16 v[92:95], v[228:231], v[100:103], v[92:95]\n\t"
    "s_waitcnt lgkmcnt(4)\n\t"
    "v_mfma_f32_16x16x32_bf16 v[80:83], v[232:235], v[104:107], v[80:83]\n\t"
    "v_mfma_f32_16x16x32_bf16 v[84:87], v[236:239], v[104:107], v[84:87]\n\t"
    "v_mfma_f32_16x16x32_bf16 v[88:91], v[232:235], v[108:111], v[88:91]\n\t"
    "v_xor_b32 %[ea], 0x10000, %[ea]\n\t"
    "v_mfma_f32_16x16x32_bf16 v[92:95], v[236:239], v[108:111], v[92:95]\n\t"
    "v_xor_b32 %[da], 0x10000, %[da]\n\t"
    "s_waitcnt lgkmcnt(2)\n\t"
    "v_mfma_f32_16x16x32_bf16 v[80:83], v[240:243], v[112:115], v[80:83]\n\t"
    "v_mfma_f32_16x16x32_bf16 v[84:87], v[244:247], v[112:115], v[84:87]\n\t"
    "v_mfma_f32_16x16x32_bf16 v[88:91], v[240:243], v[116:119], v[88:91]\n\t"
    "s_add_u32 %[b], %[b], 1\n\t"
    "v_mfma_f32_16x16x32_bf16 v[92:95], v[244:247], v[116:119], v[92:95]\n\t"
    "s_waitcnt lgkmcnt(0)\n\t"
    "v_mfma_f32_16x16x32_bf16 v[80:83], v[248:251], v[120:123], v[80:83]\n\t"
    "v_mfma_f32_16x16x32_bf16 v[84:87], v[252:255], v[120:123], v[84:87]\n\t"
    "v_mfma_f32_16x16x32_bf16 v[88:91], v[248:251], v[124:127], v[88:91]\n\t"
    "v_mfma_f32_16x16x32_bf16 v[92:95], v[252:255], v[124:127], v[92:95]\n\t"
    "v_mov_b32 v76, %[b]\n\t"
    "v_or_b32 v76, 0x100, v76\n\t"
    "s_nop 2\n\t"
    "ds_write_b128 %[rw], v[80:83] offset:0\n\t"
    "ds_write_b128 %[rw], v[84:87] offset:1024\n\t"
    "ds_write_b128 %[rw], v[88:91] offset:2048\n\t"
    "ds_write_b128 %[rw], v[92:95] offset:3072\n\t"
    "ds_write_b32 %[fa], v76\n\t"
    "s_waitcnt vmcnt(0)\n\t"
    "ds_read2_b32 v[78:79], %[sa] offset1:32\n\t"
    "s_waitcnt lgkmcnt(0)\n\t"
    "v_max_u32 v78, v78, v79\n\t"
    "v_mad_u32_u24 v78, v78, %[mu], %[ad]\n\t"
    "v_max_i32 v77, v77, v78\n\t"
    "s_branch 3b\n\t"
    "9:\n\t"
    "s_waitcnt lgkmcnt(0)\n\t"
    "s_nop 7"
    : [ea] "+&v"(ea), [da] "+&v"(da), [b] "+&s"(b), [st] "=&s"(st), "+{s[36:37]}"(p0), "+{s[38:39]}"(p1), [lb] "+&s"(lb), [cap] "+&s"(cap)
    : [ns] "s"(ns), [ab] "s"(ab), [rw] "v"(rw), [fa] "v"(fa), "{v[128:159]}"(W[0]), "{v[160:191]}"(W[1]), "{v[192:223]}"(W[2]), "{v[224:255]}"(W[3]), [wm] "s"(wm), [rq] "s"(rq), [mu] "s"(mu), [ad] "v"(ad), [dv] "v"(dv), [db] "s"(db), [sl] "s"(sl), [sa] "v"(sa), [av0] "v"(av[0]), [av1] "v"(av[1]), [av2] "v"(av[2]), [av3] "v"(av[3]), [av4] "v"(av[4]), [av5] "v"(av[5]), [av6] "v"(av[6]), [av7] "v"(av[7])
    : "v75", "v76", "v77", "v78", "v79", "v80", "v81", "v82", "v83", "v84", "v85", "v86", "v87", "v88", "v89", "v90", "v91", "v92", "v93", "v94", "v95", "v96", "v97", "v98", "v99", "v100", "v101", "v102", "v103", "v104", "v105", "v106", "v107", "v108", "v109", "v110", "v111", "v112", "v113", "v114", "v115", "v116", "v117", "v118", "v119", "v120", "v121", "v122", "v123", "v124", "v125", "v126", "v127", "vcc", "scc", "m0", "memory");
}
__device__ __forceinline__ void paced_st0(const u32x32 (&W)[4], unsigned ea, unsigned da, int b, int ns, unsigned ab, unsigned rw, unsigned fa,
    const char* p0, unsigned pp, int be, int po, unsigned cn, const char* pg, unsigned co0, unsigned co1) {
  unsigned st;
  asm volatile("1:\n\t"
    "s_barrier\n\t"
    "ds_read_b128 v[96:99], %[ea] offset:0\n\t"
    "ds_read_b128 v[100:103], %[ea] offset:2048\n\t"
    "ds_read_b128 v[104:107], %[da] offset:0\n\t"
    "ds_read_b128 v[108:111], %[da] offset:2048\n\t"
    "ds_read_b128 v[112:115], %[ea] offset:4096\n\t"
    "ds_read_b128 v[116:119], %[ea] offset:6144\n\t"
    "s_waitcnt lgkmcnt(4)\n\t"
    "v_mfma_f32_16x16x32_bf16 v[80:83], v[128:131], v[96:99], 0\n\t"
    "ds_read_b128 v[120:123], %[da] offset:4096\n\t"
    "v_mfma_f32_16x16x32_bf16 v[84:87], v[132:135], v[96:99], 0\n\t"
    "ds_read_b128 v[124:127], %[da] offset:6144\n\t"
    "v_mfma_f32_16x16x32_bf16 v[88:91], v[128:131], v[100:103], 0\n\t"
    "v_mfma_f32_16x16x32_bf16 v[92:95], v[132:135], v[100:103], 0\n\t"
    "s_barrier\n\t"
    "s_waitcnt lgkmcnt(4)\n\t"
    "v_mfma_f32_16x16x32_bf16 v[80:83], v[136:139], v[104:107], v[80:83]\n\t"
    "ds_read_b128 v[96:99], %[ea] offset:8192\n\t"
    "v_mfma_f32_16x16x32_bf16 v[84:87], v[140:143], v[104:107], v[84:87]\n\t"
    "ds_read_b128 v[100:103], %[ea] offset:10240\n\t"
    "v_mfma_f32_16x16x32_bf16 v[88:91], v[136:139], v[108:111], v[88:91]\n\t"
    "v_mfma_f32_16x16x32_bf16 v[92:95], v[140:143], v[108:111], v[92:95]\n\t"
    "s_waitcnt lgkmcnt(4)\n\t"
    "v_mfma_f32_16x16x32_bf16 v[80:83], v[144:147], v[112:115], v[80:83]\n\t"
    "ds_read_b128 v[104:107], %[da] offset:8192\n\t"
    "v_mfma_f32_16x16x32_bf16 v[84:87], v[148:151], v[112:115], v[84:87]\n\t"
    "ds_read_b128 v[108:111], %[da] offset:10240\n\t"
    "v_mfma_f32_16x16x32_bf16 v[88:91], v[144:147], v[116:119], v[88:91]\n\t"
    "v_mfma_f32_16x16x32_bf16 v[92:95], v[148:151], v[116:119], v[92:95]\n\t"
    "s_waitcnt lgkmcnt(4)\n\t"
    "v_mfma_f32_16x16x32_bf16 v[80:83], v[152:155], v[120:123], v[80:83]\n\t"
    "ds_read_b128 v[112:115], %[ea] offset:12288\n\t"
    "v_mfma_f32_16x16x32_bf16 v[84:87], v[156:159], v[120:123], v[84:87]\n\t"
    "ds_read_b128 v[116:119], %[ea] offset:14336\n\t"
    "v_mfma_f32_16x16x32_bf16 v[88:91], v[152:155], v[124:127], v[88:91]\n\t"
    "v_mfma_f32_16x16x32_bf16 v[92:95], v[156:159], v[124:127], v[92:95]\n\t"
    "s_cmp_lg_u32 %[pp], 0\n\t"
    "s_cbranch_scc1 7f\n\t"
    "8:\n\t"
    "s_waitcnt lgkmcnt(4)\n\t"
    "v_mfma_f32_16x16x32_bf16 v[80:83], v[160:163], v[96:99], v[80:83]\n\t"
    "ds_read_b128 v[120:123], %[da] offset:12288\n\t"
    "v_mfma_f32_16x16x32_bf16 v[84:87], v[164:167], v[96:99], v[84:87]\n\t"
    "ds_read_b128 v[124:127], %[da] offset:14336\n\t"
    "v_mfma_f32_16x16x32_bf16 v[88:91], v[160:163], v[100:103], v[88:91]\n\t"
    "v_mfma_f32_16x16x32_bf16 v[92:95], v[164:167], v[100:103], v[92:95]\n\t"
    "s_barrier\n\t"
    "s_waitcnt lgkmcnt(4)\n\t"
    "v_mfma_f32_16x16x32_bf16 v[80:83], v[168:171], v[104:107], v[80:83]\n\t"
    "ds_read_b128 v[96:99], %[ea] offset:16384\n\t"
    "v_mfma_f32_16x16x32_bf16 v[84:87], v[172:175], v[104:107], v[84:87]\n\t"
    "ds_read_b128 v[100:103], %[ea] offset:18432\n\t"
    "v_mfma_f32_16x16x32_bf16 v[88:91], v[168:171], v[108:111], v[88:91]\n\t"
    "v_mfma_f32_16x16x32_bf16 v[92:95], v[172:175], v[108:111], v[92:95]\n\t"
    "s_waitcnt lgkmcnt(4)\n\t"
    "v_mfma_f32_16x16x32_bf16 v[80:83], v[176:179], v[112:115], v[80:83]\n\t"
    "ds_read_b128 v[104:107], %[da] offset:16384\n\t"
    "v_mfma_f32_16x16x32_bf16 v[84:87], v[180:183], v[112:115], v[84:87]\n\t"
    "ds_read_b128 v[108:111], %[da] offset:18432\n\t"
    "v_mfma_f32_16x16x32_bf16 v[88:91], v[176:179], v[116:119], v[88:91]\n\t"
    "v_mfma_f32_16x16x32_bf16 v[92:95], v[180:183], v[116:119], v[92:95]\n\t"
    "s_waitcnt lgkmcnt(4)\n\t"
    "v_mfma_f32_16x16x32_bf16 v[80:83], v[184:187], v[120:123], v[80:83]\n\t"
    "ds_read_b128 v[112:115], %[ea] offset:20480\n\t"
    "v_mfma_f32_16x16x32_bf16 v[84:87], v[188:191], v[120:123], v[84:87]\n\t"
    "ds_read_b128 v[116:119], %[ea] offset:22528\n\t"
    "v_mfma_f32_16x16x32_bf16 v[88:91], v[184:187], v[124:127], v[88:91]\n\t"
    "v_mfma_f32_16x16x32_bf16 v[92:95], v[188:191], v[124:127], v[92:95]\n\t"
    "s_waitcnt lgkmcnt(4)\n\t"
    "v_mfma_f32_16x16x32_bf16 v[80:83], v[192:195], v[96:99], v[80:83]\n\t"
    "ds_read_b128 v[120:123], %[da] offset:20480\n\t"
    "v_mfma_f32_16x16x32_bf16 v[84:87], v[196:199], v[96:99], v[84:87]\n\t"
    "ds_read_b128 v[124:127], %[da] offset:22528\n\t"
    "v_mfma_f32_16x16x32_bf16 v[88:91], v[192:195], v[100:103], v[88:91]\n\t"
    "v_mfma_f32_16x16x32_bf16 v[92:95], v[196:199], v[100:103], v[92:95]\n\t"
    "s_barrier\n\t"
    "s_waitcnt lgkmcnt(4)\n\t"
    "v_mfma_f32_16x16x32_bf16 v[80:83], v[200:203], v[104:107], v[80:83]\n\t"
    "ds_read_b128 v[96:99], %[ea] offset:24576\n\t"
    "v_mfma_f32_16x16x32_bf16 v[84:87], v[204:207], v[104:107], v[84:87]\n\t"
    "ds_read_b128 v[100:103], %[ea] offset:26624\n\t"
    "v_mfma_f32_16x16x32_bf16 v[88:91], v[200:203], v[108:111], v[88:91]\n\t"
    "v_mfma_f32_16x16x32_bf16 v[92:95], v[204:207], v[108:111], v[92:95]\n\t"
    "s_waitcnt lgkmcnt(4)\n\t"
    "v_mfma_f32_16x16x32_bf16 v[80:83], v[208:211], v[112:115], v[80:83]\n\t"
    "ds_read_b128 v[104:107], %[da] offset:24576\n\t"
    "v_mfma_f32_16x16x32_bf16 v[84:87], v[212:215], v[112:115], v[84:87]\n\t"
    "ds_read_b128 v[108:111], %[da] offset:26624\n\t"
    "v_mfma_f32_16x16x32_bf16 v[88:91], v[208:211], v[116:119], v[88:91]\n\t"
    "v_mfma_f32_16x16x32_bf16 v[92:95], v[212:215], v[116:119], v[92:95]\n\t"
    "s_waitcnt lgkmcnt(4)\n\t"
    "v_mfma_f32_16x16x32_bf16 v[80:83], v[216:219], v[120:123], v[80:83]\n\t"
    "ds_read_b128 v[112:115], %[ea] offset:28672\n\t"
    "v_mfma_f32_16x16x32_bf16 v[84:87], v[220:223], v[120:123], v[84:87]\n\t"
    "ds_read_b128 v[116:119], %[ea] offset:30720\n\t"
    "v_mfma_f32_16x16x32_bf16 v[88:91], v[216:219], v[124:127], v[88:91]\n\t"
    "v_mfma_f32_16x16x32_bf16 v[92:95], v[220:223], v[124:127], v[92:95]\n\t"
    "s_waitcnt lgkmcnt(4)\n\t"
    "v_mfma_f32_16x16x32_bf16 v[80:83], v[224:227], v[96:99], v[80:83]\n\t"
    "ds_read_b128 v[120:123], %[da] offset:28672\n\t"
    "v_mfma_f32_16x16x32_bf16 v[84:87], v[228:231], v[96:99], v[84:87]\n\t"
    "ds_read_b128 v[124:127], %[da] offset:30720\n\t"
    "v_mfma_f32_16x16x32_bf16 v[88:91], v[224:227], v[100:103], v[88:91]\n\t"
    "v_mfma_f32_16x16x32_bf16 v[92:95], v[228:231], v[100:103], v[92:95]\n\t"
    "s_waitcnt lgkmcnt(4)\n\t"
    "v_mfma_f32_16x16x32_bf16 v[80:83], v[232:235], v[104:107], v[80:83]\n\t"
    "v_mfma_f32_16x16x32_bf16 v[84:87], v[236:239], v[104:107], v[84:87]\n\t"
    "v_mfma_f32_16x16x32_bf16 v[88:91], v[232:235], v[108:111], v[88:91]\n\t"
    "v_xor_b32 %[ea], 0x10000, %[ea]\n\t"
    "v_mfma_f32_16x16x32_bf16 v[92:95], v[236:239], v[108:111], v[92:95]\n\t"
    "v_xor_b32 %[da], 0x10000, %[da]\n\t"
    "s_waitcnt lgkmcnt(2)\n\t"
    "v_mfma_f32_16x16x32_bf16 v[80:83], v[240:243], v[112:115], v[80:83]\n\t"
    "v_mfma_f32_16x16x32_bf16 v[84:87], v[244:247], v[112:115], v[84:87]\n\t"
    "v_mfma_f32_16x16x32_bf16 v[88:91], v[240:243], v[116:119], v[88:91]\n\t"
    "s_add_u32 %[b], %[b], 1\n\t"
    "v_mfma_f32_16x16x32_bf16 v[92:95], v[244:247], v[116:119], v[92:95]\n\t"
    "s_waitcnt lgkmcnt(0)\n\t"
    "v_mfma_f32_16x16x32_bf16 v[80:83], v[248:251], v[120:123], v[80:83]\n\t"
    "v_mfma_f32_16x16x32_bf16 v[84:87], v[252:255], v[120:123], v[84:87]\n\t"
    "v_mfma_f32_16x16x32_bf16 v[88:91], v[248:251], v[124:127], v[88:91]\n\t"
    "v_mfma_f32_16x16x32_bf16 v[92:95], v[252:255], v[124:127], v[92:95]\n\t"
    "2:\n\t"
    "ds_read_b32 v39, %[fa]\n\t"
    "ds_read_b128 v[48:51], %[rw] offset:0\n\t"
    "ds_read_b128 v[56:59], %[rw] offset:2048\n\t"
    "ds_read_b128 v[52:55], %[rw] offset:1024\n\t"
    "ds_read_b128 v[60:63], %[rw] offset:3072\n\t"
    "s_waitcnt lgkmcnt(2)\n\t"
    "v_readfirstlane_b32 %[pp], v39\n\t"
    "s_and_b32 %[st], %[pp], 0xff\n\t"
    "s_cmp_eq_u32 %[st], %[b]\n\t"
    "s_cbranch_scc1 3f\n\t"
    "s_waitcnt lgkmcnt(0)\n\t"
    "s_branch 2b\n\t"
    "3:\n\t"
    "v_add_f32 v48, v48, v80\n\t"
    "v_add_f32 v49, v49, v81\n\t"
    "v_add_f32 v50, v50, v82\n\t"
    "v_add_f32 v51, v51, v83\n\t"
    "v_add_f32 v56, v56, v88\n\t"
    "v_add_f32 v57, v57, v89\n\t"
    "v_add_f32 v58, v58, v90\n\t"
    "v_add_f32 v59, v59, v91\n\t"
    "v_cmp_u_f32 vcc, v48, v56\n\t"
    "s_waitcnt lgkmcnt(0)\n\t"
    "v_add_f32 v52, v52, v84\n\t"
    "v_add_f32 v53, v53, v85\n\t"
    "v_add_f32 v54, v54, v86\n\t"
    "v_add_f32 v55, v55, v87\n\t"
    "v_add_f32 v60, v60, v92\n\t"
    "v_add_f32 v61, v61, v93\n\t"
    "v_add_f32 v62, v62, v94\n\t"
    "v_add_f32 v63, v63, v95\n\t"
    "s_cmp_lg_u64 vcc, 0\n\t"
    "s_cselect_b32 %[st], %[b], 0\n\t"
    "s_cmp_gt_i32 %[st], %[be]\n\t"
    "s_cbranch_scc1 4f\n\t"
    "v_cvt_pk_bf16_f32 v40, v48, v49\n\t"
    "v_cvt_pk_bf16_f32 v41, v50, v51\n\t"
    "v_cvt_pk_bf16_f32 v42, v52, v53\n\t"
    "v_cvt_pk_bf16_f32 v43, v54, v55\n\t"
    "global_store_dwordx4 %[co0], v[40:43], s[36:37] sc1\n\t"
    "v_cvt_pk_bf16_f32 v44, v56, v57\n\t"
    "v_cvt_pk_bf16_f32 v45, v58, v59\n\t"
    "v_cvt_pk_bf16_f32 v46, v60, v61\n\t"
    "v_cvt_pk_bf16_f32 v47, v62, v63\n\t"
    "global_store_dwordx4 %[co1], v[44:47], s[36:37] sc1\n\t"
    "s_add_u32 s36, s36, %[ab]\n\t"
    "s_addc_u32 s37, s37, 0\n\t"
    "s_cmp_le_i32 %[b], %[be]\n\t"
    "s_cbranch_scc1 5f\n\t"
    "v_mov_b32 v72, %[cn]\n\t"
    "v_mov_b32 v73, 1\n\t"
    "s_waitcnt vmcnt(2)\n\t"
    "s_mov_b64 exec, 1\n\t"
    "ds_add_rtn_u32 v73, v72, v73\n\t"
    "s_add_u32 %[st], %[b], %[po]\n\t"
    "v_mov_b32 v75, %[st]\n\t"
    "v_add_u32 v74, -1, v75\n\t"
    "v_mov_b32 v72, 0\n\t"
    "s_waitcnt lgkmcnt(0)\n\t"
    "v_readfirstlane_b32 %[st], v73\n\t"
    "s_and_b32 %[st], %[st], 3\n\t"
    "s_cmp_lg_u32 %[st], 3\n\t"
    "s_cbranch_scc1 61f\n\t"
    "global_store_dwordx2 v72, v[74:75], %[pg] sc1\n\t"
    "61:\n\t"
    "s_mov_b64 exec, -1\n\t"
    "s_mov_b32 %[pp], 0\n\t"
    "s_mov_b32 %[pp], 0\n\t"
    "s_cmp_lt_i32 %[b], %[ns]\n\t"
    "s_cbranch_scc1 1b\n\t"
    "s_barrier\n\t"
    "s_branch 9f\n\t"
    "5:\n\t"
    "s_lshr_b32 %[pp], %[pp], 8\n\t"
    "s_cbranch_scc1 1b\n\t"
    "v_mov_b32 v72, %[cn]\n\t"
    "v_mov_b32 v73, 1\n\t"
    "s_waitcnt vmcnt(0)\n\t"
    "s_mov_b64 exec, 1\n\t"
    "ds_add_rtn_u32 v73, v72, v73\n\t"
    "s_add_u32 %[st], %[b], %[po]\n\t"
    "v_mov_b32 v75, %[st]\n\t"
    "v_add_u32 v74, 0, v75\n\t"
    "v_mov_b32 v72, 0\n\t"
    "s_waitcnt lgkmcnt(0)\n\t"
    "v_readfirstlane_b32 %[st], v73\n\t"
    "s_and_b32 %[st], %[st], 3\n\t"
    "s_cmp_lg_u32 %[st], 3\n\t"
    "s_cbranch_scc1 61f\n\t"
    "global_store_dword v72, v74, %[pg] sc1\n\t"
    "61:\n\t"
    "s_mov_b64 exec, -1\n\t"
    "s_mov_b32 %[pp], 0\n\t"
    "s_cmp_lt_i32 %[b], %[ns]\n\t"
    "s_cbranch_scc1 1b\n\t"
    "s_barrier\n\t"
    "s_branch 9f\n\t"
    "4:\n\t"
    "v_mov_b32 v39, %[b]\n\t"
    "ds_write_b32 %[fa], v39 offset:16\n\t"
    "s_waitcnt lgkmcnt(0)\n\t"
    "s_barrier\n\t"
    "s_branch 9f\n\t"
    "7:\n\t"
    "v_mov_b32 v72, %[cn]\n\t"
    "v_mov_b32 v73, 1\n\t"
    "s_waitcnt vmcnt(0)\n\t"
    "s_mov_b64 exec, 1\n\t"
    "ds_add_rtn_u32 v73, v72, v73\n\t"
    "s_add_u32 %[st], %[b], %[po]\n\t"
    "v_mov_b32 v75, %[st]\n\t"
    "v_add_u32 v74, 0, v75\n\t"
    "v_mov_b32 v72, 0\n\t"
    "s_waitcnt lgkmcnt(0)\n\t"
    "v_readfirstlane_b32 %[st], v73\n\t"
    "s_and_b32 %[st], %[st], 3\n\t"
    "s_cmp_lg_u32 %[st], 3\n\t"
    "s_cbranch_scc1 61f\n\t"
    "global_store_dword v72, v74, %[pg] sc1\n\t"
    "61:\n\t"
    "s_mov_b64 exec, -1\n\t"
    "s_mov_b32 %[pp], 0\n\t"
    "s_branch 8b\n\t"
    "9:"
    : [ea] "+&v"(ea), [da] "+&v"(da), [b] "+&s"(b), [st] "=&s"(st), "+{s[36:37]}"(p0), [pp] "+&s"(pp)
    : [ns] "s"(ns), [ab] "s"(ab), [rw] "v"(rw), [fa] "v"(fa), "{v[128:159]}"(W[0]), "{v[160:191]}"(W[1]), "{v[192:223]}"(W[2]), "{v[224:255]}"(W[3]), [be] "s"(be), [po] "s"(po), [cn] "s"(cn), [pg] "s"(pg), [co0] "v"(co0), [co1] "v"(co1)
    : "v39", "v40", "v41", "v42", "v43", "v44", "v45", "v46", "v47", "v48", "v49", "v50", "v51", "v52", "v53", "v54", "v55", "v56", "v57", "v58", "v59", "v60", "v61", "v62", "v63", "v72", "v73", "v74", "v75", "v80", "v81", "v82", "v83", "v84", "v85", "v86", "v87", "v88", "v89", "v90", "v91", "v92", "v93", "v94", "v95", "v96", "v97", "v98", "v99", "v100", "v101", "v102", "v103", "v104", "v105", "v106", "v107", "v108", "v109", "v110", "v111", "v112", "v113", "v114", "v115", "v116", "v117", "v118", "v119", "v120", "v121", "v122", "v123", "v124", "v125", "v126", "v127", "vcc", "scc", "m0", "memory");
}
__device__ __forceinline__ void paced_st1(const u32x32 (&W)[4], unsigned ea, unsigned da, int b, int ns, unsigned ab, unsigned rw, unsigned fa,
    const char* p0, unsigned pp, int be, int po, unsigned cn, const char* pg, unsigned co0, unsigned co1) {
  unsigned st;
  asm volatile("1:\n\t"
    "s_barrier\n\t"
    "ds_read_b128 v[96:99], %[ea] offset:0\n\t"
    "ds_read_b128 v[100:103], %[ea] offset:2048\n\t"
    "ds_read_b128 v[104:107], %[da] offset:0\n\t"
    "ds_read_b128 v[108:111], %[da] offset:2048\n\t"
    "ds_read_b128 v[112:115], %[ea] offset:4096\n\t"
    "ds_read_b128 v[116:119], %[ea] offset:6144\n\t"
    "s_waitcnt lgkmcnt(4)\n\t"
    "v_mfma_f32_16x16x32_bf16 v[80:83], v[128:131], v[96:99], 0\n\t"
    "ds_read_b128 v[120:123], %[da] offset:4096\n\t"
    "v_mfma_f32_16x16x32_bf16 v[84:87], v[132:135], v[96:99], 0\n\t"
    "ds_read_b128 v[124:127], %[da] offset:6144\n\t"
    "v_mfma_f32_16x16x32_bf16 v[88:91], v[128:131], v[100:103], 0\n\t"
    "v_mfma_f32_16x16x32_bf16 v[92:95], v[132:135], v[100:103], 0\n\t"
    "s_barrier\n\t"
    "s_waitcnt lgkmcnt(4)\n\t"
    "v_mfma_f32_16x16x32_bf16 v[80:83], v[136:139], v[104:107], v[80:83]\n\t"
    "ds_read_b128 v[96:99], %[ea] offset:8192\n\t"
    "v_mfma_f32_16x16x32_bf16 v[84:87], v[140:143], v[104:107], v[84:87]\n\t"
    "ds_read_b128 v[100:103], %[ea] offset:10240\n\t"
    "v_mfma_f32_16x16x32_bf16 v[88:91], v[136:139], v[108:111], v[88:91]\n\t"
    "v_mfma_f32_16x16x32_bf16 v[92:95], v[140:143], v[108:111], v[92:95]\n\t"
    "s_waitcnt lgkmcnt(4)\n\t"
    "v_mfma_f32_16x16x32_bf16 v[80:83], v[144:147], v[112:115], v[80:83]\n\t"
    "ds_read_b128 v[104:107], %[da] offset:8192\n\t"
    "v_mfma_f32_16x16x32_bf16 v[84:87], v[148:151], v[112:115], v[84:87]\n\t"
    "ds_read_b128 v[108:111], %[da] offset:10240\n\t"
    "v_mfma_f32_16x16x32_bf16 v[88:91], v[144:147], v[116:119], v[88:91]\n\t"
    "v_mfma_f32_16x16x32_bf16 v[92:95], v[148:151], v[116:119], v[92:95]\n\t"
    "s_waitcnt lgkmcnt(4)\n\t"
    "v_mfma_f32_16x16x32_bf16 v[80:83], v[152:155], v[120:123], v[80:83]\n\t"
    "ds_read_b128 v[112:115], %[ea] offset:12288\n\t"
    "v_mfma_f32_16x16x32_bf16 v[84:87], v[156:159], v[120:123], v[84:87]\n\t"
    "ds_read_b128 v[116:119], %[ea] offset:14336\n\t"
    "v_mfma_f32_16x16x32_bf16 v[88:91], v[152:155], v[124:127], v[88:91]\n\t"
    "v_mfma_f32_16x16x32_bf16 v[92:95], v[156:159], v[124:127], v[92:95]\n\t"
    "s_cmp_lg_u32 %[pp], 0\n\t"
    "s_cbranch_scc1 7f\n\t"
    "8:\n\t"
    "s_waitcnt lgkmcnt(4)\n\t"
    "v_mfma_f32_16x16x32_bf16 v[80:83], v[160:163], v[96:99], v[80:83]\n\t"
    "ds_read_b128 v[120:123], %[da] offset:12288\n\t"
    "v_mfma_f32_16x16x32_bf16 v[84:87], v[164:167], v[96:99], v[84:87]\n\t"
    "ds_read_b128 v[124:127], %[da] offset:14336\n\t"
    "v_mfma_f32_16x16x32_bf16 v[88:91], v[160:163], v[100:103], v[88:91]\n\t"
    "v_mfma_f32_16x16x32_bf16 v[92:95], v[164:167], v[100:103], v[92:95]\n\t"
    "s_barrier\n\t"
    "s_waitcnt lgkmcnt(4)\n\t"
    "v_mfma_f32_16x16x32_bf16 v[80:83], v[168:171], v[104:107], v[80:83]\n\t"
    "ds_read_b128 v[96:99], %[ea] offset:16384\n\t"
    "v_mfma_f32_16x16x32_bf16 v[84:87], v[172:175], v[104:107], v[84:87]\n\t"
    "ds_read_b128 v[100:103], %[ea] offset:18432\n\t"
    "v_mfma_f32_16x16x32_bf16 v[88:91], v[168:171], v[108:111], v[88:91]\n\t"
    "v_mfma_f32_16x16x32_bf16 v[92:95], v[172:175], v[108:111], v[92:95]\n\t"
    "s_waitcnt lgkmcnt(4)\n\t"
    "v_mfma_f32_16x16x32_bf16 v[80:83], v[176:179], v[112:115], v[80:83]\n\t"
    "ds_read_b128 v[104:107], %[da] offset:16384\n\t"
    "v_mfma_f32_16x16x32_bf16 v[84:87], v[180:183], v[112:115], v[84:87]\n\t"
    "ds_read_b128 v[108:111], %[da] offset:18432\n\t"
    "v_mfma_f32_16x16x32_bf16 v[88:91], v[176:179], v[116:119], v[88:91]\n\t"
    "v_mfma_f32_16x16x32_bf16 v[92:95], v[180:183], v[116:119], v[92:95]\n\t"
    "s_waitcnt lgkmcnt(4)\n\t"
    "v_mfma_f32_16x16x32_bf16 v[80:83], v[184:187], v[120:123], v[80:83]\n\t"
    "ds_read_b128 v[112:115], %[ea] offset:20480\n\t"
    "v_mfma_f32_16x16x32_bf16 v[84:87], v[188:191], v[120:123], v[84:87]\n\t"
    "ds_read_b128 v[116:119], %[ea] offset:22528\n\t"
    "v_mfma_f32_16x16x32_bf16 v[88:91], v[184:187], v[124:127], v[88:91]\n\t"
    "v_mfma_f32_16x16x32_bf16 v[92:95], v[188:191], v[124:127], v[92:95]\n\t"
    "s_waitcnt lgkmcnt(4)\n\t"
    "v_mfma_f32_16x16x32_bf16 v[80:83], v[192:195], v[96:99], v[80:83]\n\t"
    "ds_read_b128 v[120:123], %[da] offset:20480\n\t"
    "v_mfma_f32_16x16x32_bf16 v[84:87], v[196:199], v[96:99], v[84:87]\n\t"
    "ds_read_b128 v[124:127], %[da] offset:22528\n\t"
    "v_mfma_f32_16x16x32_bf16 v[88:91], v[192:195], v[100:103], v[88:91]\n\t"
    "v_mfma_f32_16x16x32_bf16 v[92:95], v[196:199], v[100:103], v[92:95]\n\t"
    "s_barrier\n\t"
    "s_waitcnt lgkmcnt(4)\n\t"
    "v_mfma_f32_16x16x32_bf16 v[80:83], v[200:203], v[104:107], v[80:83]\n\t"
    "ds_read_b128 v[96:99], %[ea] offset:24576\n\t"
    "v_mfma_f32_16x16x32_bf16 v[84:87], v[204:207], v[104:107], v[84:87]\n\t"
    "ds_read_b128 v[100:103], %[ea] offset:26624\n\t"
    "v_mfma_f32_16x16x32_bf16 v[88:91], v[200:203], v[108:111], v[88:91]\n\t"
    "v_mfma_f32_16x16x32_bf16 v[92:95], v[204:207], v[108:111], v[92:95]\n\t"
    "s_waitcnt lgkmcnt(4)\n\t"
    "v_mfma_f32_16x16x32_bf16 v[80:83], v[208:211], v[112:115], v[80:83]\n\t"
    "ds_read_b128 v[104:107], %[da] offset:24576\n\t"
    "v_mfma_f32_16x16x32_bf16 v[84:87], v[212:215], v[112:115], v[84:87]\n\t"
    "ds_read_b128 v[108:111], %[da] offset:26624\n\t"
    "v_mfma_f32_16x16x32_bf16 v[88:91], v[208:211], v[116:119], v[88:91]\n\t"
    "v_mfma_f32_16x16x32_bf16 v[92:95], v[212:215], v[116:119], v[92:95]\n\t"
    "s_waitcnt lgkmcnt(4)\n\t"
    "v_mfma_f32_16x16x32_bf16 v[80:83], v[216:219], v[120:123], v[80:83]\n\t"
    "ds_read_b128 v[112:115], %[ea] offset:28672\n\t"
    "v_mfma_f32_16x16x32_bf16 v[84:87], v[220:223], v[120:123], v[84:87]\n\t"
    "ds_read_b128 v[116:119], %[ea] offset:30720\n\t"
    "v_mfma_f32_16x16x32_bf16 v[88:91], v[216:219], v[124:127], v[88:91]\n\t"
    "v_mfma_f32_16x16x32_bf16 v[92:95], v[220:223], v[124:127], v[92:95]\n\t"
    "s_waitcnt lgkmcnt(4)\n\t"
    "v_mfma_f32_16x16x32_bf16 v[80:83], v[224:227], v[96:99], v[80:83]\n\t"
    "ds_read_b128 v[120:123], %[da] offset:28672\n\t"
    "v_mfma_f32_16x16x32_bf16 v[84:87], v[228:231], v[96:99], v[84:87]\n\t"
    "ds_read_b128 v[124:127], %[da] offset:30720\n\t"
    "v_mfma_f32_16x16x32_bf16 v[88:91], v[224:227], v[100:103], v[88:91]\n\t"
    "v_mfma_f32_16x16x32_bf16 v[92:95], v[228:231], v[100:103], v[92:95]\n\t"
    "s_waitcnt lgkmcnt(4)\n\t"
    "v_mfma_f32_16x16x32_bf16 v[80:83], v[232:235], v[104:107], v[80:83]\n\t"
    "v_mfma_f32_16x16x32_bf16 v[84:87], v[236:239], v[104:107], v[84:87]\n\t"
    "v_mfma_f32_16x16x32_bf16 v[88:91], v[232:235], v[108:111], v[88:91]\n\t"
    "v_xor_b32 %[ea], 0x10000, %[ea]\n\t"
    "v_mfma_f32_16x16x32_bf16 v[92:95], v[236:239], v[108:111], v[92:95]\n\t"
    "v_xor_b32 %[da], 0x10000, %[da]\n\t"
    "s_waitcnt lgkmcnt(2)\n\t"
    "v_mfma_f32_16x16x32_bf16 v[80:83], v[240:243], v[112:115], v[80:83]\n\t"
    "v_mfma_f32_16x16x32_bf16 v[84:87], v[244:247], v[112:115], v[84:87]\n\t"
    "v_mfma_f32_16x16x32_bf16 v[88:91], v[240:243], v[116:119], v[88:91]\n\t"
    "s_add_u32 %[b], %[b], 1\n\t"
    "v_mfma_f32_16x16x32_bf16 v[92:95], v[244:247], v[116:119], v[92:95]\n\t"
    "s_waitcnt lgkmcnt(0)\n\t"
    "v_mfma_f32_16x16x32_bf16 v[80:83], v[248:251], v[120:123], v[80:83]\n\t"
    "v_mfma_f32_16x16x32_bf16 v[84:87], v[252:255], v[120:123], v[84:87]\n\t"
    "v_mfma_f32_16x16x32_bf16 v[88:91], v[248:251], v[124:127], v[88:91]\n\t"
    "v_mfma_f32_16x16x32_bf16 v[92:95], v[252:255], v[124:127], v[92:95]\n\t"
    "2:\n\t"
    "ds_read_b32 v39, %[fa]\n\t"
    "ds_read_b128 v[48:51], %[rw] offset:0\n\t"
    "ds_read_b128 v[56:59], %[rw] offset:2048\n\t"
    "ds_read_b128 v[52:55], %[rw] offset:1024\n\t"
    "ds_read_b128 v[60:63], %[rw] offset:3072\n\t"
    "s_waitcnt lgkmcnt(2)\n\t"
    "v_readfirstlane_b32 %[pp], v39\n\t"
    "s_and_b32 %[st], %[pp], 0xff\n\t"
    "s_cmp_eq_u32 %[st], %[b]\n\t"
    "s_cbranch_scc1 3f\n\t"
    "s_waitcnt lgkmcnt(0)\n\t"
    "s_branch 2b\n\t"
    "3:\n\t"
    "v_add_f32 v48, v48, v80\n\t"
    "v_add_f32 v49, v49, v81\n\t"
    "v_add_f32 v50, v50, v82\n\t"
    "v_add_f32 v51, v51, v83\n\t"
    "v_add_f32 v56, v56, v88\n\t"
    "v_add_f32 v57, v57, v89\n\t"
    "v_add_f32 v58, v58, v90\n\t"
    "v_add_f32 v59, v59, v91\n\t"
    "v_cmp_u_f32 vcc, v48, v56\n\t"
    "s_waitcnt lgkmcnt(0)\n\t"
    "v_add_f32 v52, v52, v84\n\t"
    "v_add_f32 v53, v53, v85\n\t"
    "v_add_f32 v54, v54, v86\n\t"
    "v_add_f32 v55, v55, v87\n\t"
    "v_add_f32 v60, v60, v92\n\t"
    "v_add_f32 v61, v61, v93\n\t"
    "v_add_f32 v62, v62, v94\n\t"
    "v_add_f32 v63, v63, v95\n\t"
    "s_cmp_lg_u64 vcc, 0\n\t"
    "s_cselect_b32 %[st], %[b], 0\n\t"
    "s_cmp_gt_i32 %[st], %[be]\n\t"
    "s_cbranch_scc1 4f\n\t"
    "v_mul_f32 v40, %[nl], v48\n\t"
    "v_mul_f32 v41, %[nl], v49\n\t"
    "v_mul_f32 v42, %[nl], v50\n\t"
    "v_mul_f32 v43, %[nl], v51\n\t"
    "v_mul_f32 v44, %[nl], v56\n\t"
    "v_mul_f32 v45, %[nl], v57\n\t"
    "v_mul_f32 v46, %[nl], v58\n\t"
    "v_mul_f32 v47, %[nl], v59\n\t"
    "v_exp_f32 v40, v40\n\t"
    "v_exp_f32 v41, v41\n\t"
    "v_exp_f32 v42, v42\n\t"
    "v_exp_f32 v43, v43\n\t"
    "v_exp_f32 v44, v44\n\t"
    "v_exp_f32 v45, v45\n\t"
    "v_exp_f32 v46, v46\n\t"
    "v_exp_f32 v47, v47\n\t"
    "v_add_f32 v40, 1.0, v40\n\t"
    "v_add_f32 v41, 1.0, v41\n\t"
    "v_add_f32 v42, 1.0, v42\n\t"
    "v_add_f32 v43, 1.0, v43\n\t"
    "v_add_f32 v44, 1.0, v44\n\t"
    "v_add_f32 v45, 1.0, v45\n\t"
    "v_add_f32 v46, 1.0, v46\n\t"
    "v_add_f32 v47, 1.0, v47\n\t"
    "v_rcp_f32 v40, v40\n\t"
    "v_rcp_f32 v41, v41\n\t"
    "v_rcp_f32 v42, v42\n\t"
    "v_rcp_f32 v43, v43\n\t"
    "v_rcp_f32 v44, v44\n\t"
    "v_rcp_f32 v45, v45\n\t"
    "v_rcp_f32 v46, v46\n\t"
    "v_rcp_f32 v47, v47\n\t"
    "v_mul_f32 v40, v48, v40\n\t"
    "v_mul_f32 v41, v49, v41\n\t"
    "v_mul_f32 v42, v50, v42\n\t"
    "v_mul_f32 v43, v51, v43\n\t"
    "v_mul_f32 v44, v56, v44\n\t"
    "v_mul_f32 v45, v57, v45\n\t"
    "v_mul_f32 v46, v58, v46\n\t"
    "v_mul_f32 v47, v59, v47\n\t"
    "v_mul_f32 v40, v40, v52\n\t"
    "v_mul_f32 v41, v41, v53\n\t"
    "v_mul_f32 v42, v42, v54\n\t"
    "v_mul_f32 v43, v43, v55\n\t"
    "v_mul_f32 v44, v44, v60\n\t"
    "v_mul_f32 v45, v45, v61\n\t"
    "v_mul_f32 v46, v46, v62\n\t"
    "v_mul_f32 v47, v47, v63\n\t"
    "v_cvt_pk_bf16_f32 v48, v40, v41\n\t"
    "v_cvt_pk_bf16_f32 v49, v42, v43\n\t"
    "v_cvt_pk_bf16_f32 v50, v44, v45\n\t"
    "v_cvt_pk_bf16_f32 v51, v46, v47\n\t"
    "s_nop 1\n\t"
    "v_permlane16_swap_b32 v48, v50\n\t"
    "v_permlane16_swap_b32 v49, v51\n\t"
    "global_store_dwordx4 %[co0], v[48:51], s[36:37] sc1\n\t"
    "s_add_u32 s36, s36, %[ab]\n\t"
    "s_addc_u32 s37, s37, 0\n\t"
    "s_cmp_le_i32 %[b], %[be]\n\t"
    "s_cbranch_scc1 5f\n\t"
    "v_mov_b32 v72, %[cn]\n\t"
    "v_mov_b32 v73, 1\n\t"
    "s_waitcnt vmcnt(1)\n\t"
    "s_mov_b64 exec, 1\n\t"
    "ds_add_rtn_u32 v73, v72, v73\n\t"
    "s_add_u32 %[st], %[b], %[po]\n\t"
    "v_mov_b32 v75, %[st]\n\t"
    "v_add_u32 v74, -1, v75\n\t"
    "v_mov_b32 v72, 0\n\t"
    "s_waitcnt lgkmcnt(0)\n\t"
    "v_readfirstlane_b32 %[st], v73\n\t"
    "s_and_b32 %[st], %[st], 3\n\t"
    "s_cmp_lg_u32 %[st], 3\n\t"
    "s_cbranch_scc1 61f\n\t"
    "global_store_dwordx2 v72, v[74:75], %[pg] sc1\n\t"
    "61:\n\t"
    "s_mov_b64 exec, -1\n\t"
    "s_mov_b32 %[pp], 0\n\t"
    "s_mov_b32 %[pp], 0\n\t"
    "s_cmp_lt_i32 %[b], %[ns]\n\t"
    "s_cbranch_scc1 1b\n\t"
    "s_barrier\n\t"
    "s_branch 9f\n\t"
    "5:\n\t"
    "s_lshr_b32 %[pp], %[pp], 8\n\t"
    "s_cbranch_scc1 1b\n\t"
    "v_mov_b32 v72, %[cn]\n\t"
    "v_mov_b32 v73, 1\n\t"
    "s_waitcnt vmcnt(0)\n\t"
    "s_mov_b64 exec, 1\n\t"
    "ds_add_rtn_u32 v73, v72, v73\n\t"
    "s_add_u32 %[st], %[b], %[po]\n\t"
    "v_mov_b32 v75, %[st]\n\t"
    "v_add_u32 v74, 0, v75\n\t"
    "v_mov_b32 v72, 0\n\t"
    "s_waitcnt lgkmcnt(0)\n\t"
    "v_readfirstlane_b32 %[st], v73\n\t"
    "s_and_b32 %[st], %[st], 3\n\t"
    "s_cmp_lg_u32 %[st], 3\n\t"
    "s_cbranch_scc1 61f\n\t"
    "global_store_dword v72, v74, %[pg] sc1\n\t"
    "61:\n\t"
    "s_mov_b64 exec, -1\n\t"
    "s_mov_b32 %[pp], 0\n\t"
    "s_cmp_lt_i32 %[b], %[ns]\n\t"
    "s_cbranch_scc1 1b\n\t"
    "s_barrier\n\t"
    "s_branch 9f\n\t"
    "4:\n\t"
    "v_mov_b32 v39, %[b]\n\t"
    "ds_write_b32 %[fa], v39 offset:16\n\t"
    "s_waitcnt lgkmcnt(0)\n\t"
    "s_barrier\n\t"
    "s_branch 9f\n\t"
    "7:\n\t"
    "v_mov_b32 v72, %[cn]\n\t"
    "v_mov_b32 v73, 1\n\t"
    "s_waitcnt vmcnt(0)\n\t"
    "s_mov_b64 exec, 1\n\t"
    "ds_add_rtn_u32 v73, v72, v73\n\t"
    "s_add_u32 %[st], %[b], %[po]\n\t"
    "v_mov_b32 v75, %[st]\n\t"
    "v_add_u32 v74, 0, v75\n\t"
    "v_mov_b32 v72, 0\n\t"
    "s_waitcnt lgkmcnt(0)\n\t"
    "v_readfirstlane_b32 %[st], v73\n\t"
    "s_and_b32 %[st], %[st], 3\n\t"
    "s_cmp_lg_u32 %[st], 3\n\t"
    "s_cbranch_scc1 61f\n\t"
    "global_store_dword v72, v74, %[pg] sc1\n\t"
    "61:\n\t"
    "s_mov_b64 exec, -1\n\t"
    "s_mov_b32 %[pp], 0\n\t"
    "s_branch 8b\n\t"
    "9:"
    : [ea] "+&v"(ea), [da] "+&v"(da), [b] "+&s"(b), [st] "=&s"(st), "+{s[36:37]}"(p0), [pp] "+&s"(pp)
    : [ns] "s"(ns), [ab] "s"(ab), [rw] "v"(rw), [fa] "v"(fa), "{v[128:159]}"(W[0]), "{v[160:191]}"(W[1]), "{v[192:223]}"(W[2]), "{v[224:255]}"(W[3]), [be] "s"(be), [po] "s"(po), [cn] "s"(cn), [pg] "s"(pg), [co0] "v"(co0), [nl] "s"(-LOG2E)
    : "v39", "v40", "v41", "v42", "v43", "v44", "v45", "v46", "v47", "v48", "v49", "v50", "v51", "v52", "v53", "v54", "v55", "v56", "v57", "v58", "v59", "v60", "v61", "v62", "v63", "v72", "v73", "v74", "v75", "v80", "v81", "v82", "v83", "v84", "v85", "v86", "v87", "v88", "v89", "v90", "v91", "v92", "v93", "v94", "v95", "v96", "v97", "v98", "v99", "v100", "v101", "v102", "v103", "v104", "v105", "v106", "v107", "v108", "v109", "v110", "v111", "v112", "v113", "v114", "v115", "v116", "v117", "v118", "v119", "v120", "v121", "v122", "v123", "v124", "v125", "v126", "v127", "vcc", "scc", "m0", "memory");
}
__device__ __forceinline__ void paced_st2(const u32x32 (&W)[4], unsigned ea, unsigned da, int b, int ns, unsigned ab, unsigned rw, unsigned fa,
    const char* p0, unsigned pp, int be, int po, unsigned cn, const char* pg, unsigned co0, unsigned co1) {
  unsigned st;
  asm volatile("1:\n\t"
    "s_barrier\n\t"
    "ds_read_b128 v[96:99], %[ea] offset:0\n\t"
    "ds_read_b128 v[100:103], %[ea] offset:2048\n\t"
    "ds_read_b128 v[104:107], %[da] offset:0\n\t"
    "ds_read_b128 v[108:111], %[da] offset:2048\n\t"
    "ds_read_b128 v[112:115], %[ea] offset:4096\n\t"
    "ds_read_b128 v[116:119], %[ea] offset:6144\n\t"
    "s_waitcnt lgkmcnt(4)\n\t"
    "v_mfma_f32_16x16x32_bf16 v[80:83], v[128:131], v[96:99], 0\n\t"
    "ds_read_b128 v[120:123], %[da] offset:4096\n\t"
    "v_mfma_f32_16x16x32_bf16 v[84:87], v[132:135], v[96:99], 0\n\t"
    "ds_read_b128 v[124:127], %[da] offset:6144\n\t"
    "v_mfma_f32_16x16x32_bf16 v[88:91], v[128:131], v[100:103], 0\n\t"
    "v_mfma_f32_16x16x32_bf16 v[92:95], v[132:135], v[100:103], 0\n\t"
    "s_barrier\n\t"
    "s_waitcnt lgkmcnt(4)\n\t"
    "v_mfma_f32_16x16x32_bf16 v[80:83], v[136:139], v[104:107], v[80:83]\n\t"
    "ds_read_b128 v[96:99], %[ea] offset:8192\n\t"
    "v_mfma_f32_16x16x32_bf16 v[84:87], v[140:143], v[104:107], v[84:87]\n\t"
    "ds_read_b128 v[100:103], %[ea] offset:10240\n\t"
    "v_mfma_f32_16x16x32_bf16 v[88:91], v[136:139], v[108:111], v[88:91]\n\t"
    "v_mfma_f32_16x16x32_bf16 v[92:95], v[140:143], v[108:111], v[92:95]\n\t"
    "s_waitcnt lgkmcnt(4)\n\t"
    "v_mfma_f32_16x16x32_bf16 v[80:83], v[144:147], v[112:115], v[80:83]\n\t"
    "ds_read_b128 v[104:107], %[da] offset:8192\n\t"
    "v_mfma_f32_16x16x32_bf16 v[84:87], v[148:151], v[112:115], v[84:87]\n\t"
    "ds_read_b128 v[108:111], %[da] offset:10240\n\t"
    "v_mfma_f32_16x16x32_bf16 v[88:91], v[144:147], v[116:119], v[88:91]\n\t"
    "v_mfma_f32_16x16x32_bf16 v[92:95], v[148:151], v[116:119], v[92:95]\n\t"
    "s_waitcnt lgkmcnt(4)\n\t"
    "v_mfma_f32_16x16x32_bf16 v[80:83], v[152:155], v[120:123], v[80:83]\n\t"
    "ds_read_b128 v[112:115], %[ea] offset:12288\n\t"
    "v_mfma_f32_16x16x32_bf16 v[84:87], v[156:159], v[120:123], v[84:87]\n\t"
    "ds_read_b128 v[116:119], %[ea] offset:14336\n\t"
    "v_mfma_f32_16x16x32_bf16 v[88:91], v[152:155], v[124:127], v[88:91]\n\t"
    "v_mfma_f32_16x16x32_bf16 v[92:95], v[156:159], v[124:127], v[92:95]\n\t"
    "s_cmp_lg_u32 %[pp], 0\n\t"
    "s_cbranch_scc1 7f\n\t"
    "8:\n\t"
    "s_waitcnt lgkmcnt(4)\n\t"
    "v_mfma_f32_16x16x32_bf16 v[80:83], v[160:163], v[96:99], v[80:83]\n\t"
    "ds_read_b128 v[120:123], %[da] offset:12288\n\t"
    "v_mfma_f32_16x16x32_bf16 v[84:87], v[164:167], v[96:99], v[84:87]\n\t"
    "ds_read_b128 v[124:127], %[da] offset:14336\n\t"
    "v_mfma_f32_16x16x32_bf16 v[88:91], v[160:163], v[100:103], v[88:91]\n\t"
    "v_mfma_f32_16x16x32_bf16 v[92:95], v[164:167], v[100:103], v[92:95]\n\t"
    "s_barrier\n\t"
    "s_waitcnt lgkmcnt(4)\n\t"
    "v_mfma_f32_16x16x32_bf16 v[80:83], v[168:171], v[104:107], v[80:83]\n\t"
    "ds_read_b128 v[96:99], %[ea] offset:16384\n\t"
    "v_mfma_f32_16x16x32_bf16 v[84:87], v[172:175], v[104:107], v[84:87]\n\t"
    "ds_read_b128 v[100:103], %[ea] offset:18432\n\t"
    "v_mfma_f32_16x16x32_bf16 v[88:91], v[168:171], v[108:111], v[88:91]\n\t"
    "v_mfma_f32_16x16x32_bf16 v[92:95], v[172:175], v[108:111], v[92:95]\n\t"
    "s_waitcnt lgkmcnt(4)\n\t"
    "v_mfma_f32_16x16x32_bf16 v[80:83], v[176:179], v[112:115], v[80:83]\n\t"
    "ds_read_b128 v[104:107], %[da] offset:16384\n\t"
    "v_mfma_f32_16x16x32_bf16 v[84:87], v[180:183], v[112:115], v[84:87]\n\t"
    "ds_read_b128 v[108:111], %[da] offset:18432\n\t"
    "v_mfma_f32_16x16x32_bf16 v[88:91], v[176:179], v[116:119], v[88:91]\n\t"
    "v_mfma_f32_16x16x32_bf16 v[92:95], v[180:183], v[116:119], v[92:95]\n\t"
    "s_waitcnt lgkmcnt(4)\n\t"
    "v_mfma_f32_16x16x32_bf16 v[80:83], v[184:187], v[120:123], v[80:83]\n\t"
    "ds_read_b128 v[112:115], %[ea] offset:20480\n\t"
    "v_mfma_f32_16x16x32_bf16 v[84:87], v[188:191], v[120:123], v[84:87]\n\t"
    "ds_read_b128 v[116:119], %[ea] offset:22528\n\t"
    "v_mfma_f32_16x16x32_bf16 v[88:91], v[184:187], v[124:127], v[88:91]\n\t"
    "v_mfma_f32_16x16x32_bf16 v[92:95], v[188:191], v[124:127], v[92:95]\n\t"
    "s_waitcnt lgkmcnt(4)\n\t"
    "v_mfma_f32_16x16x32_bf16 v[80:83], v[192:195], v[96:99], v[80:83]\n\t"
    "ds_read_b128 v[120:123], %[da] offset:20480\n\t"
    "v_mfma_f32_16x16x32_bf16 v[84:87], v[196:199], v[96:99], v[84:87]\n\t"
    "ds_read_b128 v[124:127], %[da] offset:22528\n\t"
    "v_mfma_f32_16x16x32_bf16 v[88:91], v[192:195], v[100:103], v[88:91]\n\t"
    "v_mfma_f32_16x16x32_bf16 v[92:95], v[196:199], v[100:103], v[92:95]\n\t"
    "s_barrier\n\t"
    "s_waitcnt lgkmcnt(4)\n\t"
    "v_mfma_f32_16x16x32_bf16 v[80:83], v[200:203], v[104:107], v[80:83]\n\t"
    "ds_read_b128 v[96:99], %[ea] offset:24576\n\t"
    "v_mfma_f32_16x16x32_bf16 v[84:87], v[204:207], v[104:107], v[84:87]\n\t"
    "ds_read_b128 v[100:103], %[ea] offset:26624\n\t"
    "v_mfma_f32_16x16x32_bf16 v[88:91], v[200:203], v[108:111], v[88:91]\n\t"
    "v_mfma_f32_16x16x32_bf16 v[92:95], v[204:207], v[108:111], v[92:95]\n\t"
    "s_waitcnt lgkmcnt(4)\n\t"
    "v_mfma_f32_16x16x32_bf16 v[80:83], v[208:211], v[112:115], v[80:83]\n\t"
    "ds_read_b128 v[104:107], %[da] offset:24576\n\t"
    "v_mfma_f32_16x16x32_bf16 v[84:87], v[212:215], v[112:115], v[84:87]\n\t"
    "ds_read_b128 v[108:111], %[da] offset:26624\n\t"
    "v_mfma_f32_16x16x32_bf16 v[88:91], v[208:211], v[116:119], v[88:91]\n\t"
    "v_mfma_f32_16x16x32_bf16 v[92:95], v[212:215], v[116:119], v[92:95]\n\t"
    "s_waitcnt lgkmcnt(4)\n\t"
    "v_mfma_f32_16x16x32_bf16 v[80:83], v[216:219], v[120:123], v[80:83]\n\t"
    "ds_read_b128 v[112:115], %[ea] offset:28672\n\t"
    "v_mfma_f32_16x16x32_bf16 v[84:87], v[220:223], v[120:123], v[84:87]\n\t"
    "ds_read_b128 v[116:119], %[ea] offset:30720\n\t"
    "v_mfma_f32_16x16x32_bf16 v[88:91], v[216:219], v[124:127], v[88:91]\n\t"
    "v_mfma_f32_16x16x32_bf16 v[92:95], v[220:223], v[124:127], v[92:95]\n\t"
    "s_waitcnt lgkmcnt(4)\n\t"
    "v_mfma_f32_16x16x32_bf16 v[80:83], v[224:227], v[96:99], v[80:83]\n\t"
    "ds_read_b128 v[120:123], %[da] offset:28672\n\t"
    "v_mfma_f32_16x16x32_bf16 v[84:87], v[228:231], v[96:99], v[84:87]\n\t"
    "ds_read_b128 v[124:127], %[da] offset:30720\n\t"
    "v_mfma_f32_16x16x32_bf16 v[88:91], v[224:227], v[100:103], v[88:91]\n\t"
    "v_mfma_f32_16x16x32_bf16 v[92:95], v[228:231], v[100:103], v[92:95]\n\t"
    "s_waitcnt lgkmcnt(4)\n\t"
    "v_mfma_f32_16x16x32_bf16 v[80:83], v[232:235], v[104:107], v[80:83]\n\t"
    "v_mfma_f32_16x16x32_bf16 v[84:87], v[236:239], v[104:107], v[84:87]\n\t"
    "v_mfma_f32_16x16x32_bf16 v[88:91], v[232:235], v[108:111], v[88:91]\n\t"
    "v_xor_b32 %[ea], 0x10000, %[ea]\n\t"
    "v_mfma_f32_16x16x32_bf16 v[92:95], v[236:239], v[108:111], v[92:95]\n\t"
    "v_xor_b32 %[da], 0x10000, %[da]\n\t"
    "s_waitcnt lgkmcnt(2)\n\t"
    "v_mfma_f32_16x16x32_bf16 v[80:83], v[240:243], v[112:115], v[80:83]\n\t"
    "v_mfma_f32_16x16x32_bf16 v[84:87], v[244:247], v[112:115], v[84:87]\n\t"
    "v_mfma_f32_16x16x32_bf16 v[88:91], v[240:243], v[116:119], v[88:91]\n\t"
    "s_add_u32 %[b], %[b], 1\n\t"
    "v_mfma_f32_16x16x32_bf16 v[92:95], v[244:247], v[116:119], v[92:95]\n\t"
    "s_waitcnt lgkmcnt(0)\n\t"
    "v_mfma_f32_16x16x32_bf16 v[80:83], v[248:251], v[120:123], v[80:83]\n\t"
    "v_mfma_f32_16x16x32_bf16 v[84:87], v[252:255], v[120:123], v[84:87]\n\t"
    "v_mfma_f32_16x16x32_bf16 v[88:91], v[248:251], v[124:127], v[88:91]\n\t"
    "v_mfma_f32_16x16x32_bf16 v[92:95], v[252:255], v[124:127], v[92:95]\n\t"
    "s_nop 6\n\t"
    "v_cmp_u_f32 vcc, v80, v88\n\t"
    "s_cmp_lg_u64 vcc, 0\n\t"
    "s_cselect_b32 %[st], %[b], 0\n\t"
    "s_cmp_gt_i32 %[st], %[be]\n\t"
    "s_cbranch_scc1 4f\n\t"
    "v_cvt_pk_bf16_f32 v40, v80, v81\n\t"
    "v_cvt_pk_bf16_f32 v41, v82, v83\n\t"
    "v_cvt_pk_bf16_f32 v42, v84, v85\n\t"
    "v_cvt_pk_bf16_f32 v43, v86, v87\n\t"
    "global_store_dwordx4 %[co0], v[40:43], s[36:37] sc1\n\t"
    "v_cvt_pk_bf16_f32 v44, v88, v89\n\t"
    "v_cvt_pk_bf16_f32 v45, v90, v91\n\t"
    "v_cvt_pk_bf16_f32 v46, v92, v93\n\t"
    "v_cvt_pk_bf16_f32 v47, v94, v95\n\t"
    "global_store_dwordx4 %[co1], v[44:47], s[36:37] sc1\n\t"
    "2:\n\t"
    "ds_read_b32 v39, %[fa]\n\t"
    "ds_read_b128 v[48:51], %[rw] offset:0\n\t"
    "ds_read_b128 v[52:55], %[rw] offset:1024\n\t"
    "ds_read_b128 v[56:59], %[rw] offset:2048\n\t"
    "ds_read_b128 v[60:63], %[rw] offset:3072\n\t"
    "s_waitcnt lgkmcnt(2)\n\t"
    "v_readfirstlane_b32 %[pp], v39\n\t"
    "s_and_b32 %[st], %[pp], 0xff\n\t"
    "s_cmp_eq_u32 %[st], %[b]\n\t"
    "s_cbranch_scc1 3f\n\t"
    "s_waitcnt lgkmcnt(0)\n\t"
    "s_branch 2b\n\t"
    "3:\n\t"
    "v_cvt_pk_bf16_f32 v48, v48, v49\n\t"
    "v_cvt_pk_bf16_f32 v49, v50, v51\n\t"
    "v_cvt_pk_bf16_f32 v50, v52, v53\n\t"
    "v_cvt_pk_bf16_f32 v51, v54, v55\n\t"
    "global_store_dwordx4 %[co0], v[48:51], s[36:37] offset:2048 sc1\n\t"
    "s_waitcnt lgkmcnt(0)\n\t"
    "v_cvt_pk_bf16_f32 v56, v56, v57\n\t"
    "v_cvt_pk_bf16_f32 v57, v58, v59\n\t"
    "v_cvt_pk_bf16_f32 v58, v60, v61\n\t"
    "v_cvt_pk_bf16_f32 v59, v62, v63\n\t"
    "global_store_dwordx4 %[co1], v[56:59], s[36:37] offset:2048 sc1\n\t"
    "s_add_u32 s36, s36, %[ab]\n\t"
    "s_addc_u32 s37, s37, 0\n\t"
    "s_cmp_le_i32 %[b], %[be]\n\t"
    "s_cbranch_scc1 5f\n\t"
    "v_mov_b32 v72, %[cn]\n\t"
    "v_mov_b32 v73, 1\n\t"
    "s_waitcnt vmcnt(4)\n\t"
    "s_mov_b64 exec, 1\n\t"
    "ds_add_rtn_u32 v73, v72, v73\n\t"
    "s_add_u32 %[st], %[b], %[po]\n\t"
    "v_mov_b32 v75, %[st]\n\t"
    "v_add_u32 v74, -1, v75\n\t"
    "v_mov_b32 v72, 0\n\t"
    "s_waitcnt lgkmcnt(0)\n\t"
    "v_readfirstlane_b32 %[st], v73\n\t"
    "s_and_b32 %[st], %[st], 3\n\t"
    "s_cmp_lg_u32 %[st], 3\n\t"
    "s_cbranch_scc1 61f\n\t"
    "global_store_dwordx2 v72, v[74:75], %[pg] sc1\n\t"
    "61:\n\t"
    "s_mov_b64 exec, -1\n\t"
    "s_mov_b32 %[pp], 0\n\t"
    "s_mov_b32 %[pp], 0\n\t"
    "s_cmp_lt_i32 %[b], %[ns]\n\t"
    "s_cbranch_scc1 1b\n\t"
    "s_barrier\n\t"
    "s_branch 9f\n\t"
    "5:\n\t"
    "s_lshr_b32 %[pp], %[pp], 8\n\t"
    "s_cbranch_scc1 1b\n\t"
    "v_mov_b32 v72, %[cn]\n\t"
    "v_mov_b32 v73, 1\n\t"
    "s_waitcnt vmcnt(0)\n\t"
    "s_mov_b64 exec, 1\n\t"
    "ds_add_rtn_u32 v73, v72, v73\n\t"
    "s_add_u32 %[st], %[b], %[po]\n\t"
    "v_mov_b32 v75, %[st]\n\t"
    "v_add_u32 v74, 0, v75\n\t"
    "v_mov_b32 v72, 0\n\t"
    "s_waitcnt lgkmcnt(0)\n\t"
    "v_readfirstlane_b32 %[st], v73\n\t"
    "s_and_b32 %[st], %[st], 3\n\t"
    "s_cmp_lg_u32 %[st], 3\n\t"
    "s_cbranch_scc1 61f\n\t"
    "global_store_dword v72, v74, %[pg] sc1\n\t"
    "61:\n\t"
    "s_mov_b64 exec, -1\n\t"
    "s_mov_b32 %[pp], 0\n\t"
    "s_cmp_lt_i32 %[b], %[ns]\n\t"
    "s_cbranch_scc1 1b\n\t"
    "s_barrier\n\t"
    "s_branch 9f\n\t"
    "4:\n\t"
    "v_mov_b32 v39, %[b]\n\t"
    "ds_write_b32 %[fa], v39 offset:16\n\t"
    "s_waitcnt lgkmcnt(0)\n\t"
    "s_barrier\n\t"
    "s_branch 9f\n\t"
    "7:\n\t"
    "v_mov_b32 v72, %[cn]\n\t"
    "v_mov_b32 v73, 1\n\t"
    "s_waitcnt vmcnt(0)\n\t"
    "s_mov_b64 exec, 1\n\t"
    "ds_add_rtn_u32 v73, v72, v73\n\t"
    "s_add_u32 %[st], %[b], %[po]\n\t"
    "v_mov_b32 v75, %[st]\n\t"
    "v_add_u32 v74, 0, v75\n\t"
    "v_mov_b32 v72, 0\n\t"
    "s_waitcnt lgkmcnt(0)\n\t"
    "v_readfirstlane_b32 %[st], v73\n\t"
    "s_and_b32 %[st], %[st], 3\n\t"
    "s_cmp_lg_u32 %[st], 3\n\t"
    "s_cbranch_scc1 61f\n\t"
    "global_store_dword v72, v74, %[pg] sc1\n\t"
    "61:\n\t"
    "s_mov_b64 exec, -1\n\t"
    "s_mov_b32 %[pp], 0\n\t"
    "s_branch 8b\n\t"
    "9:"
    : [ea] "+&v"(ea), [da] "+&v"(da), [b] "+&s"(b), [st] "=&s"(st), "+{s[36:37]}"(p0), [pp] "+&s"(pp)
    : [ns] "s"(ns), [ab] "s"(ab), [rw] "v"(rw), [fa] "v"(fa), "{v[128:159]}"(W[0]), "{v[160:191]}"(W[1]), "{v[192:223]}"(W[2]), "{v[224:255]}"(W[3]), [be] "s"(be), [po] "s"(po), [cn] "s"(cn), [pg] "s"(pg), [co0] "v"(co0), [co1] "v"(co1)
    : "v39", "v40", "v41", "v42", "v43", "v44", "v45", "v46", "v47", "v48", "v49", "v50", "v51", "v52", "v53", "v54", "v55", "v56", "v57", "v58", "v59", "v60", "v61", "v62", "v63", "v72", "v73", "v74", "v75", "v80", "v81", "v82", "v83", "v84", "v85", "v86", "v87", "v88", "v89", "v90", "v91", "v92", "v93", "v94", "v95", "v96", "v97", "v98", "v99", "v100", "v101", "v102", "v103", "v104", "v105", "v106", "v107", "v108", "v109", "v110", "v111", "v112", "v113", "v114", "v115", "v116", "v117", "v118", "v119", "v120", "v121", "v122", "v123", "v124", "v125", "v126", "v127", "vcc", "scc", "m0", "memory");
}
__device__ __forceinline__ void fin_ld(const u32x16& pd, unsigned rw, unsigned fa, unsigned fv) {
  asm volatile("ds_write_b128 %[rw], v[80:83] offset:0\n\t"
    "ds_write_b128 %[rw], v[84:87] offset:1024\n\t"
    "ds_write_b128 %[rw], v[88:91] offset:2048\n\t"
    "ds_write_b128 %[rw], v[92:95] offset:3072\n\t"
    "ds_write_b32 %[fa], %[fv]"
    :
    : [rw] "v"(rw), "{v[80:95]}"(pd), [fa] "v"(fa), [fv] "v"(fv)
    : "memory");
}
__device__ __forceinline__ void fin_st0(const u32x16& pd, unsigned rw, unsigned fa, unsigned fv, const char* cb, unsigned co0, unsigned co1) {
  asm volatile("1:\n\t"
    "ds_read_b32 v39, %[fa]\n\t"
    "ds_read_b128 v[48:51], %[rw] offset:0\n\t"
    "ds_read_b128 v[52:55], %[rw] offset:1024\n\t"
    "ds_read_b128 v[56:59], %[rw] offset:2048\n\t"
    "ds_read_b128 v[60:63], %[rw] offset:3072\n\t"
    "s_waitcnt lgkmcnt(2)\n\t"
    "v_cmp_ne_u32 vcc, v39, %[fv]\n\t"
    "s_cbranch_vccz 2f\n\t"
    "s_waitcnt lgkmcnt(0)\n\t"
    "s_branch 1b\n\t"
    "2:\n\t"
    "v_add_f32 v48, v48, v80\n\t"
    "v_add_f32 v49, v49, v81\n\t"
    "v_add_f32 v50, v50, v82\n\t"
    "v_add_f32 v51, v51, v83\n\t"
    "v_add_f32 v52, v52, v84\n\t"
    "v_add_f32 v53, v53, v85\n\t"
    "v_add_f32 v54, v54, v86\n\t"
    "v_add_f32 v55, v55, v87\n\t"
    "v_cvt_pk_bf16_f32 v40, v48, v49\n\t"
    "v_cvt_pk_bf16_f32 v41, v50, v51\n\t"
    "v_cvt_pk_bf16_f32 v42, v52, v53\n\t"
    "v_cvt_pk_bf16_f32 v43, v54, v55\n\t"
    "global_store_dwordx4 %[co0], v[40:43], %[cb] sc1\n\t"
    "s_waitcnt lgkmcnt(0)\n\t"
    "v_add_f32 v56, v56, v88\n\t"
    "v_add_f32 v57, v57, v89\n\t"
    "v_add_f32 v58, v58, v90\n\t"
    "v_add_f32 v59, v59, v91\n\t"
    "v_add_f32 v60, v60, v92\n\t"
    "v_add_f32 v61, v61, v93\n\t"
    "v_add_f32 v62, v62, v94\n\t"
    "v_add_f32 v63, v63, v95\n\t"
    "v_cvt_pk_bf16_f32 v44, v56, v57\n\t"
    "v_cvt_pk_bf16_f32 v45, v58, v59\n\t"
    "v_cvt_pk_bf16_f32 v46, v60, v61\n\t"
    "v_cvt_pk_bf16_f32 v47, v62, v63\n\t"
    "global_store_dwordx4 %[co1], v[44:47], %[cb] sc1\n\t"
    "s_nop 1"
    :
    : [rw] "v"(rw), "{v[80:95]}"(pd), [fa] "v"(fa), [fv] "v"(fv), [cb] "s"(cb), [co0] "v"(co0), [co1] "v"(co1)
    : "v39", "v40", "v41", "v42", "v43", "v44", "v45", "v46", "v47", "v48", "v49", "v50", "v51", "v52", "v53", "v54", "v55", "v56", "v57", "v58", "v59", "v60", "v61", "v62", "v63", "vcc", "memory");
}
__device__ __forceinline__ void fin_st1(const u32x16& pd, unsigned rw, unsigned fa, unsigned fv, const char* cb, unsigned co0, unsigned co1) {
  asm volatile("1:\n\t"
    "ds_read_b32 v39, %[fa]\n\t"
    "ds_read_b128 v[48:51], %[rw] offset:0\n\t"
    "ds_read_b128 v[52:55], %[rw] offset:1024\n\t"
    "ds_read_b128 v[56:59], %[rw] offset:2048\n\t"
    "ds_read_b128 v[60:63], %[rw] offset:3072\n\t"
    "s_waitcnt lgkmcnt(2)\n\t"
    "v_cmp_ne_u32 vcc, v39, %[fv]\n\t"
    "s_cbranch_vccz 2f\n\t"
    "s_waitcnt lgkmcnt(0)\n\t"
    "s_branch 1b\n\t"
    "2:\n\t"
    "v_add_f32 v48, v48, v80\n\t"
    "v_add_f32 v49, v49, v81\n\t"
    "v_add_f32 v50, v50, v82\n\t"
    "v_add_f32 v51, v51, v83\n\t"
    "v_add_f32 v52, v52, v84\n\t"
    "v_add_f32 v53, v53, v85\n\t"
    "v_add_f32 v54, v54, v86\n\t"
    "v_add_f32 v55, v55, v87\n\t"
    "s_waitcnt lgkmcnt(0)\n\t"
    "v_add_f32 v56, v56, v88\n\t"
    "v_add_f32 v57, v57, v89\n\t"
    "v_add_f32 v58, v58, v90\n\t"
    "v_add_f32 v59, v59, v91\n\t"
    "v_add_f32 v60, v60, v92\n\t"
    "v_add_f32 v61, v61, v93\n\t"
    "v_add_f32 v62, v62, v94\n\t"
    "v_add_f32 v63, v63, v95\n\t"
    "v_mul_f32 v40, %[nl], v48\n\t"
    "v_mul_f32 v41, %[nl], v49\n\t"
    "v_mul_f32 v42, %[nl], v50\n\t"
    "v_mul_f32 v43, %[nl], v51\n\t"
    "v_mul_f32 v44, %[nl], v56\n\t"
    "v_mul_f32 v45, %[nl], v57\n\t"
    "v_mul_f32 v46, %[nl], v58\n\t"
    "v_mul_f32 v47, %[nl], v59\n\t"
    "v_exp_f32 v40, v40\n\t"
    "v_exp_f32 v41, v41\n\t"
    "v_exp_f32 v42, v42\n\t"
    "v_exp_f32 v43, v43\n\t"
    "v_exp_f32 v44, v44\n\t"
    "v_exp_f32 v45, v45\n\t"
    "v_exp_f32 v46, v46\n\t"
    "v_exp_f32 v47, v47\n\t"
    "v_add_f32 v40, 1.0, v40\n\t"
    "v_add_f32 v41, 1.0, v41\n\t"
    "v_add_f32 v42, 1.0, v42\n\t"
    "v_add_f32 v43, 1.0, v43\n\t"
    "v_add_f32 v44, 1.0, v44\n\t"
    "v_add_f32 v45, 1.0, v45\n\t"
    "v_add_f32 v46, 1.0, v46\n\t"
    "v_add_f32 v47, 1.0, v47\n\t"
    "v_rcp_f32 v40, v40\n\t"
    "v_rcp_f32 v41, v41\n\t"
    "v_rcp_f32 v42, v42\n\t"
    "v_rcp_f32 v43, v43\n\t"
    "v_rcp_f32 v44, v44\n\t"
    "v_rcp_f32 v45, v45\n\t"
    "v_rcp_f32 v46, v46\n\t"
    "v_rcp_f32 v47, v47\n\t"
    "v_mul_f32 v40, v48, v40\n\t"
    "v_mul_f32 v41, v49, v41\n\t"
    "v_mul_f32 v42, v50, v42\n\t"
    "v_mul_f32 v43, v51, v43\n\t"
    "v_mul_f32 v44, v56, v44\n\t"
    "v_mul_f32 v45, v57, v45\n\t"
    "v_mul_f32 v46, v58, v46\n\t"
    "v_mul_f32 v47, v59, v47\n\t"
    "v_mul_f32 v40, v40, v52\n\t"
    "v_mul_f32 v41, v41, v53\n\t"
    "v_mul_f32 v42, v42, v54\n\t"
    "v_mul_f32 v43, v43, v55\n\t"
    "v_mul_f32 v44, v44, v60\n\t"
    "v_mul_f32 v45, v45, v61\n\t"
    "v_mul_f32 v46, v46, v62\n\t"
    "v_mul_f32 v47, v47, v63\n\t"
    "v_cvt_pk_bf16_f32 v48, v40, v41\n\t"
    "v_cvt_pk_bf16_f32 v49, v42, v43\n\t"
    "v_cvt_pk_bf16_f32 v50, v44, v45\n\t"
    "v_cvt_pk_bf16_f32 v51, v46, v47\n\t"
    "s_nop 1\n\t"
    "v_permlane16_swap_b32 v48, v50\n\t"
    "v_permlane16_swap_b32 v49, v51\n\t"
    "global_store_dwordx4 %[co0], v[48:51], %[cb] sc1\n\t"
    "s_nop 1"
    :
    : [rw] "v"(rw), "{v[80:95]}"(pd), [fa] "v"(fa), [fv] "v"(fv), [cb] "s"(cb), [co0] "v"(co0), [nl] "s"(-LOG2E)
    : "v39", "v40", "v41", "v42", "v43", "v44", "v45", "v46", "v47", "v48", "v49", "v50", "v51", "v52", "v53", "v54", "v55", "v56", "v57", "v58", "v59", "v60", "v61", "v62", "v63", "vcc", "memory");
}
__device__ __forceinline__ void fin_st2(const u32x16& pd, unsigned rw, unsigned fa, unsigned fv, const char* cb, unsigned co0, unsigned co1) {
  asm volatile("s_nop 1\n\t"
    "v_cvt_pk_bf16_f32 v40, v80, v81\n\t"
    "v_cvt_pk_bf16_f32 v41, v82, v83\n\t"
    "v_cvt_pk_bf16_f32 v42, v84, v85\n\t"
    "v_cvt_pk_bf16_f32 v43, v86, v87\n\t"
    "global_store_dwordx4 %[co0], v[40:43], %[cb] sc1\n\t"
    "v_cvt_pk_bf16_f32 v44, v88, v89\n\t"
    "v_cvt_pk_bf16_f32 v45, v90, v91\n\t"
    "v_cvt_pk_bf16_f32 v46, v92, v93\n\t"
    "v_cvt_pk_bf16_f32 v47, v94, v95\n\t"
    "global_store_dwordx4 %[co1], v[44:47], %[cb] sc1\n\t"
    "1:\n\t"
    "ds_read_b32 v39, %[fa]\n\t"
    "ds_read_b128 v[48:51], %[rw] offset:0\n\t"
    "ds_read_b128 v[52:55], %[rw] offset:1024\n\t"
    "ds_read_b128 v[56:59], %[rw] offset:2048\n\t"
    "ds_read_b128 v[60:63], %[rw] offset:3072\n\t"
    "s_waitcnt lgkmcnt(2)\n\t"
    "v_cmp_ne_u32 vcc, v39, %[fv]\n\t"
    "s_cbranch_vccz 2f\n\t"
    "s_waitcnt lgkmcnt(0)\n\t"
    "s_branch 1b\n\t"
    "2:\n\t"
    "v_cvt_pk_bf16_f32 v48, v48, v49\n\t"
    "v_cvt_pk_bf16_f32 v49, v50, v51\n\t"
    "v_cvt_pk_bf16_f32 v50, v52, v53\n\t"
    "v_cvt_pk_bf16_f32 v51, v54, v55\n\t"
    "global_store_dwordx4 %[co0], v[48:51], %[cb] offset:2048 sc1\n\t"
    "s_waitcnt lgkmcnt(0)\n\t"
    "v_cvt_pk_bf16_f32 v56, v56, v57\n\t"
    "v_cvt_pk_bf16_f32 v57, v58, v59\n\t"
    "v_cvt_pk_bf16_f32 v58, v60, v61\n\t"
    "v_cvt_pk_bf16_f32 v59, v62, v63\n\t"
    "global_store_dwordx4 %[co1], v[56:59], %[cb] offset:2048 sc1\n\t"
    "s_nop 1"
    :
    : [rw] "v"(rw), "{v[80:95]}"(pd), [fa] "v"(fa), [fv] "v"(fv), [cb] "s"(cb), [co0] "v"(co0), [co1] "v"(co1)
    : "v39", "v40", "v41", "v42", "v43", "v44", "v45", "v46", "v47", "v48", "v49", "v50", "v51", "v52", "v53", "v54", "v55", "v56", "v57", "v58", "v59", "v60", "v61", "v62", "v63", "vcc", "memory");
}
template <int R> __device__ __forceinline__ void gw_load(u32x32& w, unsigned a0, unsigned a1);
template <> __device__ __forceinline__ void gw_load<0>(u32x32& w, unsigned a0, unsigned a1) {
  asm volatile("ds_read_b64_tr_b16 v[128:129], %1 offset:0\n\t"
    "ds_read_b64_tr_b16 v[130:131], %1 offset:512\n\t"
    "ds_read_b64_tr_b16 v[132:133], %2 offset:0\n\t"
    "ds_read_b64_tr_b16 v[134:135], %2 offset:512\n\t"
    "ds_read_b64_tr_b16 v[136:137], %1 offset:4096\n\t"
    "ds_read_b64_tr_b16 v[138:139], %1 offset:4608\n\t"
    "ds_read_b64_tr_b16 v[140:141], %2 offset:4096\n\t"
    "ds_read_b64_tr_b16 v[142:143], %2 offset:4608\n\t"
    "s_waitcnt lgkmcnt(0)\n\t"
    "ds_read_b64_tr_b16 v[144:145], %1 offset:16384\n\t"
    "ds_read_b64_tr_b16 v[146:147], %1 offset:16896\n\t"
    "ds_read_b64_tr_b16 v[148:149], %2 offset:16384\n\t"
    "ds_read_b64_tr_b16 v[150:151], %2 offset:16896\n\t"
    "ds_read_b64_tr_b16 v[152:153], %1 offset:20480\n\t"
    "ds_read_b64_tr_b16 v[154:155], %1 offset:20992\n\t"
    "ds_read_b64_tr_b16 v[156:157], %2 offset:20480\n\t"
    "ds_read_b64_tr_b16 v[158:159], %2 offset:20992\n\t"
    "s_waitcnt lgkmcnt(0)"
    : "=&{v[128:159]}"(w) : "v"(a0), "v"(a1) : "memory");
}
template <> __device__ __forceinline__ void gw_load<1>(u32x32& w, unsigned a0, unsigned a1) {
  asm volatile("ds_read_b64_tr_b16 v[160:161], %1 offset:0\n\t"
    "ds_read_b64_tr_b16 v[162:163], %1 offset:512\n\t"
    "ds_read_b64_tr_b16 v[164:165], %2 offset:0\n\t"
    "ds_read_b64_tr_b16 v[166:167], %2 offset:512\n\t"
    "ds_read_b64_tr_b16 v[168:169], %1 offset:4096\n\t"
    "ds_read_b64_tr_b16 v[170:171], %1 offset:4608\n\t"
    "ds_read_b64_tr_b16 v[172:173], %2 offset:4096\n\t"
    "ds_read_b64_tr_b16 v[174:175], %2 offset:4608\n\t"
    "s_waitcnt lgkmcnt(0)\n\t"
    "ds_read_b64_tr_b16 v[176:177], %1 offset:16384\n\t"
    "ds_read_b64_tr_b16 v[178:179], %1 offset:16896\n\t"
    "ds_read_b64_tr_b16 v[180:181], %2 offset:16384\n\t"
    "ds_read_b64_tr_b16 v[182:183], %2 offset:16896\n\t"
    "ds_read_b64_tr_b16 v[184:185], %1 offset:20480\n\t"
    "ds_read_b64_tr_b16 v[186:187], %1 offset:20992\n\t"
    "ds_read_b64_tr_b16 v[188:189], %2 offset:20480\n\t"
    "ds_read_b64_tr_b16 v[190:191], %2 offset:20992\n\t"
    "s_waitcnt lgkmcnt(0)"
    : "=&{v[160:191]}"(w) : "v"(a0), "v"(a1) : "memory");
}
template <> __device__ __forceinline__ void gw_load<2>(u32x32& w, unsigned a0, unsigned a1) {
  asm volatile("ds_read_b64_tr_b16 v[192:193], %1 offset:0\n\t"
    "ds_read_b64_tr_b16 v[194:195], %1 offset:512\n\t"
    "ds_read_b64_tr_b16 v[196:197], %2 offset:0\n\t"
    "ds_read_b64_tr_b16 v[198:199], %2 offset:512\n\t"
    "ds_read_b64_tr_b16 v[200:201], %1 offset:4096\n\t"
    "ds_read_b64_tr_b16 v[202:203], %1 offset:4608\n\t"
    "ds_read_b64_tr_b16 v[204:205], %2 offset:4096\n\t"
    "ds_read_b64_tr_b16 v[206:207], %2 offset:4608\n\t"
    "s_waitcnt lgkmcnt(0)\n\t"
    "ds_read_b64_tr_b16 v[208:209], %1 offset:16384\n\t"
    "ds_read_b64_tr_b16 v[210:211], %1 offset:16896\n\t"
    "ds_read_b64_tr_b16 v[212:213], %2 offset:16384\n\t"
    "ds_read_b64_tr_b16 v[214:215], %2 offset:16896\n\t"
    "ds_read_b64_tr_b16 v[216:217], %1 offset:20480\n\t"
    "ds_read_b64_tr_b16 v[218:219], %1 offset:20992\n\t"
    "ds_read_b64_tr_b16 v[220:221], %2 offset:20480\n\t"
    "ds_read_b64_tr_b16 v[222:223], %2 offset:20992\n\t"
    "s_waitcnt lgkmcnt(0)"
    : "=&{v[192:223]}"(w) : "v"(a0), "v"(a1) : "memory");
}
template <> __device__ __forceinline__ void gw_load<3>(u32x32& w, unsigned a0, unsigned a1) {
  asm volatile("ds_read_b64_tr_b16 v[224:225], %1 offset:0\n\t"
    "ds_read_b64_tr_b16 v[226:227], %1 offset:512\n\t"
    "ds_read_b64_tr_b16 v[228:229], %2 offset:0\n\t"
    "ds_read_b64_tr_b16 v[230:231], %2 offset:512\n\t"
    "ds_read_b64_tr_b16 v[232:233], %1 offset:4096\n\t"
    "ds_read_b64_tr_b16 v[234:235], %1 offset:4608\n\t"
    "ds_read_b64_tr_b16 v[236:237], %2 offset:4096\n\t"
    "ds_read_b64_tr_b16 v[238:239], %2 offset:4608\n\t"
    "s_waitcnt lgkmcnt(0)\n\t"
    "ds_read_b64_tr_b16 v[240:241], %1 offset:16384\n\t"
    "ds_read_b64_tr_b16 v[242:243], %1 offset:16896\n\t"
    "ds_read_b64_tr_b16 v[244:245], %2 offset:16384\n\t"
    "ds_read_b64_tr_b16 v[246:247], %2 offset:16896\n\t"
    "ds_read_b64_tr_b16 v[248:249], %1 offset:20480\n\t"
    "ds_read_b64_tr_b16 v[250:251], %1 offset:20992\n\t"
    "ds_read_b64_tr_b16 v[252:253], %2 offset:20480\n\t"
    "ds_read_b64_tr_b16 v[254:255], %2 offset:20992\n\t"
    "s_waitcnt lgkmcnt(0)"
    : "=&{v[224:255]}"(w) : "v"(a0), "v"(a1) : "memory");
}
template <int I, int E, class F> __device__ __forceinline__ void sfor(F&& f) { if constexpr (I < E) { f(std::integral_constant<int, I>{}); sfor<I + 1, E>(f); } }
template <int ROLE, int KQ, class F0, class F>
__device__ __forceinline__ void wload(char* smem, int wave_id_, const u16* W0, const u16* W1, int ldb, u32x32 (&bw)[4], int rot, F0&& hook0, F&& hook, unsigned long long* wts = nullptr) {
  constexpr int ABUF = 65536;
  const int tid = opaque_tid(), lane = tid & 63, wave = WAVE_ID;
  const int kq = KQ == 2 ? wave >> 2 : 0, nq = KQ == 2 ? wave & 3 : wave;
  const int g = lane >> 4, r16 = lane & 15, q4 = r16 >> 2, p4 = lane & 3;
  const unsigned ldsb = __builtin_amdgcn_readfirstlane((unsigned)(size_t)(LAS char*)smem);
  {
    const int srow = 8 * wave + (lane >> 3), ch = (lane & 7) ^ bswz(srow);
    const unsigned voff = (unsigned)(srow * ldb + ch * 8) * 2;
    const char* w0 = uni((const char*)W0); const char* w1 = uni((const char*)(ROLE == R_F1 ? W1 : W0 + 64));
    const size_t kb = (size_t)64 * ldb * 2;
    auto wsrc = [&](int r, int i) {
      const int kbi = KQ == 2 ? (ROLE == R_PART2 ? 0 : (i >> 2) * 8) + ((2 * r + ((i >> 1) & 1)) ^ rot) : 2 * r + (i >> 2);
      return KQ == 2 ? ((i & 1) ? w1 : w0) + kbi * kb + (ROLE == R_PART2 ? (i >> 2) * 256 : 0) : w0 + kbi * kb + (i & 3) * 128;
    };
    auto wstage = [&](int r) {
      const unsigned ld = ldsb + (r & 1) * ABUF + wave * 1024;
#pragma unroll
      for (int i = 0; i < 8; ++i) {
        unsigned keep;
        asm volatile("s_mov_b32 %0, m0\n\ts_mov_b32 m0, %3\n\ts_nop 0\n\tglobal_load_lds_dwordx4 %1, %2" WNT_ "\n\ts_mov_b32 m0, %0"
                     : "=&s"(keep) : "v"(voff), "s"(wsrc(r, i)), "s"(ld + i * 8192) : "memory");
      }
    };
    const int brow = 8 * g + q4, bs = bswz(brow);
    unsigned la[2];
#pragma unroll
    for (int j = 0; j < 2; ++j) {
      int blk, c16, sub;
      if (ROLE == R_F1) { blk = j; c16 = nq * 2 + (p4 >> 1); sub = (p4 & 1) * 8; }
      else { blk = nq >> 1; c16 = (nq & 1) * 4 + p4; sub = j * 8; }
      int set = kq;
      if (ROLE == R_PART2 && TLP) { set = nq >> 1; blk = nq & 1; c16 = kq * 4 + p4; }
      la[j] = (unsigned)((KQ == 2 ? set * 4 + blk : blk) * 8192 + brow * 128 + ((c16 ^ bs) << 4) + sub);
    }
    wstage(0); wstage(1);
    u32x4 raw[2][8];
#pragma unroll
    for (int r = 0; r < 2; ++r)
#pragma unroll
      for (int i = 0; i < 8; ++i) asm volatile("global_load_dwordx4 %0, %1, %2" WNT_ : "=&v"(raw[r][i]) : "v"(voff), "s"(wsrc(r + 2, i)) : "memory");
    sfor<0, 4>([&](auto r_c) __attribute__((always_inline)) {
      constexpr int r = decltype(r_c)::value;
      if (r == 0) WAIT_V(24); else if (r == 1) WAIT_V(16);
      if (r == 3) { BAR(); hook(); } else if (r < 2) { __builtin_amdgcn_s_barrier(); asm volatile("" ::: "memory"); }
      gw_load<r>(bw[r], ldsb + (r & 1) * ABUF + la[0], ldsb + (r & 1) * ABUF + la[1]);
      if (r == 1 || r == 2) {
        u32x4 (&q)[8] = raw[(r + 1) & 1];
        if (r == 1) hook0();
        if (r == 1) asm volatile("s_waitcnt vmcnt(8)" : "+v"(q[0]), "+v"(q[1]), "+v"(q[2]), "+v"(q[3]), "+v"(q[4]), "+v"(q[5]), "+v"(q[6]), "+v"(q[7]) :: "memory");
        else asm volatile("s_waitcnt vmcnt(0)" : "+v"(q[0]), "+v"(q[1]), "+v"(q[2]), "+v"(q[3]), "+v"(q[4]), "+v"(q[5]), "+v"(q[6]), "+v"(q[7]) :: "memory");
#pragma unroll
        for (int i = 0; i < 8; ++i) *(u32x4*)(smem + ((r + 1) & 1) * ABUF + wave * 1024 + i * 8192 + lane * 16) = q[i];
        if (r == 1) BAR();
      }
      if (r == 3) BAR();
    });
  }
}
#define DSR2(d0, d1, a0_, a1_, off) asm volatile("ds_read_b128 %0, %2 offset:%4\n\tds_read_b128 %1, %3 offset:%4" : "=&v"(d0), "=&v"(d1) : "v"(a0_), "v"(a1_), "i"(off))
template <int N> __device__ __forceinline__ void waitl(u32x4& a, u32x4& b) { asm volatile("s_waitcnt lgkmcnt(%2)" : "+v"(a), "+v"(b) : "i"(N)); }
__device__ __forceinline__ void glds1(const char* sbase, unsigned voff, unsigned lds) {
  unsigned keep;
  asm volatile("s_mov_b32 %0, m0\n\ts_mov_b32 m0, %3\n\ts_nop 0\n\tglobal_load_lds_dwordx4 %1, %2 sc1\n\ts_mov_b32 m0, %0"
               : "=&s"(keep) : "v"(voff), "s"(sbase), "s"(lds) : "memory");
}
#define XS(i) do { } while (0)
#define XFL(b_) do { } while (0)
#define XA_ 0
#define XF_ 0
#define XE_ 0
#define XP_ 0
#define PUBK 9
#define LATE 0
#define NOWP 2
#define NEARM 15
#define PNEAR 2
#define LATEQ 0
#define DEFP 1
#define ROTM 0
#define ROTK 7
#define BPACE1 46
#define BPACE2 52
#define BPACE 40
#define W2 1
#define PDEEP 4
template <int ROLE>
__device__ __forceinline__ void gemm7(char* smem, int wave_id_, const u16* W0, const u16* W1, int ldb, const char* A_, size_t a_blk, Dep dep,
                                      char* C_, size_t c_blk, unsigned* myprog, unsigned* tmo, int slot, unsigned long long* mts,
                                      bool gl, int nimm = 0, int nseg = NSEG, int wm0 = 0, int pofs = 0, unsigned long long tag = 0, unsigned* par = nullptr) {
  volatile LAS unsigned* wsl = (volatile LAS unsigned*)(smem + L_SLOT + 512);
  if (opaque_tid() < 144) *(volatile LAS unsigned*)(smem + L_SLOT + opaque_tid() * 4) = 0u;
  const int wave = WAVE_ID;
  const int kq = wave >> 2, nq = wave & 3;
  const unsigned ldsb = __builtin_amdgcn_readfirstlane((unsigned)(size_t)(LAS char*)smem);
  const int rot = (ROTM >> ROLE) & 1 ? slot & ROTK : 0;
  const char* A = uni(A_) + nq * 1024;
  constexpr bool NEAR = (NEARM >> ROLE) & 1;
  constexpr int AH = NEAR ? 2 : 3, PD = NEAR ? PNEAR : PDEEP;
  constexpr bool P2C = ROLE == R_PART2;
  int pw = 0;
  int wm = wm0, nh = 0;
  auto lpoll = [&](int b, bool hs = false) {
    unsigned w;
    if (wave == 4) {
      for (int spins = 0;; ++spins) {
        unsigned v;
        if (hs) { WAIT_V(0); v = *(volatile LAS unsigned*)(smem + L_SLOT + 768 + lane_id() * 4); }
        else asm volatile("s_nop 4\n\tglobal_load_dword %0, %1, %2 sc1\n\ts_waitcnt vmcnt(0)" : "=&v"(v) : "v"(dep.voff), "s"(dep.base) : "memory");
        w = dep_eval(v, dep);
        if ((int)w > b) break;
        if (!hs) __builtin_amdgcn_s_sleep(2);
        hs = false;
        if (give_up(spins, tmo, 9)) { w = NSEG; break; }
      }
      w = w < 255u ? w : 255u;
      if (lane_id() == 0) wsl[0] = ((unsigned)(b + 1) << 8) | w;
    } else {
      for (;;) {
        w = __builtin_amdgcn_readfirstlane(wsl[0]);
        if ((w >> 8) == (unsigned)(b + 1)) break;
        __builtin_amdgcn_s_sleep(1);
      }
      w &= 255u;
    }
    return (int)w;
  };
  auto pubw = [&](int n) {
    WAIT_V(0);
    if (lane_id() == 0) {
      const unsigned o = __hip_atomic_fetch_add((LAS unsigned*)(smem + L_SLOT + 516), 1u, __ATOMIC_RELAXED, __HIP_MEMORY_SCOPE_WORKGROUP);
      if ((o & 3u) == 3u) {
        st4_sc1(myprog, (unsigned)(n + pofs));
      }
    }
    asm volatile("s_waitcnt lgkmcnt(0)" ::: "memory");
  };
  auto hsrc = [&](int hh) { return A + (size_t)(hh >> 1) * a_blk + ((hh ^ (rot >> 2)) & 1) * 16384; };
  auto hlds = [&](int hh) { return ldsb + (unsigned)(((hh >> 1) & 1) * 65536 + (hh & 1) * 16384 + nq * 1024); };
#define POFF(i_) (((i_) & 1) * 32768 + ((i_) >> 1) * 4096)
#define PIECE(s_, l_, i_) asm volatile("s_add_u32 m0, %2, %3\n\ts_nop 0\n\tglobal_load_lds_dwordx4 %0, %1 sc1" :: "v"(av[i_]), "s"(s_), "s"(l_), "i"(POFF(i_)) : "memory", "scc", "m0")
  u32x32 BW[4];
  KTS(1);
  wload<ROLE, 2>(smem, wave_id_, W0, W1, ldb, BW, rot, [&]() {
    if (ROLE == R_QKV && nimm > 0 && wave == 7) ini_request((const char*)tmo - OFF_TMO, ldsb + L_GB);
  }, [&]() {
    if (ROLE == R_QKV && nimm > 0) {
      *par = ini_resolve((const char*)tmo - OFF_TMO, smem + L_GB, tag);
      dep.base += (*par >> 2) << 13; myprog += (*par >> 2) << 11;
    }
    if (kq) {
      if (wm0 > 0) wait_l1((const unsigned*)dep.base, 8u, tmo);
      else wm = lpoll(0);
      const unsigned l16 = lane_id() * 16;
#pragma unroll
      for (int i = 0; i < 16; ++i)
        if (!(P2C && (i & 1))) glds1(hsrc(i >> 3) + (i & 1) * 32768 + ((((i & 7) >> 1) ^ rot) & 3) * 4096, l16, hlds(i >> 3) + POFF(i & 7));
    }
    nh = 2;
  }, ROLE == R_QKV ? (unsigned long long*)((char*)tmo - OFF_TMO + ((size_t)246 << 20)) + (128 + slot) * 8 : nullptr);
  KTS(2);
  if (wm0 == 0 && !kq) wm = (int)(__builtin_amdgcn_readfirstlane(wsl[0]) & 255u);
  if (ROLE == R_QKV && opaque_tid() == 0) STS((char*)tmo - OFF_TMO, 128 + slot, 0);
  auto body = [&](auto ldr_c) __attribute__((always_inline)) {
    constexpr bool LDR = decltype(ldr_c)::value;
    if (XP_ == 0 ? !LDR : (XP_ == 1 ? LDR : false)) __builtin_amdgcn_s_setprio(1);
    unsigned av[8];
    const int lane = lane_id();
#pragma unroll
    for (int i = 0; i < 8; ++i) av[i] = lane * 16 + (P2C ? 0 : (i & 1) * 32768) + (((i >> 1) ^ rot) & 3) * 4096;
    auto issue_all = [&](int hh) {
      if (LDR) {
        const char* s_ = hsrc(hh); const unsigned l_ = hlds(hh);
#pragma unroll
        for (int i = 0; i < 8; ++i) if (!(P2C && (i & 1))) PIECE(s_, l_, i);
      }
    };
    int b = 0;
    bool hs = false, redo = false;
    while (b < nseg) {
      const int ln = lane_id(), g = ln >> 4, r16 = ln & 15;
      const unsigned e0 = ldsb + (unsigned)((P2C ? 0 : kq * 32768) + (b & 1) * 65536 + (gl ? (g >> 1) * 512 + r16 * 32 + (g & 1) * 16 : r16 * 128 + ((g ^ (r16 & 7)) << 4)));
      const unsigned d0 = e0 ^ (gl ? 1024u : 64u);
      const unsigned rw = ldsb + L_RED + nq * 4096 + ln * 16;
      const unsigned co0 = ROLE == R_F1 ? (unsigned)((g & 1) * 2048 + nq * 512 + r16 * 32 + (g >> 1) * 16)
                         : ((ROLE == R_QKV ? TLQ : TLP) ? (unsigned)(nq * (P2C ? 4096 : 2048) + r16 * 64 + g * 16)
                         : (ROLE == R_QKV ? (unsigned)(r16 * 256 + (((4 * nq + g) ^ r16) << 4)) : (unsigned)(r16 * PROW + (32 * nq + 8 * g) * 2)));
      const unsigned co1 = co0 + ((ROLE == R_QKV ? TLQ : TLP) ? 1024u : (ROLE == R_QKV ? 4096u : 16u * PROW));
      u32x16 pv_;
      bool smp = false, lt0 = false;
      if ((wm < nseg ? wm : nseg) >= b + PD && b >= nimm && nh <= 2 * b + AH && !redo) {
        __builtin_amdgcn_s_barrier(); asm volatile("" ::: "memory");
        for (; nh < 2 * b + AH; ++nh) issue_all(nh);
        int lf = (wm < nseg ? wm : nseg) - (AH - 1), cp = nseg - (AH - 1);
        if (!LDR) KTS(128 + b);
        const unsigned sa = ldsb + L_SLOT + ln * 4, ad = dep.add - (AH - 1);
        if constexpr (LDR) (NEAR ? (P2C ? near_ld2 : near_ld0) : (P2C ? fast_ld2 : fast_ld0))(BW, pv_, e0, d0, b, lf, cp, sa, dep.mul, ad, rw, (unsigned)a_blk, hsrc(2 * b + AH), hsrc(2 * b + AH + 1), hlds(2 * b + AH), hlds(2 * b + AH + 1), av);
        else {
          const char* c0_ = uni((const char*)tmo - OFF_TMO + OFF_DUMP); const char* c1_ = uni(C_ + (size_t)b * c_blk);
          const int sf = b + pofs - (pw > 0 ? 1 : 0);
          const char* pg = uni((const char*)myprog);
          (NEAR ? (ROLE == R_F1 ? near_st1 : (P2C ? near_st2 : near_st0)) : (ROLE == R_F1 ? fast_st1 : (P2C ? fast_st2 : fast_st0)))(BW, pv_, e0, d0, b, lf, cp, sa, dep.mul, ad, rw, (unsigned)c_blk, c0_, c1_, co0, co1, pofs, sf, (unsigned)wave,
                                                                   ldsb + L_SLOT, dep.voff, dep.base, pg);
        }
        if (!LDR) KTS(64 + b);
        wm = lf + (AH - 1) > wm ? lf + (AH - 1) : wm;
        nh = 2 * b + AH;
        --b;
        __builtin_amdgcn_s_barrier(); asm volatile("" ::: "memory");
      } else if (NEAR && b >= (ROLE == R_QKV ? nseg - 1 : (gl ? BPACE2 : (ROLE == R_F1 ? BPACE1 : BPACE))) && !redo) {
        if constexpr (LDR) {
          int cap = wave == 4 ? SPIN_CAP : 8 * SPIN_CAP;
          if (hs) WAIT_V(0);
          (P2C ? paced_ld2 : paced_ld0)(BW, e0, d0, b, nseg, (unsigned)a_blk, rw, ldsb + L_SLOT + 528 + nq * 4, hsrc(nh), hsrc(nh + 1), hlds(nh), cap, wm, (unsigned)(nh - 2 * b),
                                        dep.mul, dep.add, dep.voff + (ROLE == R_QKV ? 0 : (ln >> 5) * 4), dep.base, wave == 4 ? ldsb + L_SLOT : 0u, ldsb + L_SLOT + (ln & 31) * 4, av);
          if (cap == 0 && ln == 0) st4_sc1(tmo, 15u);
        } else (ROLE == R_F1 ? paced_st1 : (P2C ? paced_st2 : paced_st0))(BW, e0, d0, b, nseg, (unsigned)c_blk, rw, ldsb + L_SLOT + 528 + nq * 4, uni(C_ + (size_t)b * c_blk),
                                                                          __builtin_amdgcn_readfirstlane(pw > 0 ? 1u : 0u), ROLE == R_QKV ? 999 : BEARLY, pofs, ldsb + L_SLOT + 516,
                                                                          uni((const char*)myprog), co0, co1);
        const int fb = (int)__builtin_amdgcn_readfirstlane(*(volatile LAS unsigned*)(smem + L_SLOT + 544 + nq * 4));
        if (fb == 0) { if (!LDR && ROLE != R_QKV) pubw(nseg); break; }
        WAIT_V(0); BAR();
        if (LDR && ln == 0) { *(volatile LAS unsigned*)(smem + L_SLOT + 528 + nq * 4) = 0u; *(volatile LAS unsigned*)(smem + L_SLOT + 544 + nq * 4) = 0u; wsl[0] = 0u; }
        BAR();
        b = fb - 1; nh = 2 * b; wm = b; pw = 0; hs = false; redo = true;
        if (!LDR) pubw(b);
        continue;
      } else {
        const bool pl = wm <= b;
        XS(0);
        if (pl && LDR) wm = lpoll(b, hs);
        XS(1);
        const bool cold = nh < 2 * b + 2;
        { const int upto = nh == 2 * b + 3 ? 2 * b + 4 : 2 * b + 2; for (; nh < upto; ++nh) issue_all(nh); }
        smp = nh == 2 * b + 2 && b + 1 < nseg && wm0 < nseg;
        if (LDR && wave == 4 && smp)
          asm volatile("s_mov_b32 m0, %0\n\ts_nop 3\n\tglobal_load_lds_dword %1, %2 sc1" :: "s"(ldsb + L_SLOT + (b & 1) * 256), "v"(dep.voff), "s"(dep.base) : "memory", "m0");
        XS(2);
        if (LDR && LEAN_QB) { if (nh >= 2 * b + 4) { if (P2C) WAIT_V(14); else WAIT_V(28); } else { if (P2C) WAIT_V(6); else WAIT_V(12); } }
        else if (LDR) { if (nh >= 2 * b + 4) { if (P2C) WAIT_V(12); else WAIT_V(24); } else { if (P2C) WAIT_V(4); else WAIT_V(8); } }
        BAR();
        XS(3);
        if (pl && !LDR) wm = (int)(__builtin_amdgcn_readfirstlane(wsl[0]) & 255u);
        const bool dm = nh == 2 * b + 2 && wm > b + 1 && b + 1 < nseg;
        if (ROLE == R_QKV && b < 2 && opaque_tid() == 0) STS((char*)tmo - OFF_TMO, 128 + slot, 1 + b);
        lt0 = smp && !dm;
        if constexpr (LDR) {
          if (dm) (P2C ? lean_dm1 : lean_dm0)(BW, pv_, e0, d0, 0u, hlds(nh), hsrc(nh), hsrc(nh + 1), av);
          else (P2C ? lean_ld1 : lean_ld0)(BW, pv_, e0, d0, (unsigned)(nh - 2 * b - 2), hlds(nh), hsrc(nh), hsrc(nh + 1), av, ldsb + L_SLOT + (b & 1) * 256 + ln * 4, dep.mul, dep.add, LATEQ && lt0 ? b + 1 : 0x7FFFFFFF,
                                           __builtin_amdgcn_readfirstlane(lt0 && wave == 4 ? ldsb + L_SLOT + 768 : 0u), dep.voff, dep.base);
        }
        else lean_st0(BW, pv_, e0, d0, __builtin_amdgcn_readfirstlane(pw > 0 ? (cold ? 4u : 1u) : 0u), (unsigned)(pw + pofs), ldsb + L_SLOT + 516, ldsb + L_SLOT + 256,
                     dep.voff, dep.base, uni((const char*)myprog));
        if (dm) nh += 2;
        XS(5);
      }
      if constexpr (LDR) fin_ld(pv_, rw, ldsb + L_SLOT + 528 + nq * 4, (unsigned)(b + 1));
      else (ROLE == R_F1 ? fin_st1 : (P2C ? fin_st2 : fin_st0))(pv_, rw, ldsb + L_SLOT + 528 + nq * 4, (unsigned)(b + 1), uni(C_ + (size_t)b * c_blk), co0, co1);
      XS(6);
      if (smp) {
        const int w = (int)dep_eval(*(volatile LAS unsigned*)(smem + L_SLOT + (b & 1) * 256 + lane_id() * 4), dep);
        if (LATEQ && lt0 && w > b + 1) nh += 2;
        wm = w > wm ? w : wm;
      }
      bool now = (NOWP == 2 ? nh < 2 * b + 4 && wm < b + 1 + PD : wm <= b + 1) || b + 1 >= nseg || b < nimm;
      if (!LDR && now) {
        if (DEFP && b >= nimm && b + 1 < nseg && (wm > b + 1 || (__builtin_amdgcn_readfirstlane(wsl[0]) >> 8) == (unsigned)(b + 2))) now = false;
        else pubw(b + 1);
      }
      XS(7);
      XFL(b);
      pw = now ? 0 : b + 1;
      hs = W2 && lt0;
      redo = false;
      ++b;
    }
    __builtin_amdgcn_s_setprio(0);
  };
  if (kq) body(std::true_type{}); else body(std::false_type{});
#undef PIECE
#undef POFF
  WAIT_V(0);
  BAR();
  KTS(3);
}
__device__ __forceinline__ void fin_role(char* smem, int wave_id_, int fidx, const float* mod, const char* P2, const u16* x1, u16* out, unsigned* cnt, unsigned* tmo,
                                         unsigned long long* mts) {
  const int tid = opaque_tid(), lane = tid & 63, wave = WAVE_ID;
  row_table(smem + L_GB, mod, cnt, tmo, tid);
  volatile LAS unsigned* wsl = (volatile LAS unsigned*)(smem + L_SLOT + 512);
  int wf = 0, par = 0;
  for (int nf = fidx >> 3; nf < NSEG; nf += 2) {
    if (wf <= nf) {
      if (wave == 0) {
        unsigned w;
        for (int spins = 0;; ++spins) {
          u32x2 v2; asm volatile("global_load_dwordx2 %0, %1, off sc1\n\ts_waitcnt vmcnt(0)" : "=&v"(v2) : "v"(cnt + (PF2 + ((lane & 31) < 28 ? (lane & 31) : 0)) * 32) : "memory");
          const unsigned v = rowmin16(max(v2.x, v2.y));
          w = min(rdl(v, 0), rdl(v, 16));
          if ((int)w > nf) break;
          __builtin_amdgcn_s_sleep(2);
          if (give_up(spins, tmo, 9)) { w = NSEG; break; }
        }
        if (lane == 0) wsl[par] = w;
      }
      BAR();
      wf = (int)__builtin_amdgcn_readfirstlane(wsl[par]);
      par ^= 1;
    }
    int ln = lane; asm volatile("" : "+v"(ln));
    if (wave == 0) WTS(256 + 8 + fidx, nf / 2, 0);
    for (bool chk = nf >= BEARLY; !fin_half(P2, x1, smem + L_GB + 12288, out, nf * 32 + 4 * (fidx & 7) + (wave >> 1), wave & 1, ln, chk); chk = false)
      for (int spins = 0;; ++spins) {
        const unsigned v = ld4_sc1(cnt + (ln < 28 ? PF2 + ln : PLN + fidx) * 32);
        if (__all((ln < 28 ? v : 2 * v) > (unsigned)nf)) break;
        __builtin_amdgcn_s_sleep(2);
        if (give_up(spins, tmo, 9)) break;
      }
    if (wave == 0) WTS(256 + 8 + fidx, nf / 2, 2);
  }
}
constexpr int Q_MEM = 0;
constexpr int Q_QK  = 16384;
constexpr int Q_TM  = 22528;
constexpr int Q_PQ  = 24576;
constexpr int Q_K   = 32768;
constexpr int Q_RING = 32768;
constexpr int NCHAIN = 96;
#define NQQ 6
#define NQK 4
#define E_AG 7
#define RDYI -1
#define ROTW 2
#define DQU 6
#define DQM 18
#define DQD 1
#define ORSEL (xg >> 1)
#define D_Q 96
#define D_O 3
#define D_A 0
#define WOF 1
#define E_S 0
#define SPF 0
#define STE 0
#define RAE 0
#define SPUB 62
#define QDL 0
#define KFB 1
#define E_B1 0
#define ADQ 1
#define D_V 4
#define DRY 0
#define D_0 72
#define RAW 5
#define E_MIN 3
#define E_W 3
#define DOU 0
#define DOM 12
#define CL0 1
#define TSW 0
#define E_B 6
#define E_O 1900
#define KDEL 5
#define VPOS 0

typedef __bf16 bf16x16 __attribute__((ext_vector_type(16)));
typedef unsigned u32x8 __attribute__((ext_vector_type(8)));
__device__ __forceinline__ f32x4 SMF(u32x4 a, u32x8 b, f32x4 c, int idx) {
  return __builtin_amdgcn_smfmac_f32_16x16x64_bf16(__builtin_bit_cast(bf16x8, a), __builtin_bit_cast(bf16x16, b), c, idx, 0, 0);
}
__device__ __forceinline__ float fold32(float a, float b) {
  const auto p = __builtin_amdgcn_permlane32_swap(__float_as_uint(a), __float_as_uint(b), false, false);
  return __uint_as_float(p[0]) + __uint_as_float(p[1]);
}
__device__ __forceinline__ f32x2 rowsum(f32x4 x) { return f32x2{fold32(x[0], x[2]), fold32(x[1], x[3])}; }
__device__ __forceinline__ unsigned nosent1(unsigned v) { return v < 0xFFFFFFFEu ? v : 0xFFFFFFFEu; }
__device__ __forceinline__ void st4s(const char* sb, unsigned vo, unsigned v) { asm volatile("global_store_dword %0, %1, %2 sc1\n\ts_nop 0" :: "v"(vo), "v"(v), "s"(sb) : "memory"); }
__device__ __forceinline__ void st8s(const char* sb, unsigned vo, u32x2 v) { asm volatile("global_store_dwordx2 %0, %1, %2 sc1\n\ts_nop 1" :: "v"(vo), "v"(v), "s"(sb) : "memory"); }
__device__ __forceinline__ void gldsw(const char* sbase, unsigned voff, unsigned lds) {
  unsigned keep;
  asm volatile("s_mov_b32 %0, m0\n\ts_mov_b32 m0, %3\n\ts_nop 0\n\tglobal_load_lds_dwordx4 %1, %2" CNT_ "\n\ts_mov_b32 m0, %0"
               : "=&s"(keep) : "v"(voff), "s"(sbase), "s"(lds) : "memory");
}
#define PWC 1
#define PWL 0
__device__ __forceinline__ void wait_clk(unsigned t0, unsigned d) {
  while ((unsigned)__builtin_amdgcn_s_memtime() - t0 < d) __builtin_amdgcn_s_sleep(PWC);
}
__device__ __forceinline__ unsigned bell_time(const char* a, unsigned vo, unsigned tag, unsigned cap, unsigned v = 0) {
  for (unsigned spins = 0; !__all((v >> 24) >= tag); ++spins) {
    asm volatile("s_nop 4\n\tglobal_load_dword %0, %1, %2 sc1\n\ts_waitcnt vmcnt(0)" : "=&v"(v) : "v"(vo), "s"(a) : "memory");
    if (__all((v >> 24) >= tag) || spins >= cap) break;
  }
  const unsigned now = (unsigned)__builtin_amdgcn_s_memrealtime() + 250u;
  const unsigned age = rowmin16((now - v) & 0xFFFFFFu);
  unsigned r = __builtin_amdgcn_readfirstlane(now - min(min(min(rdl(age, 0), rdl(age, 16)), min(rdl(age, 32), rdl(age, 48))), 500u));
  asm volatile("" : "+s"(r));
  return r;
}
#define PWS 0
__device__ __forceinline__ void wait_rt(unsigned t0, int d) {
  for (;;) {
    const int r = d - (int)((unsigned)__builtin_amdgcn_s_memrealtime() - t0);
    if (r <= 0) break;
    if (PWS && r > 24) __builtin_amdgcn_s_sleep(6); else if (PWS && r > 9) __builtin_amdgcn_s_sleep(2);
  }
}
#define WBAR() do { __builtin_amdgcn_s_barrier(); asm volatile("" ::: "memory"); } while (0)
#define SB() __builtin_amdgcn_sched_barrier(0)
#define TN(k, v) do { } while (0)
template <bool HALF>
__device__ __forceinline__ void stage_proj(const char* src, unsigned lds, int tid, int wave) {
  const int row = tid >> 4, f = 2 * ((row & 1) | (((row >> 2) & 3) << 1));
#pragma unroll
  for (int j = 0; j < 4; ++j) {
    const int pc = (tid & 15) ^ f;
    gldsw(src + (size_t)j * 32 * HD * 2, (unsigned)(HALF ? (row + (pc >> 3) * 128) * HD * 2 + ((pc & 7) << 4) : row * HD * 2 + (pc << 4)), lds + (wave * 64 + 512 * j) * 16);
  }
}
__device__ __forceinline__ unsigned proj_base(int T, int lane) {
  const int g = lane >> 4, q4 = (lane & 15) >> 2, p4 = lane & 3, fs = 2 * ((q4 & 1) | ((g & 1) << 1) | ((q4 >> 1) << 2));
  return (unsigned)((16 * (g >> 1) + 4 * (g & 1) + (q4 & 1) + 8 * (q4 >> 1)) * 256 + (((2 * T) ^ fs) << 4) + p4 * 8);
}
__device__ __forceinline__ f32x4 zero4() { float z = 0.0f; asm volatile("" : "+v"(z)); return f32x4{z, z, z, z}; }
__device__ __forceinline__ unsigned lds_word(const char* p) { return __builtin_amdgcn_readfirstlane(*(volatile LAS unsigned*)p); }

#define MFMA16(a, b, c) __builtin_amdgcn_mfma_f32_16x16x16bf16_1k((a), (b), (c), 0, 0, 0)
#define UF(x) __uint_as_float(x)
__device__ __forceinline__ unsigned umax3(unsigned a, unsigned b, unsigned c) { return max(max(a, b), c); }
__device__ __forceinline__ f32x2 logit2(float a, float b) {
  return f32x2{a, b};
}
constexpr float LSC = LOG2E * SCALE;

#define FCS() do { } while (0)
struct QKW { u32x32 w0, w1, w2, w3, w4, w5; };
template <int W_> __device__ __forceinline__ void qk_step(const QKW& W, const char* ag, const char* vs, const char* v2, const char* db, const char* od, unsigned pb, unsigned sn, unsigned& cap, unsigned& sp,
    unsigned long long& tc, unsigned long long* ts, unsigned& d0, unsigned& d1);
template <> __device__ __forceinline__ void qk_step<0>(const QKW& W, const char* ag, const char* vs, const char* v2, const char* db, const char* od, unsigned pb, unsigned sn, unsigned& cap, unsigned& sp,
    unsigned long long& tc, unsigned long long* ts, unsigned& d0, unsigned& d1) {
  unsigned st, bg;
  asm volatile("v_mbcnt_lo_u32_b32 v60, -1, 0\n\t"
    "v_mbcnt_hi_u32_b32 v60, -1, v60\n\t"
    "v_lshlrev_b32 v5, 4, v60\n\t"
    "s_mov_b32 %[sp], 0\n\t"
    "s_setprio 2\n\t"
    "global_load_dwordx4 v[24:27], v5, %[ag] sc1\n\t"
    "global_load_dwordx4 v[28:31], v5, %[ag] offset:1024 sc1\n\t"
    "s_memtime %[tc]\n\t"
    "v_lshrrev_b32 v32, 4, v60\n\t"
    "v_and_b32 v33, 15, v60\n\t"
    "v_lshrrev_b32 v34, 2, v33\n\t"
    "v_and_b32 v35, 3, v60\n\t"
    "v_and_b32 v61, 1, v60\n\t"
    "v_lshlrev_b32 v61, 3, v61\n\t"
    "v_lshl_or_b32 v7, v34, 4, v61\n\t"
    "v_lshl_or_b32 v7, v32, 7, v7\n\t"
    "v_and_b32 v10, 2, v34\n\t"
    "v_xor_b32 v10, v10, v32\n\t"
    "v_lshl_or_b32 v10, v10, 4, %[pb]\n\t"
    "v_lshl_or_b32 v10, v33, 6, v10\n\t"
    "v_and_b32 v14, 32, v60\n\t"
    "v_lshl_or_b32 v14, v35, 3, v14\n\t"
    "v_lshrrev_b32 v34, 2, v60\n\t"
    "v_lshl_or_b32 v14, v34, 6, v14\n\t"
    "v_or_b32 v14, %[pb], v14\n\t"
    "v_and_b32 v61, 3, v60\n\t"
    "v_lshlrev_b32 v61, 6, v61\n\t"
    "v_lshl_or_b32 v4, v32, 4, v61\n\t"
    "v_and_b32 v61, 4, v60\n\t"
    "v_lshl_or_b32 v4, v61, 10, v4\n\t"
    "v_and_b32 v61, 8, v60\n\t"
    "v_lshl_or_b32 v4, v61, 14, v4\n\t"
    "v_or_b32 v4, 0, v4\n\t"
    "v_and_b32 v61, 2, v60\n\t"
    "v_mul_u32_u24 v61, 0x5555, v61\n\t"
    "v_or_b32 v6, %[c44], v61\n\t"
    "v_lshlrev_b32 v61, 2, v32\n\t"
    "v_lshl_or_b32 v15, v33, 4, v61\n\t"
    "3:\n\t"
    "v_mov_b32 v16, 0\n\t"
    "v_mov_b32 v17, 0\n\t"
    "v_mov_b32 v18, 0\n\t"
    "v_mov_b32 v19, 0\n\t"
    "v_mov_b32 v20, 0\n\t"
    "v_mov_b32 v21, 0\n\t"
    "v_mov_b32 v22, 0\n\t"
    "v_mov_b32 v23, 0\n\t"
    "s_waitcnt vmcnt(0) lgkmcnt(0)\n\t"
    "33:\n\t"
    "ds_write_b128 v5, v[24:27] offset:0\n\t"
    "ds_write_b128 v5, v[28:31] offset:1024\n\t"
    "v_smfmac_f32_16x16x64_bf16 v[16:19], v[24:27], v[64:71], v6\n\t"
    "v_max3_u32 v60, v24, v25, v26\n\t"
    "v_max3_u32 v61, v27, v28, v29\n\t"
    "v_smfmac_f32_16x16x64_bf16 v[20:23], v[24:27], v[72:79], v6\n\t"
    "v_max3_u32 v60, v60, v30, v31\n\t"
    "v_max_u32 v60, v60, v61\n\t"
    "v_cmp_eq_u32 vcc, -1, v60\n\t"
    "s_cbranch_vccnz 1f\n\t"
    "2:\n\t"
    "s_waitcnt lgkmcnt(0)\n\t"
    "s_barrier\n\t"
    "ds_read_b128 v[32:35], v5 offset:2048\n\t"
    "ds_read_b128 v[36:39], v5 offset:3072\n\t"
    "v_smfmac_f32_16x16x64_bf16 v[16:19], v[28:31], v[80:87], v6\n\t"
    "ds_read_b128 v[40:43], v5 offset:4096\n\t"
    "ds_read_b128 v[44:47], v5 offset:5120\n\t"
    "v_smfmac_f32_16x16x64_bf16 v[20:23], v[28:31], v[88:95], v6\n\t"
    "s_waitcnt lgkmcnt(3)\n\t"
    "v_smfmac_f32_16x16x64_bf16 v[16:19], v[32:35], v[96:103], v6\n\t"
    "ds_read_b128 v[48:51], v5 offset:6144\n\t"
    "v_smfmac_f32_16x16x64_bf16 v[20:23], v[32:35], v[104:111], v6\n\t"
    "ds_read_b128 v[52:55], v5 offset:7168\n\t"
    "s_waitcnt lgkmcnt(4)\n\t"
    "v_smfmac_f32_16x16x64_bf16 v[16:19], v[36:39], v[112:119], v6\n\t"
    "ds_read_b128 v[24:27], v5 offset:8192\n\t"
    "v_smfmac_f32_16x16x64_bf16 v[20:23], v[36:39], v[120:127], v6\n\t"
    "ds_read_b128 v[28:31], v5 offset:9216\n\t"
    "s_waitcnt lgkmcnt(5)\n\t"
    "v_smfmac_f32_16x16x64_bf16 v[16:19], v[40:43], v[128:135], v6\n\t"
    "ds_read_b128 v[56:59], v5 offset:10240\n\t"
    "v_smfmac_f32_16x16x64_bf16 v[20:23], v[40:43], v[136:143], v6\n\t"
    "ds_read_b128 v[60:63], v5 offset:11264\n\t"
    "ds_read_b128 v[36:39], v10 offset:35840\n\t"
    "s_waitcnt lgkmcnt(7)\n\t"
    "v_smfmac_f32_16x16x64_bf16 v[16:19], v[44:47], v[144:151], v6\n\t"
    "ds_read_b128 v[40:43], v10 offset:36864\n\t"
    "v_smfmac_f32_16x16x64_bf16 v[20:23], v[44:47], v[152:159], v6\n\t"
    "ds_read_b128 v[44:47], v10 offset:37888\n\t"
    "s_waitcnt lgkmcnt(8)\n\t"
    "v_smfmac_f32_16x16x64_bf16 v[16:19], v[48:51], v[160:167], v6\n\t"
    "v_smfmac_f32_16x16x64_bf16 v[20:23], v[48:51], v[168:175], v6\n\t"
    "ds_read_b128 v[48:51], v10 offset:38912\n\t"
    "s_waitcnt lgkmcnt(8)\n\t"
    "v_smfmac_f32_16x16x64_bf16 v[16:19], v[52:55], v[176:183], v6\n\t"
    "v_smfmac_f32_16x16x64_bf16 v[20:23], v[52:55], v[184:191], v6\n\t"
    "ds_read_b128 v[52:55], v10 offset:39936\n\t"
    "s_waitcnt lgkmcnt(8)\n\t"
    "v_smfmac_f32_16x16x64_bf16 v[16:19], v[24:27], v[192:199], v6\n\t"
    "v_smfmac_f32_16x16x64_bf16 v[20:23], v[24:27], v[200:207], v6\n\t"
    "ds_read_b128 v[24:27], v10 offset:32768\n\t"
    "s_waitcnt lgkmcnt(8)\n\t"
    "v_smfmac_f32_16x16x64_bf16 v[16:19], v[28:31], v[208:215], v6\n\t"
    "ds_read_b32 v8, v15 offset:28672\n\t"
    "ds_read_b128 v[32:35], v5 offset:24576\n\t"
    "s_setprio 3\n\t"
    "s_waitcnt lgkmcnt(9)\n\t"
    "v_smfmac_f32_16x16x64_bf16 v[16:19], v[56:59], v[224:231], v6\n\t"
    "s_waitcnt lgkmcnt(8)\n\t"
    "v_smfmac_f32_16x16x64_bf16 v[16:19], v[60:63], v[240:247], v6\n\t"
    "v_smfmac_f32_16x16x64_bf16 v[20:23], v[28:31], v[216:223], v6\n\t"
    "s_waitcnt lgkmcnt(0)\n\t"
    "21:\n\t"
    "v_cmp_ne_u32 vcc, %[sn], v8\n\t"
    "s_cbranch_vccnz 22f\n\t"
    "v_mul_f32 v32, %[lsc], v32\n\t"
    "v_mul_f32 v33, %[lsc], v33\n\t"
    "v_smfmac_f32_16x16x64_bf16 v[20:23], v[56:59], v[232:239], v6\n\t"
    "v_mul_f32 v34, %[lsc], v34\n\t"
    "v_mul_f32 v35, %[lsc], v35\n\t"
    "v_add_f32 v16, v16, v18\n\t"
    "v_add_f32 v17, v17, v19\n\t"
    "v_smfmac_f32_16x16x64_bf16 v[20:23], v[60:63], v[248:255], v6\n\t"
    "v_fma_f32 v16, v16, %[lsc], v32\n\t"
    "v_fma_f32 v17, v17, %[lsc], v33\n\t"
    "v_cvt_pk_bf16_f32 v16, v16, v17\n\t"
    "ds_write_b32 v15, v16 offset:16384\n\t"
    "s_mov_b32 %[bg], %[big]\n\t"
    "s_nop 2\n\t"
    "v_add_f32 v20, v20, v22\n\t"
    "v_add_f32 v21, v21, v23\n\t"
    "v_fma_f32 v20, v20, %[lsc], v34\n\t"
    "v_fma_f32 v21, v21, %[lsc], v35\n\t"
    "v_cvt_pk_bf16_f32 v20, v20, v21\n\t"
    "ds_write_b32 v15, v20 offset:16640\n\t"
    "ds_read_b128 v[28:31], v10 offset:33792\n\t"
    "ds_read_b128 v[32:35], v10 offset:34816\n\t"
    "s_waitcnt lgkmcnt(2)\n\t"
    "s_barrier\n\t"
    "ds_read_b64_tr_b16 v[16:17], v7 offset:16384\n\t"
    "ds_read_b64_tr_b16 v[18:19], v7 offset:16448\n\t"
    "ds_read_b64_tr_b16 v[20:21], v7 offset:16896\n\t"
    "ds_read_b64_tr_b16 v[22:23], v7 offset:16960\n\t"
    "ds_read_b64_tr_b16 v[56:57], v7 offset:17408\n\t"
    "ds_read_b64_tr_b16 v[58:59], v7 offset:17472\n\t"
    "ds_read_b64_tr_b16 v[60:61], v7 offset:17920\n\t"
    "ds_read_b64_tr_b16 v[62:63], v7 offset:17984\n\t"
    "global_load_dwordx4 v[6:9], v5, %[vs] sc1\n\t"
    "global_load_dwordx4 v[10:13], v5, %[vs] offset:1024 sc1\n\t"
    "s_waitcnt lgkmcnt(6)\n\t"
    "v_mfma_f32_16x16x32_bf16 v[24:27], v[24:27], v[16:19], 0\n\t"
    "v_mfma_f32_16x16x32_bf16 v[28:31], v[28:31], v[16:19], 0\n\t"
    "s_waitcnt lgkmcnt(4)\n\t"
    "v_mfma_f32_16x16x32_bf16 v[24:27], v[32:35], v[20:23], v[24:27]\n\t"
    "v_mfma_f32_16x16x32_bf16 v[28:31], v[36:39], v[20:23], v[28:31]\n\t"
    "v_xor_b32 v39, 32, v14\n\t"
    "ds_read_b64_tr_b16 v[32:33], v14 offset:40960\n\t"
    "ds_read_b64_tr_b16 v[34:35], v14 offset:41984\n\t"
    "ds_read_b64_tr_b16 v[36:37], v39 offset:40960\n\t"
    "ds_read_b64_tr_b16 v[38:39], v39 offset:41984\n\t"
    "s_waitcnt lgkmcnt(6)\n\t"
    "v_mfma_f32_16x16x32_bf16 v[24:27], v[40:43], v[56:59], v[24:27]\n\t"
    "v_mfma_f32_16x16x32_bf16 v[28:31], v[44:47], v[56:59], v[28:31]\n\t"
    "s_waitcnt lgkmcnt(4)\n\t"
    "v_mfma_f32_16x16x32_bf16 v[24:27], v[48:51], v[60:63], v[24:27]\n\t"
    "v_mfma_f32_16x16x32_bf16 v[28:31], v[52:55], v[60:63], v[28:31]\n\t"
    "v_mov_b32 v52, %[one]\n\t"
    "v_mov_b32 v53, %[one]\n\t"
    "v_mov_b32 v54, %[one]\n\t"
    "v_mov_b32 v55, %[one]\n\t"
    "v_mov_b32 v45, 0\n\t"
    "v_mov_b32 v47, 0\n\t"
    "v_mov_b32 v48, %[one]\n\t"
    "v_max3_f32 v14, |v24|, |v25|, |v26|\n\t"
    "v_max3_f32 v40, |v27|, |v28|, |v29|\n\t"
    "v_max3_f32 v14, v14, |v30|, |v31|\n\t"
    "v_max_f32 v14, v14, v40\n\t"
    "v_cmp_nge_f32 vcc, %[big], v14\n\t"
    "v_mov_b32 v14, 0\n\t"
    "s_cbranch_vccnz 12f\n\t"
    "13:\n\t"
    "v_exp_f32 v24, v24\n\t"
    "v_exp_f32 v25, v25\n\t"
    "v_exp_f32 v26, v26\n\t"
    "v_exp_f32 v27, v27\n\t"
    "v_exp_f32 v28, v28\n\t"
    "v_exp_f32 v29, v29\n\t"
    "v_exp_f32 v30, v30\n\t"
    "v_exp_f32 v31, v31\n\t"
    "v_cvt_pk_bf16_f32 v40, v24, v25\n\t"
    "v_cvt_pk_bf16_f32 v41, v26, v27\n\t"
    "v_cvt_pk_bf16_f32 v42, v28, v29\n\t"
    "v_cvt_pk_bf16_f32 v43, v30, v31\n\t"
    "v_mov_b32 v49, 0\n\t"
    "v_mov_b32 v51, 0\n\t"
    "s_waitcnt lgkmcnt(0)\n\t"
    "v_mfma_f32_16x16x32_bf16 v[52:55], v[52:55], v[40:43], 0\n\t"
    "v_mfma_f32_16x16x32_bf16 v[24:27], v[32:35], v[40:43], 0\n\t"
    "v_mfma_f32_16x16x32_bf16 v[28:31], v[36:39], v[40:43], 0\n\t"
    "s_waitcnt vmcnt(1)\n\t"
    "5:\n\t"
    "v_mfma_f32_16x16x32_bf16 v[32:35], v[6:9], v[16:19], 0\n\t"
    "v_mfma_f32_16x16x32_bf16 v[36:39], v[6:9], v[20:23], 0\n\t"
    "s_waitcnt vmcnt(0)\n\t"
    "v_mfma_f32_16x16x32_bf16 v[32:35], v[10:13], v[56:59], v[32:35]\n\t"
    "v_mfma_f32_16x16x32_bf16 v[36:39], v[10:13], v[60:63], v[36:39]\n\t"
    "ds_read_b32 v44, v15 offset:18432\n\t"
    "ds_read_b32 v46, v15 offset:18688\n\t"
    "v_max3_u32 v40, v6, v7, v8\n\t"
    "v_max3_u32 v41, v9, v10, v11\n\t"
    "v_max3_u32 v40, v40, v12, v13\n\t"
    "v_max_u32 v40, v40, v41\n\t"
    "v_cmp_eq_u32 vcc, -1, v40\n\t"
    "s_cbranch_vccnz 4f\n\t"
    "6:\n\t"
    "s_nop 0\n\t"
    "v_add_f32 v32, v32, v38\n\t"
    "v_add_f32 v33, v33, v39\n\t"
    "v_exp_f32 v34, v32\n\t"
    "v_max_f32 v40, v32, v33\n\t"
    "v_exp_f32 v35, v33\n\t"
    "v_cmp_nge_f32 vcc, %[bg], v40\n\t"
    "s_cbranch_vccnz 10f\n\t"
    "11:\n\t"
    "v_cvt_pk_bf16_f32 v50, v34, v35\n\t"
    "17:\n\t"
    "s_waitcnt vmcnt(0) lgkmcnt(0)\n\t"
    "v_max_u32 v40, v44, v46\n\t"
    "v_cmp_eq_u32 vcc, -1, v40\n\t"
    "s_cbranch_vccnz 18f\n\t"
    "19:\n\t"
    "v_mfma_f32_16x16x16_bf16 v[52:55], v[48:49], v[50:51], v[52:55]\n\t"
    "v_mfma_f32_16x16x16_bf16 v[24:27], v[44:45], v[50:51], v[24:27]\n\t"
    "v_mfma_f32_16x16x16_bf16 v[28:31], v[46:47], v[50:51], v[28:31]\n\t"
    "s_nop 5\n\t"
    "v_rcp_f32 v42, v52\n\t"
    "s_nop 0\n\t"
    "v_pk_mul_f32 v[24:25], v[24:25], v[42:43] op_sel_hi:[1,0]\n\t"
    "v_pk_mul_f32 v[26:27], v[26:27], v[42:43] op_sel_hi:[1,0]\n\t"
    "v_pk_mul_f32 v[28:29], v[28:29], v[42:43] op_sel_hi:[1,0]\n\t"
    "v_pk_mul_f32 v[30:31], v[30:31], v[42:43] op_sel_hi:[1,0]\n\t"
    "v_cvt_pk_bf16_f32 v36, v24, v25\n\t"
    "v_cvt_pk_bf16_f32 v37, v26, v27\n\t"
    "v_cvt_pk_bf16_f32 v38, v28, v29\n\t"
    "v_cvt_pk_bf16_f32 v39, v30, v31\n\t"
    "v_min_u32 v36, -2, v36\n\t"
    "v_min_u32 v37, -2, v37\n\t"
    "v_min_u32 v38, -2, v38\n\t"
    "v_min_u32 v39, -2, v39\n\t"
    "global_store_dwordx4 v4, v[36:39], %[od] sc1\n\t"
    "ds_write_b128 v5, v[6:9] offset:20480\n\t"
    "s_setprio 1\n\t"
    "s_waitcnt lgkmcnt(0)\n\t"
    "s_barrier\n\t"
    "s_waitcnt lgkmcnt(0)\n\t"
    "s_branch 9f\n\t"
    "1:\n\t"
    "s_add_u32 %[sp], %[sp], 1\n\t"
    "s_cmp_ge_u32 %[sp], %[cap]\n\t"
    "s_cbranch_scc1 7f\n\t"
    "global_load_dwordx4 v[24:27], v5, %[ag] sc1\n\t"
    "global_load_dwordx4 v[28:31], v5, %[ag] offset:1024 sc1\n\t"
    "s_waitcnt lgkmcnt(0)\n\t"
    "s_memtime %[tc]\n\t"
    "s_nop 7\n\t"
    "s_branch 3b\n\t"
    "7:\n\t"
    "s_mov_b32 %[cap], 0\n\t"
    "s_branch 2b\n\t"
    "4:\n\t"
    "s_add_u32 %[sp], %[sp], 0x10000\n\t"
    "s_lshr_b32 %[st], %[sp], 16\n\t"
    "s_cmp_ge_u32 %[st], %[cap]\n\t"
    "s_cbranch_scc1 8f\n\t"
    "global_load_dwordx4 v[6:9], v5, %[vs] sc1\n\t"
    "global_load_dwordx4 v[10:13], v5, %[vs] offset:1024 sc1\n\t"
    "s_waitcnt vmcnt(0) lgkmcnt(0)\n\t"
    "s_branch 5b\n\t"
    "8:\n\t"
    "s_mov_b32 %[cap], 0\n\t"
    "s_branch 6b\n\t"
    "18:\n\t"
    "s_add_u32 %[sp], %[sp], 0x10000\n\t"
    "s_lshr_b32 %[st], %[sp], 16\n\t"
    "s_cmp_ge_u32 %[st], %[cap]\n\t"
    "s_cbranch_scc1 20f\n\t"
    "ds_read_b32 v44, v15 offset:18432\n\t"
    "ds_read_b32 v46, v15 offset:18688\n\t"
    "s_waitcnt vmcnt(0) lgkmcnt(0)\n\t"
    "s_branch 17b\n\t"
    "20:\n\t"
    "s_mov_b32 %[cap], 0\n\t"
    "s_branch 19b\n\t"
    "22:\n\t"
    "ds_read_b32 v8, v15 offset:28672\n\t"
    "ds_read_b128 v[32:35], v5 offset:24576\n\t"
    "s_waitcnt lgkmcnt(0)\n\t"
    "s_branch 21b\n\t"
    "10:\n\t"
    "v_max_f32 v40, v40, v14\n\t"
    "v_mov_b32 v42, v40\n\t"
    "s_nop 1\n\t"
    "v_permlane16_swap_b32 v40, v42\n\t"
    "v_max_f32 v40, v40, v42\n\t"
    "v_mov_b32 v42, v40\n\t"
    "s_nop 1\n\t"
    "v_permlane32_swap_b32 v40, v42\n\t"
    "v_max_f32 v40, v40, v42\n\t"
    "v_sub_f32 v34, v32, v40\n\t"
    "v_sub_f32 v35, v33, v40\n\t"
    "v_sub_f32 v40, v14, v40\n\t"
    "v_exp_f32 v40, v40\n\t"
    "v_exp_f32 v34, v34\n\t"
    "v_exp_f32 v35, v35\n\t"
    "v_mul_f32 v24, v24, v40\n\t"
    "v_mul_f32 v25, v25, v40\n\t"
    "v_mul_f32 v26, v26, v40\n\t"
    "v_mul_f32 v27, v27, v40\n\t"
    "v_mul_f32 v28, v28, v40\n\t"
    "v_mul_f32 v29, v29, v40\n\t"
    "v_mul_f32 v30, v30, v40\n\t"
    "v_mul_f32 v31, v31, v40\n\t"
    "v_mul_f32 v52, v52, v40\n\t"
    "v_mul_f32 v53, v53, v40\n\t"
    "v_mul_f32 v54, v54, v40\n\t"
    "v_mul_f32 v55, v55, v40\n\t"
    "s_branch 11b\n\t"
    "12:\n\t"
    "v_max3_f32 v14, v24, v25, v26\n\t"
    "v_max3_f32 v40, v27, v28, v29\n\t"
    "v_max3_f32 v14, v14, v30, v31\n\t"
    "v_max3_f32 v14, v14, v40, %[neg]\n\t"
    "v_mov_b32 v40, v14\n\t"
    "s_nop 1\n\t"
    "v_permlane16_swap_b32 v14, v40\n\t"
    "v_max_f32 v14, v14, v40\n\t"
    "v_mov_b32 v40, v14\n\t"
    "s_nop 1\n\t"
    "v_permlane32_swap_b32 v14, v40\n\t"
    "v_max_f32 v14, v14, v40\n\t"
    "v_sub_f32 v24, v24, v14\n\t"
    "v_sub_f32 v25, v25, v14\n\t"
    "v_sub_f32 v26, v26, v14\n\t"
    "v_sub_f32 v27, v27, v14\n\t"
    "v_sub_f32 v28, v28, v14\n\t"
    "v_sub_f32 v29, v29, v14\n\t"
    "v_sub_f32 v30, v30, v14\n\t"
    "v_sub_f32 v31, v31, v14\n\t"
    "s_mov_b32 %[bg], 0x7fc00000\n\t"
    "s_branch 13b\n\t"
    "9:"
    : [tc] "=&s"(tc), [sp] "=&s"(sp), [cap] "+s"(cap), [st] "=&s"(st), [bg] "=&s"(bg)
    : [ag] "s"(ag), [c44] "s"(0x4444u), [lsc] "s"(LSC), [sn] "s"(sn), [pb] "s"(pb), [vs] "s"(vs), [od] "s"(od), [neg] "s"(-1.0e38f), [one] "s"(0x3F803F80u), [big] "s"(24.0f), "{v[64:95]}"(W.w0), "{v[96:127]}"(W.w1), "{v[128:159]}"(W.w2), "{v[160:191]}"(W.w3), "{v[192:223]}"(W.w4), "{v[224:255]}"(W.w5)
    : "v4", "v5", "v6", "v7", "v8", "v9", "v10", "v11", "v12", "v13", "v14", "v15", "v16", "v17", "v18", "v19", "v20", "v21", "v22", "v23", "v24", "v25", "v26", "v27", "v28", "v29", "v30", "v31", "v32", "v33", "v34", "v35", "v36", "v37", "v38", "v39", "v40", "v41", "v42", "v43", "v44", "v45", "v46", "v47", "v48", "v49", "v50", "v51", "v52", "v53", "v54", "v55", "v56", "v57", "v58", "v59", "v60", "v61", "v62", "v63", "vcc", "scc", "m0", "memory");
}
template <> __device__ __forceinline__ void qk_step<1>(const QKW& W, const char* ag, const char* vs, const char* v2, const char* db, const char* od, unsigned pb, unsigned sn, unsigned& cap, unsigned& sp,
    unsigned long long& tc, unsigned long long* ts, unsigned& d0, unsigned& d1) {
  unsigned st, bg;
  asm volatile("v_mbcnt_lo_u32_b32 v60, -1, 0\n\t"
    "v_mbcnt_hi_u32_b32 v60, -1, v60\n\t"
    "v_lshlrev_b32 v5, 4, v60\n\t"
    "s_mov_b32 %[sp], 0\n\t"
    "s_setprio 2\n\t"
    "global_load_dwordx4 v[24:27], v5, %[ag] sc1\n\t"
    "global_load_dwordx4 v[28:31], v5, %[ag] offset:1024 sc1\n\t"
    "s_memtime %[tc]\n\t"
    "v_lshrrev_b32 v32, 4, v60\n\t"
    "v_and_b32 v33, 15, v60\n\t"
    "v_lshrrev_b32 v34, 2, v33\n\t"
    "v_and_b32 v35, 3, v60\n\t"
    "v_and_b32 v61, 1, v60\n\t"
    "v_lshlrev_b32 v61, 3, v61\n\t"
    "v_lshl_or_b32 v7, v34, 4, v61\n\t"
    "v_lshl_or_b32 v7, v32, 7, v7\n\t"
    "v_and_b32 v10, 2, v34\n\t"
    "v_xor_b32 v10, v10, v32\n\t"
    "v_lshl_or_b32 v10, v10, 4, %[pb]\n\t"
    "v_lshl_or_b32 v10, v33, 6, v10\n\t"
    "v_and_b32 v14, 32, v60\n\t"
    "v_lshl_or_b32 v14, v35, 3, v14\n\t"
    "v_lshrrev_b32 v34, 2, v60\n\t"
    "v_lshl_or_b32 v14, v34, 6, v14\n\t"
    "v_or_b32 v14, %[pb], v14\n\t"
    "v_and_b32 v61, 3, v60\n\t"
    "v_lshlrev_b32 v61, 6, v61\n\t"
    "v_lshl_or_b32 v4, v32, 4, v61\n\t"
    "v_and_b32 v61, 4, v60\n\t"
    "v_lshl_or_b32 v4, v61, 10, v4\n\t"
    "v_and_b32 v61, 8, v60\n\t"
    "v_lshl_or_b32 v4, v61, 14, v4\n\t"
    "v_or_b32 v4, 256, v4\n\t"
    "v_and_b32 v61, 2, v60\n\t"
    "v_mul_u32_u24 v61, 0x5555, v61\n\t"
    "v_or_b32 v6, %[c44], v61\n\t"
    "v_lshlrev_b32 v61, 2, v32\n\t"
    "v_lshl_or_b32 v15, v33, 4, v61\n\t"
    "3:\n\t"
    "v_mov_b32 v16, 0\n\t"
    "v_mov_b32 v17, 0\n\t"
    "v_mov_b32 v18, 0\n\t"
    "v_mov_b32 v19, 0\n\t"
    "v_mov_b32 v20, 0\n\t"
    "v_mov_b32 v21, 0\n\t"
    "v_mov_b32 v22, 0\n\t"
    "v_mov_b32 v23, 0\n\t"
    "s_waitcnt vmcnt(0) lgkmcnt(0)\n\t"
    "33:\n\t"
    "ds_write_b128 v5, v[24:27] offset:2048\n\t"
    "ds_write_b128 v5, v[28:31] offset:3072\n\t"
    "v_smfmac_f32_16x16x64_bf16 v[16:19], v[24:27], v[96:103], v6\n\t"
    "v_max3_u32 v60, v24, v25, v26\n\t"
    "v_max3_u32 v61, v27, v28, v29\n\t"
    "v_smfmac_f32_16x16x64_bf16 v[20:23], v[24:27], v[104:111], v6\n\t"
    "v_max3_u32 v60, v60, v30, v31\n\t"
    "v_max_u32 v60, v60, v61\n\t"
    "v_cmp_eq_u32 vcc, -1, v60\n\t"
    "s_cbranch_vccnz 1f\n\t"
    "2:\n\t"
    "s_waitcnt lgkmcnt(0)\n\t"
    "s_barrier\n\t"
    "ds_read_b128 v[32:35], v5 offset:0\n\t"
    "ds_read_b128 v[36:39], v5 offset:1024\n\t"
    "v_smfmac_f32_16x16x64_bf16 v[16:19], v[28:31], v[112:119], v6\n\t"
    "ds_read_b128 v[40:43], v5 offset:4096\n\t"
    "ds_read_b128 v[44:47], v5 offset:5120\n\t"
    "v_smfmac_f32_16x16x64_bf16 v[20:23], v[28:31], v[120:127], v6\n\t"
    "s_waitcnt lgkmcnt(3)\n\t"
    "v_smfmac_f32_16x16x64_bf16 v[16:19], v[32:35], v[64:71], v6\n\t"
    "ds_read_b128 v[48:51], v5 offset:6144\n\t"
    "v_smfmac_f32_16x16x64_bf16 v[20:23], v[32:35], v[72:79], v6\n\t"
    "ds_read_b128 v[52:55], v5 offset:7168\n\t"
    "s_waitcnt lgkmcnt(4)\n\t"
    "v_smfmac_f32_16x16x64_bf16 v[16:19], v[36:39], v[80:87], v6\n\t"
    "ds_read_b128 v[24:27], v5 offset:8192\n\t"
    "v_smfmac_f32_16x16x64_bf16 v[20:23], v[36:39], v[88:95], v6\n\t"
    "ds_read_b128 v[28:31], v5 offset:9216\n\t"
    "s_waitcnt lgkmcnt(5)\n\t"
    "v_smfmac_f32_16x16x64_bf16 v[16:19], v[40:43], v[128:135], v6\n\t"
    "ds_read_b128 v[56:59], v5 offset:10240\n\t"
    "v_smfmac_f32_16x16x64_bf16 v[20:23], v[40:43], v[136:143], v6\n\t"
    "ds_read_b128 v[60:63], v5 offset:11264\n\t"
    "ds_read_b128 v[36:39], v10 offset:35840\n\t"
    "s_waitcnt lgkmcnt(7)\n\t"
    "v_smfmac_f32_16x16x64_bf16 v[16:19], v[44:47], v[144:151], v6\n\t"
    "ds_read_b128 v[40:43], v10 offset:36864\n\t"
    "v_smfmac_f32_16x16x64_bf16 v[20:23], v[44:47], v[152:159], v6\n\t"
    "ds_read_b128 v[44:47], v10 offset:37888\n\t"
    "s_waitcnt lgkmcnt(8)\n\t"
    "v_smfmac_f32_16x16x64_bf16 v[16:19], v[48:51], v[160:167], v6\n\t"
    "v_smfmac_f32_16x16x64_bf16 v[20:23], v[48:51], v[168:175], v6\n\t"
    "ds_read_b128 v[48:51], v10 offset:38912\n\t"
    "s_waitcnt lgkmcnt(8)\n\t"
    "v_smfmac_f32_16x16x64_bf16 v[16:19], v[52:55], v[176:183], v6\n\t"
    "v_smfmac_f32_16x16x64_bf16 v[20:23], v[52:55], v[184:191], v6\n\t"
    "ds_read_b128 v[52:55], v10 offset:39936\n\t"
    "s_waitcnt lgkmcnt(8)\n\t"
    "v_smfmac_f32_16x16x64_bf16 v[16:19], v[24:27], v[192:199], v6\n\t"
    "v_smfmac_f32_16x16x64_bf16 v[20:23], v[24:27], v[200:207], v6\n\t"
    "ds_read_b128 v[24:27], v10 offset:32768\n\t"
    "s_waitcnt lgkmcnt(8)\n\t"
    "v_smfmac_f32_16x16x64_bf16 v[16:19], v[28:31], v[208:215], v6\n\t"
    "ds_read_b32 v8, v15 offset:28928\n\t"
    "ds_read_b128 v[32:35], v5 offset:25600\n\t"
    "s_setprio 3\n\t"
    "s_waitcnt lgkmcnt(9)\n\t"
    "v_smfmac_f32_16x16x64_bf16 v[16:19], v[56:59], v[224:231], v6\n\t"
    "s_waitcnt lgkmcnt(8)\n\t"
    "v_smfmac_f32_16x16x64_bf16 v[16:19], v[60:63], v[240:247], v6\n\t"
    "v_smfmac_f32_16x16x64_bf16 v[20:23], v[28:31], v[216:223], v6\n\t"
    "s_waitcnt lgkmcnt(0)\n\t"
    "21:\n\t"
    "v_cmp_ne_u32 vcc, %[sn], v8\n\t"
    "s_cbranch_vccnz 22f\n\t"
    "v_mul_f32 v32, %[lsc], v32\n\t"
    "v_mul_f32 v33, %[lsc], v33\n\t"
    "v_smfmac_f32_16x16x64_bf16 v[20:23], v[56:59], v[232:239], v6\n\t"
    "v_mul_f32 v34, %[lsc], v34\n\t"
    "v_mul_f32 v35, %[lsc], v35\n\t"
    "v_add_f32 v16, v16, v18\n\t"
    "v_add_f32 v17, v17, v19\n\t"
    "v_smfmac_f32_16x16x64_bf16 v[20:23], v[60:63], v[248:255], v6\n\t"
    "v_fma_f32 v16, v16, %[lsc], v32\n\t"
    "v_fma_f32 v17, v17, %[lsc], v33\n\t"
    "v_cvt_pk_bf16_f32 v16, v16, v17\n\t"
    "ds_write_b32 v15, v16 offset:16896\n\t"
    "s_mov_b32 %[bg], %[big]\n\t"
    "s_nop 2\n\t"
    "v_add_f32 v20, v20, v22\n\t"
    "v_add_f32 v21, v21, v23\n\t"
    "v_fma_f32 v20, v20, %[lsc], v34\n\t"
    "v_fma_f32 v21, v21, %[lsc], v35\n\t"
    "v_cvt_pk_bf16_f32 v20, v20, v21\n\t"
    "ds_write_b32 v15, v20 offset:17152\n\t"
    "ds_read_b128 v[28:31], v10 offset:33792\n\t"
    "ds_read_b128 v[32:35], v10 offset:34816\n\t"
    "s_waitcnt lgkmcnt(2)\n\t"
    "s_barrier\n\t"
    "ds_read_b64_tr_b16 v[16:17], v7 offset:16384\n\t"
    "ds_read_b64_tr_b16 v[18:19], v7 offset:16448\n\t"
    "ds_read_b64_tr_b16 v[20:21], v7 offset:16896\n\t"
    "ds_read_b64_tr_b16 v[22:23], v7 offset:16960\n\t"
    "ds_read_b64_tr_b16 v[56:57], v7 offset:17408\n\t"
    "ds_read_b64_tr_b16 v[58:59], v7 offset:17472\n\t"
    "ds_read_b64_tr_b16 v[60:61], v7 offset:17920\n\t"
    "ds_read_b64_tr_b16 v[62:63], v7 offset:17984\n\t"
    "global_load_dwordx4 v[6:9], v5, %[vs] sc1\n\t"
    "global_load_dwordx4 v[10:13], v5, %[vs] offset:1024 sc1\n\t"
    "s_waitcnt lgkmcnt(6)\n\t"
    "v_mfma_f32_16x16x32_bf16 v[24:27], v[24:27], v[16:19], 0\n\t"
    "v_mfma_f32_16x16x32_bf16 v[28:31], v[28:31], v[16:19], 0\n\t"
    "s_waitcnt lgkmcnt(4)\n\t"
    "v_mfma_f32_16x16x32_bf16 v[24:27], v[32:35], v[20:23], v[24:27]\n\t"
    "v_mfma_f32_16x16x32_bf16 v[28:31], v[36:39], v[20:23], v[28:31]\n\t"
    "v_xor_b32 v39, 32, v14\n\t"
    "ds_read_b64_tr_b16 v[32:33], v14 offset:43008\n\t"
    "ds_read_b64_tr_b16 v[34:35], v14 offset:44032\n\t"
    "ds_read_b64_tr_b16 v[36:37], v39 offset:43008\n\t"
    "ds_read_b64_tr_b16 v[38:39], v39 offset:44032\n\t"
    "s_waitcnt lgkmcnt(6)\n\t"
    "v_mfma_f32_16x16x32_bf16 v[24:27], v[40:43], v[56:59], v[24:27]\n\t"
    "v_mfma_f32_16x16x32_bf16 v[28:31], v[44:47], v[56:59], v[28:31]\n\t"
    "s_waitcnt lgkmcnt(4)\n\t"
    "v_mfma_f32_16x16x32_bf16 v[24:27], v[48:51], v[60:63], v[24:27]\n\t"
    "v_mfma_f32_16x16x32_bf16 v[28:31], v[52:55], v[60:63], v[28:31]\n\t"
    "v_mov_b32 v52, %[one]\n\t"
    "v_mov_b32 v53, %[one]\n\t"
    "v_mov_b32 v54, %[one]\n\t"
    "v_mov_b32 v55, %[one]\n\t"
    "v_mov_b32 v45, 0\n\t"
    "v_mov_b32 v47, 0\n\t"
    "v_mov_b32 v48, %[one]\n\t"
    "v_max3_f32 v14, |v24|, |v25|, |v26|\n\t"
    "v_max3_f32 v40, |v27|, |v28|, |v29|\n\t"
    "v_max3_f32 v14, v14, |v30|, |v31|\n\t"
    "v_max_f32 v14, v14, v40\n\t"
    "v_cmp_nge_f32 vcc, %[big], v14\n\t"
    "v_mov_b32 v14, 0\n\t"
    "s_cbranch_vccnz 12f\n\t"
    "13:\n\t"
    "v_exp_f32 v24, v24\n\t"
    "v_exp_f32 v25, v25\n\t"
    "v_exp_f32 v26, v26\n\t"
    "v_exp_f32 v27, v27\n\t"
    "v_exp_f32 v28, v28\n\t"
    "v_exp_f32 v29, v29\n\t"
    "v_exp_f32 v30, v30\n\t"
    "v_exp_f32 v31, v31\n\t"
    "v_cvt_pk_bf16_f32 v40, v24, v25\n\t"
    "v_cvt_pk_bf16_f32 v41, v26, v27\n\t"
    "v_cvt_pk_bf16_f32 v42, v28, v29\n\t"
    "v_cvt_pk_bf16_f32 v43, v30, v31\n\t"
    "v_mov_b32 v49, 0\n\t"
    "v_mov_b32 v51, 0\n\t"
    "s_waitcnt lgkmcnt(0)\n\t"
    "v_mfma_f32_16x16x32_bf16 v[52:55], v[52:55], v[40:43], 0\n\t"
    "v_mfma_f32_16x16x32_bf16 v[24:27], v[32:35], v[40:43], 0\n\t"
    "v_mfma_f32_16x16x32_bf16 v[28:31], v[36:39], v[40:43], 0\n\t"
    "s_waitcnt vmcnt(1)\n\t"
    "5:\n\t"
    "v_mfma_f32_16x16x32_bf16 v[32:35], v[6:9], v[16:19], 0\n\t"
    "v_mfma_f32_16x16x32_bf16 v[36:39], v[6:9], v[20:23], 0\n\t"
    "s_waitcnt vmcnt(0)\n\t"
    "v_mfma_f32_16x16x32_bf16 v[32:35], v[10:13], v[56:59], v[32:35]\n\t"
    "v_mfma_f32_16x16x32_bf16 v[36:39], v[10:13], v[60:63], v[36:39]\n\t"
    "ds_read_b32 v44, v15 offset:18944\n\t"
    "ds_read_b32 v46, v15 offset:19200\n\t"
    "v_max3_u32 v40, v6, v7, v8\n\t"
    "v_max3_u32 v41, v9, v10, v11\n\t"
    "v_max3_u32 v40, v40, v12, v13\n\t"
    "v_max_u32 v40, v40, v41\n\t"
    "v_cmp_eq_u32 vcc, -1, v40\n\t"
    "s_cbranch_vccnz 4f\n\t"
    "6:\n\t"
    "s_nop 0\n\t"
    "v_add_f32 v32, v32, v38\n\t"
    "v_add_f32 v33, v33, v39\n\t"
    "v_exp_f32 v34, v32\n\t"
    "v_max_f32 v40, v32, v33\n\t"
    "v_exp_f32 v35, v33\n\t"
    "v_cmp_nge_f32 vcc, %[bg], v40\n\t"
    "s_cbranch_vccnz 10f\n\t"
    "11:\n\t"
    "v_cvt_pk_bf16_f32 v50, v34, v35\n\t"
    "17:\n\t"
    "s_waitcnt vmcnt(0) lgkmcnt(0)\n\t"
    "v_max_u32 v40, v44, v46\n\t"
    "v_cmp_eq_u32 vcc, -1, v40\n\t"
    "s_cbranch_vccnz 18f\n\t"
    "19:\n\t"
    "v_mfma_f32_16x16x16_bf16 v[52:55], v[48:49], v[50:51], v[52:55]\n\t"
    "v_mfma_f32_16x16x16_bf16 v[24:27], v[44:45], v[50:51], v[24:27]\n\t"
    "v_mfma_f32_16x16x16_bf16 v[28:31], v[46:47], v[50:51], v[28:31]\n\t"
    "s_nop 5\n\t"
    "v_rcp_f32 v42, v52\n\t"
    "s_nop 0\n\t"
    "v_pk_mul_f32 v[24:25], v[24:25], v[42:43] op_sel_hi:[1,0]\n\t"
    "v_pk_mul_f32 v[26:27], v[26:27], v[42:43] op_sel_hi:[1,0]\n\t"
    "v_pk_mul_f32 v[28:29], v[28:29], v[42:43] op_sel_hi:[1,0]\n\t"
    "v_pk_mul_f32 v[30:31], v[30:31], v[42:43] op_sel_hi:[1,0]\n\t"
    "v_cvt_pk_bf16_f32 v36, v24, v25\n\t"
    "v_cvt_pk_bf16_f32 v37, v26, v27\n\t"
    "v_cvt_pk_bf16_f32 v38, v28, v29\n\t"
    "v_cvt_pk_bf16_f32 v39, v30, v31\n\t"
    "v_min_u32 v36, -2, v36\n\t"
    "v_min_u32 v37, -2, v37\n\t"
    "v_min_u32 v38, -2, v38\n\t"
    "v_min_u32 v39, -2, v39\n\t"
    "global_store_dwordx4 v4, v[36:39], %[od] sc1\n\t"
    "ds_write_b128 v5, v[10:13] offset:21504\n\t"
    "s_setprio 1\n\t"
    "s_waitcnt lgkmcnt(0)\n\t"
    "s_barrier\n\t"
    "s_waitcnt lgkmcnt(0)\n\t"
    "s_branch 9f\n\t"
    "1:\n\t"
    "s_add_u32 %[sp], %[sp], 1\n\t"
    "s_cmp_ge_u32 %[sp], %[cap]\n\t"
    "s_cbranch_scc1 7f\n\t"
    "global_load_dwordx4 v[24:27], v5, %[ag] sc1\n\t"
    "global_load_dwordx4 v[28:31], v5, %[ag] offset:1024 sc1\n\t"
    "s_waitcnt lgkmcnt(0)\n\t"
    "s_memtime %[tc]\n\t"
    "s_nop 7\n\t"
    "s_branch 3b\n\t"
    "7:\n\t"
    "s_mov_b32 %[cap], 0\n\t"
    "s_branch 2b\n\t"
    "4:\n\t"
    "s_add_u32 %[sp], %[sp], 0x10000\n\t"
    "s_lshr_b32 %[st], %[sp], 16\n\t"
    "s_cmp_ge_u32 %[st], %[cap]\n\t"
    "s_cbranch_scc1 8f\n\t"
    "global_load_dwordx4 v[6:9], v5, %[vs] sc1\n\t"
    "global_load_dwordx4 v[10:13], v5, %[vs] offset:1024 sc1\n\t"
    "s_waitcnt vmcnt(0) lgkmcnt(0)\n\t"
    "s_branch 5b\n\t"
    "8:\n\t"
    "s_mov_b32 %[cap], 0\n\t"
    "s_branch 6b\n\t"
    "18:\n\t"
    "s_add_u32 %[sp], %[sp], 0x10000\n\t"
    "s_lshr_b32 %[st], %[sp], 16\n\t"
    "s_cmp_ge_u32 %[st], %[cap]\n\t"
    "s_cbranch_scc1 20f\n\t"
    "ds_read_b32 v44, v15 offset:18944\n\t"
    "ds_read_b32 v46, v15 offset:19200\n\t"
    "s_waitcnt vmcnt(0) lgkmcnt(0)\n\t"
    "s_branch 17b\n\t"
    "20:\n\t"
    "s_mov_b32 %[cap], 0\n\t"
    "s_branch 19b\n\t"
    "22:\n\t"
    "ds_read_b32 v8, v15 offset:28928\n\t"
    "ds_read_b128 v[32:35], v5 offset:25600\n\t"
    "s_waitcnt lgkmcnt(0)\n\t"
    "s_branch 21b\n\t"
    "10:\n\t"
    "v_max_f32 v40, v40, v14\n\t"
    "v_mov_b32 v42, v40\n\t"
    "s_nop 1\n\t"
    "v_permlane16_swap_b32 v40, v42\n\t"
    "v_max_f32 v40, v40, v42\n\t"
    "v_mov_b32 v42, v40\n\t"
    "s_nop 1\n\t"
    "v_permlane32_swap_b32 v40, v42\n\t"
    "v_max_f32 v40, v40, v42\n\t"
    "v_sub_f32 v34, v32, v40\n\t"
    "v_sub_f32 v35, v33, v40\n\t"
    "v_sub_f32 v40, v14, v40\n\t"
    "v_exp_f32 v40, v40\n\t"
    "v_exp_f32 v34, v34\n\t"
    "v_exp_f32 v35, v35\n\t"
    "v_mul_f32 v24, v24, v40\n\t"
    "v_mul_f32 v25, v25, v40\n\t"
    "v_mul_f32 v26, v26, v40\n\t"
    "v_mul_f32 v27, v27, v40\n\t"
    "v_mul_f32 v28, v28, v40\n\t"
    "v_mul_f32 v29, v29, v40\n\t"
    "v_mul_f32 v30, v30, v40\n\t"
    "v_mul_f32 v31, v31, v40\n\t"
    "v_mul_f32 v52, v52, v40\n\t"
    "v_mul_f32 v53, v53, v40\n\t"
    "v_mul_f32 v54, v54, v40\n\t"
    "v_mul_f32 v55, v55, v40\n\t"
    "s_branch 11b\n\t"
    "12:\n\t"
    "v_max3_f32 v14, v24, v25, v26\n\t"
    "v_max3_f32 v40, v27, v28, v29\n\t"
    "v_max3_f32 v14, v14, v30, v31\n\t"
    "v_max3_f32 v14, v14, v40, %[neg]\n\t"
    "v_mov_b32 v40, v14\n\t"
    "s_nop 1\n\t"
    "v_permlane16_swap_b32 v14, v40\n\t"
    "v_max_f32 v14, v14, v40\n\t"
    "v_mov_b32 v40, v14\n\t"
    "s_nop 1\n\t"
    "v_permlane32_swap_b32 v14, v40\n\t"
    "v_max_f32 v14, v14, v40\n\t"
    "v_sub_f32 v24, v24, v14\n\t"
    "v_sub_f32 v25, v25, v14\n\t"
    "v_sub_f32 v26, v26, v14\n\t"
    "v_sub_f32 v27, v27, v14\n\t"
    "v_sub_f32 v28, v28, v14\n\t"
    "v_sub_f32 v29, v29, v14\n\t"
    "v_sub_f32 v30, v30, v14\n\t"
    "v_sub_f32 v31, v31, v14\n\t"
    "s_mov_b32 %[bg], 0x7fc00000\n\t"
    "s_branch 13b\n\t"
    "9:"
    : [tc] "=&s"(tc), [sp] "=&s"(sp), [cap] "+s"(cap), [st] "=&s"(st), [bg] "=&s"(bg)
    : [ag] "s"(ag), [c44] "s"(0x4444u), [lsc] "s"(LSC), [sn] "s"(sn), [pb] "s"(pb), [vs] "s"(vs), [od] "s"(od), [neg] "s"(-1.0e38f), [one] "s"(0x3F803F80u), [big] "s"(24.0f), "{v[64:95]}"(W.w0), "{v[96:127]}"(W.w1), "{v[128:159]}"(W.w2), "{v[160:191]}"(W.w3), "{v[192:223]}"(W.w4), "{v[224:255]}"(W.w5)
    : "v4", "v5", "v6", "v7", "v8", "v9", "v10", "v11", "v12", "v13", "v14", "v15", "v16", "v17", "v18", "v19", "v20", "v21", "v22", "v23", "v24", "v25", "v26", "v27", "v28", "v29", "v30", "v31", "v32", "v33", "v34", "v35", "v36", "v37", "v38", "v39", "v40", "v41", "v42", "v43", "v44", "v45", "v46", "v47", "v48", "v49", "v50", "v51", "v52", "v53", "v54", "v55", "v56", "v57", "v58", "v59", "v60", "v61", "v62", "v63", "vcc", "scc", "m0", "memory");
}
template <> __device__ __forceinline__ void qk_step<2>(const QKW& W, const char* ag, const char* vs, const char* v2, const char* db, const char* od, unsigned pb, unsigned sn, unsigned& cap, unsigned& sp,
    unsigned long long& tc, unsigned long long* ts, unsigned& d0, unsigned& d1) {
  unsigned st, bg;
  asm volatile("v_mbcnt_lo_u32_b32 v60, -1, 0\n\t"
    "v_mbcnt_hi_u32_b32 v60, -1, v60\n\t"
    "v_lshlrev_b32 v5, 4, v60\n\t"
    "s_mov_b32 %[sp], 0\n\t"
    "s_setprio 2\n\t"
    "global_load_dwordx4 v[24:27], v5, %[ag] sc1\n\t"
    "global_load_dwordx4 v[28:31], v5, %[ag] offset:1024 sc1\n\t"
    "s_memtime %[tc]\n\t"
    "v_lshrrev_b32 v32, 4, v60\n\t"
    "v_and_b32 v33, 15, v60\n\t"
    "v_lshrrev_b32 v34, 2, v33\n\t"
    "v_and_b32 v35, 3, v60\n\t"
    "v_and_b32 v61, 1, v60\n\t"
    "v_lshlrev_b32 v61, 3, v61\n\t"
    "v_lshl_or_b32 v7, v34, 4, v61\n\t"
    "v_lshl_or_b32 v7, v32, 7, v7\n\t"
    "v_and_b32 v10, 2, v34\n\t"
    "v_xor_b32 v10, v10, v32\n\t"
    "v_lshl_or_b32 v10, v10, 4, %[pb]\n\t"
    "v_lshl_or_b32 v10, v33, 6, v10\n\t"
    "v_and_b32 v14, 32, v60\n\t"
    "v_lshl_or_b32 v14, v35, 3, v14\n\t"
    "v_lshrrev_b32 v34, 2, v60\n\t"
    "v_lshl_or_b32 v14, v34, 6, v14\n\t"
    "v_or_b32 v14, %[pb], v14\n\t"
    "v_and_b32 v61, 3, v60\n\t"
    "v_lshlrev_b32 v61, 6, v61\n\t"
    "v_lshl_or_b32 v4, v32, 4, v61\n\t"
    "v_and_b32 v61, 4, v60\n\t"
    "v_lshl_or_b32 v4, v61, 10, v4\n\t"
    "v_and_b32 v61, 8, v60\n\t"
    "v_lshl_or_b32 v4, v61, 14, v4\n\t"
    "v_or_b32 v4, 512, v4\n\t"
    "v_and_b32 v61, 2, v60\n\t"
    "v_mul_u32_u24 v61, 0x5555, v61\n\t"
    "v_or_b32 v6, %[c44], v61\n\t"
    "v_lshlrev_b32 v61, 2, v32\n\t"
    "v_lshl_or_b32 v15, v33, 4, v61\n\t"
    "3:\n\t"
    "v_mov_b32 v16, 0\n\t"
    "v_mov_b32 v17, 0\n\t"
    "v_mov_b32 v18, 0\n\t"
    "v_mov_b32 v19, 0\n\t"
    "v_mov_b32 v20, 0\n\t"
    "v_mov_b32 v21, 0\n\t"
    "v_mov_b32 v22, 0\n\t"
    "v_mov_b32 v23, 0\n\t"
    "s_waitcnt vmcnt(0) lgkmcnt(0)\n\t"
    "33:\n\t"
    "ds_write_b128 v5, v[24:27] offset:4096\n\t"
    "ds_write_b128 v5, v[28:31] offset:5120\n\t"
    "v_smfmac_f32_16x16x64_bf16 v[16:19], v[24:27], v[128:135], v6\n\t"
    "v_max3_u32 v60, v24, v25, v26\n\t"
    "v_max3_u32 v61, v27, v28, v29\n\t"
    "v_smfmac_f32_16x16x64_bf16 v[20:23], v[24:27], v[136:143], v6\n\t"
    "v_max3_u32 v60, v60, v30, v31\n\t"
    "v_max_u32 v60, v60, v61\n\t"
    "v_cmp_eq_u32 vcc, -1, v60\n\t"
    "s_cbranch_vccnz 1f\n\t"
    "2:\n\t"
    "s_waitcnt lgkmcnt(0)\n\t"
    "s_barrier\n\t"
    "ds_read_b128 v[32:35], v5 offset:0\n\t"
    "ds_read_b128 v[36:39], v5 offset:1024\n\t"
    "v_smfmac_f32_16x16x64_bf16 v[16:19], v[28:31], v[144:151], v6\n\t"
    "ds_read_b128 v[40:43], v5 offset:2048\n\t"
    "ds_read_b128 v[44:47], v5 offset:3072\n\t"
    "v_smfmac_f32_16x16x64_bf16 v[20:23], v[28:31], v[152:159], v6\n\t"
    "s_waitcnt lgkmcnt(3)\n\t"
    "v_smfmac_f32_16x16x64_bf16 v[16:19], v[32:35], v[64:71], v6\n\t"
    "ds_read_b128 v[48:51], v5 offset:6144\n\t"
    "v_smfmac_f32_16x16x64_bf16 v[20:23], v[32:35], v[72:79], v6\n\t"
    "ds_read_b128 v[52:55], v5 offset:7168\n\t"
    "s_waitcnt lgkmcnt(4)\n\t"
    "v_smfmac_f32_16x16x64_bf16 v[16:19], v[36:39], v[80:87], v6\n\t"
    "ds_read_b128 v[24:27], v5 offset:8192\n\t"
    "v_smfmac_f32_16x16x64_bf16 v[20:23], v[36:39], v[88:95], v6\n\t"
    "ds_read_b128 v[28:31], v5 offset:9216\n\t"
    "s_waitcnt lgkmcnt(5)\n\t"
    "v_smfmac_f32_16x16x64_bf16 v[16:19], v[40:43], v[96:103], v6\n\t"
    "ds_read_b128 v[56:59], v5 offset:10240\n\t"
    "v_smfmac_f32_16x16x64_bf16 v[20:23], v[40:43], v[104:111], v6\n\t"
    "ds_read_b128 v[60:63], v5 offset:11264\n\t"
    "ds_read_b128 v[36:39], v10 offset:35840\n\t"
    "s_waitcnt lgkmcnt(7)\n\t"
    "v_smfmac_f32_16x16x64_bf16 v[16:19], v[44:47], v[112:119], v6\n\t"
    "ds_read_b128 v[40:43], v10 offset:36864\n\t"
    "v_smfmac_f32_16x16x64_bf16 v[20:23], v[44:47], v[120:127], v6\n\t"
    "ds_read_b128 v[44:47], v10 offset:37888\n\t"
    "s_waitcnt lgkmcnt(8)\n\t"
    "v_smfmac_f32_16x16x64_bf16 v[16:19], v[48:51], v[160:167], v6\n\t"
    "v_smfmac_f32_16x16x64_bf16 v[20:23], v[48:51], v[168:175], v6\n\t"
    "ds_read_b128 v[48:51], v10 offset:38912\n\t"
    "s_waitcnt lgkmcnt(8)\n\t"
    "v_smfmac_f32_16x16x64_bf16 v[16:19], v[52:55], v[176:183], v6\n\t"
    "v_smfmac_f32_16x16x64_bf16 v[20:23], v[52:55], v[184:191], v6\n\t"
    "ds_read_b128 v[52:55], v10 offset:39936\n\t"
    "s_waitcnt lgkmcnt(8)\n\t"
    "v_smfmac_f32_16x16x64_bf16 v[16:19], v[24:27], v[192:199], v6\n\t"
    "v_smfmac_f32_16x16x64_bf16 v[20:23], v[24:27], v[200:207], v6\n\t"
    "ds_read_b128 v[24:27], v10 offset:32768\n\t"
    "s_waitcnt lgkmcnt(8)\n\t"
    "v_smfmac_f32_16x16x64_bf16 v[16:19], v[28:31], v[208:215], v6\n\t"
    "ds_read_b32 v8, v15 offset:29184\n\t"
    "ds_read_b128 v[32:35], v5 offset:26624\n\t"
    "s_setprio 3\n\t"
    "s_waitcnt lgkmcnt(9)\n\t"
    "v_smfmac_f32_16x16x64_bf16 v[16:19], v[56:59], v[224:231], v6\n\t"
    "s_waitcnt lgkmcnt(8)\n\t"
    "v_smfmac_f32_16x16x64_bf16 v[16:19], v[60:63], v[240:247], v6\n\t"
    "v_smfmac_f32_16x16x64_bf16 v[20:23], v[28:31], v[216:223], v6\n\t"
    "s_waitcnt lgkmcnt(0)\n\t"
    "21:\n\t"
    "v_cmp_ne_u32 vcc, %[sn], v8\n\t"
    "s_cbranch_vccnz 22f\n\t"
    "v_mul_f32 v32, %[lsc], v32\n\t"
    "v_mul_f32 v33, %[lsc], v33\n\t"
    "v_smfmac_f32_16x16x64_bf16 v[20:23], v[56:59], v[232:239], v6\n\t"
    "v_mul_f32 v34, %[lsc], v34\n\t"
    "v_mul_f32 v35, %[lsc], v35\n\t"
    "v_add_f32 v16, v16, v18\n\t"
    "v_add_f32 v17, v17, v19\n\t"
    "v_smfmac_f32_16x16x64_bf16 v[20:23], v[60:63], v[248:255], v6\n\t"
    "v_fma_f32 v16, v16, %[lsc], v32\n\t"
    "v_fma_f32 v17, v17, %[lsc], v33\n\t"
    "v_cvt_pk_bf16_f32 v16, v16, v17\n\t"
    "ds_write_b32 v15, v16 offset:17408\n\t"
    "s_mov_b32 %[bg], %[big]\n\t"
    "s_nop 2\n\t"
    "v_add_f32 v20, v20, v22\n\t"
    "v_add_f32 v21, v21, v23\n\t"
    "v_fma_f32 v20, v20, %[lsc], v34\n\t"
    "v_fma_f32 v21, v21, %[lsc], v35\n\t"
    "v_cvt_pk_bf16_f32 v20, v20, v21\n\t"
    "ds_write_b32 v15, v20 offset:17664\n\t"
    "ds_read_b128 v[28:31], v10 offset:33792\n\t"
    "ds_read_b128 v[32:35], v10 offset:34816\n\t"
    "s_waitcnt lgkmcnt(2)\n\t"
    "s_barrier\n\t"
    "ds_read_b64_tr_b16 v[16:17], v7 offset:16384\n\t"
    "ds_read_b64_tr_b16 v[18:19], v7 offset:16448\n\t"
    "ds_read_b64_tr_b16 v[20:21], v7 offset:16896\n\t"
    "ds_read_b64_tr_b16 v[22:23], v7 offset:16960\n\t"
    "ds_read_b64_tr_b16 v[56:57], v7 offset:17408\n\t"
    "ds_read_b64_tr_b16 v[58:59], v7 offset:17472\n\t"
    "ds_read_b64_tr_b16 v[60:61], v7 offset:17920\n\t"
    "ds_read_b64_tr_b16 v[62:63], v7 offset:17984\n\t"
    "global_load_dwordx4 v[6:9], v5, %[vs] sc1\n\t"
    "global_load_dwordx4 v[10:13], v5, %[vs] offset:1024 sc1\n\t"
    "s_waitcnt lgkmcnt(6)\n\t"
    "v_mfma_f32_16x16x32_bf16 v[24:27], v[24:27], v[16:19], 0\n\t"
    "v_mfma_f32_16x16x32_bf16 v[28:31], v[28:31], v[16:19], 0\n\t"
    "s_waitcnt lgkmcnt(4)\n\t"
    "v_mfma_f32_16x16x32_bf16 v[24:27], v[32:35], v[20:23], v[24:27]\n\t"
    "v_mfma_f32_16x16x32_bf16 v[28:31], v[36:39], v[20:23], v[28:31]\n\t"
    "v_xor_b32 v39, 32, v14\n\t"
    "ds_read_b64_tr_b16 v[32:33], v14 offset:45056\n\t"
    "ds_read_b64_tr_b16 v[34:35], v14 offset:46080\n\t"
    "ds_read_b64_tr_b16 v[36:37], v39 offset:45056\n\t"
    "ds_read_b64_tr_b16 v[38:39], v39 offset:46080\n\t"
    "s_waitcnt lgkmcnt(6)\n\t"
    "v_mfma_f32_16x16x32_bf16 v[24:27], v[40:43], v[56:59], v[24:27]\n\t"
    "v_mfma_f32_16x16x32_bf16 v[28:31], v[44:47], v[56:59], v[28:31]\n\t"
    "s_waitcnt lgkmcnt(4)\n\t"
    "v_mfma_f32_16x16x32_bf16 v[24:27], v[48:51], v[60:63], v[24:27]\n\t"
    "v_mfma_f32_16x16x32_bf16 v[28:31], v[52:55], v[60:63], v[28:31]\n\t"
    "v_mov_b32 v52, %[one]\n\t"
    "v_mov_b32 v53, %[one]\n\t"
    "v_mov_b32 v54, %[one]\n\t"
    "v_mov_b32 v55, %[one]\n\t"
    "v_mov_b32 v45, 0\n\t"
    "v_mov_b32 v47, 0\n\t"
    "v_mov_b32 v48, %[one]\n\t"
    "v_max3_f32 v14, |v24|, |v25|, |v26|\n\t"
    "v_max3_f32 v40, |v27|, |v28|, |v29|\n\t"
    "v_max3_f32 v14, v14, |v30|, |v31|\n\t"
    "v_max_f32 v14, v14, v40\n\t"
    "v_cmp_nge_f32 vcc, %[big], v14\n\t"
    "v_mov_b32 v14, 0\n\t"
    "s_cbranch_vccnz 12f\n\t"
    "13:\n\t"
    "v_exp_f32 v24, v24\n\t"
    "v_exp_f32 v25, v25\n\t"
    "v_exp_f32 v26, v26\n\t"
    "v_exp_f32 v27, v27\n\t"
    "v_exp_f32 v28, v28\n\t"
    "v_exp_f32 v29, v29\n\t"
    "v_exp_f32 v30, v30\n\t"
    "v_exp_f32 v31, v31\n\t"
    "v_lshrrev_b32 v46, 2, v5\n\t"
    "global_load_dword v44, v46, %[v2] sc1\n\t"
    "global_load_dword v46, v46, %[v2] offset:256 sc1\n\t"
    "v_cvt_pk_bf16_f32 v40, v24, v25\n\t"
    "v_cvt_pk_bf16_f32 v41, v26, v27\n\t"
    "v_cvt_pk_bf16_f32 v42, v28, v29\n\t"
    "v_cvt_pk_bf16_f32 v43, v30, v31\n\t"
    "v_mov_b32 v49, 0\n\t"
    "v_mov_b32 v51, 0\n\t"
    "s_waitcnt lgkmcnt(0)\n\t"
    "v_mfma_f32_16x16x32_bf16 v[52:55], v[52:55], v[40:43], 0\n\t"
    "v_mfma_f32_16x16x32_bf16 v[24:27], v[32:35], v[40:43], 0\n\t"
    "v_mfma_f32_16x16x32_bf16 v[28:31], v[36:39], v[40:43], 0\n\t"
    "s_waitcnt vmcnt(3)\n\t"
    "5:\n\t"
    "v_mfma_f32_16x16x32_bf16 v[32:35], v[6:9], v[16:19], 0\n\t"
    "v_mfma_f32_16x16x32_bf16 v[36:39], v[6:9], v[20:23], 0\n\t"
    "s_waitcnt vmcnt(2)\n\t"
    "v_mfma_f32_16x16x32_bf16 v[32:35], v[10:13], v[56:59], v[32:35]\n\t"
    "v_mfma_f32_16x16x32_bf16 v[36:39], v[10:13], v[60:63], v[36:39]\n\t"
    "v_max3_u32 v40, v6, v7, v8\n\t"
    "v_max3_u32 v41, v9, v10, v11\n\t"
    "v_max3_u32 v40, v40, v12, v13\n\t"
    "v_max_u32 v40, v40, v41\n\t"
    "v_cmp_eq_u32 vcc, -1, v40\n\t"
    "s_cbranch_vccnz 4f\n\t"
    "6:\n\t"
    "s_nop 0\n\t"
    "s_nop 0\n\t"
    "v_add_f32 v32, v32, v38\n\t"
    "v_add_f32 v33, v33, v39\n\t"
    "v_exp_f32 v34, v32\n\t"
    "v_max_f32 v40, v32, v33\n\t"
    "v_exp_f32 v35, v33\n\t"
    "v_cmp_nge_f32 vcc, %[bg], v40\n\t"
    "s_cbranch_vccnz 10f\n\t"
    "11:\n\t"
    "v_cvt_pk_bf16_f32 v50, v34, v35\n\t"
    "17:\n\t"
    "s_waitcnt vmcnt(0) lgkmcnt(0)\n\t"
    "v_max_u32 v40, v44, v46\n\t"
    "v_cmp_eq_u32 vcc, -1, v40\n\t"
    "s_cbranch_vccnz 18f\n\t"
    "19:\n\t"
    "s_memrealtime s[36:37]\n\t"
    "v_mfma_f32_16x16x16_bf16 v[52:55], v[48:49], v[50:51], v[52:55]\n\t"
    "v_mfma_f32_16x16x16_bf16 v[24:27], v[44:45], v[50:51], v[24:27]\n\t"
    "v_mfma_f32_16x16x16_bf16 v[28:31], v[46:47], v[50:51], v[28:31]\n\t"
    "s_nop 5\n\t"
    "v_rcp_f32 v42, v52\n\t"
    "s_nop 0\n\t"
    "v_pk_mul_f32 v[24:25], v[24:25], v[42:43] op_sel_hi:[1,0]\n\t"
    "v_pk_mul_f32 v[26:27], v[26:27], v[42:43] op_sel_hi:[1,0]\n\t"
    "v_pk_mul_f32 v[28:29], v[28:29], v[42:43] op_sel_hi:[1,0]\n\t"
    "v_pk_mul_f32 v[30:31], v[30:31], v[42:43] op_sel_hi:[1,0]\n\t"
    "v_cvt_pk_bf16_f32 v36, v24, v25\n\t"
    "v_cvt_pk_bf16_f32 v37, v26, v27\n\t"
    "v_cvt_pk_bf16_f32 v38, v28, v29\n\t"
    "v_cvt_pk_bf16_f32 v39, v30, v31\n\t"
    "v_min_u32 v36, -2, v36\n\t"
    "v_min_u32 v37, -2, v37\n\t"
    "v_min_u32 v38, -2, v38\n\t"
    "v_min_u32 v39, -2, v39\n\t"
    "global_store_dwordx4 v4, v[36:39], %[od] sc1\n\t"
    "v_lshlrev_b32 v40, 10, v5\n\t"
    "s_waitcnt lgkmcnt(0)\n\t"
    "s_and_b32 s36, s36, 0xffffff\n\t"
    "s_or_b32 s36, s36, %[sn]\n\t"
    "v_mov_b32 v41, s36\n\t"
    "s_mov_b64 exec, 7\n\t"
    "global_store_dword v40, v41, %[db] offset:0 sc1\n\t"
    "s_mov_b64 exec, -1\n\t"
    "ds_write_b32 v15, v44 offset:19456\n\t"
    "ds_write_b32 v15, v46 offset:19712\n\t"
    "s_setprio 1\n\t"
    "s_waitcnt lgkmcnt(0)\n\t"
    "s_barrier\n\t"
    "s_waitcnt lgkmcnt(0)\n\t"
    "s_branch 9f\n\t"
    "1:\n\t"
    "s_add_u32 %[sp], %[sp], 1\n\t"
    "s_cmp_ge_u32 %[sp], %[cap]\n\t"
    "s_cbranch_scc1 7f\n\t"
    "global_load_dwordx4 v[24:27], v5, %[ag] sc1\n\t"
    "global_load_dwordx4 v[28:31], v5, %[ag] offset:1024 sc1\n\t"
    "s_waitcnt lgkmcnt(0)\n\t"
    "s_memtime %[tc]\n\t"
    "s_nop 7\n\t"
    "s_branch 3b\n\t"
    "7:\n\t"
    "s_mov_b32 %[cap], 0\n\t"
    "s_branch 2b\n\t"
    "4:\n\t"
    "s_add_u32 %[sp], %[sp], 0x10000\n\t"
    "s_lshr_b32 %[st], %[sp], 16\n\t"
    "s_cmp_ge_u32 %[st], %[cap]\n\t"
    "s_cbranch_scc1 8f\n\t"
    "global_load_dwordx4 v[6:9], v5, %[vs] sc1\n\t"
    "global_load_dwordx4 v[10:13], v5, %[vs] offset:1024 sc1\n\t"
    "s_waitcnt vmcnt(0) lgkmcnt(0)\n\t"
    "s_branch 5b\n\t"
    "8:\n\t"
    "s_mov_b32 %[cap], 0\n\t"
    "s_branch 6b\n\t"
    "18:\n\t"
    "s_add_u32 %[sp], %[sp], 0x10000\n\t"
    "s_lshr_b32 %[st], %[sp], 16\n\t"
    "s_cmp_ge_u32 %[st], %[cap]\n\t"
    "s_cbranch_scc1 20f\n\t"
    "v_lshrrev_b32 v46, 2, v5\n\t"
    "global_load_dword v44, v46, %[v2] sc1\n\t"
    "global_load_dword v46, v46, %[v2] offset:256 sc1\n\t"
    "s_waitcnt vmcnt(0) lgkmcnt(0)\n\t"
    "s_branch 17b\n\t"
    "20:\n\t"
    "s_mov_b32 %[cap], 0\n\t"
    "s_branch 19b\n\t"
    "22:\n\t"
    "ds_read_b32 v8, v15 offset:29184\n\t"
    "ds_read_b128 v[32:35], v5 offset:26624\n\t"
    "s_waitcnt lgkmcnt(0)\n\t"
    "s_branch 21b\n\t"
    "10:\n\t"
    "v_max_f32 v40, v40, v14\n\t"
    "v_mov_b32 v42, v40\n\t"
    "s_nop 1\n\t"
    "v_permlane16_swap_b32 v40, v42\n\t"
    "v_max_f32 v40, v40, v42\n\t"
    "v_mov_b32 v42, v40\n\t"
    "s_nop 1\n\t"
    "v_permlane32_swap_b32 v40, v42\n\t"
    "v_max_f32 v40, v40, v42\n\t"
    "v_sub_f32 v34, v32, v40\n\t"
    "v_sub_f32 v35, v33, v40\n\t"
    "v_sub_f32 v40, v14, v40\n\t"
    "v_exp_f32 v40, v40\n\t"
    "v_exp_f32 v34, v34\n\t"
    "v_exp_f32 v35, v35\n\t"
    "v_mul_f32 v24, v24, v40\n\t"
    "v_mul_f32 v25, v25, v40\n\t"
    "v_mul_f32 v26, v26, v40\n\t"
    "v_mul_f32 v27, v27, v40\n\t"
    "v_mul_f32 v28, v28, v40\n\t"
    "v_mul_f32 v29, v29, v40\n\t"
    "v_mul_f32 v30, v30, v40\n\t"
    "v_mul_f32 v31, v31, v40\n\t"
    "v_mul_f32 v52, v52, v40\n\t"
    "v_mul_f32 v53, v53, v40\n\t"
    "v_mul_f32 v54, v54, v40\n\t"
    "v_mul_f32 v55, v55, v40\n\t"
    "s_branch 11b\n\t"
    "12:\n\t"
    "v_max3_f32 v14, v24, v25, v26\n\t"
    "v_max3_f32 v40, v27, v28, v29\n\t"
    "v_max3_f32 v14, v14, v30, v31\n\t"
    "v_max3_f32 v14, v14, v40, %[neg]\n\t"
    "v_mov_b32 v40, v14\n\t"
    "s_nop 1\n\t"
    "v_permlane16_swap_b32 v14, v40\n\t"
    "v_max_f32 v14, v14, v40\n\t"
    "v_mov_b32 v40, v14\n\t"
    "s_nop 1\n\t"
    "v_permlane32_swap_b32 v14, v40\n\t"
    "v_max_f32 v14, v14, v40\n\t"
    "v_sub_f32 v24, v24, v14\n\t"
    "v_sub_f32 v25, v25, v14\n\t"
    "v_sub_f32 v26, v26, v14\n\t"
    "v_sub_f32 v27, v27, v14\n\t"
    "v_sub_f32 v28, v28, v14\n\t"
    "v_sub_f32 v29, v29, v14\n\t"
    "v_sub_f32 v30, v30, v14\n\t"
    "v_sub_f32 v31, v31, v14\n\t"
    "s_mov_b32 %[bg], 0x7fc00000\n\t"
    "s_branch 13b\n\t"
    "9:"
    : [tc] "=&s"(tc), [sp] "=&s"(sp), [cap] "+s"(cap), [st] "=&s"(st), [bg] "=&s"(bg)
    : [ag] "s"(ag), [c44] "s"(0x4444u), [lsc] "s"(LSC), [sn] "s"(sn), [pb] "s"(pb), [vs] "s"(vs), [od] "s"(od), [neg] "s"(-1.0e38f), [one] "s"(0x3F803F80u), [big] "s"(24.0f), [v2] "s"(v2), [db] "s"(db), "{v[64:95]}"(W.w0), "{v[96:127]}"(W.w1), "{v[128:159]}"(W.w2), "{v[160:191]}"(W.w3), "{v[192:223]}"(W.w4), "{v[224:255]}"(W.w5)
    : "v4", "v5", "v6", "v7", "v8", "v9", "v10", "v11", "v12", "v13", "v14", "v15", "v16", "v17", "v18", "v19", "v20", "v21", "v22", "v23", "v24", "v25", "v26", "v27", "v28", "v29", "v30", "v31", "v32", "v33", "v34", "v35", "v36", "v37", "v38", "v39", "v40", "v41", "v42", "v43", "v44", "v45", "v46", "v47", "v48", "v49", "v50", "v51", "v52", "v53", "v54", "v55", "v56", "v57", "v58", "v59", "v60", "v61", "v62", "v63", "vcc", "scc", "m0", "memory", "s36", "s37", "s38");
}
template <> __device__ __forceinline__ void qk_step<3>(const QKW& W, const char* ag, const char* vs, const char* v2, const char* db, const char* od, unsigned pb, unsigned sn, unsigned& cap, unsigned& sp,
    unsigned long long& tc, unsigned long long* ts, unsigned& d0, unsigned& d1) {
  unsigned st, bg;
  asm volatile("v_mbcnt_lo_u32_b32 v60, -1, 0\n\t"
    "v_mbcnt_hi_u32_b32 v60, -1, v60\n\t"
    "v_lshlrev_b32 v5, 4, v60\n\t"
    "s_mov_b32 %[sp], 0\n\t"
    "s_setprio 2\n\t"
    "global_load_dwordx4 v[24:27], v5, %[ag] sc1\n\t"
    "global_load_dwordx4 v[28:31], v5, %[ag] offset:1024 sc1\n\t"
    "s_memtime %[tc]\n\t"
    "v_lshrrev_b32 v32, 4, v60\n\t"
    "v_and_b32 v33, 15, v60\n\t"
    "v_lshrrev_b32 v34, 2, v33\n\t"
    "v_and_b32 v35, 3, v60\n\t"
    "v_and_b32 v61, 1, v60\n\t"
    "v_lshlrev_b32 v61, 3, v61\n\t"
    "v_lshl_or_b32 v7, v34, 4, v61\n\t"
    "v_lshl_or_b32 v7, v32, 7, v7\n\t"
    "v_and_b32 v10, 2, v34\n\t"
    "v_xor_b32 v10, v10, v32\n\t"
    "v_lshl_or_b32 v10, v10, 4, %[pb]\n\t"
    "v_lshl_or_b32 v10, v33, 6, v10\n\t"
    "v_and_b32 v14, 32, v60\n\t"
    "v_lshl_or_b32 v14, v35, 3, v14\n\t"
    "v_lshrrev_b32 v34, 2, v60\n\t"
    "v_lshl_or_b32 v14, v34, 6, v14\n\t"
    "v_or_b32 v14, %[pb], v14\n\t"
    "v_and_b32 v61, 3, v60\n\t"
    "v_lshlrev_b32 v61, 6, v61\n\t"
    "v_lshl_or_b32 v4, v32, 4, v61\n\t"
    "v_and_b32 v61, 4, v60\n\t"
    "v_lshl_or_b32 v4, v61, 10, v4\n\t"
    "v_and_b32 v61, 8, v60\n\t"
    "v_lshl_or_b32 v4, v61, 14, v4\n\t"
    "v_or_b32 v4, 768, v4\n\t"
    "v_and_b32 v61, 2, v60\n\t"
    "v_mul_u32_u24 v61, 0x5555, v61\n\t"
    "v_or_b32 v6, %[c44], v61\n\t"
    "v_lshlrev_b32 v61, 2, v32\n\t"
    "v_lshl_or_b32 v15, v33, 4, v61\n\t"
    "3:\n\t"
    "v_mov_b32 v16, 0\n\t"
    "v_mov_b32 v17, 0\n\t"
    "v_mov_b32 v18, 0\n\t"
    "v_mov_b32 v19, 0\n\t"
    "v_mov_b32 v20, 0\n\t"
    "v_mov_b32 v21, 0\n\t"
    "v_mov_b32 v22, 0\n\t"
    "v_mov_b32 v23, 0\n\t"
    "s_waitcnt vmcnt(0) lgkmcnt(0)\n\t"
    "33:\n\t"
    "ds_write_b128 v5, v[24:27] offset:6144\n\t"
    "ds_write_b128 v5, v[28:31] offset:7168\n\t"
    "v_smfmac_f32_16x16x64_bf16 v[16:19], v[24:27], v[160:167], v6\n\t"
    "v_max3_u32 v60, v24, v25, v26\n\t"
    "v_max3_u32 v61, v27, v28, v29\n\t"
    "v_smfmac_f32_16x16x64_bf16 v[20:23], v[24:27], v[168:175], v6\n\t"
    "v_max3_u32 v60, v60, v30, v31\n\t"
    "v_max_u32 v60, v60, v61\n\t"
    "v_cmp_eq_u32 vcc, -1, v60\n\t"
    "s_cbranch_vccnz 1f\n\t"
    "2:\n\t"
    "s_waitcnt lgkmcnt(0)\n\t"
    "s_barrier\n\t"
    "ds_read_b128 v[32:35], v5 offset:0\n\t"
    "ds_read_b128 v[36:39], v5 offset:1024\n\t"
    "v_smfmac_f32_16x16x64_bf16 v[16:19], v[28:31], v[176:183], v6\n\t"
    "ds_read_b128 v[40:43], v5 offset:2048\n\t"
    "ds_read_b128 v[44:47], v5 offset:3072\n\t"
    "v_smfmac_f32_16x16x64_bf16 v[20:23], v[28:31], v[184:191], v6\n\t"
    "s_waitcnt lgkmcnt(3)\n\t"
    "v_smfmac_f32_16x16x64_bf16 v[16:19], v[32:35], v[64:71], v6\n\t"
    "ds_read_b128 v[48:51], v5 offset:4096\n\t"
    "v_smfmac_f32_16x16x64_bf16 v[20:23], v[32:35], v[72:79], v6\n\t"
    "ds_read_b128 v[52:55], v5 offset:5120\n\t"
    "s_waitcnt lgkmcnt(4)\n\t"
    "v_smfmac_f32_16x16x64_bf16 v[16:19], v[36:39], v[80:87], v6\n\t"
    "ds_read_b128 v[24:27], v5 offset:8192\n\t"
    "v_smfmac_f32_16x16x64_bf16 v[20:23], v[36:39], v[88:95], v6\n\t"
    "ds_read_b128 v[28:31], v5 offset:9216\n\t"
    "s_waitcnt lgkmcnt(5)\n\t"
    "v_smfmac_f32_16x16x64_bf16 v[16:19], v[40:43], v[96:103], v6\n\t"
    "ds_read_b128 v[56:59], v5 offset:10240\n\t"
    "v_smfmac_f32_16x16x64_bf16 v[20:23], v[40:43], v[104:111], v6\n\t"
    "ds_read_b128 v[60:63], v5 offset:11264\n\t"
    "ds_read_b128 v[36:39], v10 offset:35840\n\t"
    "s_waitcnt lgkmcnt(7)\n\t"
    "v_smfmac_f32_16x16x64_bf16 v[16:19], v[44:47], v[112:119], v6\n\t"
    "ds_read_b128 v[40:43], v10 offset:36864\n\t"
    "v_smfmac_f32_16x16x64_bf16 v[20:23], v[44:47], v[120:127], v6\n\t"
    "ds_read_b128 v[44:47], v10 offset:37888\n\t"
    "s_waitcnt lgkmcnt(8)\n\t"
    "v_smfmac_f32_16x16x64_bf16 v[16:19], v[48:51], v[128:135], v6\n\t"
    "v_smfmac_f32_16x16x64_bf16 v[20:23], v[48:51], v[136:143], v6\n\t"
    "ds_read_b128 v[48:51], v10 offset:38912\n\t"
    "s_waitcnt lgkmcnt(8)\n\t"
    "v_smfmac_f32_16x16x64_bf16 v[16:19], v[52:55], v[144:151], v6\n\t"
    "v_smfmac_f32_16x16x64_bf16 v[20:23], v[52:55], v[152:159], v6\n\t"
    "ds_read_b128 v[52:55], v10 offset:39936\n\t"
    "s_waitcnt lgkmcnt(8)\n\t"
    "v_smfmac_f32_16x16x64_bf16 v[16:19], v[24:27], v[192:199], v6\n\t"
    "v_smfmac_f32_16x16x64_bf16 v[20:23], v[24:27], v[200:207], v6\n\t"
    "ds_read_b128 v[24:27], v10 offset:32768\n\t"
    "s_waitcnt lgkmcnt(8)\n\t"
    "v_smfmac_f32_16x16x64_bf16 v[16:19], v[28:31], v[208:215], v6\n\t"
    "ds_read_b32 v8, v15 offset:29440\n\t"
    "ds_read_b128 v[32:35], v5 offset:27648\n\t"
    "s_setprio 3\n\t"
    "s_waitcnt lgkmcnt(9)\n\t"
    "v_smfmac_f32_16x16x64_bf16 v[16:19], v[56:59], v[224:231], v6\n\t"
    "s_waitcnt lgkmcnt(8)\n\t"
    "v_smfmac_f32_16x16x64_bf16 v[16:19], v[60:63], v[240:247], v6\n\t"
    "v_smfmac_f32_16x16x64_bf16 v[20:23], v[28:31], v[216:223], v6\n\t"
    "s_waitcnt lgkmcnt(0)\n\t"
    "21:\n\t"
    "v_cmp_ne_u32 vcc, %[sn], v8\n\t"
    "s_cbranch_vccnz 22f\n\t"
    "v_mul_f32 v32, %[lsc], v32\n\t"
    "v_mul_f32 v33, %[lsc], v33\n\t"
    "v_smfmac_f32_16x16x64_bf16 v[20:23], v[56:59], v[232:239], v6\n\t"
    "v_mul_f32 v34, %[lsc], v34\n\t"
    "v_mul_f32 v35, %[lsc], v35\n\t"
    "v_add_f32 v16, v16, v18\n\t"
    "v_add_f32 v17, v17, v19\n\t"
    "v_smfmac_f32_16x16x64_bf16 v[20:23], v[60:63], v[248:255], v6\n\t"
    "v_fma_f32 v16, v16, %[lsc], v32\n\t"
    "v_fma_f32 v17, v17, %[lsc], v33\n\t"
    "v_cvt_pk_bf16_f32 v16, v16, v17\n\t"
    "ds_write_b32 v15, v16 offset:17920\n\t"
    "s_mov_b32 %[bg], %[big]\n\t"
    "s_nop 2\n\t"
    "v_add_f32 v20, v20, v22\n\t"
    "v_add_f32 v21, v21, v23\n\t"
    "v_fma_f32 v20, v20, %[lsc], v34\n\t"
    "v_fma_f32 v21, v21, %[lsc], v35\n\t"
    "v_cvt_pk_bf16_f32 v20, v20, v21\n\t"
    "ds_write_b32 v15, v20 offset:18176\n\t"
    "ds_read_b128 v[28:31], v10 offset:33792\n\t"
    "ds_read_b128 v[32:35], v10 offset:34816\n\t"
    "s_waitcnt lgkmcnt(2)\n\t"
    "s_barrier\n\t"
    "ds_read_b64_tr_b16 v[16:17], v7 offset:16384\n\t"
    "ds_read_b64_tr_b16 v[18:19], v7 offset:16448\n\t"
    "ds_read_b64_tr_b16 v[20:21], v7 offset:16896\n\t"
    "ds_read_b64_tr_b16 v[22:23], v7 offset:16960\n\t"
    "ds_read_b64_tr_b16 v[56:57], v7 offset:17408\n\t"
    "ds_read_b64_tr_b16 v[58:59], v7 offset:17472\n\t"
    "ds_read_b64_tr_b16 v[60:61], v7 offset:17920\n\t"
    "ds_read_b64_tr_b16 v[62:63], v7 offset:17984\n\t"
    "global_load_dwordx4 v[6:9], v5, %[vs] sc1\n\t"
    "global_load_dwordx4 v[10:13], v5, %[vs] offset:1024 sc1\n\t"
    "s_waitcnt lgkmcnt(6)\n\t"
    "v_mfma_f32_16x16x32_bf16 v[24:27], v[24:27], v[16:19], 0\n\t"
    "v_mfma_f32_16x16x32_bf16 v[28:31], v[28:31], v[16:19], 0\n\t"
    "s_waitcnt lgkmcnt(4)\n\t"
    "v_mfma_f32_16x16x32_bf16 v[24:27], v[32:35], v[20:23], v[24:27]\n\t"
    "v_mfma_f32_16x16x32_bf16 v[28:31], v[36:39], v[20:23], v[28:31]\n\t"
    "v_xor_b32 v39, 32, v14\n\t"
    "ds_read_b64_tr_b16 v[32:33], v14 offset:47104\n\t"
    "ds_read_b64_tr_b16 v[34:35], v14 offset:48128\n\t"
    "ds_read_b64_tr_b16 v[36:37], v39 offset:47104\n\t"
    "ds_read_b64_tr_b16 v[38:39], v39 offset:48128\n\t"
    "s_waitcnt lgkmcnt(6)\n\t"
    "v_mfma_f32_16x16x32_bf16 v[24:27], v[40:43], v[56:59], v[24:27]\n\t"
    "v_mfma_f32_16x16x32_bf16 v[28:31], v[44:47], v[56:59], v[28:31]\n\t"
    "s_waitcnt lgkmcnt(4)\n\t"
    "v_mfma_f32_16x16x32_bf16 v[24:27], v[48:51], v[60:63], v[24:27]\n\t"
    "v_mfma_f32_16x16x32_bf16 v[28:31], v[52:55], v[60:63], v[28:31]\n\t"
    "v_mov_b32 v52, %[one]\n\t"
    "v_mov_b32 v53, %[one]\n\t"
    "v_mov_b32 v54, %[one]\n\t"
    "v_mov_b32 v55, %[one]\n\t"
    "v_mov_b32 v45, 0\n\t"
    "v_mov_b32 v47, 0\n\t"
    "v_mov_b32 v48, %[one]\n\t"
    "v_max3_f32 v14, |v24|, |v25|, |v26|\n\t"
    "v_max3_f32 v40, |v27|, |v28|, |v29|\n\t"
    "v_max3_f32 v14, v14, |v30|, |v31|\n\t"
    "v_max_f32 v14, v14, v40\n\t"
    "v_cmp_nge_f32 vcc, %[big], v14\n\t"
    "v_mov_b32 v14, 0\n\t"
    "s_cbranch_vccnz 12f\n\t"
    "13:\n\t"
    "v_exp_f32 v24, v24\n\t"
    "v_exp_f32 v25, v25\n\t"
    "v_exp_f32 v26, v26\n\t"
    "v_exp_f32 v27, v27\n\t"
    "v_exp_f32 v28, v28\n\t"
    "v_exp_f32 v29, v29\n\t"
    "v_exp_f32 v30, v30\n\t"
    "v_exp_f32 v31, v31\n\t"
    "v_lshrrev_b32 v46, 2, v5\n\t"
    "global_load_dword v44, v46, %[v2] sc1\n\t"
    "global_load_dword v46, v46, %[v2] offset:256 sc1\n\t"
    "v_cvt_pk_bf16_f32 v40, v24, v25\n\t"
    "v_cvt_pk_bf16_f32 v41, v26, v27\n\t"
    "v_cvt_pk_bf16_f32 v42, v28, v29\n\t"
    "v_cvt_pk_bf16_f32 v43, v30, v31\n\t"
    "v_mov_b32 v49, 0\n\t"
    "v_mov_b32 v51, 0\n\t"
    "s_waitcnt lgkmcnt(0)\n\t"
    "v_mfma_f32_16x16x32_bf16 v[52:55], v[52:55], v[40:43], 0\n\t"
    "v_mfma_f32_16x16x32_bf16 v[24:27], v[32:35], v[40:43], 0\n\t"
    "v_mfma_f32_16x16x32_bf16 v[28:31], v[36:39], v[40:43], 0\n\t"
    "s_waitcnt vmcnt(3)\n\t"
    "5:\n\t"
    "v_mfma_f32_16x16x32_bf16 v[32:35], v[6:9], v[16:19], 0\n\t"
    "v_mfma_f32_16x16x32_bf16 v[36:39], v[6:9], v[20:23], 0\n\t"
    "s_waitcnt vmcnt(2)\n\t"
    "v_mfma_f32_16x16x32_bf16 v[32:35], v[10:13], v[56:59], v[32:35]\n\t"
    "v_mfma_f32_16x16x32_bf16 v[36:39], v[10:13], v[60:63], v[36:39]\n\t"
    "v_max3_u32 v40, v6, v7, v8\n\t"
    "v_max3_u32 v41, v9, v10, v11\n\t"
    "v_max3_u32 v40, v40, v12, v13\n\t"
    "v_max_u32 v40, v40, v41\n\t"
    "v_cmp_eq_u32 vcc, -1, v40\n\t"
    "s_cbranch_vccnz 4f\n\t"
    "6:\n\t"
    "s_nop 0\n\t"
    "s_nop 0\n\t"
    "v_add_f32 v32, v32, v38\n\t"
    "v_add_f32 v33, v33, v39\n\t"
    "v_exp_f32 v34, v32\n\t"
    "v_max_f32 v40, v32, v33\n\t"
    "v_exp_f32 v35, v33\n\t"
    "v_cmp_nge_f32 vcc, %[bg], v40\n\t"
    "s_cbranch_vccnz 10f\n\t"
    "11:\n\t"
    "v_cvt_pk_bf16_f32 v50, v34, v35\n\t"
    "17:\n\t"
    "s_waitcnt vmcnt(0) lgkmcnt(0)\n\t"
    "v_max_u32 v40, v44, v46\n\t"
    "v_cmp_eq_u32 vcc, -1, v40\n\t"
    "s_cbranch_vccnz 18f\n\t"
    "19:\n\t"
    "s_memrealtime s[36:37]\n\t"
    "v_mfma_f32_16x16x16_bf16 v[52:55], v[48:49], v[50:51], v[52:55]\n\t"
    "v_mfma_f32_16x16x16_bf16 v[24:27], v[44:45], v[50:51], v[24:27]\n\t"
    "v_mfma_f32_16x16x16_bf16 v[28:31], v[46:47], v[50:51], v[28:31]\n\t"
    "s_nop 5\n\t"
    "v_rcp_f32 v42, v52\n\t"
    "s_nop 0\n\t"
    "v_pk_mul_f32 v[24:25], v[24:25], v[42:43] op_sel_hi:[1,0]\n\t"
    "v_pk_mul_f32 v[26:27], v[26:27], v[42:43] op_sel_hi:[1,0]\n\t"
    "v_pk_mul_f32 v[28:29], v[28:29], v[42:43] op_sel_hi:[1,0]\n\t"
    "v_pk_mul_f32 v[30:31], v[30:31], v[42:43] op_sel_hi:[1,0]\n\t"
    "v_cvt_pk_bf16_f32 v36, v24, v25\n\t"
    "v_cvt_pk_bf16_f32 v37, v26, v27\n\t"
    "v_cvt_pk_bf16_f32 v38, v28, v29\n\t"
    "v_cvt_pk_bf16_f32 v39, v30, v31\n\t"
    "v_min_u32 v36, -2, v36\n\t"
    "v_min_u32 v37, -2, v37\n\t"
    "v_min_u32 v38, -2, v38\n\t"
    "v_min_u32 v39, -2, v39\n\t"
    "global_store_dwordx4 v4, v[36:39], %[od] sc1\n\t"
    "v_lshlrev_b32 v40, 10, v5\n\t"
    "s_waitcnt lgkmcnt(0)\n\t"
    "s_and_b32 s36, s36, 0xffffff\n\t"
    "s_or_b32 s36, s36, %[sn]\n\t"
    "v_mov_b32 v41, s36\n\t"
    "s_mov_b64 exec, 7\n\t"
    "global_store_dword v40, v41, %[db] offset:8 sc1\n\t"
    "s_mov_b64 exec, -1\n\t"
    "ds_write_b32 v15, v44 offset:19968\n\t"
    "ds_write_b32 v15, v46 offset:20224\n\t"
    "s_setprio 1\n\t"
    "s_waitcnt lgkmcnt(0)\n\t"
    "s_barrier\n\t"
    "s_waitcnt lgkmcnt(0)\n\t"
    "s_branch 9f\n\t"
    "1:\n\t"
    "s_add_u32 %[sp], %[sp], 1\n\t"
    "s_cmp_ge_u32 %[sp], %[cap]\n\t"
    "s_cbranch_scc1 7f\n\t"
    "global_load_dwordx4 v[24:27], v5, %[ag] sc1\n\t"
    "global_load_dwordx4 v[28:31], v5, %[ag] offset:1024 sc1\n\t"
    "s_waitcnt lgkmcnt(0)\n\t"
    "s_memtime %[tc]\n\t"
    "s_nop 7\n\t"
    "s_branch 3b\n\t"
    "7:\n\t"
    "s_mov_b32 %[cap], 0\n\t"
    "s_branch 2b\n\t"
    "4:\n\t"
    "s_add_u32 %[sp], %[sp], 0x10000\n\t"
    "s_lshr_b32 %[st], %[sp], 16\n\t"
    "s_cmp_ge_u32 %[st], %[cap]\n\t"
    "s_cbranch_scc1 8f\n\t"
    "global_load_dwordx4 v[6:9], v5, %[vs] sc1\n\t"
    "global_load_dwordx4 v[10:13], v5, %[vs] offset:1024 sc1\n\t"
    "s_waitcnt vmcnt(0) lgkmcnt(0)\n\t"
    "s_branch 5b\n\t"
    "8:\n\t"
    "s_mov_b32 %[cap], 0\n\t"
    "s_branch 6b\n\t"
    "18:\n\t"
    "s_add_u32 %[sp], %[sp], 0x10000\n\t"
    "s_lshr_b32 %[st], %[sp], 16\n\t"
    "s_cmp_ge_u32 %[st], %[cap]\n\t"
    "s_cbranch_scc1 20f\n\t"
    "v_lshrrev_b32 v46, 2, v5\n\t"
    "global_load_dword v44, v46, %[v2] sc1\n\t"
    "global_load_dword v46, v46, %[v2] offset:256 sc1\n\t"
    "s_waitcnt vmcnt(0) lgkmcnt(0)\n\t"
    "s_branch 17b\n\t"
    "20:\n\t"
    "s_mov_b32 %[cap], 0\n\t"
    "s_branch 19b\n\t"
    "22:\n\t"
    "ds_read_b32 v8, v15 offset:29440\n\t"
    "ds_read_b128 v[32:35], v5 offset:27648\n\t"
    "s_waitcnt lgkmcnt(0)\n\t"
    "s_branch 21b\n\t"
    "10:\n\t"
    "v_max_f32 v40, v40, v14\n\t"
    "v_mov_b32 v42, v40\n\t"
    "s_nop 1\n\t"
    "v_permlane16_swap_b32 v40, v42\n\t"
    "v_max_f32 v40, v40, v42\n\t"
    "v_mov_b32 v42, v40\n\t"
    "s_nop 1\n\t"
    "v_permlane32_swap_b32 v40, v42\n\t"
    "v_max_f32 v40, v40, v42\n\t"
    "v_sub_f32 v34, v32, v40\n\t"
    "v_sub_f32 v35, v33, v40\n\t"
    "v_sub_f32 v40, v14, v40\n\t"
    "v_exp_f32 v40, v40\n\t"
    "v_exp_f32 v34, v34\n\t"
    "v_exp_f32 v35, v35\n\t"
    "v_mul_f32 v24, v24, v40\n\t"
    "v_mul_f32 v25, v25, v40\n\t"
    "v_mul_f32 v26, v26, v40\n\t"
    "v_mul_f32 v27, v27, v40\n\t"
    "v_mul_f32 v28, v28, v40\n\t"
    "v_mul_f32 v29, v29, v40\n\t"
    "v_mul_f32 v30, v30, v40\n\t"
    "v_mul_f32 v31, v31, v40\n\t"
    "v_mul_f32 v52, v52, v40\n\t"
    "v_mul_f32 v53, v53, v40\n\t"
    "v_mul_f32 v54, v54, v40\n\t"
    "v_mul_f32 v55, v55, v40\n\t"
    "s_branch 11b\n\t"
    "12:\n\t"
    "v_max3_f32 v14, v24, v25, v26\n\t"
    "v_max3_f32 v40, v27, v28, v29\n\t"
    "v_max3_f32 v14, v14, v30, v31\n\t"
    "v_max3_f32 v14, v14, v40, %[neg]\n\t"
    "v_mov_b32 v40, v14\n\t"
    "s_nop 1\n\t"
    "v_permlane16_swap_b32 v14, v40\n\t"
    "v_max_f32 v14, v14, v40\n\t"
    "v_mov_b32 v40, v14\n\t"
    "s_nop 1\n\t"
    "v_permlane32_swap_b32 v14, v40\n\t"
    "v_max_f32 v14, v14, v40\n\t"
    "v_sub_f32 v24, v24, v14\n\t"
    "v_sub_f32 v25, v25, v14\n\t"
    "v_sub_f32 v26, v26, v14\n\t"
    "v_sub_f32 v27, v27, v14\n\t"
    "v_sub_f32 v28, v28, v14\n\t"
    "v_sub_f32 v29, v29, v14\n\t"
    "v_sub_f32 v30, v30, v14\n\t"
    "v_sub_f32 v31, v31, v14\n\t"
    "s_mov_b32 %[bg], 0x7fc00000\n\t"
    "s_branch 13b\n\t"
    "9:"
    : [tc] "=&s"(tc), [sp] "=&s"(sp), [cap] "+s"(cap), [st] "=&s"(st), [bg] "=&s"(bg)
    : [ag] "s"(ag), [c44] "s"(0x4444u), [lsc] "s"(LSC), [sn] "s"(sn), [pb] "s"(pb), [vs] "s"(vs), [od] "s"(od), [neg] "s"(-1.0e38f), [one] "s"(0x3F803F80u), [big] "s"(24.0f), [v2] "s"(v2), [db] "s"(db), "{v[64:95]}"(W.w0), "{v[96:127]}"(W.w1), "{v[128:159]}"(W.w2), "{v[160:191]}"(W.w3), "{v[192:223]}"(W.w4), "{v[224:255]}"(W.w5)
    : "v4", "v5", "v6", "v7", "v8", "v9", "v10", "v11", "v12", "v13", "v14", "v15", "v16", "v17", "v18", "v19", "v20", "v21", "v22", "v23", "v24", "v25", "v26", "v27", "v28", "v29", "v30", "v31", "v32", "v33", "v34", "v35", "v36", "v37", "v38", "v39", "v40", "v41", "v42", "v43", "v44", "v45", "v46", "v47", "v48", "v49", "v50", "v51", "v52", "v53", "v54", "v55", "v56", "v57", "v58", "v59", "v60", "v61", "v62", "v63", "vcc", "scc", "m0", "memory", "s36", "s37", "s38");
}
template <> __device__ __forceinline__ void qk_step<4>(const QKW& W, const char* ag, const char* vs, const char* v2, const char* db, const char* od, unsigned pb, unsigned sn, unsigned& cap, unsigned& sp,
    unsigned long long& tc, unsigned long long* ts, unsigned& d0, unsigned& d1) {
  unsigned st, bg;
  asm volatile("v_mbcnt_lo_u32_b32 v60, -1, 0\n\t"
    "v_mbcnt_hi_u32_b32 v60, -1, v60\n\t"
    "v_lshlrev_b32 v6, 4, v60\n\t"
    "s_mov_b32 %[sp], 0\n\t"
    "s_setprio 3\n\t"
    "global_load_dwordx4 v[24:27], v6, %[ag] sc1\n\t"
    "global_load_dwordx4 v[28:31], v6, %[ag] offset:-4096 sc1\n\t"
    "s_memtime %[tc]\n\t"
    "v_lshrrev_b32 v56, 4, v60\n\t"
    "v_and_b32 v57, 15, v60\n\t"
    "v_mov_b32 v20, 0\n\t"
    "v_mov_b32 v21, 0\n\t"
    "v_mov_b32 v22, 0\n\t"
    "v_mov_b32 v23, 0\n\t"
    "v_mov_b32 v11, %[sn]\n\t"
    "v_mov_b32 v10, -1\n\t"
    "v_and_b32 v61, 2, v60\n\t"
    "v_mul_u32_u24 v61, 0x5555, v61\n\t"
    "v_or_b32 v8, %[c44], v61\n\t"
    "v_lshlrev_b32 v61, 2, v56\n\t"
    "v_lshl_or_b32 v9, v57, 4, v61\n\t"
    "3:\n\t"
    "v_mov_b32 v12, 0\n\t"
    "v_mov_b32 v13, 0\n\t"
    "v_mov_b32 v14, 0\n\t"
    "v_mov_b32 v15, 0\n\t"
    "v_mov_b32 v16, 0\n\t"
    "v_mov_b32 v17, 0\n\t"
    "v_mov_b32 v18, 0\n\t"
    "v_mov_b32 v19, 0\n\t"
    "s_waitcnt vmcnt(0) lgkmcnt(0)\n\t"
    "33:\n\t"
    "ds_write_b128 v6, v[24:27] offset:12288\n\t"
    "ds_write_b128 v6, v[28:31] offset:8192\n\t"
    "v_smfmac_f32_16x16x64_bf16 v[12:15], v[24:27], v[64:71], v8\n\t"
    "v_max3_u32 v60, v24, v25, v26\n\t"
    "v_max3_u32 v61, v27, v28, v29\n\t"
    "v_smfmac_f32_16x16x64_bf16 v[16:19], v[24:27], v[72:79], v8\n\t"
    "v_max3_u32 v60, v60, v30, v31\n\t"
    "v_max_u32 v60, v60, v61\n\t"
    "v_cmp_eq_u32 vcc, -1, v60\n\t"
    "s_cbranch_vccnz 1f\n\t"
    "2:\n\t"
    "s_waitcnt lgkmcnt(0)\n\t"
    "s_barrier\n\t"
    "ds_read_b128 v[32:35], v6 offset:13312\n\t"
    "ds_read_b128 v[36:39], v6 offset:14336\n\t"
    "ds_read_b128 v[40:43], v6 offset:15360\n\t"
    "ds_write_b32 v9, v10 offset:18432\n\t"
    "s_waitcnt lgkmcnt(3)\n\t"
    "v_smfmac_f32_16x16x64_bf16 v[12:15], v[32:35], v[80:87], v8\n\t"
    "v_smfmac_f32_16x16x64_bf16 v[16:19], v[32:35], v[88:95], v8\n\t"
    "s_waitcnt lgkmcnt(2)\n\t"
    "v_smfmac_f32_16x16x64_bf16 v[12:15], v[36:39], v[96:103], v8\n\t"
    "v_smfmac_f32_16x16x64_bf16 v[16:19], v[36:39], v[104:111], v8\n\t"
    "s_waitcnt lgkmcnt(1)\n\t"
    "v_smfmac_f32_16x16x64_bf16 v[12:15], v[40:43], v[112:119], v8\n\t"
    "v_smfmac_f32_16x16x64_bf16 v[16:19], v[40:43], v[120:127], v8\n\t"
    "s_setprio 0\n\t"
    "s_sleep 1\n\t"
    "s_nop 6\n\t"
    "v_add_f32 v12, v12, v14\n\t"
    "v_add_f32 v13, v13, v15\n\t"
    "v_add_f32 v14, v16, v18\n\t"
    "v_add_f32 v15, v17, v19\n\t"
    "ds_write_b128 v6, v[12:15] offset:24576\n\t"
    "ds_write_b32 v9, v11 offset:28672\n\t"
    "s_setprio 0\n\t"
    "s_memrealtime s[36:37]\n\t"
    "s_waitcnt lgkmcnt(0)\n\t"
    "s_mov_b32 s38, s36\n\t"
    "s_min_u32 s37, %[d0], %[d1]\n\t"
    "s_add_u32 s36, s36, s37\n\t"
    "s_and_b32 s36, s36, 0xffffff\n\t"
    "s_or_b32 s36, s36, %[sn]\n\t"
    "v_lshlrev_b32 v10, 10, v6\n\t"
    "v_mov_b32 v5, s36\n\t"
    "s_mov_b64 exec, 15\n\t"
    "global_store_dword v10, v5, %[vs] offset:-4096 sc1\n\t"
    "s_mov_b64 exec, -1\n\t"
    "ds_read_b128 v[44:47], v6 offset:0\n\t"
    "ds_read_b128 v[48:51], v6 offset:1024\n\t"
    "ds_read_b128 v[52:55], v6 offset:2048\n\t"
    "ds_read_b128 v[56:59], v6 offset:3072\n\t"
    "ds_read_b128 v[60:63], v6 offset:4096\n\t"
    "s_sleep 1\n\t"
    "v_smfmac_f32_16x16x64_bf16 v[20:23], v[28:31], v[192:199], v8\n\t"
    "ds_read_b128 v[28:31], v6 offset:5120\n\t"
    "v_smfmac_f32_16x16x64_bf16 v[20:23], v[24:27], v[224:231], v8\n\t"
    "ds_read_b128 v[24:27], v6 offset:6144\n\t"
    "v_smfmac_f32_16x16x64_bf16 v[20:23], v[32:35], v[240:247], v8\n\t"
    "ds_read_b128 v[32:35], v6 offset:7168\n\t"
    "v_smfmac_f32_16x16x64_bf16 v[20:23], v[36:39], v[232:239], v8\n\t"
    "ds_read_b128 v[36:39], v6 offset:9216\n\t"
    "v_smfmac_f32_16x16x64_bf16 v[20:23], v[40:43], v[248:255], v8\n\t"
    "ds_read_b128 v[40:43], v6 offset:10240\n\t"
    "s_waitcnt lgkmcnt(9)\n\t"
    "v_smfmac_f32_16x16x64_bf16 v[20:23], v[44:47], v[128:135], v8\n\t"
    "ds_read_b128 v[44:47], v6 offset:11264\n\t"
    "s_barrier\n\t"
    "s_setprio 0\n\t"
    "s_waitcnt lgkmcnt(9)\n\t"
    "v_smfmac_f32_16x16x64_bf16 v[20:23], v[48:51], v[144:151], v8\n\t"
    "s_waitcnt lgkmcnt(8)\n\t"
    "v_smfmac_f32_16x16x64_bf16 v[20:23], v[52:55], v[136:143], v8\n\t"
    "s_waitcnt lgkmcnt(7)\n\t"
    "v_smfmac_f32_16x16x64_bf16 v[20:23], v[56:59], v[152:159], v8\n\t"
    "s_waitcnt lgkmcnt(6)\n\t"
    "v_smfmac_f32_16x16x64_bf16 v[20:23], v[60:63], v[160:167], v8\n\t"
    "s_waitcnt lgkmcnt(5)\n\t"
    "v_smfmac_f32_16x16x64_bf16 v[20:23], v[28:31], v[176:183], v8\n\t"
    "s_waitcnt lgkmcnt(4)\n\t"
    "v_smfmac_f32_16x16x64_bf16 v[20:23], v[24:27], v[168:175], v8\n\t"
    "s_waitcnt lgkmcnt(3)\n\t"
    "v_smfmac_f32_16x16x64_bf16 v[20:23], v[32:35], v[184:191], v8\n\t"
    "s_waitcnt lgkmcnt(2)\n\t"
    "v_smfmac_f32_16x16x64_bf16 v[20:23], v[36:39], v[208:215], v8\n\t"
    "s_waitcnt lgkmcnt(1)\n\t"
    "v_smfmac_f32_16x16x64_bf16 v[20:23], v[40:43], v[200:207], v8\n\t"
    "s_waitcnt lgkmcnt(0)\n\t"
    "v_smfmac_f32_16x16x64_bf16 v[20:23], v[44:47], v[216:223], v8\n\t"
    "s_nop 7\n\t"
    "v_add_f32 v20, v20, v22\n\t"
    "v_add_f32 v21, v21, v23\n\t"
    "v_cvt_pk_bf16_f32 v20, v20, v21\n\t"
    "v_min_u32 v20, -2, v20\n\t"
    "ds_write_b32 v9, v20 offset:18432\n\t"
    "s_waitcnt lgkmcnt(0)\n\t"
    "s_setprio 3\n\t"
    "s_barrier\n\t"
    "s_memrealtime s[36:37]\n\t"
    "s_mov_b32 %[d1], %[d0]\n\t"
    "s_waitcnt lgkmcnt(0)\n\t"
    "s_sub_u32 %[d0], s36, s38\n\t"
    "s_min_u32 %[d0], %[d0], 200\n\t"
    "s_waitcnt lgkmcnt(0)\n\t"
    "s_branch 9f\n\t"
    "1:\n\t"
    "s_add_u32 %[sp], %[sp], 1\n\t"
    "s_cmp_ge_u32 %[sp], %[cap]\n\t"
    "s_cbranch_scc1 7f\n\t"
    "global_load_dwordx4 v[24:27], v6, %[ag] sc1\n\t"
    "global_load_dwordx4 v[28:31], v6, %[ag] offset:-4096 sc1\n\t"
    "s_waitcnt lgkmcnt(0)\n\t"
    "s_memtime %[tc]\n\t"
    "s_nop 7\n\t"
    "s_branch 3b\n\t"
    "7:\n\t"
    "s_mov_b32 %[cap], 0\n\t"
    "s_branch 2b\n\t"
    "9:"
    : [tc] "=&s"(tc), [sp] "=&s"(sp), [cap] "+s"(cap), [st] "=&s"(st), [d0] "+s"(d0), [d1] "+s"(d1)
    : [ag] "s"(ag), [c44] "s"(0x4444u), [lsc] "s"(LSC), [sn] "s"(sn), [vs] "s"(vs), "{v[64:95]}"(W.w0), "{v[96:127]}"(W.w1), "{v[128:159]}"(W.w2), "{v[160:191]}"(W.w3), "{v[192:223]}"(W.w4), "{v[224:255]}"(W.w5)
    : "v5", "v6", "v7", "v8", "v9", "v10", "v11", "v12", "v13", "v14", "v15", "v16", "v17", "v18", "v19", "v20", "v21", "v22", "v23", "v24", "v25", "v26", "v27", "v28", "v29", "v30", "v31", "v32", "v33", "v34", "v35", "v36", "v37", "v38", "v39", "v40", "v41", "v42", "v43", "v44", "v45", "v46", "v47", "v48", "v49", "v50", "v51", "v52", "v53", "v54", "v55", "v56", "v57", "v58", "v59", "v60", "v61", "v62", "v63", "vcc", "scc", "m0", "memory", "s36", "s37", "s38");
}
template <> __device__ __forceinline__ void qk_step<5>(const QKW& W, const char* ag, const char* vs, const char* v2, const char* db, const char* od, unsigned pb, unsigned sn, unsigned& cap, unsigned& sp,
    unsigned long long& tc, unsigned long long* ts, unsigned& d0, unsigned& d1) {
  unsigned st, bg;
  asm volatile("v_mbcnt_lo_u32_b32 v60, -1, 0\n\t"
    "v_mbcnt_hi_u32_b32 v60, -1, v60\n\t"
    "v_lshlrev_b32 v6, 4, v60\n\t"
    "s_mov_b32 %[sp], 0\n\t"
    "s_setprio 3\n\t"
    "global_load_dwordx4 v[24:27], v6, %[ag] sc1\n\t"
    "global_load_dwordx4 v[28:31], v6, %[ag] offset:-4096 sc1\n\t"
    "s_memtime %[tc]\n\t"
    "v_lshrrev_b32 v56, 4, v60\n\t"
    "v_and_b32 v57, 15, v60\n\t"
    "v_mov_b32 v20, 0\n\t"
    "v_mov_b32 v21, 0\n\t"
    "v_mov_b32 v22, 0\n\t"
    "v_mov_b32 v23, 0\n\t"
    "v_mov_b32 v11, %[sn]\n\t"
    "v_mov_b32 v10, -1\n\t"
    "v_and_b32 v61, 2, v60\n\t"
    "v_mul_u32_u24 v61, 0x5555, v61\n\t"
    "v_or_b32 v8, %[c44], v61\n\t"
    "v_lshlrev_b32 v61, 2, v56\n\t"
    "v_lshl_or_b32 v9, v57, 4, v61\n\t"
    "3:\n\t"
    "v_mov_b32 v12, 0\n\t"
    "v_mov_b32 v13, 0\n\t"
    "v_mov_b32 v14, 0\n\t"
    "v_mov_b32 v15, 0\n\t"
    "v_mov_b32 v16, 0\n\t"
    "v_mov_b32 v17, 0\n\t"
    "v_mov_b32 v18, 0\n\t"
    "v_mov_b32 v19, 0\n\t"
    "s_waitcnt vmcnt(0) lgkmcnt(0)\n\t"
    "33:\n\t"
    "ds_write_b128 v6, v[24:27] offset:13312\n\t"
    "ds_write_b128 v6, v[28:31] offset:9216\n\t"
    "v_smfmac_f32_16x16x64_bf16 v[12:15], v[24:27], v[80:87], v8\n\t"
    "v_max3_u32 v60, v24, v25, v26\n\t"
    "v_max3_u32 v61, v27, v28, v29\n\t"
    "v_smfmac_f32_16x16x64_bf16 v[16:19], v[24:27], v[88:95], v8\n\t"
    "v_max3_u32 v60, v60, v30, v31\n\t"
    "v_max_u32 v60, v60, v61\n\t"
    "v_cmp_eq_u32 vcc, -1, v60\n\t"
    "s_cbranch_vccnz 1f\n\t"
    "2:\n\t"
    "s_waitcnt lgkmcnt(0)\n\t"
    "s_barrier\n\t"
    "ds_read_b128 v[32:35], v6 offset:12288\n\t"
    "ds_read_b128 v[36:39], v6 offset:14336\n\t"
    "ds_read_b128 v[40:43], v6 offset:15360\n\t"
    "ds_write_b32 v9, v10 offset:18688\n\t"
    "s_waitcnt lgkmcnt(3)\n\t"
    "v_smfmac_f32_16x16x64_bf16 v[12:15], v[32:35], v[64:71], v8\n\t"
    "v_smfmac_f32_16x16x64_bf16 v[16:19], v[32:35], v[72:79], v8\n\t"
    "s_waitcnt lgkmcnt(2)\n\t"
    "v_smfmac_f32_16x16x64_bf16 v[12:15], v[36:39], v[96:103], v8\n\t"
    "v_smfmac_f32_16x16x64_bf16 v[16:19], v[36:39], v[104:111], v8\n\t"
    "s_waitcnt lgkmcnt(1)\n\t"
    "v_smfmac_f32_16x16x64_bf16 v[12:15], v[40:43], v[112:119], v8\n\t"
    "v_smfmac_f32_16x16x64_bf16 v[16:19], v[40:43], v[120:127], v8\n\t"
    "s_setprio 0\n\t"
    "s_sleep 1\n\t"
    "s_nop 6\n\t"
    "v_add_f32 v12, v12, v14\n\t"
    "v_add_f32 v13, v13, v15\n\t"
    "v_add_f32 v14, v16, v18\n\t"
    "v_add_f32 v15, v17, v19\n\t"
    "ds_write_b128 v6, v[12:15] offset:25600\n\t"
    "ds_write_b32 v9, v11 offset:28928\n\t"
    "s_setprio 0\n\t"
    "ds_read_b128 v[44:47], v6 offset:0\n\t"
    "ds_read_b128 v[48:51], v6 offset:1024\n\t"
    "ds_read_b128 v[52:55], v6 offset:2048\n\t"
    "ds_read_b128 v[56:59], v6 offset:3072\n\t"
    "ds_read_b128 v[60:63], v6 offset:4096\n\t"
    "s_sleep 1\n\t"
    "v_smfmac_f32_16x16x64_bf16 v[20:23], v[28:31], v[208:215], v8\n\t"
    "ds_read_b128 v[28:31], v6 offset:5120\n\t"
    "v_smfmac_f32_16x16x64_bf16 v[20:23], v[24:27], v[240:247], v8\n\t"
    "ds_read_b128 v[24:27], v6 offset:6144\n\t"
    "v_smfmac_f32_16x16x64_bf16 v[20:23], v[32:35], v[224:231], v8\n\t"
    "ds_read_b128 v[32:35], v6 offset:7168\n\t"
    "v_smfmac_f32_16x16x64_bf16 v[20:23], v[36:39], v[232:239], v8\n\t"
    "ds_read_b128 v[36:39], v6 offset:8192\n\t"
    "v_smfmac_f32_16x16x64_bf16 v[20:23], v[40:43], v[248:255], v8\n\t"
    "ds_read_b128 v[40:43], v6 offset:10240\n\t"
    "s_waitcnt lgkmcnt(9)\n\t"
    "v_smfmac_f32_16x16x64_bf16 v[20:23], v[44:47], v[128:135], v8\n\t"
    "ds_read_b128 v[44:47], v6 offset:11264\n\t"
    "s_barrier\n\t"
    "s_setprio 0\n\t"
    "s_waitcnt lgkmcnt(9)\n\t"
    "v_smfmac_f32_16x16x64_bf16 v[20:23], v[48:51], v[144:151], v8\n\t"
    "s_waitcnt lgkmcnt(8)\n\t"
    "v_smfmac_f32_16x16x64_bf16 v[20:23], v[52:55], v[136:143], v8\n\t"
    "s_waitcnt lgkmcnt(7)\n\t"
    "v_smfmac_f32_16x16x64_bf16 v[20:23], v[56:59], v[152:159], v8\n\t"
    "s_waitcnt lgkmcnt(6)\n\t"
    "v_smfmac_f32_16x16x64_bf16 v[20:23], v[60:63], v[160:167], v8\n\t"
    "s_waitcnt lgkmcnt(5)\n\t"
    "v_smfmac_f32_16x16x64_bf16 v[20:23], v[28:31], v[176:183], v8\n\t"
    "s_waitcnt lgkmcnt(4)\n\t"
    "v_smfmac_f32_16x16x64_bf16 v[20:23], v[24:27], v[168:175], v8\n\t"
    "s_waitcnt lgkmcnt(3)\n\t"
    "v_smfmac_f32_16x16x64_bf16 v[20:23], v[32:35], v[184:191], v8\n\t"
    "s_waitcnt lgkmcnt(2)\n\t"
    "v_smfmac_f32_16x16x64_bf16 v[20:23], v[36:39], v[192:199], v8\n\t"
    "s_waitcnt lgkmcnt(1)\n\t"
    "v_smfmac_f32_16x16x64_bf16 v[20:23], v[40:43], v[200:207], v8\n\t"
    "s_waitcnt lgkmcnt(0)\n\t"
    "v_smfmac_f32_16x16x64_bf16 v[20:23], v[44:47], v[216:223], v8\n\t"
    "s_nop 7\n\t"
    "v_add_f32 v20, v20, v22\n\t"
    "v_add_f32 v21, v21, v23\n\t"
    "v_cvt_pk_bf16_f32 v20, v20, v21\n\t"
    "v_min_u32 v20, -2, v20\n\t"
    "ds_write_b32 v9, v20 offset:18688\n\t"
    "s_waitcnt lgkmcnt(0)\n\t"
    "s_setprio 0\n\t"
    "s_setprio 0\n\t"
    "s_waitcnt lgkmcnt(0)\n\t"
    "s_branch 9f\n\t"
    "1:\n\t"
    "s_add_u32 %[sp], %[sp], 1\n\t"
    "s_cmp_ge_u32 %[sp], %[cap]\n\t"
    "s_cbranch_scc1 7f\n\t"
    "global_load_dwordx4 v[24:27], v6, %[ag] sc1\n\t"
    "global_load_dwordx4 v[28:31], v6, %[ag] offset:-4096 sc1\n\t"
    "s_waitcnt lgkmcnt(0)\n\t"
    "s_memtime %[tc]\n\t"
    "s_nop 7\n\t"
    "s_branch 3b\n\t"
    "7:\n\t"
    "s_mov_b32 %[cap], 0\n\t"
    "s_branch 2b\n\t"
    "9:"
    : [tc] "=&s"(tc), [sp] "=&s"(sp), [cap] "+s"(cap), [st] "=&s"(st)
    : [ag] "s"(ag), [c44] "s"(0x4444u), [lsc] "s"(LSC), [sn] "s"(sn), "{v[64:95]}"(W.w0), "{v[96:127]}"(W.w1), "{v[128:159]}"(W.w2), "{v[160:191]}"(W.w3), "{v[192:223]}"(W.w4), "{v[224:255]}"(W.w5)
    : "v6", "v7", "v8", "v9", "v10", "v11", "v12", "v13", "v14", "v15", "v16", "v17", "v18", "v19", "v20", "v21", "v22", "v23", "v24", "v25", "v26", "v27", "v28", "v29", "v30", "v31", "v32", "v33", "v34", "v35", "v36", "v37", "v38", "v39", "v40", "v41", "v42", "v43", "v44", "v45", "v46", "v47", "v48", "v49", "v50", "v51", "v52", "v53", "v54", "v55", "v56", "v57", "v58", "v59", "v60", "v61", "v62", "v63", "vcc", "scc", "m0", "memory");
}
template <> __device__ __forceinline__ void qk_step<6>(const QKW& W, const char* ag, const char* vs, const char* v2, const char* db, const char* od, unsigned pb, unsigned sn, unsigned& cap, unsigned& sp,
    unsigned long long& tc, unsigned long long* ts, unsigned& d0, unsigned& d1) {
  unsigned st, bg;
  asm volatile("v_mbcnt_lo_u32_b32 v60, -1, 0\n\t"
    "v_mbcnt_hi_u32_b32 v60, -1, v60\n\t"
    "v_lshlrev_b32 v6, 4, v60\n\t"
    "s_mov_b32 %[sp], 0\n\t"
    "s_setprio 3\n\t"
    "global_load_dwordx4 v[24:27], v6, %[ag] sc1\n\t"
    "global_load_dwordx4 v[28:31], v6, %[ag] offset:-4096 sc1\n\t"
    "s_memtime %[tc]\n\t"
    "v_lshrrev_b32 v56, 4, v60\n\t"
    "v_and_b32 v57, 15, v60\n\t"
    "v_mov_b32 v20, 0\n\t"
    "v_mov_b32 v21, 0\n\t"
    "v_mov_b32 v22, 0\n\t"
    "v_mov_b32 v23, 0\n\t"
    "v_mov_b32 v11, %[sn]\n\t"
    "v_mov_b32 v10, -1\n\t"
    "v_and_b32 v61, 2, v60\n\t"
    "v_mul_u32_u24 v61, 0x5555, v61\n\t"
    "v_or_b32 v8, %[c44], v61\n\t"
    "v_lshlrev_b32 v61, 2, v56\n\t"
    "v_lshl_or_b32 v9, v57, 4, v61\n\t"
    "s_setprio 0\n\t"
    "s_setprio 3\n\t"
    "3:\n\t"
    "v_mov_b32 v12, 0\n\t"
    "v_mov_b32 v13, 0\n\t"
    "v_mov_b32 v14, 0\n\t"
    "v_mov_b32 v15, 0\n\t"
    "v_mov_b32 v16, 0\n\t"
    "v_mov_b32 v17, 0\n\t"
    "v_mov_b32 v18, 0\n\t"
    "v_mov_b32 v19, 0\n\t"
    "s_waitcnt vmcnt(0) lgkmcnt(0)\n\t"
    "33:\n\t"
    "ds_write_b128 v6, v[24:27] offset:14336\n\t"
    "ds_write_b128 v6, v[28:31] offset:10240\n\t"
    "v_smfmac_f32_16x16x64_bf16 v[12:15], v[24:27], v[96:103], v8\n\t"
    "v_max3_u32 v60, v24, v25, v26\n\t"
    "v_max3_u32 v61, v27, v28, v29\n\t"
    "v_smfmac_f32_16x16x64_bf16 v[16:19], v[24:27], v[104:111], v8\n\t"
    "v_max3_u32 v60, v60, v30, v31\n\t"
    "v_max_u32 v60, v60, v61\n\t"
    "v_cmp_eq_u32 vcc, -1, v60\n\t"
    "s_cbranch_vccnz 1f\n\t"
    "2:\n\t"
    "s_waitcnt lgkmcnt(0)\n\t"
    "s_barrier\n\t"
    "ds_read_b128 v[32:35], v6 offset:12288\n\t"
    "ds_read_b128 v[36:39], v6 offset:13312\n\t"
    "ds_read_b128 v[40:43], v6 offset:15360\n\t"
    "ds_write_b32 v9, v10 offset:18944\n\t"
    "s_waitcnt lgkmcnt(3)\n\t"
    "v_smfmac_f32_16x16x64_bf16 v[12:15], v[32:35], v[64:71], v8\n\t"
    "v_smfmac_f32_16x16x64_bf16 v[16:19], v[32:35], v[72:79], v8\n\t"
    "s_waitcnt lgkmcnt(2)\n\t"
    "v_smfmac_f32_16x16x64_bf16 v[12:15], v[36:39], v[80:87], v8\n\t"
    "v_smfmac_f32_16x16x64_bf16 v[16:19], v[36:39], v[88:95], v8\n\t"
    "s_waitcnt lgkmcnt(1)\n\t"
    "v_smfmac_f32_16x16x64_bf16 v[12:15], v[40:43], v[112:119], v8\n\t"
    "v_smfmac_f32_16x16x64_bf16 v[16:19], v[40:43], v[120:127], v8\n\t"
    "s_setprio 0\n\t"
    "s_sleep 1\n\t"
    "s_nop 6\n\t"
    "v_add_f32 v12, v12, v14\n\t"
    "v_add_f32 v13, v13, v15\n\t"
    "v_add_f32 v14, v16, v18\n\t"
    "v_add_f32 v15, v17, v19\n\t"
    "ds_write_b128 v6, v[12:15] offset:26624\n\t"
    "ds_write_b32 v9, v11 offset:29184\n\t"
    "s_setprio 0\n\t"
    "ds_read_b128 v[44:47], v6 offset:0\n\t"
    "ds_read_b128 v[48:51], v6 offset:1024\n\t"
    "ds_read_b128 v[52:55], v6 offset:2048\n\t"
    "ds_read_b128 v[56:59], v6 offset:3072\n\t"
    "ds_read_b128 v[60:63], v6 offset:4096\n\t"
    "s_sleep 1\n\t"
    "v_smfmac_f32_16x16x64_bf16 v[20:23], v[28:31], v[200:207], v8\n\t"
    "ds_read_b128 v[28:31], v6 offset:5120\n\t"
    "v_smfmac_f32_16x16x64_bf16 v[20:23], v[24:27], v[232:239], v8\n\t"
    "ds_read_b128 v[24:27], v6 offset:6144\n\t"
    "v_smfmac_f32_16x16x64_bf16 v[20:23], v[32:35], v[224:231], v8\n\t"
    "ds_read_b128 v[32:35], v6 offset:7168\n\t"
    "v_smfmac_f32_16x16x64_bf16 v[20:23], v[36:39], v[240:247], v8\n\t"
    "ds_read_b128 v[36:39], v6 offset:8192\n\t"
    "v_smfmac_f32_16x16x64_bf16 v[20:23], v[40:43], v[248:255], v8\n\t"
    "ds_read_b128 v[40:43], v6 offset:9216\n\t"
    "s_waitcnt lgkmcnt(9)\n\t"
    "v_smfmac_f32_16x16x64_bf16 v[20:23], v[44:47], v[128:135], v8\n\t"
    "ds_read_b128 v[44:47], v6 offset:11264\n\t"
    "s_barrier\n\t"
    "s_setprio 0\n\t"
    "s_waitcnt lgkmcnt(9)\n\t"
    "v_smfmac_f32_16x16x64_bf16 v[20:23], v[48:51], v[144:151], v8\n\t"
    "s_waitcnt lgkmcnt(8)\n\t"
    "v_smfmac_f32_16x16x64_bf16 v[20:23], v[52:55], v[136:143], v8\n\t"
    "s_waitcnt lgkmcnt(7)\n\t"
    "v_smfmac_f32_16x16x64_bf16 v[20:23], v[56:59], v[152:159], v8\n\t"
    "s_waitcnt lgkmcnt(6)\n\t"
    "v_smfmac_f32_16x16x64_bf16 v[20:23], v[60:63], v[160:167], v8\n\t"
    "s_waitcnt lgkmcnt(5)\n\t"
    "v_smfmac_f32_16x16x64_bf16 v[20:23], v[28:31], v[176:183], v8\n\t"
    "s_waitcnt lgkmcnt(4)\n\t"
    "v_smfmac_f32_16x16x64_bf16 v[20:23], v[24:27], v[168:175], v8\n\t"
    "s_waitcnt lgkmcnt(3)\n\t"
    "v_smfmac_f32_16x16x64_bf16 v[20:23], v[32:35], v[184:191], v8\n\t"
    "s_waitcnt lgkmcnt(2)\n\t"
    "v_smfmac_f32_16x16x64_bf16 v[20:23], v[36:39], v[192:199], v8\n\t"
    "s_waitcnt lgkmcnt(1)\n\t"
    "v_smfmac_f32_16x16x64_bf16 v[20:23], v[40:43], v[208:215], v8\n\t"
    "s_waitcnt lgkmcnt(0)\n\t"
    "v_smfmac_f32_16x16x64_bf16 v[20:23], v[44:47], v[216:223], v8\n\t"
    "s_nop 7\n\t"
    "v_add_f32 v20, v20, v22\n\t"
    "v_add_f32 v21, v21, v23\n\t"
    "v_cvt_pk_bf16_f32 v20, v20, v21\n\t"
    "v_min_u32 v20, -2, v20\n\t"
    "ds_write_b32 v9, v20 offset:18944\n\t"
    "s_waitcnt lgkmcnt(0)\n\t"
    "s_setprio 0\n\t"
    "s_setprio 0\n\t"
    "s_waitcnt lgkmcnt(0)\n\t"
    "s_branch 9f\n\t"
    "1:\n\t"
    "s_add_u32 %[sp], %[sp], 1\n\t"
    "s_cmp_ge_u32 %[sp], %[cap]\n\t"
    "s_cbranch_scc1 7f\n\t"
    "global_load_dwordx4 v[24:27], v6, %[ag] sc1\n\t"
    "global_load_dwordx4 v[28:31], v6, %[ag] offset:-4096 sc1\n\t"
    "s_waitcnt lgkmcnt(0)\n\t"
    "s_memtime %[tc]\n\t"
    "s_nop 7\n\t"
    "s_branch 3b\n\t"
    "7:\n\t"
    "s_mov_b32 %[cap], 0\n\t"
    "s_branch 2b\n\t"
    "9:"
    : [tc] "=&s"(tc), [sp] "=&s"(sp), [cap] "+s"(cap), [st] "=&s"(st)
    : [ag] "s"(ag), [c44] "s"(0x4444u), [lsc] "s"(LSC), [sn] "s"(sn), "{v[64:95]}"(W.w0), "{v[96:127]}"(W.w1), "{v[128:159]}"(W.w2), "{v[160:191]}"(W.w3), "{v[192:223]}"(W.w4), "{v[224:255]}"(W.w5)
    : "v6", "v7", "v8", "v9", "v10", "v11", "v12", "v13", "v14", "v15", "v16", "v17", "v18", "v19", "v20", "v21", "v22", "v23", "v24", "v25", "v26", "v27", "v28", "v29", "v30", "v31", "v32", "v33", "v34", "v35", "v36", "v37", "v38", "v39", "v40", "v41", "v42", "v43", "v44", "v45", "v46", "v47", "v48", "v49", "v50", "v51", "v52", "v53", "v54", "v55", "v56", "v57", "v58", "v59", "v60", "v61", "v62", "v63", "vcc", "scc", "m0", "memory");
}
template <> __device__ __forceinline__ void qk_step<7>(const QKW& W, const char* ag, const char* vs, const char* v2, const char* db, const char* od, unsigned pb, unsigned sn, unsigned& cap, unsigned& sp,
    unsigned long long& tc, unsigned long long* ts, unsigned& d0, unsigned& d1) {
  unsigned st, bg;
  asm volatile("v_mbcnt_lo_u32_b32 v60, -1, 0\n\t"
    "v_mbcnt_hi_u32_b32 v60, -1, v60\n\t"
    "v_lshlrev_b32 v6, 4, v60\n\t"
    "s_mov_b32 %[sp], 0\n\t"
    "s_setprio 3\n\t"
    "global_load_dwordx4 v[24:27], v6, %[ag] sc1\n\t"
    "global_load_dwordx4 v[28:31], v6, %[ag] offset:-4096 sc1\n\t"
    "s_memtime %[tc]\n\t"
    "v_lshrrev_b32 v56, 4, v60\n\t"
    "v_and_b32 v57, 15, v60\n\t"
    "v_mov_b32 v20, 0\n\t"
    "v_mov_b32 v21, 0\n\t"
    "v_mov_b32 v22, 0\n\t"
    "v_mov_b32 v23, 0\n\t"
    "v_mov_b32 v11, %[sn]\n\t"
    "v_mov_b32 v10, -1\n\t"
    "v_and_b32 v61, 2, v60\n\t"
    "v_mul_u32_u24 v61, 0x5555, v61\n\t"
    "v_or_b32 v8, %[c44], v61\n\t"
    "v_lshlrev_b32 v61, 2, v56\n\t"
    "v_lshl_or_b32 v9, v57, 4, v61\n\t"
    "s_setprio 0\n\t"
    "s_setprio 3\n\t"
    "3:\n\t"
    "v_mov_b32 v12, 0\n\t"
    "v_mov_b32 v13, 0\n\t"
    "v_mov_b32 v14, 0\n\t"
    "v_mov_b32 v15, 0\n\t"
    "v_mov_b32 v16, 0\n\t"
    "v_mov_b32 v17, 0\n\t"
    "v_mov_b32 v18, 0\n\t"
    "v_mov_b32 v19, 0\n\t"
    "s_waitcnt vmcnt(0) lgkmcnt(0)\n\t"
    "33:\n\t"
    "ds_write_b128 v6, v[24:27] offset:15360\n\t"
    "ds_write_b128 v6, v[28:31] offset:11264\n\t"
    "v_smfmac_f32_16x16x64_bf16 v[12:15], v[24:27], v[112:119], v8\n\t"
    "v_max3_u32 v60, v24, v25, v26\n\t"
    "v_max3_u32 v61, v27, v28, v29\n\t"
    "v_smfmac_f32_16x16x64_bf16 v[16:19], v[24:27], v[120:127], v8\n\t"
    "v_max3_u32 v60, v60, v30, v31\n\t"
    "v_max_u32 v60, v60, v61\n\t"
    "v_cmp_eq_u32 vcc, -1, v60\n\t"
    "s_cbranch_vccnz 1f\n\t"
    "2:\n\t"
    "s_waitcnt lgkmcnt(0)\n\t"
    "s_barrier\n\t"
    "ds_read_b128 v[32:35], v6 offset:12288\n\t"
    "ds_read_b128 v[36:39], v6 offset:13312\n\t"
    "ds_read_b128 v[40:43], v6 offset:14336\n\t"
    "ds_write_b32 v9, v10 offset:19200\n\t"
    "s_waitcnt lgkmcnt(3)\n\t"
    "v_smfmac_f32_16x16x64_bf16 v[12:15], v[32:35], v[64:71], v8\n\t"
    "v_smfmac_f32_16x16x64_bf16 v[16:19], v[32:35], v[72:79], v8\n\t"
    "s_waitcnt lgkmcnt(2)\n\t"
    "v_smfmac_f32_16x16x64_bf16 v[12:15], v[36:39], v[80:87], v8\n\t"
    "v_smfmac_f32_16x16x64_bf16 v[16:19], v[36:39], v[88:95], v8\n\t"
    "s_waitcnt lgkmcnt(1)\n\t"
    "v_smfmac_f32_16x16x64_bf16 v[12:15], v[40:43], v[96:103], v8\n\t"
    "v_smfmac_f32_16x16x64_bf16 v[16:19], v[40:43], v[104:111], v8\n\t"
    "s_setprio 0\n\t"
    "s_sleep 1\n\t"
    "s_nop 6\n\t"
    "v_add_f32 v12, v12, v14\n\t"
    "v_add_f32 v13, v13, v15\n\t"
    "v_add_f32 v14, v16, v18\n\t"
    "v_add_f32 v15, v17, v19\n\t"
    "ds_write_b128 v6, v[12:15] offset:27648\n\t"
    "ds_write_b32 v9, v11 offset:29440\n\t"
    "s_setprio 0\n\t"
    "v_lshrrev_b32 v10, 24, v11\n\t"
    "v_add_u32 v10, -1, v10\n\t"
    "s_mov_b64 exec, 1\n\t"
    "global_store_dword v6, v10, %[vs] sc1\n\t"
    "s_mov_b64 exec, -1\n\t"
    "ds_read_b128 v[44:47], v6 offset:0\n\t"
    "ds_read_b128 v[48:51], v6 offset:1024\n\t"
    "ds_read_b128 v[52:55], v6 offset:2048\n\t"
    "ds_read_b128 v[56:59], v6 offset:3072\n\t"
    "ds_read_b128 v[60:63], v6 offset:4096\n\t"
    "s_sleep 1\n\t"
    "v_smfmac_f32_16x16x64_bf16 v[20:23], v[28:31], v[216:223], v8\n\t"
    "ds_read_b128 v[28:31], v6 offset:5120\n\t"
    "v_smfmac_f32_16x16x64_bf16 v[20:23], v[24:27], v[248:255], v8\n\t"
    "ds_read_b128 v[24:27], v6 offset:6144\n\t"
    "v_smfmac_f32_16x16x64_bf16 v[20:23], v[32:35], v[224:231], v8\n\t"
    "ds_read_b128 v[32:35], v6 offset:7168\n\t"
    "v_smfmac_f32_16x16x64_bf16 v[20:23], v[36:39], v[240:247], v8\n\t"
    "ds_read_b128 v[36:39], v6 offset:8192\n\t"
    "v_smfmac_f32_16x16x64_bf16 v[20:23], v[40:43], v[232:239], v8\n\t"
    "ds_read_b128 v[40:43], v6 offset:9216\n\t"
    "s_waitcnt lgkmcnt(9)\n\t"
    "v_smfmac_f32_16x16x64_bf16 v[20:23], v[44:47], v[128:135], v8\n\t"
    "ds_read_b128 v[44:47], v6 offset:10240\n\t"
    "s_barrier\n\t"
    "s_setprio 0\n\t"
    "s_waitcnt lgkmcnt(9)\n\t"
    "v_smfmac_f32_16x16x64_bf16 v[20:23], v[48:51], v[144:151], v8\n\t"
    "s_waitcnt lgkmcnt(8)\n\t"
    "v_smfmac_f32_16x16x64_bf16 v[20:23], v[52:55], v[136:143], v8\n\t"
    "s_waitcnt lgkmcnt(7)\n\t"
    "v_smfmac_f32_16x16x64_bf16 v[20:23], v[56:59], v[152:159], v8\n\t"
    "s_waitcnt lgkmcnt(6)\n\t"
    "v_smfmac_f32_16x16x64_bf16 v[20:23], v[60:63], v[160:167], v8\n\t"
    "s_waitcnt lgkmcnt(5)\n\t"
    "v_smfmac_f32_16x16x64_bf16 v[20:23], v[28:31], v[176:183], v8\n\t"
    "s_waitcnt lgkmcnt(4)\n\t"
    "v_smfmac_f32_16x16x64_bf16 v[20:23], v[24:27], v[168:175], v8\n\t"
    "s_waitcnt lgkmcnt(3)\n\t"
    "v_smfmac_f32_16x16x64_bf16 v[20:23], v[32:35], v[184:191], v8\n\t"
    "s_waitcnt lgkmcnt(2)\n\t"
    "v_smfmac_f32_16x16x64_bf16 v[20:23], v[36:39], v[192:199], v8\n\t"
    "s_waitcnt lgkmcnt(1)\n\t"
    "v_smfmac_f32_16x16x64_bf16 v[20:23], v[40:43], v[208:215], v8\n\t"
    "s_waitcnt lgkmcnt(0)\n\t"
    "v_smfmac_f32_16x16x64_bf16 v[20:23], v[44:47], v[200:207], v8\n\t"
    "s_nop 7\n\t"
    "v_add_f32 v20, v20, v22\n\t"
    "v_add_f32 v21, v21, v23\n\t"
    "v_cvt_pk_bf16_f32 v20, v20, v21\n\t"
    "v_min_u32 v20, -2, v20\n\t"
    "ds_write_b32 v9, v20 offset:19200\n\t"
    "s_waitcnt lgkmcnt(0)\n\t"
    "s_setprio 0\n\t"
    "s_waitcnt lgkmcnt(0)\n\t"
    "s_branch 9f\n\t"
    "1:\n\t"
    "s_add_u32 %[sp], %[sp], 1\n\t"
    "s_cmp_ge_u32 %[sp], %[cap]\n\t"
    "s_cbranch_scc1 7f\n\t"
    "global_load_dwordx4 v[24:27], v6, %[ag] sc1\n\t"
    "global_load_dwordx4 v[28:31], v6, %[ag] offset:-4096 sc1\n\t"
    "s_waitcnt lgkmcnt(0)\n\t"
    "s_memtime %[tc]\n\t"
    "s_nop 7\n\t"
    "s_branch 3b\n\t"
    "7:\n\t"
    "s_mov_b32 %[cap], 0\n\t"
    "s_branch 2b\n\t"
    "9:"
    : [tc] "=&s"(tc), [sp] "=&s"(sp), [cap] "+s"(cap), [st] "=&s"(st)
    : [ag] "s"(ag), [c44] "s"(0x4444u), [lsc] "s"(LSC), [sn] "s"(sn), [vs] "s"(vs), "{v[64:95]}"(W.w0), "{v[96:127]}"(W.w1), "{v[128:159]}"(W.w2), "{v[160:191]}"(W.w3), "{v[192:223]}"(W.w4), "{v[224:255]}"(W.w5)
    : "v6", "v7", "v8", "v9", "v10", "v11", "v12", "v13", "v14", "v15", "v16", "v17", "v18", "v19", "v20", "v21", "v22", "v23", "v24", "v25", "v26", "v27", "v28", "v29", "v30", "v31", "v32", "v33", "v34", "v35", "v36", "v37", "v38", "v39", "v40", "v41", "v42", "v43", "v44", "v45", "v46", "v47", "v48", "v49", "v50", "v51", "v52", "v53", "v54", "v55", "v56", "v57", "v58", "v59", "v60", "v61", "v62", "v63", "vcc", "scc", "m0", "memory");
}
template <int W_> __device__ __forceinline__ void seg_attn(const QKW& W, unsigned pb, const char* od, unsigned long long* ts) { }
template <> __device__ __forceinline__ void seg_attn<0>(const QKW& W, unsigned pb, const char* od, unsigned long long* ts) {
  asm volatile("v_mbcnt_lo_u32_b32 v32, -1, 0\n\t"
    "v_mbcnt_hi_u32_b32 v32, -1, v32\n\t"
    "v_lshlrev_b32 v35, 4, v32\n\t"
    "ds_read_b128 v[24:27], v35 offset:20480\n\t"
    "ds_read_b128 v[28:31], v35 offset:21504\n\t"
    "v_lshrrev_b32 v33, 4, v32\n\t"
    "v_and_b32 v34, 15, v32\n\t"
    "v_lshrrev_b32 v4, 2, v34\n\t"
    "v_and_b32 v4, 2, v4\n\t"
    "v_xor_b32 v4, v33, v4\n\t"
    "v_lshl_or_b32 v4, v4, 4, %[pb]\n\t"
    "v_lshl_or_b32 v4, v34, 6, v4\n\t"
    "ds_read_b128 v[8:11], v4 offset:49152\n\t"
    "ds_read_b128 v[12:15], v4 offset:51200\n\t"
    "ds_read_b128 v[16:19], v4 offset:53248\n\t"
    "ds_read_b128 v[20:23], v4 offset:55296\n\t"
    "ds_read_b128 v[40:43], v4 offset:32768\n\t"
    "ds_read_b128 v[44:47], v4 offset:34816\n\t"
    "ds_read_b128 v[48:51], v4 offset:36864\n\t"
    "ds_read_b128 v[52:55], v4 offset:38912\n\t"
    "v_lshlrev_b32 v35, 2, v33\n\t"
    "v_sub_u32 v35, v34, v35\n\t"
    "v_ashrrev_i32 v60, 31, v35\n\t"
    "v_and_b32 v60, %[neg], v60\n\t"
    "v_add_u32 v61, -1, v35\n\t"
    "v_ashrrev_i32 v61, 31, v61\n\t"
    "v_and_b32 v61, %[neg], v61\n\t"
    "v_add_u32 v62, -2, v35\n\t"
    "v_ashrrev_i32 v62, 31, v62\n\t"
    "v_and_b32 v62, %[neg], v62\n\t"
    "v_add_u32 v63, -3, v35\n\t"
    "v_ashrrev_i32 v63, 31, v63\n\t"
    "v_and_b32 v63, %[neg], v63\n\t"
    "s_waitcnt lgkmcnt(6)\n\t"
    "v_mfma_f32_16x16x32_bf16 v[32:35], v[24:27], v[8:11], 0\n\t"
    "v_mfma_f32_16x16x32_bf16 v[36:39], v[24:27], v[12:15], 0\n\t"
    "s_waitcnt lgkmcnt(4)\n\t"
    "v_mfma_f32_16x16x32_bf16 v[32:35], v[28:31], v[16:19], v[32:35]\n\t"
    "v_mfma_f32_16x16x32_bf16 v[36:39], v[28:31], v[20:23], v[36:39]\n\t"
    "s_waitcnt lgkmcnt(3)\n\t"
    "v_mfma_f32_16x16x32_bf16 v[60:63], v[40:43], v[8:11], v[60:63]\n\t"
    "s_waitcnt lgkmcnt(2)\n\t"
    "v_mfma_f32_16x16x32_bf16 v[60:63], v[44:47], v[12:15], v[60:63]\n\t"
    "v_mbcnt_lo_u32_b32 v7, -1, 0\n\t"
    "v_mbcnt_hi_u32_b32 v7, -1, v7\n\t"
    "s_waitcnt lgkmcnt(1)\n\t"
    "v_mfma_f32_16x16x32_bf16 v[60:63], v[48:51], v[16:19], v[60:63]\n\t"
    "v_lshrrev_b32 v5, 4, v7\n\t"
    "v_and_b32 v5, 2, v5\n\t"
    "s_waitcnt lgkmcnt(0)\n\t"
    "v_mfma_f32_16x16x32_bf16 v[60:63], v[52:55], v[20:23], v[60:63]\n\t"
    "v_xor_b32 v5, v5, v7\n\t"
    "v_lshl_or_b32 v5, v5, 4, %[pb]\n\t"
    "ds_read_b64_tr_b16 v[48:49], v5 offset:40960\n\t"
    "ds_read_b64_tr_b16 v[52:53], v5 offset:40968\n\t"
    "v_add_f32 v32, v32, v38\n\t"
    "v_add_f32 v33, v33, v39\n\t"
    "v_and_b32 v6, 12, v7\n\t"
    "v_and_b32 v4, 3, v7\n\t"
    "v_lshl_add_u32 v4, v6, 1, v4\n\t"
    "v_lshrrev_b32 v6, 4, v7\n\t"
    "v_lshl_add_u32 v4, v4, 2, v6\n\t"
    "v_lshlrev_b32 v4, 2, v4\n\t"
    "v_add_u32 v4, 18432, v4\n\t"
    "ds_read_b64_tr_b16 v[40:41], v5 offset:43008\n\t"
    "ds_read_b64_tr_b16 v[44:45], v5 offset:43016\n\t"
    "ds_read2_b32 v[36:37], v4 offset0:0 offset1:16\n\t"
    "ds_read2_b32 v[38:39], v4 offset0:128 offset1:144\n\t"
    "v_and_b32 v4, 7, v7\n\t"
    "v_xor_b32 v6, v6, v4\n\t"
    "v_max3_f32 v34, v32, v33, %[lo]\n\t"
    "v_max3_f32 v34, v34, v60, v61\n\t"
    "v_max3_f32 v34, v34, v62, v63\n\t"
    "v_mov_b32 v35, v34\n\t"
    "v_lshlrev_b32 v6, 4, v6\n\t"
    "v_and_b32 v4, 15, v7\n\t"
    "v_permlane16_swap_b32 v34, v35\n\t"
    "v_max_f32 v34, v34, v35\n\t"
    "v_mov_b32 v35, v34\n\t"
    "v_lshl_or_b32 v6, v4, 7, v6\n\t"
    "v_xor_b32 v4, 64, v6\n\t"
    "v_permlane32_swap_b32 v34, v35\n\t"
    "v_max_f32 v34, v34, v35\n\t"
    "v_mul_f32 v34, %[nl], v34\n\t"
    "v_fma_f32 v32, v32, %[lsc], v34\n\t"
    "v_fma_f32 v33, v33, %[lsc], v34\n\t"
    "v_fma_f32 v60, v60, %[lsc], v34\n\t"
    "v_fma_f32 v61, v61, %[lsc], v34\n\t"
    "v_fma_f32 v62, v62, %[lsc], v34\n\t"
    "v_fma_f32 v63, v63, %[lsc], v34\n\t"
    "v_exp_f32 v60, v60\n\t"
    "v_exp_f32 v61, v61\n\t"
    "v_exp_f32 v62, v62\n\t"
    "v_exp_f32 v63, v63\n\t"
    "v_exp_f32 v32, v32\n\t"
    "v_exp_f32 v33, v33\n\t"
    "v_cvt_pk_bf16_f32 v24, v60, v61\n\t"
    "v_cvt_pk_bf16_f32 v25, v62, v63\n\t"
    "v_cvt_pk_bf16_f32 v28, v32, v33\n\t"
    "s_waitcnt lgkmcnt(0)\n\t"
    "v_mfma_f32_16x16x16_bf16 v[8:11], v[48:49], v[24:25], 0\n\t"
    "v_mov_b32 v56, %[one]\n\t"
    "v_mov_b32 v57, %[one]\n\t"
    "v_mfma_f32_16x16x16_bf16 v[12:15], v[52:53], v[24:25], 0\n\t"
    "v_mov_b32 v58, %[one]\n\t"
    "v_mov_b32 v59, %[one]\n\t"
    "v_mfma_f32_16x16x16_bf16 v[16:19], v[40:41], v[24:25], 0\n\t"
    "v_mov_b32 v29, 0\n\t"
    "v_mov_b32 v30, 0\n\t"
    "v_mfma_f32_16x16x16_bf16 v[60:63], v[56:57], v[24:25], 0\n\t"
    "v_mov_b32 v31, v28\n\t"
    "v_mfma_f32_16x16x16_bf16 v[20:23], v[44:45], v[24:25], 0\n\t"
    "v_mfma_f32_16x16x16_bf16 v[8:11], v[36:37], v[28:29], v[8:11]\n\t"
    "v_mfma_f32_16x16x16_bf16 v[12:15], v[36:37], v[30:31], v[12:15]\n\t"
    "s_nop 1\n\t"
    "v_mfma_f32_16x16x16_bf16 v[16:19], v[38:39], v[28:29], v[16:19]\n\t"
    "v_mfma_f32_16x16x16_bf16 v[60:63], v[56:57], v[28:29], v[60:63]\n\t"
    "s_nop 1\n\t"
    "v_mfma_f32_16x16x16_bf16 v[20:23], v[38:39], v[30:31], v[20:23]\n\t"
    "s_nop 4\n\t"
    "v_rcp_f32 v60, v60\n\t"
    "s_nop 0\n\t"
    "v_pk_mul_f32 v[8:9], v[8:9], v[60:61] op_sel_hi:[1,0]\n\t"
    "v_pk_mul_f32 v[10:11], v[10:11], v[60:61] op_sel_hi:[1,0]\n\t"
    "v_pk_mul_f32 v[12:13], v[12:13], v[60:61] op_sel_hi:[1,0]\n\t"
    "v_pk_mul_f32 v[14:15], v[14:15], v[60:61] op_sel_hi:[1,0]\n\t"
    "v_cvt_pk_bf16_f32 v24, v8, v9\n\t"
    "v_cvt_pk_bf16_f32 v25, v10, v11\n\t"
    "v_cvt_pk_bf16_f32 v26, v12, v13\n\t"
    "v_cvt_pk_bf16_f32 v27, v14, v15\n\t"
    "global_store_dwordx4 v6, v[24:27], %[od] offset:0 sc1\n\t"
    "v_pk_mul_f32 v[16:17], v[16:17], v[60:61] op_sel_hi:[1,0]\n\t"
    "v_pk_mul_f32 v[18:19], v[18:19], v[60:61] op_sel_hi:[1,0]\n\t"
    "v_pk_mul_f32 v[20:21], v[20:21], v[60:61] op_sel_hi:[1,0]\n\t"
    "v_pk_mul_f32 v[22:23], v[22:23], v[60:61] op_sel_hi:[1,0]\n\t"
    "v_cvt_pk_bf16_f32 v28, v16, v17\n\t"
    "v_cvt_pk_bf16_f32 v29, v18, v19\n\t"
    "v_cvt_pk_bf16_f32 v30, v20, v21\n\t"
    "v_cvt_pk_bf16_f32 v31, v22, v23\n\t"
    "global_store_dwordx4 v4, v[28:31], %[od] offset:0 sc1\n\t"
    "s_waitcnt lgkmcnt(0)\n\t"
    "s_nop 0"
    :
    : [pb] "s"(pb), [od] "s"(od), [neg] "s"(-3.0e38f), [lo] "s"(-1.0e38f), [one] "s"(0x3F803F80u), [lsc] "s"(LSC), [nl] "s"(-LSC), "{v[64:95]}"(W.w0), "{v[96:127]}"(W.w1), "{v[128:159]}"(W.w2), "{v[160:191]}"(W.w3), "{v[192:223]}"(W.w4), "{v[224:255]}"(W.w5)
    : "v4", "v5", "v6", "v7", "v8", "v9", "v10", "v11", "v12", "v13", "v14", "v15", "v16", "v17", "v18", "v19", "v20", "v21", "v22", "v23", "v24", "v25", "v26", "v27", "v28", "v29", "v30", "v31", "v32", "v33", "v34", "v35", "v36", "v37", "v38", "v39", "v40", "v41", "v42", "v43", "v44", "v45", "v46", "v47", "v48", "v49", "v50", "v51", "v52", "v53", "v54", "v55", "v56", "v57", "v58", "v59", "v60", "v61", "v62", "v63", "memory");
}
template <> __device__ __forceinline__ void seg_attn<1>(const QKW& W, unsigned pb, const char* od, unsigned long long* ts) {
  asm volatile("v_mbcnt_lo_u32_b32 v32, -1, 0\n\t"
    "v_mbcnt_hi_u32_b32 v32, -1, v32\n\t"
    "v_lshlrev_b32 v35, 4, v32\n\t"
    "ds_read_b128 v[24:27], v35 offset:20480\n\t"
    "ds_read_b128 v[28:31], v35 offset:21504\n\t"
    "v_lshrrev_b32 v33, 4, v32\n\t"
    "v_and_b32 v34, 15, v32\n\t"
    "v_lshrrev_b32 v4, 2, v34\n\t"
    "v_and_b32 v4, 2, v4\n\t"
    "v_xor_b32 v4, v33, v4\n\t"
    "v_lshl_or_b32 v4, v4, 4, %[pb]\n\t"
    "v_lshl_or_b32 v4, v34, 6, v4\n\t"
    "ds_read_b128 v[8:11], v4 offset:50176\n\t"
    "ds_read_b128 v[12:15], v4 offset:52224\n\t"
    "ds_read_b128 v[16:19], v4 offset:54272\n\t"
    "ds_read_b128 v[20:23], v4 offset:56320\n\t"
    "ds_read_b128 v[40:43], v4 offset:32768\n\t"
    "ds_read_b128 v[44:47], v4 offset:34816\n\t"
    "ds_read_b128 v[48:51], v4 offset:36864\n\t"
    "ds_read_b128 v[52:55], v4 offset:38912\n\t"
    "v_lshlrev_b32 v35, 2, v33\n\t"
    "v_sub_u32 v35, v34, v35\n\t"
    "v_ashrrev_i32 v60, 31, v35\n\t"
    "v_and_b32 v60, %[neg], v60\n\t"
    "v_add_u32 v61, -1, v35\n\t"
    "v_ashrrev_i32 v61, 31, v61\n\t"
    "v_and_b32 v61, %[neg], v61\n\t"
    "v_add_u32 v62, -2, v35\n\t"
    "v_ashrrev_i32 v62, 31, v62\n\t"
    "v_and_b32 v62, %[neg], v62\n\t"
    "v_add_u32 v63, -3, v35\n\t"
    "v_ashrrev_i32 v63, 31, v63\n\t"
    "v_and_b32 v63, %[neg], v63\n\t"
    "s_waitcnt lgkmcnt(6)\n\t"
    "v_mfma_f32_16x16x32_bf16 v[32:35], v[24:27], v[8:11], 0\n\t"
    "v_mfma_f32_16x16x32_bf16 v[36:39], v[24:27], v[12:15], 0\n\t"
    "s_waitcnt lgkmcnt(4)\n\t"
    "v_mfma_f32_16x16x32_bf16 v[32:35], v[28:31], v[16:19], v[32:35]\n\t"
    "v_mfma_f32_16x16x32_bf16 v[36:39], v[28:31], v[20:23], v[36:39]\n\t"
    "ds_read_b128 v[24:27], v4 offset:33792\n\t"
    "ds_read_b128 v[28:31], v4 offset:35840\n\t"
    "s_waitcnt lgkmcnt(5)\n\t"
    "v_mfma_f32_16x16x32_bf16 v[56:59], v[40:43], v[8:11], 0\n\t"
    "s_waitcnt lgkmcnt(4)\n\t"
    "v_mfma_f32_16x16x32_bf16 v[56:59], v[44:47], v[12:15], v[56:59]\n\t"
    "ds_read_b128 v[40:43], v4 offset:37888\n\t"
    "ds_read_b128 v[44:47], v4 offset:39936\n\t"
    "v_mbcnt_lo_u32_b32 v7, -1, 0\n\t"
    "v_mbcnt_hi_u32_b32 v7, -1, v7\n\t"
    "s_waitcnt lgkmcnt(5)\n\t"
    "v_mfma_f32_16x16x32_bf16 v[56:59], v[48:51], v[16:19], v[56:59]\n\t"
    "v_lshrrev_b32 v5, 4, v7\n\t"
    "v_and_b32 v5, 2, v5\n\t"
    "s_waitcnt lgkmcnt(4)\n\t"
    "v_mfma_f32_16x16x32_bf16 v[56:59], v[52:55], v[20:23], v[56:59]\n\t"
    "v_xor_b32 v5, v5, v7\n\t"
    "v_lshl_or_b32 v5, v5, 4, %[pb]\n\t"
    "ds_read_b64_tr_b16 v[48:49], v5 offset:40960\n\t"
    "ds_read_b64_tr_b16 v[50:51], v5 offset:41984\n\t"
    "ds_read_b64_tr_b16 v[52:53], v5 offset:40968\n\t"
    "ds_read_b64_tr_b16 v[54:55], v5 offset:41992\n\t"
    "v_add_f32 v32, v32, v38\n\t"
    "v_add_f32 v33, v33, v39\n\t"
    "s_waitcnt lgkmcnt(7)\n\t"
    "v_mfma_f32_16x16x32_bf16 v[60:63], v[24:27], v[8:11], v[60:63]\n\t"
    "v_and_b32 v6, 12, v7\n\t"
    "v_and_b32 v4, 3, v7\n\t"
    "s_waitcnt lgkmcnt(6)\n\t"
    "v_mfma_f32_16x16x32_bf16 v[60:63], v[28:31], v[12:15], v[60:63]\n\t"
    "v_lshl_add_u32 v4, v6, 1, v4\n\t"
    "v_lshrrev_b32 v6, 4, v7\n\t"
    "s_waitcnt lgkmcnt(5)\n\t"
    "v_mfma_f32_16x16x32_bf16 v[60:63], v[40:43], v[16:19], v[60:63]\n\t"
    "v_lshl_add_u32 v4, v4, 2, v6\n\t"
    "v_lshlrev_b32 v4, 2, v4\n\t"
    "s_waitcnt lgkmcnt(4)\n\t"
    "v_mfma_f32_16x16x32_bf16 v[60:63], v[44:47], v[20:23], v[60:63]\n\t"
    "v_add_u32 v4, 18432, v4\n\t"
    "ds_read_b64_tr_b16 v[40:41], v5 offset:43008\n\t"
    "ds_read_b64_tr_b16 v[42:43], v5 offset:44032\n\t"
    "ds_read_b64_tr_b16 v[44:45], v5 offset:43016\n\t"
    "ds_read_b64_tr_b16 v[46:47], v5 offset:44040\n\t"
    "ds_read2_b32 v[36:37], v4 offset0:0 offset1:16\n\t"
    "ds_read2_b32 v[38:39], v4 offset0:128 offset1:144\n\t"
    "v_and_b32 v4, 7, v7\n\t"
    "v_xor_b32 v6, v6, v4\n\t"
    "v_max3_f32 v34, v32, v33, %[lo]\n\t"
    "v_max3_f32 v34, v34, v56, v57\n\t"
    "v_max3_f32 v34, v34, v58, v59\n\t"
    "v_max3_f32 v34, v34, v60, v61\n\t"
    "v_max3_f32 v34, v34, v62, v63\n\t"
    "v_mov_b32 v35, v34\n\t"
    "v_lshlrev_b32 v6, 4, v6\n\t"
    "v_and_b32 v4, 15, v7\n\t"
    "v_permlane16_swap_b32 v34, v35\n\t"
    "v_max_f32 v34, v34, v35\n\t"
    "v_mov_b32 v35, v34\n\t"
    "v_lshl_or_b32 v6, v4, 7, v6\n\t"
    "v_xor_b32 v4, 64, v6\n\t"
    "v_permlane32_swap_b32 v34, v35\n\t"
    "v_max_f32 v34, v34, v35\n\t"
    "v_mul_f32 v34, %[nl], v34\n\t"
    "v_fma_f32 v32, v32, %[lsc], v34\n\t"
    "v_fma_f32 v33, v33, %[lsc], v34\n\t"
    "v_fma_f32 v56, v56, %[lsc], v34\n\t"
    "v_fma_f32 v57, v57, %[lsc], v34\n\t"
    "v_fma_f32 v58, v58, %[lsc], v34\n\t"
    "v_fma_f32 v59, v59, %[lsc], v34\n\t"
    "v_fma_f32 v60, v60, %[lsc], v34\n\t"
    "v_fma_f32 v61, v61, %[lsc], v34\n\t"
    "v_fma_f32 v62, v62, %[lsc], v34\n\t"
    "v_fma_f32 v63, v63, %[lsc], v34\n\t"
    "v_exp_f32 v56, v56\n\t"
    "v_exp_f32 v57, v57\n\t"
    "v_exp_f32 v58, v58\n\t"
    "v_exp_f32 v59, v59\n\t"
    "v_exp_f32 v60, v60\n\t"
    "v_exp_f32 v61, v61\n\t"
    "v_exp_f32 v62, v62\n\t"
    "v_exp_f32 v63, v63\n\t"
    "v_exp_f32 v32, v32\n\t"
    "v_exp_f32 v33, v33\n\t"
    "v_cvt_pk_bf16_f32 v24, v56, v57\n\t"
    "v_cvt_pk_bf16_f32 v25, v58, v59\n\t"
    "v_cvt_pk_bf16_f32 v26, v60, v61\n\t"
    "v_cvt_pk_bf16_f32 v27, v62, v63\n\t"
    "v_cvt_pk_bf16_f32 v28, v32, v33\n\t"
    "s_waitcnt lgkmcnt(0)\n\t"
    "v_mfma_f32_16x16x32_bf16 v[8:11], v[48:51], v[24:27], 0\n\t"
    "v_mov_b32 v56, %[one]\n\t"
    "v_mov_b32 v57, %[one]\n\t"
    "v_mfma_f32_16x16x32_bf16 v[12:15], v[52:55], v[24:27], 0\n\t"
    "v_mov_b32 v58, %[one]\n\t"
    "v_mov_b32 v59, %[one]\n\t"
    "v_mfma_f32_16x16x32_bf16 v[16:19], v[40:43], v[24:27], 0\n\t"
    "v_mov_b32 v29, 0\n\t"
    "v_mov_b32 v30, 0\n\t"
    "v_mfma_f32_16x16x32_bf16 v[60:63], v[56:59], v[24:27], 0\n\t"
    "v_mov_b32 v31, v28\n\t"
    "v_mfma_f32_16x16x32_bf16 v[20:23], v[44:47], v[24:27], 0\n\t"
    "v_mfma_f32_16x16x16_bf16 v[8:11], v[36:37], v[28:29], v[8:11]\n\t"
    "v_mfma_f32_16x16x16_bf16 v[12:15], v[36:37], v[30:31], v[12:15]\n\t"
    "s_nop 1\n\t"
    "v_mfma_f32_16x16x16_bf16 v[16:19], v[38:39], v[28:29], v[16:19]\n\t"
    "v_mfma_f32_16x16x16_bf16 v[60:63], v[56:57], v[28:29], v[60:63]\n\t"
    "s_nop 1\n\t"
    "v_mfma_f32_16x16x16_bf16 v[20:23], v[38:39], v[30:31], v[20:23]\n\t"
    "s_nop 4\n\t"
    "v_rcp_f32 v60, v60\n\t"
    "s_nop 0\n\t"
    "v_pk_mul_f32 v[8:9], v[8:9], v[60:61] op_sel_hi:[1,0]\n\t"
    "v_pk_mul_f32 v[10:11], v[10:11], v[60:61] op_sel_hi:[1,0]\n\t"
    "v_pk_mul_f32 v[12:13], v[12:13], v[60:61] op_sel_hi:[1,0]\n\t"
    "v_pk_mul_f32 v[14:15], v[14:15], v[60:61] op_sel_hi:[1,0]\n\t"
    "v_cvt_pk_bf16_f32 v24, v8, v9\n\t"
    "v_cvt_pk_bf16_f32 v25, v10, v11\n\t"
    "v_cvt_pk_bf16_f32 v26, v12, v13\n\t"
    "v_cvt_pk_bf16_f32 v27, v14, v15\n\t"
    "global_store_dwordx4 v6, v[24:27], %[od] offset:2048 sc1\n\t"
    "v_pk_mul_f32 v[16:17], v[16:17], v[60:61] op_sel_hi:[1,0]\n\t"
    "v_pk_mul_f32 v[18:19], v[18:19], v[60:61] op_sel_hi:[1,0]\n\t"
    "v_pk_mul_f32 v[20:21], v[20:21], v[60:61] op_sel_hi:[1,0]\n\t"
    "v_pk_mul_f32 v[22:23], v[22:23], v[60:61] op_sel_hi:[1,0]\n\t"
    "v_cvt_pk_bf16_f32 v28, v16, v17\n\t"
    "v_cvt_pk_bf16_f32 v29, v18, v19\n\t"
    "v_cvt_pk_bf16_f32 v30, v20, v21\n\t"
    "v_cvt_pk_bf16_f32 v31, v22, v23\n\t"
    "global_store_dwordx4 v4, v[28:31], %[od] offset:2048 sc1\n\t"
    "s_waitcnt lgkmcnt(0)\n\t"
    "s_nop 0"
    :
    : [pb] "s"(pb), [od] "s"(od), [neg] "s"(-3.0e38f), [lo] "s"(-1.0e38f), [one] "s"(0x3F803F80u), [lsc] "s"(LSC), [nl] "s"(-LSC), "{v[64:95]}"(W.w0), "{v[96:127]}"(W.w1), "{v[128:159]}"(W.w2), "{v[160:191]}"(W.w3), "{v[192:223]}"(W.w4), "{v[224:255]}"(W.w5)
    : "v4", "v5", "v6", "v7", "v8", "v9", "v10", "v11", "v12", "v13", "v14", "v15", "v16", "v17", "v18", "v19", "v20", "v21", "v22", "v23", "v24", "v25", "v26", "v27", "v28", "v29", "v30", "v31", "v32", "v33", "v34", "v35", "v36", "v37", "v38", "v39", "v40", "v41", "v42", "v43", "v44", "v45", "v46", "v47", "v48", "v49", "v50", "v51", "v52", "v53", "v54", "v55", "v56", "v57", "v58", "v59", "v60", "v61", "v62", "v63", "memory");
}
template <> __device__ __forceinline__ void seg_attn<2>(const QKW& W, unsigned pb, const char* od, unsigned long long* ts) {
  asm volatile("v_mbcnt_lo_u32_b32 v32, -1, 0\n\t"
    "v_mbcnt_hi_u32_b32 v32, -1, v32\n\t"
    "v_lshlrev_b32 v35, 4, v32\n\t"
    "ds_read_b128 v[24:27], v35 offset:20480\n\t"
    "ds_read_b128 v[28:31], v35 offset:21504\n\t"
    "v_lshrrev_b32 v33, 4, v32\n\t"
    "v_and_b32 v34, 15, v32\n\t"
    "v_lshrrev_b32 v4, 2, v34\n\t"
    "v_and_b32 v4, 2, v4\n\t"
    "v_xor_b32 v4, v33, v4\n\t"
    "v_lshl_or_b32 v4, v4, 4, %[pb]\n\t"
    "v_lshl_or_b32 v4, v34, 6, v4\n\t"
    "ds_read_b128 v[8:11], v4 offset:49152\n\t"
    "ds_read_b128 v[12:15], v4 offset:51200\n\t"
    "ds_read_b128 v[16:19], v4 offset:53248\n\t"
    "ds_read_b128 v[20:23], v4 offset:55296\n\t"
    "ds_read_b128 v[40:43], v4 offset:32768\n\t"
    "ds_read_b128 v[44:47], v4 offset:34816\n\t"
    "ds_read_b128 v[48:51], v4 offset:36864\n\t"
    "ds_read_b128 v[52:55], v4 offset:38912\n\t"
    "v_lshlrev_b32 v35, 2, v33\n\t"
    "v_sub_u32 v35, v34, v35\n\t"
    "v_ashrrev_i32 v60, 31, v35\n\t"
    "v_and_b32 v60, %[neg], v60\n\t"
    "v_add_u32 v61, -1, v35\n\t"
    "v_ashrrev_i32 v61, 31, v61\n\t"
    "v_and_b32 v61, %[neg], v61\n\t"
    "v_add_u32 v62, -2, v35\n\t"
    "v_ashrrev_i32 v62, 31, v62\n\t"
    "v_and_b32 v62, %[neg], v62\n\t"
    "v_add_u32 v63, -3, v35\n\t"
    "v_ashrrev_i32 v63, 31, v63\n\t"
    "v_and_b32 v63, %[neg], v63\n\t"
    "s_waitcnt lgkmcnt(6)\n\t"
    "v_mfma_f32_16x16x32_bf16 v[32:35], v[24:27], v[8:11], 0\n\t"
    "v_mfma_f32_16x16x32_bf16 v[36:39], v[24:27], v[12:15], 0\n\t"
    "s_waitcnt lgkmcnt(4)\n\t"
    "v_mfma_f32_16x16x32_bf16 v[32:35], v[28:31], v[16:19], v[32:35]\n\t"
    "v_mfma_f32_16x16x32_bf16 v[36:39], v[28:31], v[20:23], v[36:39]\n\t"
    "s_waitcnt lgkmcnt(3)\n\t"
    "v_mfma_f32_16x16x32_bf16 v[60:63], v[40:43], v[8:11], v[60:63]\n\t"
    "s_waitcnt lgkmcnt(2)\n\t"
    "v_mfma_f32_16x16x32_bf16 v[60:63], v[44:47], v[12:15], v[60:63]\n\t"
    "v_mbcnt_lo_u32_b32 v7, -1, 0\n\t"
    "v_mbcnt_hi_u32_b32 v7, -1, v7\n\t"
    "s_waitcnt lgkmcnt(1)\n\t"
    "v_mfma_f32_16x16x32_bf16 v[60:63], v[48:51], v[16:19], v[60:63]\n\t"
    "v_lshrrev_b32 v5, 4, v7\n\t"
    "v_and_b32 v5, 2, v5\n\t"
    "s_waitcnt lgkmcnt(0)\n\t"
    "v_mfma_f32_16x16x32_bf16 v[60:63], v[52:55], v[20:23], v[60:63]\n\t"
    "v_xor_b32 v5, v5, v7\n\t"
    "v_lshl_or_b32 v5, v5, 4, %[pb]\n\t"
    "ds_read_b64_tr_b16 v[48:49], v5 offset:45056\n\t"
    "ds_read_b64_tr_b16 v[52:53], v5 offset:45064\n\t"
    "v_add_f32 v32, v32, v38\n\t"
    "v_add_f32 v33, v33, v39\n\t"
    "v_and_b32 v6, 12, v7\n\t"
    "v_and_b32 v4, 3, v7\n\t"
    "v_lshl_add_u32 v4, v6, 1, v4\n\t"
    "v_lshrrev_b32 v6, 4, v7\n\t"
    "v_lshl_add_u32 v4, v4, 2, v6\n\t"
    "v_lshlrev_b32 v4, 2, v4\n\t"
    "v_add_u32 v4, 19456, v4\n\t"
    "ds_read_b64_tr_b16 v[40:41], v5 offset:47104\n\t"
    "ds_read_b64_tr_b16 v[44:45], v5 offset:47112\n\t"
    "ds_read2_b32 v[36:37], v4 offset0:0 offset1:16\n\t"
    "ds_read2_b32 v[38:39], v4 offset0:128 offset1:144\n\t"
    "v_and_b32 v4, 7, v7\n\t"
    "v_xor_b32 v6, v6, v4\n\t"
    "v_max3_f32 v34, v32, v33, %[lo]\n\t"
    "v_max3_f32 v34, v34, v60, v61\n\t"
    "v_max3_f32 v34, v34, v62, v63\n\t"
    "v_mov_b32 v35, v34\n\t"
    "v_lshlrev_b32 v6, 4, v6\n\t"
    "v_and_b32 v4, 15, v7\n\t"
    "v_permlane16_swap_b32 v34, v35\n\t"
    "v_max_f32 v34, v34, v35\n\t"
    "v_mov_b32 v35, v34\n\t"
    "v_lshl_or_b32 v6, v4, 7, v6\n\t"
    "v_xor_b32 v4, 64, v6\n\t"
    "v_permlane32_swap_b32 v34, v35\n\t"
    "v_max_f32 v34, v34, v35\n\t"
    "v_mul_f32 v34, %[nl], v34\n\t"
    "v_fma_f32 v32, v32, %[lsc], v34\n\t"
    "v_fma_f32 v33, v33, %[lsc], v34\n\t"
    "v_fma_f32 v60, v60, %[lsc], v34\n\t"
    "v_fma_f32 v61, v61, %[lsc], v34\n\t"
    "v_fma_f32 v62, v62, %[lsc], v34\n\t"
    "v_fma_f32 v63, v63, %[lsc], v34\n\t"
    "v_exp_f32 v60, v60\n\t"
    "v_exp_f32 v61, v61\n\t"
    "v_exp_f32 v62, v62\n\t"
    "v_exp_f32 v63, v63\n\t"
    "v_exp_f32 v32, v32\n\t"
    "v_exp_f32 v33, v33\n\t"
    "v_cvt_pk_bf16_f32 v24, v60, v61\n\t"
    "v_cvt_pk_bf16_f32 v25, v62, v63\n\t"
    "v_cvt_pk_bf16_f32 v28, v32, v33\n\t"
    "s_waitcnt lgkmcnt(0)\n\t"
    "v_mfma_f32_16x16x16_bf16 v[8:11], v[48:49], v[24:25], 0\n\t"
    "v_mov_b32 v56, %[one]\n\t"
    "v_mov_b32 v57, %[one]\n\t"
    "v_mfma_f32_16x16x16_bf16 v[12:15], v[52:53], v[24:25], 0\n\t"
    "v_mov_b32 v58, %[one]\n\t"
    "v_mov_b32 v59, %[one]\n\t"
    "v_mfma_f32_16x16x16_bf16 v[16:19], v[40:41], v[24:25], 0\n\t"
    "v_mov_b32 v29, 0\n\t"
    "v_mov_b32 v30, 0\n\t"
    "v_mfma_f32_16x16x16_bf16 v[60:63], v[56:57], v[24:25], 0\n\t"
    "v_mov_b32 v31, v28\n\t"
    "v_mfma_f32_16x16x16_bf16 v[20:23], v[44:45], v[24:25], 0\n\t"
    "v_mfma_f32_16x16x16_bf16 v[8:11], v[36:37], v[28:29], v[8:11]\n\t"
    "v_mfma_f32_16x16x16_bf16 v[12:15], v[36:37], v[30:31], v[12:15]\n\t"
    "s_nop 1\n\t"
    "v_mfma_f32_16x16x16_bf16 v[16:19], v[38:39], v[28:29], v[16:19]\n\t"
    "v_mfma_f32_16x16x16_bf16 v[60:63], v[56:57], v[28:29], v[60:63]\n\t"
    "s_nop 1\n\t"
    "v_mfma_f32_16x16x16_bf16 v[20:23], v[38:39], v[30:31], v[20:23]\n\t"
    "s_nop 4\n\t"
    "v_rcp_f32 v60, v60\n\t"
    "s_nop 0\n\t"
    "v_pk_mul_f32 v[8:9], v[8:9], v[60:61] op_sel_hi:[1,0]\n\t"
    "v_pk_mul_f32 v[10:11], v[10:11], v[60:61] op_sel_hi:[1,0]\n\t"
    "v_pk_mul_f32 v[12:13], v[12:13], v[60:61] op_sel_hi:[1,0]\n\t"
    "v_pk_mul_f32 v[14:15], v[14:15], v[60:61] op_sel_hi:[1,0]\n\t"
    "v_cvt_pk_bf16_f32 v24, v8, v9\n\t"
    "v_cvt_pk_bf16_f32 v25, v10, v11\n\t"
    "v_cvt_pk_bf16_f32 v26, v12, v13\n\t"
    "v_cvt_pk_bf16_f32 v27, v14, v15\n\t"
    "global_store_dwordx4 v6, v[24:27], %[od] offset:0 sc1\n\t"
    "v_pk_mul_f32 v[16:17], v[16:17], v[60:61] op_sel_hi:[1,0]\n\t"
    "v_pk_mul_f32 v[18:19], v[18:19], v[60:61] op_sel_hi:[1,0]\n\t"
    "v_pk_mul_f32 v[20:21], v[20:21], v[60:61] op_sel_hi:[1,0]\n\t"
    "v_pk_mul_f32 v[22:23], v[22:23], v[60:61] op_sel_hi:[1,0]\n\t"
    "v_cvt_pk_bf16_f32 v28, v16, v17\n\t"
    "v_cvt_pk_bf16_f32 v29, v18, v19\n\t"
    "v_cvt_pk_bf16_f32 v30, v20, v21\n\t"
    "v_cvt_pk_bf16_f32 v31, v22, v23\n\t"
    "global_store_dwordx4 v4, v[28:31], %[od] offset:0 sc1\n\t"
    "s_waitcnt lgkmcnt(0)\n\t"
    "s_nop 0"
    :
    : [pb] "s"(pb), [od] "s"(od), [neg] "s"(-3.0e38f), [lo] "s"(-1.0e38f), [one] "s"(0x3F803F80u), [lsc] "s"(LSC), [nl] "s"(-LSC), "{v[64:95]}"(W.w0), "{v[96:127]}"(W.w1), "{v[128:159]}"(W.w2), "{v[160:191]}"(W.w3), "{v[192:223]}"(W.w4), "{v[224:255]}"(W.w5)
    : "v4", "v5", "v6", "v7", "v8", "v9", "v10", "v11", "v12", "v13", "v14", "v15", "v16", "v17", "v18", "v19", "v20", "v21", "v22", "v23", "v24", "v25", "v26", "v27", "v28", "v29", "v30", "v31", "v32", "v33", "v34", "v35", "v36", "v37", "v38", "v39", "v40", "v41", "v42", "v43", "v44", "v45", "v46", "v47", "v48", "v49", "v50", "v51", "v52", "v53", "v54", "v55", "v56", "v57", "v58", "v59", "v60", "v61", "v62", "v63", "memory");
}
template <> __device__ __forceinline__ void seg_attn<3>(const QKW& W, unsigned pb, const char* od, unsigned long long* ts) {
  asm volatile("v_mbcnt_lo_u32_b32 v32, -1, 0\n\t"
    "v_mbcnt_hi_u32_b32 v32, -1, v32\n\t"
    "v_lshlrev_b32 v35, 4, v32\n\t"
    "ds_read_b128 v[24:27], v35 offset:20480\n\t"
    "ds_read_b128 v[28:31], v35 offset:21504\n\t"
    "v_lshrrev_b32 v33, 4, v32\n\t"
    "v_and_b32 v34, 15, v32\n\t"
    "v_lshrrev_b32 v4, 2, v34\n\t"
    "v_and_b32 v4, 2, v4\n\t"
    "v_xor_b32 v4, v33, v4\n\t"
    "v_lshl_or_b32 v4, v4, 4, %[pb]\n\t"
    "v_lshl_or_b32 v4, v34, 6, v4\n\t"
    "ds_read_b128 v[8:11], v4 offset:50176\n\t"
    "ds_read_b128 v[12:15], v4 offset:52224\n\t"
    "ds_read_b128 v[16:19], v4 offset:54272\n\t"
    "ds_read_b128 v[20:23], v4 offset:56320\n\t"
    "ds_read_b128 v[40:43], v4 offset:32768\n\t"
    "ds_read_b128 v[44:47], v4 offset:34816\n\t"
    "ds_read_b128 v[48:51], v4 offset:36864\n\t"
    "ds_read_b128 v[52:55], v4 offset:38912\n\t"
    "v_lshlrev_b32 v35, 2, v33\n\t"
    "v_sub_u32 v35, v34, v35\n\t"
    "v_ashrrev_i32 v60, 31, v35\n\t"
    "v_and_b32 v60, %[neg], v60\n\t"
    "v_add_u32 v61, -1, v35\n\t"
    "v_ashrrev_i32 v61, 31, v61\n\t"
    "v_and_b32 v61, %[neg], v61\n\t"
    "v_add_u32 v62, -2, v35\n\t"
    "v_ashrrev_i32 v62, 31, v62\n\t"
    "v_and_b32 v62, %[neg], v62\n\t"
    "v_add_u32 v63, -3, v35\n\t"
    "v_ashrrev_i32 v63, 31, v63\n\t"
    "v_and_b32 v63, %[neg], v63\n\t"
    "s_waitcnt lgkmcnt(6)\n\t"
    "v_mfma_f32_16x16x32_bf16 v[32:35], v[24:27], v[8:11], 0\n\t"
    "v_mfma_f32_16x16x32_bf16 v[36:39], v[24:27], v[12:15], 0\n\t"
    "s_waitcnt lgkmcnt(4)\n\t"
    "v_mfma_f32_16x16x32_bf16 v[32:35], v[28:31], v[16:19], v[32:35]\n\t"
    "v_mfma_f32_16x16x32_bf16 v[36:39], v[28:31], v[20:23], v[36:39]\n\t"
    "ds_read_b128 v[24:27], v4 offset:33792\n\t"
    "ds_read_b128 v[28:31], v4 offset:35840\n\t"
    "s_waitcnt lgkmcnt(5)\n\t"
    "v_mfma_f32_16x16x32_bf16 v[56:59], v[40:43], v[8:11], 0\n\t"
    "s_waitcnt lgkmcnt(4)\n\t"
    "v_mfma_f32_16x16x32_bf16 v[56:59], v[44:47], v[12:15], v[56:59]\n\t"
    "ds_read_b128 v[40:43], v4 offset:37888\n\t"
    "ds_read_b128 v[44:47], v4 offset:39936\n\t"
    "v_mbcnt_lo_u32_b32 v7, -1, 0\n\t"
    "v_mbcnt_hi_u32_b32 v7, -1, v7\n\t"
    "s_waitcnt lgkmcnt(5)\n\t"
    "v_mfma_f32_16x16x32_bf16 v[56:59], v[48:51], v[16:19], v[56:59]\n\t"
    "v_lshrrev_b32 v5, 4, v7\n\t"
    "v_and_b32 v5, 2, v5\n\t"
    "s_waitcnt lgkmcnt(4)\n\t"
    "v_mfma_f32_16x16x32_bf16 v[56:59], v[52:55], v[20:23], v[56:59]\n\t"
    "v_xor_b32 v5, v5, v7\n\t"
    "v_lshl_or_b32 v5, v5, 4, %[pb]\n\t"
    "ds_read_b64_tr_b16 v[48:49], v5 offset:45056\n\t"
    "ds_read_b64_tr_b16 v[50:51], v5 offset:46080\n\t"
    "ds_read_b64_tr_b16 v[52:53], v5 offset:45064\n\t"
    "ds_read_b64_tr_b16 v[54:55], v5 offset:46088\n\t"
    "v_add_f32 v32, v32, v38\n\t"
    "v_add_f32 v33, v33, v39\n\t"
    "s_waitcnt lgkmcnt(7)\n\t"
    "v_mfma_f32_16x16x32_bf16 v[60:63], v[24:27], v[8:11], v[60:63]\n\t"
    "v_and_b32 v6, 12, v7\n\t"
    "v_and_b32 v4, 3, v7\n\t"
    "s_waitcnt lgkmcnt(6)\n\t"
    "v_mfma_f32_16x16x32_bf16 v[60:63], v[28:31], v[12:15], v[60:63]\n\t"
    "v_lshl_add_u32 v4, v6, 1, v4\n\t"
    "v_lshrrev_b32 v6, 4, v7\n\t"
    "s_waitcnt lgkmcnt(5)\n\t"
    "v_mfma_f32_16x16x32_bf16 v[60:63], v[40:43], v[16:19], v[60:63]\n\t"
    "v_lshl_add_u32 v4, v4, 2, v6\n\t"
    "v_lshlrev_b32 v4, 2, v4\n\t"
    "s_waitcnt lgkmcnt(4)\n\t"
    "v_mfma_f32_16x16x32_bf16 v[60:63], v[44:47], v[20:23], v[60:63]\n\t"
    "v_add_u32 v4, 19456, v4\n\t"
    "ds_read_b64_tr_b16 v[40:41], v5 offset:47104\n\t"
    "ds_read_b64_tr_b16 v[42:43], v5 offset:48128\n\t"
    "ds_read_b64_tr_b16 v[44:45], v5 offset:47112\n\t"
    "ds_read_b64_tr_b16 v[46:47], v5 offset:48136\n\t"
    "ds_read2_b32 v[36:37], v4 offset0:0 offset1:16\n\t"
    "ds_read2_b32 v[38:39], v4 offset0:128 offset1:144\n\t"
    "v_and_b32 v4, 7, v7\n\t"
    "v_xor_b32 v6, v6, v4\n\t"
    "v_max3_f32 v34, v32, v33, %[lo]\n\t"
    "v_max3_f32 v34, v34, v56, v57\n\t"
    "v_max3_f32 v34, v34, v58, v59\n\t"
    "v_max3_f32 v34, v34, v60, v61\n\t"
    "v_max3_f32 v34, v34, v62, v63\n\t"
    "v_mov_b32 v35, v34\n\t"
    "v_lshlrev_b32 v6, 4, v6\n\t"
    "v_and_b32 v4, 15, v7\n\t"
    "v_permlane16_swap_b32 v34, v35\n\t"
    "v_max_f32 v34, v34, v35\n\t"
    "v_mov_b32 v35, v34\n\t"
    "v_lshl_or_b32 v6, v4, 7, v6\n\t"
    "v_xor_b32 v4, 64, v6\n\t"
    "v_permlane32_swap_b32 v34, v35\n\t"
    "v_max_f32 v34, v34, v35\n\t"
    "v_mul_f32 v34, %[nl], v34\n\t"
    "v_fma_f32 v32, v32, %[lsc], v34\n\t"
    "v_fma_f32 v33, v33, %[lsc], v34\n\t"
    "v_fma_f32 v56, v56, %[lsc], v34\n\t"
    "v_fma_f32 v57, v57, %[lsc], v34\n\t"
    "v_fma_f32 v58, v58, %[lsc], v34\n\t"
    "v_fma_f32 v59, v59, %[lsc], v34\n\t"
    "v_fma_f32 v60, v60, %[lsc], v34\n\t"
    "v_fma_f32 v61, v61, %[lsc], v34\n\t"
    "v_fma_f32 v62, v62, %[lsc], v34\n\t"
    "v_fma_f32 v63, v63, %[lsc], v34\n\t"
    "v_exp_f32 v56, v56\n\t"
    "v_exp_f32 v57, v57\n\t"
    "v_exp_f32 v58, v58\n\t"
    "v_exp_f32 v59, v59\n\t"
    "v_exp_f32 v60, v60\n\t"
    "v_exp_f32 v61, v61\n\t"
    "v_exp_f32 v62, v62\n\t"
    "v_exp_f32 v63, v63\n\t"
    "v_exp_f32 v32, v32\n\t"
    "v_exp_f32 v33, v33\n\t"
    "v_cvt_pk_bf16_f32 v24, v56, v57\n\t"
    "v_cvt_pk_bf16_f32 v25, v58, v59\n\t"
    "v_cvt_pk_bf16_f32 v26, v60, v61\n\t"
    "v_cvt_pk_bf16_f32 v27, v62, v63\n\t"
    "v_cvt_pk_bf16_f32 v28, v32, v33\n\t"
    "s_waitcnt lgkmcnt(0)\n\t"
    "v_mfma_f32_16x16x32_bf16 v[8:11], v[48:51], v[24:27], 0\n\t"
    "v_mov_b32 v56, %[one]\n\t"
    "v_mov_b32 v57, %[one]\n\t"
    "v_mfma_f32_16x16x32_bf16 v[12:15], v[52:55], v[24:27], 0\n\t"
    "v_mov_b32 v58, %[one]\n\t"
    "v_mov_b32 v59, %[one]\n\t"
    "v_mfma_f32_16x16x32_bf16 v[16:19], v[40:43], v[24:27], 0\n\t"
    "v_mov_b32 v29, 0\n\t"
    "v_mov_b32 v30, 0\n\t"
    "v_mfma_f32_16x16x32_bf16 v[60:63], v[56:59], v[24:27], 0\n\t"
    "v_mov_b32 v31, v28\n\t"
    "v_mfma_f32_16x16x32_bf16 v[20:23], v[44:47], v[24:27], 0\n\t"
    "v_mfma_f32_16x16x16_bf16 v[8:11], v[36:37], v[28:29], v[8:11]\n\t"
    "v_mfma_f32_16x16x16_bf16 v[12:15], v[36:37], v[30:31], v[12:15]\n\t"
    "s_nop 1\n\t"
    "v_mfma_f32_16x16x16_bf16 v[16:19], v[38:39], v[28:29], v[16:19]\n\t"
    "v_mfma_f32_16x16x16_bf16 v[60:63], v[56:57], v[28:29], v[60:63]\n\t"
    "s_nop 1\n\t"
    "v_mfma_f32_16x16x16_bf16 v[20:23], v[38:39], v[30:31], v[20:23]\n\t"
    "s_nop 4\n\t"
    "v_rcp_f32 v60, v60\n\t"
    "s_nop 0\n\t"
    "v_pk_mul_f32 v[8:9], v[8:9], v[60:61] op_sel_hi:[1,0]\n\t"
    "v_pk_mul_f32 v[10:11], v[10:11], v[60:61] op_sel_hi:[1,0]\n\t"
    "v_pk_mul_f32 v[12:13], v[12:13], v[60:61] op_sel_hi:[1,0]\n\t"
    "v_pk_mul_f32 v[14:15], v[14:15], v[60:61] op_sel_hi:[1,0]\n\t"
    "v_cvt_pk_bf16_f32 v24, v8, v9\n\t"
    "v_cvt_pk_bf16_f32 v25, v10, v11\n\t"
    "v_cvt_pk_bf16_f32 v26, v12, v13\n\t"
    "v_cvt_pk_bf16_f32 v27, v14, v15\n\t"
    "global_store_dwordx4 v6, v[24:27], %[od] offset:2048 sc1\n\t"
    "v_pk_mul_f32 v[16:17], v[16:17], v[60:61] op_sel_hi:[1,0]\n\t"
    "v_pk_mul_f32 v[18:19], v[18:19], v[60:61] op_sel_hi:[1,0]\n\t"
    "v_pk_mul_f32 v[20:21], v[20:21], v[60:61] op_sel_hi:[1,0]\n\t"
    "v_pk_mul_f32 v[22:23], v[22:23], v[60:61] op_sel_hi:[1,0]\n\t"
    "v_cvt_pk_bf16_f32 v28, v16, v17\n\t"
    "v_cvt_pk_bf16_f32 v29, v18, v19\n\t"
    "v_cvt_pk_bf16_f32 v30, v20, v21\n\t"
    "v_cvt_pk_bf16_f32 v31, v22, v23\n\t"
    "global_store_dwordx4 v4, v[28:31], %[od] offset:2048 sc1\n\t"
    "s_waitcnt lgkmcnt(0)\n\t"
    "s_nop 0"
    :
    : [pb] "s"(pb), [od] "s"(od), [neg] "s"(-3.0e38f), [lo] "s"(-1.0e38f), [one] "s"(0x3F803F80u), [lsc] "s"(LSC), [nl] "s"(-LSC), "{v[64:95]}"(W.w0), "{v[96:127]}"(W.w1), "{v[128:159]}"(W.w2), "{v[160:191]}"(W.w3), "{v[192:223]}"(W.w4), "{v[224:255]}"(W.w5)
    : "v4", "v5", "v6", "v7", "v8", "v9", "v10", "v11", "v12", "v13", "v14", "v15", "v16", "v17", "v18", "v19", "v20", "v21", "v22", "v23", "v24", "v25", "v26", "v27", "v28", "v29", "v30", "v31", "v32", "v33", "v34", "v35", "v36", "v37", "v38", "v39", "v40", "v41", "v42", "v43", "v44", "v45", "v46", "v47", "v48", "v49", "v50", "v51", "v52", "v53", "v54", "v55", "v56", "v57", "v58", "v59", "v60", "v61", "v62", "v63", "memory");
}
template <int I> __device__ __forceinline__ void qk_wload(u32x32& w, unsigned a0, unsigned a1);
template <> __device__ __forceinline__ void qk_wload<0>(u32x32& w, unsigned a0, unsigned a1) {
  asm volatile("ds_read_b64_tr_b16 v[64:65], %1 offset:0\n\t"
    "ds_read_b64_tr_b16 v[66:67], %1 offset:512\n\t"
    "ds_read_b64_tr_b16 v[68:69], %1 offset:8192\n\t"
    "ds_read_b64_tr_b16 v[70:71], %1 offset:8704\n\t"
    "ds_read_b64_tr_b16 v[72:73], %2 offset:0\n\t"
    "ds_read_b64_tr_b16 v[74:75], %2 offset:512\n\t"
    "ds_read_b64_tr_b16 v[76:77], %2 offset:8192\n\t"
    "ds_read_b64_tr_b16 v[78:79], %2 offset:8704\n\t"
    "s_waitcnt lgkmcnt(0)\n\t"
    "ds_read_b64_tr_b16 v[80:81], %1 offset:16384\n\t"
    "ds_read_b64_tr_b16 v[82:83], %1 offset:16896\n\t"
    "ds_read_b64_tr_b16 v[84:85], %1 offset:24576\n\t"
    "ds_read_b64_tr_b16 v[86:87], %1 offset:25088\n\t"
    "ds_read_b64_tr_b16 v[88:89], %2 offset:16384\n\t"
    "ds_read_b64_tr_b16 v[90:91], %2 offset:16896\n\t"
    "ds_read_b64_tr_b16 v[92:93], %2 offset:24576\n\t"
    "ds_read_b64_tr_b16 v[94:95], %2 offset:25088\n\t"
    "s_waitcnt lgkmcnt(0)"
    : "=&{v[64:95]}"(w) : "v"(a0), "v"(a1) : "memory");
}
template <> __device__ __forceinline__ void qk_wload<1>(u32x32& w, unsigned a0, unsigned a1) {
  asm volatile("ds_read_b64_tr_b16 v[96:97], %1 offset:0\n\t"
    "ds_read_b64_tr_b16 v[98:99], %1 offset:512\n\t"
    "ds_read_b64_tr_b16 v[100:101], %1 offset:8192\n\t"
    "ds_read_b64_tr_b16 v[102:103], %1 offset:8704\n\t"
    "ds_read_b64_tr_b16 v[104:105], %2 offset:0\n\t"
    "ds_read_b64_tr_b16 v[106:107], %2 offset:512\n\t"
    "ds_read_b64_tr_b16 v[108:109], %2 offset:8192\n\t"
    "ds_read_b64_tr_b16 v[110:111], %2 offset:8704\n\t"
    "s_waitcnt lgkmcnt(0)\n\t"
    "ds_read_b64_tr_b16 v[112:113], %1 offset:16384\n\t"
    "ds_read_b64_tr_b16 v[114:115], %1 offset:16896\n\t"
    "ds_read_b64_tr_b16 v[116:117], %1 offset:24576\n\t"
    "ds_read_b64_tr_b16 v[118:119], %1 offset:25088\n\t"
    "ds_read_b64_tr_b16 v[120:121], %2 offset:16384\n\t"
    "ds_read_b64_tr_b16 v[122:123], %2 offset:16896\n\t"
    "ds_read_b64_tr_b16 v[124:125], %2 offset:24576\n\t"
    "ds_read_b64_tr_b16 v[126:127], %2 offset:25088\n\t"
    "s_waitcnt lgkmcnt(0)"
    : "=&{v[96:127]}"(w) : "v"(a0), "v"(a1) : "memory");
}
template <> __device__ __forceinline__ void qk_wload<2>(u32x32& w, unsigned a0, unsigned a1) {
  asm volatile("ds_read_b64_tr_b16 v[128:129], %1 offset:0\n\t"
    "ds_read_b64_tr_b16 v[130:131], %1 offset:512\n\t"
    "ds_read_b64_tr_b16 v[132:133], %1 offset:8192\n\t"
    "ds_read_b64_tr_b16 v[134:135], %1 offset:8704\n\t"
    "ds_read_b64_tr_b16 v[136:137], %2 offset:0\n\t"
    "ds_read_b64_tr_b16 v[138:139], %2 offset:512\n\t"
    "ds_read_b64_tr_b16 v[140:141], %2 offset:8192\n\t"
    "ds_read_b64_tr_b16 v[142:143], %2 offset:8704\n\t"
    "s_waitcnt lgkmcnt(0)\n\t"
    "ds_read_b64_tr_b16 v[144:145], %1 offset:16384\n\t"
    "ds_read_b64_tr_b16 v[146:147], %1 offset:16896\n\t"
    "ds_read_b64_tr_b16 v[148:149], %1 offset:24576\n\t"
    "ds_read_b64_tr_b16 v[150:151], %1 offset:25088\n\t"
    "ds_read_b64_tr_b16 v[152:153], %2 offset:16384\n\t"
    "ds_read_b64_tr_b16 v[154:155], %2 offset:16896\n\t"
    "ds_read_b64_tr_b16 v[156:157], %2 offset:24576\n\t"
    "ds_read_b64_tr_b16 v[158:159], %2 offset:25088\n\t"
    "s_waitcnt lgkmcnt(0)"
    : "=&{v[128:159]}"(w) : "v"(a0), "v"(a1) : "memory");
}
template <> __device__ __forceinline__ void qk_wload<3>(u32x32& w, unsigned a0, unsigned a1) {
  asm volatile("ds_read_b64_tr_b16 v[160:161], %1 offset:0\n\t"
    "ds_read_b64_tr_b16 v[162:163], %1 offset:512\n\t"
    "ds_read_b64_tr_b16 v[164:165], %1 offset:8192\n\t"
    "ds_read_b64_tr_b16 v[166:167], %1 offset:8704\n\t"
    "ds_read_b64_tr_b16 v[168:169], %2 offset:0\n\t"
    "ds_read_b64_tr_b16 v[170:171], %2 offset:512\n\t"
    "ds_read_b64_tr_b16 v[172:173], %2 offset:8192\n\t"
    "ds_read_b64_tr_b16 v[174:175], %2 offset:8704\n\t"
    "s_waitcnt lgkmcnt(0)\n\t"
    "ds_read_b64_tr_b16 v[176:177], %1 offset:16384\n\t"
    "ds_read_b64_tr_b16 v[178:179], %1 offset:16896\n\t"
    "ds_read_b64_tr_b16 v[180:181], %1 offset:24576\n\t"
    "ds_read_b64_tr_b16 v[182:183], %1 offset:25088\n\t"
    "ds_read_b64_tr_b16 v[184:185], %2 offset:16384\n\t"
    "ds_read_b64_tr_b16 v[186:187], %2 offset:16896\n\t"
    "ds_read_b64_tr_b16 v[188:189], %2 offset:24576\n\t"
    "ds_read_b64_tr_b16 v[190:191], %2 offset:25088\n\t"
    "s_waitcnt lgkmcnt(0)"
    : "=&{v[160:191]}"(w) : "v"(a0), "v"(a1) : "memory");
}
template <> __device__ __forceinline__ void qk_wload<4>(u32x32& w, unsigned a0, unsigned a1) {
  asm volatile("ds_read_b64_tr_b16 v[192:193], %1 offset:0\n\t"
    "ds_read_b64_tr_b16 v[194:195], %1 offset:512\n\t"
    "ds_read_b64_tr_b16 v[196:197], %1 offset:8192\n\t"
    "ds_read_b64_tr_b16 v[198:199], %1 offset:8704\n\t"
    "ds_read_b64_tr_b16 v[200:201], %2 offset:0\n\t"
    "ds_read_b64_tr_b16 v[202:203], %2 offset:512\n\t"
    "ds_read_b64_tr_b16 v[204:205], %2 offset:8192\n\t"
    "ds_read_b64_tr_b16 v[206:207], %2 offset:8704\n\t"
    "s_waitcnt lgkmcnt(0)\n\t"
    "ds_read_b64_tr_b16 v[208:209], %1 offset:16384\n\t"
    "ds_read_b64_tr_b16 v[210:211], %1 offset:16896\n\t"
    "ds_read_b64_tr_b16 v[212:213], %1 offset:24576\n\t"
    "ds_read_b64_tr_b16 v[214:215], %1 offset:25088\n\t"
    "ds_read_b64_tr_b16 v[216:217], %2 offset:16384\n\t"
    "ds_read_b64_tr_b16 v[218:219], %2 offset:16896\n\t"
    "ds_read_b64_tr_b16 v[220:221], %2 offset:24576\n\t"
    "ds_read_b64_tr_b16 v[222:223], %2 offset:25088\n\t"
    "s_waitcnt lgkmcnt(0)"
    : "=&{v[192:223]}"(w) : "v"(a0), "v"(a1) : "memory");
}
template <> __device__ __forceinline__ void qk_wload<5>(u32x32& w, unsigned a0, unsigned a1) {
  asm volatile("ds_read_b64_tr_b16 v[224:225], %1 offset:0\n\t"
    "ds_read_b64_tr_b16 v[226:227], %1 offset:512\n\t"
    "ds_read_b64_tr_b16 v[228:229], %1 offset:8192\n\t"
    "ds_read_b64_tr_b16 v[230:231], %1 offset:8704\n\t"
    "ds_read_b64_tr_b16 v[232:233], %2 offset:0\n\t"
    "ds_read_b64_tr_b16 v[234:235], %2 offset:512\n\t"
    "ds_read_b64_tr_b16 v[236:237], %2 offset:8192\n\t"
    "ds_read_b64_tr_b16 v[238:239], %2 offset:8704\n\t"
    "s_waitcnt lgkmcnt(0)\n\t"
    "ds_read_b64_tr_b16 v[240:241], %1 offset:16384\n\t"
    "ds_read_b64_tr_b16 v[242:243], %1 offset:16896\n\t"
    "ds_read_b64_tr_b16 v[244:245], %1 offset:24576\n\t"
    "ds_read_b64_tr_b16 v[246:247], %1 offset:25088\n\t"
    "ds_read_b64_tr_b16 v[248:249], %2 offset:16384\n\t"
    "ds_read_b64_tr_b16 v[250:251], %2 offset:16896\n\t"
    "ds_read_b64_tr_b16 v[252:253], %2 offset:24576\n\t"
    "ds_read_b64_tr_b16 v[254:255], %2 offset:25088\n\t"
    "s_waitcnt lgkmcnt(0)"
    : "=&{v[224:255]}"(w) : "v"(a0), "v"(a1) : "memory");
}
#define OREP 2
#define AREP 2
#define BELL1 0
#define RINGA 1
constexpr int V_P1 = 131072, V_PV = 149632, V_P2 = 0;
struct VRW { u32x32 k0, k1, o0, o1, o2, o3; };
__device__ __forceinline__ void vr_v(const VRW& W, const char* ag, const char* kd, const char* vd, unsigned wl, unsigned wa, unsigned wb, unsigned ra, unsigned rb, unsigned& cap, unsigned& sp, unsigned long long& tc, unsigned long long* ts) {
  asm volatile("s_mov_b32 %[sp], 0\n\t"
    "v_mbcnt_lo_u32_b32 v20, -1, 0\n\t"
    "v_mbcnt_hi_u32_b32 v20, -1, v20\n\t"
    "v_lshlrev_b32 v21, 4, v20\n\t"
    "s_nop 0\n\t"
    "global_load_dwordx4 v[24:27], v21, %[ag] sc1\n\t"
    "global_load_dwordx4 v[28:31], v21, %[ag] offset:1024 sc1\n\t"
    "s_memtime %[tc]\n\t"
    "v_lshl_add_u32 v6, v20, 4, %[wl]\n\t"
    "ds_read_b128 v[32:35], v6 offset:0\n\t"
    "ds_read_b128 v[36:39], v6 offset:1024\n\t"
    "ds_read_b128 v[40:43], v6 offset:2048\n\t"
    "ds_read_b128 v[44:47], v6 offset:3072\n\t"
    "ds_read_b128 v[48:51], v6 offset:4096\n\t"
    "ds_read_b128 v[52:55], v6 offset:5120\n\t"
    "ds_read_b128 v[56:59], v6 offset:6144\n\t"
    "ds_read_b128 v[60:63], v6 offset:7168\n\t"
    "s_setprio 3\n\t"
    "v_and_b32 v6, 2, v20\n\t"
    "v_mul_u32_u24 v6, 0x5555, v6\n\t"
    "v_or_b32 v4, %[c44], v6\n\t"
    "v_and_b32 v6, 15, v20\n\t"
    "v_lshl_add_u32 v5, v6, 2, %[wa]\n\t"
    "v_and_b32 v6, 48, v20\n\t"
    "v_mad_u32_u24 v5, v6, 36, v5\n\t"
    "v_and_b32 v6, 32, v20\n\t"
    "v_mul_u32_u24 v7, 18, v6\n\t"
    "v_and_b32 v6, 4, v20\n\t"
    "v_mul_u32_u24 v6, 72, v6\n\t"
    "v_add_u32 v7, v7, v6\n\t"
    "v_and_b32 v6, 16, v20\n\t"
    "v_lshl_add_u32 v7, v6, 3, v7\n\t"
    "v_and_b32 v6, 3, v20\n\t"
    "v_lshl_add_u32 v7, v6, 3, v7\n\t"
    "v_and_b32 v6, 8, v20\n\t"
    "v_mul_u32_u24 v6, 1160, v6\n\t"
    "v_add3_u32 v7, v7, v6, %[ra]\n\t"
    "3:\n\t"
    "v_mov_b32 v8, 0\n\t"
    "v_mov_b32 v9, 0\n\t"
    "v_mov_b32 v10, 0\n\t"
    "v_mov_b32 v11, 0\n\t"
    "v_mov_b32 v12, 0\n\t"
    "v_mov_b32 v13, 0\n\t"
    "v_mov_b32 v14, 0\n\t"
    "v_mov_b32 v15, 0\n\t"
    "v_mov_b32 v16, 0\n\t"
    "v_mov_b32 v17, 0\n\t"
    "v_mov_b32 v18, 0\n\t"
    "v_mov_b32 v19, 0\n\t"
    "v_mov_b32 v20, 0\n\t"
    "v_mov_b32 v21, 0\n\t"
    "v_mov_b32 v22, 0\n\t"
    "v_mov_b32 v23, 0\n\t"
    "s_waitcnt vmcnt(0) lgkmcnt(0)\n\t"
    "v_smfmac_f32_16x16x64_bf16 v[8:11], v[24:27], v[64:71], v4\n\t"
    "v_smfmac_f32_16x16x64_bf16 v[8:11], v[28:31], v[72:79], v4\n\t"
    "v_max3_u32 v6, v24, v25, v26\n\t"
    "v_max3_u32 v6, v6, v27, v28\n\t"
    "v_smfmac_f32_16x16x64_bf16 v[12:15], v[24:27], v[80:87], v4\n\t"
    "v_max3_u32 v6, v6, v29, v30\n\t"
    "v_max_u32 v6, v6, v31\n\t"
    "v_smfmac_f32_16x16x64_bf16 v[12:15], v[28:31], v[88:95], v4\n\t"
    "v_cmp_eq_u32 vcc, -1, v6\n\t"
    "s_cbranch_vccnz 1f\n\t"
    "2:\n\t"
    "v_smfmac_f32_16x16x64_bf16 v[16:19], v[24:27], v[96:103], v4\n\t"
    "v_smfmac_f32_16x16x64_bf16 v[16:19], v[28:31], v[104:111], v4\n\t"
    "v_add_f32 v8, v8, v10\n\t"
    "v_add_f32 v9, v9, v11\n\t"
    "ds_write2_b32 v5, v8, v9 offset0:0 offset1:72\n\t"
    "v_smfmac_f32_16x16x64_bf16 v[20:23], v[24:27], v[112:119], v4\n\t"
    "v_smfmac_f32_16x16x64_bf16 v[20:23], v[28:31], v[120:127], v4\n\t"
    "v_add_f32 v12, v12, v14\n\t"
    "v_add_f32 v13, v13, v15\n\t"
    "ds_write2_b32 v5, v12, v13 offset0:16 offset1:88\n\t"
    "v_add_f32 v16, v16, v18\n\t"
    "v_add_f32 v17, v17, v19\n\t"
    "ds_write2_b32 v5, v16, v17 offset0:32 offset1:104\n\t"
    "s_nop 1\n\t"
    "v_add_f32 v20, v20, v22\n\t"
    "v_add_f32 v21, v21, v23\n\t"
    "ds_write2_b32 v5, v20, v21 offset0:48 offset1:120\n\t"
    "v_mov_b32 v8, 0\n\t"
    "v_mov_b32 v9, 0\n\t"
    "v_mov_b32 v10, 0\n\t"
    "v_mov_b32 v11, 0\n\t"
    "v_mov_b32 v12, 0\n\t"
    "v_mov_b32 v13, 0\n\t"
    "v_mov_b32 v14, 0\n\t"
    "v_mov_b32 v15, 0\n\t"
    "v_mbcnt_lo_u32_b32 v5, -1, 0\n\t"
    "v_mbcnt_hi_u32_b32 v5, -1, v5\n\t"
    "s_waitcnt lgkmcnt(0)\n\t"
    "s_barrier\n\t"
    "ds_read_b64 v[16:17], v7 offset:0\n\t"
    "ds_read_b64 v[18:19], v7 offset:2304\n\t"
    "ds_read_b64 v[20:21], v7 offset:4608\n\t"
    "ds_read_b64 v[22:23], v7 offset:6912\n\t"
    "v_smfmac_f32_16x16x64_bf16 v[8:11], v[24:27], v[32:39], v4\n\t"
    "v_smfmac_f32_16x16x64_bf16 v[8:11], v[28:31], v[40:47], v4\n\t"
    "v_smfmac_f32_16x16x64_bf16 v[12:15], v[24:27], v[48:55], v4\n\t"
    "v_smfmac_f32_16x16x64_bf16 v[12:15], v[28:31], v[56:63], v4\n\t"
    "v_and_b32 v6, 48, v5\n\t"
    "v_and_b32 v7, 7, v5\n\t"
    "v_lshlrev_b32 v7, 2, v7\n\t"
    "v_lshl_or_b32 v7, v6, 1, v7\n\t"
    "s_waitcnt lgkmcnt(2)\n\t"
    "v_pk_add_f32 v[16:17], v[16:17], v[18:19]\n\t"
    "s_waitcnt lgkmcnt(0)\n\t"
    "v_pk_add_f32 v[20:21], v[20:21], v[22:23]\n\t"
    "v_pk_add_f32 v[16:17], v[16:17], v[20:21]\n\t"
    "s_nop 1\n\t"
    "v_add_f32_dpp v16, v16, v16 row_ror:8 row_mask:0xf bank_mask:0xf\n\t"
    "v_add_f32_dpp v17, v17, v17 row_ror:8 row_mask:0xf bank_mask:0xf\n\t"
    "v_cvt_pk_bf16_f32 v6, v16, v17\n\t"
    "v_min_u32 v6, -2, v6\n\t"
    "s_mov_b64 exec, %[em]\n\t"
    "global_store_dword v7, v6, %[kd] sc1\n\t"
    "s_mov_b64 exec, -1\n\t"
    "v_lshl_add_u32 v16, v5, 3, %[wb]\n\t"
    "v_add_f32 v8, v8, v10\n\t"
    "v_add_f32 v9, v9, v11\n\t"
    "v_add_f32 v12, v12, v14\n\t"
    "v_add_f32 v13, v13, v15\n\t"
    "ds_write2st64_b64 v16, v[8:9], v[12:13] offset1:1\n\t"
    "v_and_b32 v6, 8, v5\n\t"
    "v_mul_u32_u24 v6, 528, v6\n\t"
    "v_lshl_add_u32 v5, v7, 1, v6\n\t"
    "v_add_u32 v5, %[rb], v5\n\t"
    "s_cmp_ge_u32 %[wb], 153728\n\t"
    "s_waitcnt lgkmcnt(0)\n\t"
    "s_barrier\n\t"
    "s_cbranch_scc1 8f\n\t"
    "ds_read_b64 v[16:17], v5 offset:0\n\t"
    "ds_read_b64 v[18:19], v5 offset:1024\n\t"
    "ds_read_b64 v[20:21], v5 offset:2048\n\t"
    "ds_read_b64 v[22:23], v5 offset:3072\n\t"
    "s_waitcnt lgkmcnt(2)\n\t"
    "v_pk_add_f32 v[16:17], v[16:17], v[18:19]\n\t"
    "s_waitcnt lgkmcnt(0)\n\t"
    "v_pk_add_f32 v[20:21], v[20:21], v[22:23]\n\t"
    "v_pk_add_f32 v[16:17], v[16:17], v[20:21]\n\t"
    "s_nop 1\n\t"
    "v_add_f32_dpp v16, v16, v16 row_ror:8 row_mask:0xf bank_mask:0xf\n\t"
    "v_add_f32_dpp v17, v17, v17 row_ror:8 row_mask:0xf bank_mask:0xf\n\t"
    "v_cvt_pk_bf16_f32 v6, v16, v17\n\t"
    "v_min_u32 v6, -2, v6\n\t"
    "s_mov_b64 exec, %[em]\n\t"
    "global_store_dword v7, v6, %[vd] sc1\n\t"
    "s_mov_b64 exec, -1\n\t"
    "8:\n\t"
    "s_setprio 2\n\t"
    "s_waitcnt lgkmcnt(0)\n\t"
    "s_branch 9f\n\t"
    "1:\n\t"
    "s_add_u32 %[sp], %[sp], 1\n\t"
    "s_cmp_ge_u32 %[sp], %[cap]\n\t"
    "s_cbranch_scc1 7f\n\t"
    "v_mbcnt_lo_u32_b32 v20, -1, 0\n\t"
    "v_mbcnt_hi_u32_b32 v20, -1, v20\n\t"
    "v_lshlrev_b32 v21, 4, v20\n\t"
    "s_nop 0\n\t"
    "global_load_dwordx4 v[24:27], v21, %[ag] sc1\n\t"
    "global_load_dwordx4 v[28:31], v21, %[ag] offset:1024 sc1\n\t"
    "s_waitcnt lgkmcnt(0)\n\t"
    "s_memtime %[tc]\n\t"
    "s_nop 7\n\t"
    "s_branch 3b\n\t"
    "7:\n\t"
    "s_mov_b32 %[cap], 0\n\t"
    "s_branch 2b\n\t"
    "9:"
    : [tc] "=&s"(tc), [sp] "=&s"(sp), [cap] "+s"(cap)
    : [ag] "s"(ag), [kd] "s"(kd), [vd] "s"(vd), [wl] "s"(wl), [wa] "s"(wa), [wb] "s"(wb), [ra] "s"(ra), [rb] "s"(rb), [c44] "s"(0x4444u), [em] "s"(0x00FF00FF00FF00FFull), "{v[64:95]}"(W.k0), "{v[96:127]}"(W.k1), "{v[128:159]}"(W.o0), "{v[160:191]}"(W.o1), "{v[192:223]}"(W.o2), "{v[224:255]}"(W.o3)
    : "v4", "v5", "v6", "v7", "v8", "v9", "v10", "v11", "v12", "v13", "v14", "v15", "v16", "v17", "v18", "v19", "v20", "v21", "v22", "v23", "v24", "v25", "v26", "v27", "v28", "v29", "v30", "v31", "v32", "v33", "v34", "v35", "v36", "v37", "v38", "v39", "v40", "v41", "v42", "v43", "v44", "v45", "v46", "v47", "v48", "v49", "v50", "v51", "v52", "v53", "v54", "v55", "v56", "v57", "v58", "v59", "v60", "v61", "v62", "v63", "vcc", "scc", "memory");
}
__device__ __forceinline__ void vr_r(const VRW& W, const char* os, const char* ad, unsigned wa, unsigned ra, unsigned& cap, unsigned& sp, unsigned long long* ts) {
  asm volatile("v_mbcnt_lo_u32_b32 v4, -1, 0\n\t"
    "v_mbcnt_hi_u32_b32 v4, -1, v4\n\t"
    "v_lshlrev_b32 v5, 4, v4\n\t"
    "global_load_dwordx4 v[12:15], v5, %[os] offset:0 sc1\n\t"
    "global_load_dwordx4 v[16:19], v5, %[os] offset:1024 sc1\n\t"
    "global_load_dwordx4 v[20:23], v5, %[os] offset:2048 sc1\n\t"
    "global_load_dwordx4 v[24:27], v5, %[os] offset:3072 sc1\n\t"
    "s_mov_b32 %[sp], 0\n\t"
    "v_and_b32 v10, 1, v4\n\t"
    "v_mul_u32_u24 v10, 0xaaaa, v10\n\t"
    "v_or_b32 v6, %[c44], v10\n\t"
    "v_and_b32 v10, 15, v4\n\t"
    "v_lshl_add_u32 v7, v10, 2, %[wa]\n\t"
    "v_and_b32 v10, 48, v4\n\t"
    "v_mad_u32_u24 v7, v10, 12, v7\n\t"
    "v_and_b32 v10, 32, v4\n\t"
    "v_mul_u32_u24 v8, 12, v10\n\t"
    "v_and_b32 v10, 4, v4\n\t"
    "v_mad_u32_u24 v8, v10, 48, v8\n\t"
    "v_and_b32 v10, 16, v4\n\t"
    "v_lshl_add_u32 v8, v10, 1, v8\n\t"
    "v_and_b32 v10, 3, v4\n\t"
    "v_lshl_add_u32 v8, v10, 3, v8\n\t"
    "v_and_b32 v10, 8, v4\n\t"
    "v_lshlrev_b32 v9, 12, v10\n\t"
    "v_mul_u32_u24 v10, 400, v10\n\t"
    "v_add3_u32 v8, v8, v10, %[ra]\n\t"
    "v_and_b32 v10, 48, v4\n\t"
    "v_lshl_or_b32 v9, v10, 1, v9\n\t"
    "v_and_b32 v10, 7, v4\n\t"
    "v_lshl_or_b32 v9, v10, 2, v9\n\t"
    "3:\n\t"
    "v_mov_b32 v28, 0\n\t"
    "v_mov_b32 v29, 0\n\t"
    "v_mov_b32 v30, 0\n\t"
    "v_mov_b32 v31, 0\n\t"
    "v_mov_b32 v32, 0\n\t"
    "v_mov_b32 v33, 0\n\t"
    "v_mov_b32 v34, 0\n\t"
    "v_mov_b32 v35, 0\n\t"
    "v_mov_b32 v36, 0\n\t"
    "v_mov_b32 v37, 0\n\t"
    "v_mov_b32 v38, 0\n\t"
    "v_mov_b32 v39, 0\n\t"
    "v_mov_b32 v40, 0\n\t"
    "v_mov_b32 v41, 0\n\t"
    "v_mov_b32 v42, 0\n\t"
    "v_mov_b32 v43, 0\n\t"
    "v_mov_b32 v10, 0\n\t"
    "s_waitcnt vmcnt(3)\n\t"
    "v_smfmac_f32_16x16x64_bf16 v[28:31], v[12:15], v[128:135], v6\n\t"
    "v_max3_u32 v10, v10, v12, v13\n\t"
    "v_smfmac_f32_16x16x64_bf16 v[32:35], v[12:15], v[136:143], v6\n\t"
    "v_max3_u32 v10, v10, v14, v15\n\t"
    "v_smfmac_f32_16x16x64_bf16 v[36:39], v[12:15], v[144:151], v6\n\t"
    "v_smfmac_f32_16x16x64_bf16 v[40:43], v[12:15], v[152:159], v6\n\t"
    "s_waitcnt vmcnt(2)\n\t"
    "v_smfmac_f32_16x16x64_bf16 v[28:31], v[16:19], v[160:167], v6\n\t"
    "v_max3_u32 v10, v10, v16, v17\n\t"
    "v_smfmac_f32_16x16x64_bf16 v[32:35], v[16:19], v[168:175], v6\n\t"
    "v_max3_u32 v10, v10, v18, v19\n\t"
    "v_smfmac_f32_16x16x64_bf16 v[36:39], v[16:19], v[176:183], v6\n\t"
    "v_smfmac_f32_16x16x64_bf16 v[40:43], v[16:19], v[184:191], v6\n\t"
    "s_waitcnt vmcnt(1)\n\t"
    "v_smfmac_f32_16x16x64_bf16 v[28:31], v[20:23], v[192:199], v6\n\t"
    "v_max3_u32 v10, v10, v20, v21\n\t"
    "v_smfmac_f32_16x16x64_bf16 v[32:35], v[20:23], v[200:207], v6\n\t"
    "v_max3_u32 v10, v10, v22, v23\n\t"
    "v_smfmac_f32_16x16x64_bf16 v[36:39], v[20:23], v[208:215], v6\n\t"
    "v_smfmac_f32_16x16x64_bf16 v[40:43], v[20:23], v[216:223], v6\n\t"
    "s_waitcnt vmcnt(0)\n\t"
    "v_smfmac_f32_16x16x64_bf16 v[28:31], v[24:27], v[224:231], v6\n\t"
    "v_max3_u32 v10, v10, v24, v25\n\t"
    "v_smfmac_f32_16x16x64_bf16 v[32:35], v[24:27], v[232:239], v6\n\t"
    "v_max3_u32 v10, v10, v26, v27\n\t"
    "v_smfmac_f32_16x16x64_bf16 v[36:39], v[24:27], v[240:247], v6\n\t"
    "v_cmp_eq_u32 vcc, -1, v10\n\t"
    "v_smfmac_f32_16x16x64_bf16 v[40:43], v[24:27], v[248:255], v6\n\t"
    "s_cbranch_vccnz 4f\n\t"
    "5:\n\t"
    "s_cmp_ge_u32 %[wa], 1536\n\t"
    "s_nop 1\n\t"
    "v_add_f32 v28, v28, v29\n\t"
    "v_add_f32 v32, v32, v33\n\t"
    "v_add_f32 v36, v38, v39\n\t"
    "v_add_f32 v28, v28, v36\n\t"
    "v_add_f32 v40, v42, v43\n\t"
    "v_add_f32 v32, v32, v40\n\t"
    "ds_write2_b32 v7, v28, v32 offset1:16\n\t"
    "s_waitcnt lgkmcnt(0)\n\t"
    "s_barrier\n\t"
    "s_cbranch_scc1 9f\n\t"
    "ds_read_b64 v[44:45], v8 offset:0\n\t"
    "ds_read_b64 v[46:47], v8 offset:768\n\t"
    "ds_read_b64 v[48:49], v8 offset:1536\n\t"
    "ds_read_b64 v[50:51], v8 offset:2304\n\t"
    "s_waitcnt lgkmcnt(2)\n\t"
    "v_pk_add_f32 v[44:45], v[44:45], v[46:47]\n\t"
    "s_waitcnt lgkmcnt(0)\n\t"
    "v_pk_add_f32 v[48:49], v[48:49], v[50:51]\n\t"
    "v_pk_add_f32 v[44:45], v[44:45], v[48:49]\n\t"
    "s_nop 1\n\t"
    "v_add_f32_dpp v44, v44, v44 row_ror:8 row_mask:0xf bank_mask:0xf\n\t"
    "v_add_f32_dpp v45, v45, v45 row_ror:8 row_mask:0xf bank_mask:0xf\n\t"
    "v_cvt_pk_bf16_f32 v10, v44, v45\n\t"
    "v_min_u32 v10, -2, v10\n\t"
    "global_store_dword v9, v10, %[ad] sc1\n\t"
    "s_waitcnt lgkmcnt(0)\n\t"
    "s_branch 9f\n\t"
    "4:\n\t"
    "s_add_u32 %[sp], %[sp], 1\n\t"
    "s_cmp_ge_u32 %[sp], %[cap]\n\t"
    "s_cbranch_scc1 7f\n\t"
    "global_load_dwordx4 v[12:15], v5, %[os] offset:0 sc1\n\t"
    "global_load_dwordx4 v[16:19], v5, %[os] offset:1024 sc1\n\t"
    "global_load_dwordx4 v[20:23], v5, %[os] offset:2048 sc1\n\t"
    "global_load_dwordx4 v[24:27], v5, %[os] offset:3072 sc1\n\t"
    "s_nop 3\n\t"
    "s_branch 3b\n\t"
    "7:\n\t"
    "s_mov_b32 %[cap], 0\n\t"
    "s_branch 5b\n\t"
    "9:"
    : [sp] "=&s"(sp), [cap] "+s"(cap)
    : [os] "s"(os), [ad] "s"(ad), [wa] "s"(wa), [ra] "s"(ra), [c44] "s"(0x4444u), "{v[64:95]}"(W.k0), "{v[96:127]}"(W.k1), "{v[128:159]}"(W.o0), "{v[160:191]}"(W.o1), "{v[192:223]}"(W.o2), "{v[224:255]}"(W.o3)
    : "v4", "v5", "v6", "v7", "v8", "v9", "v10", "v11", "v12", "v13", "v14", "v15", "v16", "v17", "v18", "v19", "v20", "v21", "v22", "v23", "v24", "v25", "v26", "v27", "v28", "v29", "v30", "v31", "v32", "v33", "v34", "v35", "v36", "v37", "v38", "v39", "v40", "v41", "v42", "v43", "v44", "v45", "v46", "v47", "v48", "v49", "v50", "v51", "v52", "v53", "v54", "v55", "v56", "v57", "v58", "v59", "vcc", "scc", "memory");
}
__device__ __forceinline__ void kr_load_k0(VRW& W, unsigned a) {
  asm volatile("ds_read_b64_tr_b16 v[64:65], %[a] offset:0\n\t"
    "ds_read_b64_tr_b16 v[66:67], %[a] offset:256\n\t"
    "ds_read_b64_tr_b16 v[68:69], %[a] offset:4096\n\t"
    "ds_read_b64_tr_b16 v[70:71], %[a] offset:4352\n\t"
    "ds_read_b64_tr_b16 v[80:81], %[a] offset:32\n\t"
    "ds_read_b64_tr_b16 v[82:83], %[a] offset:288\n\t"
    "ds_read_b64_tr_b16 v[84:85], %[a] offset:4128\n\t"
    "ds_read_b64_tr_b16 v[86:87], %[a] offset:4384\n\t"
    "s_waitcnt lgkmcnt(0)\n\t"
    "ds_read_b64_tr_b16 v[96:97], %[a] offset:64\n\t"
    "ds_read_b64_tr_b16 v[98:99], %[a] offset:320\n\t"
    "ds_read_b64_tr_b16 v[100:101], %[a] offset:4160\n\t"
    "ds_read_b64_tr_b16 v[102:103], %[a] offset:4416\n\t"
    "ds_read_b64_tr_b16 v[112:113], %[a] offset:96\n\t"
    "ds_read_b64_tr_b16 v[114:115], %[a] offset:352\n\t"
    "ds_read_b64_tr_b16 v[116:117], %[a] offset:4192\n\t"
    "ds_read_b64_tr_b16 v[118:119], %[a] offset:4448\n\t"
    "s_waitcnt lgkmcnt(0)"
    : "=&{v[64:95]}"(W.k0), "=&{v[96:127]}"(W.k1) : [a] "v"(a) : "memory");
}
__device__ __forceinline__ void kr_load_k1(VRW& W, unsigned a) {
  asm volatile("ds_read_b64_tr_b16 v[72:73], %[a] offset:0\n\t"
    "ds_read_b64_tr_b16 v[74:75], %[a] offset:256\n\t"
    "ds_read_b64_tr_b16 v[76:77], %[a] offset:4096\n\t"
    "ds_read_b64_tr_b16 v[78:79], %[a] offset:4352\n\t"
    "ds_read_b64_tr_b16 v[88:89], %[a] offset:32\n\t"
    "ds_read_b64_tr_b16 v[90:91], %[a] offset:288\n\t"
    "ds_read_b64_tr_b16 v[92:93], %[a] offset:4128\n\t"
    "ds_read_b64_tr_b16 v[94:95], %[a] offset:4384\n\t"
    "s_waitcnt lgkmcnt(0)\n\t"
    "ds_read_b64_tr_b16 v[104:105], %[a] offset:64\n\t"
    "ds_read_b64_tr_b16 v[106:107], %[a] offset:320\n\t"
    "ds_read_b64_tr_b16 v[108:109], %[a] offset:4160\n\t"
    "ds_read_b64_tr_b16 v[110:111], %[a] offset:4416\n\t"
    "ds_read_b64_tr_b16 v[120:121], %[a] offset:96\n\t"
    "ds_read_b64_tr_b16 v[122:123], %[a] offset:352\n\t"
    "ds_read_b64_tr_b16 v[124:125], %[a] offset:4192\n\t"
    "ds_read_b64_tr_b16 v[126:127], %[a] offset:4448\n\t"
    "s_waitcnt lgkmcnt(0)"
    : "+&{v[64:95]}"(W.k0), "+&{v[96:127]}"(W.k1) : [a] "v"(a) : "memory");
}
__device__ __forceinline__ void kr_load_o0(VRW& W, unsigned a) {
  asm volatile("ds_read_b64_tr_b16 v[128:129], %[a] offset:0\n\t"
    "ds_read_b64_tr_b16 v[130:131], %[a] offset:128\n\t"
    "ds_read_b64_tr_b16 v[132:133], %[a] offset:1024\n\t"
    "ds_read_b64_tr_b16 v[134:135], %[a] offset:1152\n\t"
    "ds_read_b64_tr_b16 v[136:137], %[a] offset:32\n\t"
    "ds_read_b64_tr_b16 v[138:139], %[a] offset:160\n\t"
    "ds_read_b64_tr_b16 v[140:141], %[a] offset:1056\n\t"
    "ds_read_b64_tr_b16 v[142:143], %[a] offset:1184\n\t"
    "s_waitcnt lgkmcnt(0)\n\t"
    "ds_read_b64_tr_b16 v[144:145], %[a] offset:4096\n\t"
    "ds_read_b64_tr_b16 v[146:147], %[a] offset:4224\n\t"
    "ds_read_b64_tr_b16 v[148:149], %[a] offset:5120\n\t"
    "ds_read_b64_tr_b16 v[150:151], %[a] offset:5248\n\t"
    "ds_read_b64_tr_b16 v[152:153], %[a] offset:4128\n\t"
    "ds_read_b64_tr_b16 v[154:155], %[a] offset:4256\n\t"
    "ds_read_b64_tr_b16 v[156:157], %[a] offset:5152\n\t"
    "ds_read_b64_tr_b16 v[158:159], %[a] offset:5280\n\t"
    "s_waitcnt lgkmcnt(0)"
    : "=&{v[128:159]}"(W.o0) : [a] "v"(a) : "memory");
}
__device__ __forceinline__ void kr_load_o1(VRW& W, unsigned a) {
  asm volatile("ds_read_b64_tr_b16 v[160:161], %[a] offset:0\n\t"
    "ds_read_b64_tr_b16 v[162:163], %[a] offset:128\n\t"
    "ds_read_b64_tr_b16 v[164:165], %[a] offset:1024\n\t"
    "ds_read_b64_tr_b16 v[166:167], %[a] offset:1152\n\t"
    "ds_read_b64_tr_b16 v[168:169], %[a] offset:32\n\t"
    "ds_read_b64_tr_b16 v[170:171], %[a] offset:160\n\t"
    "ds_read_b64_tr_b16 v[172:173], %[a] offset:1056\n\t"
    "ds_read_b64_tr_b16 v[174:175], %[a] offset:1184\n\t"
    "s_waitcnt lgkmcnt(0)\n\t"
    "ds_read_b64_tr_b16 v[176:177], %[a] offset:4096\n\t"
    "ds_read_b64_tr_b16 v[178:179], %[a] offset:4224\n\t"
    "ds_read_b64_tr_b16 v[180:181], %[a] offset:5120\n\t"
    "ds_read_b64_tr_b16 v[182:183], %[a] offset:5248\n\t"
    "ds_read_b64_tr_b16 v[184:185], %[a] offset:4128\n\t"
    "ds_read_b64_tr_b16 v[186:187], %[a] offset:4256\n\t"
    "ds_read_b64_tr_b16 v[188:189], %[a] offset:5152\n\t"
    "ds_read_b64_tr_b16 v[190:191], %[a] offset:5280\n\t"
    "s_waitcnt lgkmcnt(0)"
    : "=&{v[160:191]}"(W.o1) : [a] "v"(a) : "memory");
}
__device__ __forceinline__ void kr_load_o2(VRW& W, unsigned a) {
  asm volatile("ds_read_b64_tr_b16 v[192:193], %[a] offset:0\n\t"
    "ds_read_b64_tr_b16 v[194:195], %[a] offset:128\n\t"
    "ds_read_b64_tr_b16 v[196:197], %[a] offset:1024\n\t"
    "ds_read_b64_tr_b16 v[198:199], %[a] offset:1152\n\t"
    "ds_read_b64_tr_b16 v[200:201], %[a] offset:32\n\t"
    "ds_read_b64_tr_b16 v[202:203], %[a] offset:160\n\t"
    "ds_read_b64_tr_b16 v[204:205], %[a] offset:1056\n\t"
    "ds_read_b64_tr_b16 v[206:207], %[a] offset:1184\n\t"
    "s_waitcnt lgkmcnt(0)\n\t"
    "ds_read_b64_tr_b16 v[208:209], %[a] offset:4096\n\t"
    "ds_read_b64_tr_b16 v[210:211], %[a] offset:4224\n\t"
    "ds_read_b64_tr_b16 v[212:213], %[a] offset:5120\n\t"
    "ds_read_b64_tr_b16 v[214:215], %[a] offset:5248\n\t"
    "ds_read_b64_tr_b16 v[216:217], %[a] offset:4128\n\t"
    "ds_read_b64_tr_b16 v[218:219], %[a] offset:4256\n\t"
    "ds_read_b64_tr_b16 v[220:221], %[a] offset:5152\n\t"
    "ds_read_b64_tr_b16 v[222:223], %[a] offset:5280\n\t"
    "s_waitcnt lgkmcnt(0)"
    : "=&{v[192:223]}"(W.o2) : [a] "v"(a) : "memory");
}
__device__ __forceinline__ void kr_load_o3(VRW& W, unsigned a) {
  asm volatile("ds_read_b64_tr_b16 v[224:225], %[a] offset:0\n\t"
    "ds_read_b64_tr_b16 v[226:227], %[a] offset:128\n\t"
    "ds_read_b64_tr_b16 v[228:229], %[a] offset:1024\n\t"
    "ds_read_b64_tr_b16 v[230:231], %[a] offset:1152\n\t"
    "ds_read_b64_tr_b16 v[232:233], %[a] offset:32\n\t"
    "ds_read_b64_tr_b16 v[234:235], %[a] offset:160\n\t"
    "ds_read_b64_tr_b16 v[236:237], %[a] offset:1056\n\t"
    "ds_read_b64_tr_b16 v[238:239], %[a] offset:1184\n\t"
    "s_waitcnt lgkmcnt(0)\n\t"
    "ds_read_b64_tr_b16 v[240:241], %[a] offset:4096\n\t"
    "ds_read_b64_tr_b16 v[242:243], %[a] offset:4224\n\t"
    "ds_read_b64_tr_b16 v[244:245], %[a] offset:5120\n\t"
    "ds_read_b64_tr_b16 v[246:247], %[a] offset:5248\n\t"
    "ds_read_b64_tr_b16 v[248:249], %[a] offset:4128\n\t"
    "ds_read_b64_tr_b16 v[250:251], %[a] offset:4256\n\t"
    "ds_read_b64_tr_b16 v[252:253], %[a] offset:5152\n\t"
    "ds_read_b64_tr_b16 v[254:255], %[a] offset:5280\n\t"
    "s_waitcnt lgkmcnt(0)"
    : "=&{v[224:255]}"(W.o3) : [a] "v"(a) : "memory");
}
#define XPTRS(l) const unsigned par_ = ini_resolve(ws, l, tag); char* xch_ = xch_base(ws); \
  const char* xb = uni(xch_ + ((size_t)3 << 21) + (((par_ >> 1) & 1) << 13)); const char* ob = uni(xch_ + ((par_ & 1) << 13)); \
  unsigned* cnt = (unsigned*)(xch_ + ((size_t)4 << 21) + ((par_ >> 2) << 13))
__device__ __forceinline__ void qk_body(char* smem, int wave_id_, int xg, int h, int wg, const u16* __restrict__ Wq, const u16* __restrict__ Wv,
                                        char* ws, unsigned* tmo, unsigned long long tag) {
  const int wave = WAVE_ID;
  const int ty = wave >> 2, w4 = wave & 3;
  const char* wq = uni((const char*)(Wq + h * DH)); const char* wv = uni((const char*)(Wv + h * DH));
  auto stage2 = [&](int i) {
    const int tid = wave * 64 + lane_id();
    if (i < 6) {
      stage_proj<false>(wq + (size_t)i * 128 * HD * 2, Q_RING + ((2 * i) & 3) * 32768, tid, wave);
      if (i < 2) stage_proj<false>(wq + (size_t)(i + 6) * 128 * HD * 2, Q_RING + ((2 * i + 1) & 3) * 32768, tid, wave);
      else stage_proj<true>(wv + (size_t)(i - 2) * 256 * HD * 2, Q_RING + ((2 * i + 1) & 3) * 32768, tid, wave);
    }
  };
  QKW W;
  delay64<QDL>();
  stage2(0); stage2(1);
#define WLD(i, w) do { if ((i) < 5) WAIT_V(8); else WAIT_V(0); \
    WBAR(); { const int l_ = lane_id(); const unsigned o_ = Q_RING + ty * 32768 + ((i) & 1) * 65536; const bool v_ = ty && (i) >= 2; \
      qk_wload<i>(w, proj_base(v_ ? w4 : 2 * w4, l_) + o_, proj_base(v_ ? w4 + 4 : 2 * w4 + 1, l_) + o_); } \
    WBAR(); stage2((i) + 2); if (wave == 0) STS(ws, wg, 1 + (i)); if ((i) == 4 && wave == 7) ini_request(ws, 30720); } while (0)
  WLD(0, W.w0); WLD(1, W.w1); WLD(2, W.w2); WLD(3, W.w3); WLD(4, W.w4); WLD(5, W.w5);
#undef WLD
  XPTRS(smem + 30720);
  if (opaque_tid() == 64) st4_sc1(cnt + (PQKV + h) * 32 + 3, 1u);
  const char* oh = ob + (size_t)(h >> 2) * 16384 + (h & 3) * 1024;
  auto rearm = [&](int ps) {
    unsigned z = SENT; asm volatile("" : "+v"(z));
    const unsigned l16 = lane_id() * 16;
#pragma unroll
    for (int r = 0; r < 2 * OREP; ++r) st16s(oh + (size_t)(ps * 4 + (r >> 1)) * 131072, (r & 1) * 4096u + l16, u32x4{z, z, z, z});
  };
  const char* dbw = xb + (size_t)48 * 16384 + 4096 + h * 128;
  if (wave == 4) *(volatile LAS u32x4*)(smem + Q_TM) = u32x4{0u, 0u, 0u, (unsigned)__builtin_amdgcn_s_memrealtime()};
  if (wave == 6) *(u32x4*)(smem + Q_PQ + 4096 + lane_id() * 16) = u32x4{0u, 0u, 0u, 0u};
  BAR();
  if (wave == 0) STS(ws, wg, 0);
  unsigned avail = 0;
  auto kvq = [&](int s) { return ws + (wave == 5 ? OFF_K : (wave == 6 ? OFF_V : OFF_Q)) + ((size_t)s * NH + h) * 8192; };
  auto stage_kv = [&](int s, int buf) {
    if (avail <= (unsigned)s) {
      for (int spins = 0;; ++spins) {
        avail = qkv_avail<0>(cnt + (PQKV + h) * 32);
        if (avail > (unsigned)s) break;
        __builtin_amdgcn_s_sleep(8);
        if (give_up(spins, tmo, 5)) break;
      }
    }
    const unsigned lane = lane_id(), v0 = (lane ^ ((lane >> 4) & 2)) * 16;
    const char* src = uni(kvq(s));
#pragma unroll
    for (int i = 0; i < 4; ++i) glds16x2s<0>(src + i * 2048, v0, Q_K + buf * 32768 + (wave - 5) * 8192 + i * 2048);
  };
  auto loop = [&](auto w_c) __attribute__((always_inline)) {
    constexpr int WV = decltype(w_c)::value;
    if (WV >= 5 && !DRY) stage_kv(0, 0);
    unsigned t0 = 0, cap = 60000, d0 = D_0, d1 = D_0;
    int dq = 0, e0 = D_Q, e1 = D_Q;
    unsigned tp = 0;
    int sl = DRY ? 2 : 0;
    unsigned fc = 0;
    for (int s = -DRY; s < NSEG; ++s) {
      TSDECL;
      const bool dry = DRY && s < 0;
      const char* dump = ws + OFF_DUMP + 262144;
      const char* ag = xb + (size_t)(((dry ? 0 : sl * 4) + (AREP == 4 ? xg : xg >> 1)) * 2 + (WV >> 2)) * 16384 + (WV < 4 ? WV * 2048 : WV * 1024);
      const int nl = sl == 2 ? 0 : sl + 1;
      const unsigned pb = sl * 32768;
      const char* vs = dry ? (WV < 4 ? ag : dump) : WV == 4 ? dbw : WV == 7 ? (const char*)(cnt + (PROG + h) * 32) : xb + (size_t)(24 + sl * 8 + (h >> 2)) * 16384 + (h & 3) * 2048;
      const char* v2 = dry ? ag : xb + (size_t)(52 + sl * 4 + (h >> 3)) * 16384 + (h & 7) * 1024 + (WV & 1) * 512;
      const char* od = dry ? dump : oh + (size_t)sl * 524288;
      const unsigned sn = (unsigned)(dry ? 255 : s + 1) << 24;
      if (s > 0) wait_rt(t0, dq);
      TS(0);
      unsigned long long tc; unsigned sp;
      qk_step<WV>(W, ag, vs, v2, dry ? dump : dbw, od, pb, sn, cap, sp, tc, nullptr, d0, d1);
      if (dry) d0 = D_0;
      dq = (sp & 0xFFFFu) ? min(dq + DQU, DQM) : max(dq - DQD, 0);
      if (WV >= 5) {
        if (RAE && WV == RAW && s > 0 && s < NSEG - 1) rearm(sl == 0 ? 2 : sl - 1);
        WBAR();
        delay64<E_S>();
        if (s + 1 < NSEG) stage_kv(s + 1, nl);
      }
      if (!RAE && WV == RAW && s > 0 && s < NSEG - 1) rearm(sl == 0 ? 2 : sl - 1);
      unsigned long long* tsp = nullptr;
      seg_attn<WV>(W, pb, dry ? dump : ws + OFF_O + ((size_t)s * 64 + 2 * h + (WV >> 1)) * 4096, tsp);
      if (WV == 1) TSFLUSH(0x8000);
      if (WV < 4 && s >= BEARLY && lane_id() == 0 && (__hip_atomic_fetch_add((LAS unsigned*)(smem + Q_TM + 8), 1u, __ATOMIC_RELAXED, __HIP_MEMORY_SCOPE_WORKGROUP) & 3u) == 3u)
        st4_sc1(cnt + (PROG + h) * 32 + 1, (unsigned)(s + 1));
      if (WV == 6 && s == BEARLY - 8) wait_arm(cnt, tmo);
      if (s < NSEG - 1 && !dry) {
        if (WV == 4) {
          delay64<E_B>();
          u32x2 v;
          for (unsigned spins = 0;; ++spins) {
            const int lane = lane_id();
            asm volatile("s_nop 4\n\tglobal_load_dwordx2 %0, %1, %2 sc1\n\ts_waitcnt vmcnt(0)" : "=&v"(v) : "v"((lane & 31) * 128 + (lane >> 5) * 8), "s"(xb + (size_t)(48 + xg) * 16384 + 4096) : "memory");
            if (__all((v.x >> 24) > (unsigned)s) || spins >= cap) break;
          }
          const unsigned now = (unsigned)__builtin_amdgcn_s_memrealtime();
          const unsigned age = rowmin16((now - v.x) & 0xFFFFFFu);
          const unsigned tb = __builtin_amdgcn_readfirstlane(now - min(min(min(rdl(age, 0), rdl(age, 16)), min(rdl(age, 32), rdl(age, 48))), 500u));
          t0 = tb + min(e0, e1);
          *(volatile LAS u32x2*)(smem + Q_TM) = u32x2{(unsigned)(s + 1), t0};
          if (ADQ && s > 1 && __all((v.y >> 24) == (unsigned)s)) {
            const unsigned ag2 = rowmin16((now - v.y) & 0xFFFFFFu);
            e1 = e0;
            e0 = __builtin_amdgcn_readfirstlane(min(max((int)(now - min(min(rdl(ag2, 0), rdl(ag2, 16)), min(rdl(ag2, 32), rdl(ag2, 48))) - tp) + D_V, D_Q - DQM), D_Q + DQM));
          }
          tp = tb;
          asm volatile("" : "+s"(t0), "+s"(e0), "+s"(e1), "+s"(tp));
          TS(14);
          TSFLUSH(0x4000);
        } else {
          for (;;) {
            const u32x2 v = *(volatile LAS u32x2*)(smem + Q_TM);
            t0 = __builtin_amdgcn_readfirstlane(v.y);
            if (__builtin_amdgcn_readfirstlane(v.x) == (unsigned)(s + 1)) break;
            __builtin_amdgcn_s_sleep(1);
          }
        }
      }
      sl = sl == 2 ? 0 : sl + 1;
    }
    WAIT_V(0);
    WBAR();
    if (WV == 7 && lane_id() == 0) st4_sc1(cnt + (PROG + h) * 32, (unsigned)NSEG);
    if (cap == 0 && lane_id() == 0) st4_sc1(tmo, 1u);
    FCS();
  };
  switch (wave) {
    case 0: loop(std::integral_constant<int, 0>{}); break;
    case 1: loop(std::integral_constant<int, 1>{}); break;
    case 2: loop(std::integral_constant<int, 2>{}); break;
    case 3: loop(std::integral_constant<int, 3>{}); break;
    case 4: loop(std::integral_constant<int, 4>{}); break;
    case 5: loop(std::integral_constant<int, 5>{}); break;
    case 6: loop(std::integral_constant<int, 6>{}); break;
    default: loop(std::integral_constant<int, 7>{}); break;
  }
  __builtin_amdgcn_s_setprio(0);
}

__device__ __forceinline__ void vr_body(char* smem, int wave_id_, int xg, int h, int c, int wg, const u16* __restrict__ Wk, const u16* __restrict__ Wv,
                                        const u16* __restrict__ Wo, char* ws, unsigned* tmo, unsigned long long tag) {
  const int wave = WAVE_ID;
  const unsigned po = (unsigned)(h >> 4) * 16384u + ((h >> 1) & 7) * 1024 + (h & 1) * 512 + c * 128;
  const unsigned ko_ = (unsigned)(24 + (h >> 2)) * 16384u + (h & 3) * 2048 + c * 1024;
  const unsigned vo_ = (unsigned)(52 + (h >> 3)) * 16384u + (h & 7) * 1024 + c * 512;
  const int hg = (wave + ROTW * ((2 * h + c) & 3)) & 7;
  VRW W;
  {
    const int lane = lane_id();
    const int g = lane >> 4, q4 = (lane & 15) >> 2, p4 = lane & 3, fr = 4 * g + (q4 & 1) + 32 * (q4 >> 1);
    const unsigned lw = wave * 16384;
    const char* sk = uni((const char*)(Wk + (size_t)(128 * wave) * HD + h * DH + 64 * c));
    const char* sv_ = uni((const char*)(Wv + (size_t)(128 * wave) * HD + h * DH + 64 + 32 * c));
    const char* so = uni((const char*)(Wo + (size_t)(512 * hg) * DM + 32 * h));
    const int kp = lane >> 2, sr = 32 * (kp >> 3) + 16 * ((kp >> 2) & 1) + (kp & 3);
    auto issue = [&](int p) {
#pragma unroll
      for (int i = 0; i < 8; ++i) {
        const unsigned d = lw + (p & 1) * 8192 + i * 1024;
        if (p < 2) gldsw(sk + (size_t)(8 * p + i) * 8 * HD * 2, (unsigned)((lane >> 3) * HD * 2 + (lane & 7) * 16), d);
        else if (p < 6 && (NTM & 8)) gldsw(so + (size_t)(128 * (p - 2) + 64 * (i & 1) + 4 * (i >> 1)) * DM * 2, (unsigned)(sr * DM * 2 + p4 * 16), d);
        else if (p < 6) glds16s<0>(so + (size_t)(128 * (p - 2) + 64 * (i & 1) + 4 * (i >> 1)) * DM * 2, (unsigned)(sr * DM * 2 + p4 * 16), d);
        else if (NTM & 16) gldsw(sv_ + (size_t)i * 16 * HD * 2, (unsigned)(kp * HD * 2 + p4 * 16), d);
        else glds16s<0>(sv_ + (size_t)i * 16 * HD * 2, (unsigned)(kp * HD * 2 + p4 * 16), d);
      }
    };
#define VTS(p) do { } while (0)
    const int fm = 16 * (g >> 1) + 4 * (g & 1) + (q4 & 1) + 8 * (q4 >> 1);
    const unsigned ak = lw + fm * 128 + p4 * 8, ao = lw + fr * 64 + p4 * 8;
#define KB() do { if (KFB) WBAR(); } while (0)
    issue(2); KB(); issue(3);
    WAIT_V(8); VTS(1); kr_load_o0(W, ao); KB(); issue(4);
    WAIT_V(8); VTS(2); kr_load_o1(W, ao + 8192); KB(); issue(5);
    WAIT_V(8); VTS(3); kr_load_o2(W, ao); KB(); issue(0);
    WAIT_V(8); VTS(4); kr_load_o3(W, ao + 8192); KB(); issue(1);
    WAIT_V(8); VTS(5); kr_load_k0(W, ak); KB(); issue(6);
    WAIT_V(8); VTS(6); kr_load_k1(W, ak + 8192);
    if (wave == 0) ini_request(ws, V_PV);
    WAIT_V(0);
    u32x8 x[4];
#pragma unroll
    for (int j = 0; j < 4; ++j) {
      const unsigned a = lw + fm * 64 + p4 * 8 + (j & 1) * 4096 + (j >> 1) * 32;
      const u32x2 t0 = trread(smem, a), t1 = trread(smem, a + 128), t2 = trread(smem, a + 2048), t3 = trread(smem, a + 2176);
      x[j] = u32x8{t0.x, t0.y, t1.x, t1.y, t2.x, t2.y, t3.x, t3.y};
    }
    char* d = smem + lw + 8192 + lane * 16;
#pragma unroll
    for (int j = 0; j < 4; ++j) { *(u32x4*)(d + j * 2048) = u32x4{x[j][0], x[j][1], x[j][2], x[j][3]}; *(u32x4*)(d + j * 2048 + 1024) = u32x4{x[j][4], x[j][5], x[j][6], x[j][7]}; }
    VTS(7);
  }
  BAR();
  XPTRS(smem + V_PV);
  (void)cnt;
  auto rearm = [&](int ps) {
    const int lane = lane_id();
    unsigned z = SENT; asm volatile("" : "+v"(z));
    const u32x4 sv = u32x4{z, z, z, z};
    static_assert(AREP == 2, "");
    st16s(xb, lane < 32 ? (unsigned)(ps * 8 + (lane >> 4) * 2) * 16384u + po + ((lane >> 3) & 1) * 256 + (lane & 7) * 16 : (unsigned)ps * 65536u + vo_ + (lane & 31) * 16, sv);
    st16s(xb, (unsigned)ps * 131072u + ko_ + lane * 16, sv);
  };
  if (wave == 0) STS(ws, wg, 0);
  __builtin_amdgcn_s_setprio(2);
  {
    unsigned cap = 60000, tr = 0, fc = 0, tm = 0;
    int da = D_A, dd = D_O;
    int sl = 0;
    for (int s = 0; s < NSEG; ++s) {
      TSDECL;
      const int sn = sl == 2 ? 0 : sl + 1;
      const char* ag = xb + (size_t)((sl * 4 + (AREP == 4 ? xg : xg >> 1)) * 2 + (wave >> 2)) * 16384 + (wave & 3) * 2048;
      const char* kd = xb + (size_t)sl * 131072 + ko_ + wave * 128;
      const char* vd = xb + (size_t)sl * 65536 + vo_ + (wave & 3) * 128;
      const char* os = ob + (size_t)((sl * 4 + ORSEL) * 8 + hg) * 16384 + c * 4096;
      const char* ad = xb + (size_t)(sn * 8) * 16384 + po + (wave & 1) * 256;
      if (s > 0) { if (wave < AREP) delay64<E_AG - E_W>(); else delay64<E_AG>(); }
      TS(0);
      unsigned long long tc; unsigned sp;
      unsigned long long* tsp = nullptr;
      vr_v(W, ag, kd, vd, wave * 16384 + 8192, V_P1 + wave * 2304 + (wave >> 2) * 64, V_PV + wave * 1024 + (wave >> 2) * 128, V_P1 + (wave >> 1) * 32 + (wave & 1) * 1152,
           V_PV + (wave & 3) * 256, cap, sp, tc, tsp);
      TN(12, sp + 1);
      if (s > 3 && sp) fc += 1u;
      da = sp ? min(da + DQU, D_A + DQM) : max(da - DQD, D_A);
      if (s == NSEG - 1) break;
      if (ADQ && wave == 0 && s > 0) { const int l_ = lane_id(); if (l_ < 3) st4s(xb, (unsigned)(48 + l_) * 16384u + 4096 + h * 128 + c * 8 + 4, (tm & 0xFFFFFFu) | (unsigned)s << 24); }
      if (wave == 7 && s > 0) rearm(sl == 0 ? 2 : sl - 1);
      wait_clk((unsigned)tc, E_O);
      TS(3);
      const unsigned tb = bell_time(xb + (size_t)(48 + xg) * 16384 + hg * 512, (lane_id() & 3) * 128, (unsigned)(s + 1), cap);
      tr = tb;
      wait_rt(tb, dd);
      TS(4);
      vr_r(W, os, ad, V_P2 + wave * 768 + (wave >> 2) * 128, V_P2 + (wave & 1) * 64, cap, sp, tsp);
      if (ADQ && wave == 0) tm = (unsigned)__builtin_amdgcn_s_memrealtime();
      if (s > 3 && sp) fc += 0x10000u;
      dd = sp ? min(dd + DOU, D_O + DOM) : max(dd - DQD, D_O);
      TN(14, (sp & 0xFFFF) + 1);
      if (wave == 1) TSFLUSH(0x6078);
      sl = sn;
    }
    if (cap == 0 && lane_id() == 0) st4_sc1(tmo, 2u);
    FCS();
  }
  __builtin_amdgcn_s_setprio(0);
}
#define NCS 6
__device__ __forceinline__ int calibrate(char* ws, int i) {
  const int t = lane_id(), ci = (i & 15) % 3, w = (i >> 4) * 4 + (i & 15) / 3, chunk = ci == 0 ? 0 : ci + 2;
  char* base = xch_base(ws) + ((size_t)chunk << 21) + 127 * 16384 + w * 512;
  const char* sb[2] = {uni(base + (w & 1) * 8192), uni(base + (~w & 1) * 8192)};
  if ((t & 31) < 24) st16_sc1(base + (t >> 5) * 8192 + (t & 31) * 16, u32x4{SENT, SENT, SENT, SENT});
  unsigned v, best[2] = {1u << 30, 1u << 30};
  asm volatile("s_load_dword %0, %1, 384 glc\n\ts_waitcnt vmcnt(0) lgkmcnt(0)" : "=&s"(v) : "s"(sb[0]) : "memory");
#pragma unroll
  for (int r = 0; r < NCS; ++r) {
    unsigned long long t0, t1;
    asm volatile("s_memtime %0\n\ts_waitcnt lgkmcnt(0)\n\ts_load_dword %2, %3, %4 glc\n\ts_waitcnt lgkmcnt(0)\n\ts_memtime %1\n\ts_waitcnt lgkmcnt(0)"
                 : "=&s"(t0), "=&s"(t1), "=&s"(v) : "s"(sb[r & 1]), "i"((r >> 1) * 128) : "memory");
    best[r & 1] = min(best[r & 1], (unsigned)(t1 - t0));
  }
  return (w & 1) ? (int)best[0] - (int)best[1] : (int)best[1] - (int)best[0];
}
__device__ __forceinline__ void init_sums(char* smem, int t, int i, const u16* __restrict__ cvec, const u16* __restrict__ wmod) {
  const int cg = t & 15, r0 = t >> 4, k0 = (i >> 4) * 256 + r0 * 16;
  u32x4 w[16];
#pragma unroll
  for (int j = 0; j < 16; ++j) w[j] = ldnt<1>(wmod + (size_t)(k0 + j) * (6 * DM) + (i & 15) * 128 + cg * 8);
  const u32x4 c0 = *(const u32x4*)(cvec + k0), c1 = *(const u32x4*)(cvec + k0 + 8);
  f32x2 acc[4] = {f32x2{0, 0}, f32x2{0, 0}, f32x2{0, 0}, f32x2{0, 0}};
#pragma unroll
  for (int j = 0; j < 16; ++j) {
    const unsigned cw = j < 8 ? c0[(j >> 1) & 3] : c1[(j >> 1) & 3];
    const float c = (j & 1) ? bfhi(cw) : bflo(cw), sv = bfr(c / (1.0f + __expf(-c)));
#pragma unroll
    for (int e = 0; e < 4; ++e) acc[e] = pkfma(f32x2{sv, sv}, f32x2{bflo(w[j][e]), bfhi(w[j][e])}, acc[e]);
  }
  float* red = (float*)smem;
  *(f32x4*)(red + r0 * 128 + cg * 8) = f32x4{acc[0][0], acc[0][1], acc[1][0], acc[1][1]};
  *(f32x4*)(red + r0 * 128 + cg * 8 + 4) = f32x4{acc[2][0], acc[2][1], acc[3][0], acc[3][1]};
}
__device__ __forceinline__ void init_role(char* smem, int wave_id_, int i, const u16* __restrict__ mem0, char* ws, unsigned long long tag, unsigned vote) {
  const int t = opaque_tid();
  char* xch = xch_base(ws);
  if (t < 128) {
    const float* red = (const float*)smem;
    float s = 0;
#pragma unroll
    for (int r = 0; r < 16; ++r) s += red[r * 128 + t];
    st4_sc1(ws + OFF_MOD + ((i >> 4) * (2 * DM) + (i & 15) * 128 + t) * 4, __builtin_bit_cast(unsigned, s));
  }
  static_assert(OREP == 2 && AREP == 2, "");
  for (int b = i; b < 105; b += 64) {
    if (b < 101) {
      char* p = b < 48 ? xch + (size_t)((b >> 4) * 32 + (b & 15)) * 16384
              : (b < 56 ? xch + ((size_t)3 << 21) + (size_t)(8 + ((b - 48) >> 2) * 8 + (b & 3)) * 16384
              : (b < 96 ? xch + ((size_t)3 << 21) + (size_t)(b - 56 + 24) * 16384 : xch + ((size_t)4 << 21) + (size_t)(b - 96) * 16384));
      const unsigned v = b < 80 || (b >= 84 && b < 96) ? SENT : 0u;
#pragma unroll
      for (int j = 0; j < 2; ++j) st16_sc1(p + j * 8192 + t * 16, u32x4{v, v, v, v});
    } else {
      const int rep = (b - 101) & 1, par = (b - 101) >> 1;
      if (b == 101 && t == 0) st4_sc1(ws + OFF_TMO, 0u);
#pragma unroll
      for (int j = 0; j < 2; ++j) {
        const int u = t + 512 * j, lane = u & 63, g = lane >> 4, hf = (lane >> 1) & 1, row = ((lane >> 1) & 6) + (lane & 1);
        u32x4 v = *(const u32x4*)(mem0 + row * DM + 64 * (u >> 6) + 16 * g + 8 * hf);
        v.x = nosent(v.x); v.y = nosent(v.y); v.z = nosent(v.z); v.w = nosent(v.w);
        st16_sc1(xch + ((size_t)3 << 21) + (size_t)(rep * 2 + j) * 16384 + par * 8192 + t * 16, v);
      }
    }
  }
  if (wave_id_ == 7) IS(4);
  WAIT_V(0);
  BAR();
  if (wave_id_ == 7) IS(5);
  if (t == 0) st8_sc1(ws + OFF_INI + i * 128, u32x2{(unsigned)tag, ((unsigned)(tag >> 48) << 16) | vote});
}
#define GEMMX gemm7
#define CPF 128
#define PFD 2
#define PFW 0
#define NIMM 1
enum { T_NONE, T_QV, T_KR, T_WO, T_F1, T_F2, T_F2C, T_FIN, T_LN, T_SA };
__device__ __attribute__((aligned(16))) const unsigned ROLES[256][6] = {
  {0x1, 0x0, 0x0, 0x0, 0x0, 0x0},
  {0x11, 0x0, 0x0, 0x0, 0x0, 0x0},
  {0x21, 0x0, 0x0, 0x0, 0x0, 0x0},
  {0x31, 0x0, 0x0, 0x0, 0x0, 0x0},
  {0x41, 0x0, 0x0, 0x0, 0x0, 0x0},
  {0x51, 0x0, 0x0, 0x0, 0x0, 0x0},
  {0x61, 0x0, 0x0, 0x0, 0x0, 0x0},
  {0x71, 0x0, 0x0, 0x0, 0x0, 0x0},
  {0x81, 0x0, 0x0, 0x0, 0x0, 0x0},
  {0x91, 0x0, 0x0, 0x0, 0x0, 0x0},
  {0xa1, 0x0, 0x0, 0x0, 0x0, 0x0},
  {0xb1, 0x0, 0x0, 0x0, 0x0, 0x0},
  {0x2, 0x0, 0x0, 0x0, 0x0, 0x0},
  {0x12, 0x0, 0x0, 0x0, 0x0, 0x0},
  {0x22, 0x0, 0x0, 0x0, 0x0, 0x0},
  {0x32, 0x0, 0x0, 0x0, 0x0, 0x0},
  {0x42, 0x0, 0x0, 0x0, 0x0, 0x0},
  {0x52, 0x0, 0x0, 0x0, 0x0, 0x0},
  {0x62, 0x0, 0x0, 0x0, 0x0, 0x0},
  {0x72, 0x0, 0x0, 0x0, 0x0, 0x0},
  {0x82, 0x0, 0x0, 0x0, 0x0, 0x0},
  {0x92, 0x0, 0x0, 0x0, 0x0, 0x0},
  {0xa2, 0x0, 0x0, 0x0, 0x0, 0x0},
  {0xb2, 0x0, 0x0, 0x0, 0x0, 0x0},
  {0xc2, 0x0, 0x0, 0x0, 0x0, 0x0},
  {0xd2, 0x0, 0x0, 0x0, 0x0, 0x0},
  {0xe2, 0x0, 0x0, 0x0, 0x0, 0x0},
  {0xf2, 0x0, 0x0, 0x0, 0x0, 0x0},
  {0x102, 0x0, 0x0, 0x0, 0x0, 0x0},
  {0x112, 0x0, 0x0, 0x0, 0x0, 0x0},
  {0x122, 0x0, 0x0, 0x0, 0x0, 0x0},
  {0x132, 0x0, 0x0, 0x0, 0x0, 0x0},
  {0xc1, 0x0, 0x0, 0x0, 0x0, 0x0},
  {0xd1, 0x0, 0x0, 0x0, 0x0, 0x0},
  {0xe1, 0x0, 0x0, 0x0, 0x0, 0x0},
  {0xf1, 0x0, 0x0, 0x0, 0x0, 0x0},
  {0x101, 0x0, 0x0, 0x0, 0x0, 0x0},
  {0x111, 0x0, 0x0, 0x0, 0x0, 0x0},
  {0x121, 0x0, 0x0, 0x0, 0x0, 0x0},
  {0x131, 0x0, 0x0, 0x0, 0x0, 0x0},
  {0x142, 0x0, 0x0, 0x0, 0x0, 0x0},
  {0x152, 0x0, 0x0, 0x0, 0x0, 0x0},
  {0x162, 0x0, 0x0, 0x0, 0x0, 0x0},
  {0x172, 0x0, 0x0, 0x0, 0x0, 0x0},
  {0x182, 0x0, 0x0, 0x0, 0x0, 0x0},
  {0x192, 0x0, 0x0, 0x0, 0x0, 0x0},
  {0x1a2, 0x0, 0x0, 0x0, 0x0, 0x0},
  {0x1b2, 0x0, 0x0, 0x0, 0x0, 0x0},
  {0x1c2, 0x0, 0x0, 0x0, 0x0, 0x0},
  {0x1d2, 0x0, 0x0, 0x0, 0x0, 0x0},
  {0x1e2, 0x0, 0x0, 0x0, 0x0, 0x0},
  {0x1f2, 0x0, 0x0, 0x0, 0x0, 0x0},
  {0x202, 0x0, 0x0, 0x0, 0x0, 0x0},
  {0x212, 0x0, 0x0, 0x0, 0x0, 0x0},
  {0x222, 0x0, 0x0, 0x0, 0x0, 0x0},
  {0x232, 0x0, 0x0, 0x0, 0x0, 0x0},
  {0x242, 0x0, 0x0, 0x0, 0x0, 0x0},
  {0x252, 0x0, 0x0, 0x0, 0x0, 0x0},
  {0x262, 0x0, 0x0, 0x0, 0x0, 0x0},
  {0x272, 0x0, 0x0, 0x0, 0x0, 0x0},
  {0x282, 0x0, 0x0, 0x0, 0x0, 0x0},
  {0x292, 0x0, 0x0, 0x0, 0x0, 0x0},
  {0x2a2, 0x0, 0x0, 0x0, 0x0, 0x0},
  {0x2b2, 0x0, 0x0, 0x0, 0x0, 0x0},
  {0x141, 0x0, 0x0, 0x0, 0x0, 0x0},
  {0x151, 0x0, 0x0, 0x0, 0x0, 0x0},
  {0x161, 0x0, 0x0, 0x0, 0x0, 0x0},
  {0x171, 0x0, 0x0, 0x0, 0x0, 0x0},
  {0x181, 0x0, 0x0, 0x0, 0x0, 0x0},
  {0x191, 0x0, 0x0, 0x0, 0x0, 0x0},
  {0x1a1, 0x0, 0x0, 0x0, 0x0, 0x0},
  {0x1b1, 0x0, 0x0, 0x0, 0x0, 0x0},
  {0x1c1, 0x0, 0x0, 0x0, 0x0, 0x0},
  {0x1d1, 0x0, 0x0, 0x0, 0x0, 0x0},
  {0x1e1, 0x0, 0x0, 0x0, 0x0, 0x0},
  {0x1f1, 0x0, 0x0, 0x0, 0x0, 0x0},
  {0x2c2, 0x0, 0x0, 0x0, 0x0, 0x0},
  {0x2d2, 0x0, 0x0, 0x0, 0x0, 0x0},
  {0x2e2, 0x0, 0x0, 0x0, 0x0, 0x0},
  {0x2f2, 0x0, 0x0, 0x0, 0x0, 0x0},
  {0x302, 0x0, 0x0, 0x0, 0x0, 0x0},
  {0x312, 0x0, 0x0, 0x0, 0x0, 0x0},
  {0x322, 0x0, 0x0, 0x0, 0x0, 0x0},
  {0x332, 0x0, 0x0, 0x0, 0x0, 0x0},
  {0x342, 0x0, 0x0, 0x0, 0x0, 0x0},
  {0x352, 0x0, 0x0, 0x0, 0x0, 0x0},
  {0x362, 0x0, 0x0, 0x0, 0x0, 0x0},
  {0x372, 0x0, 0x0, 0x0, 0x0, 0x0},
  {0x382, 0x0, 0x0, 0x0, 0x0, 0x0},
  {0x392, 0x0, 0x0, 0x0, 0x0, 0x0},
  {0x3a2, 0x0, 0x0, 0x0, 0x0, 0x0},
  {0x3b2, 0x0, 0x0, 0x0, 0x0, 0x0},
  {0x3c2, 0x0, 0x0, 0x0, 0x0, 0x0},
  {0x3d2, 0x0, 0x0, 0x0, 0x0, 0x0},
  {0x3e2, 0x0, 0x0, 0x0, 0x0, 0x0},
  {0x3f2, 0x0, 0x0, 0x0, 0x0, 0x0},
  {0x100005, 0x2e0000, 0x0, 0x0, 0x0, 0x0},
  {0x100015, 0x2e0004, 0x0, 0x0, 0x0, 0x0},
  {0x100025, 0x2e0008, 0x0, 0x0, 0x0, 0x0},
  {0x100035, 0x2e000c, 0x0, 0x0, 0x0, 0x0},
  {0x100045, 0x2e0010, 0x0, 0x0, 0x0, 0x0},
  {0x100055, 0x2e0014, 0x0, 0x0, 0x0, 0x0},
  {0x100065, 0x2e0018, 0x0, 0x0, 0x0, 0x0},
  {0x200037, 0x2e001c, 0x140291e, 0x0, 0x0, 0x0},
  {0x100075, 0x2e0020, 0x0, 0x0, 0x0, 0x0},
  {0x100085, 0x2e0024, 0x0, 0x0, 0x0, 0x0},
  {0x100095, 0x2e0028, 0x0, 0x0, 0x0, 0x0},
  {0x1000a5, 0x2e002c, 0x0, 0x0, 0x0, 0x0},
  {0x1000b5, 0x2e0030, 0x0, 0x0, 0x0, 0x0},
  {0x1000c5, 0x2e0034, 0x0, 0x0, 0x0, 0x0},
  {0x1000d5, 0x2e0038, 0x0, 0x0, 0x0, 0x0},
  {0x200077, 0x2e003c, 0x140293e, 0x0, 0x0, 0x0},
  {0x1000e5, 0x2e0040, 0x0, 0x0, 0x0, 0x0},
  {0x1000f5, 0x2e0044, 0x0, 0x0, 0x0, 0x0},
  {0x100105, 0x2e0048, 0x0, 0x0, 0x0, 0x0},
  {0x100115, 0x2e004c, 0x0, 0x0, 0x0, 0x0},
  {0x100125, 0x2e0050, 0x0, 0x0, 0x0, 0x0},
  {0x100135, 0x2e0054, 0x0, 0x0, 0x0, 0x0},
  {0x100145, 0x2e0058, 0x0, 0x0, 0x0, 0x0},
  {0x2000b7, 0x2e005c, 0x140295e, 0x0, 0x0, 0x0},
  {0x100155, 0x2e0060, 0x0, 0x0, 0x0, 0x0},
  {0x100165, 0x2e0064, 0x0, 0x0, 0x0, 0x0},
  {0x100175, 0x2e0068, 0x0, 0x0, 0x0, 0x0},
  {0x100006, 0x2e006c, 0x0, 0x0, 0x0, 0x0},
  {0x100016, 0x2e0070, 0x0, 0x0, 0x0, 0x0},
  {0x100026, 0x2e0074, 0x0, 0x0, 0x0, 0x0},
  {0x100036, 0x2e0078, 0x0, 0x0, 0x0, 0x0},
  {0x2000f7, 0x2e007c, 0x140297e, 0x0, 0x0, 0x0},
  {0x100034, 0x5402902, 0x0, 0x0, 0x0, 0x0},
  {0x1000b4, 0x5402906, 0x0, 0x0, 0x0, 0x0},
  {0x1100004, 0x540290a, 0x0, 0x0, 0x0, 0x0},
  {0x2100014, 0x540290e, 0x0, 0x0, 0x0, 0x0},
  {0x100003, 0x5402e00, 0x0, 0x0, 0x0, 0x0},
  {0x100013, 0x5402e04, 0x0, 0x0, 0x0, 0x0},
  {0x100023, 0x5402e08, 0x0, 0x0, 0x0, 0x0},
  {0x100033, 0x5402e0c, 0x0, 0x0, 0x0, 0x0},
  {0x200007, 0x350001, 0x1402912, 0x0, 0x0, 0x0},
  {0x200017, 0x350005, 0x1402916, 0x0, 0x0, 0x0},
  {0x200027, 0x350009, 0x1402919, 0x0, 0x0, 0x0},
  {0x200000, 0x35000d, 0x140291a, 0x0, 0x0, 0x0},
  {0x200000, 0x350011, 0x140291d, 0x0, 0x0, 0x0},
  {0x200000, 0x400015, 0x1402e18, 0x0, 0x0, 0x0},
  {0x100024, 0x290019, 0x0, 0x0, 0x0, 0x0},
  {0x100044, 0x29001d, 0x0, 0x0, 0x0, 0x0},
  {0x100054, 0x290002, 0x0, 0x0, 0x0, 0x0},
  {0x100064, 0x290006, 0x0, 0x0, 0x0, 0x0},
  {0x100074, 0x29000a, 0x0, 0x0, 0x0, 0x0},
  {0x100084, 0x29000e, 0x0, 0x0, 0x0, 0x0},
  {0x100094, 0x290012, 0x0, 0x0, 0x0, 0x0},
  {0x1000a4, 0x290016, 0x0, 0x0, 0x0, 0x0},
  {0x1000c4, 0x29001a, 0x0, 0x0, 0x0, 0x0},
  {0x1000d4, 0x29001e, 0x0, 0x0, 0x0, 0x0},
  {0x101043, 0x1403501, 0x0, 0x0, 0x0, 0x0},
  {0x102053, 0x140350d, 0x0, 0x0, 0x0, 0x0},
  {0x103063, 0x1403505, 0x0, 0x0, 0x0, 0x0},
  {0x104073, 0x1403509, 0x0, 0x0, 0x0, 0x0},
  {0x105008, 0x1402e10, 0x0, 0x0, 0x0, 0x0},
  {0x106088, 0x1403511, 0x0, 0x0, 0x0, 0x0},
  {0x107018, 0x1402e14, 0x0, 0x0, 0x0, 0x0},
  {0x108098, 0x1402e1c, 0x0, 0x0, 0x0, 0x0},
  {0x100134, 0x5402922, 0x0, 0x0, 0x0, 0x0},
  {0x1001b4, 0x5402926, 0x0, 0x0, 0x0, 0x0},
  {0x31000e4, 0x540292a, 0x0, 0x0, 0x0, 0x0},
  {0x41000f4, 0x540292e, 0x0, 0x0, 0x0, 0x0},
  {0x100083, 0x5402e20, 0x0, 0x0, 0x0, 0x0},
  {0x100093, 0x5402e24, 0x0, 0x0, 0x0, 0x0},
  {0x1000a3, 0x5402e28, 0x0, 0x0, 0x0, 0x0},
  {0x1000b3, 0x5402e2c, 0x0, 0x0, 0x0, 0x0},
  {0x200047, 0x350021, 0x1402932, 0x0, 0x0, 0x0},
  {0x200057, 0x350025, 0x1402936, 0x0, 0x0, 0x0},
  {0x200067, 0x350029, 0x1402939, 0x0, 0x0, 0x0},
  {0x200000, 0x35002d, 0x140293a, 0x0, 0x0, 0x0},
  {0x200000, 0x350031, 0x140293d, 0x0, 0x0, 0x0},
  {0x200000, 0x400035, 0x1402e38, 0x0, 0x0, 0x0},
  {0x100104, 0x290039, 0x0, 0x0, 0x0, 0x0},
  {0x100114, 0x29003d, 0x0, 0x0, 0x0, 0x0},
  {0x100124, 0x290022, 0x0, 0x0, 0x0, 0x0},
  {0x100144, 0x290026, 0x0, 0x0, 0x0, 0x0},
  {0x100154, 0x29002a, 0x0, 0x0, 0x0, 0x0},
  {0x100164, 0x29002e, 0x0, 0x0, 0x0, 0x0},
  {0x100174, 0x290032, 0x0, 0x0, 0x0, 0x0},
  {0x100184, 0x290036, 0x0, 0x0, 0x0, 0x0},
  {0x100194, 0x29003a, 0x0, 0x0, 0x0, 0x0},
  {0x1001a4, 0x29003e, 0x0, 0x0, 0x0, 0x0},
  {0x1090c3, 0x1403521, 0x0, 0x0, 0x0, 0x0},
  {0x10a0d3, 0x140352d, 0x0, 0x0, 0x0, 0x0},
  {0x10b0e3, 0x1403525, 0x0, 0x0, 0x0, 0x0},
  {0x10c0f3, 0x1403529, 0x0, 0x0, 0x0, 0x0},
  {0x10d028, 0x1402e30, 0x0, 0x0, 0x0, 0x0},
  {0x10e0a8, 0x1403531, 0x0, 0x0, 0x0, 0x0},
  {0x10f038, 0x1402e34, 0x0, 0x0, 0x0, 0x0},
  {0x1100b8, 0x1402e3c, 0x0, 0x0, 0x0, 0x0},
  {0x100234, 0x5402942, 0x0, 0x0, 0x0, 0x0},
  {0x1001c4, 0x5402946, 0x0, 0x0, 0x0, 0x0},
  {0x51001d4, 0x540294a, 0x0, 0x0, 0x0, 0x0},
  {0x61001e4, 0x540294e, 0x0, 0x0, 0x0, 0x0},
  {0x100103, 0x5402e40, 0x0, 0x0, 0x0, 0x0},
  {0x100113, 0x5402e44, 0x0, 0x0, 0x0, 0x0},
  {0x100123, 0x5402e48, 0x0, 0x0, 0x0, 0x0},
  {0x100133, 0x5402e4c, 0x0, 0x0, 0x0, 0x0},
  {0x200087, 0x350041, 0x1402952, 0x0, 0x0, 0x0},
  {0x200097, 0x350045, 0x1402956, 0x0, 0x0, 0x0},
  {0x2000a7, 0x350049, 0x1402959, 0x0, 0x0, 0x0},
  {0x200000, 0x35004d, 0x140295a, 0x0, 0x0, 0x0},
  {0x200000, 0x350051, 0x140295d, 0x0, 0x0, 0x0},
  {0x200000, 0x400055, 0x1402e58, 0x0, 0x0, 0x0},
  {0x1001f4, 0x290059, 0x0, 0x0, 0x0, 0x0},
  {0x100204, 0x29005d, 0x0, 0x0, 0x0, 0x0},
  {0x100214, 0x290042, 0x0, 0x0, 0x0, 0x0},
  {0x100224, 0x290046, 0x0, 0x0, 0x0, 0x0},
  {0x100244, 0x29004a, 0x0, 0x0, 0x0, 0x0},
  {0x100254, 0x29004e, 0x0, 0x0, 0x0, 0x0},
  {0x100264, 0x290052, 0x0, 0x0, 0x0, 0x0},
  {0x100274, 0x290056, 0x0, 0x0, 0x0, 0x0},
  {0x100284, 0x29005a, 0x0, 0x0, 0x0, 0x0},
  {0x100294, 0x29005e, 0x0, 0x0, 0x0, 0x0},
  {0x111143, 0x1403541, 0x0, 0x0, 0x0, 0x0},
  {0x112153, 0x140354d, 0x0, 0x0, 0x0, 0x0},
  {0x113163, 0x1403545, 0x0, 0x0, 0x0, 0x0},
  {0x114173, 0x1403549, 0x0, 0x0, 0x0, 0x0},
  {0x115048, 0x1402e50, 0x0, 0x0, 0x0, 0x0},
  {0x1160c8, 0x1403551, 0x0, 0x0, 0x0, 0x0},
  {0x117058, 0x1402e54, 0x0, 0x0, 0x0, 0x0},
  {0x1180d8, 0x1402e5c, 0x0, 0x0, 0x0, 0x0},
  {0x1002b4, 0x5402962, 0x0, 0x0, 0x0, 0x0},
  {0x100334, 0x5402966, 0x0, 0x0, 0x0, 0x0},
  {0x71002a4, 0x540296a, 0x0, 0x0, 0x0, 0x0},
  {0x81002c4, 0x540296e, 0x0, 0x0, 0x0, 0x0},
  {0x100183, 0x5402e60, 0x0, 0x0, 0x0, 0x0},
  {0x100193, 0x5402e64, 0x0, 0x0, 0x0, 0x0},
  {0x1001a3, 0x5402e68, 0x0, 0x0, 0x0, 0x0},
  {0x1001b3, 0x5402e6c, 0x0, 0x0, 0x0, 0x0},
  {0x2000c7, 0x350061, 0x1402972, 0x0, 0x0, 0x0},
  {0x2000d7, 0x350065, 0x1402976, 0x0, 0x0, 0x0},
  {0x2000e7, 0x350069, 0x1402979, 0x0, 0x0, 0x0},
  {0x200000, 0x35006d, 0x140297a, 0x0, 0x0, 0x0},
  {0x200000, 0x350071, 0x140297d, 0x0, 0x0, 0x0},
  {0x200000, 0x400075, 0x1402e78, 0x0, 0x0, 0x0},
  {0x1002d4, 0x290079, 0x0, 0x0, 0x0, 0x0},
  {0x1002e4, 0x29007d, 0x0, 0x0, 0x0, 0x0},
  {0x1002f4, 0x290062, 0x0, 0x0, 0x0, 0x0},
  {0x100304, 0x290066, 0x0, 0x0, 0x0, 0x0},
  {0x100314, 0x29006a, 0x0, 0x0, 0x0, 0x0},
  {0x100324, 0x29006e, 0x0, 0x0, 0x0, 0x0},
  {0x100344, 0x290072, 0x0, 0x0, 0x0, 0x0},
  {0x100354, 0x290076, 0x0, 0x0, 0x0, 0x0},
  {0x100364, 0x29007a, 0x0, 0x0, 0x0, 0x0},
  {0x100374, 0x29007e, 0x0, 0x0, 0x0, 0x0},
  {0x1191c3, 0x1403561, 0x0, 0x0, 0x0, 0x0},
  {0x11a1d3, 0x140356d, 0x0, 0x0, 0x0, 0x0},
  {0x11b1e3, 0x1403565, 0x0, 0x0, 0x0, 0x0},
  {0x11c1f3, 0x1403569, 0x0, 0x0, 0x0, 0x0},
  {0x11d068, 0x1402e70, 0x0, 0x0, 0x0, 0x0},
  {0x11e0e8, 0x1403571, 0x0, 0x0, 0x0, 0x0},
  {0x11f078, 0x1402e74, 0x0, 0x0, 0x0, 0x0},
  {0x1200f8, 0x1402e7c, 0x0, 0x0, 0x0, 0x0},
};
constexpr unsigned NQV = 0xc080c;
__device__ __attribute__((aligned(16))) const unsigned PFT[8][128] = {
  {0x40000f, 0x40000e, 0x0, 0x0, 0x41000f, 0x41000e, 0x0, 0x0, 0x42000f, 0x42000e, 0x0, 0x0, 0x43000f, 0x43000e, 0x0, 0x0, 0x44000f, 0x40002d, 0x0, 0x0, 0x45000f, 0x41002d, 0x0, 0x0, 0x46000f, 0x42002d, 0x0, 0x0, 0x47000f, 0x43002d, 0x0, 0x0, 0x48000f, 0x40002e, 0x0, 0x0, 0x49000f, 0x41002e, 0x0, 0x0, 0x4a000f, 0x42002e, 0x0, 0x0, 0x4b000f, 0x43002e, 0x0, 0x0, 0x4c000f, 0x40004d, 0x0, 0x0, 0x4d000f, 0x41004d, 0x0, 0x0, 0x4e000f, 0x42004d, 0x0, 0x0, 0x4f000f, 0x43004d, 0x0, 0x0, 0x40000c, 0x40004e, 0x0, 0x0, 0x41000c, 0x41004e, 0x0, 0x0, 0x42000c, 0x42004e, 0x0, 0x0, 0x43000c, 0x43004e, 0x0, 0x0, 0x40002c, 0x400003, 0x0, 0x0, 0x41002c, 0x400007, 0x0, 0x0, 0x42002c, 0x40000b, 0x0, 0x0, 0x43002c, 0x400013, 0x0, 0x0, 0x40004c, 0x420003, 0x0, 0x0, 0x41004c, 0x420007, 0x0, 0x0, 0x42004c, 0x42000b, 0x0, 0x0, 0x43004c, 0x420013, 0x0, 0x0, 0x40000d, 0x440003, 0x0, 0x0, 0x41000d, 0x440007, 0x0, 0x0, 0x42000d, 0x44000b, 0x0, 0x0, 0x43000d, 0x440013, 0x0, 0x0},
  {0x40006c, 0x400017, 0x0, 0x0, 0x41006c, 0x40001b, 0x0, 0x0, 0x42006c, 0x40001f, 0x0, 0x0, 0x43006c, 0x400023, 0x0, 0x0, 0x40008c, 0x400027, 0x0, 0x0, 0x41008c, 0x40002b, 0x0, 0x0, 0x42008c, 0x420017, 0x0, 0x0, 0x43008c, 0x42001b, 0x0, 0x0, 0x40006d, 0x42001f, 0x0, 0x0, 0x41006d, 0x420023, 0x0, 0x0, 0x42006d, 0x420027, 0x0, 0x0, 0x43006d, 0x42002b, 0x0, 0x0, 0x40006e, 0x440017, 0x0, 0x0, 0x41006e, 0x44001b, 0x0, 0x0, 0x42006e, 0x44001f, 0x0, 0x0, 0x43006e, 0x440023, 0x0, 0x0, 0x40008d, 0x440027, 0x0, 0x0, 0x41008d, 0x44002b, 0x0, 0x0, 0x42008d, 0x460017, 0x0, 0x0, 0x43008d, 0x46001b, 0x0, 0x0, 0x40008e, 0x46001f, 0x0, 0x0, 0x41008e, 0x460023, 0x0, 0x0, 0x42008e, 0x460027, 0x0, 0x0, 0x43008e, 0x46002b, 0x0, 0x0, 0x4000ad, 0x480017, 0x0, 0x0, 0x4100ad, 0x48001b, 0x0, 0x0, 0x4200ad, 0x48001f, 0x0, 0x0, 0x4300ad, 0x480023, 0x0, 0x0, 0x4000ae, 0x480027, 0x0, 0x0, 0x4100ae, 0x48002b, 0x0, 0x0, 0x4200ae, 0x4a0017, 0x0, 0x0, 0x4300ae, 0x4a001b, 0x0, 0x0},
  {0x40002f, 0x4000ce, 0x0, 0x0, 0x41002f, 0x4100ce, 0x0, 0x0, 0x42002f, 0x4200ce, 0x0, 0x0, 0x43002f, 0x4300ce, 0x0, 0x0, 0x44002f, 0x4000ed, 0x0, 0x0, 0x45002f, 0x4100ed, 0x0, 0x0, 0x46002f, 0x4200ed, 0x0, 0x0, 0x47002f, 0x4300ed, 0x0, 0x0, 0x48002f, 0x4000ee, 0x0, 0x0, 0x49002f, 0x4100ee, 0x0, 0x0, 0x4a002f, 0x4200ee, 0x0, 0x0, 0x4b002f, 0x4300ee, 0x0, 0x0, 0x4c002f, 0x4000fe, 0x0, 0x0, 0x4d002f, 0x4100fe, 0x0, 0x0, 0x4e002f, 0x4200fe, 0x0, 0x0, 0x4f002f, 0x4300fe, 0x0, 0x0, 0x4000ac, 0x4000fd, 0x0, 0x0, 0x4100ac, 0x4100fd, 0x0, 0x0, 0x4200ac, 0x4200fd, 0x0, 0x0, 0x4300ac, 0x4300fd, 0x0, 0x0, 0x4000cc, 0x40003f, 0x0, 0x0, 0x4100cc, 0x41003f, 0x0, 0x0, 0x4200cc, 0x42003f, 0x0, 0x0, 0x4300cc, 0x43003f, 0x0, 0x0, 0x4000ec, 0x44003f, 0x0, 0x0, 0x4100ec, 0x45003f, 0x0, 0x0, 0x4200ec, 0x46003f, 0x0, 0x0, 0x4300ec, 0x47003f, 0x0, 0x0, 0x4000cd, 0x48003f, 0x0, 0x0, 0x4100cd, 0x49003f, 0x0, 0x0, 0x4200cd, 0x4a003f, 0x0, 0x0, 0x4300cd, 0x4b003f, 0x0, 0x0},
  {0x40000c, 0x0, 0x0, 0x0, 0x41000c, 0x0, 0x0, 0x0, 0x42000c, 0x0, 0x0, 0x0, 0x43000c, 0x0, 0x0, 0x0, 0x40002c, 0x0, 0x0, 0x0, 0x41002c, 0x0, 0x0, 0x0, 0x42002c, 0x0, 0x0, 0x0, 0x43002c, 0x0, 0x0, 0x0, 0x40004c, 0x0, 0x0, 0x0, 0x41004c, 0x0, 0x0, 0x0, 0x42004c, 0x0, 0x0, 0x0, 0x43004c, 0x0, 0x0, 0x0, 0x40006c, 0x0, 0x0, 0x0, 0x41006c, 0x0, 0x0, 0x0, 0x42006c, 0x0, 0x0, 0x0, 0x43006c, 0x0, 0x0, 0x0, 0x40008c, 0x0, 0x0, 0x0, 0x41008c, 0x0, 0x0, 0x0, 0x42008c, 0x0, 0x0, 0x0, 0x43008c, 0x0, 0x0, 0x0, 0x4000ac, 0x0, 0x0, 0x0, 0x4100ac, 0x0, 0x0, 0x0, 0x4200ac, 0x0, 0x0, 0x0, 0x4300ac, 0x0, 0x0, 0x0, 0x4000cc, 0x0, 0x0, 0x0, 0x4100cc, 0x0, 0x0, 0x0, 0x4200cc, 0x0, 0x0, 0x0, 0x4300cc, 0x0, 0x0, 0x0, 0x4000ec, 0x0, 0x0, 0x0, 0x4100ec, 0x0, 0x0, 0x0, 0x4200ec, 0x0, 0x0, 0x0, 0x4300ec, 0x0, 0x0, 0x0},
  {0x0, 0x0, 0x0, 0x0, 0x0, 0x0, 0x0, 0x0, 0x0, 0x0, 0x0, 0x0, 0x0, 0x0, 0x0, 0x0, 0x0, 0x0, 0x0, 0x0, 0x0, 0x0, 0x0, 0x0, 0x0, 0x0, 0x0, 0x0, 0x0, 0x0, 0x0, 0x0, 0x40000d, 0x0, 0x0, 0x0, 0x41000d, 0x0, 0x0, 0x0, 0x42000d, 0x0, 0x0, 0x0, 0x43000d, 0x0, 0x0, 0x0, 0x40002d, 0x0, 0x0, 0x0, 0x41002d, 0x0, 0x0, 0x0, 0x42002d, 0x0, 0x0, 0x0, 0x43002d, 0x0, 0x0, 0x0, 0x40000e, 0x0, 0x0, 0x0, 0x41000e, 0x0, 0x0, 0x0, 0x42000e, 0x0, 0x0, 0x0, 0x43000e, 0x0, 0x0, 0x0, 0x40002e, 0x0, 0x0, 0x0, 0x41002e, 0x0, 0x0, 0x0, 0x42002e, 0x0, 0x0, 0x0, 0x43002e, 0x0, 0x0, 0x0, 0x0, 0x0, 0x0, 0x0, 0x0, 0x0, 0x0, 0x0, 0x0, 0x0, 0x0, 0x0, 0x0, 0x0, 0x0, 0x0, 0x0, 0x0, 0x0, 0x0, 0x0, 0x0, 0x0, 0x0, 0x0, 0x0, 0x0, 0x0, 0x0, 0x0, 0x0, 0x0},
  {0x0, 0x0, 0x0, 0x0, 0x0, 0x0, 0x0, 0x0, 0x0, 0x0, 0x0, 0x0, 0x0, 0x0, 0x0, 0x0, 0x0, 0x0, 0x0, 0x0, 0x0, 0x0, 0x0, 0x0, 0x0, 0x0, 0x0, 0x0, 0x0, 0x0, 0x0, 0x0, 0x40004d, 0x0, 0x0, 0x0, 0x41004d, 0x0, 0x0, 0x0, 0x42004d, 0x0, 0x0, 0x0, 0x43004d, 0x0, 0x0, 0x0, 0x40006d, 0x0, 0x0, 0x0, 0x41006d, 0x0, 0x0, 0x0, 0x42006d, 0x0, 0x0, 0x0, 0x43006d, 0x0, 0x0, 0x0, 0x40004e, 0x0, 0x0, 0x0, 0x41004e, 0x0, 0x0, 0x0, 0x42004e, 0x0, 0x0, 0x0, 0x43004e, 0x0, 0x0, 0x0, 0x40006e, 0x0, 0x0, 0x0, 0x41006e, 0x0, 0x0, 0x0, 0x42006e, 0x0, 0x0, 0x0, 0x43006e, 0x0, 0x0, 0x0, 0x0, 0x0, 0x0, 0x0, 0x0, 0x0, 0x0, 0x0, 0x0, 0x0, 0x0, 0x0, 0x0, 0x0, 0x0, 0x0, 0x0, 0x0, 0x0, 0x0, 0x0, 0x0, 0x0, 0x0, 0x0, 0x0, 0x0, 0x0, 0x0, 0x0, 0x0, 0x0},
  {0x0, 0x0, 0x0, 0x0, 0x0, 0x0, 0x0, 0x0, 0x0, 0x0, 0x0, 0x0, 0x0, 0x0, 0x0, 0x0, 0x0, 0x0, 0x0, 0x0, 0x0, 0x0, 0x0, 0x0, 0x0, 0x0, 0x0, 0x0, 0x0, 0x0, 0x0, 0x0, 0x40008d, 0x0, 0x0, 0x0, 0x41008d, 0x0, 0x0, 0x0, 0x42008d, 0x0, 0x0, 0x0, 0x43008d, 0x0, 0x0, 0x0, 0x4000ad, 0x0, 0x0, 0x0, 0x4100ad, 0x0, 0x0, 0x0, 0x4200ad, 0x0, 0x0, 0x0, 0x4300ad, 0x0, 0x0, 0x0, 0x40008e, 0x0, 0x0, 0x0, 0x41008e, 0x0, 0x0, 0x0, 0x42008e, 0x0, 0x0, 0x0, 0x43008e, 0x0, 0x0, 0x0, 0x4000ae, 0x0, 0x0, 0x0, 0x4100ae, 0x0, 0x0, 0x0, 0x4200ae, 0x0, 0x0, 0x0, 0x4300ae, 0x0, 0x0, 0x0, 0x0, 0x0, 0x0, 0x0, 0x0, 0x0, 0x0, 0x0, 0x0, 0x0, 0x0, 0x0, 0x0, 0x0, 0x0, 0x0, 0x0, 0x0, 0x0, 0x0, 0x0, 0x0, 0x0, 0x0, 0x0, 0x0, 0x0, 0x0, 0x0, 0x0, 0x0, 0x0},
  {0x0, 0x0, 0x0, 0x0, 0x0, 0x0, 0x0, 0x0, 0x0, 0x0, 0x0, 0x0, 0x0, 0x0, 0x0, 0x0, 0x0, 0x0, 0x0, 0x0, 0x0, 0x0, 0x0, 0x0, 0x0, 0x0, 0x0, 0x0, 0x0, 0x0, 0x0, 0x0, 0x4000cd, 0x0, 0x0, 0x0, 0x4100cd, 0x0, 0x0, 0x0, 0x4200cd, 0x0, 0x0, 0x0, 0x4300cd, 0x0, 0x0, 0x0, 0x4000ed, 0x0, 0x0, 0x0, 0x4100ed, 0x0, 0x0, 0x0, 0x4200ed, 0x0, 0x0, 0x0, 0x4300ed, 0x0, 0x0, 0x0, 0x4000ce, 0x0, 0x0, 0x0, 0x4100ce, 0x0, 0x0, 0x0, 0x4200ce, 0x0, 0x0, 0x0, 0x4300ce, 0x0, 0x0, 0x0, 0x4000ee, 0x0, 0x0, 0x0, 0x4100ee, 0x0, 0x0, 0x0, 0x4200ee, 0x0, 0x0, 0x0, 0x4300ee, 0x0, 0x0, 0x0, 0x0, 0x0, 0x0, 0x0, 0x0, 0x0, 0x0, 0x0, 0x0, 0x0, 0x0, 0x0, 0x0, 0x0, 0x0, 0x0, 0x0, 0x0, 0x0, 0x0, 0x0, 0x0, 0x0, 0x0, 0x0, 0x0, 0x0, 0x0, 0x0, 0x0, 0x0, 0x0},
};
__global__ void __launch_bounds__(512, 2) k_mega(const u16* __restrict__ Wq, const u16* __restrict__ Wk, const u16* __restrict__ Wv,
                                                 const u16* __restrict__ Wo, const u16* __restrict__ w1, const u16* __restrict__ w3,
                                                 const u16* __restrict__ w2, const u16* __restrict__ x, const u16* __restrict__ cvec,
                                                 const u16* __restrict__ wmod, const u16* __restrict__ mem0, char* ws, u16* out) {
  extern __shared__ __attribute__((aligned(16))) char smem[];
  int wave_id_ = __builtin_amdgcn_readfirstlane((int)threadIdx.x >> 6);
  asm volatile("" : "+s"(wave_id_));
  unsigned* tmo = (unsigned*)(ws + OFF_TMO);
  unsigned long long* mts = nullptr;
  const float* mod = (const float*)(ws + OFF_MOD);
  volatile LAS unsigned* tslot = (volatile LAS unsigned*)(smem + L_SLOT + 768);
  unsigned xcc; asm volatile("s_getreg_b32 %0, hwreg(HW_REG_XCC_ID)" : "=s"(xcc)); xcc &= 7;
  auto ini_of = [](int sl) { return sl >= 128 && ((sl + 8) & 31) < 16 ? ((sl >> 5) - 4) * 16 + (sl & 15) : -1; };
  const int guess = xcc * 32 + ((blockIdx.x >> 3) & 31);
  if (wave_id_ == 3) {
    const char* pc; asm volatile("s_getpc_b64 %0" : "=s"(pc));
    const unsigned o = (unsigned)(lane_id() * 32 + ((blockIdx.x >> 3) & 31)) * 128;
    if (o < CPF * 1024) { const unsigned v = *(const unsigned*)(pc + o); asm volatile("" :: "v"(v)); }
  }
  if (wave_id_ >= 4 && ini_of(guess) >= 0) { init_sums(smem, opaque_tid() - 256, ini_of(guess), cvec, wmod); if (wave_id_ == 7) IS(3); }
  u32x2 rt_; u32x4 pfe;
  unsigned long long tag;
  asm volatile("s_load_dwordx2 %0, %3, %6\n\ts_load_dwordx2 %1, %4, 0\n\ts_load_dwordx4 %2, %5, 0\n\ts_waitcnt lgkmcnt(0)" : "=&s"(tag), "=&s"(rt_), "=&s"(pfe)
               : "s"(ws), "s"(&ROLES[guess][0]), "s"(&PFT[xcc][((blockIdx.x >> 3) & 31) * 4]), "i"((int)OFF_EP) : "memory");
  tag = (tag ^ (tag >> 31) ^ 0x9E3779B97F4A7C15ull) * 0xBF58476D1CE4E5B9ull; tag = (tag ^ (tag >> 29)) * 0x94D049BB133111EBull; tag ^= tag >> 32;
  if (wave_id_ == 0) {
    if (lane_id() == 0) {
      const unsigned long long g = tag; IS(0);
      unsigned sl = 0xFFFFu;
      for (int i = 0; i < 256; ++i)
        if (__hip_atomic_exchange((unsigned long long*)(ws + OFF_CLM) + ((guess + i) & 255), g, __ATOMIC_RELAXED, __HIP_MEMORY_SCOPE_AGENT) != g) { sl = (guess + i) & 255; break; }
      IS(1);
      tslot[0] = sl;
    }
  } else if (wave_id_ == 1) {
    const int ig = ini_of(guess);
    tslot[1] = ig >= 0 && (ig & 15) < 12 ? (unsigned)calibrate(ws, ig) : 0u;
    IS(2);
  } else if (ini_of(guess) >= 0) {
    if (wave_id_ < 4) { const unsigned v = *(const unsigned*)(mem0 + (opaque_tid() - 128) * 64); asm volatile("" :: "v"(v)); }
  } else if (wave_id_ >= 4) {
    delay64<PFD>();
#pragma unroll
    for (int i = 0; i < 4; ++i) {
      const unsigned e = pfe[i];
      const int t = opaque_tid() - 256, m = e & 3, wide = (e >> 21) & 1, row = ((e >> 8) & 8191) + (wide ? t >> 4 : t), cl = ((e >> 2) & 63) + (wide ? t & 15 : 0);
      const u16* p = (m == 0 ? Wq : (m == 1 ? Wk : (m == 2 ? Wv : Wo))) + (size_t)row * (m == 3 ? DM : HD) + cl * 64;
      if (e) asm volatile("s_mov_b32 m0, %1\n\ts_nop 0\n\tglobal_load_lds_dword %0, off" :: "v"(p), "s"((rt_.x & 15) == T_QV ? 31232 : L_GB + 8192) : "memory", "m0");
    }
    if (PFW) WAIT_V(0);
  }
  BAR();
  const int slot = (int)__builtin_amdgcn_readfirstlane(tslot[0]);
  const int cd = (int)__builtin_amdgcn_readfirstlane(tslot[1]);
  if (slot != guess) { rt_ = *(const u32x2*)ROLES[slot & 255]; WAIT_V(0); }
  BAR();
  asm volatile("" : "+s"(wave_id_));
  if (slot >= 256) return;
  const int xg = slot >> 5;
  const unsigned rw = rt_[0];
  const int role = rw & 15, idx = (rw >> 4) & 255;
  if (role == T_QV || role == T_KR) {
    const int wg = role == T_QV ? (idx >> 3) * 32 + (idx & 7) : (idx >> 4) * 32 + 8 + (idx & 15);
    if (role == T_QV) qk_body(smem, wave_id_, xg, idx, wg, Wq, Wv, ws, tmo, tag);
    else vr_body(smem, wave_id_, xg, idx >> 1, idx & 1, wg, Wk, Wv, Wo, ws, tmo, tag);
    return;
  }
  unsigned* cnt = (unsigned*)(xch_base(ws) + ((size_t)4 << 21));
  const int ini = ini_of(slot);
  if (ini >= 0) {
    if (slot != guess) { if (wave_id_ < 4) init_sums(smem, opaque_tid(), ini, cvec, wmod); BAR(); }
    init_role(smem, wave_id_, ini, mem0, ws, tag, slot != guess || cd == 0 ? 0u : ((cd > 0) == !(xcc & 4) ? 1u : 2u));
    if ((rw >> 12) & 255) l1_role(smem, wave_id_, ((rw >> 12) & 255) - 1, x, mod, cvec, wmod, ws + OFF_H1, cnt, tmo, mts, tag);
    else cnt += (ini_resolve1(smem, wave_id_, ws, tag) >> 2) << 11;
  }
  const int ntask = (rw >> 20) & 15;
  for (int it = 0; it < ntask; ++it) {
    const unsigned tk = it == 0 ? rt_[1] : ROLES[slot][1 + it];
    const int m = tk & 3, h = (tk >> 2) & 31, bo = (tk >> 8) & 255, ns = ((tk >> 16) & 255) - bo, part = (tk >> 24) & 3;
    const int lane = lane_id();
    const Dep d{uni((const char*)cnt), (unsigned)(PL1 + (lane & 31)) * 128, 8u, (unsigned)-bo};
    unsigned par = 0;
    if (wave_id_ == 0) {
      if ((tk >> 26) & 1) wait_k0(cnt, h, tmo);
    }
    BAR();
    gemm7<R_QKV>(smem, wave_id_, (m == 0 ? Wq : (m == 1 ? Wk : Wv)) + h * DH, nullptr, HD, ws + OFF_H1 + (size_t)bo * 65536, 65536, d,
                    ws + (m == 0 ? OFF_Q : (m == 1 ? OFF_K : OFF_V)) + h * 8192 + (size_t)bo * 32 * 8192, 32 * 8192,
                    cnt + (PQKV + h) * 32 + 4 * part + m, tmo, part ? 319 : (h >> 3) * 32 + m * 8 + (h & 7), mts, false,
                    bo ? 0 : NIMM, ns, bo ? ns : 0, bo * 257, tag, &par);
    cnt += (par >> 2) << 11;
  }
  __syncthreads();
  if (rw >> 24) {
    constexpr size_t off[6] = {OFF_O, OFF_P, OFF_P2, OFF_G, OFF_X1, OFF_H2};
    constexpr int bs[6] = {262144, 262144, 262144, 229376, 65536, 65536};
    const int a = (rw >> 24) - 1;
#pragma unroll
    for (int i = 0; i < 6; ++i)
      for (int o = (a * 512 + opaque_tid()) * 16; o < (NSEG - BEARLY) * bs[i]; o += NARM * 8192) st16_sc1(ws + off[i] + (size_t)BEARLY * bs[i] + o, u32x4{SENT, SENT, SENT, SENT});
    WAIT_V(0);
    BAR();
    if (opaque_tid() == 0) st4_sc1(cnt + (PARM + a) * 32, 1u);
  }
  const char* cb = uni((const char*)cnt);
  if (role == T_WO || role == T_F2) {
    const bool wo = role == T_WO;
    const int ks = idx >> 3, cn = idx & 7;
    const int lane = lane_id();
    const Dep d{cb, (unsigned)(wo ? PROG + 8 * ks + (lane & 7) : PF1 + 16 * ks + (lane & 15)) * 128, 1u, 0u};
    gemm7<R_PART>(smem, wave_id_, (wo ? Wo : w2) + (size_t)ks * 1024 * DM + cn * 128, nullptr, DM, ws + (wo ? OFF_O : OFF_G) + ks * 16 * 4096, (wo ? 64 : 56) * 4096, d,
                  ws + (wo ? OFF_P : OFF_P2) + (ks * DM + cn * 128) * (TLP ? 64 : 2), (size_t)32 * PROW, cnt + ((wo ? PWO : PF2) + idx) * 32, tmo,
                  wo ? 128 + ks * 32 + cn : 128 + (idx / 7) * 32 + 22 + idx % 7, mts, !wo);
  } else if (role == T_F2C) {
    const int lane = lane_id();
    const Dep d{cb, (unsigned)(PF1 + 48 + (lane & 7)) * 128, 1u, 0u};
    gemm7<R_PART2>(smem, wave_id_, w2 + (size_t)3072 * DM + idx * 256, nullptr, DM, ws + OFF_G + 48 * 4096, 56 * 4096, d,
                   ws + OFF_P2 + (3 * DM + idx * 256) * (TLP ? 64 : 2), (size_t)32 * PROW, cnt + (PF2 + 24 + idx) * 32, tmo, 128 + 96 + 25 + idx, mts, true);
  } else if (role == T_F1) {
    const int lane = lane_id();
    const Dep d{cb, (unsigned)(PLN + (lane & 15)) * 128, 2u, (unsigned)((lane >> 3) & 1)};
    gemm7<R_F1>(smem, wave_id_, w1 + 64 * idx, w3 + 64 * idx, FF, ws + OFF_H2, 65536, d, ws + OFF_G + idx * 4096, 56 * 4096, cnt + (PF1 + idx) * 32, tmo,
                128 + (idx / 14) * 32 + 8 + idx % 14, mts, false);
  } else if (role == T_FIN) {
    fin_role(smem, wave_id_, idx, mod, ws + OFF_P2, (const u16*)(ws + OFF_X1), out, cnt, tmo, mts);
    if (idx == 15 && opaque_tid() == 0) st8_sc1(ws + OFF_EP, u32x2{(unsigned)tag, (unsigned)(tag >> 32)});
  } else if (role == T_LN) {
    row_role(smem, wave_id_, idx & 7, idx >> 3, mod, ws + OFF_P, x, (u16*)(ws + OFF_X1), ws + OFF_H2, cnt, tmo, 256 + ((idx >> 1) & 3), mts);
  }
}

extern "C" void kernel_launch(void* const* d_in, const int* in_sizes, int n_in, void* d_out, int out_size, void* d_ws, size_t ws_size,
                              hipStream_t stream) {
  const u16* x    = (const u16*)d_in[0];
  const u16* mem0 = (const u16*)d_in[1];
  const u16* Wq   = (const u16*)d_in[2];
  const u16* Wk   = (const u16*)d_in[3];
  const u16* Wv   = (const u16*)d_in[4];
  const u16* Wo   = (const u16*)d_in[5];
  const u16* w1   = (const u16*)d_in[8];
  const u16* w3   = (const u16*)d_in[9];
  const u16* w2   = (const u16*)d_in[10];
  const u16* c    = (const u16*)d_in[11];
  const u16* wmod = (const u16*)d_in[12];
#define WSHIFT 0
  char* ws = (char*)d_ws + ((size_t)WSHIFT << 20);
  hipFuncSetAttribute((const void*)k_mega, hipFuncAttributeMaxDynamicSharedMemorySize, MEGA_LDS);
  k_mega<<<256, 512, MEGA_LDS, stream>>>(Wq, Wk, Wv, Wo, w1, w3, w2, x, c, wmod, mem0, ws, (u16*)d_out);
}
```

```cpp
#include <hip/hip_runtime.h>
#include <hip/hip_bf16.h>
#include <stdint.h>
#include <type_traits>
#pragma clang diagnostic ignored "-Winline-asm"
#pragma clang diagnostic ignored "-Wunused-value"

typedef unsigned short u16;
typedef short s16x4 __attribute__((ext_vector_type(4)));
typedef __bf16 bf16x8 __attribute__((ext_vector_type(8)));
typedef float f32x4 __attribute__((ext_vector_type(4)));
typedef float f32x2 __attribute__((ext_vector_type(2)));
typedef unsigned u32x4 __attribute__((ext_vector_type(4)));
typedef unsigned u32x2 __attribute__((ext_vector_type(2)));
#define LAS __attribute__((address_space(3)))

constexpr int SEQ = 2048, DM = 1024, NH = 32, DH = 128, HD = 4096, NMEM = 8, SEG = 32, NSEG = 64, FF = 3584;
constexpr unsigned SENT = 0xFFFFFFFFu;

__device__ __forceinline__ int lane_id() { int l; asm volatile("v_mbcnt_lo_u32_b32 %0, -1, 0\n\tv_mbcnt_hi_u32_b32 %0, -1, %0" : "=v"(l)); return l; }
#define WAVE_ID wave_id_
#define opaque_tid() (WAVE_ID * 64 + lane_id())
__device__ __forceinline__ float bf2f(u16 v) { return __builtin_bit_cast(float, (unsigned)v << 16); }
__device__ __forceinline__ float bflo(unsigned v) { return __builtin_bit_cast(float, v << 16); }
__device__ __forceinline__ float bfhi(unsigned v) { return __builtin_bit_cast(float, v & 0xffff0000u); }
__device__ __forceinline__ float bfr(float x) { return (float)(__bf16)x; }
__device__ __forceinline__ unsigned pack2(float a, float b) {
  typedef __bf16 bf16x2 __attribute__((ext_vector_type(2)));
  bf16x2 v; v[0] = (__bf16)a; v[1] = (__bf16)b; return __builtin_bit_cast(unsigned, v);
}
__device__ __forceinline__ unsigned nosent(unsigned v) { return v == SENT ? 0x7FC07FC0u : v; }
__device__ __forceinline__ bf16x8 asbf8(u32x4 v) { return __builtin_bit_cast(bf16x8, v); }
__device__ __forceinline__ s16x4 ass4(u32x2 v) { return __builtin_bit_cast(s16x4, v); }
__device__ __forceinline__ u32x2 trread(const char* lds_base, unsigned off) {
  s16x4 v = __builtin_amdgcn_ds_read_tr16_b64_v4i16((LAS s16x4*)(lds_base + off));
  return __builtin_bit_cast(u32x2, v);
}
__device__ __forceinline__ void glds16(const void* gsrc, unsigned lds_dst) {
  unsigned keep;
  asm volatile("s_mov_b32 %0, m0\n\ts_mov_b32 m0, %2\n\ts_nop 0\n\tglobal_load_lds_dwordx4 %1, off\n\ts_mov_b32 m0, %0"
               : "=&s"(keep) : "v"(gsrc), "s"(lds_dst) : "memory");
}
#define CNT_ " nt"
#define WNT_ " nt"
__device__ __forceinline__ void glds16_w(const void* gsrc, unsigned lds_dst) {
  unsigned keep;
  asm volatile("s_mov_b32 %0, m0\n\ts_mov_b32 m0, %2\n\ts_nop 0\n\tglobal_load_lds_dwordx4 %1, off" CNT_ "\n\ts_mov_b32 m0, %0"
               : "=&s"(keep) : "v"(gsrc), "s"(lds_dst) : "memory");
}
__device__ __forceinline__ void glds16_sc1(const void* gsrc, unsigned lds_dst) {
  unsigned keep;
  asm volatile("s_mov_b32 %0, m0\n\ts_mov_b32 m0, %2\n\ts_nop 0\n\tglobal_load_lds_dwordx4 %1, off sc1\n\ts_mov_b32 m0, %0"
               : "=&s"(keep) : "v"(gsrc), "s"(lds_dst) : "memory");
}
#define NTM 1
template <int B> __device__ __forceinline__ u32x4 ldnt(const void* p) { if (NTM & B) return __builtin_nontemporal_load((const u32x4*)p); else return *(const u32x4*)p; }
#define MFMA32(a, b, c) __builtin_amdgcn_mfma_f32_16x16x32_bf16((a), (b), (c), 0, 0, 0)

__device__ __forceinline__ void st16_sc1(void* p, u32x4 v) { asm volatile("global_store_dwordx4 %0, %1, off sc1\n\ts_nop 1" :: "v"(p), "v"(v) : "memory"); }
__device__ __forceinline__ void st8_sc1(void* p, u32x2 v) { asm volatile("global_store_dwordx2 %0, %1, off sc1\n\ts_nop 1" :: "v"(p), "v"(v) : "memory"); }
template <bool WT> __device__ __forceinline__ void st8(void* p, u32x2 v) { if (WT) st8_sc1(p, v); else *(u32x2*)p = v; }
template <bool WT> __device__ __forceinline__ void st16(void* p, u32x4 v) { if (WT) st16_sc1(p, v); else *(u32x4*)p = v; }
__device__ __forceinline__ u32x4 ld16_sc1(const void* p) {
  u32x4 r; asm volatile("global_load_dwordx4 %0, %1, off sc1\n\ts_waitcnt vmcnt(0)" : "=&v"(r) : "v"(p) : "memory"); return r;
}
__device__ __forceinline__ void ld16x4_sc1(const void* p, u32x4& a, u32x4& b, u32x4& c, u32x4& d) {
  asm volatile("global_load_dwordx4 %0, %4, off sc1\n\tglobal_load_dwordx4 %1, %4, off offset:1024 sc1\n\t"
               "global_load_dwordx4 %2, %4, off offset:2048 sc1\n\tglobal_load_dwordx4 %3, %4, off offset:3072 sc1\n\ts_waitcnt vmcnt(0)"
               : "=&v"(a), "=&v"(b), "=&v"(c), "=&v"(d) : "v"(p) : "memory");
}
typedef __amdgpu_buffer_rsrc_t rsrc_t;
__device__ __forceinline__ rsrc_t mkrs(const void* p) { return __builtin_amdgcn_make_buffer_rsrc((void*)p, 0, 0x7FFFFFFF, 0x00020000); }
__device__ __forceinline__ u32x4 bld16(rsrc_t r, unsigned off) { return __builtin_amdgcn_raw_buffer_load_b128(r, off, 0, 16); }
__device__ __forceinline__ u32x2 bld8(rsrc_t r, unsigned off) { return __builtin_amdgcn_raw_buffer_load_b64(r, off, 0, 16); }
__device__ __forceinline__ bool ok4(u32x4 v) { return v.x != SENT && v.y != SENT && v.z != SENT && v.w != SENT; }
constexpr int SPIN_CAP = 2000000;
__device__ __forceinline__ bool give_up(int spins, unsigned* tmo, unsigned code) {
  if ((spins & 4095) != 4095) return false;
  if (spins > SPIN_CAP) { __hip_atomic_store(tmo, code, __ATOMIC_RELAXED, __HIP_MEMORY_SCOPE_AGENT); return true; }
  return __hip_atomic_load(tmo, __ATOMIC_RELAXED, __HIP_MEMORY_SCOPE_AGENT) != 0u;
}
template <bool ACQ = true>
__device__ __forceinline__ void wait_cnt(unsigned* c, unsigned n, unsigned* tmo) {
  for (int spins = 0;; ++spins) {
    const unsigned v = __hip_atomic_load(c, __ATOMIC_RELAXED, __HIP_MEMORY_SCOPE_AGENT);
    if (v >= n) break;
    __builtin_amdgcn_s_sleep(8);
    if (give_up(spins, tmo, 5)) break;
  }
  if (ACQ) {
    __builtin_amdgcn_fence(__ATOMIC_ACQUIRE, "agent");
    asm volatile("s_waitcnt vmcnt(0)" ::: "memory");
  }
}

__device__ __forceinline__ const char* uni(const char* p) {
  const unsigned long long v = (unsigned long long)p;
  const unsigned lo = __builtin_amdgcn_readfirstlane((unsigned)v), hi = __builtin_amdgcn_readfirstlane((unsigned)(v >> 32));
  return (const char*)(((unsigned long long)hi << 32) | lo);
}
#define TSCLK "s_memrealtime"
#define TC(k) do { } while (0)
#define TS(k) do { } while (0)
#define TSDECL do { } while (0)
#define TSFLUSH(mask) do { } while (0)

constexpr size_t OFF_MOD  = 0;
constexpr size_t OFF_MOD2 = 65536;
constexpr size_t OFF_TMO  = 98304;
constexpr size_t OFF_EP   = 98304 + 256;
constexpr size_t OFF_CLM  = 102400;
constexpr size_t OFF_INI  = 131072;
constexpr size_t OFF_H1   = 262144;
constexpr size_t OFF_Q    = OFF_H1 + (size_t)SEQ * DM * 2;
constexpr size_t OFF_K    = OFF_Q + (size_t)SEQ * HD * 2;
constexpr size_t OFF_V    = OFF_K + (size_t)SEQ * HD * 2;
constexpr size_t OFF_O    = OFF_V + (size_t)SEQ * HD * 2;
constexpr size_t OFF_X1   = OFF_O + (size_t)SEQ * HD * 2;
constexpr size_t OFF_H2   = OFF_X1 + (size_t)SEQ * DM * 2;
constexpr size_t OFF_G    = OFF_H2 + (size_t)SEQ * DM * 2;
constexpr size_t OFF_XCH  = (size_t)100 << 20;
constexpr size_t OFF_P    = (size_t)112 << 20;
constexpr size_t OFF_P2   = (size_t)128 << 20;
constexpr size_t OFF_DUMP = (size_t)144 << 20;
constexpr int PROW = 4 * DM * 2;
#define TLQ 1
#define TLP 1
static_assert(OFF_G + (size_t)SEQ * FF * 2 <= OFF_XCH, "layout");
__device__ __forceinline__ char* xch_base(char* ws) { return (char*)(((size_t)(ws + OFF_XCH) + ((2u << 20) - 1)) & ~(size_t)((2u << 20) - 1)); }
constexpr int PROG = 16;
constexpr int PQKV = 64;
constexpr int PL1 = 96;
constexpr int PARM = 128;
constexpr int PWO = 192;
constexpr int PLN = 224;
constexpr int PF2 = 320;
constexpr int PF1 = 448;
#define BEARLY 58
#define BEG BEARLY
#define BEH BEARLY
#define ARMW 52
#define LAZY 0
static_assert(BEH <= BEARLY && BEG <= BEARLY && BEH % 2 == 0, "");
constexpr int L_RED = 131072;
constexpr int L_SLOT = L_RED + 16384;
constexpr int L_GB = L_SLOT + 1024;
constexpr int MEGA_LDS = 163840;
static_assert(BEARLY % 2 == 0, "");
static_assert(L_GB + 12288 + 2048 <= MEGA_LDS, "lds");
__device__ __forceinline__ int bswz(int row) { return (((row >> 1) & 1) | (((row >> 3) & 1) << 1)) << 1; }
template <int OFF, bool SC1 = false>
__device__ __forceinline__ void glds16s(const char* sbase, unsigned voff, unsigned lds_base) {
  unsigned keep;
  if (SC1)
    asm volatile("s_mov_b32 %0, m0\n\ts_add_u32 m0, %3, %4\n\ts_nop 0\n\tglobal_load_lds_dwordx4 %1, %2 sc1\n\ts_mov_b32 m0, %0"
                 : "=&s"(keep) : "v"(voff), "s"(sbase), "s"(lds_base), "i"(OFF) : "memory", "scc");
  else
    asm volatile("s_mov_b32 %0, m0\n\ts_add_u32 m0, %3, %4\n\ts_nop 0\n\tglobal_load_lds_dwordx4 %1, %2\n\ts_mov_b32 m0, %0"
                 : "=&s"(keep) : "v"(voff), "s"(sbase), "s"(lds_base), "i"(OFF) : "memory", "scc");
}
template <int OFF>
__device__ __forceinline__ void glds16x2s(const char* sbase, unsigned voff, unsigned lds_base) {
  unsigned keep;
  asm volatile("s_mov_b32 %0, m0\n\ts_add_u32 m0, %3, %4\n\ts_nop 0\n\tglobal_load_lds_dwordx4 %1, %2 sc1\n\tglobal_load_lds_dwordx4 %1, %2 offset:1024 sc1\n\ts_mov_b32 m0, %0"
               : "=&s"(keep) : "v"(voff), "s"(sbase), "s"(lds_base), "i"(OFF) : "memory", "scc");
}
__device__ __forceinline__ void st16s(const char* sb, unsigned vo, u32x4 v) { asm volatile("global_store_dwordx4 %0, %1, %2 sc1\n\ts_nop 1" :: "v"(vo), "v"(v), "s"(sb) : "memory"); }
__device__ __forceinline__ void head_to_lds(const char* src, int lane, unsigned lds) {
  if (TLQ) {
    const int c = (lane & 15) ^ (lane >> 4);
    const unsigned v0 = (unsigned)((c >> 2) * 2048 + (lane >> 4) * 64 + (c & 3) * 16);
#pragma unroll
    for (int i = 0; i < 8; ++i) glds16s<0, true>(src + i * 256, v0 ^ ((i & 3) * 2048), lds + i * 1024);
  } else {
#pragma unroll
    for (int i = 0; i < 4; ++i) glds16x2s<0>(src + i * 2048, (unsigned)lane * 16, lds + i * 2048);
  }
}
#define WAIT_V(n) asm volatile("s_waitcnt vmcnt(" #n ")" ::: "memory")
#define BAR() do { asm volatile("s_waitcnt lgkmcnt(0)" ::: "memory"); __builtin_amdgcn_s_barrier(); asm volatile("" ::: "memory"); } while (0)
__device__ __forceinline__ void st4_sc1(void* p, unsigned v) { asm volatile("global_store_dword %0, %1, off sc1" :: "v"(p), "v"(v) : "memory"); }
__device__ __forceinline__ unsigned ld4_sc1(const void* p) {
  unsigned r; asm volatile("global_load_dword %0, %1, off sc1\n\ts_waitcnt vmcnt(0)" : "=&v"(r) : "v"(p) : "memory"); return r;
}
template <int CTRL> __device__ __forceinline__ unsigned dppu(unsigned x) { return (unsigned)__builtin_amdgcn_update_dpp(0, (int)x, CTRL, 0xf, 0xf, false); }
__device__ __forceinline__ unsigned rowmin16(unsigned v) {
  v = min(v, dppu<0x128>(v)); v = min(v, dppu<0x124>(v)); v = min(v, dppu<0x122>(v)); v = min(v, dppu<0x121>(v));
  return v;
}
__device__ __forceinline__ unsigned rdl(unsigned v, int l) { return (unsigned)__builtin_amdgcn_readlane((int)v, l); }
constexpr float SCALE = 0.08838834764831845f;
constexpr float LOG2E = 1.4426950408889634f;
template <int N> __device__ __forceinline__ void delay64() {
  if (N >= 16) { __builtin_amdgcn_s_sleep(15); delay64<(N >= 16 ? N - 15 : 0)>(); } else if (N > 0) __builtin_amdgcn_s_sleep(N);
}
__device__ __forceinline__ void sw16(float x, float& a, float& b) {
  a = x; b = x; asm volatile("s_nop 1\n\tv_permlane16_swap_b32 %0, %1" : "+v"(a), "+v"(b));
}
__device__ __forceinline__ void sw32(float x, float& a, float& b) {
  a = x; b = x; asm volatile("s_nop 1\n\tv_permlane32_swap_b32 %0, %1" : "+v"(a), "+v"(b));
}
__device__ __forceinline__ float red16_32_max(float x) {
  float a, b; sw16(x, a, b); x = fmaxf(a, b); sw32(x, a, b); return fmaxf(a, b);
}
__device__ __forceinline__ float red16_32_add(float x) {
  float a, b; sw16(x, a, b); x = a + b; sw32(x, a, b); return a + b;
}
__device__ __forceinline__ unsigned cvtpk(float a, float b) { unsigned r; asm("v_cvt_pk_bf16_f32 %0, %1, %2" : "=v"(r) : "v"(a), "v"(b)); return r; }
__device__ __forceinline__ float max3(float a, float b, float c) { float r; asm("v_max3_f32 %0, %1, %2, %3" : "=v"(r) : "v"(a), "v"(b), "v"(c)); return r; }
__device__ __forceinline__ float max2(float a, float b) { float r; asm("v_max_f32 %0, %1, %2" : "=v"(r) : "v"(a), "v"(b)); return r; }
__device__ __forceinline__ unsigned ror8u(unsigned x) { return (unsigned)__builtin_amdgcn_update_dpp(0, (int)x, 0x128, 0xf, 0xf, false); }
__device__ __forceinline__ float ror8(float x) { return __builtin_bit_cast(float, ror8u(__builtin_bit_cast(unsigned, x))); }
__device__ __forceinline__ float add_ror8(float x) {
  return x + __builtin_bit_cast(float, __builtin_amdgcn_update_dpp(0, __builtin_bit_cast(int, x), 0x128, 0xf, 0xf, false));
}

#define RSL 4
__device__ __forceinline__ void ini_request(const char* ws, unsigned lds) {
  const char* p = uni(ws + OFF_INI);
  asm volatile("s_mov_b32 m0, %2\n\ts_nop 3\n\tglobal_load_lds_dword %0, %1 sc1\n\ts_add_u32 m0, %2, 252\n\ts_nop 0\n\tglobal_load_lds_dword %0, %1 offset:4 sc1"
               :: "v"(lane_id() * 128), "s"(p), "s"(lds) : "memory", "m0", "scc");
}
__device__ __forceinline__ unsigned ini_resolve(const char* ws, const char* l, unsigned long long tag) {
  const int lane = lane_id();
  u32x2 v = u32x2{~(unsigned)tag, 0u};
  if (l) v = u32x2{*(const volatile LAS unsigned*)(l + lane * 4), *(const volatile LAS unsigned*)(l + 256 + lane * 4)};
  for (int spins = 0; !__all(v.x == (unsigned)tag && (v.y >> 16) == (unsigned)(tag >> 48)) && spins < SPIN_CAP; ++spins) {
    if (spins) __builtin_amdgcn_s_sleep(RSL);
    asm volatile("global_load_dwordx2 %0, %1, off sc1\n\ts_waitcnt vmcnt(0)" : "=&v"(v) : "v"(ws + OFF_INI + lane * 128) : "memory");
  }
  const unsigned long long up = __ballot((v.y & 3u) == 1u), dn = __ballot((v.y & 3u) == 2u);
  unsigned par = 0;
#pragma unroll
  for (int c = 0; c < 3; ++c) { const unsigned long long m = (0x249ull << c) * 0x0001000100010001ull; par |= (__popcll(up & m) > __popcll(dn & m) ? 0u : 1u) << c; }
  par = __builtin_amdgcn_readfirstlane(par);
  asm volatile("" : "+s"(par));
  return par;
}
__device__ __forceinline__ unsigned ini_resolve1(char* smem, int wave_id_, const char* ws, unsigned long long tag) {
  volatile LAS unsigned* w = (volatile LAS unsigned*)(smem + L_SLOT + 800);
  if (WAVE_ID == 1) *w = ini_resolve(ws, nullptr, tag);
  BAR();
  return __builtin_amdgcn_readfirstlane(*w);
}
template <int M0> __device__ __forceinline__ unsigned qkv_avail(const unsigned* line) {
  const unsigned v = ld4_sc1(line + (lane_id() & 15));
  unsigned r = NSEG;
#pragma unroll
  for (int m = M0; m < 3; ++m) {
    unsigned a = rdl(v, m);
#pragma unroll
    for (int p = 1; p < 3; ++p) { const unsigned w = rdl(v, 4 * p + m); if (a >= (w >> 8)) a = max(a, w & 255u); }
    r = min(r, a);
  }
  return r;
}
__device__ __forceinline__ void wait_l1(const unsigned* cnt, unsigned n, unsigned* tmo) {
  const int lane = lane_id();
  for (int spins = 0;; ++spins) {
    const unsigned v = ld4_sc1(cnt + (PL1 + (lane & 31)) * 32);
    if (__all(v >= n)) break;
    __builtin_amdgcn_s_sleep(8);
    if (give_up(spins, tmo, 12)) break;
  }
}
constexpr int NARM = 16;
__device__ __forceinline__ void wait_arm(const unsigned* cnt, unsigned* tmo) {
  for (int spins = 0;; ++spins) {
    if (__all(ld4_sc1(cnt + (PARM + min(lane_id(), NARM - 1)) * 32) != 0u)) break;
    __builtin_amdgcn_s_sleep(8);
    if (give_up(spins, tmo, 12)) break;
  }
}
#define GKV 1
__device__ __forceinline__ void wait_k0(const unsigned* cnt, int h, unsigned* tmo, bool kv = true) {
  for (int spins = 0;; ++spins) {
    const int l = lane_id();
    const unsigned v = ld4_sc1(cnt + (PQKV + h) * 32 + (l == 0 || !kv ? 3 : 1 + (l & 1)));
    if (__all(v >= (l == 0 ? 1u : (unsigned)GKV))) break;
    __builtin_amdgcn_s_sleep(32);
    if (give_up(spins, tmo, 13)) break;
  }
}
#define IS(k) do { } while (0)
#define KTON 0
#define WCLK(slot, b) do { } while (0)
#define WTM(slot, b, k) do { } while (0)
#define WTS(slot, b, k) do { } while (0)
#define KTS(k_) do { } while (0)
#define STS(ws_, slot_, k_) do { } while (0)
typedef __bf16 bf16x2 __attribute__((ext_vector_type(2)));
__device__ __forceinline__ f32x2 pkfma(f32x2 a, f32x2 b, f32x2 c) { return __builtin_elementwise_fma(a, b, c); }
__device__ __forceinline__ float wavesum(float v) {
  v += __builtin_bit_cast(float, dppu<0x128>(__builtin_bit_cast(unsigned, v))); v += __builtin_bit_cast(float, dppu<0x124>(__builtin_bit_cast(unsigned, v)));
  v += __builtin_bit_cast(float, dppu<0x122>(__builtin_bit_cast(unsigned, v))); v += __builtin_bit_cast(float, dppu<0x121>(__builtin_bit_cast(unsigned, v)));
  return red16_32_add(v);
}
__device__ __forceinline__ void bst16(rsrc_t r, unsigned off, unsigned soff, u32x4 v) { __builtin_amdgcn_raw_buffer_store_b128(v, r, off + soff, 0, 16); }
__device__ __forceinline__ float adds(unsigned v, unsigned sel, float c) { return __builtin_amdgcn_fdot2_f32_bf16(__builtin_bit_cast(bf16x2, v), __builtin_bit_cast(bf16x2, sel), c, false); }
__device__ __forceinline__ void sum4(const u32x4 (&p)[4], f32x2 (&a)[4]) {
  unsigned slo = 0x00003F80u, shi = 0x3F800000u;
  asm("" : "+s"(slo), "+s"(shi));
#pragma unroll
  for (int e = 0; e < 4; ++e) {
    float lo = bflo(p[0][e]), hi = bfhi(p[0][e]);
#pragma unroll
    for (int q = 1; q < 4; ++q) { lo = adds(p[q][e], slo, lo); hi = adds(p[q][e], shi, hi); }
    a[e] = f32x2{lo, hi};
  }
}
__device__ __forceinline__ u32x4 norm8(const f32x2 (&v)[4], const char* mt, float mb, float rden) {
  const f32x4 h0 = *(const f32x4*)(mt + 4096), h1 = *(const f32x4*)(mt + 4096 + 1024), c0 = *(const f32x4*)(mt + 8192), c1 = *(const f32x4*)(mt + 8192 + 1024);
  const f32x2 m2 = f32x2{mb, mb}, d2 = f32x2{rden, rden};
  const f32x2 r0 = pkfma(v[0] - m2, f32x2{c0[0], c0[1]} * d2, f32x2{h0[0], h0[1]}), r1 = pkfma(v[1] - m2, f32x2{c0[2], c0[3]} * d2, f32x2{h0[2], h0[3]});
  const f32x2 r2 = pkfma(v[2] - m2, f32x2{c1[0], c1[1]} * d2, f32x2{h1[0], h1[1]}), r3 = pkfma(v[3] - m2, f32x2{c1[2], c1[3]} * d2, f32x2{h1[2], h1[3]});
  return u32x4{pack2(r0[0], r0[1]), pack2(r1[0], r1[1]), pack2(r2[0], r2[1]), pack2(r3[0], r3[1])};
}
__device__ __forceinline__ bool fin_half(const char* P, const u16* res, const char* gbt, u16* __restrict__ o1, int row, int hf, int lane, bool chk) {
  const rsrc_t rp = mkrs(P), rr = mkrs(res);
  const unsigned po = (unsigned)((row >> 5) * 32 * PROW + (hf * 16 + (lane >> 2)) * 2048 + (row & 31) * 64 + (lane & 3) * 16), ro = (unsigned)(row * DM * 2 + hf * 1024 + lane * 16);
  u32x4 p[4];
#pragma unroll
  for (int q = 0; q < 4; ++q) p[q] = bld16(rp, po + q * 65536);
  const u32x4 rv = bld16(rr, ro);
  if (chk && !__all(ok4(p[0]) && ok4(p[1]) && ok4(p[2]) && ok4(p[3]) && ok4(rv))) return false;
  f32x2 a[4];
  sum4(p, a);
  const u32x4 gv = *(const u32x4*)(gbt + hf * 1024 + lane * 16);
  u32x4 o;
#pragma unroll
  for (int e = 0; e < 4; ++e) { const f32x2 s = pkfma(f32x2{bflo(gv[e]), bfhi(gv[e])}, a[e], f32x2{bflo(rv[e]), bfhi(rv[e])}); o[e] = pack2(s[0], s[1]); }
  st16_sc1(o1 + (size_t)row * DM + hf * 512 + lane * 8, o);
  return true;
}
__device__ __forceinline__ void ln_half(const u32x4 (&p)[4], u32x4 rv, const f32x4 (&tb)[6], volatile LAS float* xs, const char* x1, unsigned xo, const char* h2, unsigned ho,
                                        int wave, int lane) {
  f32x2 a[4], v[4];
  sum4(p, a);
  u32x4 o;
#pragma unroll
  for (int e = 0; e < 4; ++e) {
    const f32x2 s = pkfma(f32x2{tb[e >> 1][2 * (e & 1)], tb[e >> 1][2 * (e & 1) + 1]}, a[e], f32x2{bflo(rv[e]), bfhi(rv[e])});
    o[e] = pack2(s[0], s[1]);
    v[e] = f32x2{bflo(o[e]), bfhi(o[e])};
  }
  const float c = __builtin_bit_cast(float, __builtin_amdgcn_readfirstlane(__builtin_bit_cast(int, v[0][0])));
  f32x2 s2 = f32x2{0, 0}, q2 = s2;
#pragma unroll
  for (int i = 0; i < 4; ++i) { const f32x2 d = v[i] - f32x2{c, c}; s2 += d; q2 = pkfma(d, d, q2); }
  float s = s2[0] + s2[1], q = q2[0] + q2[1];
#define DA(x, k) x += __builtin_bit_cast(float, dppu<k>(__builtin_bit_cast(unsigned, x)))
  DA(s, 0x128); DA(q, 0x128); DA(s, 0x124); DA(q, 0x124); DA(s, 0x122); DA(q, 0x122); DA(s, 0x121); DA(q, 0x121);
#undef DA
  {
    asm volatile("s_nop 1\n\tv_permlane16_swap_b32 %0, %1" : "+v"(s), "+v"(q));
    s += q; q = s;
    asm volatile("s_nop 1\n\tv_permlane32_swap_b32 %0, %1" : "+v"(s), "+v"(q));
    s += q;
  }
  if (lane < 48 && (lane & 15) == 0) xs[wave * 4 + (lane >> 4)] = lane == 32 ? c : s;
  st16s(x1, xo, o);
  BAR();
  const f32x4 u0 = *(const volatile LAS f32x4*)(xs + (wave & 6) * 4), u1 = *(const volatile LAS f32x4*)(xs + (wave | 1) * 4);
  const float m0 = u0[0] * (2.0f / DM), m1 = u1[0] * (2.0f / DM);
  const float dm = (u0[2] - u1[2]) + (m0 - m1);
  const float mean = ((u0[2] + m0) + (u1[2] + m1)) * 0.5f;
  const float qq = ((u0[1] - u0[0] * m0) + (u1[1] - u1[0] * m1)) + dm * dm * (DM / 4);
  const float mb = bfr(mean), vb = bfr(fmaxf(qq, 0.0f) * (1.0f / DM));
  const float rden = __builtin_amdgcn_rcpf(bfr(__builtin_amdgcn_sqrtf(bfr(vb + 1e-5f))));
  const f32x2 m2 = f32x2{mb, mb}, d2 = f32x2{rden, rden};
  u32x4 r;
#pragma unroll
  for (int e = 0; e < 4; ++e) {
    const f32x2 t = pkfma(v[e] - m2, f32x2{tb[4 + (e >> 1)][2 * (e & 1)], tb[4 + (e >> 1)][2 * (e & 1) + 1]} * d2, f32x2{tb[2 + (e >> 1)][2 * (e & 1)], tb[2 + (e >> 1)][2 * (e & 1) + 1]});
    r[e] = pack2(t[0], t[1]);
  }
  st16s(h2, ho, r);
}
__device__ __forceinline__ void row_table(char* tab, const float* mod, const unsigned* cnt, unsigned* tmo, int tid) {
  wait_l1(cnt, 9u, tmo);
  const rsrc_t rm = mkrs((const char*)mod + OFF_MOD2);
#pragma unroll
  for (int i = 0; i < 2; ++i) {
    const int u = tid + 512 * i, a = u >> 8, c = u & 255;
    f32x4 v = __builtin_bit_cast(f32x4, bld16(rm, (unsigned)u * 16));
    v = f32x4{bfr(v[0]), bfr(v[1]), bfr(v[2]), bfr(v[3])};
    if (a == 2) v = f32x4{bfr(1.0f + v[0]), bfr(1.0f + v[1]), bfr(1.0f + v[2]), bfr(1.0f + v[3])};
    if (a < 3) *(f32x4*)(tab + a * 4096 + ((c >> 7) * 2 + (c & 1)) * 1024 + ((c >> 1) & 63) * 16) = v;
    else *(u32x2*)(tab + 12288 + c * 8) = u32x2{pack2(v[0], v[1]), pack2(v[2], v[3])};
  }
  __syncthreads();
}
#define LS(i) do { } while (0)
#define ARM0 8
#define LNSL 2
__device__ __forceinline__ void row_role(char* smem, int wave_id_, int grp, int set, const float* mod, const char* P, const u16* x, u16* x1, char* h2c,
                                         unsigned* cnt, unsigned* tmo, int slot, unsigned long long* mts) {
  const int tid = opaque_tid(), lane = tid & 63, wave = WAVE_ID;
  if (tid < 2) *(volatile LAS unsigned*)(smem + 16384 + 256 + tid * 4) = 0u;
  row_table(smem, mod, cnt, tmo, tid);
  static_assert(TLP, "");
  const int hf = wave & 1, r4 = 4 * grp + (wave >> 1);
  const unsigned pw2 = (unsigned)(PWO + ((lane >> 2) & 3) * 8 + hf * 4 + (lane & 3)) * 128 + ((lane >> 4) & 1) * 4;
  auto rdy = [&](unsigned v, int bb, bool early) { const unsigned long long m = __ballot(v > (unsigned)bb); return ((early ? m | m >> 16 : m) & 0xFFFFull) == 0xFFFFull; };
  unsigned* myp = cnt + (PLN + 8 * set + grp) * 32;
  unsigned pv = 0;
  u32x4 p[4], rv, rn;
  bool req = false;
  const char* pb = uni(P); const char* cb = uni((const char*)cnt); const char* xs_ = uni((const char*)x);
  auto publish = [&](int n, int e = 0) {
    if (lane == 0 && (__hip_atomic_fetch_add((LAS unsigned*)(smem + 16384 + 256 + 4 * e), 1u, __ATOMIC_RELAXED, __HIP_MEMORY_SCOPE_WORKGROUP) & 7u) == 7u) {
      if (e) st8_sc1(myp, u32x2{(unsigned)n - 1u, (unsigned)n}); else st4_sc1(myp, (unsigned)n);
    }
  };
  f32x4 tb[6];
#pragma unroll
  for (int i = 0; i < 6; ++i) tb[i] = *(const f32x4*)(smem + (i >> 1) * 4096 + hf * 2048 + (i & 1) * 1024 + lane * 16);
  const unsigned po = (unsigned)((hf * 16 + (lane >> 2)) * 2048 + r4 * 64 + (lane & 3) * 16), xo = (unsigned)(r4 * DM * 2 + hf * 1024 + lane * 16);
  const unsigned ho = (unsigned)(r4 * 128 + (((lane & 7) ^ (r4 & 7)) << 4) + (lane >> 3) * 4096 + hf * 32768);
  rn = *(const u32x4*)(xs_ + set * 65536 + xo);
  for (int bb = set; bb < NSEG; bb += 2) {
    for (bool early = true;; early = false) {
      for (int spins = 0; !rdy(pv, bb, early); ++spins) {
        pv = ld4_sc1((const char*)cnt + pw2);
        if (rdy(pv, bb, early)) break;
        __builtin_amdgcn_s_sleep(LNSL);
        if (give_up(spins, tmo, 11)) break;
      }
      WTM(slot, bb, 0);
      LS(0);
      asm volatile("s_nop 4\n\tglobal_load_dwordx4 %0, %5, %9 sc1\n\tglobal_load_dwordx4 %1, %6, %9 sc1\n\tglobal_load_dwordx4 %2, %7, %9 sc1\n\tglobal_load_dwordx4 %3, %8, %9 sc1\n\t"
                   "global_load_dword %4, %10, %11 sc1\n\ts_waitcnt vmcnt(0)"
                   : "=&v"(p[0]), "=&v"(p[1]), "=&v"(p[2]), "=&v"(p[3]), "=&v"(pv)
                   : "v"(po), "v"(po + 65536), "v"(po + 131072), "v"(po + 196608), "s"(pb + (size_t)bb * 32 * PROW), "v"(pw2), "s"(cb) : "memory");
      if (bb < BEARLY || !early || __all(ok4(p[0]) && ok4(p[1]) && ok4(p[2]) && ok4(p[3]))) break;
    }
    asm volatile("v_mov_b32 %0, %4\n\tv_mov_b32 %1, %5\n\tv_mov_b32 %2, %6\n\tv_mov_b32 %3, %7" : "=&v"(rv.x), "=&v"(rv.y), "=&v"(rv.z), "=&v"(rv.w) : "v"(rn.x), "v"(rn.y), "v"(rn.z), "v"(rn.w) : "memory");
    if (bb + 2 < NSEG) rn = *(const u32x4*)(xs_ + (size_t)(bb + 2) * 65536 + xo);
    LS(1);
    if (req && bb < BEH) publish(bb >> 1);
    ln_half(p, rv, tb, (volatile LAS float*)(smem + 16384 + ((bb >> 1) & 1) * 128), uni((const char*)x1) + (size_t)bb * 65536, xo, uni(h2c) + (size_t)bb * 65536, ho, wave, lane);
    LS(2);
    {
      static_assert(BEG == BEARLY && BEH == BEARLY && ARM0 + 2 * (NSEG - BEARLY) * 3 <= ARMW, "");
      const int j = (bb - ARM0) >> 1, ab = BEARLY + j / 3, o = (8 * set + grp) * 8192 + tid * 16;
      if (bb >= ARM0 && ab < NSEG) {
        char* w0 = (char*)tmo - OFF_TMO;
        const u32x4 z = u32x4{SENT, SENT, SENT, SENT};
        char* p0 = w0 + (j % 3 == 0 ? OFF_O : (j % 3 == 1 ? OFF_P : OFF_P2)) + (size_t)ab * 262144 + o;
        st16_sc1(p0, z); st16_sc1(p0 + 131072, z);
        if (j % 3 == 2) st16_sc1(w0 + (set ? OFF_H2 : OFF_X1) + (size_t)ab * 65536 + (o & 65535), z);
        else if ((j % 3) * 131072 + o < 229376) st16_sc1(w0 + OFF_G + (size_t)ab * 229376 + (j % 3) * 131072 + o, z);
      }
      if (bb >= ARM0 && j == (NSEG - BEARLY) * 3 && tid == 0) st4_sc1(cnt + (PARM + 8 * set + grp) * 32, 1u);
    }
    if (bb >= BEH) publish((bb >> 1) + 1, 1);
    req = bb + 2 < NSEG && (bb >= BEH || rdy(pv, bb + 2, true));
    if (!req) {
      if (bb + 2 < NSEG) pv = ld4_sc1((const char*)cnt + pw2);
      if (bb >= BEH) BAR();
      WAIT_V(0);
      LS(3);
      publish((bb >> 1) + 1);
    }
    WTM(slot, bb, 2);
  }
}
#define L1G 0
__device__ __forceinline__ void l1_role(char* smem, int wave_id_, int c, const u16* __restrict__ x, const float* __restrict__ mod, const u16* __restrict__ cvec,
                                        const u16* __restrict__ wmod, char* __restrict__ h1c, unsigned*& cnt, unsigned* tmo, unsigned long long* mts, unsigned long long tag) {
  const int tid = opaque_tid(), lane = tid & 63, wave = WAVE_ID;
  u32x4 xr[8][2];
  const u16* src = x + (size_t)(8 * c + wave) * DM + lane * 8;
  xr[0][0] = ldnt<2>(src); xr[0][1] = ldnt<2>(src + 512);
  {
    const float sva = bf2f(cvec[tid]), svb = bf2f(cvec[tid + 512]);
    cnt += (ini_resolve1(smem, wave_id_, (const char*)mod - OFF_MOD, tag) >> 2) << 11;
    if (wave == 7) IS(6);
    f32x4 m[4];
    const rsrc_t rm = mkrs(mod);
#pragma unroll
    for (int k = 0; k < 4; ++k) m[k] = __builtin_bit_cast(f32x4, bld16(rm, (unsigned)(k * (2 * DM) + tid * 4) * 4));
    f32x4 v = (m[0] + m[1]) + (m[2] + m[3]);
    v = f32x4{bfr(v[0]), bfr(v[1]), bfr(v[2]), bfr(v[3])};
    const int a = tid >> 8, cc = tid & 255;
    if (a) v = f32x4{bfr(1.0f + v[0]), bfr(1.0f + v[1]), bfr(1.0f + v[2]), bfr(1.0f + v[3])};
    *(f32x4*)(smem + 4096 + a * 4096 + ((cc >> 7) * 2 + (cc & 1)) * 1024 + ((cc >> 1) & 63) * 16) = v;
    float* sv = (float*)(smem + 12288);
    sv[tid] = bfr(sva / (1.0f + __expf(-sva))); sv[tid + 512] = bfr(svb / (1.0f + __expf(-svb)));
  }
  __syncthreads();
  if (wave == 7) IS(7);
#pragma unroll
  for (int p = 0; p < 8; ++p) {
    f32x2 v[2][4];
#pragma unroll
    for (int i = 0; i < 2; ++i)
#pragma unroll
      for (int e = 0; e < 4; ++e) v[i][e] = f32x2{bflo(xr[p][i][e]), bfhi(xr[p][i][e])};
    const f32x2 s2 = ((v[0][0] + v[0][1]) + (v[0][2] + v[0][3])) + ((v[1][0] + v[1][1]) + (v[1][2] + v[1][3]));
    const float mean = wavesum(s2[0] + s2[1]) * (1.0f / DM);
    f32x2 q2 = f32x2{0, 0};
#pragma unroll
    for (int i = 0; i < 8; ++i) { const f32x2 d = v[i >> 2][i & 3] - f32x2{mean, mean}; q2 = pkfma(d, d, q2); }
    const float q = wavesum(q2[0] + q2[1]);
    if (p == 0 && wave == 0) STS((char*)tmo - OFF_TMO, 128 + (c >> 3) * 32 + 24 + (c & 7), 0);
    const float mb = bfr(mean), vb = bfr(q * (1.0f / DM));
    const float rden = 1.0f / bfr(sqrtf(bfr(vb + 1e-5f)));
    const int row = 256 * p + 8 * c + wave, rb = row & 31;
    char* dst = h1c + (size_t)(row >> 5) * 65536 + (lane >> 3) * 4096 + rb * 128 + (((lane & 7) ^ (rb & 7)) << 4);
    const char* ml = smem + lane * 16;
#pragma unroll
    for (int i = 0; i < 2; ++i) st16_sc1(dst + i * 32768, norm8(v[i], ml + i * 2048, mb, rden));
    WAIT_V(0);
    __builtin_amdgcn_s_barrier();
    if (tid == 0) st4_sc1(cnt + (PL1 + c) * 32, (unsigned)(p + 1));
    if (p == 0) {
      if (L1G) wait_k0(cnt, c, tmo, L1G == 1);
#pragma unroll
      for (int pp = 1; pp < 8; ++pp) { xr[pp][0] = ldnt<2>(src + (size_t)pp * 256 * DM); xr[pp][1] = ldnt<2>(src + (size_t)pp * 256 * DM + 512); }
    }
    if (wave == 0) WTS(c, p, 0);
    if (p == 0 && wave == 0) STS((char*)tmo - OFF_TMO, 128 + (c >> 3) * 32 + 24 + (c & 7), 1);
  }
  wait_k0(cnt, c, tmo);
  {
    const int cg = tid & 15, r0 = tid >> 4;
    const u16* wp = wmod + (size_t)r0 * (6 * DM) + 2 * DM + 128 * c + cg * 8;
    const float* sv = (const float*)(smem + 12288) + r0;
    f32x2 acc[4] = {f32x2{0, 0}, f32x2{0, 0}, f32x2{0, 0}, f32x2{0, 0}};
#pragma unroll 1
    for (int o = 0; o < 4; ++o) {
      u32x4 w[8];
#pragma unroll
      for (int i = 0; i < 8; ++i) w[i] = ldnt<1>(wp + (size_t)(o * 8 + i) * 32 * (6 * DM));
#pragma unroll
      for (int i = 0; i < 8; ++i) {
        const float s = sv[(o * 8 + i) * 32];
#pragma unroll
        for (int e = 0; e < 4; ++e) acc[e] = pkfma(f32x2{s, s}, f32x2{bflo(w[i][e]), bfhi(w[i][e])}, acc[e]);
      }
    }
    float* red = (float*)(smem + 16384);
    *(f32x4*)(red + r0 * 128 + cg * 8) = f32x4{acc[0][0], acc[0][1], acc[1][0], acc[1][1]};
    *(f32x4*)(red + r0 * 128 + cg * 8 + 4) = f32x4{acc[2][0], acc[2][1], acc[3][0], acc[3][1]};
    __syncthreads();
    if (tid < 128) {
      float s = 0;
#pragma unroll
      for (int r = 0; r < 32; ++r) s += red[r * 128 + tid];
      st4_sc1((char*)mod + OFF_MOD2 + (128 * c + tid) * 4, __builtin_bit_cast(unsigned, s));
    }
    WAIT_V(0);
    __builtin_amdgcn_s_barrier();
    if (tid == 0) st4_sc1(cnt + (PL1 + c) * 32, 9u);
  }
}
enum { R_QKV = 0, R_PART = 1, R_F1 = 2, R_PART2 = 3 };
struct Dep {
  const char* base; unsigned voff; unsigned mul, add;
};
__device__ __forceinline__ unsigned dep_eval(unsigned v, const Dep& d) {
  v = rowmin16(v * d.mul + d.add);
  return min(min(rdl(v, 0), rdl(v, 16)), min(rdl(v, 32), rdl(v, 48)));
}
typedef unsigned u32x32 __attribute__((ext_vector_type(32)));
typedef unsigned u32x16 __attribute__((ext_vector_type(16)));
__device__ __forceinline__ void fast_ld0(const u32x32 (&W)[4], u32x16& pd, unsigned ea, unsigned da, int& b, int& lf, int cp, unsigned sa, unsigned mu, unsigned ad,
    unsigned rw, unsigned ab, const char* p1, const char* p0, unsigned l1, unsigned l0, const unsigned (&av)[8]) {
  asm volatile("s_setprio 0\n\t"
    "s_waitcnt vmcnt(12)\n\t"
    "s_barrier\n\t"
    "ds_read_b128 v[96:99], %[ea] offset:0\n\t"
    "ds_read_b128 v[100:103], %[ea] offset:2048\n\t"
    "ds_read_b128 v[104:107], %[da] offset:0\n\t"
    "ds_read_b128 v[108:111], %[da] offset:2048\n\t"
    "ds_read_b128 v[112:115], %[ea] offset:4096\n\t"
    "ds_read_b128 v[116:119], %[ea] offset:6144\n\t"
    "s_branch 2f\n\t"
    "1:\n\t"
    "s_waitcnt vmcnt(12)\n\t"
    "s_nop 1\n\t"
    "s_barrier\n\t"
    "2:\n\t"
    "s_waitcnt lgkmcnt(4)\n\t"
    "v_mfma_f32_16x16x32_bf16 v[64:67], v[128:131], v[96:99], 0\n\t"
    "ds_read_b128 v[120:123], %[da] offset:4096\n\t"
    "s_add_u32 m0, %[l1], 0\n\t"
    "ds_write_b128 %[rw], v[80:83] offset:0\n\t"
    "v_mfma_f32_16x16x32_bf16 v[68:71], v[132:135], v[96:99], 0\n\t"
    "ds_read_b128 v[124:127], %[da] offset:6144\n\t"
    "global_load_lds_dwordx4 %[av0], s[36:37] sc1\n\t"
    "ds_write_b128 %[rw], v[84:87] offset:1024\n\t"
    "v_mfma_f32_16x16x32_bf16 v[72:75], v[128:131], v[100:103], 0\n\t"
    "s_add_u32 m0, %[l1], 32768\n\t"
    "ds_write_b128 %[rw], v[88:91] offset:2048\n\t"
    "v_mfma_f32_16x16x32_bf16 v[76:79], v[132:135], v[100:103], 0\n\t"
    "global_load_lds_dwordx4 %[av1], s[36:37] sc1\n\t"
    "ds_write_b128 %[rw], v[92:95] offset:3072\n\t"
    "s_waitcnt lgkmcnt(8)\n\t"
    "v_mfma_f32_16x16x32_bf16 v[64:67], v[136:139], v[104:107], v[64:67]\n\t"
    "ds_read_b128 v[96:99], %[ea] offset:8192\n\t"
    "s_add_u32 m0, %[l1], 4096\n\t"
    "v_mfma_f32_16x16x32_bf16 v[68:71], v[140:143], v[104:107], v[68:71]\n\t"
    "ds_read_b128 v[100:103], %[ea] offset:10240\n\t"
    "global_load_lds_dwordx4 %[av2], s[36:37] sc1\n\t"
    "v_mfma_f32_16x16x32_bf16 v[72:75], v[136:139], v[108:111], v[72:75]\n\t"
    "s_add_u32 m0, %[l1], 36864\n\t"
    "v_mfma_f32_16x16x32_bf16 v[76:79], v[140:143], v[108:111], v[76:79]\n\t"
    "global_load_lds_dwordx4 %[av3], s[36:37] sc1\n\t"
    "s_waitcnt lgkmcnt(8)\n\t"
    "v_mfma_f32_16x16x32_bf16 v[64:67], v[144:147], v[112:115], v[64:67]\n\t"
    "ds_read_b128 v[104:107], %[da] offset:8192\n\t"
    "s_add_u32 m0, %[l1], 8192\n\t"
    "v_mfma_f32_16x16x32_bf16 v[68:71], v[148:151], v[112:115], v[68:71]\n\t"
    "ds_read_b128 v[108:111], %[da] offset:10240\n\t"
    "global_load_lds_dwordx4 %[av4], s[36:37] sc1\n\t"
    "v_mfma_f32_16x16x32_bf16 v[72:75], v[144:147], v[116:119], v[72:75]\n\t"
    "s_add_u32 m0, %[l1], 40960\n\t"
    "v_mfma_f32_16x16x32_bf16 v[76:79], v[148:151], v[116:119], v[76:79]\n\t"
    "global_load_lds_dwordx4 %[av5], s[36:37] sc1\n\t"
    "s_waitcnt lgkmcnt(7)\n\t"
    "v_mfma_f32_16x16x32_bf16 v[64:67], v[152:155], v[120:123], v[64:67]\n\t"
    "ds_read_b128 v[112:115], %[ea] offset:12288\n\t"
    "s_add_u32 m0, %[l1], 12288\n\t"
    "v_mfma_f32_16x16x32_bf16 v[68:71], v[156:159], v[120:123], v[68:71]\n\t"
    "ds_read_b128 v[116:119], %[ea] offset:14336\n\t"
    "global_load_lds_dwordx4 %[av6], s[36:37] sc1\n\t"
    "v_mfma_f32_16x16x32_bf16 v[72:75], v[152:155], v[124:127], v[72:75]\n\t"
    "s_add_u32 m0, %[l1], 45056\n\t"
    "v_mfma_f32_16x16x32_bf16 v[76:79], v[156:159], v[124:127], v[76:79]\n\t"
    "global_load_lds_dwordx4 %[av7], s[36:37] sc1\n\t"
    "s_waitcnt lgkmcnt(4)\n\t"
    "v_mfma_f32_16x16x32_bf16 v[64:67], v[160:163], v[96:99], v[64:67]\n\t"
    "ds_read_b128 v[120:123], %[da] offset:12288\n\t"
    "v_mfma_f32_16x16x32_bf16 v[68:71], v[164:167], v[96:99], v[68:71]\n\t"
    "ds_read_b128 v[124:127], %[da] offset:14336\n\t"
    "v_mfma_f32_16x16x32_bf16 v[72:75], v[160:163], v[100:103], v[72:75]\n\t"
    "v_mfma_f32_16x16x32_bf16 v[76:79], v[164:167], v[100:103], v[76:79]\n\t"
    "s_waitcnt lgkmcnt(4)\n\t"
    "v_mfma_f32_16x16x32_bf16 v[64:67], v[168:171], v[104:107], v[64:67]\n\t"
    "ds_read_b128 v[96:99], %[ea] offset:16384\n\t"
    "v_mfma_f32_16x16x32_bf16 v[68:71], v[172:175], v[104:107], v[68:71]\n\t"
    "ds_read_b128 v[100:103], %[ea] offset:18432\n\t"
    "v_mfma_f32_16x16x32_bf16 v[72:75], v[168:171], v[108:111], v[72:75]\n\t"
    "v_mfma_f32_16x16x32_bf16 v[76:79], v[172:175], v[108:111], v[76:79]\n\t"
    "s_waitcnt lgkmcnt(4)\n\t"
    "v_mfma_f32_16x16x32_bf16 v[64:67], v[176:179], v[112:115], v[64:67]\n\t"
    "ds_read_b128 v[104:107], %[da] offset:16384\n\t"
    "v_mfma_f32_16x16x32_bf16 v[68:71], v[180:183], v[112:115], v[68:71]\n\t"
    "ds_read_b128 v[108:111], %[da] offset:18432\n\t"
    "v_mfma_f32_16x16x32_bf16 v[72:75], v[176:179], v[116:119], v[72:75]\n\t"
    "v_mfma_f32_16x16x32_bf16 v[76:79], v[180:183], v[116:119], v[76:79]\n\t"
    "s_waitcnt lgkmcnt(4)\n\t"
    "v_mfma_f32_16x16x32_bf16 v[64:67], v[184:187], v[120:123], v[64:67]\n\t"
    "ds_read_b128 v[112:115], %[ea] offset:20480\n\t"
    "v_mfma_f32_16x16x32_bf16 v[68:71], v[188:191], v[120:123], v[68:71]\n\t"
    "ds_read_b128 v[116:119], %[ea] offset:22528\n\t"
    "v_mfma_f32_16x16x32_bf16 v[72:75], v[184:187], v[124:127], v[72:75]\n\t"
    "v_mfma_f32_16x16x32_bf16 v[76:79], v[188:191], v[124:127], v[76:79]\n\t"
    "s_waitcnt vmcnt(12)\n\t"
    "s_barrier\n\t"
    "s_waitcnt lgkmcnt(4)\n\t"
    "v_mfma_f32_16x16x32_bf16 v[64:67], v[192:195], v[96:99], v[64:67]\n\t"
    "ds_read_b128 v[120:123], %[da] offset:20480\n\t"
    "s_add_u32 m0, %[l0], 0\n\t"
    "v_mfma_f32_16x16x32_bf16 v[68:71], v[196:199], v[96:99], v[68:71]\n\t"
    "ds_read_b128 v[124:127], %[da] offset:22528\n\t"
    "global_load_lds_dwordx4 %[av0], s[38:39] sc1\n\t"
    "v_mfma_f32_16x16x32_bf16 v[72:75], v[192:195], v[100:103], v[72:75]\n\t"
    "s_add_u32 m0, %[l0], 32768\n\t"
    "v_mfma_f32_16x16x32_bf16 v[76:79], v[196:199], v[100:103], v[76:79]\n\t"
    "global_load_lds_dwordx4 %[av1], s[38:39] sc1\n\t"
    "s_waitcnt lgkmcnt(4)\n\t"
    "v_mfma_f32_16x16x32_bf16 v[64:67], v[200:203], v[104:107], v[64:67]\n\t"
    "ds_read_b128 v[96:99], %[ea] offset:24576\n\t"
    "s_add_u32 m0, %[l0], 4096\n\t"
    "v_mfma_f32_16x16x32_bf16 v[68:71], v[204:207], v[104:107], v[68:71]\n\t"
    "ds_read_b128 v[100:103], %[ea] offset:26624\n\t"
    "global_load_lds_dwordx4 %[av2], s[38:39] sc1\n\t"
    "v_mfma_f32_16x16x32_bf16 v[72:75], v[200:203], v[108:111], v[72:75]\n\t"
    "s_add_u32 m0, %[l0], 36864\n\t"
    "v_mfma_f32_16x16x32_bf16 v[76:79], v[204:207], v[108:111], v[76:79]\n\t"
    "global_load_lds_dwordx4 %[av3], s[38:39] sc1\n\t"
    "s_add_u32 s36, s36, %[ab]\n\t"
    "s_addc_u32 s37, s37, 0\n\t"
    "s_waitcnt lgkmcnt(4)\n\t"
    "v_mfma_f32_16x16x32_bf16 v[64:67], v[208:211], v[112:115], v[64:67]\n\t"
    "ds_read_b128 v[104:107], %[da] offset:24576\n\t"
    "s_add_u32 m0, %[l0], 8192\n\t"
    "v_mfma_f32_16x16x32_bf16 v[68:71], v[212:215], v[112:115], v[68:71]\n\t"
    "ds_read_b128 v[108:111], %[da] offset:26624\n\t"
    "global_load_lds_dwordx4 %[av4], s[38:39] sc1\n\t"
    "v_mfma_f32_16x16x32_bf16 v[72:75], v[208:211], v[116:119], v[72:75]\n\t"
    "ds_read_b32 v39, %[sa]\n\t"
    "s_add_u32 m0, %[l0], 40960\n\t"
    "v_mfma_f32_16x16x32_bf16 v[76:79], v[212:215], v[116:119], v[76:79]\n\t"
    "v_xor_b32 %[sa], 0x100, %[sa]\n\t"
    "global_load_lds_dwordx4 %[av5], s[38:39] sc1\n\t"
    "s_xor_b32 %[l1], %[l1], 0x10000\n\t"
    "s_waitcnt lgkmcnt(5)\n\t"
    "v_mfma_f32_16x16x32_bf16 v[64:67], v[216:219], v[120:123], v[64:67]\n\t"
    "ds_read_b128 v[112:115], %[ea] offset:28672\n\t"
    "s_add_u32 m0, %[l0], 12288\n\t"
    "v_mfma_f32_16x16x32_bf16 v[68:71], v[220:223], v[120:123], v[68:71]\n\t"
    "ds_read_b128 v[116:119], %[ea] offset:30720\n\t"
    "global_load_lds_dwordx4 %[av6], s[38:39] sc1\n\t"
    "v_mfma_f32_16x16x32_bf16 v[72:75], v[216:219], v[124:127], v[72:75]\n\t"
    "v_xor_b32 %[ea], 0x10000, %[ea]\n\t"
    "s_add_u32 m0, %[l0], 45056\n\t"
    "v_mfma_f32_16x16x32_bf16 v[76:79], v[220:223], v[124:127], v[76:79]\n\t"
    "global_load_lds_dwordx4 %[av7], s[38:39] sc1\n\t"
    "s_waitcnt lgkmcnt(5)\n\t"
    "v_mfma_f32_16x16x32_bf16 v[64:67], v[224:227], v[96:99], v[64:67]\n\t"
    "ds_read_b128 v[120:123], %[da] offset:28672\n\t"
    "v_mfma_f32_16x16x32_bf16 v[68:71], v[228:231], v[96:99], v[68:71]\n\t"
    "ds_read_b128 v[124:127], %[da] offset:30720\n\t"
    "v_mfma_f32_16x16x32_bf16 v[72:75], v[224:227], v[100:103], v[72:75]\n\t"
    "v_xor_b32 %[da], 0x10000, %[da]\n\t"
    "v_mfma_f32_16x16x32_bf16 v[76:79], v[228:231], v[100:103], v[76:79]\n\t"
    "s_waitcnt lgkmcnt(5)\n\t"
    "v_mfma_f32_16x16x32_bf16 v[64:67], v[232:235], v[104:107], v[64:67]\n\t"
    "ds_read_b128 v[96:99], %[ea] offset:0\n\t"
    "v_mfma_f32_16x16x32_bf16 v[68:71], v[236:239], v[104:107], v[68:71]\n\t"
    "ds_read_b128 v[100:103], %[ea] offset:2048\n\t"
    "v_mfma_f32_16x16x32_bf16 v[72:75], v[232:235], v[108:111], v[72:75]\n\t"
    "v_mfma_f32_16x16x32_bf16 v[76:79], v[236:239], v[108:111], v[76:79]\n\t"
    "s_waitcnt lgkmcnt(4)\n\t"
    "v_mfma_f32_16x16x32_bf16 v[64:67], v[240:243], v[112:115], v[64:67]\n\t"
    "ds_read_b128 v[104:107], %[da] offset:0\n\t"
    "v_mad_u32_u24 v39, v39, %[mu], %[ad]\n\t"
    "v_mfma_f32_16x16x32_bf16 v[68:71], v[244:247], v[112:115], v[68:71]\n\t"
    "ds_read_b128 v[108:111], %[da] offset:2048\n\t"
    "v_cmp_ge_i32 vcc, %[lf], v39\n\t"
    "s_cmp_eq_u64 vcc, 0\n\t"
    "s_addc_u32 %[lf], %[lf], 0\n\t"
    "v_mfma_f32_16x16x32_bf16 v[72:75], v[240:243], v[116:119], v[72:75]\n\t"
    "v_mfma_f32_16x16x32_bf16 v[76:79], v[244:247], v[116:119], v[76:79]\n\t"
    "v_cmp_ge_i32 vcc, %[lf], v39\n\t"
    "s_cmp_eq_u64 vcc, 0\n\t"
    "s_addc_u32 %[lf], %[lf], 0\n\t"
    "s_waitcnt lgkmcnt(4)\n\t"
    "v_mfma_f32_16x16x32_bf16 v[80:83], v[248:251], v[120:123], v[64:67]\n\t"
    "ds_read_b128 v[112:115], %[ea] offset:4096\n\t"
    "s_min_i32 %[lf], %[lf], %[cp]\n\t"
    "s_add_u32 %[b], %[b], 1\n\t"
    "v_mfma_f32_16x16x32_bf16 v[84:87], v[252:255], v[120:123], v[68:71]\n\t"
    "ds_read_b128 v[116:119], %[ea] offset:6144\n\t"
    "v_mfma_f32_16x16x32_bf16 v[88:91], v[248:251], v[124:127], v[72:75]\n\t"
    "v_mfma_f32_16x16x32_bf16 v[92:95], v[252:255], v[124:127], v[76:79]\n\t"
    "s_add_u32 s38, s38, %[ab]\n\t"
    "s_addc_u32 s39, s39, 0\n\t"
    "s_xor_b32 %[l0], %[l0], 0x10000\n\t"
    "s_cmp_lt_i32 %[b], %[lf]\n\t"
    "s_cbranch_scc1 1b\n\t"
    "s_waitcnt lgkmcnt(0)\n\t"
    "s_nop 4"
    : "=&{v[80:95]}"(pd), [ea] "+&v"(ea), [da] "+&v"(da), [b] "+&s"(b), [lf] "+&s"(lf), "+{s[36:37]}"(p1), "+{s[38:39]}"(p0), [sa] "+&v"(sa), [l1] "+&s"(l1), [l0] "+&s"(l0)
    : [rw] "v"(rw), [ab] "s"(ab), [cp] "s"(cp), [mu] "s"(mu), [ad] "v"(ad), "{v[128:159]}"(W[0]), "{v[160:191]}"(W[1]), "{v[192:223]}"(W[2]), "{v[224:255]}"(W[3]), [av0] "v"(av[0]), [av1] "v"(av[1]), [av2] "v"(av[2]), [av3] "v"(av[3]), [av4] "v"(av[4]), [av5] "v"(av[5]), [av6] "v"(av[6]), [av7] "v"(av[7])
    : "v39", "v64", "v65", "v66", "v67", "v68", "v69", "v70", "v71", "v72", "v73", "v74", "v75", "v76", "v77", "v78", "v79", "v96", "v97", "v98", "v99", "v100", "v101", "v102", "v103", "v104", "v105", "v106", "v107", "v108", "v109", "v110", "v111", "v112", "v113", "v114", "v115", "v116", "v117", "v118", "v119", "v120", "v121", "v122", "v123", "v124", "v125", "v126", "v127", "vcc", "scc", "m0", "memory");
}
__device__ __forceinline__ void fast_ld2(const u32x32 (&W)[4], u32x16& pd, unsigned ea, unsigned da, int& b, int& lf, int cp, unsigned sa, unsigned mu, unsigned ad,
    unsigned rw, unsigned ab, const char* p1, const char* p0, unsigned l1, unsigned l0, const unsigned (&av)[8]) {
  asm volatile("s_setprio 0\n\t"
    "s_waitcnt vmcnt(6)\n\t"
    "s_barrier\n\t"
    "ds_read_b128 v[96:99], %[ea] offset:0\n\t"
    "ds_read_b128 v[100:103], %[ea] offset:2048\n\t"
    "ds_read_b128 v[104:107], %[da] offset:0\n\t"
    "ds_read_b128 v[108:111], %[da] offset:2048\n\t"
    "ds_read_b128 v[112:115], %[ea] offset:4096\n\t"
    "ds_read_b128 v[116:119], %[ea] offset:6144\n\t"
    "s_branch 2f\n\t"
    "1:\n\t"
    "s_waitcnt vmcnt(6)\n\t"
    "s_nop 1\n\t"
    "s_barrier\n\t"
    "2:\n\t"
    "s_waitcnt lgkmcnt(4)\n\t"
    "v_mfma_f32_16x16x32_bf16 v[64:67], v[128:131], v[96:99], 0\n\t"
    "ds_read_b128 v[120:123], %[da] offset:4096\n\t"
    "s_add_u32 m0, %[l1], 0\n\t"
    "ds_write_b128 %[rw], v[80:83] offset:0\n\t"
    "v_mfma_f32_16x16x32_bf16 v[68:71], v[132:135], v[96:99], 0\n\t"
    "ds_read_b128 v[124:127], %[da] offset:6144\n\t"
    "global_load_lds_dwordx4 %[av0], s[36:37] sc1\n\t"
    "ds_write_b128 %[rw], v[84:87] offset:1024\n\t"
    "v_mfma_f32_16x16x32_bf16 v[72:75], v[128:131], v[100:103], 0\n\t"
    "ds_write_b128 %[rw], v[88:91] offset:2048\n\t"
    "v_mfma_f32_16x16x32_bf16 v[76:79], v[132:135], v[100:103], 0\n\t"
    "ds_write_b128 %[rw], v[92:95] offset:3072\n\t"
    "s_waitcnt lgkmcnt(8)\n\t"
    "v_mfma_f32_16x16x32_bf16 v[64:67], v[136:139], v[104:107], v[64:67]\n\t"
    "ds_read_b128 v[96:99], %[ea] offset:8192\n\t"
    "s_add_u32 m0, %[l1], 4096\n\t"
    "v_mfma_f32_16x16x32_bf16 v[68:71], v[140:143], v[104:107], v[68:71]\n\t"
    "ds_read_b128 v[100:103], %[ea] offset:10240\n\t"
    "global_load_lds_dwordx4 %[av2], s[36:37] sc1\n\t"
    "v_mfma_f32_16x16x32_bf16 v[72:75], v[136:139], v[108:111], v[72:75]\n\t"
    "v_mfma_f32_16x16x32_bf16 v[76:79], v[140:143], v[108:111], v[76:79]\n\t"
    "s_waitcnt lgkmcnt(8)\n\t"
    "v_mfma_f32_16x16x32_bf16 v[64:67], v[144:147], v[112:115], v[64:67]\n\t"
    "ds_read_b128 v[104:107], %[da] offset:8192\n\t"
    "s_add_u32 m0, %[l1], 8192\n\t"
    "v_mfma_f32_16x16x32_bf16 v[68:71], v[148:151], v[112:115], v[68:71]\n\t"
    "ds_read_b128 v[108:111], %[da] offset:10240\n\t"
    "global_load_lds_dwordx4 %[av4], s[36:37] sc1\n\t"
    "v_mfma_f32_16x16x32_bf16 v[72:75], v[144:147], v[116:119], v[72:75]\n\t"
    "v_mfma_f32_16x16x32_bf16 v[76:79], v[148:151], v[116:119], v[76:79]\n\t"
    "s_waitcnt lgkmcnt(7)\n\t"
    "v_mfma_f32_16x16x32_bf16 v[64:67], v[152:155], v[120:123], v[64:67]\n\t"
    "ds_read_b128 v[112:115], %[ea] offset:12288\n\t"
    "s_add_u32 m0, %[l1], 12288\n\t"
    "v_mfma_f32_16x16x32_bf16 v[68:71], v[156:159], v[120:123], v[68:71]\n\t"
    "ds_read_b128 v[116:119], %[ea] offset:14336\n\t"
    "global_load_lds_dwordx4 %[av6], s[36:37] sc1\n\t"
    "v_mfma_f32_16x16x32_bf16 v[72:75], v[152:155], v[124:127], v[72:75]\n\t"
    "v_mfma_f32_16x16x32_bf16 v[76:79], v[156:159], v[124:127], v[76:79]\n\t"
    "s_waitcnt lgkmcnt(4)\n\t"
    "v_mfma_f32_16x16x32_bf16 v[64:67], v[160:163], v[96:99], v[64:67]\n\t"
    "ds_read_b128 v[120:123], %[da] offset:12288\n\t"
    "v_mfma_f32_16x16x32_bf16 v[68:71], v[164:167], v[96:99], v[68:71]\n\t"
    "ds_read_b128 v[124:127], %[da] offset:14336\n\t"
    "v_mfma_f32_16x16x32_bf16 v[72:75], v[160:163], v[100:103], v[72:75]\n\t"
    "v_mfma_f32_16x16x32_bf16 v[76:79], v[164:167], v[100:103], v[76:79]\n\t"
    "s_waitcnt lgkmcnt(4)\n\t"
    "v_mfma_f32_16x16x32_bf16 v[64:67], v[168:171], v[104:107], v[64:67]\n\t"
    "ds_read_b128 v[96:99], %[ea] offset:16384\n\t"
    "v_mfma_f32_16x16x32_bf16 v[68:71], v[172:175], v[104:107], v[68:71]\n\t"
    "ds_read_b128 v[100:103], %[ea] offset:18432\n\t"
    "v_mfma_f32_16x16x32_bf16 v[72:75], v[168:171], v[108:111], v[72:75]\n\t"
    "v_mfma_f32_16x16x32_bf16 v[76:79], v[172:175], v[108:111], v[76:79]\n\t"
    "s_waitcnt lgkmcnt(4)\n\t"
    "v_mfma_f32_16x16x32_bf16 v[64:67], v[176:179], v[112:115], v[64:67]\n\t"
    "ds_read_b128 v[104:107], %[da] offset:16384\n\t"
    "v_mfma_f32_16x16x32_bf16 v[68:71], v[180:183], v[112:115], v[68:71]\n\t"
    "ds_read_b128 v[108:111], %[da] offset:18432\n\t"
    "v_mfma_f32_16x16x32_bf16 v[72:75], v[176:179], v[116:119], v[72:75]\n\t"
    "v_mfma_f32_16x16x32_bf16 v[76:79], v[180:183], v[116:119], v[76:79]\n\t"
    "s_waitcnt lgkmcnt(4)\n\t"
    "v_mfma_f32_16x16x32_bf16 v[64:67], v[184:187], v[120:123], v[64:67]\n\t"
    "ds_read_b128 v[112:115], %[ea] offset:20480\n\t"
    "v_mfma_f32_16x16x32_bf16 v[68:71], v[188:191], v[120:123], v[68:71]\n\t"
    "ds_read_b128 v[116:119], %[ea] offset:22528\n\t"
    "v_mfma_f32_16x16x32_bf16 v[72:75], v[184:187], v[124:127], v[72:75]\n\t"
    "v_mfma_f32_16x16x32_bf16 v[76:79], v[188:191], v[124:127], v[76:79]\n\t"
    "s_waitcnt vmcnt(6)\n\t"
    "s_barrier\n\t"
    "s_waitcnt lgkmcnt(4)\n\t"
    "v_mfma_f32_16x16x32_bf16 v[64:67], v[192:195], v[96:99], v[64:67]\n\t"
    "ds_read_b128 v[120:123], %[da] offset:20480\n\t"
    "s_add_u32 m0, %[l0], 0\n\t"
    "v_mfma_f32_16x16x32_bf16 v[68:71], v[196:199], v[96:99], v[68:71]\n\t"
    "ds_read_b128 v[124:127], %[da] offset:22528\n\t"
    "global_load_lds_dwordx4 %[av0], s[38:39] sc1\n\t"
    "v_mfma_f32_16x16x32_bf16 v[72:75], v[192:195], v[100:103], v[72:75]\n\t"
    "v_mfma_f32_16x16x32_bf16 v[76:79], v[196:199], v[100:103], v[76:79]\n\t"
    "s_waitcnt lgkmcnt(4)\n\t"
    "v_mfma_f32_16x16x32_bf16 v[64:67], v[200:203], v[104:107], v[64:67]\n\t"
    "ds_read_b128 v[96:99], %[ea] offset:24576\n\t"
    "s_add_u32 m0, %[l0], 4096\n\t"
    "v_mfma_f32_16x16x32_bf16 v[68:71], v[204:207], v[104:107], v[68:71]\n\t"
    "ds_read_b128 v[100:103], %[ea] offset:26624\n\t"
    "global_load_lds_dwordx4 %[av2], s[38:39] sc1\n\t"
    "v_mfma_f32_16x16x32_bf16 v[72:75], v[200:203], v[108:111], v[72:75]\n\t"
    "v_mfma_f32_16x16x32_bf16 v[76:79], v[204:207], v[108:111], v[76:79]\n\t"
    "s_add_u32 s36, s36, %[ab]\n\t"
    "s_addc_u32 s37, s37, 0\n\t"
    "s_waitcnt lgkmcnt(4)\n\t"
    "v_mfma_f32_16x16x32_bf16 v[64:67], v[208:211], v[112:115], v[64:67]\n\t"
    "ds_read_b128 v[104:107], %[da] offset:24576\n\t"
    "s_add_u32 m0, %[l0], 8192\n\t"
    "v_mfma_f32_16x16x32_bf16 v[68:71], v[212:215], v[112:115], v[68:71]\n\t"
    "ds_read_b128 v[108:111], %[da] offset:26624\n\t"
    "global_load_lds_dwordx4 %[av4], s[38:39] sc1\n\t"
    "v_mfma_f32_16x16x32_bf16 v[72:75], v[208:211], v[116:119], v[72:75]\n\t"
    "ds_read_b32 v39, %[sa]\n\t"
    "v_mfma_f32_16x16x32_bf16 v[76:79], v[212:215], v[116:119], v[76:79]\n\t"
    "v_xor_b32 %[sa], 0x100, %[sa]\n\t"
    "s_xor_b32 %[l1], %[l1], 0x10000\n\t"
    "s_waitcnt lgkmcnt(5)\n\t"
    "v_mfma_f32_16x16x32_bf16 v[64:67], v[216:219], v[120:123], v[64:67]\n\t"
    "ds_read_b128 v[112:115], %[ea] offset:28672\n\t"
    "s_add_u32 m0, %[l0], 12288\n\t"
    "v_mfma_f32_16x16x32_bf16 v[68:71], v[220:223], v[120:123], v[68:71]\n\t"
    "ds_read_b128 v[116:119], %[ea] offset:30720\n\t"
    "global_load_lds_dwordx4 %[av6], s[38:39] sc1\n\t"
    "v_mfma_f32_16x16x32_bf16 v[72:75], v[216:219], v[124:127], v[72:75]\n\t"
    "v_xor_b32 %[ea], 0x10000, %[ea]\n\t"
    "v_mfma_f32_16x16x32_bf16 v[76:79], v[220:223], v[124:127], v[76:79]\n\t"
    "s_waitcnt lgkmcnt(5)\n\t"
    "v_mfma_f32_16x16x32_bf16 v[64:67], v[224:227], v[96:99], v[64:67]\n\t"
    "ds_read_b128 v[120:123], %[da] offset:28672\n\t"
    "v_mfma_f32_16x16x32_bf16 v[68:71], v[228:231], v[96:99], v[68:71]\n\t"
    "ds_read_b128 v[124:127], %[da] offset:30720\n\t"
    "v_mfma_f32_16x16x32_bf16 v[72:75], v[224:227], v[100:103], v[72:75]\n\t"
    "v_xor_b32 %[da], 0x10000, %[da]\n\t"
    "v_mfma_f32_16x16x32_bf16 v[76:79], v[228:231], v[100:103], v[76:79]\n\t"
    "s_waitcnt lgkmcnt(5)\n\t"
    "v_mfma_f32_16x16x32_bf16 v[64:67], v[232:235], v[104:107], v[64:67]\n\t"
    "ds_read_b128 v[96:99], %[ea] offset:0\n\t"
    "v_mfma_f32_16x16x32_bf16 v[68:71], v[236:239], v[104:107], v[68:71]\n\t"
    "ds_read_b128 v[100:103], %[ea] offset:2048\n\t"
    "v_mfma_f32_16x16x32_bf16 v[72:75], v[232:235], v[108:111], v[72:75]\n\t"
    "v_mfma_f32_16x16x32_bf16 v[76:79], v[236:239], v[108:111], v[76:79]\n\t"
    "s_waitcnt lgkmcnt(4)\n\t"
    "v_mfma_f32_16x16x32_bf16 v[64:67], v[240:243], v[112:115], v[64:67]\n\t"
    "ds_read_b128 v[104:107], %[da] offset:0\n\t"
    "v_mad_u32_u24 v39, v39, %[mu], %[ad]\n\t"
    "v_mfma_f32_16x16x32_bf16 v[68:71], v[244:247], v[112:115], v[68:71]\n\t"
    "ds_read_b128 v[108:111], %[da] offset:2048\n\t"
    "v_cmp_ge_i32 vcc, %[lf], v39\n\t"
    "s_cmp_eq_u64 vcc, 0\n\t"
    "s_addc_u32 %[lf], %[lf], 0\n\t"
    "v_mfma_f32_16x16x32_bf16 v[72:75], v[240:243], v[116:119], v[72:75]\n\t"
    "v_mfma_f32_16x16x32_bf16 v[76:79], v[244:247], v[116:119], v[76:79]\n\t"
    "v_cmp_ge_i32 vcc, %[lf], v39\n\t"
    "s_cmp_eq_u64 vcc, 0\n\t"
    "s_addc_u32 %[lf], %[lf], 0\n\t"
    "s_waitcnt lgkmcnt(4)\n\t"
    "v_mfma_f32_16x16x32_bf16 v[80:83], v[248:251], v[120:123], v[64:67]\n\t"
    "ds_read_b128 v[112:115], %[ea] offset:4096\n\t"
    "s_min_i32 %[lf], %[lf], %[cp]\n\t"
    "s_add_u32 %[b], %[b], 1\n\t"
    "v_mfma_f32_16x16x32_bf16 v[84:87], v[252:255], v[120:123], v[68:71]\n\t"
    "ds_read_b128 v[116:119], %[ea] offset:6144\n\t"
    "v_mfma_f32_16x16x32_bf16 v[88:91], v[248:251], v[124:127], v[72:75]\n\t"
    "v_mfma_f32_16x16x32_bf16 v[92:95], v[252:255], v[124:127], v[76:79]\n\t"
    "s_add_u32 s38, s38, %[ab]\n\t"
    "s_addc_u32 s39, s39, 0\n\t"
    "s_xor_b32 %[l0], %[l0], 0x10000\n\t"
    "s_cmp_lt_i32 %[b], %[lf]\n\t"
    "s_cbranch_scc1 1b\n\t"
    "s_waitcnt lgkmcnt(0)\n\t"
    "s_nop 4"
    : "=&{v[80:95]}"(pd), [ea] "+&v"(ea), [da] "+&v"(da), [b] "+&s"(b), [lf] "+&s"(lf), "+{s[36:37]}"(p1), "+{s[38:39]}"(p0), [sa] "+&v"(sa), [l1] "+&s"(l1), [l0] "+&s"(l0)
    : [rw] "v"(rw), [ab] "s"(ab), [cp] "s"(cp), [mu] "s"(mu), [ad] "v"(ad), "{v[128:159]}"(W[0]), "{v[160:191]}"(W[1]), "{v[192:223]}"(W[2]), "{v[224:255]}"(W[3]), [av0] "v"(av[0]), [av1] "v"(av[1]), [av2] "v"(av[2]), [av3] "v"(av[3]), [av4] "v"(av[4]), [av5] "v"(av[5]), [av6] "v"(av[6]), [av7] "v"(av[7])
    : "v39", "v64", "v65", "v66", "v67", "v68", "v69", "v70", "v71", "v72", "v73", "v74", "v75", "v76", "v77", "v78", "v79", "v96", "v97", "v98", "v99", "v100", "v101", "v102", "v103", "v104", "v105", "v106", "v107", "v108", "v109", "v110", "v111", "v112", "v113", "v114", "v115", "v116", "v117", "v118", "v119", "v120", "v121", "v122", "v123", "v124", "v125", "v126", "v127", "vcc", "scc", "m0", "memory");
}
__device__ __forceinline__ void fast_st0(const u32x32 (&W)[4], u32x16& pd, unsigned ea, unsigned da, int& b, int& lf, int cp, unsigned sa, unsigned mu, unsigned ad,
    unsigned rw, unsigned ab, const char* p1, const char* p0, unsigned co0, unsigned co1, int po, int pf, unsigned w0, unsigned sl, unsigned dv, const char* db, const char* pg) {
  unsigned st;
  asm volatile("s_barrier\n\t"
    "ds_read_b128 v[96:99], %[ea] offset:0\n\t"
    "ds_read_b128 v[100:103], %[ea] offset:2048\n\t"
    "ds_read_b128 v[104:107], %[da] offset:0\n\t"
    "ds_read_b128 v[108:111], %[da] offset:2048\n\t"
    "ds_read_b128 v[112:115], %[ea] offset:4096\n\t"
    "ds_read_b128 v[116:119], %[ea] offset:6144\n\t"
    "s_branch 2f\n\t"
    "1:\n\t"
    "s_barrier\n\t"
    "2:\n\t"
    "s_waitcnt lgkmcnt(4)\n\t"
    "v_mfma_f32_16x16x32_bf16 v[64:67], v[128:131], v[96:99], 0\n\t"
    "ds_read_b128 v[120:123], %[da] offset:4096\n\t"
    "v_mfma_f32_16x16x32_bf16 v[68:71], v[132:135], v[96:99], 0\n\t"
    "ds_read_b128 v[124:127], %[da] offset:6144\n\t"
    "v_mfma_f32_16x16x32_bf16 v[72:75], v[128:131], v[100:103], 0\n\t"
    "v_mfma_f32_16x16x32_bf16 v[76:79], v[132:135], v[100:103], 0\n\t"
    "s_waitcnt lgkmcnt(4)\n\t"
    "v_mfma_f32_16x16x32_bf16 v[64:67], v[136:139], v[104:107], v[64:67]\n\t"
    "ds_read_b128 v[96:99], %[ea] offset:8192\n\t"
    "v_mfma_f32_16x16x32_bf16 v[68:71], v[140:143], v[104:107], v[68:71]\n\t"
    "ds_read_b128 v[100:103], %[ea] offset:10240\n\t"
    "v_mfma_f32_16x16x32_bf16 v[72:75], v[136:139], v[108:111], v[72:75]\n\t"
    "v_mfma_f32_16x16x32_bf16 v[76:79], v[140:143], v[108:111], v[76:79]\n\t"
    "s_waitcnt lgkmcnt(4)\n\t"
    "v_mfma_f32_16x16x32_bf16 v[64:67], v[144:147], v[112:115], v[64:67]\n\t"
    "ds_read_b128 v[104:107], %[da] offset:8192\n\t"
    "v_mfma_f32_16x16x32_bf16 v[68:71], v[148:151], v[112:115], v[68:71]\n\t"
    "ds_read_b128 v[108:111], %[da] offset:10240\n\t"
    "v_mfma_f32_16x16x32_bf16 v[72:75], v[144:147], v[116:119], v[72:75]\n\t"
    "v_mfma_f32_16x16x32_bf16 v[76:79], v[148:151], v[116:119], v[76:79]\n\t"
    "s_waitcnt lgkmcnt(4)\n\t"
    "v_mfma_f32_16x16x32_bf16 v[64:67], v[152:155], v[120:123], v[64:67]\n\t"
    "ds_read_b128 v[112:115], %[ea] offset:12288\n\t"
    "v_mfma_f32_16x16x32_bf16 v[68:71], v[156:159], v[120:123], v[68:71]\n\t"
    "ds_read_b128 v[116:119], %[ea] offset:14336\n\t"
    "v_mfma_f32_16x16x32_bf16 v[72:75], v[152:155], v[124:127], v[72:75]\n\t"
    "v_mfma_f32_16x16x32_bf16 v[76:79], v[156:159], v[124:127], v[76:79]\n\t"
    "s_waitcnt lgkmcnt(4)\n\t"
    "v_mfma_f32_16x16x32_bf16 v[64:67], v[160:163], v[96:99], v[64:67]\n\t"
    "ds_read_b128 v[120:123], %[da] offset:12288\n\t"
    "v_mfma_f32_16x16x32_bf16 v[68:71], v[164:167], v[96:99], v[68:71]\n\t"
    "ds_read_b128 v[124:127], %[da] offset:14336\n\t"
    "v_mfma_f32_16x16x32_bf16 v[72:75], v[160:163], v[100:103], v[72:75]\n\t"
    "v_mfma_f32_16x16x32_bf16 v[76:79], v[164:167], v[100:103], v[76:79]\n\t"
    "s_waitcnt lgkmcnt(4)\n\t"
    "v_mfma_f32_16x16x32_bf16 v[64:67], v[168:171], v[104:107], v[64:67]\n\t"
    "ds_read_b128 v[96:99], %[ea] offset:16384\n\t"
    "v_mfma_f32_16x16x32_bf16 v[68:71], v[172:175], v[104:107], v[68:71]\n\t"
    "ds_read_b128 v[100:103], %[ea] offset:18432\n\t"
    "v_mfma_f32_16x16x32_bf16 v[72:75], v[168:171], v[108:111], v[72:75]\n\t"
    "v_mfma_f32_16x16x32_bf16 v[76:79], v[172:175], v[108:111], v[76:79]\n\t"
    "s_waitcnt lgkmcnt(4)\n\t"
    "v_mfma_f32_16x16x32_bf16 v[64:67], v[176:179], v[112:115], v[64:67]\n\t"
    "ds_read_b128 v[104:107], %[da] offset:16384\n\t"
    "v_mfma_f32_16x16x32_bf16 v[68:71], v[180:183], v[112:115], v[68:71]\n\t"
    "ds_read_b128 v[108:111], %[da] offset:18432\n\t"
    "v_mfma_f32_16x16x32_bf16 v[72:75], v[176:179], v[116:119], v[72:75]\n\t"
    "v_mfma_f32_16x16x32_bf16 v[76:79], v[180:183], v[116:119], v[76:79]\n\t"
    "s_waitcnt lgkmcnt(4)\n\t"
    "v_mfma_f32_16x16x32_bf16 v[64:67], v[184:187], v[120:123], v[64:67]\n\t"
    "ds_read_b128 v[112:115], %[ea] offset:20480\n\t"
    "v_mfma_f32_16x16x32_bf16 v[68:71], v[188:191], v[120:123], v[68:71]\n\t"
    "ds_read_b128 v[116:119], %[ea] offset:22528\n\t"
    "v_mfma_f32_16x16x32_bf16 v[72:75], v[184:187], v[124:127], v[72:75]\n\t"
    "v_mfma_f32_16x16x32_bf16 v[76:79], v[188:191], v[124:127], v[76:79]\n\t"
    "s_waitcnt vmcnt(0)\n\t"
    "s_barrier\n\t"
    "s_waitcnt lgkmcnt(4)\n\t"
    "v_mfma_f32_16x16x32_bf16 v[64:67], v[192:195], v[96:99], v[64:67]\n\t"
    "ds_read_b128 v[120:123], %[da] offset:20480\n\t"
    "v_mfma_f32_16x16x32_bf16 v[68:71], v[196:199], v[96:99], v[68:71]\n\t"
    "ds_read_b128 v[124:127], %[da] offset:22528\n\t"
    "v_mfma_f32_16x16x32_bf16 v[72:75], v[192:195], v[100:103], v[72:75]\n\t"
    "ds_read_b128 v[48:51], %[rw] offset:0\n\t"
    "v_mfma_f32_16x16x32_bf16 v[76:79], v[196:199], v[100:103], v[76:79]\n\t"
    "ds_read_b128 v[52:55], %[rw] offset:1024\n\t"
    "s_cmp_eq_u32 %[w0], 0\n\t"
    "s_cbranch_scc1 5f\n\t"
    "6:\n\t"
    "s_waitcnt lgkmcnt(6)\n\t"
    "v_mfma_f32_16x16x32_bf16 v[64:67], v[200:203], v[104:107], v[64:67]\n\t"
    "ds_read_b128 v[96:99], %[ea] offset:24576\n\t"
    "v_mfma_f32_16x16x32_bf16 v[68:71], v[204:207], v[104:107], v[68:71]\n\t"
    "ds_read_b128 v[100:103], %[ea] offset:26624\n\t"
    "v_mfma_f32_16x16x32_bf16 v[72:75], v[200:203], v[108:111], v[72:75]\n\t"
    "ds_read_b128 v[56:59], %[rw] offset:2048\n\t"
    "v_mfma_f32_16x16x32_bf16 v[76:79], v[204:207], v[108:111], v[76:79]\n\t"
    "ds_read_b128 v[60:63], %[rw] offset:3072\n\t"
    "s_waitcnt lgkmcnt(0)\n\t"
    "v_mfma_f32_16x16x32_bf16 v[64:67], v[208:211], v[112:115], v[64:67]\n\t"
    "ds_read_b128 v[104:107], %[da] offset:24576\n\t"
    "v_add_f32 v48, v48, v80\n\t"
    "v_add_f32 v49, v49, v81\n\t"
    "v_mfma_f32_16x16x32_bf16 v[68:71], v[212:215], v[112:115], v[68:71]\n\t"
    "ds_read_b128 v[108:111], %[da] offset:26624\n\t"
    "v_add_f32 v50, v50, v82\n\t"
    "v_mfma_f32_16x16x32_bf16 v[72:75], v[208:211], v[116:119], v[72:75]\n\t"
    "ds_read_b32 v39, %[sa]\n\t"
    "v_add_f32 v51, v51, v83\n\t"
    "v_mfma_f32_16x16x32_bf16 v[76:79], v[212:215], v[116:119], v[76:79]\n\t"
    "v_xor_b32 %[sa], 0x100, %[sa]\n\t"
    "v_add_f32 v52, v52, v84\n\t"
    "v_add_f32 v53, v53, v85\n\t"
    "v_mfma_f32_16x16x32_bf16 v[64:67], v[216:219], v[120:123], v[64:67]\n\t"
    "ds_read_b128 v[112:115], %[ea] offset:28672\n\t"
    "v_add_f32 v54, v54, v86\n\t"
    "v_mfma_f32_16x16x32_bf16 v[68:71], v[220:223], v[120:123], v[68:71]\n\t"
    "ds_read_b128 v[116:119], %[ea] offset:30720\n\t"
    "v_add_f32 v55, v55, v87\n\t"
    "v_mfma_f32_16x16x32_bf16 v[72:75], v[216:219], v[124:127], v[72:75]\n\t"
    "v_xor_b32 %[ea], 0x10000, %[ea]\n\t"
    "v_add_f32 v56, v56, v88\n\t"
    "v_add_f32 v57, v57, v89\n\t"
    "v_mfma_f32_16x16x32_bf16 v[76:79], v[220:223], v[124:127], v[76:79]\n\t"
    "v_add_f32 v58, v58, v90\n\t"
    "v_mfma_f32_16x16x32_bf16 v[64:67], v[224:227], v[96:99], v[64:67]\n\t"
    "ds_read_b128 v[120:123], %[da] offset:28672\n\t"
    "v_add_f32 v59, v59, v91\n\t"
    "v_mfma_f32_16x16x32_bf16 v[68:71], v[228:231], v[96:99], v[68:71]\n\t"
    "ds_read_b128 v[124:127], %[da] offset:30720\n\t"
    "v_add_f32 v60, v60, v92\n\t"
    "v_mfma_f32_16x16x32_bf16 v[72:75], v[224:227], v[100:103], v[72:75]\n\t"
    "v_xor_b32 %[da], 0x10000, %[da]\n\t"
    "v_add_f32 v61, v61, v93\n\t"
    "v_add_f32 v62, v62, v94\n\t"
    "v_mfma_f32_16x16x32_bf16 v[76:79], v[228:231], v[100:103], v[76:79]\n\t"
    "v_add_f32 v63, v63, v95\n\t"
    "s_waitcnt lgkmcnt(5)\n\t"
    "v_mfma_f32_16x16x32_bf16 v[64:67], v[232:235], v[104:107], v[64:67]\n\t"
    "ds_read_b128 v[96:99], %[ea] offset:0\n\t"
    "v_cvt_pk_bf16_f32 v40, v48, v49\n\t"
    "v_mfma_f32_16x16x32_bf16 v[68:71], v[236:239], v[104:107], v[68:71]\n\t"
    "ds_read_b128 v[100:103], %[ea] offset:2048\n\t"
    "v_cvt_pk_bf16_f32 v41, v50, v51\n\t"
    "v_cvt_pk_bf16_f32 v42, v52, v53\n\t"
    "v_mfma_f32_16x16x32_bf16 v[72:75], v[232:235], v[108:111], v[72:75]\n\t"
    "v_cvt_pk_bf16_f32 v43, v54, v55\n\t"
    "v_mfma_f32_16x16x32_bf16 v[76:79], v[236:239], v[108:111], v[76:79]\n\t"
    "global_store_dwordx4 %[co0], v[40:43], s[36:37] sc1\n\t"
    "s_waitcnt lgkmcnt(4)\n\t"
    "v_mfma_f32_16x16x32_bf16 v[64:67], v[240:243], v[112:115], v[64:67]\n\t"
    "ds_read_b128 v[104:107], %[da] offset:0\n\t"
    "v_mad_u32_u24 v39, v39, %[mu], %[ad]\n\t"
    "v_cvt_pk_bf16_f32 v44, v56, v57\n\t"
    "v_cvt_pk_bf16_f32 v45, v58, v59\n\t"
    "v_mfma_f32_16x16x32_bf16 v[68:71], v[244:247], v[112:115], v[68:71]\n\t"
    "ds_read_b128 v[108:111], %[da] offset:2048\n\t"
    "v_cmp_ge_i32 vcc, %[lf], v39\n\t"
    "s_cmp_eq_u64 vcc, 0\n\t"
    "s_addc_u32 %[lf], %[lf], 0\n\t"
    "v_cvt_pk_bf16_f32 v46, v60, v61\n\t"
    "v_mfma_f32_16x16x32_bf16 v[72:75], v[240:243], v[116:119], v[72:75]\n\t"
    "v_cvt_pk_bf16_f32 v47, v62, v63\n\t"
    "v_mfma_f32_16x16x32_bf16 v[76:79], v[244:247], v[116:119], v[76:79]\n\t"
    "v_cmp_ge_i32 vcc, %[lf], v39\n\t"
    "s_cmp_eq_u64 vcc, 0\n\t"
    "s_addc_u32 %[lf], %[lf], 0\n\t"
    "global_store_dwordx4 %[co1], v[44:47], s[36:37] sc1\n\t"
    "s_waitcnt lgkmcnt(4)\n\t"
    "v_mfma_f32_16x16x32_bf16 v[80:83], v[248:251], v[120:123], v[64:67]\n\t"
    "ds_read_b128 v[112:115], %[ea] offset:4096\n\t"
    "s_min_i32 %[lf], %[lf], %[cp]\n\t"
    "s_add_u32 %[b], %[b], 1\n\t"
    "v_mfma_f32_16x16x32_bf16 v[84:87], v[252:255], v[120:123], v[68:71]\n\t"
    "ds_read_b128 v[116:119], %[ea] offset:6144\n\t"
    "v_mfma_f32_16x16x32_bf16 v[88:91], v[248:251], v[124:127], v[72:75]\n\t"
    "v_mfma_f32_16x16x32_bf16 v[92:95], v[252:255], v[124:127], v[76:79]\n\t"
    "s_mov_b64 s[36:37], s[38:39]\n\t"
    "s_add_u32 s38, s38, %[ab]\n\t"
    "s_addc_u32 s39, s39, 0\n\t"
    "s_cmp_lt_i32 %[b], %[lf]\n\t"
    "s_cbranch_scc1 1b\n\t"
    "s_waitcnt lgkmcnt(0)\n\t"
    "s_nop 4\n\t"
    "s_branch 9f\n\t"
    "5:\n\t"
    "s_add_u32 %[st], %[b], %[po]\n\t"
    "s_max_i32 %[st], %[st], %[p0]\n\t"
    "v_mov_b32 v39, %[st]\n\t"
    "v_mov_b32 v38, 0\n\t"
    "s_mov_b64 exec, 1\n\t"
    "global_store_dword v38, v39, %[pg] sc1\n\t"
    "s_mov_b64 exec, -1\n\t"
    "s_mov_b32 %[p0], %[p1]\n\t"
    "s_xor_b32 %[sl], %[sl], 0x100\n\t"
    "s_cmp_ge_i32 %[lf], %[cp]\n\t"
    "s_cbranch_scc1 6b\n\t"
    "s_mov_b32 m0, %[sl]\n\t"
    "s_nop 0\n\t"
    "global_load_lds_dword %[dv], %[db] sc1\n\t"
    "s_branch 6b\n\t"
    "9:"
    : "=&{v[80:95]}"(pd), [ea] "+&v"(ea), [da] "+&v"(da), [b] "+&s"(b), [lf] "+&s"(lf), "+{s[36:37]}"(p1), "+{s[38:39]}"(p0), [sa] "+&v"(sa), [st] "=&s"(st), [sl] "+&s"(sl), [p0] "+&s"(pf)
    : [rw] "v"(rw), [ab] "s"(ab), [cp] "s"(cp), [mu] "s"(mu), [ad] "v"(ad), "{v[128:159]}"(W[0]), "{v[160:191]}"(W[1]), "{v[192:223]}"(W[2]), "{v[224:255]}"(W[3]), [co0] "v"(co0), [w0] "s"(w0), [dv] "v"(dv), [db] "s"(db), [pg] "s"(pg), [po] "s"(po - 1), [p1] "s"(b + po), [co1] "v"(co1)
    : "v38", "v39", "v40", "v41", "v42", "v43", "v44", "v45", "v46", "v47", "v48", "v49", "v50", "v51", "v52", "v53", "v54", "v55", "v56", "v57", "v58", "v59", "v60", "v61", "v62", "v63", "v64", "v65", "v66", "v67", "v68", "v69", "v70", "v71", "v72", "v73", "v74", "v75", "v76", "v77", "v78", "v79", "v96", "v97", "v98", "v99", "v100", "v101", "v102", "v103", "v104", "v105", "v106", "v107", "v108", "v109", "v110", "v111", "v112", "v113", "v114", "v115", "v116", "v117", "v118", "v119", "v120", "v121", "v122", "v123", "v124", "v125", "v126", "v127", "vcc", "scc", "m0", "memory");
}
__device__ __forceinline__ void fast_st1(const u32x32 (&W)[4], u32x16& pd, unsigned ea, unsigned da, int& b, int& lf, int cp, unsigned sa, unsigned mu, unsigned ad,
    unsigned rw, unsigned ab, const char* p1, const char* p0, unsigned co0, unsigned co1, int po, int pf, unsigned w0, unsigned sl, unsigned dv, const char* db, const char* pg) {
  unsigned st;
  asm volatile("s_barrier\n\t"
    "ds_read_b128 v[96:99], %[ea] offset:0\n\t"
    "ds_read_b128 v[100:103], %[ea] offset:2048\n\t"
    "ds_read_b128 v[104:107], %[da] offset:0\n\t"
    "ds_read_b128 v[108:111], %[da] offset:2048\n\t"
    "ds_read_b128 v[112:115], %[ea] offset:4096\n\t"
    "ds_read_b128 v[116:119], %[ea] offset:6144\n\t"
    "s_branch 2f\n\t"
    "1:\n\t"
    "s_barrier\n\t"
    "2:\n\t"
    "s_waitcnt lgkmcnt(4)\n\t"
    "v_mfma_f32_16x16x32_bf16 v[64:67], v[128:131], v[96:99], 0\n\t"
    "ds_read_b128 v[120:123], %[da] offset:4096\n\t"
    "v_mfma_f32_16x16x32_bf16 v[68:71], v[132:135], v[96:99], 0\n\t"
    "ds_read_b128 v[124:127], %[da] offset:6144\n\t"
    "v_mfma_f32_16x16x32_bf16 v[72:75], v[128:131], v[100:103], 0\n\t"
    "v_mfma_f32_16x16x32_bf16 v[76:79], v[132:135], v[100:103], 0\n\t"
    "s_waitcnt lgkmcnt(4)\n\t"
    "v_mfma_f32_16x16x32_bf16 v[64:67], v[136:139], v[104:107], v[64:67]\n\t"
    "ds_read_b128 v[96:99], %[ea] offset:8192\n\t"
    "v_mfma_f32_16x16x32_bf16 v[68:71], v[140:143], v[104:107], v[68:71]\n\t"
    "ds_read_b128 v[100:103], %[ea] offset:10240\n\t"
    "v_mfma_f32_16x16x32_bf16 v[72:75], v[136:139], v[108:111], v[72:75]\n\t"
    "v_mfma_f32_16x16x32_bf16 v[76:79], v[140:143], v[108:111], v[76:79]\n\t"
    "s_waitcnt lgkmcnt(4)\n\t"
    "v_mfma_f32_16x16x32_bf16 v[64:67], v[144:147], v[112:115], v[64:67]\n\t"
    "ds_read_b128 v[104:107], %[da] offset:8192\n\t"
    "v_mfma_f32_16x16x32_bf16 v[68:71], v[148:151], v[112:115], v[68:71]\n\t"
    "ds_read_b128 v[108:111], %[da] offset:10240\n\t"
    "v_mfma_f32_16x16x32_bf16 v[72:75], v[144:147], v[116:119], v[72:75]\n\t"
    "v_mfma_f32_16x16x32_bf16 v[76:79], v[148:151], v[116:119], v[76:79]\n\t"
    "s_waitcnt lgkmcnt(4)\n\t"
    "v_mfma_f32_16x16x32_bf16 v[64:67], v[152:155], v[120:123], v[64:67]\n\t"
    "ds_read_b128 v[112:115], %[ea] offset:12288\n\t"
    "v_mfma_f32_16x16x32_bf16 v[68:71], v[156:159], v[120:123], v[68:71]\n\t"
    "ds_read_b128 v[116:119], %[ea] offset:14336\n\t"
    "v_mfma_f32_16x16x32_bf16 v[72:75], v[152:155], v[124:127], v[72:75]\n\t"
    "v_mfma_f32_16x16x32_bf16 v[76:79], v[156:159], v[124:127], v[76:79]\n\t"
    "s_waitcnt lgkmcnt(4)\n\t"
    "v_mfma_f32_16x16x32_bf16 v[64:67], v[160:163], v[96:99], v[64:67]\n\t"
    "ds_read_b128 v[120:123], %[da] offset:12288\n\t"
    "v_mfma_f32_16x16x32_bf16 v[68:71], v[164:167], v[96:99], v[68:71]\n\t"
    "ds_read_b128 v[124:127], %[da] offset:14336\n\t"
    "v_mfma_f32_16x16x32_bf16 v[72:75], v[160:163], v[100:103], v[72:75]\n\t"
    "v_mfma_f32_16x16x32_bf16 v[76:79], v[164:167], v[100:103], v[76:79]\n\t"
    "s_waitcnt lgkmcnt(4)\n\t"
    "v_mfma_f32_16x16x32_bf16 v[64:67], v[168:171], v[104:107], v[64:67]\n\t"
    "ds_read_b128 v[96:99], %[ea] offset:16384\n\t"
    "v_mfma_f32_16x16x32_bf16 v[68:71], v[172:175], v[104:107], v[68:71]\n\t"
    "ds_read_b128 v[100:103], %[ea] offset:18432\n\t"
    "v_mfma_f32_16x16x32_bf16 v[72:75], v[168:171], v[108:111], v[72:75]\n\t"
    "v_mfma_f32_16x16x32_bf16 v[76:79], v[172:175], v[108:111], v[76:79]\n\t"
    "s_waitcnt lgkmcnt(4)\n\t"
    "v_mfma_f32_16x16x32_bf16 v[64:67], v[176:179], v[112:115], v[64:67]\n\t"
    "ds_read_b128 v[104:107], %[da] offset:16384\n\t"
    "v_mfma_f32_16x16x32_bf16 v[68:71], v[180:183], v[112:115], v[68:71]\n\t"
    "ds_read_b128 v[108:111], %[da] offset:18432\n\t"
    "v_mfma_f32_16x16x32_bf16 v[72:75], v[176:179], v[116:119], v[72:75]\n\t"
    "v_mfma_f32_16x16x32_bf16 v[76:79], v[180:183], v[116:119], v[76:79]\n\t"
    "s_waitcnt lgkmcnt(4)\n\t"
    "v_mfma_f32_16x16x32_bf16 v[64:67], v[184:187], v[120:123], v[64:67]\n\t"
    "ds_read_b128 v[112:115], %[ea] offset:20480\n\t"
    "v_mfma_f32_16x16x32_bf16 v[68:71], v[188:191], v[120:123], v[68:71]\n\t"
    "ds_read_b128 v[116:119], %[ea] offset:22528\n\t"
    "v_mfma_f32_16x16x32_bf16 v[72:75], v[184:187], v[124:127], v[72:75]\n\t"
    "v_mfma_f32_16x16x32_bf16 v[76:79], v[188:191], v[124:127], v[76:79]\n\t"
    "s_waitcnt vmcnt(0)\n\t"
    "s_barrier\n\t"
    "s_waitcnt lgkmcnt(4)\n\t"
    "v_mfma_f32_16x16x32_bf16 v[64:67], v[192:195], v[96:99], v[64:67]\n\t"
    "ds_read_b128 v[120:123], %[da] offset:20480\n\t"
    "v_mfma_f32_16x16x32_bf16 v[68:71], v[196:199], v[96:99], v[68:71]\n\t"
    "ds_read_b128 v[124:127], %[da] offset:22528\n\t"
    "v_mfma_f32_16x16x32_bf16 v[72:75], v[192:195], v[100:103], v[72:75]\n\t"
    "ds_read_b128 v[48:51], %[rw] offset:0\n\t"
    "v_mfma_f32_16x16x32_bf16 v[76:79], v[196:199], v[100:103], v[76:79]\n\t"
    "ds_read_b128 v[52:55], %[rw] offset:1024\n\t"
    "s_cmp_eq_u32 %[w0], 0\n\t"
    "s_cbranch_scc1 5f\n\t"
    "6:\n\t"
    "s_waitcnt lgkmcnt(6)\n\t"
    "v_mfma_f32_16x16x32_bf16 v[64:67], v[200:203], v[104:107], v[64:67]\n\t"
    "ds_read_b128 v[96:99], %[ea] offset:24576\n\t"
    "v_mfma_f32_16x16x32_bf16 v[68:71], v[204:207], v[104:107], v[68:71]\n\t"
    "ds_read_b128 v[100:103], %[ea] offset:26624\n\t"
    "v_mfma_f32_16x16x32_bf16 v[72:75], v[200:203], v[108:111], v[72:75]\n\t"
    "ds_read_b128 v[56:59], %[rw] offset:2048\n\t"
    "v_mfma_f32_16x16x32_bf16 v[76:79], v[204:207], v[108:111], v[76:79]\n\t"
    "ds_read_b128 v[60:63], %[rw] offset:3072\n\t"
    "s_waitcnt lgkmcnt(0)\n\t"
    "v_mfma_f32_16x16x32_bf16 v[64:67], v[208:211], v[112:115], v[64:67]\n\t"
    "ds_read_b128 v[104:107], %[da] offset:24576\n\t"
    "v_add_f32 v48, v48, v80\n\t"
    "v_add_f32 v49, v49, v81\n\t"
    "v_add_f32 v50, v50, v82\n\t"
    "v_mfma_f32_16x16x32_bf16 v[68:71], v[212:215], v[112:115], v[68:71]\n\t"
    "ds_read_b128 v[108:111], %[da] offset:26624\n\t"
    "v_add_f32 v51, v51, v83\n\t"
    "v_add_f32 v52, v52, v84\n\t"
    "v_add_f32 v53, v53, v85\n\t"
    "v_mfma_f32_16x16x32_bf16 v[72:75], v[208:211], v[116:119], v[72:75]\n\t"
    "ds_read_b32 v39, %[sa]\n\t"
    "v_add_f32 v54, v54, v86\n\t"
    "v_add_f32 v55, v55, v87\n\t"
    "v_add_f32 v56, v56, v88\n\t"
    "v_mfma_f32_16x16x32_bf16 v[76:79], v[212:215], v[116:119], v[76:79]\n\t"
    "v_xor_b32 %[sa], 0x100, %[sa]\n\t"
    "v_add_f32 v57, v57, v89\n\t"
    "v_add_f32 v58, v58, v90\n\t"
    "v_add_f32 v59, v59, v91\n\t"
    "v_mfma_f32_16x16x32_bf16 v[64:67], v[216:219], v[120:123], v[64:67]\n\t"
    "ds_read_b128 v[112:115], %[ea] offset:28672\n\t"
    "v_add_f32 v60, v60, v92\n\t"
    "v_add_f32 v61, v61, v93\n\t"
    "v_add_f32 v62, v62, v94\n\t"
    "v_mfma_f32_16x16x32_bf16 v[68:71], v[220:223], v[120:123], v[68:71]\n\t"
    "ds_read_b128 v[116:119], %[ea] offset:30720\n\t"
    "v_add_f32 v63, v63, v95\n\t"
    "v_mul_f32 v40, %[nl], v48\n\t"
    "v_mul_f32 v41, %[nl], v49\n\t"
    "v_mfma_f32_16x16x32_bf16 v[72:75], v[216:219], v[124:127], v[72:75]\n\t"
    "v_xor_b32 %[ea], 0x10000, %[ea]\n\t"
    "v_mul_f32 v42, %[nl], v50\n\t"
    "v_mul_f32 v43, %[nl], v51\n\t"
    "v_mul_f32 v44, %[nl], v56\n\t"
    "v_mfma_f32_16x16x32_bf16 v[76:79], v[220:223], v[124:127], v[76:79]\n\t"
    "v_mul_f32 v45, %[nl], v57\n\t"
    "v_mul_f32 v46, %[nl], v58\n\t"
    "v_mul_f32 v47, %[nl], v59\n\t"
    "v_mfma_f32_16x16x32_bf16 v[64:67], v[224:227], v[96:99], v[64:67]\n\t"
    "ds_read_b128 v[120:123], %[da] offset:28672\n\t"
    "v_exp_f32 v40, v40\n\t"
    "v_exp_f32 v41, v41\n\t"
    "v_exp_f32 v42, v42\n\t"
    "v_mfma_f32_16x16x32_bf16 v[68:71], v[228:231], v[96:99], v[68:71]\n\t"
    "ds_read_b128 v[124:127], %[da] offset:30720\n\t"
    "v_exp_f32 v43, v43\n\t"
    "v_exp_f32 v44, v44\n\t"
    "v_exp_f32 v45, v45\n\t"
    "v_mfma_f32_16x16x32_bf16 v[72:75], v[224:227], v[100:103], v[72:75]\n\t"
    "v_xor_b32 %[da], 0x10000, %[da]\n\t"
    "v_exp_f32 v46, v46\n\t"
    "v_exp_f32 v47, v47\n\t"
    "v_add_f32 v40, 1.0, v40\n\t"
    "v_mfma_f32_16x16x32_bf16 v[76:79], v[228:231], v[100:103], v[76:79]\n\t"
    "v_add_f32 v41, 1.0, v41\n\t"
    "v_add_f32 v42, 1.0, v42\n\t"
    "v_add_f32 v43, 1.0, v43\n\t"
    "s_waitcnt lgkmcnt(5)\n\t"
    "v_mfma_f32_16x16x32_bf16 v[64:67], v[232:235], v[104:107], v[64:67]\n\t"
    "ds_read_b128 v[96:99], %[ea] offset:0\n\t"
    "v_add_f32 v44, 1.0, v44\n\t"
    "v_add_f32 v45, 1.0, v45\n\t"
    "v_add_f32 v46, 1.0, v46\n\t"
    "v_mfma_f32_16x16x32_bf16 v[68:71], v[236:239], v[104:107], v[68:71]\n\t"
    "ds_read_b128 v[100:103], %[ea] offset:2048\n\t"
    "v_add_f32 v47, 1.0, v47\n\t"
    "v_rcp_f32 v40, v40\n\t"
    "v_rcp_f32 v41, v41\n\t"
    "v_mfma_f32_16x16x32_bf16 v[72:75], v[232:235], v[108:111], v[72:75]\n\t"
    "v_rcp_f32 v42, v42\n\t"
    "v_rcp_f32 v43, v43\n\t"
    "v_rcp_f32 v44, v44\n\t"
    "v_mfma_f32_16x16x32_bf16 v[76:79], v[236:239], v[108:111], v[76:79]\n\t"
    "v_rcp_f32 v45, v45\n\t"
    "v_rcp_f32 v46, v46\n\t"
    "v_rcp_f32 v47, v47\n\t"
    "s_waitcnt lgkmcnt(4)\n\t"
    "v_mfma_f32_16x16x32_bf16 v[64:67], v[240:243], v[112:115], v[64:67]\n\t"
    "ds_read_b128 v[104:107], %[da] offset:0\n\t"
    "v_mad_u32_u24 v39, v39, %[mu], %[ad]\n\t"
    "v_mul_f32 v40, v48, v40\n\t"
    "v_mul_f32 v41, v49, v41\n\t"
    "v_mul_f32 v42, v50, v42\n\t"
    "v_mfma_f32_16x16x32_bf16 v[68:71], v[244:247], v[112:115], v[68:71]\n\t"
    "ds_read_b128 v[108:111], %[da] offset:2048\n\t"
    "v_cmp_ge_i32 vcc, %[lf], v39\n\t"
    "s_cmp_eq_u64 vcc, 0\n\t"
    "s_addc_u32 %[lf], %[lf], 0\n\t"
    "v_mul_f32 v43, v51, v43\n\t"
    "v_mul_f32 v44, v56, v44\n\t"
    "v_mul_f32 v45, v57, v45\n\t"
    "v_mfma_f32_16x16x32_bf16 v[72:75], v[240:243], v[116:119], v[72:75]\n\t"
    "v_mul_f32 v46, v58, v46\n\t"
    "v_mul_f32 v47, v59, v47\n\t"
    "v_mul_f32 v40, v40, v52\n\t"
    "v_mfma_f32_16x16x32_bf16 v[76:79], v[244:247], v[116:119], v[76:79]\n\t"
    "v_cmp_ge_i32 vcc, %[lf], v39\n\t"
    "s_cmp_eq_u64 vcc, 0\n\t"
    "s_addc_u32 %[lf], %[lf], 0\n\t"
    "v_mul_f32 v41, v41, v53\n\t"
    "v_mul_f32 v42, v42, v54\n\t"
    "v_mul_f32 v43, v43, v55\n\t"
    "s_waitcnt lgkmcnt(4)\n\t"
    "v_mfma_f32_16x16x32_bf16 v[80:83], v[248:251], v[120:123], v[64:67]\n\t"
    "ds_read_b128 v[112:115], %[ea] offset:4096\n\t"
    "s_min_i32 %[lf], %[lf], %[cp]\n\t"
    "s_add_u32 %[b], %[b], 1\n\t"
    "v_mul_f32 v44, v44, v60\n\t"
    "v_mul_f32 v45, v45, v61\n\t"
    "v_mul_f32 v46, v46, v62\n\t"
    "v_mfma_f32_16x16x32_bf16 v[84:87], v[252:255], v[120:123], v[68:71]\n\t"
    "ds_read_b128 v[116:119], %[ea] offset:6144\n\t"
    "v_mul_f32 v47, v47, v63\n\t"
    "v_cvt_pk_bf16_f32 v48, v40, v41\n\t"
    "v_cvt_pk_bf16_f32 v49, v42, v43\n\t"
    "v_mfma_f32_16x16x32_bf16 v[88:91], v[248:251], v[124:127], v[72:75]\n\t"
    "v_cvt_pk_bf16_f32 v50, v44, v45\n\t"
    "v_cvt_pk_bf16_f32 v51, v46, v47\n\t"
    "s_nop 1\n\t"
    "v_mfma_f32_16x16x32_bf16 v[92:95], v[252:255], v[124:127], v[76:79]\n\t"
    "v_permlane16_swap_b32 v48, v50\n\t"
    "v_permlane16_swap_b32 v49, v51\n\t"
    "global_store_dwordx4 %[co0], v[48:51], s[36:37] sc1\n\t"
    "s_mov_b64 s[36:37], s[38:39]\n\t"
    "s_add_u32 s38, s38, %[ab]\n\t"
    "s_addc_u32 s39, s39, 0\n\t"
    "s_cmp_lt_i32 %[b], %[lf]\n\t"
    "s_cbranch_scc1 1b\n\t"
    "s_waitcnt lgkmcnt(0)\n\t"
    "s_nop 4\n\t"
    "s_branch 9f\n\t"
    "5:\n\t"
    "s_add_u32 %[st], %[b], %[po]\n\t"
    "s_max_i32 %[st], %[st], %[p0]\n\t"
    "v_mov_b32 v39, %[st]\n\t"
    "v_mov_b32 v38, 0\n\t"
    "s_mov_b64 exec, 1\n\t"
    "global_store_dword v38, v39, %[pg] sc1\n\t"
    "s_mov_b64 exec, -1\n\t"
    "s_mov_b32 %[p0], %[p1]\n\t"
    "s_xor_b32 %[sl], %[sl], 0x100\n\t"
    "s_cmp_ge_i32 %[lf], %[cp]\n\t"
    "s_cbranch_scc1 6b\n\t"
    "s_mov_b32 m0, %[sl]\n\t"
    "s_nop 0\n\t"
    "global_load_lds_dword %[dv], %[db] sc1\n\t"
    "s_branch 6b\n\t"
    "9:"
    : "=&{v[80:95]}"(pd), [ea] "+&v"(ea), [da] "+&v"(da), [b] "+&s"(b), [lf] "+&s"(lf), "+{s[36:37]}"(p1), "+{s[38:39]}"(p0), [sa] "+&v"(sa), [st] "=&s"(st), [sl] "+&s"(sl), [p0] "+&s"(pf)
    : [rw] "v"(rw), [ab] "s"(ab), [cp] "s"(cp), [mu] "s"(mu), [ad] "v"(ad), "{v[128:159]}"(W[0]), "{v[160:191]}"(W[1]), "{v[192:223]}"(W[2]), "{v[224:255]}"(W[3]), [co0] "v"(co0), [w0] "s"(w0), [dv] "v"(dv), [db] "s"(db), [pg] "s"(pg), [po] "s"(po - 1), [p1] "s"(b + po), [nl] "s"(-LOG2E)
    : "v38", "v39", "v40", "v41", "v42", "v43", "v44", "v45", "v46", "v47", "v48", "v49", "v50", "v51", "v52", "v53", "v54", "v55", "v56", "v57", "v58", "v59", "v60", "v61", "v62", "v63", "v64", "v65", "v66", "v67", "v68", "v69", "v70", "v71", "v72", "v73", "v74", "v75", "v76", "v77", "v78", "v79", "v96", "v97", "v98", "v99", "v100", "v101", "v102", "v103", "v104", "v105", "v106", "v107", "v108", "v109", "v110", "v111", "v112", "v113", "v114", "v115", "v116", "v117", "v118", "v119", "v120", "v121", "v122", "v123", "v124", "v125", "v126", "v127", "vcc", "scc", "m0", "memory");
}
__device__ __forceinline__ void fast_st2(const u32x32 (&W)[4], u32x16& pd, unsigned ea, unsigned da, int& b, int& lf, int cp, unsigned sa, unsigned mu, unsigned ad,
    unsigned rw, unsigned ab, const char* p1, const char* p0, unsigned co0, unsigned co1, int po, int pf, unsigned w0, unsigned sl, unsigned dv, const char* db, const char* pg) {
  unsigned st;
  asm volatile("s_barrier\n\t"
    "ds_read_b128 v[96:99], %[ea] offset:0\n\t"
    "ds_read_b128 v[100:103], %[ea] offset:2048\n\t"
    "ds_read_b128 v[104:107], %[da] offset:0\n\t"
    "ds_read_b128 v[108:111], %[da] offset:2048\n\t"
    "ds_read_b128 v[112:115], %[ea] offset:4096\n\t"
    "ds_read_b128 v[116:119], %[ea] offset:6144\n\t"
    "s_branch 2f\n\t"
    "1:\n\t"
    "s_barrier\n\t"
    "2:\n\t"
    "s_waitcnt lgkmcnt(4)\n\t"
    "v_mfma_f32_16x16x32_bf16 v[64:67], v[128:131], v[96:99], 0\n\t"
    "ds_read_b128 v[120:123], %[da] offset:4096\n\t"
    "v_mfma_f32_16x16x32_bf16 v[68:71], v[132:135], v[96:99], 0\n\t"
    "ds_read_b128 v[124:127], %[da] offset:6144\n\t"
    "v_mfma_f32_16x16x32_bf16 v[72:75], v[128:131], v[100:103], 0\n\t"
    "v_mfma_f32_16x16x32_bf16 v[76:79], v[132:135], v[100:103], 0\n\t"
    "s_waitcnt lgkmcnt(4)\n\t"
    "v_mfma_f32_16x16x32_bf16 v[64:67], v[136:139], v[104:107], v[64:67]\n\t"
    "ds_read_b128 v[96:99], %[ea] offset:8192\n\t"
    "v_mfma_f32_16x16x32_bf16 v[68:71], v[140:143], v[104:107], v[68:71]\n\t"
    "ds_read_b128 v[100:103], %[ea] offset:10240\n\t"
    "v_mfma_f32_16x16x32_bf16 v[72:75], v[136:139], v[108:111], v[72:75]\n\t"
    "v_mfma_f32_16x16x32_bf16 v[76:79], v[140:143], v[108:111], v[76:79]\n\t"
    "s_waitcnt lgkmcnt(4)\n\t"
    "v_mfma_f32_16x16x32_bf16 v[64:67], v[144:147], v[112:115], v[64:67]\n\t"
    "ds_read_b128 v[104:107], %[da] offset:8192\n\t"
    "v_mfma_f32_16x16x32_bf16 v[68:71], v[148:151], v[112:115], v[68:71]\n\t"
    "ds_read_b128 v[108:111], %[da] offset:10240\n\t"
    "v_mfma_f32_16x16x32_bf16 v[72:75], v[144:147], v[116:119], v[72:75]\n\t"
    "v_mfma_f32_16x16x32_bf16 v[76:79], v[148:151], v[116:119], v[76:79]\n\t"
    "s_waitcnt lgkmcnt(4)\n\t"
    "v_mfma_f32_16x16x32_bf16 v[64:67], v[152:155], v[120:123], v[64:67]\n\t"
    "ds_read_b128 v[112:115], %[ea] offset:12288\n\t"
    "v_mfma_f32_16x16x32_bf16 v[68:71], v[156:159], v[120:123], v[68:71]\n\t"
    "ds_read_b128 v[116:119], %[ea] offset:14336\n\t"
    "v_mfma_f32_16x16x32_bf16 v[72:75], v[152:155], v[124:127], v[72:75]\n\t"
    "v_mfma_f32_16x16x32_bf16 v[76:79], v[156:159], v[124:127], v[76:79]\n\t"
    "s_waitcnt lgkmcnt(4)\n\t"
    "v_mfma_f32_16x16x32_bf16 v[64:67], v[160:163], v[96:99], v[64:67]\n\t"
    "ds_read_b128 v[120:123], %[da] offset:12288\n\t"
    "v_mfma_f32_16x16x32_bf16 v[68:71], v[164:167], v[96:99], v[68:71]\n\t"
    "ds_read_b128 v[124:127], %[da] offset:14336\n\t"
    "v_mfma_f32_16x16x32_bf16 v[72:75], v[160:163], v[100:103], v[72:75]\n\t"
    "v_mfma_f32_16x16x32_bf16 v[76:79], v[164:167], v[100:103], v[76:79]\n\t"
    "s_waitcnt lgkmcnt(4)\n\t"
    "v_mfma_f32_16x16x32_bf16 v[64:67], v[168:171], v[104:107], v[64:67]\n\t"
    "ds_read_b128 v[96:99], %[ea] offset:16384\n\t"
    "v_mfma_f32_16x16x32_bf16 v[68:71], v[172:175], v[104:107], v[68:71]\n\t"
    "ds_read_b128 v[100:103], %[ea] offset:18432\n\t"
    "v_mfma_f32_16x16x32_bf16 v[72:75], v[168:171], v[108:111], v[72:75]\n\t"
    "v_mfma_f32_16x16x32_bf16 v[76:79], v[172:175], v[108:111], v[76:79]\n\t"
    "s_waitcnt lgkmcnt(4)\n\t"
    "v_mfma_f32_16x16x32_bf16 v[64:67], v[176:179], v[112:115], v[64:67]\n\t"
    "ds_read_b128 v[104:107], %[da] offset:16384\n\t"
    "v_mfma_f32_16x16x32_bf16 v[68:71], v[180:183], v[112:115], v[68:71]\n\t"
    "ds_read_b128 v[108:111], %[da] offset:18432\n\t"
    "v_mfma_f32_16x16x32_bf16 v[72:75], v[176:179], v[116:119], v[72:75]\n\t"
    "v_mfma_f32_16x16x32_bf16 v[76:79], v[180:183], v[116:119], v[76:79]\n\t"
    "s_waitcnt lgkmcnt(4)\n\t"
    "v_mfma_f32_16x16x32_bf16 v[64:67], v[184:187], v[120:123], v[64:67]\n\t"
    "ds_read_b128 v[112:115], %[ea] offset:20480\n\t"
    "v_mfma_f32_16x16x32_bf16 v[68:71], v[188:191], v[120:123], v[68:71]\n\t"
    "ds_read_b128 v[116:119], %[ea] offset:22528\n\t"
    "v_mfma_f32_16x16x32_bf16 v[72:75], v[184:187], v[124:127], v[72:75]\n\t"
    "v_mfma_f32_16x16x32_bf16 v[76:79], v[188:191], v[124:127], v[76:79]\n\t"
    "s_waitcnt vmcnt(0)\n\t"
    "s_barrier\n\t"
    "s_waitcnt lgkmcnt(4)\n\t"
    "v_mfma_f32_16x16x32_bf16 v[64:67], v[192:195], v[96:99], v[64:67]\n\t"
    "ds_read_b128 v[120:123], %[da] offset:20480\n\t"
    "v_mfma_f32_16x16x32_bf16 v[68:71], v[196:199], v[96:99], v[68:71]\n\t"
    "ds_read_b128 v[124:127], %[da] offset:22528\n\t"
    "v_mfma_f32_16x16x32_bf16 v[72:75], v[192:195], v[100:103], v[72:75]\n\t"
    "ds_read_b128 v[48:51], %[rw] offset:0\n\t"
    "v_mfma_f32_16x16x32_bf16 v[76:79], v[196:199], v[100:103], v[76:79]\n\t"
    "ds_read_b128 v[52:55], %[rw] offset:1024\n\t"
    "s_cmp_eq_u32 %[w0], 0\n\t"
    "s_cbranch_scc1 5f\n\t"
    "6:\n\t"
    "s_waitcnt lgkmcnt(6)\n\t"
    "v_mfma_f32_16x16x32_bf16 v[64:67], v[200:203], v[104:107], v[64:67]\n\t"
    "ds_read_b128 v[96:99], %[ea] offset:24576\n\t"
    "v_mfma_f32_16x16x32_bf16 v[68:71], v[204:207], v[104:107], v[68:71]\n\t"
    "ds_read_b128 v[100:103], %[ea] offset:26624\n\t"
    "v_mfma_f32_16x16x32_bf16 v[72:75], v[200:203], v[108:111], v[72:75]\n\t"
    "ds_read_b128 v[56:59], %[rw] offset:2048\n\t"
    "v_mfma_f32_16x16x32_bf16 v[76:79], v[204:207], v[108:111], v[76:79]\n\t"
    "ds_read_b128 v[60:63], %[rw] offset:3072\n\t"
    "s_waitcnt lgkmcnt(0)\n\t"
    "v_mfma_f32_16x16x32_bf16 v[64:67], v[208:211], v[112:115], v[64:67]\n\t"
    "ds_read_b128 v[104:107], %[da] offset:24576\n\t"
    "v_cvt_pk_bf16_f32 v40, v80, v81\n\t"
    "v_mfma_f32_16x16x32_bf16 v[68:71], v[212:215], v[112:115], v[68:71]\n\t"
    "ds_read_b128 v[108:111], %[da] offset:26624\n\t"
    "v_cvt_pk_bf16_f32 v41, v82, v83\n\t"
    "v_mfma_f32_16x16x32_bf16 v[72:75], v[208:211], v[116:119], v[72:75]\n\t"
    "ds_read_b32 v39, %[sa]\n\t"
    "v_cvt_pk_bf16_f32 v42, v84, v85\n\t"
    "v_mfma_f32_16x16x32_bf16 v[76:79], v[212:215], v[116:119], v[76:79]\n\t"
    "v_xor_b32 %[sa], 0x100, %[sa]\n\t"
    "v_cvt_pk_bf16_f32 v43, v86, v87\n\t"
    "v_mfma_f32_16x16x32_bf16 v[64:67], v[216:219], v[120:123], v[64:67]\n\t"
    "ds_read_b128 v[112:115], %[ea] offset:28672\n\t"
    "global_store_dwordx4 %[co0], v[40:43], s[36:37] sc1\n\t"
    "v_mfma_f32_16x16x32_bf16 v[68:71], v[220:223], v[120:123], v[68:71]\n\t"
    "ds_read_b128 v[116:119], %[ea] offset:30720\n\t"
    "v_cvt_pk_bf16_f32 v44, v88, v89\n\t"
    "v_mfma_f32_16x16x32_bf16 v[72:75], v[216:219], v[124:127], v[72:75]\n\t"
    "v_xor_b32 %[ea], 0x10000, %[ea]\n\t"
    "v_cvt_pk_bf16_f32 v45, v90, v91\n\t"
    "v_mfma_f32_16x16x32_bf16 v[76:79], v[220:223], v[124:127], v[76:79]\n\t"
    "v_cvt_pk_bf16_f32 v46, v92, v93\n\t"
    "v_mfma_f32_16x16x32_bf16 v[64:67], v[224:227], v[96:99], v[64:67]\n\t"
    "ds_read_b128 v[120:123], %[da] offset:28672\n\t"
    "v_cvt_pk_bf16_f32 v47, v94, v95\n\t"
    "v_mfma_f32_16x16x32_bf16 v[68:71], v[228:231], v[96:99], v[68:71]\n\t"
    "ds_read_b128 v[124:127], %[da] offset:30720\n\t"
    "global_store_dwordx4 %[co1], v[44:47], s[36:37] sc1\n\t"
    "v_mfma_f32_16x16x32_bf16 v[72:75], v[224:227], v[100:103], v[72:75]\n\t"
    "v_xor_b32 %[da], 0x10000, %[da]\n\t"
    "v_cvt_pk_bf16_f32 v48, v48, v49\n\t"
    "v_mfma_f32_16x16x32_bf16 v[76:79], v[228:231], v[100:103], v[76:79]\n\t"
    "v_cvt_pk_bf16_f32 v49, v50, v51\n\t"
    "s_waitcnt lgkmcnt(5)\n\t"
    "v_mfma_f32_16x16x32_bf16 v[64:67], v[232:235], v[104:107], v[64:67]\n\t"
    "ds_read_b128 v[96:99], %[ea] offset:0\n\t"
    "v_cvt_pk_bf16_f32 v50, v52, v53\n\t"
    "v_mfma_f32_16x16x32_bf16 v[68:71], v[236:239], v[104:107], v[68:71]\n\t"
    "ds_read_b128 v[100:103], %[ea] offset:2048\n\t"
    "v_cvt_pk_bf16_f32 v51, v54, v55\n\t"
    "v_mfma_f32_16x16x32_bf16 v[72:75], v[232:235], v[108:111], v[72:75]\n\t"
    "global_store_dwordx4 %[co0], v[48:51], s[36:37] offset:2048 sc1\n\t"
    "v_mfma_f32_16x16x32_bf16 v[76:79], v[236:239], v[108:111], v[76:79]\n\t"
    "v_cvt_pk_bf16_f32 v56, v56, v57\n\t"
    "s_waitcnt lgkmcnt(4)\n\t"
    "v_mfma_f32_16x16x32_bf16 v[64:67], v[240:243], v[112:115], v[64:67]\n\t"
    "ds_read_b128 v[104:107], %[da] offset:0\n\t"
    "v_mad_u32_u24 v39, v39, %[mu], %[ad]\n\t"
    "v_cvt_pk_bf16_f32 v57, v58, v59\n\t"
    "v_mfma_f32_16x16x32_bf16 v[68:71], v[244:247], v[112:115], v[68:71]\n\t"
    "ds_read_b128 v[108:111], %[da] offset:2048\n\t"
    "v_cmp_ge_i32 vcc, %[lf], v39\n\t"
    "s_cmp_eq_u64 vcc, 0\n\t"
    "s_addc_u32 %[lf], %[lf], 0\n\t"
    "v_cvt_pk_bf16_f32 v58, v60, v61\n\t"
    "v_mfma_f32_16x16x32_bf16 v[72:75], v[240:243], v[116:119], v[72:75]\n\t"
    "v_cvt_pk_bf16_f32 v59, v62, v63\n\t"
    "v_mfma_f32_16x16x32_bf16 v[76:79], v[244:247], v[116:119], v[76:79]\n\t"
    "v_cmp_ge_i32 vcc, %[lf], v39\n\t"
    "s_cmp_eq_u64 vcc, 0\n\t"
    "s_addc_u32 %[lf], %[lf], 0\n\t"
    "global_store_dwordx4 %[co1], v[56:59], s[36:37] offset:2048 sc1\n\t"
    "s_waitcnt lgkmcnt(4)\n\t"
    "v_mfma_f32_16x16x32_bf16 v[80:83], v[248:251], v[120:123], v[64:67]\n\t"
    "ds_read_b128 v[112:115], %[ea] offset:4096\n\t"
    "s_min_i32 %[lf], %[lf], %[cp]\n\t"
    "s_add_u32 %[b], %[b], 1\n\t"
    "v_mfma_f32_16x16x32_bf16 v[84:87], v[252:255], v[120:123], v[68:71]\n\t"
    "ds_read_b128 v[116:119], %[ea] offset:6144\n\t"
    "v_mfma_f32_16x16x32_bf16 v[88:91], v[248:251], v[124:127], v[72:75]\n\t"
    "v_mfma_f32_16x16x32_bf16 v[92:95], v[252:255], v[124:127], v[76:79]\n\t"
    "s_mov_b64 s[36:37], s[38:39]\n\t"
    "s_add_u32 s38, s38, %[ab]\n\t"
    "s_addc_u32 s39, s39, 0\n\t"
    "s_cmp_lt_i32 %[b], %[lf]\n\t"
    "s_cbranch_scc1 1b\n\t"
    "s_waitcnt lgkmcnt(0)\n\t"
    "s_nop 4\n\t"
    "s_branch 9f\n\t"
    "5:\n\t"
    "s_add_u32 %[st], %[b], %[po]\n\t"
    "s_max_i32 %[st], %[st], %[p0]\n\t"
    "v_mov_b32 v39, %[st]\n\t"
    "v_mov_b32 v38, 0\n\t"
    "s_mov_b64 exec, 1\n\t"
    "global_store_dword v38, v39, %[pg] sc1\n\t"
    "s_mov_b64 exec, -1\n\t"
    "s_mov_b32 %[p0], %[p1]\n\t"
    "s_xor_b32 %[sl], %[sl], 0x100\n\t"
    "s_cmp_ge_i32 %[lf], %[cp]\n\t"
    "s_cbranch_scc1 6b\n\t"
    "s_mov_b32 m0, %[sl]\n\t"
    "s_nop 0\n\t"
    "global_load_lds_dword %[dv], %[db] sc1\n\t"
    "s_branch 6b\n\t"
    "9:"
    : "=&{v[80:95]}"(pd), [ea] "+&v"(ea), [da] "+&v"(da), [b] "+&s"(b), [lf] "+&s"(lf), "+{s[36:37]}"(p1), "+{s[38:39]}"(p0), [sa] "+&v"(sa), [st] "=&s"(st), [sl] "+&s"(sl), [p0] "+&s"(pf)
    : [rw] "v"(rw), [ab] "s"(ab), [cp] "s"(cp), [mu] "s"(mu), [ad] "v"(ad), "{v[128:159]}"(W[0]), "{v[160:191]}"(W[1]), "{v[192:223]}"(W[2]), "{v[224:255]}"(W[3]), [co0] "v"(co0), [w0] "s"(w0), [dv] "v"(dv), [db] "s"(db), [pg] "s"(pg), [po] "s"(po - 1), [p1] "s"(b + po), [co1] "v"(co1)
    : "v38", "v39", "v40", "v41", "v42", "v43", "v44", "v45", "v46", "v47", "v48", "v49", "v50", "v51", "v52", "v53", "v54", "v55", "v56", "v57", "v58", "v59", "v60", "v61", "v62", "v63", "v64", "v65", "v66", "v67", "v68", "v69", "v70", "v71", "v72", "v73", "v74", "v75", "v76", "v77", "v78", "v79", "v96", "v97", "v98", "v99", "v100", "v101", "v102", "v103", "v104", "v105", "v106", "v107", "v108", "v109", "v110", "v111", "v112", "v113", "v114", "v115", "v116", "v117", "v118", "v119", "v120", "v121", "v122", "v123", "v124", "v125", "v126", "v127", "vcc", "scc", "m0", "memory");
}
__device__ __forceinline__ void near_ld0(const u32x32 (&W)[4], u32x16& pd, unsigned ea, unsigned da, int& b, int& lf, int cp, unsigned sa, unsigned mu, unsigned ad,
    unsigned rw, unsigned ab, const char* p1, const char* p0, unsigned l1, unsigned l0, const unsigned (&av)[8]) {
  asm volatile("s_setprio 0\n\t"
    "s_waitcnt vmcnt(4)\n\t"
    "s_barrier\n\t"
    "ds_read_b128 v[96:99], %[ea] offset:0\n\t"
    "ds_read_b128 v[100:103], %[ea] offset:2048\n\t"
    "ds_read_b128 v[104:107], %[da] offset:0\n\t"
    "ds_read_b128 v[108:111], %[da] offset:2048\n\t"
    "ds_read_b128 v[112:115], %[ea] offset:4096\n\t"
    "ds_read_b128 v[116:119], %[ea] offset:6144\n\t"
    "s_branch 2f\n\t"
    "1:\n\t"
    "s_waitcnt vmcnt(4)\n\t"
    "s_nop 1\n\t"
    "s_barrier\n\t"
    "2:\n\t"
    "s_waitcnt lgkmcnt(4)\n\t"
    "v_mfma_f32_16x16x32_bf16 v[64:67], v[128:131], v[96:99], 0\n\t"
    "ds_read_b128 v[120:123], %[da] offset:4096\n\t"
    "s_add_u32 m0, %[l1], 0\n\t"
    "ds_write_b128 %[rw], v[80:83] offset:0\n\t"
    "v_mfma_f32_16x16x32_bf16 v[68:71], v[132:135], v[96:99], 0\n\t"
    "ds_read_b128 v[124:127], %[da] offset:6144\n\t"
    "global_load_lds_dwordx4 %[av0], s[36:37] sc1\n\t"
    "ds_write_b128 %[rw], v[84:87] offset:1024\n\t"
    "v_mfma_f32_16x16x32_bf16 v[72:75], v[128:131], v[100:103], 0\n\t"
    "s_add_u32 m0, %[l1], 32768\n\t"
    "ds_write_b128 %[rw], v[88:91] offset:2048\n\t"
    "v_mfma_f32_16x16x32_bf16 v[76:79], v[132:135], v[100:103], 0\n\t"
    "global_load_lds_dwordx4 %[av1], s[36:37] sc1\n\t"
    "ds_write_b128 %[rw], v[92:95] offset:3072\n\t"
    "s_waitcnt lgkmcnt(8)\n\t"
    "v_mfma_f32_16x16x32_bf16 v[64:67], v[136:139], v[104:107], v[64:67]\n\t"
    "ds_read_b128 v[96:99], %[ea] offset:8192\n\t"
    "s_add_u32 m0, %[l1], 4096\n\t"
    "v_mfma_f32_16x16x32_bf16 v[68:71], v[140:143], v[104:107], v[68:71]\n\t"
    "ds_read_b128 v[100:103], %[ea] offset:10240\n\t"
    "global_load_lds_dwordx4 %[av2], s[36:37] sc1\n\t"
    "v_mfma_f32_16x16x32_bf16 v[72:75], v[136:139], v[108:111], v[72:75]\n\t"
    "s_add_u32 m0, %[l1], 36864\n\t"
    "v_mfma_f32_16x16x32_bf16 v[76:79], v[140:143], v[108:111], v[76:79]\n\t"
    "global_load_lds_dwordx4 %[av3], s[36:37] sc1\n\t"
    "s_waitcnt lgkmcnt(2)\n\t"
    "v_mfma_f32_16x16x32_bf16 v[64:67], v[144:147], v[112:115], v[64:67]\n\t"
    "ds_read_b128 v[104:107], %[da] offset:8192\n\t"
    "s_add_u32 m0, %[l1], 8192\n\t"
    "v_mfma_f32_16x16x32_bf16 v[68:71], v[148:151], v[112:115], v[68:71]\n\t"
    "ds_read_b128 v[108:111], %[da] offset:10240\n\t"
    "global_load_lds_dwordx4 %[av4], s[36:37] sc1\n\t"
    "v_mfma_f32_16x16x32_bf16 v[72:75], v[144:147], v[116:119], v[72:75]\n\t"
    "s_add_u32 m0, %[l1], 40960\n\t"
    "v_mfma_f32_16x16x32_bf16 v[76:79], v[148:151], v[116:119], v[76:79]\n\t"
    "global_load_lds_dwordx4 %[av5], s[36:37] sc1\n\t"
    "s_waitcnt vmcnt(6)\n\t"
    "s_barrier\n\t"
    "v_mfma_f32_16x16x32_bf16 v[64:67], v[152:155], v[120:123], v[64:67]\n\t"
    "ds_read_b128 v[112:115], %[ea] offset:12288\n\t"
    "s_add_u32 m0, %[l1], 12288\n\t"
    "v_mfma_f32_16x16x32_bf16 v[68:71], v[156:159], v[120:123], v[68:71]\n\t"
    "ds_read_b128 v[116:119], %[ea] offset:14336\n\t"
    "global_load_lds_dwordx4 %[av6], s[36:37] sc1\n\t"
    "v_mfma_f32_16x16x32_bf16 v[72:75], v[152:155], v[124:127], v[72:75]\n\t"
    "s_add_u32 m0, %[l1], 45056\n\t"
    "v_mfma_f32_16x16x32_bf16 v[76:79], v[156:159], v[124:127], v[76:79]\n\t"
    "global_load_lds_dwordx4 %[av7], s[36:37] sc1\n\t"
    "s_waitcnt lgkmcnt(4)\n\t"
    "v_mfma_f32_16x16x32_bf16 v[64:67], v[160:163], v[96:99], v[64:67]\n\t"
    "ds_read_b128 v[120:123], %[da] offset:12288\n\t"
    "s_add_u32 m0, %[l0], 0\n\t"
    "v_mfma_f32_16x16x32_bf16 v[68:71], v[164:167], v[96:99], v[68:71]\n\t"
    "ds_read_b128 v[124:127], %[da] offset:14336\n\t"
    "global_load_lds_dwordx4 %[av0], s[38:39] sc1\n\t"
    "v_mfma_f32_16x16x32_bf16 v[72:75], v[160:163], v[100:103], v[72:75]\n\t"
    "s_add_u32 m0, %[l0], 32768\n\t"
    "v_mfma_f32_16x16x32_bf16 v[76:79], v[164:167], v[100:103], v[76:79]\n\t"
    "global_load_lds_dwordx4 %[av1], s[38:39] sc1\n\t"
    "s_waitcnt lgkmcnt(4)\n\t"
    "v_mfma_f32_16x16x32_bf16 v[64:67], v[168:171], v[104:107], v[64:67]\n\t"
    "ds_read_b128 v[96:99], %[ea] offset:16384\n\t"
    "s_add_u32 m0, %[l0], 4096\n\t"
    "v_mfma_f32_16x16x32_bf16 v[68:71], v[172:175], v[104:107], v[68:71]\n\t"
    "ds_read_b128 v[100:103], %[ea] offset:18432\n\t"
    "global_load_lds_dwordx4 %[av2], s[38:39] sc1\n\t"
    "v_mfma_f32_16x16x32_bf16 v[72:75], v[168:171], v[108:111], v[72:75]\n\t"
    "s_add_u32 m0, %[l0], 36864\n\t"
    "v_mfma_f32_16x16x32_bf16 v[76:79], v[172:175], v[108:111], v[76:79]\n\t"
    "global_load_lds_dwordx4 %[av3], s[38:39] sc1\n\t"
    "s_waitcnt lgkmcnt(4)\n\t"
    "v_mfma_f32_16x16x32_bf16 v[64:67], v[176:179], v[112:115], v[64:67]\n\t"
    "ds_read_b128 v[104:107], %[da] offset:16384\n\t"
    "s_add_u32 m0, %[l0], 8192\n\t"
    "v_mfma_f32_16x16x32_bf16 v[68:71], v[180:183], v[112:115], v[68:71]\n\t"
    "ds_read_b128 v[108:111], %[da] offset:18432\n\t"
    "global_load_lds_dwordx4 %[av4], s[38:39] sc1\n\t"
    "v_mfma_f32_16x16x32_bf16 v[72:75], v[176:179], v[116:119], v[72:75]\n\t"
    "s_add_u32 m0, %[l0], 40960\n\t"
    "v_mfma_f32_16x16x32_bf16 v[76:79], v[180:183], v[116:119], v[76:79]\n\t"
    "global_load_lds_dwordx4 %[av5], s[38:39] sc1\n\t"
    "s_waitcnt lgkmcnt(4)\n\t"
    "v_mfma_f32_16x16x32_bf16 v[64:67], v[184:187], v[120:123], v[64:67]\n\t"
    "ds_read_b128 v[112:115], %[ea] offset:20480\n\t"
    "s_add_u32 m0, %[l0], 12288\n\t"
    "v_mfma_f32_16x16x32_bf16 v[68:71], v[188:191], v[120:123], v[68:71]\n\t"
    "ds_read_b128 v[116:119], %[ea] offset:22528\n\t"
    "global_load_lds_dwordx4 %[av6], s[38:39] sc1\n\t"
    "v_mfma_f32_16x16x32_bf16 v[72:75], v[184:187], v[124:127], v[72:75]\n\t"
    "s_add_u32 m0, %[l0], 45056\n\t"
    "v_mfma_f32_16x16x32_bf16 v[76:79], v[188:191], v[124:127], v[76:79]\n\t"
    "global_load_lds_dwordx4 %[av7], s[38:39] sc1\n\t"
    "s_waitcnt lgkmcnt(4)\n\t"
    "v_mfma_f32_16x16x32_bf16 v[64:67], v[192:195], v[96:99], v[64:67]\n\t"
    "ds_read_b128 v[120:123], %[da] offset:20480\n\t"
    "v_mfma_f32_16x16x32_bf16 v[68:71], v[196:199], v[96:99], v[68:71]\n\t"
    "ds_read_b128 v[124:127], %[da] offset:22528\n\t"
    "v_mfma_f32_16x16x32_bf16 v[72:75], v[192:195], v[100:103], v[72:75]\n\t"
    "v_mfma_f32_16x16x32_bf16 v[76:79], v[196:199], v[100:103], v[76:79]\n\t"
    "s_waitcnt lgkmcnt(4)\n\t"
    "v_mfma_f32_16x16x32_bf16 v[64:67], v[200:203], v[104:107], v[64:67]\n\t"
    "ds_read_b128 v[96:99], %[ea] offset:24576\n\t"
    "v_mfma_f32_16x16x32_bf16 v[68:71], v[204:207], v[104:107], v[68:71]\n\t"
    "ds_read_b128 v[100:103], %[ea] offset:26624\n\t"
    "v_mfma_f32_16x16x32_bf16 v[72:75], v[200:203], v[108:111], v[72:75]\n\t"
    "v_mfma_f32_16x16x32_bf16 v[76:79], v[204:207], v[108:111], v[76:79]\n\t"
    "s_add_u32 s36, s36, %[ab]\n\t"
    "s_addc_u32 s37, s37, 0\n\t"
    "s_waitcnt lgkmcnt(4)\n\t"
    "v_mfma_f32_16x16x32_bf16 v[64:67], v[208:211], v[112:115], v[64:67]\n\t"
    "ds_read_b128 v[104:107], %[da] offset:24576\n\t"
    "v_mfma_f32_16x16x32_bf16 v[68:71], v[212:215], v[112:115], v[68:71]\n\t"
    "ds_read_b128 v[108:111], %[da] offset:26624\n\t"
    "v_mfma_f32_16x16x32_bf16 v[72:75], v[208:211], v[116:119], v[72:75]\n\t"
    "v_mfma_f32_16x16x32_bf16 v[76:79], v[212:215], v[116:119], v[76:79]\n\t"
    "s_xor_b32 %[l1], %[l1], 0x10000\n\t"
    "s_waitcnt lgkmcnt(4)\n\t"
    "v_mfma_f32_16x16x32_bf16 v[64:67], v[216:219], v[120:123], v[64:67]\n\t"
    "ds_read_b128 v[112:115], %[ea] offset:28672\n\t"
    "v_mfma_f32_16x16x32_bf16 v[68:71], v[220:223], v[120:123], v[68:71]\n\t"
    "ds_read_b128 v[116:119], %[ea] offset:30720\n\t"
    "v_mfma_f32_16x16x32_bf16 v[72:75], v[216:219], v[124:127], v[72:75]\n\t"
    "v_xor_b32 %[ea], 0x10000, %[ea]\n\t"
    "v_mfma_f32_16x16x32_bf16 v[76:79], v[220:223], v[124:127], v[76:79]\n\t"
    "s_waitcnt vmcnt(12)\n\t"
    "s_barrier\n\t"
    "s_waitcnt lgkmcnt(4)\n\t"
    "v_mfma_f32_16x16x32_bf16 v[64:67], v[224:227], v[96:99], v[64:67]\n\t"
    "ds_read_b32 v39, %[sa]\n\t"
    "ds_read_b128 v[120:123], %[da] offset:28672\n\t"
    "v_mfma_f32_16x16x32_bf16 v[68:71], v[228:231], v[96:99], v[68:71]\n\t"
    "ds_read_b128 v[124:127], %[da] offset:30720\n\t"
    "v_mfma_f32_16x16x32_bf16 v[72:75], v[224:227], v[100:103], v[72:75]\n\t"
    "v_xor_b32 %[da], 0x10000, %[da]\n\t"
    "v_mfma_f32_16x16x32_bf16 v[76:79], v[228:231], v[100:103], v[76:79]\n\t"
    "s_waitcnt lgkmcnt(5)\n\t"
    "v_mfma_f32_16x16x32_bf16 v[64:67], v[232:235], v[104:107], v[64:67]\n\t"
    "ds_read_b128 v[96:99], %[ea] offset:0\n\t"
    "v_mfma_f32_16x16x32_bf16 v[68:71], v[236:239], v[104:107], v[68:71]\n\t"
    "ds_read_b128 v[100:103], %[ea] offset:2048\n\t"
    "v_mfma_f32_16x16x32_bf16 v[72:75], v[232:235], v[108:111], v[72:75]\n\t"
    "v_mfma_f32_16x16x32_bf16 v[76:79], v[236:239], v[108:111], v[76:79]\n\t"
    "s_waitcnt lgkmcnt(4)\n\t"
    "v_mfma_f32_16x16x32_bf16 v[64:67], v[240:243], v[112:115], v[64:67]\n\t"
    "ds_read_b128 v[104:107], %[da] offset:0\n\t"
    "v_mad_u32_u24 v39, v39, %[mu], %[ad]\n\t"
    "v_mfma_f32_16x16x32_bf16 v[68:71], v[244:247], v[112:115], v[68:71]\n\t"
    "ds_read_b128 v[108:111], %[da] offset:2048\n\t"
    "v_cmp_ge_i32 vcc, %[lf], v39\n\t"
    "s_cmp_eq_u64 vcc, 0\n\t"
    "s_addc_u32 %[lf], %[lf], 0\n\t"
    "v_mfma_f32_16x16x32_bf16 v[72:75], v[240:243], v[116:119], v[72:75]\n\t"
    "v_mfma_f32_16x16x32_bf16 v[76:79], v[244:247], v[116:119], v[76:79]\n\t"
    "v_cmp_ge_i32 vcc, %[lf], v39\n\t"
    "s_cmp_eq_u64 vcc, 0\n\t"
    "s_addc_u32 %[lf], %[lf], 0\n\t"
    "s_waitcnt lgkmcnt(4)\n\t"
    "v_mfma_f32_16x16x32_bf16 v[80:83], v[248:251], v[120:123], v[64:67]\n\t"
    "ds_read_b128 v[112:115], %[ea] offset:4096\n\t"
    "s_min_i32 %[lf], %[lf], %[cp]\n\t"
    "s_add_u32 %[b], %[b], 1\n\t"
    "v_mfma_f32_16x16x32_bf16 v[84:87], v[252:255], v[120:123], v[68:71]\n\t"
    "ds_read_b128 v[116:119], %[ea] offset:6144\n\t"
    "v_mfma_f32_16x16x32_bf16 v[88:91], v[248:251], v[124:127], v[72:75]\n\t"
    "v_mfma_f32_16x16x32_bf16 v[92:95], v[252:255], v[124:127], v[76:79]\n\t"
    "s_add_u32 s38, s38, %[ab]\n\t"
    "s_addc_u32 s39, s39, 0\n\t"
    "s_xor_b32 %[l0], %[l0], 0x10000\n\t"
    "s_cmp_lt_i32 %[b], %[lf]\n\t"
    "s_cbranch_scc1 1b\n\t"
    "s_waitcnt lgkmcnt(0)\n\t"
    "s_nop 4"
    : "=&{v[80:95]}"(pd), [ea] "+&v"(ea), [da] "+&v"(da), [b] "+&s"(b), [lf] "+&s"(lf), "+{s[36:37]}"(p1), "+{s[38:39]}"(p0), [sa] "+&v"(sa), [l1] "+&s"(l1), [l0] "+&s"(l0)
    : [rw] "v"(rw), [ab] "s"(ab), [cp] "s"(cp), [mu] "s"(mu), [ad] "v"(ad), "{v[128:159]}"(W[0]), "{v[160:191]}"(W[1]), "{v[192:223]}"(W[2]), "{v[224:255]}"(W[3]), [av0] "v"(av[0]), [av1] "v"(av[1]), [av2] "v"(av[2]), [av3] "v"(av[3]), [av4] "v"(av[4]), [av5] "v"(av[5]), [av6] "v"(av[6]), [av7] "v"(av[7])
    : "v39", "v64", "v65", "v66", "v67", "v68", "v69", "v70", "v71", "v72", "v73", "v74", "v75", "v76", "v77", "v78", "v79", "v96", "v97", "v98", "v99", "v100", "v101", "v102", "v103", "v104", "v105", "v106", "v107", "v108", "v109", "v110", "v111", "v112", "v113", "v114", "v115", "v116", "v117", "v118", "v119", "v120", "v121", "v122", "v123", "v124", "v125", "v126", "v127", "vcc", "scc", "m0", "memory");
}
__device__ __forceinline__ void near_ld2(const u32x32 (&W)[4], u32x16& pd, unsigned ea, unsigned da, int& b, int& lf, int cp, unsigned sa, unsigned mu, unsigned ad,
    unsigned rw, unsigned ab, const char* p1, const char* p0, unsigned l1, unsigned l0, const unsigned (&av)[8]) {
  asm volatile("s_setprio 0\n\t"
    "s_waitcnt vmcnt(2)\n\t"
    "s_barrier\n\t"
    "ds_read_b128 v[96:99], %[ea] offset:0\n\t"
    "ds_read_b128 v[100:103], %[ea] offset:2048\n\t"
    "ds_read_b128 v[104:107], %[da] offset:0\n\t"
    "ds_read_b128 v[108:111], %[da] offset:2048\n\t"
    "ds_read_b128 v[112:115], %[ea] offset:4096\n\t"
    "ds_read_b128 v[116:119], %[ea] offset:6144\n\t"
    "s_branch 2f\n\t"
    "1:\n\t"
    "s_waitcnt vmcnt(2)\n\t"
    "s_nop 1\n\t"
    "s_barrier\n\t"
    "2:\n\t"
    "s_waitcnt lgkmcnt(4)\n\t"
    "v_mfma_f32_16x16x32_bf16 v[64:67], v[128:131], v[96:99], 0\n\t"
    "ds_read_b128 v[120:123], %[da] offset:4096\n\t"
    "s_add_u32 m0, %[l1], 0\n\t"
    "ds_write_b128 %[rw], v[80:83] offset:0\n\t"
    "v_mfma_f32_16x16x32_bf16 v[68:71], v[132:135], v[96:99], 0\n\t"
    "ds_read_b128 v[124:127], %[da] offset:6144\n\t"
    "global_load_lds_dwordx4 %[av0], s[36:37] sc1\n\t"
    "ds_write_b128 %[rw], v[84:87] offset:1024\n\t"
    "v_mfma_f32_16x16x32_bf16 v[72:75], v[128:131], v[100:103], 0\n\t"
    "ds_write_b128 %[rw], v[88:91] offset:2048\n\t"
    "v_mfma_f32_16x16x32_bf16 v[76:79], v[132:135], v[100:103], 0\n\t"
    "ds_write_b128 %[rw], v[92:95] offset:3072\n\t"
    "s_waitcnt lgkmcnt(8)\n\t"
    "v_mfma_f32_16x16x32_bf16 v[64:67], v[136:139], v[104:107], v[64:67]\n\t"
    "ds_read_b128 v[96:99], %[ea] offset:8192\n\t"
    "s_add_u32 m0, %[l1], 4096\n\t"
    "v_mfma_f32_16x16x32_bf16 v[68:71], v[140:143], v[104:107], v[68:71]\n\t"
    "ds_read_b128 v[100:103], %[ea] offset:10240\n\t"
    "global_load_lds_dwordx4 %[av2], s[36:37] sc1\n\t"
    "v_mfma_f32_16x16x32_bf16 v[72:75], v[136:139], v[108:111], v[72:75]\n\t"
    "v_mfma_f32_16x16x32_bf16 v[76:79], v[140:143], v[108:111], v[76:79]\n\t"
    "s_waitcnt lgkmcnt(2)\n\t"
    "v_mfma_f32_16x16x32_bf16 v[64:67], v[144:147], v[112:115], v[64:67]\n\t"
    "ds_read_b128 v[104:107], %[da] offset:8192\n\t"
    "s_add_u32 m0, %[l1], 8192\n\t"
    "v_mfma_f32_16x16x32_bf16 v[68:71], v[148:151], v[112:115], v[68:71]\n\t"
    "ds_read_b128 v[108:111], %[da] offset:10240\n\t"
    "global_load_lds_dwordx4 %[av4], s[36:37] sc1\n\t"
    "v_mfma_f32_16x16x32_bf16 v[72:75], v[144:147], v[116:119], v[72:75]\n\t"
    "v_mfma_f32_16x16x32_bf16 v[76:79], v[148:151], v[116:119], v[76:79]\n\t"
    "s_waitcnt vmcnt(3)\n\t"
    "s_barrier\n\t"
    "v_mfma_f32_16x16x32_bf16 v[64:67], v[152:155], v[120:123], v[64:67]\n\t"
    "ds_read_b128 v[112:115], %[ea] offset:12288\n\t"
    "s_add_u32 m0, %[l1], 12288\n\t"
    "v_mfma_f32_16x16x32_bf16 v[68:71], v[156:159], v[120:123], v[68:71]\n\t"
    "ds_read_b128 v[116:119], %[ea] offset:14336\n\t"
    "global_load_lds_dwordx4 %[av6], s[36:37] sc1\n\t"
    "v_mfma_f32_16x16x32_bf16 v[72:75], v[152:155], v[124:127], v[72:75]\n\t"
    "v_mfma_f32_16x16x32_bf16 v[76:79], v[156:159], v[124:127], v[76:79]\n\t"
    "s_waitcnt lgkmcnt(4)\n\t"
    "v_mfma_f32_16x16x32_bf16 v[64:67], v[160:163], v[96:99], v[64:67]\n\t"
    "ds_read_b128 v[120:123], %[da] offset:12288\n\t"
    "s_add_u32 m0, %[l0], 0\n\t"
    "v_mfma_f32_16x16x32_bf16 v[68:71], v[164:167], v[96:99], v[68:71]\n\t"
    "ds_read_b128 v[124:127], %[da] offset:14336\n\t"
    "global_load_lds_dwordx4 %[av0], s[38:39] sc1\n\t"
    "v_mfma_f32_16x16x32_bf16 v[72:75], v[160:163], v[100:103], v[72:75]\n\t"
    "v_mfma_f32_16x16x32_bf16 v[76:79], v[164:167], v[100:103], v[76:79]\n\t"
    "s_waitcnt lgkmcnt(4)\n\t"
    "v_mfma_f32_16x16x32_bf16 v[64:67], v[168:171], v[104:107], v[64:67]\n\t"
    "ds_read_b128 v[96:99], %[ea] offset:16384\n\t"
    "s_add_u32 m0, %[l0], 4096\n\t"
    "v_mfma_f32_16x16x32_bf16 v[68:71], v[172:175], v[104:107], v[68:71]\n\t"
    "ds_read_b128 v[100:103], %[ea] offset:18432\n\t"
    "global_load_lds_dwordx4 %[av2], s[38:39] sc1\n\t"
    "v_mfma_f32_16x16x32_bf16 v[72:75], v[168:171], v[108:111], v[72:75]\n\t"
    "v_mfma_f32_16x16x32_bf16 v[76:79], v[172:175], v[108:111], v[76:79]\n\t"
    "s_waitcnt lgkmcnt(4)\n\t"
    "v_mfma_f32_16x16x32_bf16 v[64:67], v[176:179], v[112:115], v[64:67]\n\t"
    "ds_read_b128 v[104:107], %[da] offset:16384\n\t"
    "s_add_u32 m0, %[l0], 8192\n\t"
    "v_mfma_f32_16x16x32_bf16 v[68:71], v[180:183], v[112:115], v[68:71]\n\t"
    "ds_read_b128 v[108:111], %[da] offset:18432\n\t"
    "global_load_lds_dwordx4 %[av4], s[38:39] sc1\n\t"
    "v_mfma_f32_16x16x32_bf16 v[72:75], v[176:179], v[116:119], v[72:75]\n\t"
    "v_mfma_f32_16x16x32_bf16 v[76:79], v[180:183], v[116:119], v[76:79]\n\t"
    "s_waitcnt lgkmcnt(4)\n\t"
    "v_mfma_f32_16x16x32_bf16 v[64:67], v[184:187], v[120:123], v[64:67]\n\t"
    "ds_read_b128 v[112:115], %[ea] offset:20480\n\t"
    "s_add_u32 m0, %[l0], 12288\n\t"
    "v_mfma_f32_16x16x32_bf16 v[68:71], v[188:191], v[120:123], v[68:71]\n\t"
    "ds_read_b128 v[116:119], %[ea] offset:22528\n\t"
    "global_load_lds_dwordx4 %[av6], s[38:39] sc1\n\t"
    "v_mfma_f32_16x16x32_bf16 v[72:75], v[184:187], v[124:127], v[72:75]\n\t"
    "v_mfma_f32_16x16x32_bf16 v[76:79], v[188:191], v[124:127], v[76:79]\n\t"
    "s_waitcnt lgkmcnt(4)\n\t"
    "v_mfma_f32_16x16x32_bf16 v[64:67], v[192:195], v[96:99], v[64:67]\n\t"
    "ds_read_b128 v[120:123], %[da] offset:20480\n\t"
    "v_mfma_f32_16x16x32_bf16 v[68:71], v[196:199], v[96:99], v[68:71]\n\t"
    "ds_read_b128 v[124:127], %[da] offset:22528\n\t"
    "v_mfma_f32_16x16x32_bf16 v[72:75], v[192:195], v[100:103], v[72:75]\n\t"
    "v_mfma_f32_16x16x32_bf16 v[76:79], v[196:199], v[100:103], v[76:79]\n\t"
    "s_waitcnt lgkmcnt(4)\n\t"
    "v_mfma_f32_16x16x32_bf16 v[64:67], v[200:203], v[104:107], v[64:67]\n\t"
    "ds_read_b128 v[96:99], %[ea] offset:24576\n\t"
    "v_mfma_f32_16x16x32_bf16 v[68:71], v[204:207], v[104:107], v[68:71]\n\t"
    "ds_read_b128 v[100:103], %[ea] offset:26624\n\t"
    "v_mfma_f32_16x16x32_bf16 v[72:75], v[200:203], v[108:111], v[72:75]\n\t"
    "v_mfma_f32_16x16x32_bf16 v[76:79], v[204:207], v[108:111], v[76:79]\n\t"
    "s_add_u32 s36, s36, %[ab]\n\t"
    "s_addc_u32 s37, s37, 0\n\t"
    "s_waitcnt lgkmcnt(4)\n\t"
    "v_mfma_f32_16x16x32_bf16 v[64:67], v[208:211], v[112:115], v[64:67]\n\t"
    "ds_read_b128 v[104:107], %[da] offset:24576\n\t"
    "v_mfma_f32_16x16x32_bf16 v[68:71], v[212:215], v[112:115], v[68:71]\n\t"
    "ds_read_b128 v[108:111], %[da] offset:26624\n\t"
    "v_mfma_f32_16x16x32_bf16 v[72:75], v[208:211], v[116:119], v[72:75]\n\t"
    "v_mfma_f32_16x16x32_bf16 v[76:79], v[212:215], v[116:119], v[76:79]\n\t"
    "s_xor_b32 %[l1], %[l1], 0x10000\n\t"
    "s_waitcnt lgkmcnt(4)\n\t"
    "v_mfma_f32_16x16x32_bf16 v[64:67], v[216:219], v[120:123], v[64:67]\n\t"
    "ds_read_b128 v[112:115], %[ea] offset:28672\n\t"
    "v_mfma_f32_16x16x32_bf16 v[68:71], v[220:223], v[120:123], v[68:71]\n\t"
    "ds_read_b128 v[116:119], %[ea] offset:30720\n\t"
    "v_mfma_f32_16x16x32_bf16 v[72:75], v[216:219], v[124:127], v[72:75]\n\t"
    "v_xor_b32 %[ea], 0x10000, %[ea]\n\t"
    "v_mfma_f32_16x16x32_bf16 v[76:79], v[220:223], v[124:127], v[76:79]\n\t"
    "s_waitcnt vmcnt(6)\n\t"
    "s_barrier\n\t"
    "s_waitcnt lgkmcnt(4)\n\t"
    "v_mfma_f32_16x16x32_bf16 v[64:67], v[224:227], v[96:99], v[64:67]\n\t"
    "ds_read_b32 v39, %[sa]\n\t"
    "ds_read_b128 v[120:123], %[da] offset:28672\n\t"
    "v_mfma_f32_16x16x32_bf16 v[68:71], v[228:231], v[96:99], v[68:71]\n\t"
    "ds_read_b128 v[124:127], %[da] offset:30720\n\t"
    "v_mfma_f32_16x16x32_bf16 v[72:75], v[224:227], v[100:103], v[72:75]\n\t"
    "v_xor_b32 %[da], 0x10000, %[da]\n\t"
    "v_mfma_f32_16x16x32_bf16 v[76:79], v[228:231], v[100:103], v[76:79]\n\t"
    "s_waitcnt lgkmcnt(5)\n\t"
    "v_mfma_f32_16x16x32_bf16 v[64:67], v[232:235], v[104:107], v[64:67]\n\t"
    "ds_read_b128 v[96:99], %[ea] offset:0\n\t"
    "v_mfma_f32_16x16x32_bf16 v[68:71], v[236:239], v[104:107], v[68:71]\n\t"
    "ds_read_b128 v[100:103], %[ea] offset:2048\n\t"
    "v_mfma_f32_16x16x32_bf16 v[72:75], v[232:235], v[108:111], v[72:75]\n\t"
    "v_mfma_f32_16x16x32_bf16 v[76:79], v[236:239], v[108:111], v[76:79]\n\t"
    "s_waitcnt lgkmcnt(4)\n\t"
    "v_mfma_f32_16x16x32_bf16 v[64:67], v[240:243], v[112:115], v[64:67]\n\t"
    "ds_read_b128 v[104:107], %[da] offset:0\n\t"
    "v_mad_u32_u24 v39, v39, %[mu], %[ad]\n\t"
    "v_mfma_f32_16x16x32_bf16 v[68:71], v[244:247], v[112:115], v[68:71]\n\t"
    "ds_read_b128 v[108:111], %[da] offset:2048\n\t"
    "v_cmp_ge_i32 vcc, %[lf], v39\n\t"
    "s_cmp_eq_u64 vcc, 0\n\t"
    "s_addc_u32 %[lf], %[lf], 0\n\t"
    "v_mfma_f32_16x16x32_bf16 v[72:75], v[240:243], v[116:119], v[72:75]\n\t"
    "v_mfma_f32_16x16x32_bf16 v[76:79], v[244:247], v[116:119], v[76:79]\n\t"
    "v_cmp_ge_i32 vcc, %[lf], v39\n\t"
    "s_cmp_eq_u64 vcc, 0\n\t"
    "s_addc_u32 %[lf], %[lf], 0\n\t"
    "s_waitcnt lgkmcnt(4)\n\t"
    "v_mfma_f32_16x16x32_bf16 v[80:83], v[248:251], v[120:123], v[64:67]\n\t"
    "ds_read_b128 v[112:115], %[ea] offset:4096\n\t"
    "s_min_i32 %[lf], %[lf], %[cp]\n\t"
    "s_add_u32 %[b], %[b], 1\n\t"
    "v_mfma_f32_16x16x32_bf16 v[84:87], v[252:255], v[120:123], v[68:71]\n\t"
    "ds_read_b128 v[116:119], %[ea] offset:6144\n\t"
    "v_mfma_f32_16x16x32_bf16 v[88:91], v[248:251], v[124:127], v[72:75]\n\t"
    "v_mfma_f32_16x16x32_bf16 v[92:95], v[252:255], v[124:127], v[76:79]\n\t"
    "s_add_u32 s38, s38, %[ab]\n\t"
    "s_addc_u32 s39, s39, 0\n\t"
    "s_xor_b32 %[l0], %[l0], 0x10000\n\t"
    "s_cmp_lt_i32 %[b], %[lf]\n\t"
    "s_cbranch_scc1 1b\n\t"
    "s_waitcnt lgkmcnt(0)\n\t"
    "s_nop 4"
    : "=&{v[80:95]}"(pd), [ea] "+&v"(ea), [da] "+&v"(da), [b] "+&s"(b), [lf] "+&s"(lf), "+{s[36:37]}"(p1), "+{s[38:39]}"(p0), [sa] "+&v"(sa), [l1] "+&s"(l1), [l0] "+&s"(l0)
    : [rw] "v"(rw), [ab] "s"(ab), [cp] "s"(cp), [mu] "s"(mu), [ad] "v"(ad), "{v[128:159]}"(W[0]), "{v[160:191]}"(W[1]), "{v[192:223]}"(W[2]), "{v[224:255]}"(W[3]), [av0] "v"(av[0]), [av1] "v"(av[1]), [av2] "v"(av[2]), [av3] "v"(av[3]), [av4] "v"(av[4]), [av5] "v"(av[5]), [av6] "v"(av[6]), [av7] "v"(av[7])
    : "v39", "v64", "v65", "v66", "v67", "v68", "v69", "v70", "v71", "v72", "v73", "v74", "v75", "v76", "v77", "v78", "v79", "v96", "v97", "v98", "v99", "v100", "v101", "v102", "v103", "v104", "v105", "v106", "v107", "v108", "v109", "v110", "v111", "v112", "v113", "v114", "v115", "v116", "v117", "v118", "v119", "v120", "v121", "v122", "v123", "v124", "v125", "v126", "v127", "vcc", "scc", "m0", "memory");
}
__device__ __forceinline__ void near_st0(const u32x32 (&W)[4], u32x16& pd, unsigned ea, unsigned da, int& b, int& lf, int cp, unsigned sa, unsigned mu, unsigned ad,
    unsigned rw, unsigned ab, const char* p1, const char* p0, unsigned co0, unsigned co1, int po, int pf, unsigned w0, unsigned sl, unsigned dv, const char* db, const char* pg) {
  unsigned st;
  asm volatile("s_waitcnt vmcnt(0)\n\t"
    "s_barrier\n\t"
    "ds_read_b128 v[96:99], %[ea] offset:0\n\t"
    "ds_read_b128 v[100:103], %[ea] offset:2048\n\t"
    "ds_read_b128 v[104:107], %[da] offset:0\n\t"
    "ds_read_b128 v[108:111], %[da] offset:2048\n\t"
    "ds_read_b128 v[112:115], %[ea] offset:4096\n\t"
    "ds_read_b128 v[116:119], %[ea] offset:6144\n\t"
    "s_branch 2f\n\t"
    "1:\n\t"
    "s_waitcnt vmcnt(0)\n\t"
    "s_barrier\n\t"
    "2:\n\t"
    "s_waitcnt lgkmcnt(4)\n\t"
    "v_mfma_f32_16x16x32_bf16 v[64:67], v[128:131], v[96:99], 0\n\t"
    "ds_read_b128 v[120:123], %[da] offset:4096\n\t"
    "v_mfma_f32_16x16x32_bf16 v[68:71], v[132:135], v[96:99], 0\n\t"
    "ds_read_b128 v[124:127], %[da] offset:6144\n\t"
    "v_mfma_f32_16x16x32_bf16 v[72:75], v[128:131], v[100:103], 0\n\t"
    "v_mfma_f32_16x16x32_bf16 v[76:79], v[132:135], v[100:103], 0\n\t"
    "s_cmp_eq_u32 %[w0], 0\n\t"
    "s_cbranch_scc1 5f\n\t"
    "6:\n\t"
    "s_waitcnt lgkmcnt(4)\n\t"
    "v_mfma_f32_16x16x32_bf16 v[64:67], v[136:139], v[104:107], v[64:67]\n\t"
    "ds_read_b128 v[96:99], %[ea] offset:8192\n\t"
    "v_mfma_f32_16x16x32_bf16 v[68:71], v[140:143], v[104:107], v[68:71]\n\t"
    "ds_read_b128 v[100:103], %[ea] offset:10240\n\t"
    "v_mfma_f32_16x16x32_bf16 v[72:75], v[136:139], v[108:111], v[72:75]\n\t"
    "v_mfma_f32_16x16x32_bf16 v[76:79], v[140:143], v[108:111], v[76:79]\n\t"
    "s_waitcnt lgkmcnt(4)\n\t"
    "v_mfma_f32_16x16x32_bf16 v[64:67], v[144:147], v[112:115], v[64:67]\n\t"
    "ds_read_b128 v[104:107], %[da] offset:8192\n\t"
    "v_mfma_f32_16x16x32_bf16 v[68:71], v[148:151], v[112:115], v[68:71]\n\t"
    "ds_read_b128 v[108:111], %[da] offset:10240\n\t"
    "v_mfma_f32_16x16x32_bf16 v[72:75], v[144:147], v[116:119], v[72:75]\n\t"
    "v_mfma_f32_16x16x32_bf16 v[76:79], v[148:151], v[116:119], v[76:79]\n\t"
    "s_barrier\n\t"
    "s_waitcnt lgkmcnt(4)\n\t"
    "v_mfma_f32_16x16x32_bf16 v[64:67], v[152:155], v[120:123], v[64:67]\n\t"
    "ds_read_b128 v[112:115], %[ea] offset:12288\n\t"
    "v_mfma_f32_16x16x32_bf16 v[68:71], v[156:159], v[120:123], v[68:71]\n\t"
    "ds_read_b128 v[116:119], %[ea] offset:14336\n\t"
    "v_mfma_f32_16x16x32_bf16 v[72:75], v[152:155], v[124:127], v[72:75]\n\t"
    "ds_read_b128 v[48:51], %[rw] offset:0\n\t"
    "v_mfma_f32_16x16x32_bf16 v[76:79], v[156:159], v[124:127], v[76:79]\n\t"
    "ds_read_b128 v[52:55], %[rw] offset:1024\n\t"
    "s_waitcnt lgkmcnt(6)\n\t"
    "v_mfma_f32_16x16x32_bf16 v[64:67], v[160:163], v[96:99], v[64:67]\n\t"
    "ds_read_b128 v[120:123], %[da] offset:12288\n\t"
    "v_mfma_f32_16x16x32_bf16 v[68:71], v[164:167], v[96:99], v[68:71]\n\t"
    "ds_read_b128 v[124:127], %[da] offset:14336\n\t"
    "v_mfma_f32_16x16x32_bf16 v[72:75], v[160:163], v[100:103], v[72:75]\n\t"
    "ds_read_b128 v[56:59], %[rw] offset:2048\n\t"
    "v_mfma_f32_16x16x32_bf16 v[76:79], v[164:167], v[100:103], v[76:79]\n\t"
    "ds_read_b128 v[60:63], %[rw] offset:3072\n\t"
    "s_waitcnt lgkmcnt(0)\n\t"
    "v_mfma_f32_16x16x32_bf16 v[64:67], v[168:171], v[104:107], v[64:67]\n\t"
    "ds_read_b128 v[96:99], %[ea] offset:16384\n\t"
    "v_add_f32 v48, v48, v80\n\t"
    "v_add_f32 v49, v49, v81\n\t"
    "v_mfma_f32_16x16x32_bf16 v[68:71], v[172:175], v[104:107], v[68:71]\n\t"
    "ds_read_b128 v[100:103], %[ea] offset:18432\n\t"
    "v_add_f32 v50, v50, v82\n\t"
    "v_mfma_f32_16x16x32_bf16 v[72:75], v[168:171], v[108:111], v[72:75]\n\t"
    "v_add_f32 v51, v51, v83\n\t"
    "v_mfma_f32_16x16x32_bf16 v[76:79], v[172:175], v[108:111], v[76:79]\n\t"
    "v_add_f32 v52, v52, v84\n\t"
    "v_add_f32 v53, v53, v85\n\t"
    "v_mfma_f32_16x16x32_bf16 v[64:67], v[176:179], v[112:115], v[64:67]\n\t"
    "ds_read_b128 v[104:107], %[da] offset:16384\n\t"
    "v_add_f32 v54, v54, v86\n\t"
    "v_mfma_f32_16x16x32_bf16 v[68:71], v[180:183], v[112:115], v[68:71]\n\t"
    "ds_read_b128 v[108:111], %[da] offset:18432\n\t"
    "v_add_f32 v55, v55, v87\n\t"
    "v_mfma_f32_16x16x32_bf16 v[72:75], v[176:179], v[116:119], v[72:75]\n\t"
    "v_add_f32 v56, v56, v88\n\t"
    "v_add_f32 v57, v57, v89\n\t"
    "v_mfma_f32_16x16x32_bf16 v[76:79], v[180:183], v[116:119], v[76:79]\n\t"
    "v_add_f32 v58, v58, v90\n\t"
    "v_mfma_f32_16x16x32_bf16 v[64:67], v[184:187], v[120:123], v[64:67]\n\t"
    "ds_read_b128 v[112:115], %[ea] offset:20480\n\t"
    "v_add_f32 v59, v59, v91\n\t"
    "v_mfma_f32_16x16x32_bf16 v[68:71], v[188:191], v[120:123], v[68:71]\n\t"
    "ds_read_b128 v[116:119], %[ea] offset:22528\n\t"
    "v_add_f32 v60, v60, v92\n\t"
    "v_mfma_f32_16x16x32_bf16 v[72:75], v[184:187], v[124:127], v[72:75]\n\t"
    "v_add_f32 v61, v61, v93\n\t"
    "v_add_f32 v62, v62, v94\n\t"
    "v_mfma_f32_16x16x32_bf16 v[76:79], v[188:191], v[124:127], v[76:79]\n\t"
    "v_add_f32 v63, v63, v95\n\t"
    "s_waitcnt lgkmcnt(4)\n\t"
    "v_mfma_f32_16x16x32_bf16 v[64:67], v[192:195], v[96:99], v[64:67]\n\t"
    "ds_read_b128 v[120:123], %[da] offset:20480\n\t"
    "v_cvt_pk_bf16_f32 v40, v48, v49\n\t"
    "v_mfma_f32_16x16x32_bf16 v[68:71], v[196:199], v[96:99], v[68:71]\n\t"
    "ds_read_b128 v[124:127], %[da] offset:22528\n\t"
    "v_cvt_pk_bf16_f32 v41, v50, v51\n\t"
    "v_cvt_pk_bf16_f32 v42, v52, v53\n\t"
    "v_mfma_f32_16x16x32_bf16 v[72:75], v[192:195], v[100:103], v[72:75]\n\t"
    "v_cvt_pk_bf16_f32 v43, v54, v55\n\t"
    "v_mfma_f32_16x16x32_bf16 v[76:79], v[196:199], v[100:103], v[76:79]\n\t"
    "global_store_dwordx4 %[co0], v[40:43], s[36:37] sc1\n\t"
    "s_waitcnt lgkmcnt(4)\n\t"
    "v_mfma_f32_16x16x32_bf16 v[64:67], v[200:203], v[104:107], v[64:67]\n\t"
    "ds_read_b128 v[96:99], %[ea] offset:24576\n\t"
    "v_cvt_pk_bf16_f32 v44, v56, v57\n\t"
    "v_cvt_pk_bf16_f32 v45, v58, v59\n\t"
    "v_mfma_f32_16x16x32_bf16 v[68:71], v[204:207], v[104:107], v[68:71]\n\t"
    "ds_read_b128 v[100:103], %[ea] offset:26624\n\t"
    "v_cvt_pk_bf16_f32 v46, v60, v61\n\t"
    "v_mfma_f32_16x16x32_bf16 v[72:75], v[200:203], v[108:111], v[72:75]\n\t"
    "v_cvt_pk_bf16_f32 v47, v62, v63\n\t"
    "v_mfma_f32_16x16x32_bf16 v[76:79], v[204:207], v[108:111], v[76:79]\n\t"
    "global_store_dwordx4 %[co1], v[44:47], s[36:37] sc1\n\t"
    "s_waitcnt lgkmcnt(4)\n\t"
    "v_mfma_f32_16x16x32_bf16 v[64:67], v[208:211], v[112:115], v[64:67]\n\t"
    "ds_read_b128 v[104:107], %[da] offset:24576\n\t"
    "v_mfma_f32_16x16x32_bf16 v[68:71], v[212:215], v[112:115], v[68:71]\n\t"
    "ds_read_b128 v[108:111], %[da] offset:26624\n\t"
    "v_mfma_f32_16x16x32_bf16 v[72:75], v[208:211], v[116:119], v[72:75]\n\t"
    "v_mfma_f32_16x16x32_bf16 v[76:79], v[212:215], v[116:119], v[76:79]\n\t"
    "s_waitcnt lgkmcnt(4)\n\t"
    "v_mfma_f32_16x16x32_bf16 v[64:67], v[216:219], v[120:123], v[64:67]\n\t"
    "ds_read_b128 v[112:115], %[ea] offset:28672\n\t"
    "v_mfma_f32_16x16x32_bf16 v[68:71], v[220:223], v[120:123], v[68:71]\n\t"
    "ds_read_b128 v[116:119], %[ea] offset:30720\n\t"
    "v_mfma_f32_16x16x32_bf16 v[72:75], v[216:219], v[124:127], v[72:75]\n\t"
    "v_xor_b32 %[ea], 0x10000, %[ea]\n\t"
    "v_mfma_f32_16x16x32_bf16 v[76:79], v[220:223], v[124:127], v[76:79]\n\t"
    "s_waitcnt vmcnt(2)\n\t"
    "s_barrier\n\t"
    "s_waitcnt lgkmcnt(4)\n\t"
    "v_mfma_f32_16x16x32_bf16 v[64:67], v[224:227], v[96:99], v[64:67]\n\t"
    "ds_read_b32 v39, %[sa]\n\t"
    "ds_read_b128 v[120:123], %[da] offset:28672\n\t"
    "v_mfma_f32_16x16x32_bf16 v[68:71], v[228:231], v[96:99], v[68:71]\n\t"
    "ds_read_b128 v[124:127], %[da] offset:30720\n\t"
    "v_mfma_f32_16x16x32_bf16 v[72:75], v[224:227], v[100:103], v[72:75]\n\t"
    "v_xor_b32 %[da], 0x10000, %[da]\n\t"
    "v_mfma_f32_16x16x32_bf16 v[76:79], v[228:231], v[100:103], v[76:79]\n\t"
    "s_waitcnt lgkmcnt(5)\n\t"
    "v_mfma_f32_16x16x32_bf16 v[64:67], v[232:235], v[104:107], v[64:67]\n\t"
    "ds_read_b128 v[96:99], %[ea] offset:0\n\t"
    "v_mfma_f32_16x16x32_bf16 v[68:71], v[236:239], v[104:107], v[68:71]\n\t"
    "ds_read_b128 v[100:103], %[ea] offset:2048\n\t"
    "v_mfma_f32_16x16x32_bf16 v[72:75], v[232:235], v[108:111], v[72:75]\n\t"
    "v_mfma_f32_16x16x32_bf16 v[76:79], v[236:239], v[108:111], v[76:79]\n\t"
    "s_waitcnt lgkmcnt(4)\n\t"
    "v_mfma_f32_16x16x32_bf16 v[64:67], v[240:243], v[112:115], v[64:67]\n\t"
    "ds_read_b128 v[104:107], %[da] offset:0\n\t"
    "v_mad_u32_u24 v39, v39, %[mu], %[ad]\n\t"
    "v_mfma_f32_16x16x32_bf16 v[68:71], v[244:247], v[112:115], v[68:71]\n\t"
    "ds_read_b128 v[108:111], %[da] offset:2048\n\t"
    "v_cmp_ge_i32 vcc, %[lf], v39\n\t"
    "s_cmp_eq_u64 vcc, 0\n\t"
    "s_addc_u32 %[lf], %[lf], 0\n\t"
    "v_mfma_f32_16x16x32_bf16 v[72:75], v[240:243], v[116:119], v[72:75]\n\t"
    "v_mfma_f32_16x16x32_bf16 v[76:79], v[244:247], v[116:119], v[76:79]\n\t"
    "v_cmp_ge_i32 vcc, %[lf], v39\n\t"
    "s_cmp_eq_u64 vcc, 0\n\t"
    "s_addc_u32 %[lf], %[lf], 0\n\t"
    "s_waitcnt lgkmcnt(4)\n\t"
    "v_mfma_f32_16x16x32_bf16 v[80:83], v[248:251], v[120:123], v[64:67]\n\t"
    "ds_read_b128 v[112:115], %[ea] offset:4096\n\t"
    "s_min_i32 %[lf], %[lf], %[cp]\n\t"
    "s_add_u32 %[b], %[b], 1\n\t"
    "v_mfma_f32_16x16x32_bf16 v[84:87], v[252:255], v[120:123], v[68:71]\n\t"
    "ds_read_b128 v[116:119], %[ea] offset:6144\n\t"
    "v_mfma_f32_16x16x32_bf16 v[88:91], v[248:251], v[124:127], v[72:75]\n\t"
    "v_mfma_f32_16x16x32_bf16 v[92:95], v[252:255], v[124:127], v[76:79]\n\t"
    "s_mov_b64 s[36:37], s[38:39]\n\t"
    "s_add_u32 s38, s38, %[ab]\n\t"
    "s_addc_u32 s39, s39, 0\n\t"
    "s_cmp_lt_i32 %[b], %[lf]\n\t"
    "s_cbranch_scc1 1b\n\t"
    "s_waitcnt lgkmcnt(0)\n\t"
    "s_nop 4\n\t"
    "s_branch 9f\n\t"
    "5:\n\t"
    "s_add_u32 %[st], %[b], %[po]\n\t"
    "s_max_i32 %[st], %[st], %[p0]\n\t"
    "v_mov_b32 v39, %[st]\n\t"
    "v_mov_b32 v38, 0\n\t"
    "s_mov_b64 exec, 1\n\t"
    "global_store_dword v38, v39, %[pg] sc1\n\t"
    "s_mov_b64 exec, -1\n\t"
    "s_mov_b32 %[p0], %[p1]\n\t"
    "s_cmp_ge_i32 %[lf], %[cp]\n\t"
    "s_cbranch_scc1 6b\n\t"
    "s_mov_b32 m0, %[sl]\n\t"
    "s_nop 0\n\t"
    "global_load_lds_dword %[dv], %[db] sc1\n\t"
    "s_branch 6b\n\t"
    "9:"
    : "=&{v[80:95]}"(pd), [ea] "+&v"(ea), [da] "+&v"(da), [b] "+&s"(b), [lf] "+&s"(lf), "+{s[36:37]}"(p1), "+{s[38:39]}"(p0), [sa] "+&v"(sa), [st] "=&s"(st), [sl] "+&s"(sl), [p0] "+&s"(pf)
    : [rw] "v"(rw), [ab] "s"(ab), [cp] "s"(cp), [mu] "s"(mu), [ad] "v"(ad), "{v[128:159]}"(W[0]), "{v[160:191]}"(W[1]), "{v[192:223]}"(W[2]), "{v[224:255]}"(W[3]), [co0] "v"(co0), [w0] "s"(w0), [dv] "v"(dv), [db] "s"(db), [pg] "s"(pg), [po] "s"(po - 1), [p1] "s"(b + po), [co1] "v"(co1)
    : "v38", "v39", "v40", "v41", "v42", "v43", "v44", "v45", "v46", "v47", "v48", "v49", "v50", "v51", "v52", "v53", "v54", "v55", "v56", "v57", "v58", "v59", "v60", "v61", "v62", "v63", "v64", "v65", "v66", "v67", "v68", "v69", "v70", "v71", "v72", "v73", "v74", "v75", "v76", "v77", "v78", "v79", "v96", "v97", "v98", "v99", "v100", "v101", "v102", "v103", "v104", "v105", "v106", "v107", "v108", "v109", "v110", "v111", "v112", "v113", "v114", "v115", "v116", "v117", "v118", "v119", "v120", "v121", "v122", "v123", "v124", "v125", "v126", "v127", "vcc", "scc", "m0", "memory");
}
__device__ __forceinline__ void near_st1(const u32x32 (&W)[4], u32x16& pd, unsigned ea, unsigned da, int& b, int& lf, int cp, unsigned sa, unsigned mu, unsigned ad,
    unsigned rw, unsigned ab, const char* p1, const char* p0, unsigned co0, unsigned co1, int po, int pf, unsigned w0, unsigned sl, unsigned dv, const char* db, const char* pg) {
  unsigned st;
  asm volatile("s_waitcnt vmcnt(0)\n\t"
    "s_barrier\n\t"
    "ds_read_b128 v[96:99], %[ea] offset:0\n\t"
    "ds_read_b128 v[100:103], %[ea] offset:2048\n\t"
    "ds_read_b128 v[104:107], %[da] offset:0\n\t"
    "ds_read_b128 v[108:111], %[da] offset:2048\n\t"
    "ds_read_b128 v[112:115], %[ea] offset:4096\n\t"
    "ds_read_b128 v[116:119], %[ea] offset:6144\n\t"
    "s_branch 2f\n\t"
    "1:\n\t"
    "s_waitcnt vmcnt(0)\n\t"
    "s_barrier\n\t"
    "2:\n\t"
    "s_waitcnt lgkmcnt(4)\n\t"
    "v_mfma_f32_16x16x32_bf16 v[64:67], v[128:131], v[96:99], 0\n\t"
    "ds_read_b128 v[120:123], %[da] offset:4096\n\t"
    "v_mfma_f32_16x16x32_bf16 v[68:71], v[132:135], v[96:99], 0\n\t"
    "ds_read_b128 v[124:127], %[da] offset:6144\n\t"
    "v_mfma_f32_16x16x32_bf16 v[72:75], v[128:131], v[100:103], 0\n\t"
    "v_mfma_f32_16x16x32_bf16 v[76:79], v[132:135], v[100:103], 0\n\t"
    "s_cmp_eq_u32 %[w0], 0\n\t"
    "s_cbranch_scc1 5f\n\t"
    "6:\n\t"
    "s_waitcnt lgkmcnt(4)\n\t"
    "v_mfma_f32_16x16x32_bf16 v[64:67], v[136:139], v[104:107], v[64:67]\n\t"
    "ds_read_b128 v[96:99], %[ea] offset:8192\n\t"
    "v_mfma_f32_16x16x32_bf16 v[68:71], v[140:143], v[104:107], v[68:71]\n\t"
    "ds_read_b128 v[100:103], %[ea] offset:10240\n\t"
    "v_mfma_f32_16x16x32_bf16 v[72:75], v[136:139], v[108:111], v[72:75]\n\t"
    "v_mfma_f32_16x16x32_bf16 v[76:79], v[140:143], v[108:111], v[76:79]\n\t"
    "s_waitcnt lgkmcnt(4)\n\t"
    "v_mfma_f32_16x16x32_bf16 v[64:67], v[144:147], v[112:115], v[64:67]\n\t"
    "ds_read_b128 v[104:107], %[da] offset:8192\n\t"
    "v_mfma_f32_16x16x32_bf16 v[68:71], v[148:151], v[112:115], v[68:71]\n\t"
    "ds_read_b128 v[108:111], %[da] offset:10240\n\t"
    "v_mfma_f32_16x16x32_bf16 v[72:75], v[144:147], v[116:119], v[72:75]\n\t"
    "v_mfma_f32_16x16x32_bf16 v[76:79], v[148:151], v[116:119], v[76:79]\n\t"
    "s_barrier\n\t"
    "s_waitcnt lgkmcnt(4)\n\t"
    "v_mfma_f32_16x16x32_bf16 v[64:67], v[152:155], v[120:123], v[64:67]\n\t"
    "ds_read_b128 v[112:115], %[ea] offset:12288\n\t"
    "v_mfma_f32_16x16x32_bf16 v[68:71], v[156:159], v[120:123], v[68:71]\n\t"
    "ds_read_b128 v[116:119], %[ea] offset:14336\n\t"
    "v_mfma_f32_16x16x32_bf16 v[72:75], v[152:155], v[124:127], v[72:75]\n\t"
    "ds_read_b128 v[48:51], %[rw] offset:0\n\t"
    "v_mfma_f32_16x16x32_bf16 v[76:79], v[156:159], v[124:127], v[76:79]\n\t"
    "ds_read_b128 v[52:55], %[rw] offset:1024\n\t"
    "s_waitcnt lgkmcnt(6)\n\t"
    "v_mfma_f32_16x16x32_bf16 v[64:67], v[160:163], v[96:99], v[64:67]\n\t"
    "ds_read_b128 v[120:123], %[da] offset:12288\n\t"
    "v_mfma_f32_16x16x32_bf16 v[68:71], v[164:167], v[96:99], v[68:71]\n\t"
    "ds_read_b128 v[124:127], %[da] offset:14336\n\t"
    "v_mfma_f32_16x16x32_bf16 v[72:75], v[160:163], v[100:103], v[72:75]\n\t"
    "ds_read_b128 v[56:59], %[rw] offset:2048\n\t"
    "v_mfma_f32_16x16x32_bf16 v[76:79], v[164:167], v[100:103], v[76:79]\n\t"
    "ds_read_b128 v[60:63], %[rw] offset:3072\n\t"
    "s_waitcnt lgkmcnt(0)\n\t"
    "v_mfma_f32_16x16x32_bf16 v[64:67], v[168:171], v[104:107], v[64:67]\n\t"
    "ds_read_b128 v[96:99], %[ea] offset:16384\n\t"
    "v_add_f32 v48, v48, v80\n\t"
    "v_add_f32 v49, v49, v81\n\t"
    "v_add_f32 v50, v50, v82\n\t"
    "v_mfma_f32_16x16x32_bf16 v[68:71], v[172:175], v[104:107], v[68:71]\n\t"
    "ds_read_b128 v[100:103], %[ea] offset:18432\n\t"
    "v_add_f32 v51, v51, v83\n\t"
    "v_add_f32 v52, v52, v84\n\t"
    "v_add_f32 v53, v53, v85\n\t"
    "v_mfma_f32_16x16x32_bf16 v[72:75], v[168:171], v[108:111], v[72:75]\n\t"
    "v_add_f32 v54, v54, v86\n\t"
    "v_add_f32 v55, v55, v87\n\t"
    "v_add_f32 v56, v56, v88\n\t"
    "v_mfma_f32_16x16x32_bf16 v[76:79], v[172:175], v[108:111], v[76:79]\n\t"
    "v_add_f32 v57, v57, v89\n\t"
    "v_add_f32 v58, v58, v90\n\t"
    "v_add_f32 v59, v59, v91\n\t"
    "v_mfma_f32_16x16x32_bf16 v[64:67], v[176:179], v[112:115], v[64:67]\n\t"
    "ds_read_b128 v[104:107], %[da] offset:16384\n\t"
    "v_add_f32 v60, v60, v92\n\t"
    "v_add_f32 v61, v61, v93\n\t"
    "v_add_f32 v62, v62, v94\n\t"
    "v_mfma_f32_16x16x32_bf16 v[68:71], v[180:183], v[112:115], v[68:71]\n\t"
    "ds_read_b128 v[108:111], %[da] offset:18432\n\t"
    "v_add_f32 v63, v63, v95\n\t"
    "v_mul_f32 v40, %[nl], v48\n\t"
    "v_mul_f32 v41, %[nl], v49\n\t"
    "v_mfma_f32_16x16x32_bf16 v[72:75], v[176:179], v[116:119], v[72:75]\n\t"
    "v_mul_f32 v42, %[nl], v50\n\t"
    "v_mul_f32 v43, %[nl], v51\n\t"
    "v_mul_f32 v44, %[nl], v56\n\t"
    "v_mfma_f32_16x16x32_bf16 v[76:79], v[180:183], v[116:119], v[76:79]\n\t"
    "v_mul_f32 v45, %[nl], v57\n\t"
    "v_mul_f32 v46, %[nl], v58\n\t"
    "v_mul_f32 v47, %[nl], v59\n\t"
    "v_mfma_f32_16x16x32_bf16 v[64:67], v[184:187], v[120:123], v[64:67]\n\t"
    "ds_read_b128 v[112:115], %[ea] offset:20480\n\t"
    "v_exp_f32 v40, v40\n\t"
    "v_exp_f32 v41, v41\n\t"
    "v_exp_f32 v42, v42\n\t"
    "v_mfma_f32_16x16x32_bf16 v[68:71], v[188:191], v[120:123], v[68:71]\n\t"
    "ds_read_b128 v[116:119], %[ea] offset:22528\n\t"
    "v_exp_f32 v43, v43\n\t"
    "v_exp_f32 v44, v44\n\t"
    "v_exp_f32 v45, v45\n\t"
    "v_mfma_f32_16x16x32_bf16 v[72:75], v[184:187], v[124:127], v[72:75]\n\t"
    "v_exp_f32 v46, v46\n\t"
    "v_exp_f32 v47, v47\n\t"
    "v_add_f32 v40, 1.0, v40\n\t"
    "v_mfma_f32_16x16x32_bf16 v[76:79], v[188:191], v[124:127], v[76:79]\n\t"
    "v_add_f32 v41, 1.0, v41\n\t"
    "v_add_f32 v42, 1.0, v42\n\t"
    "v_add_f32 v43, 1.0, v43\n\t"
    "s_waitcnt lgkmcnt(4)\n\t"
    "v_mfma_f32_16x16x32_bf16 v[64:67], v[192:195], v[96:99], v[64:67]\n\t"
    "ds_read_b128 v[120:123], %[da] offset:20480\n\t"
    "v_add_f32 v44, 1.0, v44\n\t"
    "v_add_f32 v45, 1.0, v45\n\t"
    "v_add_f32 v46, 1.0, v46\n\t"
    "v_mfma_f32_16x16x32_bf16 v[68:71], v[196:199], v[96:99], v[68:71]\n\t"
    "ds_read_b128 v[124:127], %[da] offset:22528\n\t"
    "v_add_f32 v47, 1.0, v47\n\t"
    "v_rcp_f32 v40, v40\n\t"
    "v_rcp_f32 v41, v41\n\t"
    "v_mfma_f32_16x16x32_bf16 v[72:75], v[192:195], v[100:103], v[72:75]\n\t"
    "v_rcp_f32 v42, v42\n\t"
    "v_rcp_f32 v43, v43\n\t"
    "v_rcp_f32 v44, v44\n\t"
    "v_mfma_f32_16x16x32_bf16 v[76:79], v[196:199], v[100:103], v[76:79]\n\t"
    "v_rcp_f32 v45, v45\n\t"
    "v_rcp_f32 v46, v46\n\t"
    "v_rcp_f32 v47, v47\n\t"
    "s_waitcnt lgkmcnt(4)\n\t"
    "v_mfma_f32_16x16x32_bf16 v[64:67], v[200:203], v[104:107], v[64:67]\n\t"
    "ds_read_b128 v[96:99], %[ea] offset:24576\n\t"
    "v_mul_f32 v40, v48, v40\n\t"
    "v_mul_f32 v41, v49, v41\n\t"
    "v_mul_f32 v42, v50, v42\n\t"
    "v_mfma_f32_16x16x32_bf16 v[68:71], v[204:207], v[104:107], v[68:71]\n\t"
    "ds_read_b128 v[100:103], %[ea] offset:26624\n\t"
    "v_mul_f32 v43, v51, v43\n\t"
    "v_mul_f32 v44, v56, v44\n\t"
    "v_mul_f32 v45, v57, v45\n\t"
    "v_mfma_f32_16x16x32_bf16 v[72:75], v[200:203], v[108:111], v[72:75]\n\t"
    "v_mul_f32 v46, v58, v46\n\t"
    "v_mul_f32 v47, v59, v47\n\t"
    "v_mul_f32 v40, v40, v52\n\t"
    "v_mfma_f32_16x16x32_bf16 v[76:79], v[204:207], v[108:111], v[76:79]\n\t"
    "v_mul_f32 v41, v41, v53\n\t"
    "v_mul_f32 v42, v42, v54\n\t"
    "v_mul_f32 v43, v43, v55\n\t"
    "s_waitcnt lgkmcnt(4)\n\t"
    "v_mfma_f32_16x16x32_bf16 v[64:67], v[208:211], v[112:115], v[64:67]\n\t"
    "ds_read_b128 v[104:107], %[da] offset:24576\n\t"
    "v_mul_f32 v44, v44, v60\n\t"
    "v_mul_f32 v45, v45, v61\n\t"
    "v_mul_f32 v46, v46, v62\n\t"
    "v_mfma_f32_16x16x32_bf16 v[68:71], v[212:215], v[112:115], v[68:71]\n\t"
    "ds_read_b128 v[108:111], %[da] offset:26624\n\t"
    "v_mul_f32 v47, v47, v63\n\t"
    "v_cvt_pk_bf16_f32 v48, v40, v41\n\t"
    "v_cvt_pk_bf16_f32 v49, v42, v43\n\t"
    "v_mfma_f32_16x16x32_bf16 v[72:75], v[208:211], v[116:119], v[72:75]\n\t"
    "v_cvt_pk_bf16_f32 v50, v44, v45\n\t"
    "v_cvt_pk_bf16_f32 v51, v46, v47\n\t"
    "s_nop 1\n\t"
    "v_mfma_f32_16x16x32_bf16 v[76:79], v[212:215], v[116:119], v[76:79]\n\t"
    "v_permlane16_swap_b32 v48, v50\n\t"
    "v_permlane16_swap_b32 v49, v51\n\t"
    "global_store_dwordx4 %[co0], v[48:51], s[36:37] sc1\n\t"
    "s_waitcnt lgkmcnt(4)\n\t"
    "v_mfma_f32_16x16x32_bf16 v[64:67], v[216:219], v[120:123], v[64:67]\n\t"
    "ds_read_b128 v[112:115], %[ea] offset:28672\n\t"
    "v_mfma_f32_16x16x32_bf16 v[68:71], v[220:223], v[120:123], v[68:71]\n\t"
    "ds_read_b128 v[116:119], %[ea] offset:30720\n\t"
    "v_mfma_f32_16x16x32_bf16 v[72:75], v[216:219], v[124:127], v[72:75]\n\t"
    "v_xor_b32 %[ea], 0x10000, %[ea]\n\t"
    "v_mfma_f32_16x16x32_bf16 v[76:79], v[220:223], v[124:127], v[76:79]\n\t"
    "s_waitcnt vmcnt(1)\n\t"
    "s_barrier\n\t"
    "s_waitcnt lgkmcnt(4)\n\t"
    "v_mfma_f32_16x16x32_bf16 v[64:67], v[224:227], v[96:99], v[64:67]\n\t"
    "ds_read_b32 v39, %[sa]\n\t"
    "ds_read_b128 v[120:123], %[da] offset:28672\n\t"
    "v_mfma_f32_16x16x32_bf16 v[68:71], v[228:231], v[96:99], v[68:71]\n\t"
    "ds_read_b128 v[124:127], %[da] offset:30720\n\t"
    "v_mfma_f32_16x16x32_bf16 v[72:75], v[224:227], v[100:103], v[72:75]\n\t"
    "v_xor_b32 %[da], 0x10000, %[da]\n\t"
    "v_mfma_f32_16x16x32_bf16 v[76:79], v[228:231], v[100:103], v[76:79]\n\t"
    "s_waitcnt lgkmcnt(5)\n\t"
    "v_mfma_f32_16x16x32_bf16 v[64:67], v[232:235], v[104:107], v[64:67]\n\t"
    "ds_read_b128 v[96:99], %[ea] offset:0\n\t"
    "v_mfma_f32_16x16x32_bf16 v[68:71], v[236:239], v[104:107], v[68:71]\n\t"
    "ds_read_b128 v[100:103], %[ea] offset:2048\n\t"
    "v_mfma_f32_16x16x32_bf16 v[72:75], v[232:235], v[108:111], v[72:75]\n\t"
    "v_mfma_f32_16x16x32_bf16 v[76:79], v[236:239], v[108:111], v[76:79]\n\t"
    "s_waitcnt lgkmcnt(4)\n\t"
    "v_mfma_f32_16x16x32_bf16 v[64:67], v[240:243], v[112:115], v[64:67]\n\t"
    "ds_read_b128 v[104:107], %[da] offset:0\n\t"
    "v_mad_u32_u24 v39, v39, %[mu], %[ad]\n\t"
    "v_mfma_f32_16x16x32_bf16 v[68:71], v[244:247], v[112:115], v[68:71]\n\t"
    "ds_read_b128 v[108:111], %[da] offset:2048\n\t"
    "v_cmp_ge_i32 vcc, %[lf], v39\n\t"
    "s_cmp_eq_u64 vcc, 0\n\t"
    "s_addc_u32 %[lf], %[lf], 0\n\t"
    "v_mfma_f32_16x16x32_bf16 v[72:75], v[240:243], v[116:119], v[72:75]\n\t"
    "v_mfma_f32_16x16x32_bf16 v[76:79], v[244:247], v[116:119], v[76:79]\n\t"
    "v_cmp_ge_i32 vcc, %[lf], v39\n\t"
    "s_cmp_eq_u64 vcc, 0\n\t"
    "s_addc_u32 %[lf], %[lf], 0\n\t"
    "s_waitcnt lgkmcnt(4)\n\t"
    "v_mfma_f32_16x16x32_bf16 v[80:83], v[248:251], v[120:123], v[64:67]\n\t"
    "ds_read_b128 v[112:115], %[ea] offset:4096\n\t"
    "s_min_i32 %[lf], %[lf], %[cp]\n\t"
    "s_add_u32 %[b], %[b], 1\n\t"
    "v_mfma_f32_16x16x32_bf16 v[84:87], v[252:255], v[120:123], v[68:71]\n\t"
    "ds_read_b128 v[116:119], %[ea] offset:6144\n\t"
    "v_mfma_f32_16x16x32_bf16 v[88:91], v[248:251], v[124:127], v[72:75]\n\t"
    "v_mfma_f32_16x16x32_bf16 v[92:95], v[252:255], v[124:127], v[76:79]\n\t"
    "s_mov_b64 s[36:37], s[38:39]\n\t"
    "s_add_u32 s38, s38, %[ab]\n\t"
    "s_addc_u32 s39, s39, 0\n\t"
    "s_cmp_lt_i32 %[b], %[lf]\n\t"
    "s_cbranch_scc1 1b\n\t"
    "s_waitcnt lgkmcnt(0)\n\t"
    "s_nop 4\n\t"
    "s_branch 9f\n\t"
    "5:\n\t"
    "s_add_u32 %[st], %[b], %[po]\n\t"
    "s_max_i32 %[st], %[st], %[p0]\n\t"
    "v_mov_b32 v39, %[st]\n\t"
    "v_mov_b32 v38, 0\n\t"
    "s_mov_b64 exec, 1\n\t"
    "global_store_dword v38, v39, %[pg] sc1\n\t"
    "s_mov_b64 exec, -1\n\t"
    "s_mov_b32 %[p0], %[p1]\n\t"
    "s_cmp_ge_i32 %[lf], %[cp]\n\t"
    "s_cbranch_scc1 6b\n\t"
    "s_mov_b32 m0, %[sl]\n\t"
    "s_nop 0\n\t"
    "global_load_lds_dword %[dv], %[db] sc1\n\t"
    "s_branch 6b\n\t"
    "9:"
    : "=&{v[80:95]}"(pd), [ea] "+&v"(ea), [da] "+&v"(da), [b] "+&s"(b), [lf] "+&s"(lf), "+{s[36:37]}"(p1), "+{s[38:39]}"(p0), [sa] "+&v"(sa), [st] "=&s"(st), [sl] "+&s"(sl), [p0] "+&s"(pf)
    : [rw] "v"(rw), [ab] "s"(ab), [cp] "s"(cp), [mu] "s"(mu), [ad] "v"(ad), "{v[128:159]}"(W[0]), "{v[160:191]}"(W[1]), "{v[192:223]}"(W[2]), "{v[224:255]}"(W[3]), [co0] "v"(co0), [w0] "s"(w0), [dv] "v"(dv), [db] "s"(db), [pg] "s"(pg), [po] "s"(po - 1), [p1] "s"(b + po), [nl] "s"(-LOG2E)
    : "v38", "v39", "v40", "v41", "v42", "v43", "v44", "v45", "v46", "v47", "v48", "v49", "v50", "v51", "v52", "v53", "v54", "v55", "v56", "v57", "v58", "v59", "v60", "v61", "v62", "v63", "v64", "v65", "v66", "v67", "v68", "v69", "v70", "v71", "v72", "v73", "v74", "v75", "v76", "v77", "v78", "v79", "v96", "v97", "v98", "v99", "v100", "v101", "v102", "v103", "v104", "v105", "v106", "v107", "v108", "v109", "v110", "v111", "v112", "v113", "v114", "v115", "v116", "v117", "v118", "v119", "v120", "v121", "v122", "v123", "v124", "v125", "v126", "v127", "vcc", "scc", "m0", "memory");
}
__device__ __forceinline__ void near_st2(const u32x32 (&W)[4], u32x16& pd, unsigned ea, unsigned da, int& b, int& lf, int cp, unsigned sa, unsigned mu, unsigned ad,
    unsigned rw, unsigned ab, const char* p1, const char* p0, unsigned co0, unsigned co1, int po, int pf, unsigned w0, unsigned sl, unsigned dv, const char* db, const char* pg) {
  unsigned st;
  asm volatile("s_waitcnt vmcnt(0)\n\t"
    "s_barrier\n\t"
    "ds_read_b128 v[96:99], %[ea] offset:0\n\t"
    "ds_read_b128 v[100:103], %[ea] offset:2048\n\t"
    "ds_read_b128 v[104:107], %[da] offset:0\n\t"
    "ds_read_b128 v[108:111], %[da] offset:2048\n\t"
    "ds_read_b128 v[112:115], %[ea] offset:4096\n\t"
    "ds_read_b128 v[116:119], %[ea] offset:6144\n\t"
    "s_branch 2f\n\t"
    "1:\n\t"
    "s_waitcnt vmcnt(0)\n\t"
    "s_barrier\n\t"
    "2:\n\t"
    "s_waitcnt lgkmcnt(4)\n\t"
    "v_mfma_f32_16x16x32_bf16 v[64:67], v[128:131], v[96:99], 0\n\t"
    "ds_read_b128 v[120:123], %[da] offset:4096\n\t"
    "v_mfma_f32_16x16x32_bf16 v[68:71], v[132:135], v[96:99], 0\n\t"
    "ds_read_b128 v[124:127], %[da] offset:6144\n\t"
    "v_mfma_f32_16x16x32_bf16 v[72:75], v[128:131], v[100:103], 0\n\t"
    "v_mfma_f32_16x16x32_bf16 v[76:79], v[132:135], v[100:103], 0\n\t"
    "s_cmp_eq_u32 %[w0], 0\n\t"
    "s_cbranch_scc1 5f\n\t"
    "6:\n\t"
    "s_waitcnt lgkmcnt(4)\n\t"
    "v_mfma_f32_16x16x32_bf16 v[64:67], v[136:139], v[104:107], v[64:67]\n\t"
    "ds_read_b128 v[96:99], %[ea] offset:8192\n\t"
    "v_mfma_f32_16x16x32_bf16 v[68:71], v[140:143], v[104:107], v[68:71]\n\t"
    "ds_read_b128 v[100:103], %[ea] offset:10240\n\t"
    "v_mfma_f32_16x16x32_bf16 v[72:75], v[136:139], v[108:111], v[72:75]\n\t"
    "v_mfma_f32_16x16x32_bf16 v[76:79], v[140:143], v[108:111], v[76:79]\n\t"
    "s_waitcnt lgkmcnt(4)\n\t"
    "v_mfma_f32_16x16x32_bf16 v[64:67], v[144:147], v[112:115], v[64:67]\n\t"
    "ds_read_b128 v[104:107], %[da] offset:8192\n\t"
    "v_mfma_f32_16x16x32_bf16 v[68:71], v[148:151], v[112:115], v[68:71]\n\t"
    "ds_read_b128 v[108:111], %[da] offset:10240\n\t"
    "v_mfma_f32_16x16x32_bf16 v[72:75], v[144:147], v[116:119], v[72:75]\n\t"
    "v_mfma_f32_16x16x32_bf16 v[76:79], v[148:151], v[116:119], v[76:79]\n\t"
    "s_barrier\n\t"
    "s_waitcnt lgkmcnt(4)\n\t"
    "v_mfma_f32_16x16x32_bf16 v[64:67], v[152:155], v[120:123], v[64:67]\n\t"
    "ds_read_b128 v[112:115], %[ea] offset:12288\n\t"
    "v_mfma_f32_16x16x32_bf16 v[68:71], v[156:159], v[120:123], v[68:71]\n\t"
    "ds_read_b128 v[116:119], %[ea] offset:14336\n\t"
    "v_mfma_f32_16x16x32_bf16 v[72:75], v[152:155], v[124:127], v[72:75]\n\t"
    "ds_read_b128 v[48:51], %[rw] offset:0\n\t"
    "v_mfma_f32_16x16x32_bf16 v[76:79], v[156:159], v[124:127], v[76:79]\n\t"
    "ds_read_b128 v[52:55], %[rw] offset:1024\n\t"
    "s_waitcnt lgkmcnt(6)\n\t"
    "v_mfma_f32_16x16x32_bf16 v[64:67], v[160:163], v[96:99], v[64:67]\n\t"
    "ds_read_b128 v[120:123], %[da] offset:12288\n\t"
    "v_mfma_f32_16x16x32_bf16 v[68:71], v[164:167], v[96:99], v[68:71]\n\t"
    "ds_read_b128 v[124:127], %[da] offset:14336\n\t"
    "v_mfma_f32_16x16x32_bf16 v[72:75], v[160:163], v[100:103], v[72:75]\n\t"
    "ds_read_b128 v[56:59], %[rw] offset:2048\n\t"
    "v_mfma_f32_16x16x32_bf16 v[76:79], v[164:167], v[100:103], v[76:79]\n\t"
    "ds_read_b128 v[60:63], %[rw] offset:3072\n\t"
    "s_waitcnt lgkmcnt(0)\n\t"
    "v_mfma_f32_16x16x32_bf16 v[64:67], v[168:171], v[104:107], v[64:67]\n\t"
    "ds_read_b128 v[96:99], %[ea] offset:16384\n\t"
    "v_cvt_pk_bf16_f32 v40, v80, v81\n\t"
    "v_mfma_f32_16x16x32_bf16 v[68:71], v[172:175], v[104:107], v[68:71]\n\t"
    "ds_read_b128 v[100:103], %[ea] offset:18432\n\t"
    "v_cvt_pk_bf16_f32 v41, v82, v83\n\t"
    "v_mfma_f32_16x16x32_bf16 v[72:75], v[168:171], v[108:111], v[72:75]\n\t"
    "v_cvt_pk_bf16_f32 v42, v84, v85\n\t"
    "v_mfma_f32_16x16x32_bf16 v[76:79], v[172:175], v[108:111], v[76:79]\n\t"
    "v_cvt_pk_bf16_f32 v43, v86, v87\n\t"
    "v_mfma_f32_16x16x32_bf16 v[64:67], v[176:179], v[112:115], v[64:67]\n\t"
    "ds_read_b128 v[104:107], %[da] offset:16384\n\t"
    "global_store_dwordx4 %[co0], v[40:43], s[36:37] sc1\n\t"
    "v_mfma_f32_16x16x32_bf16 v[68:71], v[180:183], v[112:115], v[68:71]\n\t"
    "ds_read_b128 v[108:111], %[da] offset:18432\n\t"
    "v_cvt_pk_bf16_f32 v44, v88, v89\n\t"
    "v_mfma_f32_16x16x32_bf16 v[72:75], v[176:179], v[116:119], v[72:75]\n\t"
    "v_cvt_pk_bf16_f32 v45, v90, v91\n\t"
    "v_mfma_f32_16x16x32_bf16 v[76:79], v[180:183], v[116:119], v[76:79]\n\t"
    "v_cvt_pk_bf16_f32 v46, v92, v93\n\t"
    "v_mfma_f32_16x16x32_bf16 v[64:67], v[184:187], v[120:123], v[64:67]\n\t"
    "ds_read_b128 v[112:115], %[ea] offset:20480\n\t"
    "v_cvt_pk_bf16_f32 v47, v94, v95\n\t"
    "v_mfma_f32_16x16x32_bf16 v[68:71], v[188:191], v[120:123], v[68:71]\n\t"
    "ds_read_b128 v[116:119], %[ea] offset:22528\n\t"
    "global_store_dwordx4 %[co1], v[44:47], s[36:37] sc1\n\t"
    "v_mfma_f32_16x16x32_bf16 v[72:75], v[184:187], v[124:127], v[72:75]\n\t"
    "v_cvt_pk_bf16_f32 v48, v48, v49\n\t"
    "v_mfma_f32_16x16x32_bf16 v[76:79], v[188:191], v[124:127], v[76:79]\n\t"
    "v_cvt_pk_bf16_f32 v49, v50, v51\n\t"
    "s_waitcnt lgkmcnt(4)\n\t"
    "v_mfma_f32_16x16x32_bf16 v[64:67], v[192:195], v[96:99], v[64:67]\n\t"
    "ds_read_b128 v[120:123], %[da] offset:20480\n\t"
    "v_cvt_pk_bf16_f32 v50, v52, v53\n\t"
    "v_mfma_f32_16x16x32_bf16 v[68:71], v[196:199], v[96:99], v[68:71]\n\t"
    "ds_read_b128 v[124:127], %[da] offset:22528\n\t"
    "v_cvt_pk_bf16_f32 v51, v54, v55\n\t"
    "v_mfma_f32_16x16x32_bf16 v[72:75], v[192:195], v[100:103], v[72:75]\n\t"
    "global_store_dwordx4 %[co0], v[48:51], s[36:37] offset:2048 sc1\n\t"
    "v_mfma_f32_16x16x32_bf16 v[76:79], v[196:199], v[100:103], v[76:79]\n\t"
    "v_cvt_pk_bf16_f32 v56, v56, v57\n\t"
    "s_waitcnt lgkmcnt(4)\n\t"
    "v_mfma_f32_16x16x32_bf16 v[64:67], v[200:203], v[104:107], v[64:67]\n\t"
    "ds_read_b128 v[96:99], %[ea] offset:24576\n\t"
    "v_cvt_pk_bf16_f32 v57, v58, v59\n\t"
    "v_mfma_f32_16x16x32_bf16 v[68:71], v[204:207], v[104:107], v[68:71]\n\t"
    "ds_read_b128 v[100:103], %[ea] offset:26624\n\t"
    "v_cvt_pk_bf16_f32 v58, v60, v61\n\t"
    "v_mfma_f32_16x16x32_bf16 v[72:75], v[200:203], v[108:111], v[72:75]\n\t"
    "v_cvt_pk_bf16_f32 v59, v62, v63\n\t"
    "v_mfma_f32_16x16x32_bf16 v[76:79], v[204:207], v[108:111], v[76:79]\n\t"
    "global_store_dwordx4 %[co1], v[56:59], s[36:37] offset:2048 sc1\n\t"
    "s_waitcnt lgkmcnt(4)\n\t"
    "v_mfma_f32_16x16x32_bf16 v[64:67], v[208:211], v[112:115], v[64:67]\n\t"
    "ds_read_b128 v[104:107], %[da] offset:24576\n\t"
    "v_mfma_f32_16x16x32_bf16 v[68:71], v[212:215], v[112:115], v[68:71]\n\t"
    "ds_read_b128 v[108:111], %[da] offset:26624\n\t"
    "v_mfma_f32_16x16x32_bf16 v[72:75], v[208:211], v[116:119], v[72:75]\n\t"
    "v_mfma_f32_16x16x32_bf16 v[76:79], v[212:215], v[116:119], v[76:79]\n\t"
    "s_waitcnt lgkmcnt(4)\n\t"
    "v_mfma_f32_16x16x32_bf16 v[64:67], v[216:219], v[120:123], v[64:67]\n\t"
    "ds_read_b128 v[112:115], %[ea] offset:28672\n\t"
    "v_mfma_f32_16x16x32_bf16 v[68:71], v[220:223], v[120:123], v[68:71]\n\t"
    "ds_read_b128 v[116:119], %[ea] offset:30720\n\t"
    "v_mfma_f32_16x16x32_bf16 v[72:75], v[216:219], v[124:127], v[72:75]\n\t"
    "v_xor_b32 %[ea], 0x10000, %[ea]\n\t"
    "v_mfma_f32_16x16x32_bf16 v[76:79], v[220:223], v[124:127], v[76:79]\n\t"
    "s_waitcnt vmcnt(4)\n\t"
    "s_barrier\n\t"
    "s_waitcnt lgkmcnt(4)\n\t"
    "v_mfma_f32_16x16x32_bf16 v[64:67], v[224:227], v[96:99], v[64:67]\n\t"
    "ds_read_b32 v39, %[sa]\n\t"
    "ds_read_b128 v[120:123], %[da] offset:28672\n\t"
    "v_mfma_f32_16x16x32_bf16 v[68:71], v[228:231], v[96:99], v[68:71]\n\t"
    "ds_read_b128 v[124:127], %[da] offset:30720\n\t"
    "v_mfma_f32_16x16x32_bf16 v[72:75], v[224:227], v[100:103], v[72:75]\n\t"
    "v_xor_b32 %[da], 0x10000, %[da]\n\t"
    "v_mfma_f32_16x16x32_bf16 v[76:79], v[228:231], v[100:103], v[76:79]\n\t"
    "s_waitcnt lgkmcnt(5)\n\t"
    "v_mfma_f32_16x16x32_bf16 v[64:67], v[232:235], v[104:107], v[64:67]\n\t"
    "ds_read_b128 v[96:99], %[ea] offset:0\n\t"
    "v_mfma_f32_16x16x32_bf16 v[68:71], v[236:239], v[104:107], v[68:71]\n\t"
    "ds_read_b128 v[100:103], %[ea] offset:2048\n\t"
    "v_mfma_f32_16x16x32_bf16 v[72:75], v[232:235], v[108:111], v[72:75]\n\t"
    "v_mfma_f32_16x16x32_bf16 v[76:79], v[236:239], v[108:111], v[76:79]\n\t"
    "s_waitcnt lgkmcnt(4)\n\t"
    "v_mfma_f32_16x16x32_bf16 v[64:67], v[240:243], v[112:115], v[64:67]\n\t"
    "ds_read_b128 v[104:107], %[da] offset:0\n\t"
    "v_mad_u32_u24 v39, v39, %[mu], %[ad]\n\t"
    "v_mfma_f32_16x16x32_bf16 v[68:71], v[244:247], v[112:115], v[68:71]\n\t"
    "ds_read_b128 v[108:111], %[da] offset:2048\n\t"
    "v_cmp_ge_i32 vcc, %[lf], v39\n\t"
    "s_cmp_eq_u64 vcc, 0\n\t"
    "s_addc_u32 %[lf], %[lf], 0\n\t"
    "v_mfma_f32_16x16x32_bf16 v[72:75], v[240:243], v[116:119], v[72:75]\n\t"
    "v_mfma_f32_16x16x32_bf16 v[76:79], v[244:247], v[116:119], v[76:79]\n\t"
    "v_cmp_ge_i32 vcc, %[lf], v39\n\t"
    "s_cmp_eq_u64 vcc, 0\n\t"
    "s_addc_u32 %[lf], %[lf], 0\n\t"
    "s_waitcnt lgkmcnt(4)\n\t"
    "v_mfma_f32_16x16x32_bf16 v[80:83], v[248:251], v[120:123], v[64:67]\n\t"
    "ds_read_b128 v[112:115], %[ea] offset:4096\n\t"
    "s_min_i32 %[lf], %[lf], %[cp]\n\t"
    "s_add_u32 %[b], %[b], 1\n\t"
    "v_mfma_f32_16x16x32_bf16 v[84:87], v[252:255], v[120:123], v[68:71]\n\t"
    "ds_read_b128 v[116:119], %[ea] offset:6144\n\t"
    "v_mfma_f32_16x16x32_bf16 v[88:91], v[248:251], v[124:127], v[72:75]\n\t"
    "v_mfma_f32_16x16x32_bf16 v[92:95], v[252:255], v[124:127], v[76:79]\n\t"
    "s_mov_b64 s[36:37], s[38:39]\n\t"
    "s_add_u32 s38, s38, %[ab]\n\t"
    "s_addc_u32 s39, s39, 0\n\t"
    "s_cmp_lt_i32 %[b], %[lf]\n\t"
    "s_cbranch_scc1 1b\n\t"
    "s_waitcnt lgkmcnt(0)\n\t"
    "s_nop 4\n\t"
    "s_branch 9f\n\t"
    "5:\n\t"
    "s_add_u32 %[st], %[b], %[po]\n\t"
    "s_max_i32 %[st], %[st], %[p0]\n\t"
    "v_mov_b32 v39, %[st]\n\t"
    "v_mov_b32 v38, 0\n\t"
    "s_mov_b64 exec, 1\n\t"
    "global_store_dword v38, v39, %[pg] sc1\n\t"
    "s_mov_b64 exec, -1\n\t"
    "s_mov_b32 %[p0], %[p1]\n\t"
    "s_cmp_ge_i32 %[lf], %[cp]\n\t"
    "s_cbranch_scc1 6b\n\t"
    "s_mov_b32 m0, %[sl]\n\t"
    "s_nop 0\n\t"
    "global_load_lds_dword %[dv], %[db] sc1\n\t"
    "s_branch 6b\n\t"
    "9:"
    : "=&{v[80:95]}"(pd), [ea] "+&v"(ea), [da] "+&v"(da), [b] "+&s"(b), [lf] "+&s"(lf), "+{s[36:37]}"(p1), "+{s[38:39]}"(p0), [sa] "+&v"(sa), [st] "=&s"(st), [sl] "+&s"(sl), [p0] "+&s"(pf)
    : [rw] "v"(rw), [ab] "s"(ab), [cp] "s"(cp), [mu] "s"(mu), [ad] "v"(ad), "{v[128:159]}"(W[0]), "{v[160:191]}"(W[1]), "{v[192:223]}"(W[2]), "{v[224:255]}"(W[3]), [co0] "v"(co0), [w0] "s"(w0), [dv] "v"(dv), [db] "s"(db), [pg] "s"(pg), [po] "s"(po - 1), [p1] "s"(b + po), [co1] "v"(co1)
    : "v38", "v39", "v40", "v41", "v42", "v43", "v44", "v45", "v46", "v47", "v48", "v49", "v50", "v51", "v52", "v53", "v54", "v55", "v56", "v57", "v58", "v59", "v60", "v61", "v62", "v63", "v64", "v65", "v66", "v67", "v68", "v69", "v70", "v71", "v72", "v73", "v74", "v75", "v76", "v77", "v78", "v79", "v96", "v97", "v98", "v99", "v100", "v101", "v102", "v103", "v104", "v105", "v106", "v107", "v108", "v109", "v110", "v111", "v112", "v113", "v114", "v115", "v116", "v117", "v118", "v119", "v120", "v121", "v122", "v123", "v124", "v125", "v126", "v127", "vcc", "scc", "m0", "memory");
}
__device__ __forceinline__ void lean_ld0(const u32x32 (&W)[4], u32x16& pd, unsigned ea, unsigned da, unsigned x, unsigned lb, const char* s0, const char* s1, const unsigned (&av)[8], unsigned sa, unsigned mu, unsigned ad, int th, unsigned s2, unsigned dv, const char* db) {
  asm volatile("s_setprio 2\n\t"
    "ds_read_b128 v[96:99], %[ea] offset:0\n\t"
    "ds_read_b128 v[100:103], %[ea] offset:2048\n\t"
    "ds_read_b128 v[104:107], %[da] offset:0\n\t"
    "ds_read_b128 v[108:111], %[da] offset:2048\n\t"
    "ds_read_b128 v[112:115], %[ea] offset:4096\n\t"
    "ds_read_b128 v[116:119], %[ea] offset:6144\n\t"
    "s_waitcnt lgkmcnt(4)\n\t"
    "v_mfma_f32_16x16x32_bf16 v[80:83], v[128:131], v[96:99], 0\n\t"
    "ds_read_b128 v[120:123], %[da] offset:4096\n\t"
    "v_mfma_f32_16x16x32_bf16 v[84:87], v[132:135], v[96:99], 0\n\t"
    "ds_read_b128 v[124:127], %[da] offset:6144\n\t"
    "v_mfma_f32_16x16x32_bf16 v[88:91], v[128:131], v[100:103], 0\n\t"
    "v_mfma_f32_16x16x32_bf16 v[92:95], v[132:135], v[100:103], 0\n\t"
    "s_waitcnt vmcnt(24)\n\t"
    "s_cmp_lt_u32 %[x], 2\n\t"
    "s_cbranch_scc0 41f\n\t"
    "s_waitcnt vmcnt(8)\n\t"
    "41:\n\t"
    "s_barrier\n\t"
    "s_waitcnt lgkmcnt(4)\n\t"
    "v_mfma_f32_16x16x32_bf16 v[80:83], v[136:139], v[104:107], v[80:83]\n\t"
    "ds_read_b128 v[96:99], %[ea] offset:8192\n\t"
    "v_mfma_f32_16x16x32_bf16 v[84:87], v[140:143], v[104:107], v[84:87]\n\t"
    "ds_read_b128 v[100:103], %[ea] offset:10240\n\t"
    "v_mfma_f32_16x16x32_bf16 v[88:91], v[136:139], v[108:111], v[88:91]\n\t"
    "v_mfma_f32_16x16x32_bf16 v[92:95], v[140:143], v[108:111], v[92:95]\n\t"
    "s_waitcnt lgkmcnt(4)\n\t"
    "v_mfma_f32_16x16x32_bf16 v[80:83], v[144:147], v[112:115], v[80:83]\n\t"
    "ds_read_b128 v[104:107], %[da] offset:8192\n\t"
    "v_mfma_f32_16x16x32_bf16 v[84:87], v[148:151], v[112:115], v[84:87]\n\t"
    "ds_read_b128 v[108:111], %[da] offset:10240\n\t"
    "v_mfma_f32_16x16x32_bf16 v[88:91], v[144:147], v[116:119], v[88:91]\n\t"
    "v_mfma_f32_16x16x32_bf16 v[92:95], v[148:151], v[116:119], v[92:95]\n\t"
    "s_waitcnt lgkmcnt(4)\n\t"
    "v_mfma_f32_16x16x32_bf16 v[80:83], v[152:155], v[120:123], v[80:83]\n\t"
    "ds_read_b128 v[112:115], %[ea] offset:12288\n\t"
    "v_mfma_f32_16x16x32_bf16 v[84:87], v[156:159], v[120:123], v[84:87]\n\t"
    "ds_read_b128 v[116:119], %[ea] offset:14336\n\t"
    "v_mfma_f32_16x16x32_bf16 v[88:91], v[152:155], v[124:127], v[88:91]\n\t"
    "v_mfma_f32_16x16x32_bf16 v[92:95], v[156:159], v[124:127], v[92:95]\n\t"
    "s_waitcnt lgkmcnt(4)\n\t"
    "v_mfma_f32_16x16x32_bf16 v[80:83], v[160:163], v[96:99], v[80:83]\n\t"
    "ds_read_b128 v[120:123], %[da] offset:12288\n\t"
    "v_mfma_f32_16x16x32_bf16 v[84:87], v[164:167], v[96:99], v[84:87]\n\t"
    "ds_read_b128 v[124:127], %[da] offset:14336\n\t"
    "v_mfma_f32_16x16x32_bf16 v[88:91], v[160:163], v[100:103], v[88:91]\n\t"
    "v_mfma_f32_16x16x32_bf16 v[92:95], v[164:167], v[100:103], v[92:95]\n\t"
    "s_waitcnt vmcnt(20)\n\t"
    "s_cmp_lt_u32 %[x], 2\n\t"
    "s_cbranch_scc0 45f\n\t"
    "s_waitcnt vmcnt(4)\n\t"
    "45:\n\t"
    "s_barrier\n\t"
    "s_waitcnt lgkmcnt(4)\n\t"
    "v_mfma_f32_16x16x32_bf16 v[80:83], v[168:171], v[104:107], v[80:83]\n\t"
    "ds_read_b128 v[96:99], %[ea] offset:16384\n\t"
    "v_mfma_f32_16x16x32_bf16 v[84:87], v[172:175], v[104:107], v[84:87]\n\t"
    "ds_read_b128 v[100:103], %[ea] offset:18432\n\t"
    "v_mfma_f32_16x16x32_bf16 v[88:91], v[168:171], v[108:111], v[88:91]\n\t"
    "v_mfma_f32_16x16x32_bf16 v[92:95], v[172:175], v[108:111], v[92:95]\n\t"
    "s_waitcnt lgkmcnt(4)\n\t"
    "v_mfma_f32_16x16x32_bf16 v[80:83], v[176:179], v[112:115], v[80:83]\n\t"
    "ds_read_b128 v[104:107], %[da] offset:16384\n\t"
    "v_mfma_f32_16x16x32_bf16 v[84:87], v[180:183], v[112:115], v[84:87]\n\t"
    "ds_read_b128 v[108:111], %[da] offset:18432\n\t"
    "v_mfma_f32_16x16x32_bf16 v[88:91], v[176:179], v[116:119], v[88:91]\n\t"
    "v_mfma_f32_16x16x32_bf16 v[92:95], v[180:183], v[116:119], v[92:95]\n\t"
    "s_waitcnt lgkmcnt(4)\n\t"
    "v_mfma_f32_16x16x32_bf16 v[80:83], v[184:187], v[120:123], v[80:83]\n\t"
    "ds_read_b128 v[112:115], %[ea] offset:20480\n\t"
    "v_mfma_f32_16x16x32_bf16 v[84:87], v[188:191], v[120:123], v[84:87]\n\t"
    "ds_read_b128 v[116:119], %[ea] offset:22528\n\t"
    "v_mfma_f32_16x16x32_bf16 v[88:91], v[184:187], v[124:127], v[88:91]\n\t"
    "v_mfma_f32_16x16x32_bf16 v[92:95], v[188:191], v[124:127], v[92:95]\n\t"
    "s_waitcnt lgkmcnt(4)\n\t"
    "v_mfma_f32_16x16x32_bf16 v[80:83], v[192:195], v[96:99], v[80:83]\n\t"
    "ds_read_b128 v[120:123], %[da] offset:20480\n\t"
    "v_mfma_f32_16x16x32_bf16 v[84:87], v[196:199], v[96:99], v[84:87]\n\t"
    "ds_read_b128 v[124:127], %[da] offset:22528\n\t"
    "v_mfma_f32_16x16x32_bf16 v[88:91], v[192:195], v[100:103], v[88:91]\n\t"
    "v_mfma_f32_16x16x32_bf16 v[92:95], v[196:199], v[100:103], v[92:95]\n\t"
    "s_waitcnt vmcnt(16)\n\t"
    "s_cmp_lt_u32 %[x], 2\n\t"
    "s_cbranch_scc0 49f\n\t"
    "s_waitcnt vmcnt(0)\n\t"
    "49:\n\t"
    "s_barrier\n\t"
    "ds_read_b32 v79, %[sa]\n\t"
    "s_waitcnt lgkmcnt(5)\n\t"
    "v_mfma_f32_16x16x32_bf16 v[80:83], v[200:203], v[104:107], v[80:83]\n\t"
    "ds_read_b128 v[96:99], %[ea] offset:24576\n\t"
    "v_mfma_f32_16x16x32_bf16 v[84:87], v[204:207], v[104:107], v[84:87]\n\t"
    "ds_read_b128 v[100:103], %[ea] offset:26624\n\t"
    "v_mfma_f32_16x16x32_bf16 v[88:91], v[200:203], v[108:111], v[88:91]\n\t"
    "v_mfma_f32_16x16x32_bf16 v[92:95], v[204:207], v[108:111], v[92:95]\n\t"
    "s_waitcnt lgkmcnt(5)\n\t"
    "v_mfma_f32_16x16x32_bf16 v[80:83], v[208:211], v[112:115], v[80:83]\n\t"
    "ds_read_b128 v[104:107], %[da] offset:24576\n\t"
    "v_mfma_f32_16x16x32_bf16 v[84:87], v[212:215], v[112:115], v[84:87]\n\t"
    "ds_read_b128 v[108:111], %[da] offset:26624\n\t"
    "v_mfma_f32_16x16x32_bf16 v[88:91], v[208:211], v[116:119], v[88:91]\n\t"
    "v_mfma_f32_16x16x32_bf16 v[92:95], v[212:215], v[116:119], v[92:95]\n\t"
    "s_cmp_eq_u32 %[s2], 0\n\t"
    "s_cbranch_scc1 7f\n\t"
    "s_mov_b32 m0, %[s2]\n\t"
    "s_nop 0\n\t"
    "global_load_lds_dword %[dv], %[db] sc1\n\t"
    "7:\n\t"
    "s_waitcnt lgkmcnt(4)\n\t"
    "v_mfma_f32_16x16x32_bf16 v[80:83], v[216:219], v[120:123], v[80:83]\n\t"
    "ds_read_b128 v[112:115], %[ea] offset:28672\n\t"
    "v_mfma_f32_16x16x32_bf16 v[84:87], v[220:223], v[120:123], v[84:87]\n\t"
    "ds_read_b128 v[116:119], %[ea] offset:30720\n\t"
    "v_mfma_f32_16x16x32_bf16 v[88:91], v[216:219], v[124:127], v[88:91]\n\t"
    "v_mad_u32_u24 v79, v79, %[mu], %[ad]\n\t"
    "v_cmp_ge_i32 vcc, %[th], v79\n\t"
    "v_mfma_f32_16x16x32_bf16 v[92:95], v[220:223], v[124:127], v[92:95]\n\t"
    "s_cbranch_vccz 5f\n\t"
    "s_waitcnt lgkmcnt(4)\n\t"
    "v_mfma_f32_16x16x32_bf16 v[80:83], v[224:227], v[96:99], v[80:83]\n\t"
    "ds_read_b128 v[120:123], %[da] offset:28672\n\t"
    "v_mfma_f32_16x16x32_bf16 v[84:87], v[228:231], v[96:99], v[84:87]\n\t"
    "ds_read_b128 v[124:127], %[da] offset:30720\n\t"
    "v_mfma_f32_16x16x32_bf16 v[88:91], v[224:227], v[100:103], v[88:91]\n\t"
    "v_mfma_f32_16x16x32_bf16 v[92:95], v[228:231], v[100:103], v[92:95]\n\t"
    "s_waitcnt lgkmcnt(4)\n\t"
    "v_mfma_f32_16x16x32_bf16 v[80:83], v[232:235], v[104:107], v[80:83]\n\t"
    "v_mfma_f32_16x16x32_bf16 v[84:87], v[236:239], v[104:107], v[84:87]\n\t"
    "v_mfma_f32_16x16x32_bf16 v[88:91], v[232:235], v[108:111], v[88:91]\n\t"
    "v_mfma_f32_16x16x32_bf16 v[92:95], v[236:239], v[108:111], v[92:95]\n\t"
    "s_waitcnt lgkmcnt(2)\n\t"
    "v_mfma_f32_16x16x32_bf16 v[80:83], v[240:243], v[112:115], v[80:83]\n\t"
    "v_mfma_f32_16x16x32_bf16 v[84:87], v[244:247], v[112:115], v[84:87]\n\t"
    "v_mfma_f32_16x16x32_bf16 v[88:91], v[240:243], v[116:119], v[88:91]\n\t"
    "v_mfma_f32_16x16x32_bf16 v[92:95], v[244:247], v[116:119], v[92:95]\n\t"
    "s_waitcnt lgkmcnt(0)\n\t"
    "v_mfma_f32_16x16x32_bf16 v[80:83], v[248:251], v[120:123], v[80:83]\n\t"
    "v_mfma_f32_16x16x32_bf16 v[84:87], v[252:255], v[120:123], v[84:87]\n\t"
    "v_mfma_f32_16x16x32_bf16 v[88:91], v[248:251], v[124:127], v[88:91]\n\t"
    "v_mfma_f32_16x16x32_bf16 v[92:95], v[252:255], v[124:127], v[92:95]\n\t"
    "s_branch 6f\n\t"
    "5:\n\t"
    "s_waitcnt lgkmcnt(4)\n\t"
    "v_mfma_f32_16x16x32_bf16 v[80:83], v[224:227], v[96:99], v[80:83]\n\t"
    "ds_read_b128 v[120:123], %[da] offset:28672\n\t"
    "s_add_u32 m0, %[lb], 0\n\t"
    "s_nop 0\n\t"
    "global_load_lds_dwordx4 %[av0], %[s0] sc1\n\t"
    "v_mfma_f32_16x16x32_bf16 v[84:87], v[228:231], v[96:99], v[84:87]\n\t"
    "ds_read_b128 v[124:127], %[da] offset:30720\n\t"
    "s_add_u32 m0, %[lb], 32768\n\t"
    "s_nop 0\n\t"
    "global_load_lds_dwordx4 %[av1], %[s0] sc1\n\t"
    "v_mfma_f32_16x16x32_bf16 v[88:91], v[224:227], v[100:103], v[88:91]\n\t"
    "s_add_u32 m0, %[lb], 4096\n\t"
    "s_nop 0\n\t"
    "global_load_lds_dwordx4 %[av2], %[s0] sc1\n\t"
    "v_mfma_f32_16x16x32_bf16 v[92:95], v[228:231], v[100:103], v[92:95]\n\t"
    "s_add_u32 m0, %[lb], 36864\n\t"
    "s_nop 0\n\t"
    "global_load_lds_dwordx4 %[av3], %[s0] sc1\n\t"
    "s_waitcnt lgkmcnt(4)\n\t"
    "v_mfma_f32_16x16x32_bf16 v[80:83], v[232:235], v[104:107], v[80:83]\n\t"
    "s_add_u32 m0, %[lb], 8192\n\t"
    "s_nop 0\n\t"
    "global_load_lds_dwordx4 %[av4], %[s0] sc1\n\t"
    "v_mfma_f32_16x16x32_bf16 v[84:87], v[236:239], v[104:107], v[84:87]\n\t"
    "s_add_u32 m0, %[lb], 40960\n\t"
    "s_nop 0\n\t"
    "global_load_lds_dwordx4 %[av5], %[s0] sc1\n\t"
    "v_mfma_f32_16x16x32_bf16 v[88:91], v[232:235], v[108:111], v[88:91]\n\t"
    "s_add_u32 m0, %[lb], 12288\n\t"
    "s_nop 0\n\t"
    "global_load_lds_dwordx4 %[av6], %[s0] sc1\n\t"
    "v_mfma_f32_16x16x32_bf16 v[92:95], v[236:239], v[108:111], v[92:95]\n\t"
    "s_add_u32 m0, %[lb], 45056\n\t"
    "s_nop 0\n\t"
    "global_load_lds_dwordx4 %[av7], %[s0] sc1\n\t"
    "s_waitcnt lgkmcnt(2)\n\t"
    "v_mfma_f32_16x16x32_bf16 v[80:83], v[240:243], v[112:115], v[80:83]\n\t"
    "s_add_u32 m0, %[lb], 16384\n\t"
    "s_nop 0\n\t"
    "global_load_lds_dwordx4 %[av0], %[s1] sc1\n\t"
    "v_mfma_f32_16x16x32_bf16 v[84:87], v[244:247], v[112:115], v[84:87]\n\t"
    "s_add_u32 m0, %[lb], 49152\n\t"
    "s_nop 0\n\t"
    "global_load_lds_dwordx4 %[av1], %[s1] sc1\n\t"
    "v_mfma_f32_16x16x32_bf16 v[88:91], v[240:243], v[116:119], v[88:91]\n\t"
    "s_add_u32 m0, %[lb], 20480\n\t"
    "s_nop 0\n\t"
    "global_load_lds_dwordx4 %[av2], %[s1] sc1\n\t"
    "v_mfma_f32_16x16x32_bf16 v[92:95], v[244:247], v[116:119], v[92:95]\n\t"
    "s_add_u32 m0, %[lb], 53248\n\t"
    "s_nop 0\n\t"
    "global_load_lds_dwordx4 %[av3], %[s1] sc1\n\t"
    "s_waitcnt lgkmcnt(0)\n\t"
    "v_mfma_f32_16x16x32_bf16 v[80:83], v[248:251], v[120:123], v[80:83]\n\t"
    "s_add_u32 m0, %[lb], 24576\n\t"
    "s_nop 0\n\t"
    "global_load_lds_dwordx4 %[av4], %[s1] sc1\n\t"
    "v_mfma_f32_16x16x32_bf16 v[84:87], v[252:255], v[120:123], v[84:87]\n\t"
    "s_add_u32 m0, %[lb], 57344\n\t"
    "s_nop 0\n\t"
    "global_load_lds_dwordx4 %[av5], %[s1] sc1\n\t"
    "v_mfma_f32_16x16x32_bf16 v[88:91], v[248:251], v[124:127], v[88:91]\n\t"
    "s_add_u32 m0, %[lb], 28672\n\t"
    "s_nop 0\n\t"
    "global_load_lds_dwordx4 %[av6], %[s1] sc1\n\t"
    "v_mfma_f32_16x16x32_bf16 v[92:95], v[252:255], v[124:127], v[92:95]\n\t"
    "s_add_u32 m0, %[lb], 61440\n\t"
    "s_nop 0\n\t"
    "global_load_lds_dwordx4 %[av7], %[s1] sc1\n\t"
    "6:\n\t"
    "s_nop 6\n\t"
    "s_branch 9f\n\t"
    "3:\n\t"
    "9:"
    : "=&{v[80:95]}"(pd)
    : [ea] "v"(ea), [da] "v"(da), [x] "s"(x), "{v[128:159]}"(W[0]), "{v[160:191]}"(W[1]), "{v[192:223]}"(W[2]), "{v[224:255]}"(W[3]), [lb] "s"(lb), [s0] "s"(s0), [s1] "s"(s1), [av0] "v"(av[0]), [av1] "v"(av[1]), [av2] "v"(av[2]), [av3] "v"(av[3]), [av4] "v"(av[4]), [av5] "v"(av[5]), [av6] "v"(av[6]), [av7] "v"(av[7]), [sa] "v"(sa), [mu] "s"(mu), [ad] "v"(ad), [th] "s"(th), [s2] "s"(s2), [dv] "v"(dv), [db] "s"(db)
    : "v96", "v97", "v98", "v99", "v100", "v101", "v102", "v103", "v104", "v105", "v106", "v107", "v108", "v109", "v110", "v111", "v112", "v113", "v114", "v115", "v116", "v117", "v118", "v119", "v120", "v121", "v122", "v123", "v124", "v125", "v126", "v127", "v79", "vcc", "scc", "m0", "memory");
}
__device__ __forceinline__ void lean_dm0(const u32x32 (&W)[4], u32x16& pd, unsigned ea, unsigned da, unsigned x, unsigned lb, const char* s0, const char* s1, const unsigned (&av)[8]) {
  asm volatile("s_setprio 2\n\t"
    "ds_read_b128 v[96:99], %[ea] offset:0\n\t"
    "ds_read_b128 v[100:103], %[ea] offset:2048\n\t"
    "ds_read_b128 v[104:107], %[da] offset:0\n\t"
    "ds_read_b128 v[108:111], %[da] offset:2048\n\t"
    "ds_read_b128 v[112:115], %[ea] offset:4096\n\t"
    "ds_read_b128 v[116:119], %[ea] offset:6144\n\t"
    "s_waitcnt lgkmcnt(4)\n\t"
    "v_mfma_f32_16x16x32_bf16 v[80:83], v[128:131], v[96:99], 0\n\t"
    "ds_read_b128 v[120:123], %[da] offset:4096\n\t"
    "s_add_u32 m0, %[lb], 0\n\t"
    "v_mfma_f32_16x16x32_bf16 v[84:87], v[132:135], v[96:99], 0\n\t"
    "ds_read_b128 v[124:127], %[da] offset:6144\n\t"
    "global_load_lds_dwordx4 %[av0], %[s0] sc1\n\t"
    "v_mfma_f32_16x16x32_bf16 v[88:91], v[128:131], v[100:103], 0\n\t"
    "s_add_u32 m0, %[lb], 32768\n\t"
    "v_mfma_f32_16x16x32_bf16 v[92:95], v[132:135], v[100:103], 0\n\t"
    "global_load_lds_dwordx4 %[av1], %[s0] sc1\n\t"
    "s_waitcnt vmcnt(10)\n\t"
    "s_barrier\n\t"
    "s_waitcnt lgkmcnt(4)\n\t"
    "v_mfma_f32_16x16x32_bf16 v[80:83], v[136:139], v[104:107], v[80:83]\n\t"
    "ds_read_b128 v[96:99], %[ea] offset:8192\n\t"
    "s_add_u32 m0, %[lb], 4096\n\t"
    "v_mfma_f32_16x16x32_bf16 v[84:87], v[140:143], v[104:107], v[84:87]\n\t"
    "ds_read_b128 v[100:103], %[ea] offset:10240\n\t"
    "global_load_lds_dwordx4 %[av2], %[s0] sc1\n\t"
    "v_mfma_f32_16x16x32_bf16 v[88:91], v[136:139], v[108:111], v[88:91]\n\t"
    "s_add_u32 m0, %[lb], 36864\n\t"
    "v_mfma_f32_16x16x32_bf16 v[92:95], v[140:143], v[108:111], v[92:95]\n\t"
    "global_load_lds_dwordx4 %[av3], %[s0] sc1\n\t"
    "s_waitcnt lgkmcnt(4)\n\t"
    "v_mfma_f32_16x16x32_bf16 v[80:83], v[144:147], v[112:115], v[80:83]\n\t"
    "ds_read_b128 v[104:107], %[da] offset:8192\n\t"
    "s_add_u32 m0, %[lb], 8192\n\t"
    "v_mfma_f32_16x16x32_bf16 v[84:87], v[148:151], v[112:115], v[84:87]\n\t"
    "ds_read_b128 v[108:111], %[da] offset:10240\n\t"
    "global_load_lds_dwordx4 %[av4], %[s0] sc1\n\t"
    "v_mfma_f32_16x16x32_bf16 v[88:91], v[144:147], v[116:119], v[88:91]\n\t"
    "s_add_u32 m0, %[lb], 40960\n\t"
    "v_mfma_f32_16x16x32_bf16 v[92:95], v[148:151], v[116:119], v[92:95]\n\t"
    "global_load_lds_dwordx4 %[av5], %[s0] sc1\n\t"
    "s_waitcnt lgkmcnt(4)\n\t"
    "v_mfma_f32_16x16x32_bf16 v[80:83], v[152:155], v[120:123], v[80:83]\n\t"
    "ds_read_b128 v[112:115], %[ea] offset:12288\n\t"
    "s_add_u32 m0, %[lb], 12288\n\t"
    "v_mfma_f32_16x16x32_bf16 v[84:87], v[156:159], v[120:123], v[84:87]\n\t"
    "ds_read_b128 v[116:119], %[ea] offset:14336\n\t"
    "global_load_lds_dwordx4 %[av6], %[s0] sc1\n\t"
    "v_mfma_f32_16x16x32_bf16 v[88:91], v[152:155], v[124:127], v[88:91]\n\t"
    "s_add_u32 m0, %[lb], 45056\n\t"
    "v_mfma_f32_16x16x32_bf16 v[92:95], v[156:159], v[124:127], v[92:95]\n\t"
    "global_load_lds_dwordx4 %[av7], %[s0] sc1\n\t"
    "s_waitcnt lgkmcnt(4)\n\t"
    "v_mfma_f32_16x16x32_bf16 v[80:83], v[160:163], v[96:99], v[80:83]\n\t"
    "ds_read_b128 v[120:123], %[da] offset:12288\n\t"
    "s_add_u32 m0, %[lb], 16384\n\t"
    "v_mfma_f32_16x16x32_bf16 v[84:87], v[164:167], v[96:99], v[84:87]\n\t"
    "ds_read_b128 v[124:127], %[da] offset:14336\n\t"
    "global_load_lds_dwordx4 %[av0], %[s1] sc1\n\t"
    "v_mfma_f32_16x16x32_bf16 v[88:91], v[160:163], v[100:103], v[88:91]\n\t"
    "s_add_u32 m0, %[lb], 49152\n\t"
    "v_mfma_f32_16x16x32_bf16 v[92:95], v[164:167], v[100:103], v[92:95]\n\t"
    "global_load_lds_dwordx4 %[av1], %[s1] sc1\n\t"
    "s_waitcnt vmcnt(14)\n\t"
    "s_barrier\n\t"
    "s_waitcnt lgkmcnt(4)\n\t"
    "v_mfma_f32_16x16x32_bf16 v[80:83], v[168:171], v[104:107], v[80:83]\n\t"
    "ds_read_b128 v[96:99], %[ea] offset:16384\n\t"
    "s_add_u32 m0, %[lb], 20480\n\t"
    "v_mfma_f32_16x16x32_bf16 v[84:87], v[172:175], v[104:107], v[84:87]\n\t"
    "ds_read_b128 v[100:103], %[ea] offset:18432\n\t"
    "global_load_lds_dwordx4 %[av2], %[s1] sc1\n\t"
    "v_mfma_f32_16x16x32_bf16 v[88:91], v[168:171], v[108:111], v[88:91]\n\t"
    "s_add_u32 m0, %[lb], 53248\n\t"
    "v_mfma_f32_16x16x32_bf16 v[92:95], v[172:175], v[108:111], v[92:95]\n\t"
    "global_load_lds_dwordx4 %[av3], %[s1] sc1\n\t"
    "s_waitcnt lgkmcnt(4)\n\t"
    "v_mfma_f32_16x16x32_bf16 v[80:83], v[176:179], v[112:115], v[80:83]\n\t"
    "ds_read_b128 v[104:107], %[da] offset:16384\n\t"
    "s_add_u32 m0, %[lb], 24576\n\t"
    "v_mfma_f32_16x16x32_bf16 v[84:87], v[180:183], v[112:115], v[84:87]\n\t"
    "ds_read_b128 v[108:111], %[da] offset:18432\n\t"
    "global_load_lds_dwordx4 %[av4], %[s1] sc1\n\t"
    "v_mfma_f32_16x16x32_bf16 v[88:91], v[176:179], v[116:119], v[88:91]\n\t"
    "s_add_u32 m0, %[lb], 57344\n\t"
    "v_mfma_f32_16x16x32_bf16 v[92:95], v[180:183], v[116:119], v[92:95]\n\t"
    "global_load_lds_dwordx4 %[av5], %[s1] sc1\n\t"
    "s_waitcnt lgkmcnt(4)\n\t"
    "v_mfma_f32_16x16x32_bf16 v[80:83], v[184:187], v[120:123], v[80:83]\n\t"
    "ds_read_b128 v[112:115], %[ea] offset:20480\n\t"
    "s_add_u32 m0, %[lb], 28672\n\t"
    "v_mfma_f32_16x16x32_bf16 v[84:87], v[188:191], v[120:123], v[84:87]\n\t"
    "ds_read_b128 v[116:119], %[ea] offset:22528\n\t"
    "global_load_lds_dwordx4 %[av6], %[s1] sc1\n\t"
    "v_mfma_f32_16x16x32_bf16 v[88:91], v[184:187], v[124:127], v[88:91]\n\t"
    "s_add_u32 m0, %[lb], 61440\n\t"
    "v_mfma_f32_16x16x32_bf16 v[92:95], v[188:191], v[124:127], v[92:95]\n\t"
    "global_load_lds_dwordx4 %[av7], %[s1] sc1\n\t"
    "s_waitcnt lgkmcnt(4)\n\t"
    "v_mfma_f32_16x16x32_bf16 v[80:83], v[192:195], v[96:99], v[80:83]\n\t"
    "ds_read_b128 v[120:123], %[da] offset:20480\n\t"
    "v_mfma_f32_16x16x32_bf16 v[84:87], v[196:199], v[96:99], v[84:87]\n\t"
    "ds_read_b128 v[124:127], %[da] offset:22528\n\t"
    "v_mfma_f32_16x16x32_bf16 v[88:91], v[192:195], v[100:103], v[88:91]\n\t"
    "v_mfma_f32_16x16x32_bf16 v[92:95], v[196:199], v[100:103], v[92:95]\n\t"
    "s_waitcnt vmcnt(16)\n\t"
    "s_barrier\n\t"
    "s_waitcnt lgkmcnt(4)\n\t"
    "v_mfma_f32_16x16x32_bf16 v[80:83], v[200:203], v[104:107], v[80:83]\n\t"
    "ds_read_b128 v[96:99], %[ea] offset:24576\n\t"
    "v_mfma_f32_16x16x32_bf16 v[84:87], v[204:207], v[104:107], v[84:87]\n\t"
    "ds_read_b128 v[100:103], %[ea] offset:26624\n\t"
    "v_mfma_f32_16x16x32_bf16 v[88:91], v[200:203], v[108:111], v[88:91]\n\t"
    "v_mfma_f32_16x16x32_bf16 v[92:95], v[204:207], v[108:111], v[92:95]\n\t"
    "s_waitcnt lgkmcnt(4)\n\t"
    "v_mfma_f32_16x16x32_bf16 v[80:83], v[208:211], v[112:115], v[80:83]\n\t"
    "ds_read_b128 v[104:107], %[da] offset:24576\n\t"
    "v_mfma_f32_16x16x32_bf16 v[84:87], v[212:215], v[112:115], v[84:87]\n\t"
    "ds_read_b128 v[108:111], %[da] offset:26624\n\t"
    "v_mfma_f32_16x16x32_bf16 v[88:91], v[208:211], v[116:119], v[88:91]\n\t"
    "v_mfma_f32_16x16x32_bf16 v[92:95], v[212:215], v[116:119], v[92:95]\n\t"
    "s_waitcnt lgkmcnt(4)\n\t"
    "v_mfma_f32_16x16x32_bf16 v[80:83], v[216:219], v[120:123], v[80:83]\n\t"
    "ds_read_b128 v[112:115], %[ea] offset:28672\n\t"
    "v_mfma_f32_16x16x32_bf16 v[84:87], v[220:223], v[120:123], v[84:87]\n\t"
    "ds_read_b128 v[116:119], %[ea] offset:30720\n\t"
    "v_mfma_f32_16x16x32_bf16 v[88:91], v[216:219], v[124:127], v[88:91]\n\t"
    "v_mfma_f32_16x16x32_bf16 v[92:95], v[220:223], v[124:127], v[92:95]\n\t"
    "s_waitcnt lgkmcnt(4)\n\t"
    "v_mfma_f32_16x16x32_bf16 v[80:83], v[224:227], v[96:99], v[80:83]\n\t"
    "ds_read_b128 v[120:123], %[da] offset:28672\n\t"
    "v_mfma_f32_16x16x32_bf16 v[84:87], v[228:231], v[96:99], v[84:87]\n\t"
    "ds_read_b128 v[124:127], %[da] offset:30720\n\t"
    "v_mfma_f32_16x16x32_bf16 v[88:91], v[224:227], v[100:103], v[88:91]\n\t"
    "v_mfma_f32_16x16x32_bf16 v[92:95], v[228:231], v[100:103], v[92:95]\n\t"
    "s_waitcnt lgkmcnt(4)\n\t"
    "v_mfma_f32_16x16x32_bf16 v[80:83], v[232:235], v[104:107], v[80:83]\n\t"
    "v_mfma_f32_16x16x32_bf16 v[84:87], v[236:239], v[104:107], v[84:87]\n\t"
    "v_mfma_f32_16x16x32_bf16 v[88:91], v[232:235], v[108:111], v[88:91]\n\t"
    "v_mfma_f32_16x16x32_bf16 v[92:95], v[236:239], v[108:111], v[92:95]\n\t"
    "s_waitcnt lgkmcnt(2)\n\t"
    "v_mfma_f32_16x16x32_bf16 v[80:83], v[240:243], v[112:115], v[80:83]\n\t"
    "v_mfma_f32_16x16x32_bf16 v[84:87], v[244:247], v[112:115], v[84:87]\n\t"
    "v_mfma_f32_16x16x32_bf16 v[88:91], v[240:243], v[116:119], v[88:91]\n\t"
    "v_mfma_f32_16x16x32_bf16 v[92:95], v[244:247], v[116:119], v[92:95]\n\t"
    "s_waitcnt lgkmcnt(0)\n\t"
    "v_mfma_f32_16x16x32_bf16 v[80:83], v[248:251], v[120:123], v[80:83]\n\t"
    "v_mfma_f32_16x16x32_bf16 v[84:87], v[252:255], v[120:123], v[84:87]\n\t"
    "v_mfma_f32_16x16x32_bf16 v[88:91], v[248:251], v[124:127], v[88:91]\n\t"
    "v_mfma_f32_16x16x32_bf16 v[92:95], v[252:255], v[124:127], v[92:95]\n\t"
    "s_nop 6\n\t"
    "s_branch 9f\n\t"
    "3:\n\t"
    "9:"
    : "=&{v[80:95]}"(pd)
    : [ea] "v"(ea), [da] "v"(da), [x] "s"(x), "{v[128:159]}"(W[0]), "{v[160:191]}"(W[1]), "{v[192:223]}"(W[2]), "{v[224:255]}"(W[3]), [lb] "s"(lb), [s0] "s"(s0), [s1] "s"(s1), [av0] "v"(av[0]), [av1] "v"(av[1]), [av2] "v"(av[2]), [av3] "v"(av[3]), [av4] "v"(av[4]), [av5] "v"(av[5]), [av6] "v"(av[6]), [av7] "v"(av[7])
    : "v96", "v97", "v98", "v99", "v100", "v101", "v102", "v103", "v104", "v105", "v106", "v107", "v108", "v109", "v110", "v111", "v112", "v113", "v114", "v115", "v116", "v117", "v118", "v119", "v120", "v121", "v122", "v123", "v124", "v125", "v126", "v127", "vcc", "scc", "m0", "memory");
}
__device__ __forceinline__ void lean_ld1(const u32x32 (&W)[4], u32x16& pd, unsigned ea, unsigned da, unsigned x, unsigned lb, const char* s0, const char* s1, const unsigned (&av)[8], unsigned sa, unsigned mu, unsigned ad, int th, unsigned s2, unsigned dv, const char* db) {
  asm volatile("s_setprio 2\n\t"
    "ds_read_b128 v[96:99], %[ea] offset:0\n\t"
    "ds_read_b128 v[100:103], %[ea] offset:2048\n\t"
    "ds_read_b128 v[104:107], %[da] offset:0\n\t"
    "ds_read_b128 v[108:111], %[da] offset:2048\n\t"
    "ds_read_b128 v[112:115], %[ea] offset:4096\n\t"
    "ds_read_b128 v[116:119], %[ea] offset:6144\n\t"
    "s_waitcnt lgkmcnt(4)\n\t"
    "v_mfma_f32_16x16x32_bf16 v[80:83], v[128:131], v[96:99], 0\n\t"
    "ds_read_b128 v[120:123], %[da] offset:4096\n\t"
    "v_mfma_f32_16x16x32_bf16 v[84:87], v[132:135], v[96:99], 0\n\t"
    "ds_read_b128 v[124:127], %[da] offset:6144\n\t"
    "v_mfma_f32_16x16x32_bf16 v[88:91], v[128:131], v[100:103], 0\n\t"
    "v_mfma_f32_16x16x32_bf16 v[92:95], v[132:135], v[100:103], 0\n\t"
    "s_waitcnt vmcnt(12)\n\t"
    "s_cmp_lt_u32 %[x], 2\n\t"
    "s_cbranch_scc0 41f\n\t"
    "s_waitcnt vmcnt(4)\n\t"
    "41:\n\t"
    "s_barrier\n\t"
    "s_waitcnt lgkmcnt(4)\n\t"
    "v_mfma_f32_16x16x32_bf16 v[80:83], v[136:139], v[104:107], v[80:83]\n\t"
    "ds_read_b128 v[96:99], %[ea] offset:8192\n\t"
    "v_mfma_f32_16x16x32_bf16 v[84:87], v[140:143], v[104:107], v[84:87]\n\t"
    "ds_read_b128 v[100:103], %[ea] offset:10240\n\t"
    "v_mfma_f32_16x16x32_bf16 v[88:91], v[136:139], v[108:111], v[88:91]\n\t"
    "v_mfma_f32_16x16x32_bf16 v[92:95], v[140:143], v[108:111], v[92:95]\n\t"
    "s_waitcnt lgkmcnt(4)\n\t"
    "v_mfma_f32_16x16x32_bf16 v[80:83], v[144:147], v[112:115], v[80:83]\n\t"
    "ds_read_b128 v[104:107], %[da] offset:8192\n\t"
    "v_mfma_f32_16x16x32_bf16 v[84:87], v[148:151], v[112:115], v[84:87]\n\t"
    "ds_read_b128 v[108:111], %[da] offset:10240\n\t"
    "v_mfma_f32_16x16x32_bf16 v[88:91], v[144:147], v[116:119], v[88:91]\n\t"
    "v_mfma_f32_16x16x32_bf16 v[92:95], v[148:151], v[116:119], v[92:95]\n\t"
    "s_waitcnt lgkmcnt(4)\n\t"
    "v_mfma_f32_16x16x32_bf16 v[80:83], v[152:155], v[120:123], v[80:83]\n\t"
    "ds_read_b128 v[112:115], %[ea] offset:12288\n\t"
    "v_mfma_f32_16x16x32_bf16 v[84:87], v[156:159], v[120:123], v[84:87]\n\t"
    "ds_read_b128 v[116:119], %[ea] offset:14336\n\t"
    "v_mfma_f32_16x16x32_bf16 v[88:91], v[152:155], v[124:127], v[88:91]\n\t"
    "v_mfma_f32_16x16x32_bf16 v[92:95], v[156:159], v[124:127], v[92:95]\n\t"
    "s_waitcnt lgkmcnt(4)\n\t"
    "v_mfma_f32_16x16x32_bf16 v[80:83], v[160:163], v[96:99], v[80:83]\n\t"
    "ds_read_b128 v[120:123], %[da] offset:12288\n\t"
    "v_mfma_f32_16x16x32_bf16 v[84:87], v[164:167], v[96:99], v[84:87]\n\t"
    "ds_read_b128 v[124:127], %[da] offset:14336\n\t"
    "v_mfma_f32_16x16x32_bf16 v[88:91], v[160:163], v[100:103], v[88:91]\n\t"
    "v_mfma_f32_16x16x32_bf16 v[92:95], v[164:167], v[100:103], v[92:95]\n\t"
    "s_waitcnt vmcnt(10)\n\t"
    "s_cmp_lt_u32 %[x], 2\n\t"
    "s_cbranch_scc0 45f\n\t"
    "s_waitcnt vmcnt(2)\n\t"
    "45:\n\t"
    "s_barrier\n\t"
    "s_waitcnt lgkmcnt(4)\n\t"
    "v_mfma_f32_16x16x32_bf16 v[80:83], v[168:171], v[104:107], v[80:83]\n\t"
    "ds_read_b128 v[96:99], %[ea] offset:16384\n\t"
    "v_mfma_f32_16x16x32_bf16 v[84:87], v[172:175], v[104:107], v[84:87]\n\t"
    "ds_read_b128 v[100:103], %[ea] offset:18432\n\t"
    "v_mfma_f32_16x16x32_bf16 v[88:91], v[168:171], v[108:111], v[88:91]\n\t"
    "v_mfma_f32_16x16x32_bf16 v[92:95], v[172:175], v[108:111], v[92:95]\n\t"
    "s_waitcnt lgkmcnt(4)\n\t"
    "v_mfma_f32_16x16x32_bf16 v[80:83], v[176:179], v[112:115], v[80:83]\n\t"
    "ds_read_b128 v[104:107], %[da] offset:16384\n\t"
    "v_mfma_f32_16x16x32_bf16 v[84:87], v[180:183], v[112:115], v[84:87]\n\t"
    "ds_read_b128 v[108:111], %[da] offset:18432\n\t"
    "v_mfma_f32_16x16x32_bf16 v[88:91], v[176:179], v[116:119], v[88:91]\n\t"
    "v_mfma_f32_16x16x32_bf16 v[92:95], v[180:183], v[116:119], v[92:95]\n\t"
    "s_waitcnt lgkmcnt(4)\n\t"
    "v_mfma_f32_16x16x32_bf16 v[80:83], v[184:187], v[120:123], v[80:83]\n\t"
    "ds_read_b128 v[112:115], %[ea] offset:20480\n\t"
    "v_mfma_f32_16x16x32_bf16 v[84:87], v[188:191], v[120:123], v[84:87]\n\t"
    "ds_read_b128 v[116:119], %[ea] offset:22528\n\t"
    "v_mfma_f32_16x16x32_bf16 v[88:91], v[184:187], v[124:127], v[88:91]\n\t"
    "v_mfma_f32_16x16x32_bf16 v[92:95], v[188:191], v[124:127], v[92:95]\n\t"
    "s_waitcnt lgkmcnt(4)\n\t"
    "v_mfma_f32_16x16x32_bf16 v[80:83], v[192:195], v[96:99], v[80:83]\n\t"
    "ds_read_b128 v[120:123], %[da] offset:20480\n\t"
    "v_mfma_f32_16x16x32_bf16 v[84:87], v[196:199], v[96:99], v[84:87]\n\t"
    "ds_read_b128 v[124:127], %[da] offset:22528\n\t"
    "v_mfma_f32_16x16x32_bf16 v[88:91], v[192:195], v[100:103], v[88:91]\n\t"
    "v_mfma_f32_16x16x32_bf16 v[92:95], v[196:199], v[100:103], v[92:95]\n\t"
    "s_waitcnt vmcnt(8)\n\t"
    "s_cmp_lt_u32 %[x], 2\n\t"
    "s_cbranch_scc0 49f\n\t"
    "s_waitcnt vmcnt(0)\n\t"
    "49:\n\t"
    "s_barrier\n\t"
    "ds_read_b32 v79, %[sa]\n\t"
    "s_waitcnt lgkmcnt(5)\n\t"
    "v_mfma_f32_16x16x32_bf16 v[80:83], v[200:203], v[104:107], v[80:83]\n\t"
    "ds_read_b128 v[96:99], %[ea] offset:24576\n\t"
    "v_mfma_f32_16x16x32_bf16 v[84:87], v[204:207], v[104:107], v[84:87]\n\t"
    "ds_read_b128 v[100:103], %[ea] offset:26624\n\t"
    "v_mfma_f32_16x16x32_bf16 v[88:91], v[200:203], v[108:111], v[88:91]\n\t"
    "v_mfma_f32_16x16x32_bf16 v[92:95], v[204:207], v[108:111], v[92:95]\n\t"
    "s_waitcnt lgkmcnt(5)\n\t"
    "v_mfma_f32_16x16x32_bf16 v[80:83], v[208:211], v[112:115], v[80:83]\n\t"
    "ds_read_b128 v[104:107], %[da] offset:24576\n\t"
    "v_mfma_f32_16x16x32_bf16 v[84:87], v[212:215], v[112:115], v[84:87]\n\t"
    "ds_read_b128 v[108:111], %[da] offset:26624\n\t"
    "v_mfma_f32_16x16x32_bf16 v[88:91], v[208:211], v[116:119], v[88:91]\n\t"
    "v_mfma_f32_16x16x32_bf16 v[92:95], v[212:215], v[116:119], v[92:95]\n\t"
    "s_cmp_eq_u32 %[s2], 0\n\t"
    "s_cbranch_scc1 7f\n\t"
    "s_mov_b32 m0, %[s2]\n\t"
    "s_nop 0\n\t"
    "global_load_lds_dword %[dv], %[db] sc1\n\t"
    "7:\n\t"
    "s_waitcnt lgkmcnt(4)\n\t"
    "v_mfma_f32_16x16x32_bf16 v[80:83], v[216:219], v[120:123], v[80:83]\n\t"
    "ds_read_b128 v[112:115], %[ea] offset:28672\n\t"
    "v_mfma_f32_16x16x32_bf16 v[84:87], v[220:223], v[120:123], v[84:87]\n\t"
    "ds_read_b128 v[116:119], %[ea] offset:30720\n\t"
    "v_mfma_f32_16x16x32_bf16 v[88:91], v[216:219], v[124:127], v[88:91]\n\t"
    "v_mad_u32_u24 v79, v79, %[mu], %[ad]\n\t"
    "v_cmp_ge_i32 vcc, %[th], v79\n\t"
    "v_mfma_f32_16x16x32_bf16 v[92:95], v[220:223], v[124:127], v[92:95]\n\t"
    "s_cbranch_vccz 5f\n\t"
    "s_waitcnt lgkmcnt(4)\n\t"
    "v_mfma_f32_16x16x32_bf16 v[80:83], v[224:227], v[96:99], v[80:83]\n\t"
    "ds_read_b128 v[120:123], %[da] offset:28672\n\t"
    "v_mfma_f32_16x16x32_bf16 v[84:87], v[228:231], v[96:99], v[84:87]\n\t"
    "ds_read_b128 v[124:127], %[da] offset:30720\n\t"
    "v_mfma_f32_16x16x32_bf16 v[88:91], v[224:227], v[100:103], v[88:91]\n\t"
    "v_mfma_f32_16x16x32_bf16 v[92:95], v[228:231], v[100:103], v[92:95]\n\t"
    "s_waitcnt lgkmcnt(4)\n\t"
    "v_mfma_f32_16x16x32_bf16 v[80:83], v[232:235], v[104:107], v[80:83]\n\t"
    "v_mfma_f32_16x16x32_bf16 v[84:87], v[236:239], v[104:107], v[84:87]\n\t"
    "v_mfma_f32_16x16x32_bf16 v[88:91], v[232:235], v[108:111], v[88:91]\n\t"
    "v_mfma_f32_16x16x32_bf16 v[92:95], v[236:239], v[108:111], v[92:95]\n\t"
    "s_waitcnt lgkmcnt(2)\n\t"
    "v_mfma_f32_16x16x32_bf16 v[80:83], v[240:243], v[112:115], v[80:83]\n\t"
    "v_mfma_f32_16x16x32_bf16 v[84:87], v[244:247], v[112:115], v[84:87]\n\t"
    "v_mfma_f32_16x16x32_bf16 v[88:91], v[240:243], v[116:119], v[88:91]\n\t"
    "v_mfma_f32_16x16x32_bf16 v[92:95], v[244:247], v[116:119], v[92:95]\n\t"
    "s_waitcnt lgkmcnt(0)\n\t"
    "v_mfma_f32_16x16x32_bf16 v[80:83], v[248:251], v[120:123], v[80:83]\n\t"
    "v_mfma_f32_16x16x32_bf16 v[84:87], v[252:255], v[120:123], v[84:87]\n\t"
    "v_mfma_f32_16x16x32_bf16 v[88:91], v[248:251], v[124:127], v[88:91]\n\t"
    "v_mfma_f32_16x16x32_bf16 v[92:95], v[252:255], v[124:127], v[92:95]\n\t"
    "s_branch 6f\n\t"
    "5:\n\t"
    "s_waitcnt lgkmcnt(4)\n\t"
    "v_mfma_f32_16x16x32_bf16 v[80:83], v[224:227], v[96:99], v[80:83]\n\t"
    "ds_read_b128 v[120:123], %[da] offset:28672\n\t"
    "s_add_u32 m0, %[lb], 0\n\t"
    "s_nop 0\n\t"
    "global_load_lds_dwordx4 %[av0], %[s0] sc1\n\t"
    "v_mfma_f32_16x16x32_bf16 v[84:87], v[228:231], v[96:99], v[84:87]\n\t"
    "ds_read_b128 v[124:127], %[da] offset:30720\n\t"
    "v_mfma_f32_16x16x32_bf16 v[88:91], v[224:227], v[100:103], v[88:91]\n\t"
    "s_add_u32 m0, %[lb], 4096\n\t"
    "s_nop 0\n\t"
    "global_load_lds_dwordx4 %[av2], %[s0] sc1\n\t"
    "v_mfma_f32_16x16x32_bf16 v[92:95], v[228:231], v[100:103], v[92:95]\n\t"
    "s_waitcnt lgkmcnt(4)\n\t"
    "v_mfma_f32_16x16x32_bf16 v[80:83], v[232:235], v[104:107], v[80:83]\n\t"
    "s_add_u32 m0, %[lb], 8192\n\t"
    "s_nop 0\n\t"
    "global_load_lds_dwordx4 %[av4], %[s0] sc1\n\t"
    "v_mfma_f32_16x16x32_bf16 v[84:87], v[236:239], v[104:107], v[84:87]\n\t"
    "v_mfma_f32_16x16x32_bf16 v[88:91], v[232:235], v[108:111], v[88:91]\n\t"
    "s_add_u32 m0, %[lb], 12288\n\t"
    "s_nop 0\n\t"
    "global_load_lds_dwordx4 %[av6], %[s0] sc1\n\t"
    "v_mfma_f32_16x16x32_bf16 v[92:95], v[236:239], v[108:111], v[92:95]\n\t"
    "s_waitcnt lgkmcnt(2)\n\t"
    "v_mfma_f32_16x16x32_bf16 v[80:83], v[240:243], v[112:115], v[80:83]\n\t"
    "s_add_u32 m0, %[lb], 16384\n\t"
    "s_nop 0\n\t"
    "global_load_lds_dwordx4 %[av0], %[s1] sc1\n\t"
    "v_mfma_f32_16x16x32_bf16 v[84:87], v[244:247], v[112:115], v[84:87]\n\t"
    "v_mfma_f32_16x16x32_bf16 v[88:91], v[240:243], v[116:119], v[88:91]\n\t"
    "s_add_u32 m0, %[lb], 20480\n\t"
    "s_nop 0\n\t"
    "global_load_lds_dwordx4 %[av2], %[s1] sc1\n\t"
    "v_mfma_f32_16x16x32_bf16 v[92:95], v[244:247], v[116:119], v[92:95]\n\t"
    "s_waitcnt lgkmcnt(0)\n\t"
    "v_mfma_f32_16x16x32_bf16 v[80:83], v[248:251], v[120:123], v[80:83]\n\t"
    "s_add_u32 m0, %[lb], 24576\n\t"
    "s_nop 0\n\t"
    "global_load_lds_dwordx4 %[av4], %[s1] sc1\n\t"
    "v_mfma_f32_16x16x32_bf16 v[84:87], v[252:255], v[120:123], v[84:87]\n\t"
    "v_mfma_f32_16x16x32_bf16 v[88:91], v[248:251], v[124:127], v[88:91]\n\t"
    "s_add_u32 m0, %[lb], 28672\n\t"
    "s_nop 0\n\t"
    "global_load_lds_dwordx4 %[av6], %[s1] sc1\n\t"
    "v_mfma_f32_16x16x32_bf16 v[92:95], v[252:255], v[124:127], v[92:95]\n\t"
    "6:\n\t"
    "s_nop 6\n\t"
    "s_branch 9f\n\t"
    "3:\n\t"
    "9:"
    : "=&{v[80:95]}"(pd)
    : [ea] "v"(ea), [da] "v"(da), [x] "s"(x), "{v[128:159]}"(W[0]), "{v[160:191]}"(W[1]), "{v[192:223]}"(W[2]), "{v[224:255]}"(W[3]), [lb] "s"(lb), [s0] "s"(s0), [s1] "s"(s1), [av0] "v"(av[0]), [av1] "v"(av[1]), [av2] "v"(av[2]), [av3] "v"(av[3]), [av4] "v"(av[4]), [av5] "v"(av[5]), [av6] "v"(av[6]), [av7] "v"(av[7]), [sa] "v"(sa), [mu] "s"(mu), [ad] "v"(ad), [th] "s"(th), [s2] "s"(s2), [dv] "v"(dv), [db] "s"(db)
    : "v96", "v97", "v98", "v99", "v100", "v101", "v102", "v103", "v104", "v105", "v106", "v107", "v108", "v109", "v110", "v111", "v112", "v113", "v114", "v115", "v116", "v117", "v118", "v119", "v120", "v121", "v122", "v123", "v124", "v125", "v126", "v127", "v79", "vcc", "scc", "m0", "memory");
}
__device__ __forceinline__ void lean_dm1(const u32x32 (&W)[4], u32x16& pd, unsigned ea, unsigned da, unsigned x, unsigned lb, const char* s0, const char* s1, const unsigned (&av)[8]) {
  asm volatile("s_setprio 2\n\t"
    "ds_read_b128 v[96:99], %[ea] offset:0\n\t"
    "ds_read_b128 v[100:103], %[ea] offset:2048\n\t"
    "ds_read_b128 v[104:107], %[da] offset:0\n\t"
    "ds_read_b128 v[108:111], %[da] offset:2048\n\t"
    "ds_read_b128 v[112:115], %[ea] offset:4096\n\t"
    "ds_read_b128 v[116:119], %[ea] offset:6144\n\t"
    "s_waitcnt lgkmcnt(4)\n\t"
    "v_mfma_f32_16x16x32_bf16 v[80:83], v[128:131], v[96:99], 0\n\t"
    "ds_read_b128 v[120:123], %[da] offset:4096\n\t"
    "s_add_u32 m0, %[lb], 0\n\t"
    "v_mfma_f32_16x16x32_bf16 v[84:87], v[132:135], v[96:99], 0\n\t"
    "ds_read_b128 v[124:127], %[da] offset:6144\n\t"
    "global_load_lds_dwordx4 %[av0], %[s0] sc1\n\t"
    "v_mfma_f32_16x16x32_bf16 v[88:91], v[128:131], v[100:103], 0\n\t"
    "v_mfma_f32_16x16x32_bf16 v[92:95], v[132:135], v[100:103], 0\n\t"
    "s_waitcnt vmcnt(5)\n\t"
    "s_barrier\n\t"
    "s_waitcnt lgkmcnt(4)\n\t"
    "v_mfma_f32_16x16x32_bf16 v[80:83], v[136:139], v[104:107], v[80:83]\n\t"
    "ds_read_b128 v[96:99], %[ea] offset:8192\n\t"
    "s_add_u32 m0, %[lb], 4096\n\t"
    "v_mfma_f32_16x16x32_bf16 v[84:87], v[140:143], v[104:107], v[84:87]\n\t"
    "ds_read_b128 v[100:103], %[ea] offset:10240\n\t"
    "global_load_lds_dwordx4 %[av2], %[s0] sc1\n\t"
    "v_mfma_f32_16x16x32_bf16 v[88:91], v[136:139], v[108:111], v[88:91]\n\t"
    "v_mfma_f32_16x16x32_bf16 v[92:95], v[140:143], v[108:111], v[92:95]\n\t"
    "s_waitcnt lgkmcnt(4)\n\t"
    "v_mfma_f32_16x16x32_bf16 v[80:83], v[144:147], v[112:115], v[80:83]\n\t"
    "ds_read_b128 v[104:107], %[da] offset:8192\n\t"
    "s_add_u32 m0, %[lb], 8192\n\t"
    "v_mfma_f32_16x16x32_bf16 v[84:87], v[148:151], v[112:115], v[84:87]\n\t"
    "ds_read_b128 v[108:111], %[da] offset:10240\n\t"
    "global_load_lds_dwordx4 %[av4], %[s0] sc1\n\t"
    "v_mfma_f32_16x16x32_bf16 v[88:91], v[144:147], v[116:119], v[88:91]\n\t"
    "v_mfma_f32_16x16x32_bf16 v[92:95], v[148:151], v[116:119], v[92:95]\n\t"
    "s_waitcnt lgkmcnt(4)\n\t"
    "v_mfma_f32_16x16x32_bf16 v[80:83], v[152:155], v[120:123], v[80:83]\n\t"
    "ds_read_b128 v[112:115], %[ea] offset:12288\n\t"
    "s_add_u32 m0, %[lb], 12288\n\t"
    "v_mfma_f32_16x16x32_bf16 v[84:87], v[156:159], v[120:123], v[84:87]\n\t"
    "ds_read_b128 v[116:119], %[ea] offset:14336\n\t"
    "global_load_lds_dwordx4 %[av6], %[s0] sc1\n\t"
    "v_mfma_f32_16x16x32_bf16 v[88:91], v[152:155], v[124:127], v[88:91]\n\t"
    "v_mfma_f32_16x16x32_bf16 v[92:95], v[156:159], v[124:127], v[92:95]\n\t"
    "s_waitcnt lgkmcnt(4)\n\t"
    "v_mfma_f32_16x16x32_bf16 v[80:83], v[160:163], v[96:99], v[80:83]\n\t"
    "ds_read_b128 v[120:123], %[da] offset:12288\n\t"
    "s_add_u32 m0, %[lb], 16384\n\t"
    "v_mfma_f32_16x16x32_bf16 v[84:87], v[164:167], v[96:99], v[84:87]\n\t"
    "ds_read_b128 v[124:127], %[da] offset:14336\n\t"
    "global_load_lds_dwordx4 %[av0], %[s1] sc1\n\t"
    "v_mfma_f32_16x16x32_bf16 v[88:91], v[160:163], v[100:103], v[88:91]\n\t"
    "v_mfma_f32_16x16x32_bf16 v[92:95], v[164:167], v[100:103], v[92:95]\n\t"
    "s_waitcnt vmcnt(7)\n\t"
    "s_barrier\n\t"
    "s_waitcnt lgkmcnt(4)\n\t"
    "v_mfma_f32_16x16x32_bf16 v[80:83], v[168:171], v[104:107], v[80:83]\n\t"
    "ds_read_b128 v[96:99], %[ea] offset:16384\n\t"
    "s_add_u32 m0, %[lb], 20480\n\t"
    "v_mfma_f32_16x16x32_bf16 v[84:87], v[172:175], v[104:107], v[84:87]\n\t"
    "ds_read_b128 v[100:103], %[ea] offset:18432\n\t"
    "global_load_lds_dwordx4 %[av2], %[s1] sc1\n\t"
    "v_mfma_f32_16x16x32_bf16 v[88:91], v[168:171], v[108:111], v[88:91]\n\t"
    "v_mfma_f32_16x16x32_bf16 v[92:95], v[172:175], v[108:111], v[92:95]\n\t"
    "s_waitcnt lgkmcnt(4)\n\t"
    "v_mfma_f32_16x16x32_bf16 v[80:83], v[176:179], v[112:115], v[80:83]\n\t"
    "ds_read_b128 v[104:107], %[da] offset:16384\n\t"
    "s_add_u32 m0, %[lb], 24576\n\t"
    "v_mfma_f32_16x16x32_bf16 v[84:87], v[180:183], v[112:115], v[84:87]\n\t"
    "ds_read_b128 v[108:111], %[da] offset:18432\n\t"
    "global_load_lds_dwordx4 %[av4], %[s1] sc1\n\t"
    "v_mfma_f32_16x16x32_bf16 v[88:91], v[176:179], v[116:119], v[88:91]\n\t"
    "v_mfma_f32_16x16x32_bf16 v[92:95], v[180:183], v[116:119], v[92:95]\n\t"
    "s_waitcnt lgkmcnt(4)\n\t"
    "v_mfma_f32_16x16x32_bf16 v[80:83], v[184:187], v[120:123], v[80:83]\n\t"
    "ds_read_b128 v[112:115], %[ea] offset:20480\n\t"
    "s_add_u32 m0, %[lb], 28672\n\t"
    "v_mfma_f32_16x16x32_bf16 v[84:87], v[188:191], v[120:123], v[84:87]\n\t"
    "ds_read_b128 v[116:119], %[ea] offset:22528\n\t"
    "global_load_lds_dwordx4 %[av6], %[s1] sc1\n\t"
    "v_mfma_f32_16x16x32_bf16 v[88:91], v[184:187], v[124:127], v[88:91]\n\t"
    "v_mfma_f32_16x16x32_bf16 v[92:95], v[188:191], v[124:127], v[92:95]\n\t"
    "s_waitcnt lgkmcnt(4)\n\t"
    "v_mfma_f32_16x16x32_bf16 v[80:83], v[192:195], v[96:99], v[80:83]\n\t"
    "ds_read_b128 v[120:123], %[da] offset:20480\n\t"
    "v_mfma_f32_16x16x32_bf16 v[84:87], v[196:199], v[96:99], v[84:87]\n\t"
    "ds_read_b128 v[124:127], %[da] offset:22528\n\t"
    "v_mfma_f32_16x16x32_bf16 v[88:91], v[192:195], v[100:103], v[88:91]\n\t"
    "v_mfma_f32_16x16x32_bf16 v[92:95], v[196:199], v[100:103], v[92:95]\n\t"
    "s_waitcnt vmcnt(8)\n\t"
    "s_barrier\n\t"
    "s_waitcnt lgkmcnt(4)\n\t"
    "v_mfma_f32_16x16x32_bf16 v[80:83], v[200:203], v[104:107], v[80:83]\n\t"
    "ds_read_b128 v[96:99], %[ea] offset:24576\n\t"
    "v_mfma_f32_16x16x32_bf16 v[84:87], v[204:207], v[104:107], v[84:87]\n\t"
    "ds_read_b128 v[100:103], %[ea] offset:26624\n\t"
    "v_mfma_f32_16x16x32_bf16 v[88:91], v[200:203], v[108:111], v[88:91]\n\t"
    "v_mfma_f32_16x16x32_bf16 v[92:95], v[204:207], v[108:111], v[92:95]\n\t"
    "s_waitcnt lgkmcnt(4)\n\t"
    "v_mfma_f32_16x16x32_bf16 v[80:83], v[208:211], v[112:115], v[80:83]\n\t"
    "ds_read_b128 v[104:107], %[da] offset:24576\n\t"
    "v_mfma_f32_16x16x32_bf16 v[84:87], v[212:215], v[112:115], v[84:87]\n\t"
    "ds_read_b128 v[108:111], %[da] offset:26624\n\t"
    "v_mfma_f32_16x16x32_bf16 v[88:91], v[208:211], v[116:119], v[88:91]\n\t"
    "v_mfma_f32_16x16x32_bf16 v[92:95], v[212:215], v[116:119], v[92:95]\n\t"
    "s_waitcnt lgkmcnt(4)\n\t"
    "v_mfma_f32_16x16x32_bf16 v[80:83], v[216:219], v[120:123], v[80:83]\n\t"
    "ds_read_b128 v[112:115], %[ea] offset:28672\n\t"
    "v_mfma_f32_16x16x32_bf16 v[84:87], v[220:223], v[120:123], v[84:87]\n\t"
    "ds_read_b128 v[116:119], %[ea] offset:30720\n\t"
    "v_mfma_f32_16x16x32_bf16 v[88:91], v[216:219], v[124:127], v[88:91]\n\t"
    "v_mfma_f32_16x16x32_bf16 v[92:95], v[220:223], v[124:127], v[92:95]\n\t"
    "s_waitcnt lgkmcnt(4)\n\t"
    "v_mfma_f32_16x16x32_bf16 v[80:83], v[224:227], v[96:99], v[80:83]\n\t"
    "ds_read_b128 v[120:123], %[da] offset:28672\n\t"
    "v_mfma_f32_16x16x32_bf16 v[84:87], v[228:231], v[96:99], v[84:87]\n\t"
    "ds_read_b128 v[124:127], %[da] offset:30720\n\t"
    "v_mfma_f32_16x16x32_bf16 v[88:91], v[224:227], v[100:103], v[88:91]\n\t"
    "v_mfma_f32_16x16x32_bf16 v[92:95], v[228:231], v[100:103], v[92:95]\n\t"
    "s_waitcnt lgkmcnt(4)\n\t"
    "v_mfma_f32_16x16x32_bf16 v[80:83], v[232:235], v[104:107], v[80:83]\n\t"
    "v_mfma_f32_16x16x32_bf16 v[84:87], v[236:239], v[104:107], v[84:87]\n\t"
    "v_mfma_f32_16x16x32_bf16 v[88:91], v[232:235], v[108:111], v[88:91]\n\t"
    "v_mfma_f32_16x16x32_bf16 v[92:95], v[236:239], v[108:111], v[92:95]\n\t"
    "s_waitcnt lgkmcnt(2)\n\t"
    "v_mfma_f32_16x16x32_bf16 v[80:83], v[240:243], v[112:115], v[80:83]\n\t"
    "v_mfma_f32_16x16x32_bf16 v[84:87], v[244:247], v[112:115], v[84:87]\n\t"
    "v_mfma_f32_16x16x32_bf16 v[88:91], v[240:243], v[116:119], v[88:91]\n\t"
    "v_mfma_f32_16x16x32_bf16 v[92:95], v[244:247], v[116:119], v[92:95]\n\t"
    "s_waitcnt lgkmcnt(0)\n\t"
    "v_mfma_f32_16x16x32_bf16 v[80:83], v[248:251], v[120:123], v[80:83]\n\t"
    "v_mfma_f32_16x16x32_bf16 v[84:87], v[252:255], v[120:123], v[84:87]\n\t"
    "v_mfma_f32_16x16x32_bf16 v[88:91], v[248:251], v[124:127], v[88:91]\n\t"
    "v_mfma_f32_16x16x32_bf16 v[92:95], v[252:255], v[124:127], v[92:95]\n\t"
    "s_nop 6\n\t"
    "s_branch 9f\n\t"
    "3:\n\t"
    "9:"
    : "=&{v[80:95]}"(pd)
    : [ea] "v"(ea), [da] "v"(da), [x] "s"(x), "{v[128:159]}"(W[0]), "{v[160:191]}"(W[1]), "{v[192:223]}"(W[2]), "{v[224:255]}"(W[3]), [lb] "s"(lb), [s0] "s"(s0), [s1] "s"(s1), [av0] "v"(av[0]), [av1] "v"(av[1]), [av2] "v"(av[2]), [av3] "v"(av[3]), [av4] "v"(av[4]), [av5] "v"(av[5]), [av6] "v"(av[6]), [av7] "v"(av[7])
    : "v96", "v97", "v98", "v99", "v100", "v101", "v102", "v103", "v104", "v105", "v106", "v107", "v108", "v109", "v110", "v111", "v112", "v113", "v114", "v115", "v116", "v117", "v118", "v119", "v120", "v121", "v122", "v123", "v124", "v125", "v126", "v127", "vcc", "scc", "m0", "memory");
}
__device__ __forceinline__ void lean_st0(const u32x32 (&W)[4], u32x16& pd, unsigned ea, unsigned da, unsigned x, unsigned pv, unsigned cn, unsigned sl, unsigned dv, const char* db, const char* pg) {
  unsigned st;
  asm volatile("ds_read_b128 v[96:99], %[ea] offset:0\n\t"
    "ds_read_b128 v[100:103], %[ea] offset:2048\n\t"
    "ds_read_b128 v[104:107], %[da] offset:0\n\t"
    "ds_read_b128 v[108:111], %[da] offset:2048\n\t"
    "ds_read_b128 v[112:115], %[ea] offset:4096\n\t"
    "ds_read_b128 v[116:119], %[ea] offset:6144\n\t"
    "s_waitcnt lgkmcnt(4)\n\t"
    "v_mfma_f32_16x16x32_bf16 v[80:83], v[128:131], v[96:99], 0\n\t"
    "ds_read_b128 v[120:123], %[da] offset:4096\n\t"
    "v_mfma_f32_16x16x32_bf16 v[84:87], v[132:135], v[96:99], 0\n\t"
    "ds_read_b128 v[124:127], %[da] offset:6144\n\t"
    "v_mfma_f32_16x16x32_bf16 v[88:91], v[128:131], v[100:103], 0\n\t"
    "v_mfma_f32_16x16x32_bf16 v[92:95], v[132:135], v[100:103], 0\n\t"
    "s_barrier\n\t"
    "s_waitcnt lgkmcnt(4)\n\t"
    "v_mfma_f32_16x16x32_bf16 v[80:83], v[136:139], v[104:107], v[80:83]\n\t"
    "ds_read_b128 v[96:99], %[ea] offset:8192\n\t"
    "v_mfma_f32_16x16x32_bf16 v[84:87], v[140:143], v[104:107], v[84:87]\n\t"
    "ds_read_b128 v[100:103], %[ea] offset:10240\n\t"
    "v_mfma_f32_16x16x32_bf16 v[88:91], v[136:139], v[108:111], v[88:91]\n\t"
    "v_mfma_f32_16x16x32_bf16 v[92:95], v[140:143], v[108:111], v[92:95]\n\t"
    "s_waitcnt lgkmcnt(4)\n\t"
    "v_mfma_f32_16x16x32_bf16 v[80:83], v[144:147], v[112:115], v[80:83]\n\t"
    "ds_read_b128 v[104:107], %[da] offset:8192\n\t"
    "v_mfma_f32_16x16x32_bf16 v[84:87], v[148:151], v[112:115], v[84:87]\n\t"
    "ds_read_b128 v[108:111], %[da] offset:10240\n\t"
    "v_mfma_f32_16x16x32_bf16 v[88:91], v[144:147], v[116:119], v[88:91]\n\t"
    "v_mfma_f32_16x16x32_bf16 v[92:95], v[148:151], v[116:119], v[92:95]\n\t"
    "s_waitcnt lgkmcnt(4)\n\t"
    "v_mfma_f32_16x16x32_bf16 v[80:83], v[152:155], v[120:123], v[80:83]\n\t"
    "ds_read_b128 v[112:115], %[ea] offset:12288\n\t"
    "v_mfma_f32_16x16x32_bf16 v[84:87], v[156:159], v[120:123], v[84:87]\n\t"
    "ds_read_b128 v[116:119], %[ea] offset:14336\n\t"
    "v_mfma_f32_16x16x32_bf16 v[88:91], v[152:155], v[124:127], v[88:91]\n\t"
    "v_mfma_f32_16x16x32_bf16 v[92:95], v[156:159], v[124:127], v[92:95]\n\t"
    "s_bitcmp1_b32 %[x], 2\n\t"
    "s_cbranch_scc1 7f\n\t"
    "8:\n\t"
    "s_waitcnt lgkmcnt(4)\n\t"
    "v_mfma_f32_16x16x32_bf16 v[80:83], v[160:163], v[96:99], v[80:83]\n\t"
    "ds_read_b128 v[120:123], %[da] offset:12288\n\t"
    "v_mfma_f32_16x16x32_bf16 v[84:87], v[164:167], v[96:99], v[84:87]\n\t"
    "ds_read_b128 v[124:127], %[da] offset:14336\n\t"
    "v_mfma_f32_16x16x32_bf16 v[88:91], v[160:163], v[100:103], v[88:91]\n\t"
    "v_mfma_f32_16x16x32_bf16 v[92:95], v[164:167], v[100:103], v[92:95]\n\t"
    "s_barrier\n\t"
    "s_waitcnt lgkmcnt(4)\n\t"
    "v_mfma_f32_16x16x32_bf16 v[80:83], v[168:171], v[104:107], v[80:83]\n\t"
    "ds_read_b128 v[96:99], %[ea] offset:16384\n\t"
    "v_mfma_f32_16x16x32_bf16 v[84:87], v[172:175], v[104:107], v[84:87]\n\t"
    "ds_read_b128 v[100:103], %[ea] offset:18432\n\t"
    "v_mfma_f32_16x16x32_bf16 v[88:91], v[168:171], v[108:111], v[88:91]\n\t"
    "v_mfma_f32_16x16x32_bf16 v[92:95], v[172:175], v[108:111], v[92:95]\n\t"
    "s_waitcnt lgkmcnt(4)\n\t"
    "v_mfma_f32_16x16x32_bf16 v[80:83], v[176:179], v[112:115], v[80:83]\n\t"
    "ds_read_b128 v[104:107], %[da] offset:16384\n\t"
    "v_mfma_f32_16x16x32_bf16 v[84:87], v[180:183], v[112:115], v[84:87]\n\t"
    "ds_read_b128 v[108:111], %[da] offset:18432\n\t"
    "v_mfma_f32_16x16x32_bf16 v[88:91], v[176:179], v[116:119], v[88:91]\n\t"
    "v_mfma_f32_16x16x32_bf16 v[92:95], v[180:183], v[116:119], v[92:95]\n\t"
    "s_waitcnt lgkmcnt(4)\n\t"
    "v_mfma_f32_16x16x32_bf16 v[80:83], v[184:187], v[120:123], v[80:83]\n\t"
    "ds_read_b128 v[112:115], %[ea] offset:20480\n\t"
    "v_mfma_f32_16x16x32_bf16 v[84:87], v[188:191], v[120:123], v[84:87]\n\t"
    "ds_read_b128 v[116:119], %[ea] offset:22528\n\t"
    "v_mfma_f32_16x16x32_bf16 v[88:91], v[184:187], v[124:127], v[88:91]\n\t"
    "v_mfma_f32_16x16x32_bf16 v[92:95], v[188:191], v[124:127], v[92:95]\n\t"
    "s_waitcnt lgkmcnt(4)\n\t"
    "v_mfma_f32_16x16x32_bf16 v[80:83], v[192:195], v[96:99], v[80:83]\n\t"
    "ds_read_b128 v[120:123], %[da] offset:20480\n\t"
    "v_mfma_f32_16x16x32_bf16 v[84:87], v[196:199], v[96:99], v[84:87]\n\t"
    "ds_read_b128 v[124:127], %[da] offset:22528\n\t"
    "v_mfma_f32_16x16x32_bf16 v[88:91], v[192:195], v[100:103], v[88:91]\n\t"
    "v_mfma_f32_16x16x32_bf16 v[92:95], v[196:199], v[100:103], v[92:95]\n\t"
    "s_barrier\n\t"
    "s_and_b32 %[st], %[x], 3\n\t"
    "s_cbranch_scc1 3f\n\t"
    "4:\n\t"
    "s_waitcnt lgkmcnt(4)\n\t"
    "v_mfma_f32_16x16x32_bf16 v[80:83], v[200:203], v[104:107], v[80:83]\n\t"
    "ds_read_b128 v[96:99], %[ea] offset:24576\n\t"
    "v_mfma_f32_16x16x32_bf16 v[84:87], v[204:207], v[104:107], v[84:87]\n\t"
    "ds_read_b128 v[100:103], %[ea] offset:26624\n\t"
    "v_mfma_f32_16x16x32_bf16 v[88:91], v[200:203], v[108:111], v[88:91]\n\t"
    "v_mfma_f32_16x16x32_bf16 v[92:95], v[204:207], v[108:111], v[92:95]\n\t"
    "s_waitcnt lgkmcnt(4)\n\t"
    "v_mfma_f32_16x16x32_bf16 v[80:83], v[208:211], v[112:115], v[80:83]\n\t"
    "ds_read_b128 v[104:107], %[da] offset:24576\n\t"
    "v_mfma_f32_16x16x32_bf16 v[84:87], v[212:215], v[112:115], v[84:87]\n\t"
    "ds_read_b128 v[108:111], %[da] offset:26624\n\t"
    "v_mfma_f32_16x16x32_bf16 v[88:91], v[208:211], v[116:119], v[88:91]\n\t"
    "v_mfma_f32_16x16x32_bf16 v[92:95], v[212:215], v[116:119], v[92:95]\n\t"
    "s_waitcnt lgkmcnt(4)\n\t"
    "v_mfma_f32_16x16x32_bf16 v[80:83], v[216:219], v[120:123], v[80:83]\n\t"
    "ds_read_b128 v[112:115], %[ea] offset:28672\n\t"
    "v_mfma_f32_16x16x32_bf16 v[84:87], v[220:223], v[120:123], v[84:87]\n\t"
    "ds_read_b128 v[116:119], %[ea] offset:30720\n\t"
    "v_mfma_f32_16x16x32_bf16 v[88:91], v[216:219], v[124:127], v[88:91]\n\t"
    "v_mfma_f32_16x16x32_bf16 v[92:95], v[220:223], v[124:127], v[92:95]\n\t"
    "s_waitcnt lgkmcnt(4)\n\t"
    "v_mfma_f32_16x16x32_bf16 v[80:83], v[224:227], v[96:99], v[80:83]\n\t"
    "ds_read_b128 v[120:123], %[da] offset:28672\n\t"
    "v_mfma_f32_16x16x32_bf16 v[84:87], v[228:231], v[96:99], v[84:87]\n\t"
    "ds_read_b128 v[124:127], %[da] offset:30720\n\t"
    "v_mfma_f32_16x16x32_bf16 v[88:91], v[224:227], v[100:103], v[88:91]\n\t"
    "v_mfma_f32_16x16x32_bf16 v[92:95], v[228:231], v[100:103], v[92:95]\n\t"
    "s_waitcnt lgkmcnt(4)\n\t"
    "v_mfma_f32_16x16x32_bf16 v[80:83], v[232:235], v[104:107], v[80:83]\n\t"
    "v_mfma_f32_16x16x32_bf16 v[84:87], v[236:239], v[104:107], v[84:87]\n\t"
    "v_mfma_f32_16x16x32_bf16 v[88:91], v[232:235], v[108:111], v[88:91]\n\t"
    "v_mfma_f32_16x16x32_bf16 v[92:95], v[236:239], v[108:111], v[92:95]\n\t"
    "s_waitcnt lgkmcnt(2)\n\t"
    "v_mfma_f32_16x16x32_bf16 v[80:83], v[240:243], v[112:115], v[80:83]\n\t"
    "v_mfma_f32_16x16x32_bf16 v[84:87], v[244:247], v[112:115], v[84:87]\n\t"
    "v_mfma_f32_16x16x32_bf16 v[88:91], v[240:243], v[116:119], v[88:91]\n\t"
    "v_mfma_f32_16x16x32_bf16 v[92:95], v[244:247], v[116:119], v[92:95]\n\t"
    "s_waitcnt lgkmcnt(0)\n\t"
    "v_mfma_f32_16x16x32_bf16 v[80:83], v[248:251], v[120:123], v[80:83]\n\t"
    "v_mfma_f32_16x16x32_bf16 v[84:87], v[252:255], v[120:123], v[84:87]\n\t"
    "v_mfma_f32_16x16x32_bf16 v[88:91], v[248:251], v[124:127], v[88:91]\n\t"
    "v_mfma_f32_16x16x32_bf16 v[92:95], v[252:255], v[124:127], v[92:95]\n\t"
    "s_nop 6\n\t"
    "s_branch 9f\n\t"
    "3:\n\t"
    "s_bitcmp0_b32 %[x], 0\n\t"
    "s_cbranch_scc1 5f\n\t"
    "7:\n\t"
    "v_mov_b32 v76, %[cn]\n\t"
    "v_mov_b32 v77, 1\n\t"
    "v_mov_b32 v78, %[pv]\n\t"
    "v_mov_b32 v79, 0\n\t"
    "s_waitcnt vmcnt(0)\n\t"
    "s_mov_b64 exec, 1\n\t"
    "ds_add_rtn_u32 v77, v76, v77\n\t"
    "s_waitcnt lgkmcnt(0)\n\t"
    "v_readfirstlane_b32 %[st], v77\n\t"
    "s_and_b32 %[st], %[st], 3\n\t"
    "s_cmp_lg_u32 %[st], 3\n\t"
    "s_cbranch_scc1 6f\n\t"
    "global_store_dword v79, v78, %[pg] sc1\n\t"
    "6:\n\t"
    "s_mov_b64 exec, -1\n\t"
    "s_bitcmp1_b32 %[x], 2\n\t"
    "s_cbranch_scc1 8b\n\t"
    "5:\n\t"
    "s_bitcmp0_b32 %[x], 1\n\t"
    "s_cbranch_scc1 4b\n\t"
    "s_mov_b32 m0, %[sl]\n\t"
    "s_nop 0\n\t"
    "global_load_lds_dword %[dv], %[db] sc1\n\t"
    "s_branch 4b\n\t"
    "9:"
    : "=&{v[80:95]}"(pd), [st] "=&s"(st)
    : [ea] "v"(ea), [da] "v"(da), [x] "s"(x), "{v[128:159]}"(W[0]), "{v[160:191]}"(W[1]), "{v[192:223]}"(W[2]), "{v[224:255]}"(W[3]), [pv] "s"(pv), [cn] "s"(cn), [sl] "s"(sl), [dv] "v"(dv), [db] "s"(db), [pg] "s"(pg)
    : "v96", "v97", "v98", "v99", "v100", "v101", "v102", "v103", "v104", "v105", "v106", "v107", "v108", "v109", "v110", "v111", "v112", "v113", "v114", "v115", "v116", "v117", "v118", "v119", "v120", "v121", "v122", "v123", "v124", "v125", "v126", "v127", "v76", "v77", "v78", "v79", "vcc", "scc", "m0", "memory");
}
#define LEAN_QB 1
__device__ __forceinline__ void paced_ld0(const u32x32 (&W)[4], unsigned ea, unsigned da, int b, int ns, unsigned ab, unsigned rw, unsigned fa,
    const char* p0, const char* p1, unsigned lb, int& cap, int wm, unsigned rq, unsigned mu, unsigned ad, unsigned dv, const char* db, unsigned sl, unsigned sa, const unsigned (&av)[8]) {
  unsigned st;
  asm volatile("s_setprio 2\n\t"
    "v_mov_b32 v77, %[wm]\n\t"
    "s_cmp_lg_u32 %[rq], 0\n\t"
    "s_cbranch_scc1 3f\n\t"
    "2:\n\t"
    "v_cmp_ge_i32 vcc, %[b], v77\n\t"
    "s_cbranch_vccz 4f\n\t"
    "21:\n\t"
    "s_cmp_eq_u32 %[sl], 0\n\t"
    "s_cbranch_scc1 71f\n\t"
    "s_mov_b32 m0, %[sl]\n\t"
    "s_nop 0\n\t"
    "global_load_lds_dword %[dv], %[db] sc1\n\t"
    "71:\n\t"
    "s_sub_u32 %[cap], %[cap], 1\n\t"
    "s_max_i32 %[cap], %[cap], 0\n\t"
    "s_waitcnt vmcnt(0)\n\t"
    "ds_read2_b32 v[78:79], %[sa] offset1:32\n\t"
    "s_waitcnt lgkmcnt(0)\n\t"
    "v_max_u32 v78, v78, v79\n\t"
    "v_mad_u32_u24 v78, v78, %[mu], %[ad]\n\t"
    "v_max_i32 v77, v77, v78\n\t"
    "v_cmp_ge_i32 vcc, %[b], v77\n\t"
    "s_cbranch_vccz 4f\n\t"
    "s_sleep 1\n\t"
    "s_cmp_lg_u32 %[cap], 0\n\t"
    "s_cbranch_scc1 21b\n\t"
    "4:\n\t"
    "s_add_u32 m0, %[lb], 0\n\t"
    "s_nop 0\n\t"
    "global_load_lds_dwordx4 %[av0], s[36:37] sc1\n\t"
    "s_add_u32 m0, %[lb], 32768\n\t"
    "s_nop 0\n\t"
    "global_load_lds_dwordx4 %[av1], s[36:37] sc1\n\t"
    "s_add_u32 m0, %[lb], 4096\n\t"
    "s_nop 0\n\t"
    "global_load_lds_dwordx4 %[av2], s[36:37] sc1\n\t"
    "s_add_u32 m0, %[lb], 36864\n\t"
    "s_nop 0\n\t"
    "global_load_lds_dwordx4 %[av3], s[36:37] sc1\n\t"
    "s_add_u32 m0, %[lb], 8192\n\t"
    "s_nop 0\n\t"
    "global_load_lds_dwordx4 %[av4], s[36:37] sc1\n\t"
    "s_add_u32 m0, %[lb], 40960\n\t"
    "s_nop 0\n\t"
    "global_load_lds_dwordx4 %[av5], s[36:37] sc1\n\t"
    "s_add_u32 m0, %[lb], 12288\n\t"
    "s_nop 0\n\t"
    "global_load_lds_dwordx4 %[av6], s[36:37] sc1\n\t"
    "s_add_u32 m0, %[lb], 45056\n\t"
    "s_nop 0\n\t"
    "global_load_lds_dwordx4 %[av7], s[36:37] sc1\n\t"
    "s_add_u32 m0, %[lb], 16384\n\t"
    "s_nop 0\n\t"
    "global_load_lds_dwordx4 %[av0], s[38:39] sc1\n\t"
    "s_add_u32 m0, %[lb], 49152\n\t"
    "s_nop 0\n\t"
    "global_load_lds_dwordx4 %[av1], s[38:39] sc1\n\t"
    "s_add_u32 m0, %[lb], 20480\n\t"
    "s_nop 0\n\t"
    "global_load_lds_dwordx4 %[av2], s[38:39] sc1\n\t"
    "s_add_u32 m0, %[lb], 53248\n\t"
    "s_nop 0\n\t"
    "global_load_lds_dwordx4 %[av3], s[38:39] sc1\n\t"
    "s_add_u32 m0, %[lb], 24576\n\t"
    "s_nop 0\n\t"
    "global_load_lds_dwordx4 %[av4], s[38:39] sc1\n\t"
    "s_add_u32 m0, %[lb], 57344\n\t"
    "s_nop 0\n\t"
    "global_load_lds_dwordx4 %[av5], s[38:39] sc1\n\t"
    "s_add_u32 m0, %[lb], 28672\n\t"
    "s_nop 0\n\t"
    "global_load_lds_dwordx4 %[av6], s[38:39] sc1\n\t"
    "s_add_u32 m0, %[lb], 61440\n\t"
    "s_nop 0\n\t"
    "global_load_lds_dwordx4 %[av7], s[38:39] sc1\n\t"
    "s_add_u32 s36, s36, %[ab]\n\t"
    "s_addc_u32 s37, s37, 0\n\t"
    "s_add_u32 s38, s38, %[ab]\n\t"
    "s_addc_u32 s39, s39, 0\n\t"
    "s_xor_b32 %[lb], %[lb], 0x10000\n\t"
    "3:\n\t"
    "s_add_u32 %[st], %[b], 1\n\t"
    "v_min_i32 v77, %[ns], v77\n\t"
    "v_cmp_ge_i32 vcc, %[st], v77\n\t"
    "s_waitcnt vmcnt(12) lgkmcnt(0)\n\t"
    "s_barrier\n\t"
    "ds_read_b32 v75, %[fa] offset:16\n\t"
    "ds_read_b128 v[96:99], %[ea] offset:0\n\t"
    "ds_read_b128 v[100:103], %[ea] offset:2048\n\t"
    "ds_read_b128 v[104:107], %[da] offset:0\n\t"
    "ds_read_b128 v[108:111], %[da] offset:2048\n\t"
    "ds_read_b128 v[112:115], %[ea] offset:4096\n\t"
    "ds_read_b128 v[116:119], %[ea] offset:6144\n\t"
    "s_cbranch_vccz 5f\n\t"
    "s_waitcnt lgkmcnt(4)\n\t"
    "v_mfma_f32_16x16x32_bf16 v[80:83], v[128:131], v[96:99], 0\n\t"
    "ds_read_b128 v[120:123], %[da] offset:4096\n\t"
    "v_mfma_f32_16x16x32_bf16 v[84:87], v[132:135], v[96:99], 0\n\t"
    "ds_read_b128 v[124:127], %[da] offset:6144\n\t"
    "v_readfirstlane_b32 %[st], v75\n\t"
    "v_mfma_f32_16x16x32_bf16 v[88:91], v[128:131], v[100:103], 0\n\t"
    "v_mfma_f32_16x16x32_bf16 v[92:95], v[132:135], v[100:103], 0\n\t"
    "s_cmp_lg_u32 %[st], 0\n\t"
    "s_cbranch_scc1 9f\n\t"
    "s_waitcnt vmcnt(8)\n\t"
    "s_barrier\n\t"
    "s_waitcnt lgkmcnt(4)\n\t"
    "v_mfma_f32_16x16x32_bf16 v[80:83], v[136:139], v[104:107], v[80:83]\n\t"
    "ds_read_b128 v[96:99], %[ea] offset:8192\n\t"
    "v_mfma_f32_16x16x32_bf16 v[84:87], v[140:143], v[104:107], v[84:87]\n\t"
    "ds_read_b128 v[100:103], %[ea] offset:10240\n\t"
    "v_mfma_f32_16x16x32_bf16 v[88:91], v[136:139], v[108:111], v[88:91]\n\t"
    "v_mfma_f32_16x16x32_bf16 v[92:95], v[140:143], v[108:111], v[92:95]\n\t"
    "s_waitcnt lgkmcnt(4)\n\t"
    "v_mfma_f32_16x16x32_bf16 v[80:83], v[144:147], v[112:115], v[80:83]\n\t"
    "ds_read_b128 v[104:107], %[da] offset:8192\n\t"
    "v_mfma_f32_16x16x32_bf16 v[84:87], v[148:151], v[112:115], v[84:87]\n\t"
    "ds_read_b128 v[108:111], %[da] offset:10240\n\t"
    "v_mfma_f32_16x16x32_bf16 v[88:91], v[144:147], v[116:119], v[88:91]\n\t"
    "v_mfma_f32_16x16x32_bf16 v[92:95], v[148:151], v[116:119], v[92:95]\n\t"
    "s_waitcnt lgkmcnt(4)\n\t"
    "v_mfma_f32_16x16x32_bf16 v[80:83], v[152:155], v[120:123], v[80:83]\n\t"
    "ds_read_b128 v[112:115], %[ea] offset:12288\n\t"
    "v_mfma_f32_16x16x32_bf16 v[84:87], v[156:159], v[120:123], v[84:87]\n\t"
    "ds_read_b128 v[116:119], %[ea] offset:14336\n\t"
    "v_mfma_f32_16x16x32_bf16 v[88:91], v[152:155], v[124:127], v[88:91]\n\t"
    "v_mfma_f32_16x16x32_bf16 v[92:95], v[156:159], v[124:127], v[92:95]\n\t"
    "s_waitcnt lgkmcnt(4)\n\t"
    "v_mfma_f32_16x16x32_bf16 v[80:83], v[160:163], v[96:99], v[80:83]\n\t"
    "ds_read_b128 v[120:123], %[da] offset:12288\n\t"
    "v_mfma_f32_16x16x32_bf16 v[84:87], v[164:167], v[96:99], v[84:87]\n\t"
    "ds_read_b128 v[124:127], %[da] offset:14336\n\t"
    "v_mfma_f32_16x16x32_bf16 v[88:91], v[160:163], v[100:103], v[88:91]\n\t"
    "v_mfma_f32_16x16x32_bf16 v[92:95], v[164:167], v[100:103], v[92:95]\n\t"
    "s_waitcnt vmcnt(4)\n\t"
    "s_barrier\n\t"
    "s_waitcnt lgkmcnt(4)\n\t"
    "v_mfma_f32_16x16x32_bf16 v[80:83], v[168:171], v[104:107], v[80:83]\n\t"
    "ds_read_b128 v[96:99], %[ea] offset:16384\n\t"
    "v_mfma_f32_16x16x32_bf16 v[84:87], v[172:175], v[104:107], v[84:87]\n\t"
    "ds_read_b128 v[100:103], %[ea] offset:18432\n\t"
    "v_mfma_f32_16x16x32_bf16 v[88:91], v[168:171], v[108:111], v[88:91]\n\t"
    "v_mfma_f32_16x16x32_bf16 v[92:95], v[172:175], v[108:111], v[92:95]\n\t"
    "s_waitcnt lgkmcnt(4)\n\t"
    "v_mfma_f32_16x16x32_bf16 v[80:83], v[176:179], v[112:115], v[80:83]\n\t"
    "ds_read_b128 v[104:107], %[da] offset:16384\n\t"
    "v_mfma_f32_16x16x32_bf16 v[84:87], v[180:183], v[112:115], v[84:87]\n\t"
    "ds_read_b128 v[108:111], %[da] offset:18432\n\t"
    "v_mfma_f32_16x16x32_bf16 v[88:91], v[176:179], v[116:119], v[88:91]\n\t"
    "v_mfma_f32_16x16x32_bf16 v[92:95], v[180:183], v[116:119], v[92:95]\n\t"
    "s_waitcnt lgkmcnt(4)\n\t"
    "v_mfma_f32_16x16x32_bf16 v[80:83], v[184:187], v[120:123], v[80:83]\n\t"
    "ds_read_b128 v[112:115], %[ea] offset:20480\n\t"
    "v_mfma_f32_16x16x32_bf16 v[84:87], v[188:191], v[120:123], v[84:87]\n\t"
    "ds_read_b128 v[116:119], %[ea] offset:22528\n\t"
    "v_mfma_f32_16x16x32_bf16 v[88:91], v[184:187], v[124:127], v[88:91]\n\t"
    "v_mfma_f32_16x16x32_bf16 v[92:95], v[188:191], v[124:127], v[92:95]\n\t"
    "s_waitcnt lgkmcnt(4)\n\t"
    "v_mfma_f32_16x16x32_bf16 v[80:83], v[192:195], v[96:99], v[80:83]\n\t"
    "ds_read_b128 v[120:123], %[da] offset:20480\n\t"
    "v_mfma_f32_16x16x32_bf16 v[84:87], v[196:199], v[96:99], v[84:87]\n\t"
    "ds_read_b128 v[124:127], %[da] offset:22528\n\t"
    "v_mfma_f32_16x16x32_bf16 v[88:91], v[192:195], v[100:103], v[88:91]\n\t"
    "v_mfma_f32_16x16x32_bf16 v[92:95], v[196:199], v[100:103], v[92:95]\n\t"
    "s_waitcnt vmcnt(0)\n\t"
    "s_barrier\n\t"
    "s_waitcnt lgkmcnt(4)\n\t"
    "v_mfma_f32_16x16x32_bf16 v[80:83], v[200:203], v[104:107], v[80:83]\n\t"
    "ds_read_b128 v[96:99], %[ea] offset:24576\n\t"
    "v_mfma_f32_16x16x32_bf16 v[84:87], v[204:207], v[104:107], v[84:87]\n\t"
    "ds_read_b128 v[100:103], %[ea] offset:26624\n\t"
    "v_mfma_f32_16x16x32_bf16 v[88:91], v[200:203], v[108:111], v[88:91]\n\t"
    "v_mfma_f32_16x16x32_bf16 v[92:95], v[204:207], v[108:111], v[92:95]\n\t"
    "s_cmp_eq_u32 %[sl], 0\n\t"
    "s_cbranch_scc1 72f\n\t"
    "s_mov_b32 m0, %[sl]\n\t"
    "s_nop 0\n\t"
    "global_load_lds_dword %[dv], %[db] sc1\n\t"
    "72:\n\t"
    "s_waitcnt lgkmcnt(4)\n\t"
    "v_mfma_f32_16x16x32_bf16 v[80:83], v[208:211], v[112:115], v[80:83]\n\t"
    "ds_read_b128 v[104:107], %[da] offset:24576\n\t"
    "v_mfma_f32_16x16x32_bf16 v[84:87], v[212:215], v[112:115], v[84:87]\n\t"
    "ds_read_b128 v[108:111], %[da] offset:26624\n\t"
    "v_mfma_f32_16x16x32_bf16 v[88:91], v[208:211], v[116:119], v[88:91]\n\t"
    "v_mfma_f32_16x16x32_bf16 v[92:95], v[212:215], v[116:119], v[92:95]\n\t"
    "s_waitcnt lgkmcnt(4)\n\t"
    "v_mfma_f32_16x16x32_bf16 v[80:83], v[216:219], v[120:123], v[80:83]\n\t"
    "ds_read_b128 v[112:115], %[ea] offset:28672\n\t"
    "v_mfma_f32_16x16x32_bf16 v[84:87], v[220:223], v[120:123], v[84:87]\n\t"
    "ds_read_b128 v[116:119], %[ea] offset:30720\n\t"
    "v_mfma_f32_16x16x32_bf16 v[88:91], v[216:219], v[124:127], v[88:91]\n\t"
    "v_mfma_f32_16x16x32_bf16 v[92:95], v[220:223], v[124:127], v[92:95]\n\t"
    "s_waitcnt lgkmcnt(4)\n\t"
    "v_mfma_f32_16x16x32_bf16 v[80:83], v[224:227], v[96:99], v[80:83]\n\t"
    "ds_read_b128 v[120:123], %[da] offset:28672\n\t"
    "v_mfma_f32_16x16x32_bf16 v[84:87], v[228:231], v[96:99], v[84:87]\n\t"
    "ds_read_b128 v[124:127], %[da] offset:30720\n\t"
    "v_mfma_f32_16x16x32_bf16 v[88:91], v[224:227], v[100:103], v[88:91]\n\t"
    "v_mfma_f32_16x16x32_bf16 v[92:95], v[228:231], v[100:103], v[92:95]\n\t"
    "s_waitcnt lgkmcnt(4)\n\t"
    "v_mfma_f32_16x16x32_bf16 v[80:83], v[232:235], v[104:107], v[80:83]\n\t"
    "v_mfma_f32_16x16x32_bf16 v[84:87], v[236:239], v[104:107], v[84:87]\n\t"
    "v_mfma_f32_16x16x32_bf16 v[88:91], v[232:235], v[108:111], v[88:91]\n\t"
    "v_xor_b32 %[ea], 0x10000, %[ea]\n\t"
    "v_mfma_f32_16x16x32_bf16 v[92:95], v[236:239], v[108:111], v[92:95]\n\t"
    "v_xor_b32 %[da], 0x10000, %[da]\n\t"
    "s_waitcnt lgkmcnt(2)\n\t"
    "v_mfma_f32_16x16x32_bf16 v[80:83], v[240:243], v[112:115], v[80:83]\n\t"
    "v_mfma_f32_16x16x32_bf16 v[84:87], v[244:247], v[112:115], v[84:87]\n\t"
    "v_mfma_f32_16x16x32_bf16 v[88:91], v[240:243], v[116:119], v[88:91]\n\t"
    "s_add_u32 %[b], %[b], 1\n\t"
    "v_mfma_f32_16x16x32_bf16 v[92:95], v[244:247], v[116:119], v[92:95]\n\t"
    "s_waitcnt lgkmcnt(0)\n\t"
    "v_mfma_f32_16x16x32_bf16 v[80:83], v[248:251], v[120:123], v[80:83]\n\t"
    "v_mfma_f32_16x16x32_bf16 v[84:87], v[252:255], v[120:123], v[84:87]\n\t"
    "v_mfma_f32_16x16x32_bf16 v[88:91], v[248:251], v[124:127], v[88:91]\n\t"
    "v_mfma_f32_16x16x32_bf16 v[92:95], v[252:255], v[124:127], v[92:95]\n\t"
    "s_nop 4\n\t"
    "ds_write_b128 %[rw], v[80:83] offset:0\n\t"
    "ds_write_b128 %[rw], v[84:87] offset:1024\n\t"
    "ds_write_b128 %[rw], v[88:91] offset:2048\n\t"
    "ds_write_b128 %[rw], v[92:95] offset:3072\n\t"
    "s_waitcnt vmcnt(0)\n\t"
    "ds_read2_b32 v[78:79], %[sa] offset1:32\n\t"
    "s_waitcnt lgkmcnt(0)\n\t"
    "v_max_u32 v78, v78, v79\n\t"
    "v_mad_u32_u24 v78, v78, %[mu], %[ad]\n\t"
    "v_max_i32 v77, v77, v78\n\t"
    "v_mov_b32 v76, %[b]\n\t"
    "v_cmp_ge_i32 vcc, %[b], v77\n\t"
    "s_cmp_ge_i32 %[b], %[ns]\n\t"
    "s_cbranch_scc1 6f\n\t"
    "s_cbranch_vccnz 6f\n\t"
    "v_or_b32 v76, 0x100, v76\n\t"
    "ds_write_b32 %[fa], v76\n\t"
    "s_branch 4b\n\t"
    "6:\n\t"
    "ds_write_b32 %[fa], v76\n\t"
    "s_cbranch_scc0 21b\n\t"
    "s_waitcnt lgkmcnt(0)\n\t"
    "s_barrier\n\t"
    "s_branch 9f\n\t"
    "5:\n\t"
    "s_waitcnt lgkmcnt(4)\n\t"
    "v_mfma_f32_16x16x32_bf16 v[80:83], v[128:131], v[96:99], 0\n\t"
    "ds_read_b128 v[120:123], %[da] offset:4096\n\t"
    "s_add_u32 m0, %[lb], 0\n\t"
    "v_mfma_f32_16x16x32_bf16 v[84:87], v[132:135], v[96:99], 0\n\t"
    "ds_read_b128 v[124:127], %[da] offset:6144\n\t"
    "global_load_lds_dwordx4 %[av0], s[36:37] sc1\n\t"
    "v_readfirstlane_b32 %[st], v75\n\t"
    "v_mfma_f32_16x16x32_bf16 v[88:91], v[128:131], v[100:103], 0\n\t"
    "s_add_u32 m0, %[lb], 32768\n\t"
    "v_mfma_f32_16x16x32_bf16 v[92:95], v[132:135], v[100:103], 0\n\t"
    "global_load_lds_dwordx4 %[av1], s[36:37] sc1\n\t"
    "s_cmp_lg_u32 %[st], 0\n\t"
    "s_cbranch_scc1 9f\n\t"
    "s_waitcnt vmcnt(10)\n\t"
    "s_barrier\n\t"
    "s_waitcnt lgkmcnt(4)\n\t"
    "v_mfma_f32_16x16x32_bf16 v[80:83], v[136:139], v[104:107], v[80:83]\n\t"
    "ds_read_b128 v[96:99], %[ea] offset:8192\n\t"
    "s_add_u32 m0, %[lb], 4096\n\t"
    "v_mfma_f32_16x16x32_bf16 v[84:87], v[140:143], v[104:107], v[84:87]\n\t"
    "ds_read_b128 v[100:103], %[ea] offset:10240\n\t"
    "global_load_lds_dwordx4 %[av2], s[36:37] sc1\n\t"
    "v_mfma_f32_16x16x32_bf16 v[88:91], v[136:139], v[108:111], v[88:91]\n\t"
    "s_add_u32 m0, %[lb], 36864\n\t"
    "v_mfma_f32_16x16x32_bf16 v[92:95], v[140:143], v[108:111], v[92:95]\n\t"
    "global_load_lds_dwordx4 %[av3], s[36:37] sc1\n\t"
    "s_waitcnt lgkmcnt(4)\n\t"
    "v_mfma_f32_16x16x32_bf16 v[80:83], v[144:147], v[112:115], v[80:83]\n\t"
    "ds_read_b128 v[104:107], %[da] offset:8192\n\t"
    "s_add_u32 m0, %[lb], 8192\n\t"
    "v_mfma_f32_16x16x32_bf16 v[84:87], v[148:151], v[112:115], v[84:87]\n\t"
    "ds_read_b128 v[108:111], %[da] offset:10240\n\t"
    "global_load_lds_dwordx4 %[av4], s[36:37] sc1\n\t"
    "v_mfma_f32_16x16x32_bf16 v[88:91], v[144:147], v[116:119], v[88:91]\n\t"
    "s_add_u32 m0, %[lb], 40960\n\t"
    "v_mfma_f32_16x16x32_bf16 v[92:95], v[148:151], v[116:119], v[92:95]\n\t"
    "global_load_lds_dwordx4 %[av5], s[36:37] sc1\n\t"
    "s_waitcnt lgkmcnt(4)\n\t"
    "v_mfma_f32_16x16x32_bf16 v[80:83], v[152:155], v[120:123], v[80:83]\n\t"
    "ds_read_b128 v[112:115], %[ea] offset:12288\n\t"
    "s_add_u32 m0, %[lb], 12288\n\t"
    "v_mfma_f32_16x16x32_bf16 v[84:87], v[156:159], v[120:123], v[84:87]\n\t"
    "ds_read_b128 v[116:119], %[ea] offset:14336\n\t"
    "global_load_lds_dwordx4 %[av6], s[36:37] sc1\n\t"
    "v_mfma_f32_16x16x32_bf16 v[88:91], v[152:155], v[124:127], v[88:91]\n\t"
    "s_add_u32 m0, %[lb], 45056\n\t"
    "v_mfma_f32_16x16x32_bf16 v[92:95], v[156:159], v[124:127], v[92:95]\n\t"
    "global_load_lds_dwordx4 %[av7], s[36:37] sc1\n\t"
    "s_waitcnt lgkmcnt(4)\n\t"
    "v_mfma_f32_16x16x32_bf16 v[80:83], v[160:163], v[96:99], v[80:83]\n\t"
    "ds_read_b128 v[120:123], %[da] offset:12288\n\t"
    "s_add_u32 m0, %[lb], 16384\n\t"
    "v_mfma_f32_16x16x32_bf16 v[84:87], v[164:167], v[96:99], v[84:87]\n\t"
    "ds_read_b128 v[124:127], %[da] offset:14336\n\t"
    "global_load_lds_dwordx4 %[av0], s[38:39] sc1\n\t"
    "v_mfma_f32_16x16x32_bf16 v[88:91], v[160:163], v[100:103], v[88:91]\n\t"
    "s_add_u32 m0, %[lb], 49152\n\t"
    "v_mfma_f32_16x16x32_bf16 v[92:95], v[164:167], v[100:103], v[92:95]\n\t"
    "global_load_lds_dwordx4 %[av1], s[38:39] sc1\n\t"
    "s_waitcnt vmcnt(14)\n\t"
    "s_barrier\n\t"
    "s_waitcnt lgkmcnt(4)\n\t"
    "v_mfma_f32_16x16x32_bf16 v[80:83], v[168:171], v[104:107], v[80:83]\n\t"
    "ds_read_b128 v[96:99], %[ea] offset:16384\n\t"
    "s_add_u32 m0, %[lb], 20480\n\t"
    "v_mfma_f32_16x16x32_bf16 v[84:87], v[172:175], v[104:107], v[84:87]\n\t"
    "ds_read_b128 v[100:103], %[ea] offset:18432\n\t"
    "global_load_lds_dwordx4 %[av2], s[38:39] sc1\n\t"
    "v_mfma_f32_16x16x32_bf16 v[88:91], v[168:171], v[108:111], v[88:91]\n\t"
    "s_add_u32 m0, %[lb], 53248\n\t"
    "v_mfma_f32_16x16x32_bf16 v[92:95], v[172:175], v[108:111], v[92:95]\n\t"
    "global_load_lds_dwordx4 %[av3], s[38:39] sc1\n\t"
    "s_waitcnt lgkmcnt(4)\n\t"
    "v_mfma_f32_16x16x32_bf16 v[80:83], v[176:179], v[112:115], v[80:83]\n\t"
    "ds_read_b128 v[104:107], %[da] offset:16384\n\t"
    "s_add_u32 m0, %[lb], 24576\n\t"
    "v_mfma_f32_16x16x32_bf16 v[84:87], v[180:183], v[112:115], v[84:87]\n\t"
    "ds_read_b128 v[108:111], %[da] offset:18432\n\t"
    "global_load_lds_dwordx4 %[av4], s[38:39] sc1\n\t"
    "v_mfma_f32_16x16x32_bf16 v[88:91], v[176:179], v[116:119], v[88:91]\n\t"
    "s_add_u32 m0, %[lb], 57344\n\t"
    "v_mfma_f32_16x16x32_bf16 v[92:95], v[180:183], v[116:119], v[92:95]\n\t"
    "global_load_lds_dwordx4 %[av5], s[38:39] sc1\n\t"
    "s_waitcnt lgkmcnt(4)\n\t"
    "v_mfma_f32_16x16x32_bf16 v[80:83], v[184:187], v[120:123], v[80:83]\n\t"
    "ds_read_b128 v[112:115], %[ea] offset:20480\n\t"
    "s_add_u32 m0, %[lb], 28672\n\t"
    "v_mfma_f32_16x16x32_bf16 v[84:87], v[188:191], v[120:123], v[84:87]\n\t"
    "ds_read_b128 v[116:119], %[ea] offset:22528\n\t"
    "global_load_lds_dwordx4 %[av6], s[38:39] sc1\n\t"
    "v_mfma_f32_16x16x32_bf16 v[88:91], v[184:187], v[124:127], v[88:91]\n\t"
    "s_add_u32 m0, %[lb], 61440\n\t"
    "v_mfma_f32_16x16x32_bf16 v[92:95], v[188:191], v[124:127], v[92:95]\n\t"
    "global_load_lds_dwordx4 %[av7], s[38:39] sc1\n\t"
    "s_waitcnt lgkmcnt(4)\n\t"
    "v_mfma_f32_16x16x32_bf16 v[80:83], v[192:195], v[96:99], v[80:83]\n\t"
    "ds_read_b128 v[120:123], %[da] offset:20480\n\t"
    "v_mfma_f32_16x16x32_bf16 v[84:87], v[196:199], v[96:99], v[84:87]\n\t"
    "ds_read_b128 v[124:127], %[da] offset:22528\n\t"
    "v_mfma_f32_16x16x32_bf16 v[88:91], v[192:195], v[100:103], v[88:91]\n\t"
    "v_mfma_f32_16x16x32_bf16 v[92:95], v[196:199], v[100:103], v[92:95]\n\t"
    "s_add_u32 s36, s36, %[ab]\n\t"
    "s_addc_u32 s37, s37, 0\n\t"
    "s_add_u32 s38, s38, %[ab]\n\t"
    "s_addc_u32 s39, s39, 0\n\t"
    "s_xor_b32 %[lb], %[lb], 0x10000\n\t"
    "s_waitcnt vmcnt(16)\n\t"
    "s_barrier\n\t"
    "s_waitcnt lgkmcnt(4)\n\t"
    "v_mfma_f32_16x16x32_bf16 v[80:83], v[200:203], v[104:107], v[80:83]\n\t"
    "ds_read_b128 v[96:99], %[ea] offset:24576\n\t"
    "v_mfma_f32_16x16x32_bf16 v[84:87], v[204:207], v[104:107], v[84:87]\n\t"
    "ds_read_b128 v[100:103], %[ea] offset:26624\n\t"
    "v_mfma_f32_16x16x32_bf16 v[88:91], v[200:203], v[108:111], v[88:91]\n\t"
    "v_mfma_f32_16x16x32_bf16 v[92:95], v[204:207], v[108:111], v[92:95]\n\t"
    "s_cmp_eq_u32 %[sl], 0\n\t"
    "s_cbranch_scc1 73f\n\t"
    "s_mov_b32 m0, %[sl]\n\t"
    "s_nop 0\n\t"
    "global_load_lds_dword %[dv], %[db] sc1\n\t"
    "73:\n\t"
    "s_waitcnt lgkmcnt(4)\n\t"
    "v_mfma_f32_16x16x32_bf16 v[80:83], v[208:211], v[112:115], v[80:83]\n\t"
    "ds_read_b128 v[104:107], %[da] offset:24576\n\t"
    "v_mfma_f32_16x16x32_bf16 v[84:87], v[212:215], v[112:115], v[84:87]\n\t"
    "ds_read_b128 v[108:111], %[da] offset:26624\n\t"
    "v_mfma_f32_16x16x32_bf16 v[88:91], v[208:211], v[116:119], v[88:91]\n\t"
    "v_mfma_f32_16x16x32_bf16 v[92:95], v[212:215], v[116:119], v[92:95]\n\t"
    "s_waitcnt lgkmcnt(4)\n\t"
    "v_mfma_f32_16x16x32_bf16 v[80:83], v[216:219], v[120:123], v[80:83]\n\t"
    "ds_read_b128 v[112:115], %[ea] offset:28672\n\t"
    "v_mfma_f32_16x16x32_bf16 v[84:87], v[220:223], v[120:123], v[84:87]\n\t"
    "ds_read_b128 v[116:119], %[ea] offset:30720\n\t"
    "v_mfma_f32_16x16x32_bf16 v[88:91], v[216:219], v[124:127], v[88:91]\n\t"
    "v_mfma_f32_16x16x32_bf16 v[92:95], v[220:223], v[124:127], v[92:95]\n\t"
    "s_waitcnt lgkmcnt(4)\n\t"
    "v_mfma_f32_16x16x32_bf16 v[80:83], v[224:227], v[96:99], v[80:83]\n\t"
    "ds_read_b128 v[120:123], %[da] offset:28672\n\t"
    "v_mfma_f32_16x16x32_bf16 v[84:87], v[228:231], v[96:99], v[84:87]\n\t"
    "ds_read_b128 v[124:127], %[da] offset:30720\n\t"
    "v_mfma_f32_16x16x32_bf16 v[88:91], v[224:227], v[100:103], v[88:91]\n\t"
    "v_mfma_f32_16x16x32_bf16 v[92:95], v[228:231], v[100:103], v[92:95]\n\t"
    "s_waitcnt lgkmcnt(4)\n\t"
    "v_mfma_f32_16x16x32_bf16 v[80:83], v[232:235], v[104:107], v[80:83]\n\t"
    "v_mfma_f32_16x16x32_bf16 v[84:87], v[236:239], v[104:107], v[84:87]\n\t"
    "v_mfma_f32_16x16x32_bf16 v[88:91], v[232:235], v[108:111], v[88:91]\n\t"
    "v_xor_b32 %[ea], 0x10000, %[ea]\n\t"
    "v_mfma_f32_16x16x32_bf16 v[92:95], v[236:239], v[108:111], v[92:95]\n\t"
    "v_xor_b32 %[da], 0x10000, %[da]\n\t"
    "s_waitcnt lgkmcnt(2)\n\t"
    "v_mfma_f32_16x16x32_bf16 v[80:83], v[240:243], v[112:115], v[80:83]\n\t"
    "v_mfma_f32_16x16x32_bf16 v[84:87], v[244:247], v[112:115], v[84:87]\n\t"
    "v_mfma_f32_16x16x32_bf16 v[88:91], v[240:243], v[116:119], v[88:91]\n\t"
    "s_add_u32 %[b], %[b], 1\n\t"
    "v_mfma_f32_16x16x32_bf16 v[92:95], v[244:247], v[116:119], v[92:95]\n\t"
    "s_waitcnt lgkmcnt(0)\n\t"
    "v_mfma_f32_16x16x32_bf16 v[80:83], v[248:251], v[120:123], v[80:83]\n\t"
    "v_mfma_f32_16x16x32_bf16 v[84:87], v[252:255], v[120:123], v[84:87]\n\t"
    "v_mfma_f32_16x16x32_bf16 v[88:91], v[248:251], v[124:127], v[88:91]\n\t"
    "v_mfma_f32_16x16x32_bf16 v[92:95], v[252:255], v[124:127], v[92:95]\n\t"
    "v_mov_b32 v76, %[b]\n\t"
    "v_or_b32 v76, 0x100, v76\n\t"
    "s_nop 2\n\t"
    "ds_write_b128 %[rw], v[80:83] offset:0\n\t"
    "ds_write_b128 %[rw], v[84:87] offset:1024\n\t"
    "ds_write_b128 %[rw], v[88:91] offset:2048\n\t"
    "ds_write_b128 %[rw], v[92:95] offset:3072\n\t"
    "ds_write_b32 %[fa], v76\n\t"
    "s_waitcnt vmcnt(0)\n\t"
    "ds_read2_b32 v[78:79], %[sa] offset1:32\n\t"
    "s_waitcnt lgkmcnt(0)\n\t"
    "v_max_u32 v78, v78, v79\n\t"
    "v_mad_u32_u24 v78, v78, %[mu], %[ad]\n\t"
    "v_max_i32 v77, v77, v78\n\t"
    "s_branch 3b\n\t"
    "9:\n\t"
    "s_waitcnt lgkmcnt(0)\n\t"
    "s_nop 7"
    : [ea] "+&v"(ea), [da] "+&v"(da), [b] "+&s"(b), [st] "=&s"(st), "+{s[36:37]}"(p0), "+{s[38:39]}"(p1), [lb] "+&s"(lb), [cap] "+&s"(cap)
    : [ns] "s"(ns), [ab] "s"(ab), [rw] "v"(rw), [fa] "v"(fa), "{v[128:159]}"(W[0]), "{v[160:191]}"(W[1]), "{v[192:223]}"(W[2]), "{v[224:255]}"(W[3]), [wm] "s"(wm), [rq] "s"(rq), [mu] "s"(mu), [ad] "v"(ad), [dv] "v"(dv), [db] "s"(db), [sl] "s"(sl), [sa] "v"(sa), [av0] "v"(av[0]), [av1] "v"(av[1]), [av2] "v"(av[2]), [av3] "v"(av[3]), [av4] "v"(av[4]), [av5] "v"(av[5]), [av6] "v"(av[6]), [av7] "v"(av[7])
    : "v75", "v76", "v77", "v78", "v79", "v80", "v81", "v82", "v83", "v84", "v85", "v86", "v87", "v88", "v89", "v90", "v91", "v92", "v93", "v94", "v95", "v96", "v97", "v98", "v99", "v100", "v101", "v102", "v103", "v104", "v105", "v106", "v107", "v108", "v109", "v110", "v111", "v112", "v113", "v114", "v115", "v116", "v117", "v118", "v119", "v120", "v121", "v122", "v123", "v124", "v125", "v126", "v127", "vcc", "scc", "m0", "memory");
}
__device__ __forceinline__ void paced_ld2(const u32x32 (&W)[4], unsigned ea, unsigned da, int b, int ns, unsigned ab, unsigned rw, unsigned fa,
    const char* p0, const char* p1, unsigned lb, int& cap, int wm, unsigned rq, unsigned mu, unsigned ad, unsigned dv, const char* db, unsigned sl, unsigned sa, const unsigned (&av)[8]) {
  unsigned st;
  asm volatile("s_setprio 2\n\t"
    "v_mov_b32 v77, %[wm]\n\t"
    "s_cmp_lg_u32 %[rq], 0\n\t"
    "s_cbranch_scc1 3f\n\t"
    "2:\n\t"
    "v_cmp_ge_i32 vcc, %[b], v77\n\t"
    "s_cbranch_vccz 4f\n\t"
    "21:\n\t"
    "s_cmp_eq_u32 %[sl], 0\n\t"
    "s_cbranch_scc1 71f\n\t"
    "s_mov_b32 m0, %[sl]\n\t"
    "s_nop 0\n\t"
    "global_load_lds_dword %[dv], %[db] sc1\n\t"
    "71:\n\t"
    "s_sub_u32 %[cap], %[cap], 1\n\t"
    "s_max_i32 %[cap], %[cap], 0\n\t"
    "s_waitcnt vmcnt(0)\n\t"
    "ds_read2_b32 v[78:79], %[sa] offset1:32\n\t"
    "s_waitcnt lgkmcnt(0)\n\t"
    "v_max_u32 v78, v78, v79\n\t"
    "v_mad_u32_u24 v78, v78, %[mu], %[ad]\n\t"
    "v_max_i32 v77, v77, v78\n\t"
    "v_cmp_ge_i32 vcc, %[b], v77\n\t"
    "s_cbranch_vccz 4f\n\t"
    "s_sleep 1\n\t"
    "s_cmp_lg_u32 %[cap], 0\n\t"
    "s_cbranch_scc1 21b\n\t"
    "4:\n\t"
    "s_add_u32 m0, %[lb], 0\n\t"
    "s_nop 0\n\t"
    "global_load_lds_dwordx4 %[av0], s[36:37] sc1\n\t"
    "s_add_u32 m0, %[lb], 4096\n\t"
    "s_nop 0\n\t"
    "global_load_lds_dwordx4 %[av2], s[36:37] sc1\n\t"
    "s_add_u32 m0, %[lb], 8192\n\t"
    "s_nop 0\n\t"
    "global_load_lds_dwordx4 %[av4], s[36:37] sc1\n\t"
    "s_add_u32 m0, %[lb], 12288\n\t"
    "s_nop 0\n\t"
    "global_load_lds_dwordx4 %[av6], s[36:37] sc1\n\t"
    "s_add_u32 m0, %[lb], 16384\n\t"
    "s_nop 0\n\t"
    "global_load_lds_dwordx4 %[av0], s[38:39] sc1\n\t"
    "s_add_u32 m0, %[lb], 20480\n\t"
    "s_nop 0\n\t"
    "global_load_lds_dwordx4 %[av2], s[38:39] sc1\n\t"
    "s_add_u32 m0, %[lb], 24576\n\t"
    "s_nop 0\n\t"
    "global_load_lds_dwordx4 %[av4], s[38:39] sc1\n\t"
    "s_add_u32 m0, %[lb], 28672\n\t"
    "s_nop 0\n\t"
    "global_load_lds_dwordx4 %[av6], s[38:39] sc1\n\t"
    "s_add_u32 s36, s36, %[ab]\n\t"
    "s_addc_u32 s37, s37, 0\n\t"
    "s_add_u32 s38, s38, %[ab]\n\t"
    "s_addc_u32 s39, s39, 0\n\t"
    "s_xor_b32 %[lb], %[lb], 0x10000\n\t"
    "3:\n\t"
    "s_add_u32 %[st], %[b], 1\n\t"
    "v_min_i32 v77, %[ns], v77\n\t"
    "v_cmp_ge_i32 vcc, %[st], v77\n\t"
    "s_waitcnt vmcnt(6) lgkmcnt(0)\n\t"
    "s_barrier\n\t"
    "ds_read_b32 v75, %[fa] offset:16\n\t"
    "ds_read_b128 v[96:99], %[ea] offset:0\n\t"
    "ds_read_b128 v[100:103], %[ea] offset:2048\n\t"
    "ds_read_b128 v[104:107], %[da] offset:0\n\t"
    "ds_read_b128 v[108:111], %[da] offset:2048\n\t"
    "ds_read_b128 v[112:115], %[ea] offset:4096\n\t"
    "ds_read_b128 v[116:119], %[ea] offset:6144\n\t"
    "s_cbranch_vccz 5f\n\t"
    "s_waitcnt lgkmcnt(4)\n\t"
    "v_mfma_f32_16x16x32_bf16 v[80:83], v[128:131], v[96:99], 0\n\t"
    "ds_read_b128 v[120:123], %[da] offset:4096\n\t"
    "v_mfma_f32_16x16x32_bf16 v[84:87], v[132:135], v[96:99], 0\n\t"
    "ds_read_b128 v[124:127], %[da] offset:6144\n\t"
    "v_readfirstlane_b32 %[st], v75\n\t"
    "v_mfma_f32_16x16x32_bf16 v[88:91], v[128:131], v[100:103], 0\n\t"
    "v_mfma_f32_16x16x32_bf16 v[92:95], v[132:135], v[100:103], 0\n\t"
    "s_cmp_lg_u32 %[st], 0\n\t"
    "s_cbranch_scc1 9f\n\t"
    "s_waitcnt vmcnt(4)\n\t"
    "s_barrier\n\t"
    "s_waitcnt lgkmcnt(4)\n\t"
    "v_mfma_f32_16x16x32_bf16 v[80:83], v[136:139], v[104:107], v[80:83]\n\t"
    "ds_read_b128 v[96:99], %[ea] offset:8192\n\t"
    "v_mfma_f32_16x16x32_bf16 v[84:87], v[140:143], v[104:107], v[84:87]\n\t"
    "ds_read_b128 v[100:103], %[ea] offset:10240\n\t"
    "v_mfma_f32_16x16x32_bf16 v[88:91], v[136:139], v[108:111], v[88:91]\n\t"
    "v_mfma_f32_16x16x32_bf16 v[92:95], v[140:143], v[108:111], v[92:95]\n\t"
    "s_waitcnt lgkmcnt(4)\n\t"
    "v_mfma_f32_16x16x32_bf16 v[80:83], v[144:147], v[112:115], v[80:83]\n\t"
    "ds_read_b128 v[104:107], %[da] offset:8192\n\t"
    "v_mfma_f32_16x16x32_bf16 v[84:87], v[148:151], v[112:115], v[84:87]\n\t"
    "ds_read_b128 v[108:111], %[da] offset:10240\n\t"
    "v_mfma_f32_16x16x32_bf16 v[88:91], v[144:147], v[116:119], v[88:91]\n\t"
    "v_mfma_f32_16x16x32_bf16 v[92:95], v[148:151], v[116:119], v[92:95]\n\t"
    "s_waitcnt lgkmcnt(4)\n\t"
    "v_mfma_f32_16x16x32_bf16 v[80:83], v[152:155], v[120:123], v[80:83]\n\t"
    "ds_read_b128 v[112:115], %[ea] offset:12288\n\t"
    "v_mfma_f32_16x16x32_bf16 v[84:87], v[156:159], v[120:123], v[84:87]\n\t"
    "ds_read_b128 v[116:119], %[ea] offset:14336\n\t"
    "v_mfma_f32_16x16x32_bf16 v[88:91], v[152:155], v[124:127], v[88:91]\n\t"
    "v_mfma_f32_16x16x32_bf16 v[92:95], v[156:159], v[124:127], v[92:95]\n\t"
    "s_waitcnt lgkmcnt(4)\n\t"
    "v_mfma_f32_16x16x32_bf16 v[80:83], v[160:163], v[96:99], v[80:83]\n\t"
    "ds_read_b128 v[120:123], %[da] offset:12288\n\t"
    "v_mfma_f32_16x16x32_bf16 v[84:87], v[164:167], v[96:99], v[84:87]\n\t"
    "ds_read_b128 v[124:127], %[da] offset:14336\n\t"
    "v_mfma_f32_16x16x32_bf16 v[88:91], v[160:163], v[100:103], v[88:91]\n\t"
    "v_mfma_f32_16x16x32_bf16 v[92:95], v[164:167], v[100:103], v[92:95]\n\t"
    "s_waitcnt vmcnt(2)\n\t"
    "s_barrier\n\t"
    "s_waitcnt lgkmcnt(4)\n\t"
    "v_mfma_f32_16x16x32_bf16 v[80:83], v[168:171], v[104:107], v[80:83]\n\t"
    "ds_read_b128 v[96:99], %[ea] offset:16384\n\t"
    "v_mfma_f32_16x16x32_bf16 v[84:87], v[172:175], v[104:107], v[84:87]\n\t"
    "ds_read_b128 v[100:103], %[ea] offset:18432\n\t"
    "v_mfma_f32_16x16x32_bf16 v[88:91], v[168:171], v[108:111], v[88:91]\n\t"
    "v_mfma_f32_16x16x32_bf16 v[92:95], v[172:175], v[108:111], v[92:95]\n\t"
    "s_waitcnt lgkmcnt(4)\n\t"
    "v_mfma_f32_16x16x32_bf16 v[80:83], v[176:179], v[112:115], v[80:83]\n\t"
    "ds_read_b128 v[104:107], %[da] offset:16384\n\t"
    "v_mfma_f32_16x16x32_bf16 v[84:87], v[180:183], v[112:115], v[84:87]\n\t"
    "ds_read_b128 v[108:111], %[da] offset:18432\n\t"
    "v_mfma_f32_16x16x32_bf16 v[88:91], v[176:179], v[116:119], v[88:91]\n\t"
    "v_mfma_f32_16x16x32_bf16 v[92:95], v[180:183], v[116:119], v[92:95]\n\t"
    "s_waitcnt lgkmcnt(4)\n\t"
    "v_mfma_f32_16x16x32_bf16 v[80:83], v[184:187], v[120:123], v[80:83]\n\t"
    "ds_read_b128 v[112:115], %[ea] offset:20480\n\t"
    "v_mfma_f32_16x16x32_bf16 v[84:87], v[188:191], v[120:123], v[84:87]\n\t"
    "ds_read_b128 v[116:119], %[ea] offset:22528\n\t"
    "v_mfma_f32_16x16x32_bf16 v[88:91], v[184:187], v[124:127], v[88:91]\n\t"
    "v_mfma_f32_16x16x32_bf16 v[92:95], v[188:191], v[124:127], v[92:95]\n\t"
    "s_waitcnt lgkmcnt(4)\n\t"
    "v_mfma_f32_16x16x32_bf16 v[80:83], v[192:195], v[96:99], v[80:83]\n\t"
    "ds_read_b128 v[120:123], %[da] offset:20480\n\t"
    "v_mfma_f32_16x16x32_bf16 v[84:87], v[196:199], v[96:99], v[84:87]\n\t"
    "ds_read_b128 v[124:127], %[da] offset:22528\n\t"
    "v_mfma_f32_16x16x32_bf16 v[88:91], v[192:195], v[100:103], v[88:91]\n\t"
    "v_mfma_f32_16x16x32_bf16 v[92:95], v[196:199], v[100:103], v[92:95]\n\t"
    "s_waitcnt vmcnt(0)\n\t"
    "s_barrier\n\t"
    "s_waitcnt lgkmcnt(4)\n\t"
    "v_mfma_f32_16x16x32_bf16 v[80:83], v[200:203], v[104:107], v[80:83]\n\t"
    "ds_read_b128 v[96:99], %[ea] offset:24576\n\t"
    "v_mfma_f32_16x16x32_bf16 v[84:87], v[204:207], v[104:107], v[84:87]\n\t"
    "ds_read_b128 v[100:103], %[ea] offset:26624\n\t"
    "v_mfma_f32_16x16x32_bf16 v[88:91], v[200:203], v[108:111], v[88:91]\n\t"
    "v_mfma_f32_16x16x32_bf16 v[92:95], v[204:207], v[108:111], v[92:95]\n\t"
    "s_cmp_eq_u32 %[sl], 0\n\t"
    "s_cbranch_scc1 72f\n\t"
    "s_mov_b32 m0, %[sl]\n\t"
    "s_nop 0\n\t"
    "global_load_lds_dword %[dv], %[db] sc1\n\t"
    "72:\n\t"
    "s_waitcnt lgkmcnt(4)\n\t"
    "v_mfma_f32_16x16x32_bf16 v[80:83], v[208:211], v[112:115], v[80:83]\n\t"
    "ds_read_b128 v[104:107], %[da] offset:24576\n\t"
    "v_mfma_f32_16x16x32_bf16 v[84:87], v[212:215], v[112:115], v[84:87]\n\t"
    "ds_read_b128 v[108:111], %[da] offset:26624\n\t"
    "v_mfma_f32_16x16x32_bf16 v[88:91], v[208:211], v[116:119], v[88:91]\n\t"
    "v_mfma_f32_16x16x32_bf16 v[92:95], v[212:215], v[116:119], v[92:95]\n\t"
    "s_waitcnt lgkmcnt(4)\n\t"
    "v_mfma_f32_16x16x32_bf16 v[80:83], v[216:219], v[120:123], v[80:83]\n\t"
    "ds_read_b128 v[112:115], %[ea] offset:28672\n\t"
    "v_mfma_f32_16x16x32_bf16 v[84:87], v[220:223], v[120:123], v[84:87]\n\t"
    "ds_read_b128 v[116:119], %[ea] offset:30720\n\t"
    "v_mfma_f32_16x16x32_bf16 v[88:91], v[216:219], v[124:127], v[88:91]\n\t"
    "v_mfma_f32_16x16x32_bf16 v[92:95], v[220:223], v[124:127], v[92:95]\n\t"
    "s_waitcnt lgkmcnt(4)\n\t"
    "v_mfma_f32_16x16x32_bf16 v[80:83], v[224:227], v[96:99], v[80:83]\n\t"
    "ds_read_b128 v[120:123], %[da] offset:28672\n\t"
    "v_mfma_f32_16x16x32_bf16 v[84:87], v[228:231], v[96:99], v[84:87]\n\t"
    "ds_read_b128 v[124:127], %[da] offset:30720\n\t"
    "v_mfma_f32_16x16x32_bf16 v[88:91], v[224:227], v[100:103], v[88:91]\n\t"
    "v_mfma_f32_16x16x32_bf16 v[92:95], v[228:231], v[100:103], v[92:95]\n\t"
    "s_waitcnt lgkmcnt(4)\n\t"
    "v_mfma_f32_16x16x32_bf16 v[80:83], v[232:235], v[104:107], v[80:83]\n\t"
    "v_mfma_f32_16x16x32_bf16 v[84:87], v[236:239], v[104:107], v[84:87]\n\t"
    "v_mfma_f32_16x16x32_bf16 v[88:91], v[232:235], v[108:111], v[88:91]\n\t"
    "v_xor_b32 %[ea], 0x10000, %[ea]\n\t"
    "v_mfma_f32_16x16x32_bf16 v[92:95], v[236:239], v[108:111], v[92:95]\n\t"
    "v_xor_b32 %[da], 0x10000, %[da]\n\t"
    "s_waitcnt lgkmcnt(2)\n\t"
    "v_mfma_f32_16x16x32_bf16 v[80:83], v[240:243], v[112:115], v[80:83]\n\t"
    "v_mfma_f32_16x16x32_bf16 v[84:87], v[244:247], v[112:115], v[84:87]\n\t"
    "v_mfma_f32_16x16x32_bf16 v[88:91], v[240:243], v[116:119], v[88:91]\n\t"
    "s_add_u32 %[b], %[b], 1\n\t"
    "v_mfma_f32_16x16x32_bf16 v[92:95], v[244:247], v[116:119], v[92:95]\n\t"
    "s_waitcnt lgkmcnt(0)\n\t"
    "v_mfma_f32_16x16x32_bf16 v[80:83], v[248:251], v[120:123], v[80:83]\n\t"
    "v_mfma_f32_16x16x32_bf16 v[84:87], v[252:255], v[120:123], v[84:87]\n\t"
    "v_mfma_f32_16x16x32_bf16 v[88:91], v[248:251], v[124:127], v[88:91]\n\t"
    "v_mfma_f32_16x16x32_bf16 v[92:95], v[252:255], v[124:127], v[92:95]\n\t"
    "s_nop 4\n\t"
    "ds_write_b128 %[rw], v[80:83] offset:0\n\t"
    "ds_write_b128 %[rw], v[84:87] offset:1024\n\t"
    "ds_write_b128 %[rw], v[88:91] offset:2048\n\t"
    "ds_write_b128 %[rw], v[92:95] offset:3072\n\t"
    "s_waitcnt vmcnt(0)\n\t"
    "ds_read2_b32 v[78:79], %[sa] offset1:32\n\t"
    "s_waitcnt lgkmcnt(0)\n\t"
    "v_max_u32 v78, v78, v79\n\t"
    "v_mad_u32_u24 v78, v78, %[mu], %[ad]\n\t"
    "v_max_i32 v77, v77, v78\n\t"
    "v_mov_b32 v76, %[b]\n\t"
    "v_cmp_ge_i32 vcc, %[b], v77\n\t"
    "s_cmp_ge_i32 %[b], %[ns]\n\t"
    "s_cbranch_scc1 6f\n\t"
    "s_cbranch_vccnz 6f\n\t"
    "v_or_b32 v76, 0x100, v76\n\t"
    "ds_write_b32 %[fa], v76\n\t"
    "s_branch 4b\n\t"
    "6:\n\t"
    "ds_write_b32 %[fa], v76\n\t"
    "s_cbranch_scc0 21b\n\t"
    "s_waitcnt lgkmcnt(0)\n\t"
    "s_barrier\n\t"
    "s_branch 9f\n\t"
    "5:\n\t"
    "s_waitcnt lgkmcnt(4)\n\t"
    "v_mfma_f32_16x16x32_bf16 v[80:83], v[128:131], v[96:99], 0\n\t"
    "ds_read_b128 v[120:123], %[da] offset:4096\n\t"
    "s_add_u32 m0, %[lb], 0\n\t"
    "v_mfma_f32_16x16x32_bf16 v[84:87], v[132:135], v[96:99], 0\n\t"
    "ds_read_b128 v[124:127], %[da] offset:6144\n\t"
    "global_load_lds_dwordx4 %[av0], s[36:37] sc1\n\t"
    "v_readfirstlane_b32 %[st], v75\n\t"
    "v_mfma_f32_16x16x32_bf16 v[88:91], v[128:131], v[100:103], 0\n\t"
    "v_mfma_f32_16x16x32_bf16 v[92:95], v[132:135], v[100:103], 0\n\t"
    "s_cmp_lg_u32 %[st], 0\n\t"
    "s_cbranch_scc1 9f\n\t"
    "s_waitcnt vmcnt(5)\n\t"
    "s_barrier\n\t"
    "s_waitcnt lgkmcnt(4)\n\t"
    "v_mfma_f32_16x16x32_bf16 v[80:83], v[136:139], v[104:107], v[80:83]\n\t"
    "ds_read_b128 v[96:99], %[ea] offset:8192\n\t"
    "s_add_u32 m0, %[lb], 4096\n\t"
    "v_mfma_f32_16x16x32_bf16 v[84:87], v[140:143], v[104:107], v[84:87]\n\t"
    "ds_read_b128 v[100:103], %[ea] offset:10240\n\t"
    "global_load_lds_dwordx4 %[av2], s[36:37] sc1\n\t"
    "v_mfma_f32_16x16x32_bf16 v[88:91], v[136:139], v[108:111], v[88:91]\n\t"
    "v_mfma_f32_16x16x32_bf16 v[92:95], v[140:143], v[108:111], v[92:95]\n\t"
    "s_waitcnt lgkmcnt(4)\n\t"
    "v_mfma_f32_16x16x32_bf16 v[80:83], v[144:147], v[112:115], v[80:83]\n\t"
    "ds_read_b128 v[104:107], %[da] offset:8192\n\t"
    "s_add_u32 m0, %[lb], 8192\n\t"
    "v_mfma_f32_16x16x32_bf16 v[84:87], v[148:151], v[112:115], v[84:87]\n\t"
    "ds_read_b128 v[108:111], %[da] offset:10240\n\t"
    "global_load_lds_dwordx4 %[av4], s[36:37] sc1\n\t"
    "v_mfma_f32_16x16x32_bf16 v[88:91], v[144:147], v[116:119], v[88:91]\n\t"
    "v_mfma_f32_16x16x32_bf16 v[92:95], v[148:151], v[116:119], v[92:95]\n\t"
    "s_waitcnt lgkmcnt(4)\n\t"
    "v_mfma_f32_16x16x32_bf16 v[80:83], v[152:155], v[120:123], v[80:83]\n\t"
    "ds_read_b128 v[112:115], %[ea] offset:12288\n\t"
    "s_add_u32 m0, %[lb], 12288\n\t"
    "v_mfma_f32_16x16x32_bf16 v[84:87], v[156:159], v[120:123], v[84:87]\n\t"
    "ds_read_b128 v[116:119], %[ea] offset:14336\n\t"
    "global_load_lds_dwordx4 %[av6], s[36:37] sc1\n\t"
    "v_mfma_f32_16x16x32_bf16 v[88:91], v[152:155], v[124:127], v[88:91]\n\t"
    "v_mfma_f32_16x16x32_bf16 v[92:95], v[156:159], v[124:127], v[92:95]\n\t"
    "s_waitcnt lgkmcnt(4)\n\t"
    "v_mfma_f32_16x16x32_bf16 v[80:83], v[160:163], v[96:99], v[80:83]\n\t"
    "ds_read_b128 v[120:123], %[da] offset:12288\n\t"
    "s_add_u32 m0, %[lb], 16384\n\t"
    "v_mfma_f32_16x16x32_bf16 v[84:87], v[164:167], v[96:99], v[84:87]\n\t"
    "ds_read_b128 v[124:127], %[da] offset:14336\n\t"
    "global_load_lds_dwordx4 %[av0], s[38:39] sc1\n\t"
    "v_mfma_f32_16x16x32_bf16 v[88:91], v[160:163], v[100:103], v[88:91]\n\t"
    "v_mfma_f32_16x16x32_bf16 v[92:95], v[164:167], v[100:103], v[92:95]\n\t"
    "s_waitcnt vmcnt(7)\n\t"
    "s_barrier\n\t"
    "s_waitcnt lgkmcnt(4)\n\t"
    "v_mfma_f32_16x16x32_bf16 v[80:83], v[168:171], v[104:107], v[80:83]\n\t"
    "ds_read_b128 v[96:99], %[ea] offset:16384\n\t"
    "s_add_u32 m0, %[lb], 20480\n\t"
    "v_mfma_f32_16x16x32_bf16 v[84:87], v[172:175], v[104:107], v[84:87]\n\t"
    "ds_read_b128 v[100:103], %[ea] offset:18432\n\t"
    "global_load_lds_dwordx4 %[av2], s[38:39] sc1\n\t"
    "v_mfma_f32_16x16x32_bf16 v[88:91], v[168:171], v[108:111], v[88:91]\n\t"
    "v_mfma_f32_16x16x32_bf16 v[92:95], v[172:175], v[108:111], v[92:95]\n\t"
    "s_waitcnt lgkmcnt(4)\n\t"
    "v_mfma_f32_16x16x32_bf16 v[80:83], v[176:179], v[112:115], v[80:83]\n\t"
    "ds_read_b128 v[104:107], %[da] offset:16384\n\t"
    "s_add_u32 m0, %[lb], 24576\n\t"
    "v_mfma_f32_16x16x32_bf16 v[84:87], v[180:183], v[112:115], v[84:87]\n\t"
    "ds_read_b128 v[108:111], %[da] offset:18432\n\t"
    "global_load_lds_dwordx4 %[av4], s[38:39] sc1\n\t"
    "v_mfma_f32_16x16x32_bf16 v[88:91], v[176:179], v[116:119], v[88:91]\n\t"
    "v_mfma_f32_16x16x32_bf16 v[92:95], v[180:183], v[116:119], v[92:95]\n\t"
    "s_waitcnt lgkmcnt(4)\n\t"
    "v_mfma_f32_16x16x32_bf16 v[80:83], v[184:187], v[120:123], v[80:83]\n\t"
    "ds_read_b128 v[112:115], %[ea] offset:20480\n\t"
    "s_add_u32 m0, %[lb], 28672\n\t"
    "v_mfma_f32_16x16x32_bf16 v[84:87], v[188:191], v[120:123], v[84:87]\n\t"
    "ds_read_b128 v[116:119], %[ea] offset:22528\n\t"
    "global_load_lds_dwordx4 %[av6], s[38:39] sc1\n\t"
    "v_mfma_f32_16x16x32_bf16 v[88:91], v[184:187], v[124:127], v[88:91]\n\t"
    "v_mfma_f32_16x16x32_bf16 v[92:95], v[188:191], v[124:127], v[92:95]\n\t"
    "s_waitcnt lgkmcnt(4)\n\t"
    "v_mfma_f32_16x16x32_bf16 v[80:83], v[192:195], v[96:99], v[80:83]\n\t"
    "ds_read_b128 v[120:123], %[da] offset:20480\n\t"
    "v_mfma_f32_16x16x32_bf16 v[84:87], v[196:199], v[96:99], v[84:87]\n\t"
    "ds_read_b128 v[124:127], %[da] offset:22528\n\t"
    "v_mfma_f32_16x16x32_bf16 v[88:91], v[192:195], v[100:103], v[88:91]\n\t"
    "v_mfma_f32_16x16x32_bf16 v[92:95], v[196:199], v[100:103], v[92:95]\n\t"
    "s_add_u32 s36, s36, %[ab]\n\t"
    "s_addc_u32 s37, s37, 0\n\t"
    "s_add_u32 s38, s38, %[ab]\n\t"
    "s_addc_u32 s39, s39, 0\n\t"
    "s_xor_b32 %[lb], %[lb], 0x10000\n\t"
    "s_waitcnt vmcnt(8)\n\t"
    "s_barrier\n\t"
    "s_waitcnt lgkmcnt(4)\n\t"
    "v_mfma_f32_16x16x32_bf16 v[80:83], v[200:203], v[104:107], v[80:83]\n\t"
    "ds_read_b128 v[96:99], %[ea] offset:24576\n\t"
    "v_mfma_f32_16x16x32_bf16 v[84:87], v[204:207], v[104:107], v[84:87]\n\t"
    "ds_read_b128 v[100:103], %[ea] offset:26624\n\t"
    "v_mfma_f32_16x16x32_bf16 v[88:91], v[200:203], v[108:111], v[88:91]\n\t"
    "v_mfma_f32_16x16x32_bf16 v[92:95], v[204:207], v[108:111], v[92:95]\n\t"
    "s_cmp_eq_u32 %[sl], 0\n\t"
    "s_cbranch_scc1 73f\n\t"
    "s_mov_b32 m0, %[sl]\n\t"
    "s_nop 0\n\t"
    "global_load_lds_dword %[dv], %[db] sc1\n\t"
    "73:\n\t"
    "s_waitcnt lgkmcnt(4)\n\t"
    "v_mfma_f32_16x16x32_bf16 v[80:83], v[208:211], v[112:115], v[80:83]\n\t"
    "ds_read_b128 v[104:107], %[da] offset:24576\n\t"
    "v_mfma_f32_16x16x32_bf16 v[84:87], v[212:215], v[112:115], v[84:87]\n\t"
    "ds_read_b128 v[108:111], %[da] offset:26624\n\t"
    "v_mfma_f32_16x16x32_bf16 v[88:91], v[208:211], v[116:119], v[88:91]\n\t"
    "v_mfma_f32_16x16x32_bf16 v[92:95], v[212:215], v[116:119], v[92:95]\n\t"
    "s_waitcnt lgkmcnt(4)\n\t"
    "v_mfma_f32_16x16x32_bf16 v[80:83], v[216:219], v[120:123], v[80:83]\n\t"
    "ds_read_b128 v[112:115], %[ea] offset:28672\n\t"
    "v_mfma_f32_16x16x32_bf16 v[84:87], v[220:223], v[120:123], v[84:87]\n\t"
    "ds_read_b128 v[116:119], %[ea] offset:30720\n\t"
    "v_mfma_f32_16x16x32_bf16 v[88:91], v[216:219], v[124:127], v[88:91]\n\t"
    "v_mfma_f32_16x16x32_bf16 v[92:95], v[220:223], v[124:127], v[92:95]\n\t"
    "s_waitcnt lgkmcnt(4)\n\t"
    "v_mfma_f32_16x16x32_bf16 v[80:83], v[224:227], v[96:99], v[80:83]\n\t"
    "ds_read_b128 v[120:123], %[da] offset:28672\n\t"
    "v_mfma_f32_16x16x32_bf16 v[84:87], v[228:231], v[96:99], v[84:87]\n\t"
    "ds_read_b128 v[124:127], %[da] offset:30720\n\t"
    "v_mfma_f32_16x16x32_bf16 v[88:91], v[224:227], v[100:103], v[88:91]\n\t"
    "v_mfma_f32_16x16x32_bf16 v[92:95], v[228:231], v[100:103], v[92:95]\n\t"
    "s_waitcnt lgkmcnt(4)\n\t"
    "v_mfma_f32_16x16x32_bf16 v[80:83], v[232:235], v[104:107], v[80:83]\n\t"
    "v_mfma_f32_16x16x32_bf16 v[84:87], v[236:239], v[104:107], v[84:87]\n\t"
    "v_mfma_f32_16x16x32_bf16 v[88:91], v[232:235], v[108:111], v[88:91]\n\t"
    "v_xor_b32 %[ea], 0x10000, %[ea]\n\t"
    "v_mfma_f32_16x16x32_bf16 v[92:95], v[236:239], v[108:111], v[92:95]\n\t"
    "v_xor_b32 %[da], 0x10000, %[da]\n\t"
    "s_waitcnt lgkmcnt(2)\n\t"
    "v_mfma_f32_16x16x32_bf16 v[80:83], v[240:243], v[112:115], v[80:83]\n\t"
    "v_mfma_f32_16x16x32_bf16 v[84:87], v[244:247], v[112:115], v[84:87]\n\t"
    "v_mfma_f32_16x16x32_bf16 v[88:91], v[240:243], v[116:119], v[88:91]\n\t"
    "s_add_u32 %[b], %[b], 1\n\t"
    "v_mfma_f32_16x16x32_bf16 v[92:95], v[244:247], v[116:119], v[92:95]\n\t"
    "s_waitcnt lgkmcnt(0)\n\t"
    "v_mfma_f32_16x16x32_bf16 v[80:83], v[248:251], v[120:123], v[80:83]\n\t"
    "v_mfma_f32_16x16x32_bf16 v[84:87], v[252:255], v[120:123], v[84:87]\n\t"
    "v_mfma_f32_16x16x32_bf16 v[88:91], v[248:251], v[124:127], v[88:91]\n\t"
    "v_mfma_f32_16x16x32_bf16 v[92:95], v[252:255], v[124:127], v[92:95]\n\t"
    "v_mov_b32 v76, %[b]\n\t"
    "v_or_b32 v76, 0x100, v76\n\t"
    "s_nop 2\n\t"
    "ds_write_b128 %[rw], v[80:83] offset:0\n\t"
    "ds_write_b128 %[rw], v[84:87] offset:1024\n\t"
    "ds_write_b128 %[rw], v[88:91] offset:2048\n\t"
    "ds_write_b128 %[rw], v[92:95] offset:3072\n\t"
    "ds_write_b32 %[fa], v76\n\t"
    "s_waitcnt vmcnt(0)\n\t"
    "ds_read2_b32 v[78:79], %[sa] offset1:32\n\t"
    "s_waitcnt lgkmcnt(0)\n\t"
    "v_max_u32 v78, v78, v79\n\t"
    "v_mad_u32_u24 v78, v78, %[mu], %[ad]\n\t"
    "v_max_i32 v77, v77, v78\n\t"
    "s_branch 3b\n\t"
    "9:\n\t"
    "s_waitcnt lgkmcnt(0)\n\t"
    "s_nop 7"
    : [ea] "+&v"(ea), [da] "+&v"(da), [b] "+&s"(b), [st] "=&s"(st), "+{s[36:37]}"(p0), "+{s[38:39]}"(p1), [lb] "+&s"(lb), [cap] "+&s"(cap)
    : [ns] "s"(ns), [ab] "s"(ab), [rw] "v"(rw), [fa] "v"(fa), "{v[128:159]}"(W[0]), "{v[160:191]}"(W[1]), "{v[192:223]}"(W[2]), "{v[224:255]}"(W[3]), [wm] "s"(wm), [rq] "s"(rq), [mu] "s"(mu), [ad] "v"(ad), [dv] "v"(dv), [db] "s"(db), [sl] "s"(sl), [sa] "v"(sa), [av0] "v"(av[0]), [av1] "v"(av[1]), [av2] "v"(av[2]), [av3] "v"(av[3]), [av4] "v"(av[4]), [av5] "v"(av[5]), [av6] "v"(av[6]), [av7] "v"(av[7])
    : "v75", "v76", "v77", "v78", "v79", "v80", "v81", "v82", "v83", "v84", "v85", "v86", "v87", "v88", "v89", "v90", "v91", "v92", "v93", "v94", "v95", "v96", "v97", "v98", "v99", "v100", "v101", "v102", "v103", "v104", "v105", "v106", "v107", "v108", "v109", "v110", "v111", "v112", "v113", "v114", "v115", "v116", "v117", "v118", "v119", "v120", "v121", "v122", "v123", "v124", "v125", "v126", "v127", "vcc", "scc", "m0", "memory");
}
__device__ __forceinline__ void paced_st0(const u32x32 (&W)[4], unsigned ea, unsigned da, int b, int ns, unsigned ab, unsigned rw, unsigned fa,
    const char* p0, unsigned pp, int be, int bo, int bl, int po, unsigned cn, const char* pg, unsigned co0, unsigned co1) {
  unsigned st;
  asm volatile("1:\n\t"
    "s_barrier\n\t"
    "ds_read_b128 v[96:99], %[ea] offset:0\n\t"
    "ds_read_b128 v[100:103], %[ea] offset:2048\n\t"
    "ds_read_b128 v[104:107], %[da] offset:0\n\t"
    "ds_read_b128 v[108:111], %[da] offset:2048\n\t"
    "ds_read_b128 v[112:115], %[ea] offset:4096\n\t"
    "ds_read_b128 v[116:119], %[ea] offset:6144\n\t"
    "s_waitcnt lgkmcnt(4)\n\t"
    "v_mfma_f32_16x16x32_bf16 v[80:83], v[128:131], v[96:99], 0\n\t"
    "ds_read_b128 v[120:123], %[da] offset:4096\n\t"
    "v_mfma_f32_16x16x32_bf16 v[84:87], v[132:135], v[96:99], 0\n\t"
    "ds_read_b128 v[124:127], %[da] offset:6144\n\t"
    "v_mfma_f32_16x16x32_bf16 v[88:91], v[128:131], v[100:103], 0\n\t"
    "v_mfma_f32_16x16x32_bf16 v[92:95], v[132:135], v[100:103], 0\n\t"
    "s_barrier\n\t"
    "s_waitcnt lgkmcnt(4)\n\t"
    "v_mfma_f32_16x16x32_bf16 v[80:83], v[136:139], v[104:107], v[80:83]\n\t"
    "ds_read_b128 v[96:99], %[ea] offset:8192\n\t"
    "v_mfma_f32_16x16x32_bf16 v[84:87], v[140:143], v[104:107], v[84:87]\n\t"
    "ds_read_b128 v[100:103], %[ea] offset:10240\n\t"
    "v_mfma_f32_16x16x32_bf16 v[88:91], v[136:139], v[108:111], v[88:91]\n\t"
    "v_mfma_f32_16x16x32_bf16 v[92:95], v[140:143], v[108:111], v[92:95]\n\t"
    "s_waitcnt lgkmcnt(4)\n\t"
    "v_mfma_f32_16x16x32_bf16 v[80:83], v[144:147], v[112:115], v[80:83]\n\t"
    "ds_read_b128 v[104:107], %[da] offset:8192\n\t"
    "v_mfma_f32_16x16x32_bf16 v[84:87], v[148:151], v[112:115], v[84:87]\n\t"
    "ds_read_b128 v[108:111], %[da] offset:10240\n\t"
    "v_mfma_f32_16x16x32_bf16 v[88:91], v[144:147], v[116:119], v[88:91]\n\t"
    "v_mfma_f32_16x16x32_bf16 v[92:95], v[148:151], v[116:119], v[92:95]\n\t"
    "s_waitcnt lgkmcnt(4)\n\t"
    "v_mfma_f32_16x16x32_bf16 v[80:83], v[152:155], v[120:123], v[80:83]\n\t"
    "ds_read_b128 v[112:115], %[ea] offset:12288\n\t"
    "v_mfma_f32_16x16x32_bf16 v[84:87], v[156:159], v[120:123], v[84:87]\n\t"
    "ds_read_b128 v[116:119], %[ea] offset:14336\n\t"
    "v_mfma_f32_16x16x32_bf16 v[88:91], v[152:155], v[124:127], v[88:91]\n\t"
    "v_mfma_f32_16x16x32_bf16 v[92:95], v[156:159], v[124:127], v[92:95]\n\t"
    "s_cmp_lg_u32 %[pp], 0\n\t"
    "s_cbranch_scc1 7f\n\t"
    "8:\n\t"
    "s_waitcnt lgkmcnt(4)\n\t"
    "v_mfma_f32_16x16x32_bf16 v[80:83], v[160:163], v[96:99], v[80:83]\n\t"
    "ds_read_b128 v[120:123], %[da] offset:12288\n\t"
    "v_mfma_f32_16x16x32_bf16 v[84:87], v[164:167], v[96:99], v[84:87]\n\t"
    "ds_read_b128 v[124:127], %[da] offset:14336\n\t"
    "v_mfma_f32_16x16x32_bf16 v[88:91], v[160:163], v[100:103], v[88:91]\n\t"
    "v_mfma_f32_16x16x32_bf16 v[92:95], v[164:167], v[100:103], v[92:95]\n\t"
    "s_barrier\n\t"
    "s_waitcnt lgkmcnt(4)\n\t"
    "v_mfma_f32_16x16x32_bf16 v[80:83], v[168:171], v[104:107], v[80:83]\n\t"
    "ds_read_b128 v[96:99], %[ea] offset:16384\n\t"
    "v_mfma_f32_16x16x32_bf16 v[84:87], v[172:175], v[104:107], v[84:87]\n\t"
    "ds_read_b128 v[100:103], %[ea] offset:18432\n\t"
    "v_mfma_f32_16x16x32_bf16 v[88:91], v[168:171], v[108:111], v[88:91]\n\t"
    "v_mfma_f32_16x16x32_bf16 v[92:95], v[172:175], v[108:111], v[92:95]\n\t"
    "s_waitcnt lgkmcnt(4)\n\t"
    "v_mfma_f32_16x16x32_bf16 v[80:83], v[176:179], v[112:115], v[80:83]\n\t"
    "ds_read_b128 v[104:107], %[da] offset:16384\n\t"
    "v_mfma_f32_16x16x32_bf16 v[84:87], v[180:183], v[112:115], v[84:87]\n\t"
    "ds_read_b128 v[108:111], %[da] offset:18432\n\t"
    "v_mfma_f32_16x16x32_bf16 v[88:91], v[176:179], v[116:119], v[88:91]\n\t"
    "v_mfma_f32_16x16x32_bf16 v[92:95], v[180:183], v[116:119], v[92:95]\n\t"
    "s_waitcnt lgkmcnt(4)\n\t"
    "v_mfma_f32_16x16x32_bf16 v[80:83], v[184:187], v[120:123], v[80:83]\n\t"
    "ds_read_b128 v[112:115], %[ea] offset:20480\n\t"
    "v_mfma_f32_16x16x32_bf16 v[84:87], v[188:191], v[120:123], v[84:87]\n\t"
    "ds_read_b128 v[116:119], %[ea] offset:22528\n\t"
    "v_mfma_f32_16x16x32_bf16 v[88:91], v[184:187], v[124:127], v[88:91]\n\t"
    "v_mfma_f32_16x16x32_bf16 v[92:95], v[188:191], v[124:127], v[92:95]\n\t"
    "s_waitcnt lgkmcnt(4)\n\t"
    "v_mfma_f32_16x16x32_bf16 v[80:83], v[192:195], v[96:99], v[80:83]\n\t"
    "ds_read_b128 v[120:123], %[da] offset:20480\n\t"
    "v_mfma_f32_16x16x32_bf16 v[84:87], v[196:199], v[96:99], v[84:87]\n\t"
    "ds_read_b128 v[124:127], %[da] offset:22528\n\t"
    "v_mfma_f32_16x16x32_bf16 v[88:91], v[192:195], v[100:103], v[88:91]\n\t"
    "v_mfma_f32_16x16x32_bf16 v[92:95], v[196:199], v[100:103], v[92:95]\n\t"
    "s_barrier\n\t"
    "s_waitcnt lgkmcnt(4)\n\t"
    "v_mfma_f32_16x16x32_bf16 v[80:83], v[200:203], v[104:107], v[80:83]\n\t"
    "ds_read_b128 v[96:99], %[ea] offset:24576\n\t"
    "v_mfma_f32_16x16x32_bf16 v[84:87], v[204:207], v[104:107], v[84:87]\n\t"
    "ds_read_b128 v[100:103], %[ea] offset:26624\n\t"
    "v_mfma_f32_16x16x32_bf16 v[88:91], v[200:203], v[108:111], v[88:91]\n\t"
    "v_mfma_f32_16x16x32_bf16 v[92:95], v[204:207], v[108:111], v[92:95]\n\t"
    "s_waitcnt lgkmcnt(4)\n\t"
    "v_mfma_f32_16x16x32_bf16 v[80:83], v[208:211], v[112:115], v[80:83]\n\t"
    "ds_read_b128 v[104:107], %[da] offset:24576\n\t"
    "v_mfma_f32_16x16x32_bf16 v[84:87], v[212:215], v[112:115], v[84:87]\n\t"
    "ds_read_b128 v[108:111], %[da] offset:26624\n\t"
    "v_mfma_f32_16x16x32_bf16 v[88:91], v[208:211], v[116:119], v[88:91]\n\t"
    "v_mfma_f32_16x16x32_bf16 v[92:95], v[212:215], v[116:119], v[92:95]\n\t"
    "s_waitcnt lgkmcnt(4)\n\t"
    "v_mfma_f32_16x16x32_bf16 v[80:83], v[216:219], v[120:123], v[80:83]\n\t"
    "ds_read_b128 v[112:115], %[ea] offset:28672\n\t"
    "v_mfma_f32_16x16x32_bf16 v[84:87], v[220:223], v[120:123], v[84:87]\n\t"
    "ds_read_b128 v[116:119], %[ea] offset:30720\n\t"
    "v_mfma_f32_16x16x32_bf16 v[88:91], v[216:219], v[124:127], v[88:91]\n\t"
    "v_mfma_f32_16x16x32_bf16 v[92:95], v[220:223], v[124:127], v[92:95]\n\t"
    "s_waitcnt lgkmcnt(4)\n\t"
    "v_mfma_f32_16x16x32_bf16 v[80:83], v[224:227], v[96:99], v[80:83]\n\t"
    "ds_read_b128 v[120:123], %[da] offset:28672\n\t"
    "v_mfma_f32_16x16x32_bf16 v[84:87], v[228:231], v[96:99], v[84:87]\n\t"
    "ds_read_b128 v[124:127], %[da] offset:30720\n\t"
    "v_mfma_f32_16x16x32_bf16 v[88:91], v[224:227], v[100:103], v[88:91]\n\t"
    "v_mfma_f32_16x16x32_bf16 v[92:95], v[228:231], v[100:103], v[92:95]\n\t"
    "s_waitcnt lgkmcnt(4)\n\t"
    "v_mfma_f32_16x16x32_bf16 v[80:83], v[232:235], v[104:107], v[80:83]\n\t"
    "v_mfma_f32_16x16x32_bf16 v[84:87], v[236:239], v[104:107], v[84:87]\n\t"
    "v_mfma_f32_16x16x32_bf16 v[88:91], v[232:235], v[108:111], v[88:91]\n\t"
    "v_xor_b32 %[ea], 0x10000, %[ea]\n\t"
    "v_mfma_f32_16x16x32_bf16 v[92:95], v[236:239], v[108:111], v[92:95]\n\t"
    "v_xor_b32 %[da], 0x10000, %[da]\n\t"
    "s_waitcnt lgkmcnt(2)\n\t"
    "v_mfma_f32_16x16x32_bf16 v[80:83], v[240:243], v[112:115], v[80:83]\n\t"
    "v_mfma_f32_16x16x32_bf16 v[84:87], v[244:247], v[112:115], v[84:87]\n\t"
    "v_mfma_f32_16x16x32_bf16 v[88:91], v[240:243], v[116:119], v[88:91]\n\t"
    "s_add_u32 %[b], %[b], 1\n\t"
    "v_mfma_f32_16x16x32_bf16 v[92:95], v[244:247], v[116:119], v[92:95]\n\t"
    "s_waitcnt lgkmcnt(0)\n\t"
    "v_mfma_f32_16x16x32_bf16 v[80:83], v[248:251], v[120:123], v[80:83]\n\t"
    "v_mfma_f32_16x16x32_bf16 v[84:87], v[252:255], v[120:123], v[84:87]\n\t"
    "v_mfma_f32_16x16x32_bf16 v[88:91], v[248:251], v[124:127], v[88:91]\n\t"
    "v_mfma_f32_16x16x32_bf16 v[92:95], v[252:255], v[124:127], v[92:95]\n\t"
    "2:\n\t"
    "ds_read_b32 v39, %[fa]\n\t"
    "ds_read_b128 v[48:51], %[rw] offset:0\n\t"
    "ds_read_b128 v[56:59], %[rw] offset:2048\n\t"
    "ds_read_b128 v[52:55], %[rw] offset:1024\n\t"
    "ds_read_b128 v[60:63], %[rw] offset:3072\n\t"
    "s_waitcnt lgkmcnt(2)\n\t"
    "v_readfirstlane_b32 %[pp], v39\n\t"
    "s_and_b32 %[st], %[pp], 0xff\n\t"
    "s_cmp_eq_u32 %[st], %[b]\n\t"
    "s_cbranch_scc1 3f\n\t"
    "s_waitcnt lgkmcnt(0)\n\t"
    "s_branch 2b\n\t"
    "3:\n\t"
    "v_add_f32 v48, v48, v80\n\t"
    "v_add_f32 v49, v49, v81\n\t"
    "v_add_f32 v50, v50, v82\n\t"
    "v_add_f32 v51, v51, v83\n\t"
    "v_add_f32 v56, v56, v88\n\t"
    "v_add_f32 v57, v57, v89\n\t"
    "v_add_f32 v58, v58, v90\n\t"
    "v_add_f32 v59, v59, v91\n\t"
    "v_cmp_u_f32 vcc, v48, v56\n\t"
    "s_waitcnt lgkmcnt(0)\n\t"
    "v_add_f32 v52, v52, v84\n\t"
    "v_add_f32 v53, v53, v85\n\t"
    "v_add_f32 v54, v54, v86\n\t"
    "v_add_f32 v55, v55, v87\n\t"
    "v_add_f32 v60, v60, v92\n\t"
    "v_add_f32 v61, v61, v93\n\t"
    "v_add_f32 v62, v62, v94\n\t"
    "v_add_f32 v63, v63, v95\n\t"
    "s_cmp_lg_u64 vcc, 0\n\t"
    "s_cselect_b32 %[st], %[b], 0\n\t"
    "s_cmp_gt_i32 %[st], %[be]\n\t"
    "s_cbranch_scc1 4f\n\t"
    "v_cvt_pk_bf16_f32 v40, v48, v49\n\t"
    "v_cvt_pk_bf16_f32 v41, v50, v51\n\t"
    "v_cvt_pk_bf16_f32 v42, v52, v53\n\t"
    "v_cvt_pk_bf16_f32 v43, v54, v55\n\t"
    "global_store_dwordx4 %[co0], v[40:43], s[36:37] sc1\n\t"
    "v_cvt_pk_bf16_f32 v44, v56, v57\n\t"
    "v_cvt_pk_bf16_f32 v45, v58, v59\n\t"
    "v_cvt_pk_bf16_f32 v46, v60, v61\n\t"
    "v_cvt_pk_bf16_f32 v47, v62, v63\n\t"
    "global_store_dwordx4 %[co1], v[44:47], s[36:37] sc1\n\t"
    "s_add_u32 s36, s36, %[ab]\n\t"
    "s_addc_u32 s37, s37, 0\n\t"
    "s_cmp_le_i32 %[b], %[bl]\n\t"
    "s_cbranch_scc1 5f\n\t"
    "v_mov_b32 v72, %[cn]\n\t"
    "v_mov_b32 v73, 1\n\t"
    "s_waitcnt vmcnt(2)\n\t"
    "s_mov_b64 exec, 1\n\t"
    "ds_add_rtn_u32 v73, v72, v73\n\t"
    "s_add_u32 %[st], %[b], %[po]\n\t"
    "v_mov_b32 v75, %[st]\n\t"
    "v_add_u32 v74, -1, v75\n\t"
    "s_cmp_gt_i32 %[b], %[bo]\n\t"
    "s_cselect_b32 %[st], %[st], 0\n\t"
    "v_mov_b32 v75, %[st]\n\t"
    "v_mov_b32 v72, 0\n\t"
    "s_waitcnt lgkmcnt(0)\n\t"
    "v_readfirstlane_b32 %[st], v73\n\t"
    "s_and_b32 %[st], %[st], 3\n\t"
    "s_cmp_lg_u32 %[st], 3\n\t"
    "s_cbranch_scc1 61f\n\t"
    "global_store_dwordx2 v72, v[74:75], %[pg] sc1\n\t"
    "61:\n\t"
    "s_mov_b64 exec, -1\n\t"
    "s_mov_b32 %[pp], 0\n\t"
    "s_mov_b32 %[pp], 0\n\t"
    "s_cmp_lt_i32 %[b], %[ns]\n\t"
    "s_cbranch_scc1 1b\n\t"
    "s_barrier\n\t"
    "s_branch 9f\n\t"
    "5:\n\t"
    "s_lshr_b32 %[pp], %[pp], 8\n\t"
    "s_cbranch_scc1 1b\n\t"
    "v_mov_b32 v72, %[cn]\n\t"
    "v_mov_b32 v73, 1\n\t"
    "s_waitcnt vmcnt(0)\n\t"
    "s_mov_b64 exec, 1\n\t"
    "ds_add_rtn_u32 v73, v72, v73\n\t"
    "s_add_u32 %[st], %[b], %[po]\n\t"
    "v_mov_b32 v75, %[st]\n\t"
    "v_add_u32 v74, 0, v75\n\t"
    "v_mov_b32 v72, 0\n\t"
    "s_waitcnt lgkmcnt(0)\n\t"
    "v_readfirstlane_b32 %[st], v73\n\t"
    "s_and_b32 %[st], %[st], 3\n\t"
    "s_cmp_lg_u32 %[st], 3\n\t"
    "s_cbranch_scc1 61f\n\t"
    "global_store_dword v72, v74, %[pg] sc1\n\t"
    "61:\n\t"
    "s_mov_b64 exec, -1\n\t"
    "s_mov_b32 %[pp], 0\n\t"
    "s_cmp_lt_i32 %[b], %[ns]\n\t"
    "s_cbranch_scc1 1b\n\t"
    "s_barrier\n\t"
    "s_branch 9f\n\t"
    "4:\n\t"
    "v_mov_b32 v39, %[b]\n\t"
    "ds_write_b32 %[fa], v39 offset:16\n\t"
    "s_waitcnt lgkmcnt(0)\n\t"
    "s_barrier\n\t"
    "s_branch 9f\n\t"
    "7:\n\t"
    "v_mov_b32 v72, %[cn]\n\t"
    "v_mov_b32 v73, 1\n\t"
    "s_waitcnt vmcnt(0)\n\t"
    "s_mov_b64 exec, 1\n\t"
    "ds_add_rtn_u32 v73, v72, v73\n\t"
    "s_add_u32 %[st], %[b], %[po]\n\t"
    "v_mov_b32 v75, %[st]\n\t"
    "v_add_u32 v74, 0, v75\n\t"
    "v_mov_b32 v72, 0\n\t"
    "s_waitcnt lgkmcnt(0)\n\t"
    "v_readfirstlane_b32 %[st], v73\n\t"
    "s_and_b32 %[st], %[st], 3\n\t"
    "s_cmp_lg_u32 %[st], 3\n\t"
    "s_cbranch_scc1 61f\n\t"
    "global_store_dword v72, v74, %[pg] sc1\n\t"
    "61:\n\t"
    "s_mov_b64 exec, -1\n\t"
    "s_mov_b32 %[pp], 0\n\t"
    "s_branch 8b\n\t"
    "9:"
    : [ea] "+&v"(ea), [da] "+&v"(da), [b] "+&s"(b), [st] "=&s"(st), "+{s[36:37]}"(p0), [pp] "+&s"(pp)
    : [ns] "s"(ns), [ab] "s"(ab), [rw] "v"(rw), [fa] "v"(fa), "{v[128:159]}"(W[0]), "{v[160:191]}"(W[1]), "{v[192:223]}"(W[2]), "{v[224:255]}"(W[3]), [be] "s"(be), [bo] "s"(bo), [bl] "s"(bl), [po] "s"(po), [cn] "s"(cn), [pg] "s"(pg), [co0] "v"(co0), [co1] "v"(co1)
    : "v39", "v40", "v41", "v42", "v43", "v44", "v45", "v46", "v47", "v48", "v49", "v50", "v51", "v52", "v53", "v54", "v55", "v56", "v57", "v58", "v59", "v60", "v61", "v62", "v63", "v72", "v73", "v74", "v75", "v80", "v81", "v82", "v83", "v84", "v85", "v86", "v87", "v88", "v89", "v90", "v91", "v92", "v93", "v94", "v95", "v96", "v97", "v98", "v99", "v100", "v101", "v102", "v103", "v104", "v105", "v106", "v107", "v108", "v109", "v110", "v111", "v112", "v113", "v114", "v115", "v116", "v117", "v118", "v119", "v120", "v121", "v122", "v123", "v124", "v125", "v126", "v127", "vcc", "scc", "m0", "memory");
}
__device__ __forceinline__ void paced_st1(const u32x32 (&W)[4], unsigned ea, unsigned da, int b, int ns, unsigned ab, unsigned rw, unsigned fa,
    const char* p0, unsigned pp, int be, int bo, int bl, int po, unsigned cn, const char* pg, unsigned co0, unsigned co1) {
  unsigned st;
  asm volatile("1:\n\t"
    "s_barrier\n\t"
    "ds_read_b128 v[96:99], %[ea] offset:0\n\t"
    "ds_read_b128 v[100:103], %[ea] offset:2048\n\t"
    "ds_read_b128 v[104:107], %[da] offset:0\n\t"
    "ds_read_b128 v[108:111], %[da] offset:2048\n\t"
    "ds_read_b128 v[112:115], %[ea] offset:4096\n\t"
    "ds_read_b128 v[116:119], %[ea] offset:6144\n\t"
    "s_waitcnt lgkmcnt(4)\n\t"
    "v_mfma_f32_16x16x32_bf16 v[80:83], v[128:131], v[96:99], 0\n\t"
    "ds_read_b128 v[120:123], %[da] offset:4096\n\t"
    "v_mfma_f32_16x16x32_bf16 v[84:87], v[132:135], v[96:99], 0\n\t"
    "ds_read_b128 v[124:127], %[da] offset:6144\n\t"
    "v_mfma_f32_16x16x32_bf16 v[88:91], v[128:131], v[100:103], 0\n\t"
    "v_mfma_f32_16x16x32_bf16 v[92:95], v[132:135], v[100:103], 0\n\t"
    "s_barrier\n\t"
    "s_waitcnt lgkmcnt(4)\n\t"
    "v_mfma_f32_16x16x32_bf16 v[80:83], v[136:139], v[104:107], v[80:83]\n\t"
    "ds_read_b128 v[96:99], %[ea] offset:8192\n\t"
    "v_mfma_f32_16x16x32_bf16 v[84:87], v[140:143], v[104:107], v[84:87]\n\t"
    "ds_read_b128 v[100:103], %[ea] offset:10240\n\t"
    "v_mfma_f32_16x16x32_bf16 v[88:91], v[136:139], v[108:111], v[88:91]\n\t"
    "v_mfma_f32_16x16x32_bf16 v[92:95], v[140:143], v[108:111], v[92:95]\n\t"
    "s_waitcnt lgkmcnt(4)\n\t"
    "v_mfma_f32_16x16x32_bf16 v[80:83], v[144:147], v[112:115], v[80:83]\n\t"
    "ds_read_b128 v[104:107], %[da] offset:8192\n\t"
    "v_mfma_f32_16x16x32_bf16 v[84:87], v[148:151], v[112:115], v[84:87]\n\t"
    "ds_read_b128 v[108:111], %[da] offset:10240\n\t"
    "v_mfma_f32_16x16x32_bf16 v[88:91], v[144:147], v[116:119], v[88:91]\n\t"
    "v_mfma_f32_16x16x32_bf16 v[92:95], v[148:151], v[116:119], v[92:95]\n\t"
    "s_waitcnt lgkmcnt(4)\n\t"
    "v_mfma_f32_16x16x32_bf16 v[80:83], v[152:155], v[120:123], v[80:83]\n\t"
    "ds_read_b128 v[112:115], %[ea] offset:12288\n\t"
    "v_mfma_f32_16x16x32_bf16 v[84:87], v[156:159], v[120:123], v[84:87]\n\t"
    "ds_read_b128 v[116:119], %[ea] offset:14336\n\t"
    "v_mfma_f32_16x16x32_bf16 v[88:91], v[152:155], v[124:127], v[88:91]\n\t"
    "v_mfma_f32_16x16x32_bf16 v[92:95], v[156:159], v[124:127], v[92:95]\n\t"
    "s_cmp_lg_u32 %[pp], 0\n\t"
    "s_cbranch_scc1 7f\n\t"
    "8:\n\t"
    "s_waitcnt lgkmcnt(4)\n\t"
    "v_mfma_f32_16x16x32_bf16 v[80:83], v[160:163], v[96:99], v[80:83]\n\t"
    "ds_read_b128 v[120:123], %[da] offset:12288\n\t"
    "v_mfma_f32_16x16x32_bf16 v[84:87], v[164:167], v[96:99], v[84:87]\n\t"
    "ds_read_b128 v[124:127], %[da] offset:14336\n\t"
    "v_mfma_f32_16x16x32_bf16 v[88:91], v[160:163], v[100:103], v[88:91]\n\t"
    "v_mfma_f32_16x16x32_bf16 v[92:95], v[164:167], v[100:103], v[92:95]\n\t"
    "s_barrier\n\t"
    "s_waitcnt lgkmcnt(4)\n\t"
    "v_mfma_f32_16x16x32_bf16 v[80:83], v[168:171], v[104:107], v[80:83]\n\t"
    "ds_read_b128 v[96:99], %[ea] offset:16384\n\t"
    "v_mfma_f32_16x16x32_bf16 v[84:87], v[172:175], v[104:107], v[84:87]\n\t"
    "ds_read_b128 v[100:103], %[ea] offset:18432\n\t"
    "v_mfma_f32_16x16x32_bf16 v[88:91], v[168:171], v[108:111], v[88:91]\n\t"
    "v_mfma_f32_16x16x32_bf16 v[92:95], v[172:175], v[108:111], v[92:95]\n\t"
    "s_waitcnt lgkmcnt(4)\n\t"
    "v_mfma_f32_16x16x32_bf16 v[80:83], v[176:179], v[112:115], v[80:83]\n\t"
    "ds_read_b128 v[104:107], %[da] offset:16384\n\t"
    "v_mfma_f32_16x16x32_bf16 v[84:87], v[180:183], v[112:115], v[84:87]\n\t"
    "ds_read_b128 v[108:111], %[da] offset:18432\n\t"
    "v_mfma_f32_16x16x32_bf16 v[88:91], v[176:179], v[116:119], v[88:91]\n\t"
    "v_mfma_f32_16x16x32_bf16 v[92:95], v[180:183], v[116:119], v[92:95]\n\t"
    "s_waitcnt lgkmcnt(4)\n\t"
    "v_mfma_f32_16x16x32_bf16 v[80:83], v[184:187], v[120:123], v[80:83]\n\t"
    "ds_read_b128 v[112:115], %[ea] offset:20480\n\t"
    "v_mfma_f32_16x16x32_bf16 v[84:87], v[188:191], v[120:123], v[84:87]\n\t"
    "ds_read_b128 v[116:119], %[ea] offset:22528\n\t"
    "v_mfma_f32_16x16x32_bf16 v[88:91], v[184:187], v[124:127], v[88:91]\n\t"
    "v_mfma_f32_16x16x32_bf16 v[92:95], v[188:191], v[124:127], v[92:95]\n\t"
    "s_waitcnt lgkmcnt(4)\n\t"
    "v_mfma_f32_16x16x32_bf16 v[80:83], v[192:195], v[96:99], v[80:83]\n\t"
    "ds_read_b128 v[120:123], %[da] offset:20480\n\t"
    "v_mfma_f32_16x16x32_bf16 v[84:87], v[196:199], v[96:99], v[84:87]\n\t"
    "ds_read_b128 v[124:127], %[da] offset:22528\n\t"
    "v_mfma_f32_16x16x32_bf16 v[88:91], v[192:195], v[100:103], v[88:91]\n\t"
    "v_mfma_f32_16x16x32_bf16 v[92:95], v[196:199], v[100:103], v[92:95]\n\t"
    "s_barrier\n\t"
    "s_waitcnt lgkmcnt(4)\n\t"
    "v_mfma_f32_16x16x32_bf16 v[80:83], v[200:203], v[104:107], v[80:83]\n\t"
    "ds_read_b128 v[96:99], %[ea] offset:24576\n\t"
    "v_mfma_f32_16x16x32_bf16 v[84:87], v[204:207], v[104:107], v[84:87]\n\t"
    "ds_read_b128 v[100:103], %[ea] offset:26624\n\t"
    "v_mfma_f32_16x16x32_bf16 v[88:91], v[200:203], v[108:111], v[88:91]\n\t"
    "v_mfma_f32_16x16x32_bf16 v[92:95], v[204:207], v[108:111], v[92:95]\n\t"
    "s_waitcnt lgkmcnt(4)\n\t"
    "v_mfma_f32_16x16x32_bf16 v[80:83], v[208:211], v[112:115], v[80:83]\n\t"
    "ds_read_b128 v[104:107], %[da] offset:24576\n\t"
    "v_mfma_f32_16x16x32_bf16 v[84:87], v[212:215], v[112:115], v[84:87]\n\t"
    "ds_read_b128 v[108:111], %[da] offset:26624\n\t"
    "v_mfma_f32_16x16x32_bf16 v[88:91], v[208:211], v[116:119], v[88:91]\n\t"
    "v_mfma_f32_16x16x32_bf16 v[92:95], v[212:215], v[116:119], v[92:95]\n\t"
    "s_waitcnt lgkmcnt(4)\n\t"
    "v_mfma_f32_16x16x32_bf16 v[80:83], v[216:219], v[120:123], v[80:83]\n\t"
    "ds_read_b128 v[112:115], %[ea] offset:28672\n\t"
    "v_mfma_f32_16x16x32_bf16 v[84:87], v[220:223], v[120:123], v[84:87]\n\t"
    "ds_read_b128 v[116:119], %[ea] offset:30720\n\t"
    "v_mfma_f32_16x16x32_bf16 v[88:91], v[216:219], v[124:127], v[88:91]\n\t"
    "v_mfma_f32_16x16x32_bf16 v[92:95], v[220:223], v[124:127], v[92:95]\n\t"
    "s_waitcnt lgkmcnt(4)\n\t"
    "v_mfma_f32_16x16x32_bf16 v[80:83], v[224:227], v[96:99], v[80:83]\n\t"
    "ds_read_b128 v[120:123], %[da] offset:28672\n\t"
    "v_mfma_f32_16x16x32_bf16 v[84:87], v[228:231], v[96:99], v[84:87]\n\t"
    "ds_read_b128 v[124:127], %[da] offset:30720\n\t"
    "v_mfma_f32_16x16x32_bf16 v[88:91], v[224:227], v[100:103], v[88:91]\n\t"
    "v_mfma_f32_16x16x32_bf16 v[92:95], v[228:231], v[100:103], v[92:95]\n\t"
    "s_waitcnt lgkmcnt(4)\n\t"
    "v_mfma_f32_16x16x32_bf16 v[80:83], v[232:235], v[104:107], v[80:83]\n\t"
    "v_mfma_f32_16x16x32_bf16 v[84:87], v[236:239], v[104:107], v[84:87]\n\t"
    "v_mfma_f32_16x16x32_bf16 v[88:91], v[232:235], v[108:111], v[88:91]\n\t"
    "v_xor_b32 %[ea], 0x10000, %[ea]\n\t"
    "v_mfma_f32_16x16x32_bf16 v[92:95], v[236:239], v[108:111], v[92:95]\n\t"
    "v_xor_b32 %[da], 0x10000, %[da]\n\t"
    "s_waitcnt lgkmcnt(2)\n\t"
    "v_mfma_f32_16x16x32_bf16 v[80:83], v[240:243], v[112:115], v[80:83]\n\t"
    "v_mfma_f32_16x16x32_bf16 v[84:87], v[244:247], v[112:115], v[84:87]\n\t"
    "v_mfma_f32_16x16x32_bf16 v[88:91], v[240:243], v[116:119], v[88:91]\n\t"
    "s_add_u32 %[b], %[b], 1\n\t"
    "v_mfma_f32_16x16x32_bf16 v[92:95], v[244:247], v[116:119], v[92:95]\n\t"
    "s_waitcnt lgkmcnt(0)\n\t"
    "v_mfma_f32_16x16x32_bf16 v[80:83], v[248:251], v[120:123], v[80:83]\n\t"
    "v_mfma_f32_16x16x32_bf16 v[84:87], v[252:255], v[120:123], v[84:87]\n\t"
    "v_mfma_f32_16x16x32_bf16 v[88:91], v[248:251], v[124:127], v[88:91]\n\t"
    "v_mfma_f32_16x16x32_bf16 v[92:95], v[252:255], v[124:127], v[92:95]\n\t"
    "2:\n\t"
    "ds_read_b32 v39, %[fa]\n\t"
    "ds_read_b128 v[48:51], %[rw] offset:0\n\t"
    "ds_read_b128 v[56:59], %[rw] offset:2048\n\t"
    "ds_read_b128 v[52:55], %[rw] offset:1024\n\t"
    "ds_read_b128 v[60:63], %[rw] offset:3072\n\t"
    "s_waitcnt lgkmcnt(2)\n\t"
    "v_readfirstlane_b32 %[pp], v39\n\t"
    "s_and_b32 %[st], %[pp], 0xff\n\t"
    "s_cmp_eq_u32 %[st], %[b]\n\t"
    "s_cbranch_scc1 3f\n\t"
    "s_waitcnt lgkmcnt(0)\n\t"
    "s_branch 2b\n\t"
    "3:\n\t"
    "v_add_f32 v48, v48, v80\n\t"
    "v_add_f32 v49, v49, v81\n\t"
    "v_add_f32 v50, v50, v82\n\t"
    "v_add_f32 v51, v51, v83\n\t"
    "v_add_f32 v56, v56, v88\n\t"
    "v_add_f32 v57, v57, v89\n\t"
    "v_add_f32 v58, v58, v90\n\t"
    "v_add_f32 v59, v59, v91\n\t"
    "v_cmp_u_f32 vcc, v48, v56\n\t"
    "s_waitcnt lgkmcnt(0)\n\t"
    "v_add_f32 v52, v52, v84\n\t"
    "v_add_f32 v53, v53, v85\n\t"
    "v_add_f32 v54, v54, v86\n\t"
    "v_add_f32 v55, v55, v87\n\t"
    "v_add_f32 v60, v60, v92\n\t"
    "v_add_f32 v61, v61, v93\n\t"
    "v_add_f32 v62, v62, v94\n\t"
    "v_add_f32 v63, v63, v95\n\t"
    "s_cmp_lg_u64 vcc, 0\n\t"
    "s_cselect_b32 %[st], %[b], 0\n\t"
    "s_cmp_gt_i32 %[st], %[be]\n\t"
    "s_cbranch_scc1 4f\n\t"
    "v_mul_f32 v40, %[nl], v48\n\t"
    "v_mul_f32 v41, %[nl], v49\n\t"
    "v_mul_f32 v42, %[nl], v50\n\t"
    "v_mul_f32 v43, %[nl], v51\n\t"
    "v_mul_f32 v44, %[nl], v56\n\t"
    "v_mul_f32 v45, %[nl], v57\n\t"
    "v_mul_f32 v46, %[nl], v58\n\t"
    "v_mul_f32 v47, %[nl], v59\n\t"
    "v_exp_f32 v40, v40\n\t"
    "v_exp_f32 v41, v41\n\t"
    "v_exp_f32 v42, v42\n\t"
    "v_exp_f32 v43, v43\n\t"
    "v_exp_f32 v44, v44\n\t"
    "v_exp_f32 v45, v45\n\t"
    "v_exp_f32 v46, v46\n\t"
    "v_exp_f32 v47, v47\n\t"
    "v_add_f32 v40, 1.0, v40\n\t"
    "v_add_f32 v41, 1.0, v41\n\t"
    "v_add_f32 v42, 1.0, v42\n\t"
    "v_add_f32 v43, 1.0, v43\n\t"
    "v_add_f32 v44, 1.0, v44\n\t"
    "v_add_f32 v45, 1.0, v45\n\t"
    "v_add_f32 v46, 1.0, v46\n\t"
    "v_add_f32 v47, 1.0, v47\n\t"
    "v_rcp_f32 v40, v40\n\t"
    "v_rcp_f32 v41, v41\n\t"
    "v_rcp_f32 v42, v42\n\t"
    "v_rcp_f32 v43, v43\n\t"
    "v_rcp_f32 v44, v44\n\t"
    "v_rcp_f32 v45, v45\n\t"
    "v_rcp_f32 v46, v46\n\t"
    "v_rcp_f32 v47, v47\n\t"
    "v_mul_f32 v40, v48, v40\n\t"
    "v_mul_f32 v41, v49, v41\n\t"
    "v_mul_f32 v42, v50, v42\n\t"
    "v_mul_f32 v43, v51, v43\n\t"
    "v_mul_f32 v44, v56, v44\n\t"
    "v_mul_f32 v45, v57, v45\n\t"
    "v_mul_f32 v46, v58, v46\n\t"
    "v_mul_f32 v47, v59, v47\n\t"
    "v_mul_f32 v40, v40, v52\n\t"
    "v_mul_f32 v41, v41, v53\n\t"
    "v_mul_f32 v42, v42, v54\n\t"
    "v_mul_f32 v43, v43, v55\n\t"
    "v_mul_f32 v44, v44, v60\n\t"
    "v_mul_f32 v45, v45, v61\n\t"
    "v_mul_f32 v46, v46, v62\n\t"
    "v_mul_f32 v47, v47, v63\n\t"
    "v_cvt_pk_bf16_f32 v48, v40, v41\n\t"
    "v_cvt_pk_bf16_f32 v49, v42, v43\n\t"
    "v_cvt_pk_bf16_f32 v50, v44, v45\n\t"
    "v_cvt_pk_bf16_f32 v51, v46, v47\n\t"
    "s_nop 1\n\t"
    "v_permlane16_swap_b32 v48, v50\n\t"
    "v_permlane16_swap_b32 v49, v51\n\t"
    "global_store_dwordx4 %[co0], v[48:51], s[36:37] sc1\n\t"
    "s_add_u32 s36, s36, %[ab]\n\t"
    "s_addc_u32 s37, s37, 0\n\t"
    "s_cmp_le_i32 %[b], %[bl]\n\t"
    "s_cbranch_scc1 5f\n\t"
    "v_mov_b32 v72, %[cn]\n\t"
    "v_mov_b32 v73, 1\n\t"
    "s_waitcnt vmcnt(1)\n\t"
    "s_mov_b64 exec, 1\n\t"
    "ds_add_rtn_u32 v73, v72, v73\n\t"
    "s_add_u32 %[st], %[b], %[po]\n\t"
    "v_mov_b32 v75, %[st]\n\t"
    "v_add_u32 v74, -1, v75\n\t"
    "s_cmp_gt_i32 %[b], %[bo]\n\t"
    "s_cselect_b32 %[st], %[st], 0\n\t"
    "v_mov_b32 v75, %[st]\n\t"
    "v_mov_b32 v72, 0\n\t"
    "s_waitcnt lgkmcnt(0)\n\t"
    "v_readfirstlane_b32 %[st], v73\n\t"
    "s_and_b32 %[st], %[st], 3\n\t"
    "s_cmp_lg_u32 %[st], 3\n\t"
    "s_cbranch_scc1 61f\n\t"
    "global_store_dwordx2 v72, v[74:75], %[pg] sc1\n\t"
    "61:\n\t"
    "s_mov_b64 exec, -1\n\t"
    "s_mov_b32 %[pp], 0\n\t"
    "s_mov_b32 %[pp], 0\n\t"
    "s_cmp_lt_i32 %[b], %[ns]\n\t"
    "s_cbranch_scc1 1b\n\t"
    "s_barrier\n\t"
    "s_branch 9f\n\t"
    "5:\n\t"
    "s_lshr_b32 %[pp], %[pp], 8\n\t"
    "s_cbranch_scc1 1b\n\t"
    "v_mov_b32 v72, %[cn]\n\t"
    "v_mov_b32 v73, 1\n\t"
    "s_waitcnt vmcnt(0)\n\t"
    "s_mov_b64 exec, 1\n\t"
    "ds_add_rtn_u32 v73, v72, v73\n\t"
    "s_add_u32 %[st], %[b], %[po]\n\t"
    "v_mov_b32 v75, %[st]\n\t"
    "v_add_u32 v74, 0, v75\n\t"
    "v_mov_b32 v72, 0\n\t"
    "s_waitcnt lgkmcnt(0)\n\t"
    "v_readfirstlane_b32 %[st], v73\n\t"
    "s_and_b32 %[st], %[st], 3\n\t"
    "s_cmp_lg_u32 %[st], 3\n\t"
    "s_cbranch_scc1 61f\n\t"
    "global_store_dword v72, v74, %[pg] sc1\n\t"
    "61:\n\t"
    "s_mov_b64 exec, -1\n\t"
    "s_mov_b32 %[pp], 0\n\t"
    "s_cmp_lt_i32 %[b], %[ns]\n\t"
    "s_cbranch_scc1 1b\n\t"
    "s_barrier\n\t"
    "s_branch 9f\n\t"
    "4:\n\t"
    "v_mov_b32 v39, %[b]\n\t"
    "ds_write_b32 %[fa], v39 offset:16\n\t"
    "s_waitcnt lgkmcnt(0)\n\t"
    "s_barrier\n\t"
    "s_branch 9f\n\t"
    "7:\n\t"
    "v_mov_b32 v72, %[cn]\n\t"
    "v_mov_b32 v73, 1\n\t"
    "s_waitcnt vmcnt(0)\n\t"
    "s_mov_b64 exec, 1\n\t"
    "ds_add_rtn_u32 v73, v72, v73\n\t"
    "s_add_u32 %[st], %[b], %[po]\n\t"
    "v_mov_b32 v75, %[st]\n\t"
    "v_add_u32 v74, 0, v75\n\t"
    "v_mov_b32 v72, 0\n\t"
    "s_waitcnt lgkmcnt(0)\n\t"
    "v_readfirstlane_b32 %[st], v73\n\t"
    "s_and_b32 %[st], %[st], 3\n\t"
    "s_cmp_lg_u32 %[st], 3\n\t"
    "s_cbranch_scc1 61f\n\t"
    "global_store_dword v72, v74, %[pg] sc1\n\t"
    "61:\n\t"
    "s_mov_b64 exec, -1\n\t"
    "s_mov_b32 %[pp], 0\n\t"
    "s_branch 8b\n\t"
    "9:"
    : [ea] "+&v"(ea), [da] "+&v"(da), [b] "+&s"(b), [st] "=&s"(st), "+{s[36:37]}"(p0), [pp] "+&s"(pp)
    : [ns] "s"(ns), [ab] "s"(ab), [rw] "v"(rw), [fa] "v"(fa), "{v[128:159]}"(W[0]), "{v[160:191]}"(W[1]), "{v[192:223]}"(W[2]), "{v[224:255]}"(W[3]), [be] "s"(be), [bo] "s"(bo), [bl] "s"(bl), [po] "s"(po), [cn] "s"(cn), [pg] "s"(pg), [co0] "v"(co0), [nl] "s"(-LOG2E)
    : "v39", "v40", "v41", "v42", "v43", "v44", "v45", "v46", "v47", "v48", "v49", "v50", "v51", "v52", "v53", "v54", "v55", "v56", "v57", "v58", "v59", "v60", "v61", "v62", "v63", "v72", "v73", "v74", "v75", "v80", "v81", "v82", "v83", "v84", "v85", "v86", "v87", "v88", "v89", "v90", "v91", "v92", "v93", "v94", "v95", "v96", "v97", "v98", "v99", "v100", "v101", "v102", "v103", "v104", "v105", "v106", "v107", "v108", "v109", "v110", "v111", "v112", "v113", "v114", "v115", "v116", "v117", "v118", "v119", "v120", "v121", "v122", "v123", "v124", "v125", "v126", "v127", "vcc", "scc", "m0", "memory");
}
__device__ __forceinline__ void paced_st2(const u32x32 (&W)[4], unsigned ea, unsigned da, int b, int ns, unsigned ab, unsigned rw, unsigned fa,
    const char* p0, unsigned pp, int be, int bo, int bl, int po, unsigned cn, const char* pg, unsigned co0, unsigned co1) {
  unsigned st;
  asm volatile("1:\n\t"
    "s_barrier\n\t"
    "ds_read_b128 v[96:99], %[ea] offset:0\n\t"
    "ds_read_b128 v[100:103], %[ea] offset:2048\n\t"
    "ds_read_b128 v[104:107], %[da] offset:0\n\t"
    "ds_read_b128 v[108:111], %[da] offset:2048\n\t"
    "ds_read_b128 v[112:115], %[ea] offset:4096\n\t"
    "ds_read_b128 v[116:119], %[ea] offset:6144\n\t"
    "s_waitcnt lgkmcnt(4)\n\t"
    "v_mfma_f32_16x16x32_bf16 v[80:83], v[128:131], v[96:99], 0\n\t"
    "ds_read_b128 v[120:123], %[da] offset:4096\n\t"
    "v_mfma_f32_16x16x32_bf16 v[84:87], v[132:135], v[96:99], 0\n\t"
    "ds_read_b128 v[124:127], %[da] offset:6144\n\t"
    "v_mfma_f32_16x16x32_bf16 v[88:91], v[128:131], v[100:103], 0\n\t"
    "v_mfma_f32_16x16x32_bf16 v[92:95], v[132:135], v[100:103], 0\n\t"
    "s_barrier\n\t"
    "s_waitcnt lgkmcnt(4)\n\t"
    "v_mfma_f32_16x16x32_bf16 v[80:83], v[136:139], v[104:107], v[80:83]\n\t"
    "ds_read_b128 v[96:99], %[ea] offset:8192\n\t"
    "v_mfma_f32_16x16x32_bf16 v[84:87], v[140:143], v[104:107], v[84:87]\n\t"
    "ds_read_b128 v[100:103], %[ea] offset:10240\n\t"
    "v_mfma_f32_16x16x32_bf16 v[88:91], v[136:139], v[108:111], v[88:91]\n\t"
    "v_mfma_f32_16x16x32_bf16 v[92:95], v[140:143], v[108:111], v[92:95]\n\t"
    "s_waitcnt lgkmcnt(4)\n\t"
    "v_mfma_f32_16x16x32_bf16 v[80:83], v[144:147], v[112:115], v[80:83]\n\t"
    "ds_read_b128 v[104:107], %[da] offset:8192\n\t"
    "v_mfma_f32_16x16x32_bf16 v[84:87], v[148:151], v[112:115], v[84:87]\n\t"
    "ds_read_b128 v[108:111], %[da] offset:10240\n\t"
    "v_mfma_f32_16x16x32_bf16 v[88:91], v[144:147], v[116:119], v[88:91]\n\t"
    "v_mfma_f32_16x16x32_bf16 v[92:95], v[148:151], v[116:119], v[92:95]\n\t"
    "s_waitcnt lgkmcnt(4)\n\t"
    "v_mfma_f32_16x16x32_bf16 v[80:83], v[152:155], v[120:123], v[80:83]\n\t"
    "ds_read_b128 v[112:115], %[ea] offset:12288\n\t"
    "v_mfma_f32_16x16x32_bf16 v[84:87], v[156:159], v[120:123], v[84:87]\n\t"
    "ds_read_b128 v[116:119], %[ea] offset:14336\n\t"
    "v_mfma_f32_16x16x32_bf16 v[88:91], v[152:155], v[124:127], v[88:91]\n\t"
    "v_mfma_f32_16x16x32_bf16 v[92:95], v[156:159], v[124:127], v[92:95]\n\t"
    "s_cmp_lg_u32 %[pp], 0\n\t"
    "s_cbranch_scc1 7f\n\t"
    "8:\n\t"
    "s_waitcnt lgkmcnt(4)\n\t"
    "v_mfma_f32_16x16x32_bf16 v[80:83], v[160:163], v[96:99], v[80:83]\n\t"
    "ds_read_b128 v[120:123], %[da] offset:12288\n\t"
    "v_mfma_f32_16x16x32_bf16 v[84:87], v[164:167], v[96:99], v[84:87]\n\t"
    "ds_read_b128 v[124:127], %[da] offset:14336\n\t"
    "v_mfma_f32_16x16x32_bf16 v[88:91], v[160:163], v[100:103], v[88:91]\n\t"
    "v_mfma_f32_16x16x32_bf16 v[92:95], v[164:167], v[100:103], v[92:95]\n\t"
    "s_barrier\n\t"
    "s_waitcnt lgkmcnt(4)\n\t"
    "v_mfma_f32_16x16x32_bf16 v[80:83], v[168:171], v[104:107], v[80:83]\n\t"
    "ds_read_b128 v[96:99], %[ea] offset:16384\n\t"
    "v_mfma_f32_16x16x32_bf16 v[84:87], v[172:175], v[104:107], v[84:87]\n\t"
    "ds_read_b128 v[100:103], %[ea] offset:18432\n\t"
    "v_mfma_f32_16x16x32_bf16 v[88:91], v[168:171], v[108:111], v[88:91]\n\t"
    "v_mfma_f32_16x16x32_bf16 v[92:95], v[172:175], v[108:111], v[92:95]\n\t"
    "s_waitcnt lgkmcnt(4)\n\t"
    "v_mfma_f32_16x16x32_bf16 v[80:83], v[176:179], v[112:115], v[80:83]\n\t"
    "ds_read_b128 v[104:107], %[da] offset:16384\n\t"
    "v_mfma_f32_16x16x32_bf16 v[84:87], v[180:183], v[112:115], v[84:87]\n\t"
    "ds_read_b128 v[108:111], %[da] offset:18432\n\t"
    "v_mfma_f32_16x16x32_bf16 v[88:91], v[176:179], v[116:119], v[88:91]\n\t"
    "v_mfma_f32_16x16x32_bf16 v[92:95], v[180:183], v[116:119], v[92:95]\n\t"
    "s_waitcnt lgkmcnt(4)\n\t"
    "v_mfma_f32_16x16x32_bf16 v[80:83], v[184:187], v[120:123], v[80:83]\n\t"
    "ds_read_b128 v[112:115], %[ea] offset:20480\n\t"
    "v_mfma_f32_16x16x32_bf16 v[84:87], v[188:191], v[120:123], v[84:87]\n\t"
    "ds_read_b128 v[116:119], %[ea] offset:22528\n\t"
    "v_mfma_f32_16x16x32_bf16 v[88:91], v[184:187], v[124:127], v[88:91]\n\t"
    "v_mfma_f32_16x16x32_bf16 v[92:95], v[188:191], v[124:127], v[92:95]\n\t"
    "s_waitcnt lgkmcnt(4)\n\t"
    "v_mfma_f32_16x16x32_bf16 v[80:83], v[192:195], v[96:99], v[80:83]\n\t"
    "ds_read_b128 v[120:123], %[da] offset:20480\n\t"
    "v_mfma_f32_16x16x32_bf16 v[84:87], v[196:199], v[96:99], v[84:87]\n\t"
    "ds_read_b128 v[124:127], %[da] offset:22528\n\t"
    "v_mfma_f32_16x16x32_bf16 v[88:91], v[192:195], v[100:103], v[88:91]\n\t"
    "v_mfma_f32_16x16x32_bf16 v[92:95], v[196:199], v[100:103], v[92:95]\n\t"
    "s_barrier\n\t"
    "s_waitcnt lgkmcnt(4)\n\t"
    "v_mfma_f32_16x16x32_bf16 v[80:83], v[200:203], v[104:107], v[80:83]\n\t"
    "ds_read_b128 v[96:99], %[ea] offset:24576\n\t"
    "v_mfma_f32_16x16x32_bf16 v[84:87], v[204:207], v[104:107], v[84:87]\n\t"
    "ds_read_b128 v[100:103], %[ea] offset:26624\n\t"
    "v_mfma_f32_16x16x32_bf16 v[88:91], v[200:203], v[108:111], v[88:91]\n\t"
    "v_mfma_f32_16x16x32_bf16 v[92:95], v[204:207], v[108:111], v[92:95]\n\t"
    "s_waitcnt lgkmcnt(4)\n\t"
    "v_mfma_f32_16x16x32_bf16 v[80:83], v[208:211], v[112:115], v[80:83]\n\t"
    "ds_read_b128 v[104:107], %[da] offset:24576\n\t"
    "v_mfma_f32_16x16x32_bf16 v[84:87], v[212:215], v[112:115], v[84:87]\n\t"
    "ds_read_b128 v[108:111], %[da] offset:26624\n\t"
    "v_mfma_f32_16x16x32_bf16 v[88:91], v[208:211], v[116:119], v[88:91]\n\t"
    "v_mfma_f32_16x16x32_bf16 v[92:95], v[212:215], v[116:119], v[92:95]\n\t"
    "s_waitcnt lgkmcnt(4)\n\t"
    "v_mfma_f32_16x16x32_bf16 v[80:83], v[216:219], v[120:123], v[80:83]\n\t"
    "ds_read_b128 v[112:115], %[ea] offset:28672\n\t"
    "v_mfma_f32_16x16x32_bf16 v[84:87], v[220:223], v[120:123], v[84:87]\n\t"
    "ds_read_b128 v[116:119], %[ea] offset:30720\n\t"
    "v_mfma_f32_16x16x32_bf16 v[88:91], v[216:219], v[124:127], v[88:91]\n\t"
    "v_mfma_f32_16x16x32_bf16 v[92:95], v[220:223], v[124:127], v[92:95]\n\t"
    "s_waitcnt lgkmcnt(4)\n\t"
    "v_mfma_f32_16x16x32_bf16 v[80:83], v[224:227], v[96:99], v[80:83]\n\t"
    "ds_read_b128 v[120:123], %[da] offset:28672\n\t"
    "v_mfma_f32_16x16x32_bf16 v[84:87], v[228:231], v[96:99], v[84:87]\n\t"
    "ds_read_b128 v[124:127], %[da] offset:30720\n\t"
    "v_mfma_f32_16x16x32_bf16 v[88:91], v[224:227], v[100:103], v[88:91]\n\t"
    "v_mfma_f32_16x16x32_bf16 v[92:95], v[228:231], v[100:103], v[92:95]\n\t"
    "s_waitcnt lgkmcnt(4)\n\t"
    "v_mfma_f32_16x16x32_bf16 v[80:83], v[232:235], v[104:107], v[80:83]\n\t"
    "v_mfma_f32_16x16x32_bf16 v[84:87], v[236:239], v[104:107], v[84:87]\n\t"
    "v_mfma_f32_16x16x32_bf16 v[88:91], v[232:235], v[108:111], v[88:91]\n\t"
    "v_xor_b32 %[ea], 0x10000, %[ea]\n\t"
    "v_mfma_f32_16x16x32_bf16 v[92:95], v[236:239], v[108:111], v[92:95]\n\t"
    "v_xor_b32 %[da], 0x10000, %[da]\n\t"
    "s_waitcnt lgkmcnt(2)\n\t"
    "v_mfma_f32_16x16x32_bf16 v[80:83], v[240:243], v[112:115], v[80:83]\n\t"
    "v_mfma_f32_16x16x32_bf16 v[84:87], v[244:247], v[112:115], v[84:87]\n\t"
    "v_mfma_f32_16x16x32_bf16 v[88:91], v[240:243], v[116:119], v[88:91]\n\t"
    "s_add_u32 %[b], %[b], 1\n\t"
    "v_mfma_f32_16x16x32_bf16 v[92:95], v[244:247], v[116:119], v[92:95]\n\t"
    "s_waitcnt lgkmcnt(0)\n\t"
    "v_mfma_f32_16x16x32_bf16 v[80:83], v[248:251], v[120:123], v[80:83]\n\t"
    "v_mfma_f32_16x16x32_bf16 v[84:87], v[252:255], v[120:123], v[84:87]\n\t"
    "v_mfma_f32_16x16x32_bf16 v[88:91], v[248:251], v[124:127], v[88:91]\n\t"
    "v_mfma_f32_16x16x32_bf16 v[92:95], v[252:255], v[124:127], v[92:95]\n\t"
    "s_nop 6\n\t"
    "v_cmp_u_f32 vcc, v80, v88\n\t"
    "s_cmp_lg_u64 vcc, 0\n\t"
    "s_cselect_b32 %[st], %[b], 0\n\t"
    "s_cmp_gt_i32 %[st], %[be]\n\t"
    "s_cbranch_scc1 4f\n\t"
    "v_cvt_pk_bf16_f32 v40, v80, v81\n\t"
    "v_cvt_pk_bf16_f32 v41, v82, v83\n\t"
    "v_cvt_pk_bf16_f32 v42, v84, v85\n\t"
    "v_cvt_pk_bf16_f32 v43, v86, v87\n\t"
    "global_store_dwordx4 %[co0], v[40:43], s[36:37] sc1\n\t"
    "v_cvt_pk_bf16_f32 v44, v88, v89\n\t"
    "v_cvt_pk_bf16_f32 v45, v90, v91\n\t"
    "v_cvt_pk_bf16_f32 v46, v92, v93\n\t"
    "v_cvt_pk_bf16_f32 v47, v94, v95\n\t"
    "global_store_dwordx4 %[co1], v[44:47], s[36:37] sc1\n\t"
    "2:\n\t"
    "ds_read_b32 v39, %[fa]\n\t"
    "ds_read_b128 v[48:51], %[rw] offset:0\n\t"
    "ds_read_b128 v[52:55], %[rw] offset:1024\n\t"
    "ds_read_b128 v[56:59], %[rw] offset:2048\n\t"
    "ds_read_b128 v[60:63], %[rw] offset:3072\n\t"
    "s_waitcnt lgkmcnt(2)\n\t"
    "v_readfirstlane_b32 %[pp], v39\n\t"
    "s_and_b32 %[st], %[pp], 0xff\n\t"
    "s_cmp_eq_u32 %[st], %[b]\n\t"
    "s_cbranch_scc1 3f\n\t"
    "s_waitcnt lgkmcnt(0)\n\t"
    "s_branch 2b\n\t"
    "3:\n\t"
    "v_cvt_pk_bf16_f32 v48, v48, v49\n\t"
    "v_cvt_pk_bf16_f32 v49, v50, v51\n\t"
    "v_cvt_pk_bf16_f32 v50, v52, v53\n\t"
    "v_cvt_pk_bf16_f32 v51, v54, v55\n\t"
    "global_store_dwordx4 %[co0], v[48:51], s[36:37] offset:2048 sc1\n\t"
    "s_waitcnt lgkmcnt(0)\n\t"
    "v_cvt_pk_bf16_f32 v56, v56, v57\n\t"
    "v_cvt_pk_bf16_f32 v57, v58, v59\n\t"
    "v_cvt_pk_bf16_f32 v58, v60, v61\n\t"
    "v_cvt_pk_bf16_f32 v59, v62, v63\n\t"
    "global_store_dwordx4 %[co1], v[56:59], s[36:37] offset:2048 sc1\n\t"
    "s_add_u32 s36, s36, %[ab]\n\t"
    "s_addc_u32 s37, s37, 0\n\t"
    "s_cmp_le_i32 %[b], %[bl]\n\t"
    "s_cbranch_scc1 5f\n\t"
    "v_mov_b32 v72, %[cn]\n\t"
    "v_mov_b32 v73, 1\n\t"
    "s_waitcnt vmcnt(4)\n\t"
    "s_mov_b64 exec, 1\n\t"
    "ds_add_rtn_u32 v73, v72, v73\n\t"
    "s_add_u32 %[st], %[b], %[po]\n\t"
    "v_mov_b32 v75, %[st]\n\t"
    "v_add_u32 v74, -1, v75\n\t"
    "s_cmp_gt_i32 %[b], %[bo]\n\t"
    "s_cselect_b32 %[st], %[st], 0\n\t"
    "v_mov_b32 v75, %[st]\n\t"
    "v_mov_b32 v72, 0\n\t"
    "s_waitcnt lgkmcnt(0)\n\t"
    "v_readfirstlane_b32 %[st], v73\n\t"
    "s_and_b32 %[st], %[st], 3\n\t"
    "s_cmp_lg_u32 %[st], 3\n\t"
    "s_cbranch_scc1 61f\n\t"
    "global_store_dwordx2 v72, v[74:75], %[pg] sc1\n\t"
    "61:\n\t"
    "s_mov_b64 exec, -1\n\t"
    "s_mov_b32 %[pp], 0\n\t"
    "s_mov_b32 %[pp], 0\n\t"
    "s_cmp_lt_i32 %[b], %[ns]\n\t"
    "s_cbranch_scc1 1b\n\t"
    "s_barrier\n\t"
    "s_branch 9f\n\t"
    "5:\n\t"
    "s_lshr_b32 %[pp], %[pp], 8\n\t"
    "s_cbranch_scc1 1b\n\t"
    "v_mov_b32 v72, %[cn]\n\t"
    "v_mov_b32 v73, 1\n\t"
    "s_waitcnt vmcnt(0)\n\t"
    "s_mov_b64 exec, 1\n\t"
    "ds_add_rtn_u32 v73, v72, v73\n\t"
    "s_add_u32 %[st], %[b], %[po]\n\t"
    "v_mov_b32 v75, %[st]\n\t"
    "v_add_u32 v74, 0, v75\n\t"
    "v_mov_b32 v72, 0\n\t"
    "s_waitcnt lgkmcnt(0)\n\t"
    "v_readfirstlane_b32 %[st], v73\n\t"
    "s_and_b32 %[st], %[st], 3\n\t"
    "s_cmp_lg_u32 %[st], 3\n\t"
    "s_cbranch_scc1 61f\n\t"
    "global_store_dword v72, v74, %[pg] sc1\n\t"
    "61:\n\t"
    "s_mov_b64 exec, -1\n\t"
    "s_mov_b32 %[pp], 0\n\t"
    "s_cmp_lt_i32 %[b], %[ns]\n\t"
    "s_cbranch_scc1 1b\n\t"
    "s_barrier\n\t"
    "s_branch 9f\n\t"
    "4:\n\t"
    "v_mov_b32 v39, %[b]\n\t"
    "ds_write_b32 %[fa], v39 offset:16\n\t"
    "s_waitcnt lgkmcnt(0)\n\t"
    "s_barrier\n\t"
    "s_branch 9f\n\t"
    "7:\n\t"
    "v_mov_b32 v72, %[cn]\n\t"
    "v_mov_b32 v73, 1\n\t"
    "s_waitcnt vmcnt(0)\n\t"
    "s_mov_b64 exec, 1\n\t"
    "ds_add_rtn_u32 v73, v72, v73\n\t"
    "s_add_u32 %[st], %[b], %[po]\n\t"
    "v_mov_b32 v75, %[st]\n\t"
    "v_add_u32 v74, 0, v75\n\t"
    "v_mov_b32 v72, 0\n\t"
    "s_waitcnt lgkmcnt(0)\n\t"
    "v_readfirstlane_b32 %[st], v73\n\t"
    "s_and_b32 %[st], %[st], 3\n\t"
    "s_cmp_lg_u32 %[st], 3\n\t"
    "s_cbranch_scc1 61f\n\t"
    "global_store_dword v72, v74, %[pg] sc1\n\t"
    "61:\n\t"
    "s_mov_b64 exec, -1\n\t"
    "s_mov_b32 %[pp], 0\n\t"
    "s_branch 8b\n\t"
    "9:"
    : [ea] "+&v"(ea), [da] "+&v"(da), [b] "+&s"(b), [st] "=&s"(st), "+{s[36:37]}"(p0), [pp] "+&s"(pp)
    : [ns] "s"(ns), [ab] "s"(ab), [rw] "v"(rw), [fa] "v"(fa), "{v[128:159]}"(W[0]), "{v[160:191]}"(W[1]), "{v[192:223]}"(W[2]), "{v[224:255]}"(W[3]), [be] "s"(be), [bo] "s"(bo), [bl] "s"(bl), [po] "s"(po), [cn] "s"(cn), [pg] "s"(pg), [co0] "v"(co0), [co1] "v"(co1)
    : "v39", "v40", "v41", "v42", "v43", "v44", "v45", "v46", "v47", "v48", "v49", "v50", "v51", "v52", "v53", "v54", "v55", "v56", "v57", "v58", "v59", "v60", "v61", "v62", "v63", "v72", "v73", "v74", "v75", "v80", "v81", "v82", "v83", "v84", "v85", "v86", "v87", "v88", "v89", "v90", "v91", "v92", "v93", "v94", "v95", "v96", "v97", "v98", "v99", "v100", "v101", "v102", "v103", "v104", "v105", "v106", "v107", "v108", "v109", "v110", "v111", "v112", "v113", "v114", "v115", "v116", "v117", "v118", "v119", "v120", "v121", "v122", "v123", "v124", "v125", "v126", "v127", "vcc", "scc", "m0", "memory");
}
__device__ __forceinline__ void fin_ld(const u32x16& pd, unsigned rw, unsigned fa, unsigned fv) {
  asm volatile("ds_write_b128 %[rw], v[80:83] offset:0\n\t"
    "ds_write_b128 %[rw], v[84:87] offset:1024\n\t"
    "ds_write_b128 %[rw], v[88:91] offset:2048\n\t"
    "ds_write_b128 %[rw], v[92:95] offset:3072\n\t"
    "ds_write_b32 %[fa], %[fv]"
    :
    : [rw] "v"(rw), "{v[80:95]}"(pd), [fa] "v"(fa), [fv] "v"(fv)
    : "memory");
}
__device__ __forceinline__ void fin_st0(const u32x16& pd, unsigned rw, unsigned fa, unsigned fv, const char* cb, unsigned co0, unsigned co1) {
  asm volatile("1:\n\t"
    "ds_read_b32 v39, %[fa]\n\t"
    "ds_read_b128 v[48:51], %[rw] offset:0\n\t"
    "ds_read_b128 v[52:55], %[rw] offset:1024\n\t"
    "ds_read_b128 v[56:59], %[rw] offset:2048\n\t"
    "ds_read_b128 v[60:63], %[rw] offset:3072\n\t"
    "s_waitcnt lgkmcnt(2)\n\t"
    "v_cmp_ne_u32 vcc, v39, %[fv]\n\t"
    "s_cbranch_vccz 2f\n\t"
    "s_waitcnt lgkmcnt(0)\n\t"
    "s_branch 1b\n\t"
    "2:\n\t"
    "v_add_f32 v48, v48, v80\n\t"
    "v_add_f32 v49, v49, v81\n\t"
    "v_add_f32 v50, v50, v82\n\t"
    "v_add_f32 v51, v51, v83\n\t"
    "v_add_f32 v52, v52, v84\n\t"
    "v_add_f32 v53, v53, v85\n\t"
    "v_add_f32 v54, v54, v86\n\t"
    "v_add_f32 v55, v55, v87\n\t"
    "v_cvt_pk_bf16_f32 v40, v48, v49\n\t"
    "v_cvt_pk_bf16_f32 v41, v50, v51\n\t"
    "v_cvt_pk_bf16_f32 v42, v52, v53\n\t"
    "v_cvt_pk_bf16_f32 v43, v54, v55\n\t"
    "global_store_dwordx4 %[co0], v[40:43], %[cb] sc1\n\t"
    "s_waitcnt lgkmcnt(0)\n\t"
    "v_add_f32 v56, v56, v88\n\t"
    "v_add_f32 v57, v57, v89\n\t"
    "v_add_f32 v58, v58, v90\n\t"
    "v_add_f32 v59, v59, v91\n\t"
    "v_add_f32 v60, v60, v92\n\t"
    "v_add_f32 v61, v61, v93\n\t"
    "v_add_f32 v62, v62, v94\n\t"
    "v_add_f32 v63, v63, v95\n\t"
    "v_cvt_pk_bf16_f32 v44, v56, v57\n\t"
    "v_cvt_pk_bf16_f32 v45, v58, v59\n\t"
    "v_cvt_pk_bf16_f32 v46, v60, v61\n\t"
    "v_cvt_pk_bf16_f32 v47, v62, v63\n\t"
    "global_store_dwordx4 %[co1], v[44:47], %[cb] sc1\n\t"
    "s_nop 1"
    :
    : [rw] "v"(rw), "{v[80:95]}"(pd), [fa] "v"(fa), [fv] "v"(fv), [cb] "s"(cb), [co0] "v"(co0), [co1] "v"(co1)
    : "v39", "v40", "v41", "v42", "v43", "v44", "v45", "v46", "v47", "v48", "v49", "v50", "v51", "v52", "v53", "v54", "v55", "v56", "v57", "v58", "v59", "v60", "v61", "v62", "v63", "vcc", "memory");
}
__device__ __forceinline__ void fin_st1(const u32x16& pd, unsigned rw, unsigned fa, unsigned fv, const char* cb, unsigned co0, unsigned co1) {
  asm volatile("1:\n\t"
    "ds_read_b32 v39, %[fa]\n\t"
    "ds_read_b128 v[48:51], %[rw] offset:0\n\t"
    "ds_read_b128 v[52:55], %[rw] offset:1024\n\t"
    "ds_read_b128 v[56:59], %[rw] offset:2048\n\t"
    "ds_read_b128 v[60:63], %[rw] offset:3072\n\t"
    "s_waitcnt lgkmcnt(2)\n\t"
    "v_cmp_ne_u32 vcc, v39, %[fv]\n\t"
    "s_cbranch_vccz 2f\n\t"
    "s_waitcnt lgkmcnt(0)\n\t"
    "s_branch 1b\n\t"
    "2:\n\t"
    "v_add_f32 v48, v48, v80\n\t"
    "v_add_f32 v49, v49, v81\n\t"
    "v_add_f32 v50, v50, v82\n\t"
    "v_add_f32 v51, v51, v83\n\t"
    "v_add_f32 v52, v52, v84\n\t"
    "v_add_f32 v53, v53, v85\n\t"
    "v_add_f32 v54, v54, v86\n\t"
    "v_add_f32 v55, v55, v87\n\t"
    "s_waitcnt lgkmcnt(0)\n\t"
    "v_add_f32 v56, v56, v88\n\t"
    "v_add_f32 v57, v57, v89\n\t"
    "v_add_f32 v58, v58, v90\n\t"
    "v_add_f32 v59, v59, v91\n\t"
    "v_add_f32 v60, v60, v92\n\t"
    "v_add_f32 v61, v61, v93\n\t"
    "v_add_f32 v62, v62, v94\n\t"
    "v_add_f32 v63, v63, v95\n\t"
    "v_mul_f32 v40, %[nl], v48\n\t"
    "v_mul_f32 v41, %[nl], v49\n\t"
    "v_mul_f32 v42, %[nl], v50\n\t"
    "v_mul_f32 v43, %[nl], v51\n\t"
    "v_mul_f32 v44, %[nl], v56\n\t"
    "v_mul_f32 v45, %[nl], v57\n\t"
    "v_mul_f32 v46, %[nl], v58\n\t"
    "v_mul_f32 v47, %[nl], v59\n\t"
    "v_exp_f32 v40, v40\n\t"
    "v_exp_f32 v41, v41\n\t"
    "v_exp_f32 v42, v42\n\t"
    "v_exp_f32 v43, v43\n\t"
    "v_exp_f32 v44, v44\n\t"
    "v_exp_f32 v45, v45\n\t"
    "v_exp_f32 v46, v46\n\t"
    "v_exp_f32 v47, v47\n\t"
    "v_add_f32 v40, 1.0, v40\n\t"
    "v_add_f32 v41, 1.0, v41\n\t"
    "v_add_f32 v42, 1.0, v42\n\t"
    "v_add_f32 v43, 1.0, v43\n\t"
    "v_add_f32 v44, 1.0, v44\n\t"
    "v_add_f32 v45, 1.0, v45\n\t"
    "v_add_f32 v46, 1.0, v46\n\t"
    "v_add_f32 v47, 1.0, v47\n\t"
    "v_rcp_f32 v40, v40\n\t"
    "v_rcp_f32 v41, v41\n\t"
    "v_rcp_f32 v42, v42\n\t"
    "v_rcp_f32 v43, v43\n\t"
    "v_rcp_f32 v44, v44\n\t"
    "v_rcp_f32 v45, v45\n\t"
    "v_rcp_f32 v46, v46\n\t"
    "v_rcp_f32 v47, v47\n\t"
    "v_mul_f32 v40, v48, v40\n\t"
    "v_mul_f32 v41, v49, v41\n\t"
    "v_mul_f32 v42, v50, v42\n\t"
    "v_mul_f32 v43, v51, v43\n\t"
    "v_mul_f32 v44, v56, v44\n\t"
    "v_mul_f32 v45, v57, v45\n\t"
    "v_mul_f32 v46, v58, v46\n\t"
    "v_mul_f32 v47, v59, v47\n\t"
    "v_mul_f32 v40, v40, v52\n\t"
    "v_mul_f32 v41, v41, v53\n\t"
    "v_mul_f32 v42, v42, v54\n\t"
    "v_mul_f32 v43, v43, v55\n\t"
    "v_mul_f32 v44, v44, v60\n\t"
    "v_mul_f32 v45, v45, v61\n\t"
    "v_mul_f32 v46, v46, v62\n\t"
    "v_mul_f32 v47, v47, v63\n\t"
    "v_cvt_pk_bf16_f32 v48, v40, v41\n\t"
    "v_cvt_pk_bf16_f32 v49, v42, v43\n\t"
    "v_cvt_pk_bf16_f32 v50, v44, v45\n\t"
    "v_cvt_pk_bf16_f32 v51, v46, v47\n\t"
    "s_nop 1\n\t"
    "v_permlane16_swap_b32 v48, v50\n\t"
    "v_permlane16_swap_b32 v49, v51\n\t"
    "global_store_dwordx4 %[co0], v[48:51], %[cb] sc1\n\t"
    "s_nop 1"
    :
    : [rw] "v"(rw), "{v[80:95]}"(pd), [fa] "v"(fa), [fv] "v"(fv), [cb] "s"(cb), [co0] "v"(co0), [nl] "s"(-LOG2E)
    : "v39", "v40", "v41", "v42", "v43", "v44", "v45", "v46", "v47", "v48", "v49", "v50", "v51", "v52", "v53", "v54", "v55", "v56", "v57", "v58", "v59", "v60", "v61", "v62", "v63", "vcc", "memory");
}
__device__ __forceinline__ void fin_st2(const u32x16& pd, unsigned rw, unsigned fa, unsigned fv, const char* cb, unsigned co0, unsigned co1) {
  asm volatile("s_nop 1\n\t"
    "v_cvt_pk_bf16_f32 v40, v80, v81\n\t"
    "v_cvt_pk_bf16_f32 v41, v82, v83\n\t"
    "v_cvt_pk_bf16_f32 v42, v84, v85\n\t"
    "v_cvt_pk_bf16_f32 v43, v86, v87\n\t"
    "global_store_dwordx4 %[co0], v[40:43], %[cb] sc1\n\t"
    "v_cvt_pk_bf16_f32 v44, v88, v89\n\t"
    "v_cvt_pk_bf16_f32 v45, v90, v91\n\t"
    "v_cvt_pk_bf16_f32 v46, v92, v93\n\t"
    "v_cvt_pk_bf16_f32 v47, v94, v95\n\t"
    "global_store_dwordx4 %[co1], v[44:47], %[cb] sc1\n\t"
    "1:\n\t"
    "ds_read_b32 v39, %[fa]\n\t"
    "ds_read_b128 v[48:51], %[rw] offset:0\n\t"
    "ds_read_b128 v[52:55], %[rw] offset:1024\n\t"
    "ds_read_b128 v[56:59], %[rw] offset:2048\n\t"
    "ds_read_b128 v[60:63], %[rw] offset:3072\n\t"
    "s_waitcnt lgkmcnt(2)\n\t"
    "v_cmp_ne_u32 vcc, v39, %[fv]\n\t"
    "s_cbranch_vccz 2f\n\t"
    "s_waitcnt lgkmcnt(0)\n\t"
    "s_branch 1b\n\t"
    "2:\n\t"
    "v_cvt_pk_bf16_f32 v48, v48, v49\n\t"
    "v_cvt_pk_bf16_f32 v49, v50, v51\n\t"
    "v_cvt_pk_bf16_f32 v50, v52, v53\n\t"
    "v_cvt_pk_bf16_f32 v51, v54, v55\n\t"
    "global_store_dwordx4 %[co0], v[48:51], %[cb] offset:2048 sc1\n\t"
    "s_waitcnt lgkmcnt(0)\n\t"
    "v_cvt_pk_bf16_f32 v56, v56, v57\n\t"
    "v_cvt_pk_bf16_f32 v57, v58, v59\n\t"
    "v_cvt_pk_bf16_f32 v58, v60, v61\n\t"
    "v_cvt_pk_bf16_f32 v59, v62, v63\n\t"
    "global_store_dwordx4 %[co1], v[56:59], %[cb] offset:2048 sc1\n\t"
    "s_nop 1"
    :
    : [rw] "v"(rw), "{v[80:95]}"(pd), [fa] "v"(fa), [fv] "v"(fv), [cb] "s"(cb), [co0] "v"(co0), [co1] "v"(co1)
    : "v39", "v40", "v41", "v42", "v43", "v44", "v45", "v46", "v47", "v48", "v49", "v50", "v51", "v52", "v53", "v54", "v55", "v56", "v57", "v58", "v59", "v60", "v61", "v62", "v63", "vcc", "memory");
}
template <int R> __device__ __forceinline__ void gw_load(u32x32& w, unsigned a0, unsigned a1);
template <> __device__ __forceinline__ void gw_load<0>(u32x32& w, unsigned a0, unsigned a1) {
  asm volatile("ds_read_b64_tr_b16 v[128:129], %1 offset:0\n\t"
    "ds_read_b64_tr_b16 v[130:131], %1 offset:512\n\t"
    "ds_read_b64_tr_b16 v[132:133], %2 offset:0\n\t"
    "ds_read_b64_tr_b16 v[134:135], %2 offset:512\n\t"
    "ds_read_b64_tr_b16 v[136:137], %1 offset:4096\n\t"
    "ds_read_b64_tr_b16 v[138:139], %1 offset:4608\n\t"
    "ds_read_b64_tr_b16 v[140:141], %2 offset:4096\n\t"
    "ds_read_b64_tr_b16 v[142:143], %2 offset:4608\n\t"
    "s_waitcnt lgkmcnt(0)\n\t"
    "ds_read_b64_tr_b16 v[144:145], %1 offset:16384\n\t"
    "ds_read_b64_tr_b16 v[146:147], %1 offset:16896\n\t"
    "ds_read_b64_tr_b16 v[148:149], %2 offset:16384\n\t"
    "ds_read_b64_tr_b16 v[150:151], %2 offset:16896\n\t"
    "ds_read_b64_tr_b16 v[152:153], %1 offset:20480\n\t"
    "ds_read_b64_tr_b16 v[154:155], %1 offset:20992\n\t"
    "ds_read_b64_tr_b16 v[156:157], %2 offset:20480\n\t"
    "ds_read_b64_tr_b16 v[158:159], %2 offset:20992\n\t"
    "s_waitcnt lgkmcnt(0)"
    : "=&{v[128:159]}"(w) : "v"(a0), "v"(a1) : "memory");
}
template <> __device__ __forceinline__ void gw_load<1>(u32x32& w, unsigned a0, unsigned a1) {
  asm volatile("ds_read_b64_tr_b16 v[160:161], %1 offset:0\n\t"
    "ds_read_b64_tr_b16 v[162:163], %1 offset:512\n\t"
    "ds_read_b64_tr_b16 v[164:165], %2 offset:0\n\t"
    "ds_read_b64_tr_b16 v[166:167], %2 offset:512\n\t"
    "ds_read_b64_tr_b16 v[168:169], %1 offset:4096\n\t"
    "ds_read_b64_tr_b16 v[170:171], %1 offset:4608\n\t"
    "ds_read_b64_tr_b16 v[172:173], %2 offset:4096\n\t"
    "ds_read_b64_tr_b16 v[174:175], %2 offset:4608\n\t"
    "s_waitcnt lgkmcnt(0)\n\t"
    "ds_read_b64_tr_b16 v[176:177], %1 offset:16384\n\t"
    "ds_read_b64_tr_b16 v[178:179], %1 offset:16896\n\t"
    "ds_read_b64_tr_b16 v[180:181], %2 offset:16384\n\t"
    "ds_read_b64_tr_b16 v[182:183], %2 offset:16896\n\t"
    "ds_read_b64_tr_b16 v[184:185], %1 offset:20480\n\t"
    "ds_read_b64_tr_b16 v[186:187], %1 offset:20992\n\t"
    "ds_read_b64_tr_b16 v[188:189], %2 offset:20480\n\t"
    "ds_read_b64_tr_b16 v[190:191], %2 offset:20992\n\t"
    "s_waitcnt lgkmcnt(0)"
    : "=&{v[160:191]}"(w) : "v"(a0), "v"(a1) : "memory");
}
template <> __device__ __forceinline__ void gw_load<2>(u32x32& w, unsigned a0, unsigned a1) {
  asm volatile("ds_read_b64_tr_b16 v[192:193], %1 offset:0\n\t"
    "ds_read_b64_tr_b16 v[194:195], %1 offset:512\n\t"
    "ds_read_b64_tr_b16 v[196:197], %2 offset:0\n\t"
    "ds_read_b64_tr_b16 v[198:199], %2 offset:512\n\t"
    "ds_read_b64_tr_b16 v[200:201], %1 offset:4096\n\t"
    "ds_read_b64_tr_b16 v[202:203], %1 offset:4608\n\t"
    "ds_read_b64_tr_b16 v[204:205], %2 offset:4096\n\t"
    "ds_read_b64_tr_b16 v[206:207], %2 offset:4608\n\t"
    "s_waitcnt lgkmcnt(0)\n\t"
    "ds_read_b64_tr_b16 v[208:209], %1 offset:16384\n\t"
    "ds_read_b64_tr_b16 v[210:211], %1 offset:16896\n\t"
    "ds_read_b64_tr_b16 v[212:213], %2 offset:16384\n\t"
    "ds_read_b64_tr_b16 v[214:215], %2 offset:16896\n\t"
    "ds_read_b64_tr_b16 v[216:217], %1 offset:20480\n\t"
    "ds_read_b64_tr_b16 v[218:219], %1 offset:20992\n\t"
    "ds_read_b64_tr_b16 v[220:221], %2 offset:20480\n\t"
    "ds_read_b64_tr_b16 v[222:223], %2 offset:20992\n\t"
    "s_waitcnt lgkmcnt(0)"
    : "=&{v[192:223]}"(w) : "v"(a0), "v"(a1) : "memory");
}
template <> __device__ __forceinline__ void gw_load<3>(u32x32& w, unsigned a0, unsigned a1) {
  asm volatile("ds_read_b64_tr_b16 v[224:225], %1 offset:0\n\t"
    "ds_read_b64_tr_b16 v[226:227], %1 offset:512\n\t"
    "ds_read_b64_tr_b16 v[228:229], %2 offset:0\n\t"
    "ds_read_b64_tr_b16 v[230:231], %2 offset:512\n\t"
    "ds_read_b64_tr_b16 v[232:233], %1 offset:4096\n\t"
    "ds_read_b64_tr_b16 v[234:235], %1 offset:4608\n\t"
    "ds_read_b64_tr_b16 v[236:237], %2 offset:4096\n\t"
    "ds_read_b64_tr_b16 v[238:239], %2 offset:4608\n\t"
    "s_waitcnt lgkmcnt(0)\n\t"
    "ds_read_b64_tr_b16 v[240:241], %1 offset:16384\n\t"
    "ds_read_b64_tr_b16 v[242:243], %1 offset:16896\n\t"
    "ds_read_b64_tr_b16 v[244:245], %2 offset:16384\n\t"
    "ds_read_b64_tr_b16 v[246:247], %2 offset:16896\n\t"
    "ds_read_b64_tr_b16 v[248:249], %1 offset:20480\n\t"
    "ds_read_b64_tr_b16 v[250:251], %1 offset:20992\n\t"
    "ds_read_b64_tr_b16 v[252:253], %2 offset:20480\n\t"
    "ds_read_b64_tr_b16 v[254:255], %2 offset:20992\n\t"
    "s_waitcnt lgkmcnt(0)"
    : "=&{v[224:255]}"(w) : "v"(a0), "v"(a1) : "memory");
}
template <int I, int E, class F> __device__ __forceinline__ void sfor(F&& f) { if constexpr (I < E) { f(std::integral_constant<int, I>{}); sfor<I + 1, E>(f); } }
template <int ROLE, int KQ, class F0, class F>
__device__ __forceinline__ void wload(char* smem, int wave_id_, const u16* W0, const u16* W1, int ldb, u32x32 (&bw)[4], int rot, F0&& hook0, F&& hook, unsigned long long* wts = nullptr) {
  constexpr int ABUF = 65536;
  const int tid = opaque_tid(), lane = tid & 63, wave = WAVE_ID;
  const int kq = KQ == 2 ? wave >> 2 : 0, nq = KQ == 2 ? wave & 3 : wave;
  const int g = lane >> 4, r16 = lane & 15, q4 = r16 >> 2, p4 = lane & 3;
  const unsigned ldsb = __builtin_amdgcn_readfirstlane((unsigned)(size_t)(LAS char*)smem);
  {
    const int srow = 8 * wave + (lane >> 3), ch = (lane & 7) ^ bswz(srow);
    const unsigned voff = (unsigned)(srow * ldb + ch * 8) * 2;
    const char* w0 = uni((const char*)W0); const char* w1 = uni((const char*)(ROLE == R_F1 ? W1 : W0 + 64));
    const size_t kb = (size_t)64 * ldb * 2;
    auto wsrc = [&](int r, int i) {
      const int kbi = KQ == 2 ? (ROLE == R_PART2 ? 0 : (i >> 2) * 8) + ((2 * r + ((i >> 1) & 1)) ^ rot) : 2 * r + (i >> 2);
      return KQ == 2 ? ((i & 1) ? w1 : w0) + kbi * kb + (ROLE == R_PART2 ? (i >> 2) * 256 : 0) : w0 + kbi * kb + (i & 3) * 128;
    };
    auto wstage = [&](int r) {
      const unsigned ld = ldsb + (r & 1) * ABUF + wave * 1024;
#pragma unroll
      for (int i = 0; i < 8; ++i) {
        unsigned keep;
        asm volatile("s_mov_b32 %0, m0\n\ts_mov_b32 m0, %3\n\ts_nop 0\n\tglobal_load_lds_dwordx4 %1, %2" WNT_ "\n\ts_mov_b32 m0, %0"
                     : "=&s"(keep) : "v"(voff), "s"(wsrc(r, i)), "s"(ld + i * 8192) : "memory");
      }
    };
    const int brow = 8 * g + q4, bs = bswz(brow);
    unsigned la[2];
#pragma unroll
    for (int j = 0; j < 2; ++j) {
      int blk, c16, sub;
      if (ROLE == R_F1) { blk = j; c16 = nq * 2 + (p4 >> 1); sub = (p4 & 1) * 8; }
      else { blk = nq >> 1; c16 = (nq & 1) * 4 + p4; sub = j * 8; }
      int set = kq;
      if (ROLE == R_PART2 && TLP) { set = nq >> 1; blk = nq & 1; c16 = kq * 4 + p4; }
      la[j] = (unsigned)((KQ == 2 ? set * 4 + blk : blk) * 8192 + brow * 128 + ((c16 ^ bs) << 4) + sub);
    }
    wstage(0); wstage(1);
    u32x4 raw[2][8];
#pragma unroll
    for (int r = 0; r < 2; ++r)
#pragma unroll
      for (int i = 0; i < 8; ++i) asm volatile("global_load_dwordx4 %0, %1, %2" WNT_ : "=&v"(raw[r][i]) : "v"(voff), "s"(wsrc(r + 2, i)) : "memory");
    sfor<0, 4>([&](auto r_c) __attribute__((always_inline)) {
      constexpr int r = decltype(r_c)::value;
      if (r == 0) WAIT_V(24); else if (r == 1) WAIT_V(16);
      if (r == 3) { BAR(); hook(); } else if (r < 2) { __builtin_amdgcn_s_barrier(); asm volatile("" ::: "memory"); }
      gw_load<r>(bw[r], ldsb + (r & 1) * ABUF + la[0], ldsb + (r & 1) * ABUF + la[1]);
      if (r == 1 || r == 2) {
        u32x4 (&q)[8] = raw[(r + 1) & 1];
        if (r == 1) hook0();
        if (r == 1) asm volatile("s_waitcnt vmcnt(8)" : "+v"(q[0]), "+v"(q[1]), "+v"(q[2]), "+v"(q[3]), "+v"(q[4]), "+v"(q[5]), "+v"(q[6]), "+v"(q[7]) :: "memory");
        else asm volatile("s_waitcnt vmcnt(0)" : "+v"(q[0]), "+v"(q[1]), "+v"(q[2]), "+v"(q[3]), "+v"(q[4]), "+v"(q[5]), "+v"(q[6]), "+v"(q[7]) :: "memory");
#pragma unroll
        for (int i = 0; i < 8; ++i) *(u32x4*)(smem + ((r + 1) & 1) * ABUF + wave * 1024 + i * 8192 + lane * 16) = q[i];
        if (r == 1) BAR();
      }
      if (r == 3) BAR();
    });
  }
}
#define DSR2(d0, d1, a0_, a1_, off) asm volatile("ds_read_b128 %0, %2 offset:%4\n\tds_read_b128 %1, %3 offset:%4" : "=&v"(d0), "=&v"(d1) : "v"(a0_), "v"(a1_), "i"(off))
template <int N> __device__ __forceinline__ void waitl(u32x4& a, u32x4& b) { asm volatile("s_waitcnt lgkmcnt(%2)" : "+v"(a), "+v"(b) : "i"(N)); }
__device__ __forceinline__ void glds1(const char* sbase, unsigned voff, unsigned lds) {
  unsigned keep;
  asm volatile("s_mov_b32 %0, m0\n\ts_mov_b32 m0, %3\n\ts_nop 0\n\tglobal_load_lds_dwordx4 %1, %2 sc1\n\ts_mov_b32 m0, %0"
               : "=&s"(keep) : "v"(voff), "s"(sbase), "s"(lds) : "memory");
}
#define XS(i) do { } while (0)
#define XFL(b_) do { } while (0)
#define XA_ 0
#define XF_ 0
#define XE_ 0
#define XP_ 0
#define PUBK 9
#define LATE 0
#define NOWP 2
#define NEARM 15
#define PNEAR 2
#define LATEQ 0
#define DEFP 1
#define ROTM 0
#define ROTK 7
#define BPACE1 42
#define BPACE2 46
#define BPACE 36
#define W2 1
#define PDEEP 4
template <int ROLE>
__device__ __forceinline__ void gemm7(char* smem, int wave_id_, const u16* W0, const u16* W1, int ldb, const char* A_, size_t a_blk, Dep dep,
                                      char* C_, size_t c_blk, unsigned* myprog, unsigned* tmo, int slot, unsigned long long* mts,
                                      bool gl, int nimm = 0, int nseg = NSEG, int wm0 = 0, int pofs = 0, unsigned long long tag = 0, unsigned* par = nullptr) {
  volatile LAS unsigned* wsl = (volatile LAS unsigned*)(smem + L_SLOT + 512);
  if (opaque_tid() < 144) *(volatile LAS unsigned*)(smem + L_SLOT + opaque_tid() * 4) = 0u;
  const int wave = WAVE_ID;
  const int kq = wave >> 2, nq = wave & 3;
  const unsigned ldsb = __builtin_amdgcn_readfirstlane((unsigned)(size_t)(LAS char*)smem);
  const int rot = (ROTM >> ROLE) & 1 ? slot & ROTK : 0;
  const char* A = uni(A_) + nq * 1024;
  constexpr bool NEAR = (NEARM >> ROLE) & 1;
  constexpr int AH = NEAR ? 2 : 3, PD = NEAR ? PNEAR : PDEEP;
  constexpr bool P2C = ROLE == R_PART2;
  int pw = 0;
  int wm = wm0, nh = 0;
  auto lpoll = [&](int b, bool hs = false) {
    unsigned w;
    if (wave == 4) {
      for (int spins = 0;; ++spins) {
        unsigned v;
        if (hs) { WAIT_V(0); v = *(volatile LAS unsigned*)(smem + L_SLOT + 768 + lane_id() * 4); }
        else asm volatile("s_nop 4\n\tglobal_load_dword %0, %1, %2 sc1\n\ts_waitcnt vmcnt(0)" : "=&v"(v) : "v"(dep.voff), "s"(dep.base) : "memory");
        w = dep_eval(v, dep);
        if ((int)w > b) break;
        if (!hs) __builtin_amdgcn_s_sleep(2);
        hs = false;
        if (give_up(spins, tmo, 9)) { w = NSEG; break; }
      }
      w = w < 255u ? w : 255u;
      if (lane_id() == 0) wsl[0] = ((unsigned)(b + 1) << 8) | w;
    } else {
      for (;;) {
        w = __builtin_amdgcn_readfirstlane(wsl[0]);
        if ((w >> 8) == (unsigned)(b + 1)) break;
        __builtin_amdgcn_s_sleep(1);
      }
      w &= 255u;
    }
    return (int)w;
  };
  auto pubw = [&](int n) {
    WAIT_V(0);
    if (lane_id() == 0) {
      const unsigned o = __hip_atomic_fetch_add((LAS unsigned*)(smem + L_SLOT + 516), 1u, __ATOMIC_RELAXED, __HIP_MEMORY_SCOPE_WORKGROUP);
      if ((o & 3u) == 3u) {
        st4_sc1(myprog, (unsigned)(n + pofs));
      }
    }
    asm volatile("s_waitcnt lgkmcnt(0)" ::: "memory");
  };
  auto hsrc = [&](int hh) { return A + (size_t)(hh >> 1) * a_blk + ((hh ^ (rot >> 2)) & 1) * 16384; };
  auto hlds = [&](int hh) { return ldsb + (unsigned)(((hh >> 1) & 1) * 65536 + (hh & 1) * 16384 + nq * 1024); };
#define POFF(i_) (((i_) & 1) * 32768 + ((i_) >> 1) * 4096)
#define PIECE(s_, l_, i_) asm volatile("s_add_u32 m0, %2, %3\n\ts_nop 0\n\tglobal_load_lds_dwordx4 %0, %1 sc1" :: "v"(av[i_]), "s"(s_), "s"(l_), "i"(POFF(i_)) : "memory", "scc", "m0")
  u32x32 BW[4];
  KTS(1);
  wload<ROLE, 2>(smem, wave_id_, W0, W1, ldb, BW, rot, [&]() {
    if (ROLE == R_QKV && nimm > 0 && wave == 7) ini_request((const char*)tmo - OFF_TMO, ldsb + L_GB);
  }, [&]() {
    if (ROLE == R_QKV && nimm > 0) {
      *par = ini_resolve((const char*)tmo - OFF_TMO, smem + L_GB, tag);
      dep.base += (*par >> 2) << 13; myprog += (*par >> 2) << 11;
    }
    if (kq) {
      if (wm0 > 0) wait_l1((const unsigned*)dep.base, 8u, tmo);
      else wm = lpoll(0);
      const unsigned l16 = lane_id() * 16;
#pragma unroll
      for (int i = 0; i < 16; ++i)
        if (!(P2C && (i & 1))) glds1(hsrc(i >> 3) + (i & 1) * 32768 + ((((i & 7) >> 1) ^ rot) & 3) * 4096, l16, hlds(i >> 3) + POFF(i & 7));
    }
    nh = 2;
  }, ROLE == R_QKV ? (unsigned long long*)((char*)tmo - OFF_TMO + ((size_t)246 << 20)) + (128 + slot) * 8 : nullptr);
  KTS(2);
  if (wm0 == 0 && !kq) wm = (int)(__builtin_amdgcn_readfirstlane(wsl[0]) & 255u);
  if (ROLE == R_QKV && opaque_tid() == 0) STS((char*)tmo - OFF_TMO, 128 + slot, 0);
  auto body = [&](auto ldr_c) __attribute__((always_inline)) {
    constexpr bool LDR = decltype(ldr_c)::value;
    if (XP_ == 0 ? !LDR : (XP_ == 1 ? LDR : false)) __builtin_amdgcn_s_setprio(1);
    unsigned av[8];
    const int lane = lane_id();
#pragma unroll
    for (int i = 0; i < 8; ++i) av[i] = lane * 16 + (P2C ? 0 : (i & 1) * 32768) + (((i >> 1) ^ rot) & 3) * 4096;
    auto issue_all = [&](int hh) {
      if (LDR) {
        const char* s_ = hsrc(hh); const unsigned l_ = hlds(hh);
#pragma unroll
        for (int i = 0; i < 8; ++i) if (!(P2C && (i & 1))) PIECE(s_, l_, i);
      }
    };
    int b = 0;
    bool hs = false, redo = false;
    while (b < nseg) {
      const int ln = lane_id(), g = ln >> 4, r16 = ln & 15;
      const unsigned e0 = ldsb + (unsigned)((P2C ? 0 : kq * 32768) + (b & 1) * 65536 + (gl ? (g >> 1) * 512 + r16 * 32 + (g & 1) * 16 : r16 * 128 + ((g ^ (r16 & 7)) << 4)));
      const unsigned d0 = e0 ^ (gl ? 1024u : 64u);
      const unsigned rw = ldsb + L_RED + nq * 4096 + ln * 16;
      const unsigned co0 = ROLE == R_F1 ? (unsigned)((g & 1) * 2048 + nq * 512 + r16 * 32 + (g >> 1) * 16)
                         : ((ROLE == R_QKV ? TLQ : TLP) ? (unsigned)(nq * (P2C ? 4096 : 2048) + r16 * 64 + g * 16)
                         : (ROLE == R_QKV ? (unsigned)(r16 * 256 + (((4 * nq + g) ^ r16) << 4)) : (unsigned)(r16 * PROW + (32 * nq + 8 * g) * 2)));
      const unsigned co1 = co0 + ((ROLE == R_QKV ? TLQ : TLP) ? 1024u : (ROLE == R_QKV ? 4096u : 16u * PROW));
      u32x16 pv_;
      bool smp = false, lt0 = false;
      if ((wm < nseg ? wm : nseg) >= b + PD && b >= nimm && nh <= 2 * b + AH && !redo) {
        __builtin_amdgcn_s_barrier(); asm volatile("" ::: "memory");
        for (; nh < 2 * b + AH; ++nh) issue_all(nh);
        int lf = (wm < nseg ? wm : nseg) - (AH - 1), cp = nseg - (AH - 1);
        if (!LDR) KTS(128 + b);
        const unsigned sa = ldsb + L_SLOT + ln * 4, ad = dep.add - (AH - 1);
        if constexpr (LDR) (NEAR ? (P2C ? near_ld2 : near_ld0) : (P2C ? fast_ld2 : fast_ld0))(BW, pv_, e0, d0, b, lf, cp, sa, dep.mul, ad, rw, (unsigned)a_blk, hsrc(2 * b + AH), hsrc(2 * b + AH + 1), hlds(2 * b + AH), hlds(2 * b + AH + 1), av);
        else {
          const char* c0_ = uni((const char*)tmo - OFF_TMO + OFF_DUMP); const char* c1_ = uni(C_ + (size_t)b * c_blk);
          const int sf = b + pofs - (pw > 0 ? 1 : 0);
          const char* pg = uni((const char*)myprog);
          (NEAR ? (ROLE == R_F1 ? near_st1 : (P2C ? near_st2 : near_st0)) : (ROLE == R_F1 ? fast_st1 : (P2C ? fast_st2 : fast_st0)))(BW, pv_, e0, d0, b, lf, cp, sa, dep.mul, ad, rw, (unsigned)c_blk, c0_, c1_, co0, co1, pofs, sf, (unsigned)wave,
                                                                   ldsb + L_SLOT, dep.voff, dep.base, pg);
        }
        if (!LDR) KTS(64 + b);
        wm = lf + (AH - 1) > wm ? lf + (AH - 1) : wm;
        nh = 2 * b + AH;
        --b;
        __builtin_amdgcn_s_barrier(); asm volatile("" ::: "memory");
      } else if (NEAR && b >= (ROLE == R_QKV ? nseg - 1 : (gl ? BPACE2 : (ROLE == R_F1 ? BPACE1 : BPACE))) && !redo) {
        if constexpr (LDR) {
          int cap = wave == 4 ? SPIN_CAP : 8 * SPIN_CAP;
          if (hs) WAIT_V(0);
          (P2C ? paced_ld2 : paced_ld0)(BW, e0, d0, b, nseg, (unsigned)a_blk, rw, ldsb + L_SLOT + 528 + nq * 4, hsrc(nh), hsrc(nh + 1), hlds(nh), cap, wm, (unsigned)(nh - 2 * b),
                                        dep.mul, dep.add, dep.voff + (ROLE == R_QKV ? 0 : (ln >> 5) * 4), dep.base, wave == 4 ? ldsb + L_SLOT : 0u, ldsb + L_SLOT + (ln & 31) * 4, av);
          if (cap == 0 && ln == 0) st4_sc1(tmo, 15u);
        } else (ROLE == R_F1 ? paced_st1 : (P2C ? paced_st2 : paced_st0))(BW, e0, d0, b, nseg, (unsigned)c_blk, rw, ldsb + L_SLOT + 528 + nq * 4, uni(C_ + (size_t)b * c_blk),
                                                                          __builtin_amdgcn_readfirstlane(pw > 0 ? 1u : 0u), ROLE == R_QKV ? 999 : (gl ? BEG : (ROLE == R_F1 ? BEH : BEARLY)),
                                                                          ROLE == R_F1 ? BEG : BEARLY, ROLE == R_QKV ? 999 : (gl ? LAZY : (ROLE == R_F1 ? BEG : BEARLY)), pofs, ldsb + L_SLOT + 516,
                                                                          uni((const char*)myprog), co0, co1);
        const int fb = (int)__builtin_amdgcn_readfirstlane(*(volatile LAS unsigned*)(smem + L_SLOT + 544 + nq * 4));
        if (fb == 0) { if (!LDR && ROLE != R_QKV) pubw(nseg); break; }
        WAIT_V(0); BAR();
        if (LDR && ln == 0) { *(volatile LAS unsigned*)(smem + L_SLOT + 528 + nq * 4) = 0u; *(volatile LAS unsigned*)(smem + L_SLOT + 544 + nq * 4) = 0u; wsl[0] = 0u; }
        BAR();
        b = fb - 1; nh = 2 * b; wm = b; pw = 0; hs = false; redo = true;
        if (!LDR) pubw(b);
        continue;
      } else {
        const bool pl = wm <= b;
        XS(0);
        if (pl && LDR) wm = lpoll(b, hs);
        XS(1);
        const bool cold = nh < 2 * b + 2;
        { const int upto = nh == 2 * b + 3 ? 2 * b + 4 : 2 * b + 2; for (; nh < upto; ++nh) issue_all(nh); }
        smp = nh == 2 * b + 2 && b + 1 < nseg && wm0 < nseg;
        if (LDR && wave == 4 && smp)
          asm volatile("s_mov_b32 m0, %0\n\ts_nop 3\n\tglobal_load_lds_dword %1, %2 sc1" :: "s"(ldsb + L_SLOT + (b & 1) * 256), "v"(dep.voff), "s"(dep.base) : "memory", "m0");
        XS(2);
        if (LDR && LEAN_QB) { if (nh >= 2 * b + 4) { if (P2C) WAIT_V(14); else WAIT_V(28); } else { if (P2C) WAIT_V(6); else WAIT_V(12); } }
        else if (LDR) { if (nh >= 2 * b + 4) { if (P2C) WAIT_V(12); else WAIT_V(24); } else { if (P2C) WAIT_V(4); else WAIT_V(8); } }
        BAR();
        XS(3);
        if (pl && !LDR) wm = (int)(__builtin_amdgcn_readfirstlane(wsl[0]) & 255u);
        const bool dm = nh == 2 * b + 2 && wm > b + 1 && b + 1 < nseg;
        if (ROLE == R_QKV && b < 2 && opaque_tid() == 0) STS((char*)tmo - OFF_TMO, 128 + slot, 1 + b);
        lt0 = smp && !dm;
        if constexpr (LDR) {
          if (dm) (P2C ? lean_dm1 : lean_dm0)(BW, pv_, e0, d0, 0u, hlds(nh), hsrc(nh), hsrc(nh + 1), av);
          else (P2C ? lean_ld1 : lean_ld0)(BW, pv_, e0, d0, (unsigned)(nh - 2 * b - 2), hlds(nh), hsrc(nh), hsrc(nh + 1), av, ldsb + L_SLOT + (b & 1) * 256 + ln * 4, dep.mul, dep.add, LATEQ && lt0 ? b + 1 : 0x7FFFFFFF,
                                           __builtin_amdgcn_readfirstlane(lt0 && wave == 4 ? ldsb + L_SLOT + 768 : 0u), dep.voff, dep.base);
        }
        else lean_st0(BW, pv_, e0, d0, __builtin_amdgcn_readfirstlane(pw > 0 ? (cold ? 4u : 1u) : 0u), (unsigned)(pw + pofs), ldsb + L_SLOT + 516, ldsb + L_SLOT + 256,
                     dep.voff, dep.base, uni((const char*)myprog));
        if (dm) nh += 2;
        XS(5);
      }
      if constexpr (LDR) fin_ld(pv_, rw, ldsb + L_SLOT + 528 + nq * 4, (unsigned)(b + 1));
      else (ROLE == R_F1 ? fin_st1 : (P2C ? fin_st2 : fin_st0))(pv_, rw, ldsb + L_SLOT + 528 + nq * 4, (unsigned)(b + 1), uni(C_ + (size_t)b * c_blk), co0, co1);
      XS(6);
      if (smp) {
        const int w = (int)dep_eval(*(volatile LAS unsigned*)(smem + L_SLOT + (b & 1) * 256 + lane_id() * 4), dep);
        if (LATEQ && lt0 && w > b + 1) nh += 2;
        wm = w > wm ? w : wm;
      }
      bool now = (NOWP == 2 ? nh < 2 * b + 4 && wm < b + 1 + PD : wm <= b + 1) || b + 1 >= nseg || b < nimm;
      if (!LDR && now) {
        if (DEFP && b >= nimm && b + 1 < nseg && (wm > b + 1 || (__builtin_amdgcn_readfirstlane(wsl[0]) >> 8) == (unsigned)(b + 2))) now = false;
        else pubw(b + 1);
      }
      XS(7);
      XFL(b);
      pw = now ? 0 : b + 1;
      hs = W2 && lt0;
      redo = false;
      ++b;
    }
    __builtin_amdgcn_s_setprio(0);
  };
  if (kq) body(std::true_type{}); else body(std::false_type{});
#undef PIECE
#undef POFF
  WAIT_V(0);
  BAR();
  KTS(3);
}
__device__ __forceinline__ void fin_role(char* smem, int wave_id_, int fidx, const float* mod, const char* P2, const u16* x1, u16* out, unsigned* cnt, unsigned* tmo,
                                         unsigned long long* mts) {
  const int tid = opaque_tid(), lane = tid & 63, wave = WAVE_ID;
  row_table(smem + L_GB, mod, cnt, tmo, tid);
  volatile LAS unsigned* wsl = (volatile LAS unsigned*)(smem + L_SLOT + 512);
  int wf = 0, par = 0;
  for (int nf = fidx >> 3; nf < NSEG; nf += 2) {
    if (wf <= nf) {
      if (wave == 0) {
        unsigned w;
        for (int spins = 0;; ++spins) {
          u32x2 v2; asm volatile("global_load_dwordx2 %0, %1, off sc1\n\ts_waitcnt vmcnt(0)" : "=&v"(v2) : "v"(cnt + (PF2 + ((lane & 31) < 28 ? (lane & 31) : 0)) * 32) : "memory");
          const unsigned v = rowmin16(max(v2.x, v2.y));
          w = min(rdl(v, 0), rdl(v, 16));
          if ((int)w > nf) break;
          __builtin_amdgcn_s_sleep(2);
          if (give_up(spins, tmo, 9)) { w = NSEG; break; }
        }
        if (lane == 0) wsl[par] = w;
      }
      BAR();
      wf = (int)__builtin_amdgcn_readfirstlane(wsl[par]);
      par ^= 1;
    }
    int ln = lane; asm volatile("" : "+v"(ln));
    if (wave == 0) WTS(256 + 8 + fidx, nf / 2, 0);
    for (bool chk = nf >= BEH; !fin_half(P2, x1, smem + L_GB + 12288, out, nf * 32 + 4 * (fidx & 7) + (wave >> 1), wave & 1, ln, chk); chk = false)
      for (int spins = 0;; ++spins) {
        const unsigned v = ld4_sc1(cnt + (ln < 28 ? PF2 + ln : PLN + fidx) * 32);
        if (__all((ln < 28 ? v : 2 * v) > (unsigned)nf)) break;
        __builtin_amdgcn_s_sleep(2);
        if (give_up(spins, tmo, 9)) break;
      }
    if (wave == 0) WTS(256 + 8 + fidx, nf / 2, 2);
  }
}
constexpr int Q_MEM = 0;
constexpr int Q_QK  = 16384;
constexpr int Q_TM  = 22528;
constexpr int Q_PQ  = 24576;
constexpr int Q_K   = 32768;
constexpr int Q_RING = 32768;
constexpr int NCHAIN = 96;
#define NQQ 6
#define NQK 4
#define E_AG 7
#define RDYI -1
#define ROTW 2
#define DQU 6
#define DQM 18
#define DQD 1
#define ORSEL (xg >> 1)
#define D_Q 96
#define D_O 3
#define D_A 0
#define WOF 1
#define E_S 0
#define SPF 0
#define STE 0
#define RAE 0
#define SPUB 62
#define QDL 0
#define KFB 1
#define E_B1 0
#define ADQ 1
#define D_V 4
#define DRY 0
#define D_0 72
#define KV3 0
static_assert(ARMW + 1 < BEARLY, "");
#define RAW 5
#define E_MIN 3
#define E_W 3
#define DOU 0
#define DOM 12
#define CL0 1
#define TSW 0
#define E_B 6
#define E_O 1900
#define KDEL 5
#define VPOS 0

typedef __bf16 bf16x16 __attribute__((ext_vector_type(16)));
typedef unsigned u32x8 __attribute__((ext_vector_type(8)));
__device__ __forceinline__ f32x4 SMF(u32x4 a, u32x8 b, f32x4 c, int idx) {
  return __builtin_amdgcn_smfmac_f32_16x16x64_bf16(__builtin_bit_cast(bf16x8, a), __builtin_bit_cast(bf16x16, b), c, idx, 0, 0);
}
__device__ __forceinline__ float fold32(float a, float b) {
  const auto p = __builtin_amdgcn_permlane32_swap(__float_as_uint(a), __float_as_uint(b), false, false);
  return __uint_as_float(p[0]) + __uint_as_float(p[1]);
}
__device__ __forceinline__ f32x2 rowsum(f32x4 x) { return f32x2{fold32(x[0], x[2]), fold32(x[1], x[3])}; }
__device__ __forceinline__ unsigned nosent1(unsigned v) { return v < 0xFFFFFFFEu ? v : 0xFFFFFFFEu; }
__device__ __forceinline__ void st4s(const char* sb, unsigned vo, unsigned v) { asm volatile("global_store_dword %0, %1, %2 sc1\n\ts_nop 0" :: "v"(vo), "v"(v), "s"(sb) : "memory"); }
__device__ __forceinline__ void st8s(const char* sb, unsigned vo, u32x2 v) { asm volatile("global_store_dwordx2 %0, %1, %2 sc1\n\ts_nop 1" :: "v"(vo), "v"(v), "s"(sb) : "memory"); }
__device__ __forceinline__ void gldsw(const char* sbase, unsigned voff, unsigned lds) {
  unsigned keep;
  asm volatile("s_mov_b32 %0, m0\n\ts_mov_b32 m0, %3\n\ts_nop 0\n\tglobal_load_lds_dwordx4 %1, %2" CNT_ "\n\ts_mov_b32 m0, %0"
               : "=&s"(keep) : "v"(voff), "s"(sbase), "s"(lds) : "memory");
}
#define PWC 1
#define PWL 0
__device__ __forceinline__ void wait_clk(unsigned t0, unsigned d) {
  while ((unsigned)__builtin_amdgcn_s_memtime() - t0 < d) __builtin_amdgcn_s_sleep(PWC);
}
__device__ __forceinline__ unsigned bell_time(const char* a, unsigned vo, unsigned tag, unsigned cap, unsigned v = 0) {
  for (unsigned spins = 0; !__all((v >> 24) >= tag); ++spins) {
    asm volatile("s_nop 4\n\tglobal_load_dword %0, %1, %2 sc1\n\ts_waitcnt vmcnt(0)" : "=&v"(v) : "v"(vo), "s"(a) : "memory");
    if (__all((v >> 24) >= tag) || spins >= cap) break;
  }
  const unsigned now = (unsigned)__builtin_amdgcn_s_memrealtime() + 250u;
  const unsigned age = rowmin16((now - v) & 0xFFFFFFu);
  unsigned r = __builtin_amdgcn_readfirstlane(now - min(min(min(rdl(age, 0), rdl(age, 16)), min(rdl(age, 32), rdl(age, 48))), 500u));
  asm volatile("" : "+s"(r));
  return r;
}
#define PWS 0
__device__ __forceinline__ void wait_rt(unsigned t0, int d) {
  for (;;) {
    const int r = d - (int)((unsigned)__builtin_amdgcn_s_memrealtime() - t0);
    if (r <= 0) break;
    if (PWS && r > 24) __builtin_amdgcn_s_sleep(6); else if (PWS && r > 9) __builtin_amdgcn_s_sleep(2);
  }
}
#define WBAR() do { __builtin_amdgcn_s_barrier(); asm volatile("" ::: "memory"); } while (0)
#define SB() __builtin_amdgcn_sched_barrier(0)
#define TN(k, v) do { } while (0)
template <bool HALF>
__device__ __forceinline__ void stage_proj(const char* src, unsigned lds, int tid, int wave) {
  const int row = tid >> 4, f = 2 * ((row & 1) | (((row >> 2) & 3) << 1));
#pragma unroll
  for (int j = 0; j < 4; ++j) {
    const int pc = (tid & 15) ^ f;
    gldsw(src + (size_t)j * 32 * HD * 2, (unsigned)(HALF ? (row + (pc >> 3) * 128) * HD * 2 + ((pc & 7) << 4) : row * HD * 2 + (pc << 4)), lds + (wave * 64 + 512 * j) * 16);
  }
}
__device__ __forceinline__ unsigned proj_base(int T, int lane) {
  const int g = lane >> 4, q4 = (lane & 15) >> 2, p4 = lane & 3, fs = 2 * ((q4 & 1) | ((g & 1) << 1) | ((q4 >> 1) << 2));
  return (unsigned)((16 * (g >> 1) + 4 * (g & 1) + (q4 & 1) + 8 * (q4 >> 1)) * 256 + (((2 * T) ^ fs) << 4) + p4 * 8);
}
__device__ __forceinline__ f32x4 zero4() { float z = 0.0f; asm volatile("" : "+v"(z)); return f32x4{z, z, z, z}; }
__device__ __forceinline__ unsigned lds_word(const char* p) { return __builtin_amdgcn_readfirstlane(*(volatile LAS unsigned*)p); }

#define MFMA16(a, b, c) __builtin_amdgcn_mfma_f32_16x16x16bf16_1k((a), (b), (c), 0, 0, 0)
#define UF(x) __uint_as_float(x)
__device__ __forceinline__ unsigned umax3(unsigned a, unsigned b, unsigned c) { return max(max(a, b), c); }
__device__ __forceinline__ f32x2 logit2(float a, float b) {
  return f32x2{a, b};
}
constexpr float LSC = LOG2E * SCALE;

#define FCS() do { } while (0)
struct QKW { u32x32 w0, w1, w2, w3, w4, w5; };
template <int W_> __device__ __forceinline__ void qk_step(const QKW& W, const char* ag, const char* vs, const char* v2, const char* db, const char* od, unsigned pb, unsigned sn, unsigned& cap, unsigned& sp,
    unsigned long long& tc, unsigned long long* ts, unsigned& d0, unsigned& d1);
template <> __device__ __forceinline__ void qk_step<0>(const QKW& W, const char* ag, const char* vs, const char* v2, const char* db, const char* od, unsigned pb, unsigned sn, unsigned& cap, unsigned& sp,
    unsigned long long& tc, unsigned long long* ts, unsigned& d0, unsigned& d1) {
  unsigned st, bg;
  asm volatile("v_mbcnt_lo_u32_b32 v60, -1, 0\n\t"
    "v_mbcnt_hi_u32_b32 v60, -1, v60\n\t"
    "v_lshlrev_b32 v5, 4, v60\n\t"
    "s_mov_b32 %[sp], 0\n\t"
    "s_setprio 2\n\t"
    "global_load_dwordx4 v[24:27], v5, %[ag] sc1\n\t"
    "global_load_dwordx4 v[28:31], v5, %[ag] offset:1024 sc1\n\t"
    "s_memtime %[tc]\n\t"
    "v_lshrrev_b32 v32, 4, v60\n\t"
    "v_and_b32 v33, 15, v60\n\t"
    "v_lshrrev_b32 v34, 2, v33\n\t"
    "v_and_b32 v35, 3, v60\n\t"
    "v_and_b32 v61, 1, v60\n\t"
    "v_lshlrev_b32 v61, 3, v61\n\t"
    "v_lshl_or_b32 v7, v34, 4, v61\n\t"
    "v_lshl_or_b32 v7, v32, 7, v7\n\t"
    "v_and_b32 v10, 2, v34\n\t"
    "v_xor_b32 v10, v10, v32\n\t"
    "v_lshl_or_b32 v10, v10, 4, %[pb]\n\t"
    "v_lshl_or_b32 v10, v33, 6, v10\n\t"
    "v_and_b32 v14, 32, v60\n\t"
    "v_lshl_or_b32 v14, v35, 3, v14\n\t"
    "v_lshrrev_b32 v34, 2, v60\n\t"
    "v_lshl_or_b32 v14, v34, 6, v14\n\t"
    "v_or_b32 v14, %[pb], v14\n\t"
    "v_and_b32 v61, 3, v60\n\t"
    "v_lshlrev_b32 v61, 6, v61\n\t"
    "v_lshl_or_b32 v4, v32, 4, v61\n\t"
    "v_and_b32 v61, 4, v60\n\t"
    "v_lshl_or_b32 v4, v61, 10, v4\n\t"
    "v_and_b32 v61, 8, v60\n\t"
    "v_lshl_or_b32 v4, v61, 14, v4\n\t"
    "v_or_b32 v4, 0, v4\n\t"
    "v_and_b32 v61, 2, v60\n\t"
    "v_mul_u32_u24 v61, 0x5555, v61\n\t"
    "v_or_b32 v6, %[c44], v61\n\t"
    "v_lshlrev_b32 v61, 2, v32\n\t"
    "v_lshl_or_b32 v15, v33, 4, v61\n\t"
    "3:\n\t"
    "v_mov_b32 v16, 0\n\t"
    "v_mov_b32 v17, 0\n\t"
    "v_mov_b32 v18, 0\n\t"
    "v_mov_b32 v19, 0\n\t"
    "v_mov_b32 v20, 0\n\t"
    "v_mov_b32 v21, 0\n\t"
    "v_mov_b32 v22, 0\n\t"
    "v_mov_b32 v23, 0\n\t"
    "s_waitcnt vmcnt(0) lgkmcnt(0)\n\t"
    "33:\n\t"
    "ds_write_b128 v5, v[24:27] offset:0\n\t"
    "ds_write_b128 v5, v[28:31] offset:1024\n\t"
    "v_smfmac_f32_16x16x64_bf16 v[16:19], v[24:27], v[64:71], v6\n\t"
    "v_max3_u32 v60, v24, v25, v26\n\t"
    "v_max3_u32 v61, v27, v28, v29\n\t"
    "v_smfmac_f32_16x16x64_bf16 v[20:23], v[24:27], v[72:79], v6\n\t"
    "v_max3_u32 v60, v60, v30, v31\n\t"
    "v_max_u32 v60, v60, v61\n\t"
    "v_cmp_eq_u32 vcc, -1, v60\n\t"
    "s_cbranch_vccnz 1f\n\t"
    "2:\n\t"
    "s_waitcnt lgkmcnt(0)\n\t"
    "s_barrier\n\t"
    "ds_read_b128 v[32:35], v5 offset:2048\n\t"
    "ds_read_b128 v[36:39], v5 offset:3072\n\t"
    "v_smfmac_f32_16x16x64_bf16 v[16:19], v[28:31], v[80:87], v6\n\t"
    "ds_read_b128 v[40:43], v5 offset:4096\n\t"
    "ds_read_b128 v[44:47], v5 offset:5120\n\t"
    "v_smfmac_f32_16x16x64_bf16 v[20:23], v[28:31], v[88:95], v6\n\t"
    "s_waitcnt lgkmcnt(3)\n\t"
    "v_smfmac_f32_16x16x64_bf16 v[16:19], v[32:35], v[96:103], v6\n\t"
    "ds_read_b128 v[48:51], v5 offset:6144\n\t"
    "v_smfmac_f32_16x16x64_bf16 v[20:23], v[32:35], v[104:111], v6\n\t"
    "ds_read_b128 v[52:55], v5 offset:7168\n\t"
    "s_waitcnt lgkmcnt(4)\n\t"
    "v_smfmac_f32_16x16x64_bf16 v[16:19], v[36:39], v[112:119], v6\n\t"
    "ds_read_b128 v[24:27], v5 offset:8192\n\t"
    "v_smfmac_f32_16x16x64_bf16 v[20:23], v[36:39], v[120:127], v6\n\t"
    "ds_read_b128 v[28:31], v5 offset:9216\n\t"
    "s_waitcnt lgkmcnt(5)\n\t"
    "v_smfmac_f32_16x16x64_bf16 v[16:19], v[40:43], v[128:135], v6\n\t"
    "ds_read_b128 v[56:59], v5 offset:10240\n\t"
    "v_smfmac_f32_16x16x64_bf16 v[20:23], v[40:43], v[136:143], v6\n\t"
    "ds_read_b128 v[60:63], v5 offset:11264\n\t"
    "ds_read_b128 v[36:39], v10 offset:35840\n\t"
    "s_waitcnt lgkmcnt(7)\n\t"
    "v_smfmac_f32_16x16x64_bf16 v[16:19], v[44:47], v[144:151], v6\n\t"
    "ds_read_b128 v[40:43], v10 offset:36864\n\t"
    "v_smfmac_f32_16x16x64_bf16 v[20:23], v[44:47], v[152:159], v6\n\t"
    "ds_read_b128 v[44:47], v10 offset:37888\n\t"
    "s_waitcnt lgkmcnt(8)\n\t"
    "v_smfmac_f32_16x16x64_bf16 v[16:19], v[48:51], v[160:167], v6\n\t"
    "v_smfmac_f32_16x16x64_bf16 v[20:23], v[48:51], v[168:175], v6\n\t"
    "ds_read_b128 v[48:51], v10 offset:38912\n\t"
    "s_waitcnt lgkmcnt(8)\n\t"
    "v_smfmac_f32_16x16x64_bf16 v[16:19], v[52:55], v[176:183], v6\n\t"
    "v_smfmac_f32_16x16x64_bf16 v[20:23], v[52:55], v[184:191], v6\n\t"
    "ds_read_b128 v[52:55], v10 offset:39936\n\t"
    "s_waitcnt lgkmcnt(8)\n\t"
    "v_smfmac_f32_16x16x64_bf16 v[16:19], v[24:27], v[192:199], v6\n\t"
    "v_smfmac_f32_16x16x64_bf16 v[20:23], v[24:27], v[200:207], v6\n\t"
    "ds_read_b128 v[24:27], v10 offset:32768\n\t"
    "s_waitcnt lgkmcnt(8)\n\t"
    "v_smfmac_f32_16x16x64_bf16 v[16:19], v[28:31], v[208:215], v6\n\t"
    "ds_read_b32 v8, v15 offset:28672\n\t"
    "ds_read_b128 v[32:35], v5 offset:24576\n\t"
    "s_setprio 3\n\t"
    "s_waitcnt lgkmcnt(9)\n\t"
    "v_smfmac_f32_16x16x64_bf16 v[16:19], v[56:59], v[224:231], v6\n\t"
    "s_waitcnt lgkmcnt(8)\n\t"
    "v_smfmac_f32_16x16x64_bf16 v[16:19], v[60:63], v[240:247], v6\n\t"
    "v_smfmac_f32_16x16x64_bf16 v[20:23], v[28:31], v[216:223], v6\n\t"
    "s_waitcnt lgkmcnt(0)\n\t"
    "21:\n\t"
    "v_cmp_ne_u32 vcc, %[sn], v8\n\t"
    "s_cbranch_vccnz 22f\n\t"
    "v_mul_f32 v32, %[lsc], v32\n\t"
    "v_mul_f32 v33, %[lsc], v33\n\t"
    "v_smfmac_f32_16x16x64_bf16 v[20:23], v[56:59], v[232:239], v6\n\t"
    "v_mul_f32 v34, %[lsc], v34\n\t"
    "v_mul_f32 v35, %[lsc], v35\n\t"
    "v_add_f32 v16, v16, v18\n\t"
    "v_add_f32 v17, v17, v19\n\t"
    "v_smfmac_f32_16x16x64_bf16 v[20:23], v[60:63], v[248:255], v6\n\t"
    "v_fma_f32 v16, v16, %[lsc], v32\n\t"
    "v_fma_f32 v17, v17, %[lsc], v33\n\t"
    "v_cvt_pk_bf16_f32 v16, v16, v17\n\t"
    "ds_write_b32 v15, v16 offset:16384\n\t"
    "s_mov_b32 %[bg], %[big]\n\t"
    "s_nop 2\n\t"
    "v_add_f32 v20, v20, v22\n\t"
    "v_add_f32 v21, v21, v23\n\t"
    "v_fma_f32 v20, v20, %[lsc], v34\n\t"
    "v_fma_f32 v21, v21, %[lsc], v35\n\t"
    "v_cvt_pk_bf16_f32 v20, v20, v21\n\t"
    "ds_write_b32 v15, v20 offset:16640\n\t"
    "ds_read_b128 v[28:31], v10 offset:33792\n\t"
    "ds_read_b128 v[32:35], v10 offset:34816\n\t"
    "s_waitcnt lgkmcnt(2)\n\t"
    "s_barrier\n\t"
    "ds_read_b64_tr_b16 v[16:17], v7 offset:16384\n\t"
    "ds_read_b64_tr_b16 v[18:19], v7 offset:16448\n\t"
    "ds_read_b64_tr_b16 v[20:21], v7 offset:16896\n\t"
    "ds_read_b64_tr_b16 v[22:23], v7 offset:16960\n\t"
    "ds_read_b64_tr_b16 v[56:57], v7 offset:17408\n\t"
    "ds_read_b64_tr_b16 v[58:59], v7 offset:17472\n\t"
    "ds_read_b64_tr_b16 v[60:61], v7 offset:17920\n\t"
    "ds_read_b64_tr_b16 v[62:63], v7 offset:17984\n\t"
    "global_load_dwordx4 v[6:9], v5, %[vs] sc1\n\t"
    "global_load_dwordx4 v[10:13], v5, %[vs] offset:1024 sc1\n\t"
    "s_waitcnt lgkmcnt(6)\n\t"
    "v_mfma_f32_16x16x32_bf16 v[24:27], v[24:27], v[16:19], 0\n\t"
    "v_mfma_f32_16x16x32_bf16 v[28:31], v[28:31], v[16:19], 0\n\t"
    "s_waitcnt lgkmcnt(4)\n\t"
    "v_mfma_f32_16x16x32_bf16 v[24:27], v[32:35], v[20:23], v[24:27]\n\t"
    "v_mfma_f32_16x16x32_bf16 v[28:31], v[36:39], v[20:23], v[28:31]\n\t"
    "v_xor_b32 v39, 32, v14\n\t"
    "ds_read_b64_tr_b16 v[32:33], v14 offset:40960\n\t"
    "ds_read_b64_tr_b16 v[34:35], v14 offset:41984\n\t"
    "ds_read_b64_tr_b16 v[36:37], v39 offset:40960\n\t"
    "ds_read_b64_tr_b16 v[38:39], v39 offset:41984\n\t"
    "s_waitcnt lgkmcnt(6)\n\t"
    "v_mfma_f32_16x16x32_bf16 v[24:27], v[40:43], v[56:59], v[24:27]\n\t"
    "v_mfma_f32_16x16x32_bf16 v[28:31], v[44:47], v[56:59], v[28:31]\n\t"
    "s_waitcnt lgkmcnt(4)\n\t"
    "v_mfma_f32_16x16x32_bf16 v[24:27], v[48:51], v[60:63], v[24:27]\n\t"
    "v_mfma_f32_16x16x32_bf16 v[28:31], v[52:55], v[60:63], v[28:31]\n\t"
    "v_mov_b32 v52, %[one]\n\t"
    "v_mov_b32 v53, %[one]\n\t"
    "v_mov_b32 v54, %[one]\n\t"
    "v_mov_b32 v55, %[one]\n\t"
    "v_mov_b32 v45, 0\n\t"
    "v_mov_b32 v47, 0\n\t"
    "v_mov_b32 v48, %[one]\n\t"
    "v_max3_f32 v14, |v24|, |v25|, |v26|\n\t"
    "v_max3_f32 v40, |v27|, |v28|, |v29|\n\t"
    "v_max3_f32 v14, v14, |v30|, |v31|\n\t"
    "v_max_f32 v14, v14, v40\n\t"
    "v_cmp_nge_f32 vcc, %[big], v14\n\t"
    "v_mov_b32 v14, 0\n\t"
    "s_cbranch_vccnz 12f\n\t"
    "13:\n\t"
    "v_exp_f32 v24, v24\n\t"
    "v_exp_f32 v25, v25\n\t"
    "v_exp_f32 v26, v26\n\t"
    "v_exp_f32 v27, v27\n\t"
    "v_exp_f32 v28, v28\n\t"
    "v_exp_f32 v29, v29\n\t"
    "v_exp_f32 v30, v30\n\t"
    "v_exp_f32 v31, v31\n\t"
    "v_cvt_pk_bf16_f32 v40, v24, v25\n\t"
    "v_cvt_pk_bf16_f32 v41, v26, v27\n\t"
    "v_cvt_pk_bf16_f32 v42, v28, v29\n\t"
    "v_cvt_pk_bf16_f32 v43, v30, v31\n\t"
    "v_mov_b32 v49, 0\n\t"
    "v_mov_b32 v51, 0\n\t"
    "s_waitcnt lgkmcnt(0)\n\t"
    "v_mfma_f32_16x16x32_bf16 v[52:55], v[52:55], v[40:43], 0\n\t"
    "v_mfma_f32_16x16x32_bf16 v[24:27], v[32:35], v[40:43], 0\n\t"
    "v_mfma_f32_16x16x32_bf16 v[28:31], v[36:39], v[40:43], 0\n\t"
    "s_waitcnt vmcnt(1)\n\t"
    "5:\n\t"
    "v_mfma_f32_16x16x32_bf16 v[32:35], v[6:9], v[16:19], 0\n\t"
    "v_mfma_f32_16x16x32_bf16 v[36:39], v[6:9], v[20:23], 0\n\t"
    "s_waitcnt vmcnt(0)\n\t"
    "v_mfma_f32_16x16x32_bf16 v[32:35], v[10:13], v[56:59], v[32:35]\n\t"
    "v_mfma_f32_16x16x32_bf16 v[36:39], v[10:13], v[60:63], v[36:39]\n\t"
    "ds_read_b32 v44, v15 offset:18432\n\t"
    "ds_read_b32 v46, v15 offset:18688\n\t"
    "v_max3_u32 v40, v6, v7, v8\n\t"
    "v_max3_u32 v41, v9, v10, v11\n\t"
    "v_max3_u32 v40, v40, v12, v13\n\t"
    "v_max_u32 v40, v40, v41\n\t"
    "v_cmp_eq_u32 vcc, -1, v40\n\t"
    "s_cbranch_vccnz 4f\n\t"
    "6:\n\t"
    "s_nop 0\n\t"
    "v_add_f32 v32, v32, v38\n\t"
    "v_add_f32 v33, v33, v39\n\t"
    "v_exp_f32 v34, v32\n\t"
    "v_max_f32 v40, v32, v33\n\t"
    "v_exp_f32 v35, v33\n\t"
    "v_cmp_nge_f32 vcc, %[bg], v40\n\t"
    "s_cbranch_vccnz 10f\n\t"
    "11:\n\t"
    "v_cvt_pk_bf16_f32 v50, v34, v35\n\t"
    "s_nop 1\n\t"
    "v_mfma_f32_16x16x16_bf16 v[52:55], v[48:49], v[50:51], v[52:55]\n\t"
    "17:\n\t"
    "s_waitcnt vmcnt(0) lgkmcnt(0)\n\t"
    "v_mfma_f32_16x16x16_bf16 v[32:35], v[44:45], v[50:51], v[24:27]\n\t"
    "v_mfma_f32_16x16x16_bf16 v[36:39], v[46:47], v[50:51], v[28:31]\n\t"
    "v_max_u32 v40, v44, v46\n\t"
    "v_cmp_eq_u32 vcc, -1, v40\n\t"
    "s_nop 2\n\t"
    "v_rcp_f32 v42, v52\n\t"
    "s_cbranch_vccnz 18f\n\t"
    "19:\n\t"
    "v_pk_mul_f32 v[32:33], v[32:33], v[42:43] op_sel_hi:[1,0]\n\t"
    "v_pk_mul_f32 v[34:35], v[34:35], v[42:43] op_sel_hi:[1,0]\n\t"
    "v_pk_mul_f32 v[36:37], v[36:37], v[42:43] op_sel_hi:[1,0]\n\t"
    "v_pk_mul_f32 v[38:39], v[38:39], v[42:43] op_sel_hi:[1,0]\n\t"
    "v_cvt_pk_bf16_f32 v24, v32, v33\n\t"
    "v_cvt_pk_bf16_f32 v25, v34, v35\n\t"
    "v_cvt_pk_bf16_f32 v26, v36, v37\n\t"
    "v_cvt_pk_bf16_f32 v27, v38, v39\n\t"
    "v_min_u32 v24, -2, v24\n\t"
    "v_min_u32 v25, -2, v25\n\t"
    "v_min_u32 v26, -2, v26\n\t"
    "v_min_u32 v27, -2, v27\n\t"
    "global_store_dwordx4 v4, v[24:27], %[od] sc1\n\t"
    "ds_write_b128 v5, v[6:9] offset:20480\n\t"
    "s_setprio 1\n\t"
    "s_waitcnt lgkmcnt(0)\n\t"
    "s_barrier\n\t"
    "s_waitcnt lgkmcnt(0)\n\t"
    "s_branch 9f\n\t"
    "1:\n\t"
    "s_add_u32 %[sp], %[sp], 1\n\t"
    "s_cmp_ge_u32 %[sp], %[cap]\n\t"
    "s_cbranch_scc1 7f\n\t"
    "global_load_dwordx4 v[24:27], v5, %[ag] sc1\n\t"
    "global_load_dwordx4 v[28:31], v5, %[ag] offset:1024 sc1\n\t"
    "s_waitcnt lgkmcnt(0)\n\t"
    "s_memtime %[tc]\n\t"
    "s_nop 7\n\t"
    "s_branch 3b\n\t"
    "7:\n\t"
    "s_mov_b32 %[cap], 0\n\t"
    "s_branch 2b\n\t"
    "4:\n\t"
    "s_add_u32 %[sp], %[sp], 0x10000\n\t"
    "s_lshr_b32 %[st], %[sp], 16\n\t"
    "s_cmp_ge_u32 %[st], %[cap]\n\t"
    "s_cbranch_scc1 8f\n\t"
    "global_load_dwordx4 v[6:9], v5, %[vs] sc1\n\t"
    "global_load_dwordx4 v[10:13], v5, %[vs] offset:1024 sc1\n\t"
    "s_waitcnt vmcnt(0) lgkmcnt(0)\n\t"
    "s_branch 5b\n\t"
    "8:\n\t"
    "s_mov_b32 %[cap], 0\n\t"
    "s_branch 6b\n\t"
    "18:\n\t"
    "s_add_u32 %[sp], %[sp], 0x10000\n\t"
    "s_lshr_b32 %[st], %[sp], 16\n\t"
    "s_cmp_ge_u32 %[st], %[cap]\n\t"
    "s_cbranch_scc1 20f\n\t"
    "ds_read_b32 v44, v15 offset:18432\n\t"
    "ds_read_b32 v46, v15 offset:18688\n\t"
    "s_waitcnt vmcnt(0) lgkmcnt(0)\n\t"
    "s_branch 17b\n\t"
    "20:\n\t"
    "s_mov_b32 %[cap], 0\n\t"
    "s_branch 19b\n\t"
    "22:\n\t"
    "ds_read_b32 v8, v15 offset:28672\n\t"
    "ds_read_b128 v[32:35], v5 offset:24576\n\t"
    "s_waitcnt lgkmcnt(0)\n\t"
    "s_branch 21b\n\t"
    "10:\n\t"
    "v_max_f32 v40, v40, v14\n\t"
    "v_mov_b32 v42, v40\n\t"
    "s_nop 1\n\t"
    "v_permlane16_swap_b32 v40, v42\n\t"
    "v_max_f32 v40, v40, v42\n\t"
    "v_mov_b32 v42, v40\n\t"
    "s_nop 1\n\t"
    "v_permlane32_swap_b32 v40, v42\n\t"
    "v_max_f32 v40, v40, v42\n\t"
    "v_sub_f32 v34, v32, v40\n\t"
    "v_sub_f32 v35, v33, v40\n\t"
    "v_sub_f32 v40, v14, v40\n\t"
    "v_exp_f32 v40, v40\n\t"
    "v_exp_f32 v34, v34\n\t"
    "v_exp_f32 v35, v35\n\t"
    "v_mul_f32 v24, v24, v40\n\t"
    "v_mul_f32 v25, v25, v40\n\t"
    "v_mul_f32 v26, v26, v40\n\t"
    "v_mul_f32 v27, v27, v40\n\t"
    "v_mul_f32 v28, v28, v40\n\t"
    "v_mul_f32 v29, v29, v40\n\t"
    "v_mul_f32 v30, v30, v40\n\t"
    "v_mul_f32 v31, v31, v40\n\t"
    "v_mul_f32 v52, v52, v40\n\t"
    "v_mul_f32 v53, v53, v40\n\t"
    "v_mul_f32 v54, v54, v40\n\t"
    "v_mul_f32 v55, v55, v40\n\t"
    "s_branch 11b\n\t"
    "12:\n\t"
    "v_max3_f32 v14, v24, v25, v26\n\t"
    "v_max3_f32 v40, v27, v28, v29\n\t"
    "v_max3_f32 v14, v14, v30, v31\n\t"
    "v_max3_f32 v14, v14, v40, %[neg]\n\t"
    "v_mov_b32 v40, v14\n\t"
    "s_nop 1\n\t"
    "v_permlane16_swap_b32 v14, v40\n\t"
    "v_max_f32 v14, v14, v40\n\t"
    "v_mov_b32 v40, v14\n\t"
    "s_nop 1\n\t"
    "v_permlane32_swap_b32 v14, v40\n\t"
    "v_max_f32 v14, v14, v40\n\t"
    "v_sub_f32 v24, v24, v14\n\t"
    "v_sub_f32 v25, v25, v14\n\t"
    "v_sub_f32 v26, v26, v14\n\t"
    "v_sub_f32 v27, v27, v14\n\t"
    "v_sub_f32 v28, v28, v14\n\t"
    "v_sub_f32 v29, v29, v14\n\t"
    "v_sub_f32 v30, v30, v14\n\t"
    "v_sub_f32 v31, v31, v14\n\t"
    "s_mov_b32 %[bg], 0x7fc00000\n\t"
    "s_branch 13b\n\t"
    "9:"
    : [tc] "=&s"(tc), [sp] "=&s"(sp), [cap] "+s"(cap), [st] "=&s"(st), [bg] "=&s"(bg)
    : [ag] "s"(ag), [c44] "s"(0x4444u), [lsc] "s"(LSC), [sn] "s"(sn), [pb] "s"(pb), [vs] "s"(vs), [od] "s"(od), [neg] "s"(-1.0e38f), [one] "s"(0x3F803F80u), [big] "s"(24.0f), "{v[64:95]}"(W.w0), "{v[96:127]}"(W.w1), "{v[128:159]}"(W.w2), "{v[160:191]}"(W.w3), "{v[192:223]}"(W.w4), "{v[224:255]}"(W.w5)
    : "v4", "v5", "v6", "v7", "v8", "v9", "v10", "v11", "v12", "v13", "v14", "v15", "v16", "v17", "v18", "v19", "v20", "v21", "v22", "v23", "v24", "v25", "v26", "v27", "v28", "v29", "v30", "v31", "v32", "v33", "v34", "v35", "v36", "v37", "v38", "v39", "v40", "v41", "v42", "v43", "v44", "v45", "v46", "v47", "v48", "v49", "v50", "v51", "v52", "v53", "v54", "v55", "v56", "v57", "v58", "v59", "v60", "v61", "v62", "v63", "vcc", "scc", "m0", "memory");
}
template <> __device__ __forceinline__ void qk_step<1>(const QKW& W, const char* ag, const char* vs, const char* v2, const char* db, const char* od, unsigned pb, unsigned sn, unsigned& cap, unsigned& sp,
    unsigned long long& tc, unsigned long long* ts, unsigned& d0, unsigned& d1) {
  unsigned st, bg;
  asm volatile("v_mbcnt_lo_u32_b32 v60, -1, 0\n\t"
    "v_mbcnt_hi_u32_b32 v60, -1, v60\n\t"
    "v_lshlrev_b32 v5, 4, v60\n\t"
    "s_mov_b32 %[sp], 0\n\t"
    "s_setprio 2\n\t"
    "global_load_dwordx4 v[24:27], v5, %[ag] sc1\n\t"
    "global_load_dwordx4 v[28:31], v5, %[ag] offset:1024 sc1\n\t"
    "s_memtime %[tc]\n\t"
    "v_lshrrev_b32 v32, 4, v60\n\t"
    "v_and_b32 v33, 15, v60\n\t"
    "v_lshrrev_b32 v34, 2, v33\n\t"
    "v_and_b32 v35, 3, v60\n\t"
    "v_and_b32 v61, 1, v60\n\t"
    "v_lshlrev_b32 v61, 3, v61\n\t"
    "v_lshl_or_b32 v7, v34, 4, v61\n\t"
    "v_lshl_or_b32 v7, v32, 7, v7\n\t"
    "v_and_b32 v10, 2, v34\n\t"
    "v_xor_b32 v10, v10, v32\n\t"
    "v_lshl_or_b32 v10, v10, 4, %[pb]\n\t"
    "v_lshl_or_b32 v10, v33, 6, v10\n\t"
    "v_and_b32 v14, 32, v60\n\t"
    "v_lshl_or_b32 v14, v35, 3, v14\n\t"
    "v_lshrrev_b32 v34, 2, v60\n\t"
    "v_lshl_or_b32 v14, v34, 6, v14\n\t"
    "v_or_b32 v14, %[pb], v14\n\t"
    "v_and_b32 v61, 3, v60\n\t"
    "v_lshlrev_b32 v61, 6, v61\n\t"
    "v_lshl_or_b32 v4, v32, 4, v61\n\t"
    "v_and_b32 v61, 4, v60\n\t"
    "v_lshl_or_b32 v4, v61, 10, v4\n\t"
    "v_and_b32 v61, 8, v60\n\t"
    "v_lshl_or_b32 v4, v61, 14, v4\n\t"
    "v_or_b32 v4, 256, v4\n\t"
    "v_and_b32 v61, 2, v60\n\t"
    "v_mul_u32_u24 v61, 0x5555, v61\n\t"
    "v_or_b32 v6, %[c44], v61\n\t"
    "v_lshlrev_b32 v61, 2, v32\n\t"
    "v_lshl_or_b32 v15, v33, 4, v61\n\t"
    "3:\n\t"
    "v_mov_b32 v16, 0\n\t"
    "v_mov_b32 v17, 0\n\t"
    "v_mov_b32 v18, 0\n\t"
    "v_mov_b32 v19, 0\n\t"
    "v_mov_b32 v20, 0\n\t"
    "v_mov_b32 v21, 0\n\t"
    "v_mov_b32 v22, 0\n\t"
    "v_mov_b32 v23, 0\n\t"
    "s_waitcnt vmcnt(0) lgkmcnt(0)\n\t"
    "33:\n\t"
    "ds_write_b128 v5, v[24:27] offset:2048\n\t"
    "ds_write_b128 v5, v[28:31] offset:3072\n\t"
    "v_smfmac_f32_16x16x64_bf16 v[16:19], v[24:27], v[96:103], v6\n\t"
    "v_max3_u32 v60, v24, v25, v26\n\t"
    "v_max3_u32 v61, v27, v28, v29\n\t"
    "v_smfmac_f32_16x16x64_bf16 v[20:23], v[24:27], v[104:111], v6\n\t"
    "v_max3_u32 v60, v60, v30, v31\n\t"
    "v_max_u32 v60, v60, v61\n\t"
    "v_cmp_eq_u32 vcc, -1, v60\n\t"
    "s_cbranch_vccnz 1f\n\t"
    "2:\n\t"
    "s_waitcnt lgkmcnt(0)\n\t"
    "s_barrier\n\t"
    "ds_read_b128 v[32:35], v5 offset:0\n\t"
    "ds_read_b128 v[36:39], v5 offset:1024\n\t"
    "v_smfmac_f32_16x16x64_bf16 v[16:19], v[28:31], v[112:119], v6\n\t"
    "ds_read_b128 v[40:43], v5 offset:4096\n\t"
    "ds_read_b128 v[44:47], v5 offset:5120\n\t"
    "v_smfmac_f32_16x16x64_bf16 v[20:23], v[28:31], v[120:127], v6\n\t"
    "s_waitcnt lgkmcnt(3)\n\t"
    "v_smfmac_f32_16x16x64_bf16 v[16:19], v[32:35], v[64:71], v6\n\t"
    "ds_read_b128 v[48:51], v5 offset:6144\n\t"
    "v_smfmac_f32_16x16x64_bf16 v[20:23], v[32:35], v[72:79], v6\n\t"
    "ds_read_b128 v[52:55], v5 offset:7168\n\t"
    "s_waitcnt lgkmcnt(4)\n\t"
    "v_smfmac_f32_16x16x64_bf16 v[16:19], v[36:39], v[80:87], v6\n\t"
    "ds_read_b128 v[24:27], v5 offset:8192\n\t"
    "v_smfmac_f32_16x16x64_bf16 v[20:23], v[36:39], v[88:95], v6\n\t"
    "ds_read_b128 v[28:31], v5 offset:9216\n\t"
    "s_waitcnt lgkmcnt(5)\n\t"
    "v_smfmac_f32_16x16x64_bf16 v[16:19], v[40:43], v[128:135], v6\n\t"
    "ds_read_b128 v[56:59], v5 offset:10240\n\t"
    "v_smfmac_f32_16x16x64_bf16 v[20:23], v[40:43], v[136:143], v6\n\t"
    "ds_read_b128 v[60:63], v5 offset:11264\n\t"
    "ds_read_b128 v[36:39], v10 offset:35840\n\t"
    "s_waitcnt lgkmcnt(7)\n\t"
    "v_smfmac_f32_16x16x64_bf16 v[16:19], v[44:47], v[144:151], v6\n\t"
    "ds_read_b128 v[40:43], v10 offset:36864\n\t"
    "v_smfmac_f32_16x16x64_bf16 v[20:23], v[44:47], v[152:159], v6\n\t"
    "ds_read_b128 v[44:47], v10 offset:37888\n\t"
    "s_waitcnt lgkmcnt(8)\n\t"
    "v_smfmac_f32_16x16x64_bf16 v[16:19], v[48:51], v[160:167], v6\n\t"
    "v_smfmac_f32_16x16x64_bf16 v[20:23], v[48:51], v[168:175], v6\n\t"
    "ds_read_b128 v[48:51], v10 offset:38912\n\t"
    "s_waitcnt lgkmcnt(8)\n\t"
    "v_smfmac_f32_16x16x64_bf16 v[16:19], v[52:55], v[176:183], v6\n\t"
    "v_smfmac_f32_16x16x64_bf16 v[20:23], v[52:55], v[184:191], v6\n\t"
    "ds_read_b128 v[52:55], v10 offset:39936\n\t"
    "s_waitcnt lgkmcnt(8)\n\t"
    "v_smfmac_f32_16x16x64_bf16 v[16:19], v[24:27], v[192:199], v6\n\t"
    "v_smfmac_f32_16x16x64_bf16 v[20:23], v[24:27], v[200:207], v6\n\t"
    "ds_read_b128 v[24:27], v10 offset:32768\n\t"
    "s_waitcnt lgkmcnt(8)\n\t"
    "v_smfmac_f32_16x16x64_bf16 v[16:19], v[28:31], v[208:215], v6\n\t"
    "ds_read_b32 v8, v15 offset:28928\n\t"
    "ds_read_b128 v[32:35], v5 offset:25600\n\t"
    "s_setprio 3\n\t"
    "s_waitcnt lgkmcnt(9)\n\t"
    "v_smfmac_f32_16x16x64_bf16 v[16:19], v[56:59], v[224:231], v6\n\t"
    "s_waitcnt lgkmcnt(8)\n\t"
    "v_smfmac_f32_16x16x64_bf16 v[16:19], v[60:63], v[240:247], v6\n\t"
    "v_smfmac_f32_16x16x64_bf16 v[20:23], v[28:31], v[216:223], v6\n\t"
    "s_waitcnt lgkmcnt(0)\n\t"
    "21:\n\t"
    "v_cmp_ne_u32 vcc, %[sn], v8\n\t"
    "s_cbranch_vccnz 22f\n\t"
    "v_mul_f32 v32, %[lsc], v32\n\t"
    "v_mul_f32 v33, %[lsc], v33\n\t"
    "v_smfmac_f32_16x16x64_bf16 v[20:23], v[56:59], v[232:239], v6\n\t"
    "v_mul_f32 v34, %[lsc], v34\n\t"
    "v_mul_f32 v35, %[lsc], v35\n\t"
    "v_add_f32 v16, v16, v18\n\t"
    "v_add_f32 v17, v17, v19\n\t"
    "v_smfmac_f32_16x16x64_bf16 v[20:23], v[60:63], v[248:255], v6\n\t"
    "v_fma_f32 v16, v16, %[lsc], v32\n\t"
    "v_fma_f32 v17, v17, %[lsc], v33\n\t"
    "v_cvt_pk_bf16_f32 v16, v16, v17\n\t"
    "ds_write_b32 v15, v16 offset:16896\n\t"
    "s_mov_b32 %[bg], %[big]\n\t"
    "s_nop 2\n\t"
    "v_add_f32 v20, v20, v22\n\t"
    "v_add_f32 v21, v21, v23\n\t"
    "v_fma_f32 v20, v20, %[lsc], v34\n\t"
    "v_fma_f32 v21, v21, %[lsc], v35\n\t"
    "v_cvt_pk_bf16_f32 v20, v20, v21\n\t"
    "ds_write_b32 v15, v20 offset:17152\n\t"
    "ds_read_b128 v[28:31], v10 offset:33792\n\t"
    "ds_read_b128 v[32:35], v10 offset:34816\n\t"
    "s_waitcnt lgkmcnt(2)\n\t"
    "s_barrier\n\t"
    "ds_read_b64_tr_b16 v[16:17], v7 offset:16384\n\t"
    "ds_read_b64_tr_b16 v[18:19], v7 offset:16448\n\t"
    "ds_read_b64_tr_b16 v[20:21], v7 offset:16896\n\t"
    "ds_read_b64_tr_b16 v[22:23], v7 offset:16960\n\t"
    "ds_read_b64_tr_b16 v[56:57], v7 offset:17408\n\t"
    "ds_read_b64_tr_b16 v[58:59], v7 offset:17472\n\t"
    "ds_read_b64_tr_b16 v[60:61], v7 offset:17920\n\t"
    "ds_read_b64_tr_b16 v[62:63], v7 offset:17984\n\t"
    "global_load_dwordx4 v[6:9], v5, %[vs] sc1\n\t"
    "global_load_dwordx4 v[10:13], v5, %[vs] offset:1024 sc1\n\t"
    "s_waitcnt lgkmcnt(6)\n\t"
    "v_mfma_f32_16x16x32_bf16 v[24:27], v[24:27], v[16:19], 0\n\t"
    "v_mfma_f32_16x16x32_bf16 v[28:31], v[28:31], v[16:19], 0\n\t"
    "s_waitcnt lgkmcnt(4)\n\t"
    "v_mfma_f32_16x16x32_bf16 v[24:27], v[32:35], v[20:23], v[24:27]\n\t"
    "v_mfma_f32_16x16x32_bf16 v[28:31], v[36:39], v[20:23], v[28:31]\n\t"
    "v_xor_b32 v39, 32, v14\n\t"
    "ds_read_b64_tr_b16 v[32:33], v14 offset:43008\n\t"
    "ds_read_b64_tr_b16 v[34:35], v14 offset:44032\n\t"
    "ds_read_b64_tr_b16 v[36:37], v39 offset:43008\n\t"
    "ds_read_b64_tr_b16 v[38:39], v39 offset:44032\n\t"
    "s_waitcnt lgkmcnt(6)\n\t"
    "v_mfma_f32_16x16x32_bf16 v[24:27], v[40:43], v[56:59], v[24:27]\n\t"
    "v_mfma_f32_16x16x32_bf16 v[28:31], v[44:47], v[56:59], v[28:31]\n\t"
    "s_waitcnt lgkmcnt(4)\n\t"
    "v_mfma_f32_16x16x32_bf16 v[24:27], v[48:51], v[60:63], v[24:27]\n\t"
    "v_mfma_f32_16x16x32_bf16 v[28:31], v[52:55], v[60:63], v[28:31]\n\t"
    "v_mov_b32 v52, %[one]\n\t"
    "v_mov_b32 v53, %[one]\n\t"
    "v_mov_b32 v54, %[one]\n\t"
    "v_mov_b32 v55, %[one]\n\t"
    "v_mov_b32 v45, 0\n\t"
    "v_mov_b32 v47, 0\n\t"
    "v_mov_b32 v48, %[one]\n\t"
    "v_max3_f32 v14, |v24|, |v25|, |v26|\n\t"
    "v_max3_f32 v40, |v27|, |v28|, |v29|\n\t"
    "v_max3_f32 v14, v14, |v30|, |v31|\n\t"
    "v_max_f32 v14, v14, v40\n\t"
    "v_cmp_nge_f32 vcc, %[big], v14\n\t"
    "v_mov_b32 v14, 0\n\t"
    "s_cbranch_vccnz 12f\n\t"
    "13:\n\t"
    "v_exp_f32 v24, v24\n\t"
    "v_exp_f32 v25, v25\n\t"
    "v_exp_f32 v26, v26\n\t"
    "v_exp_f32 v27, v27\n\t"
    "v_exp_f32 v28, v28\n\t"
    "v_exp_f32 v29, v29\n\t"
    "v_exp_f32 v30, v30\n\t"
    "v_exp_f32 v31, v31\n\t"
    "v_cvt_pk_bf16_f32 v40, v24, v25\n\t"
    "v_cvt_pk_bf16_f32 v41, v26, v27\n\t"
    "v_cvt_pk_bf16_f32 v42, v28, v29\n\t"
    "v_cvt_pk_bf16_f32 v43, v30, v31\n\t"
    "v_mov_b32 v49, 0\n\t"
    "v_mov_b32 v51, 0\n\t"
    "s_waitcnt lgkmcnt(0)\n\t"
    "v_mfma_f32_16x16x32_bf16 v[52:55], v[52:55], v[40:43], 0\n\t"
    "v_mfma_f32_16x16x32_bf16 v[24:27], v[32:35], v[40:43], 0\n\t"
    "v_mfma_f32_16x16x32_bf16 v[28:31], v[36:39], v[40:43], 0\n\t"
    "s_waitcnt vmcnt(1)\n\t"
    "5:\n\t"
    "v_mfma_f32_16x16x32_bf16 v[32:35], v[6:9], v[16:19], 0\n\t"
    "v_mfma_f32_16x16x32_bf16 v[36:39], v[6:9], v[20:23], 0\n\t"
    "s_waitcnt vmcnt(0)\n\t"
    "v_mfma_f32_16x16x32_bf16 v[32:35], v[10:13], v[56:59], v[32:35]\n\t"
    "v_mfma_f32_16x16x32_bf16 v[36:39], v[10:13], v[60:63], v[36:39]\n\t"
    "ds_read_b32 v44, v15 offset:18944\n\t"
    "ds_read_b32 v46, v15 offset:19200\n\t"
    "v_max3_u32 v40, v6, v7, v8\n\t"
    "v_max3_u32 v41, v9, v10, v11\n\t"
    "v_max3_u32 v40, v40, v12, v13\n\t"
    "v_max_u32 v40, v40, v41\n\t"
    "v_cmp_eq_u32 vcc, -1, v40\n\t"
    "s_cbranch_vccnz 4f\n\t"
    "6:\n\t"
    "s_nop 0\n\t"
    "v_add_f32 v32, v32, v38\n\t"
    "v_add_f32 v33, v33, v39\n\t"
    "v_exp_f32 v34, v32\n\t"
    "v_max_f32 v40, v32, v33\n\t"
    "v_exp_f32 v35, v33\n\t"
    "v_cmp_nge_f32 vcc, %[bg], v40\n\t"
    "s_cbranch_vccnz 10f\n\t"
    "11:\n\t"
    "v_cvt_pk_bf16_f32 v50, v34, v35\n\t"
    "s_nop 1\n\t"
    "v_mfma_f32_16x16x16_bf16 v[52:55], v[48:49], v[50:51], v[52:55]\n\t"
    "17:\n\t"
    "s_waitcnt vmcnt(0) lgkmcnt(0)\n\t"
    "v_mfma_f32_16x16x16_bf16 v[32:35], v[44:45], v[50:51], v[24:27]\n\t"
    "v_mfma_f32_16x16x16_bf16 v[36:39], v[46:47], v[50:51], v[28:31]\n\t"
    "v_max_u32 v40, v44, v46\n\t"
    "v_cmp_eq_u32 vcc, -1, v40\n\t"
    "s_nop 2\n\t"
    "v_rcp_f32 v42, v52\n\t"
    "s_cbranch_vccnz 18f\n\t"
    "19:\n\t"
    "v_pk_mul_f32 v[32:33], v[32:33], v[42:43] op_sel_hi:[1,0]\n\t"
    "v_pk_mul_f32 v[34:35], v[34:35], v[42:43] op_sel_hi:[1,0]\n\t"
    "v_pk_mul_f32 v[36:37], v[36:37], v[42:43] op_sel_hi:[1,0]\n\t"
    "v_pk_mul_f32 v[38:39], v[38:39], v[42:43] op_sel_hi:[1,0]\n\t"
    "v_cvt_pk_bf16_f32 v24, v32, v33\n\t"
    "v_cvt_pk_bf16_f32 v25, v34, v35\n\t"
    "v_cvt_pk_bf16_f32 v26, v36, v37\n\t"
    "v_cvt_pk_bf16_f32 v27, v38, v39\n\t"
    "v_min_u32 v24, -2, v24\n\t"
    "v_min_u32 v25, -2, v25\n\t"
    "v_min_u32 v26, -2, v26\n\t"
    "v_min_u32 v27, -2, v27\n\t"
    "global_store_dwordx4 v4, v[24:27], %[od] sc1\n\t"
    "ds_write_b128 v5, v[10:13] offset:21504\n\t"
    "s_setprio 1\n\t"
    "s_waitcnt lgkmcnt(0)\n\t"
    "s_barrier\n\t"
    "s_waitcnt lgkmcnt(0)\n\t"
    "s_branch 9f\n\t"
    "1:\n\t"
    "s_add_u32 %[sp], %[sp], 1\n\t"
    "s_cmp_ge_u32 %[sp], %[cap]\n\t"
    "s_cbranch_scc1 7f\n\t"
    "global_load_dwordx4 v[24:27], v5, %[ag] sc1\n\t"
    "global_load_dwordx4 v[28:31], v5, %[ag] offset:1024 sc1\n\t"
    "s_waitcnt lgkmcnt(0)\n\t"
    "s_memtime %[tc]\n\t"
    "s_nop 7\n\t"
    "s_branch 3b\n\t"
    "7:\n\t"
    "s_mov_b32 %[cap], 0\n\t"
    "s_branch 2b\n\t"
    "4:\n\t"
    "s_add_u32 %[sp], %[sp], 0x10000\n\t"
    "s_lshr_b32 %[st], %[sp], 16\n\t"
    "s_cmp_ge_u32 %[st], %[cap]\n\t"
    "s_cbranch_scc1 8f\n\t"
    "global_load_dwordx4 v[6:9], v5, %[vs] sc1\n\t"
    "global_load_dwordx4 v[10:13], v5, %[vs] offset:1024 sc1\n\t"
    "s_waitcnt vmcnt(0) lgkmcnt(0)\n\t"
    "s_branch 5b\n\t"
    "8:\n\t"
    "s_mov_b32 %[cap], 0\n\t"
    "s_branch 6b\n\t"
    "18:\n\t"
    "s_add_u32 %[sp], %[sp], 0x10000\n\t"
    "s_lshr_b32 %[st], %[sp], 16\n\t"
    "s_cmp_ge_u32 %[st], %[cap]\n\t"
    "s_cbranch_scc1 20f\n\t"
    "ds_read_b32 v44, v15 offset:18944\n\t"
    "ds_read_b32 v46, v15 offset:19200\n\t"
    "s_waitcnt vmcnt(0) lgkmcnt(0)\n\t"
    "s_branch 17b\n\t"
    "20:\n\t"
    "s_mov_b32 %[cap], 0\n\t"
    "s_branch 19b\n\t"
    "22:\n\t"
    "ds_read_b32 v8, v15 offset:28928\n\t"
    "ds_read_b128 v[32:35], v5 offset:25600\n\t"
    "s_waitcnt lgkmcnt(0)\n\t"
    "s_branch 21b\n\t"
    "10:\n\t"
    "v_max_f32 v40, v40, v14\n\t"
    "v_mov_b32 v42, v40\n\t"
    "s_nop 1\n\t"
    "v_permlane16_swap_b32 v40, v42\n\t"
    "v_max_f32 v40, v40, v42\n\t"
    "v_mov_b32 v42, v40\n\t"
    "s_nop 1\n\t"
    "v_permlane32_swap_b32 v40, v42\n\t"
    "v_max_f32 v40, v40, v42\n\t"
    "v_sub_f32 v34, v32, v40\n\t"
    "v_sub_f32 v35, v33, v40\n\t"
    "v_sub_f32 v40, v14, v40\n\t"
    "v_exp_f32 v40, v40\n\t"
    "v_exp_f32 v34, v34\n\t"
    "v_exp_f32 v35, v35\n\t"
    "v_mul_f32 v24, v24, v40\n\t"
    "v_mul_f32 v25, v25, v40\n\t"
    "v_mul_f32 v26, v26, v40\n\t"
    "v_mul_f32 v27, v27, v40\n\t"
    "v_mul_f32 v28, v28, v40\n\t"
    "v_mul_f32 v29, v29, v40\n\t"
    "v_mul_f32 v30, v30, v40\n\t"
    "v_mul_f32 v31, v31, v40\n\t"
    "v_mul_f32 v52, v52, v40\n\t"
    "v_mul_f32 v53, v53, v40\n\t"
    "v_mul_f32 v54, v54, v40\n\t"
    "v_mul_f32 v55, v55, v40\n\t"
    "s_branch 11b\n\t"
    "12:\n\t"
    "v_max3_f32 v14, v24, v25, v26\n\t"
    "v_max3_f32 v40, v27, v28, v29\n\t"
    "v_max3_f32 v14, v14, v30, v31\n\t"
    "v_max3_f32 v14, v14, v40, %[neg]\n\t"
    "v_mov_b32 v40, v14\n\t"
    "s_nop 1\n\t"
    "v_permlane16_swap_b32 v14, v40\n\t"
    "v_max_f32 v14, v14, v40\n\t"
    "v_mov_b32 v40, v14\n\t"
    "s_nop 1\n\t"
    "v_permlane32_swap_b32 v14, v40\n\t"
    "v_max_f32 v14, v14, v40\n\t"
    "v_sub_f32 v24, v24, v14\n\t"
    "v_sub_f32 v25, v25, v14\n\t"
    "v_sub_f32 v26, v26, v14\n\t"
    "v_sub_f32 v27, v27, v14\n\t"
    "v_sub_f32 v28, v28, v14\n\t"
    "v_sub_f32 v29, v29, v14\n\t"
    "v_sub_f32 v30, v30, v14\n\t"
    "v_sub_f32 v31, v31, v14\n\t"
    "s_mov_b32 %[bg], 0x7fc00000\n\t"
    "s_branch 13b\n\t"
    "9:"
    : [tc] "=&s"(tc), [sp] "=&s"(sp), [cap] "+s"(cap), [st] "=&s"(st), [bg] "=&s"(bg)
    : [ag] "s"(ag), [c44] "s"(0x4444u), [lsc] "s"(LSC), [sn] "s"(sn), [pb] "s"(pb), [vs] "s"(vs), [od] "s"(od), [neg] "s"(-1.0e38f), [one] "s"(0x3F803F80u), [big] "s"(24.0f), "{v[64:95]}"(W.w0), "{v[96:127]}"(W.w1), "{v[128:159]}"(W.w2), "{v[160:191]}"(W.w3), "{v[192:223]}"(W.w4), "{v[224:255]}"(W.w5)
    : "v4", "v5", "v6", "v7", "v8", "v9", "v10", "v11", "v12", "v13", "v14", "v15", "v16", "v17", "v18", "v19", "v20", "v21", "v22", "v23", "v24", "v25", "v26", "v27", "v28", "v29", "v30", "v31", "v32", "v33", "v34", "v35", "v36", "v37", "v38", "v39", "v40", "v41", "v42", "v43", "v44", "v45", "v46", "v47", "v48", "v49", "v50", "v51", "v52", "v53", "v54", "v55", "v56", "v57", "v58", "v59", "v60", "v61", "v62", "v63", "vcc", "scc", "m0", "memory");
}
template <> __device__ __forceinline__ void qk_step<2>(const QKW& W, const char* ag, const char* vs, const char* v2, const char* db, const char* od, unsigned pb, unsigned sn, unsigned& cap, unsigned& sp,
    unsigned long long& tc, unsigned long long* ts, unsigned& d0, unsigned& d1) {
  unsigned st, bg;
  asm volatile("v_mbcnt_lo_u32_b32 v60, -1, 0\n\t"
    "v_mbcnt_hi_u32_b32 v60, -1, v60\n\t"
    "v_lshlrev_b32 v5, 4, v60\n\t"
    "s_mov_b32 %[sp], 0\n\t"
    "s_setprio 2\n\t"
    "global_load_dwordx4 v[24:27], v5, %[ag] sc1\n\t"
    "global_load_dwordx4 v[28:31], v5, %[ag] offset:1024 sc1\n\t"
    "s_memtime %[tc]\n\t"
    "v_lshrrev_b32 v32, 4, v60\n\t"
    "v_and_b32 v33, 15, v60\n\t"
    "v_lshrrev_b32 v34, 2, v33\n\t"
    "v_and_b32 v35, 3, v60\n\t"
    "v_and_b32 v61, 1, v60\n\t"
    "v_lshlrev_b32 v61, 3, v61\n\t"
    "v_lshl_or_b32 v7, v34, 4, v61\n\t"
    "v_lshl_or_b32 v7, v32, 7, v7\n\t"
    "v_and_b32 v10, 2, v34\n\t"
    "v_xor_b32 v10, v10, v32\n\t"
    "v_lshl_or_b32 v10, v10, 4, %[pb]\n\t"
    "v_lshl_or_b32 v10, v33, 6, v10\n\t"
    "v_and_b32 v14, 32, v60\n\t"
    "v_lshl_or_b32 v14, v35, 3, v14\n\t"
    "v_lshrrev_b32 v34, 2, v60\n\t"
    "v_lshl_or_b32 v14, v34, 6, v14\n\t"
    "v_or_b32 v14, %[pb], v14\n\t"
    "v_and_b32 v61, 3, v60\n\t"
    "v_lshlrev_b32 v61, 6, v61\n\t"
    "v_lshl_or_b32 v4, v32, 4, v61\n\t"
    "v_and_b32 v61, 4, v60\n\t"
    "v_lshl_or_b32 v4, v61, 10, v4\n\t"
    "v_and_b32 v61, 8, v60\n\t"
    "v_lshl_or_b32 v4, v61, 14, v4\n\t"
    "v_or_b32 v4, 512, v4\n\t"
    "v_and_b32 v61, 2, v60\n\t"
    "v_mul_u32_u24 v61, 0x5555, v61\n\t"
    "v_or_b32 v6, %[c44], v61\n\t"
    "v_lshlrev_b32 v61, 2, v32\n\t"
    "v_lshl_or_b32 v15, v33, 4, v61\n\t"
    "3:\n\t"
    "v_mov_b32 v16, 0\n\t"
    "v_mov_b32 v17, 0\n\t"
    "v_mov_b32 v18, 0\n\t"
    "v_mov_b32 v19, 0\n\t"
    "v_mov_b32 v20, 0\n\t"
    "v_mov_b32 v21, 0\n\t"
    "v_mov_b32 v22, 0\n\t"
    "v_mov_b32 v23, 0\n\t"
    "s_waitcnt vmcnt(0) lgkmcnt(0)\n\t"
    "33:\n\t"
    "ds_write_b128 v5, v[24:27] offset:4096\n\t"
    "ds_write_b128 v5, v[28:31] offset:5120\n\t"
    "v_smfmac_f32_16x16x64_bf16 v[16:19], v[24:27], v[128:135], v6\n\t"
    "v_max3_u32 v60, v24, v25, v26\n\t"
    "v_max3_u32 v61, v27, v28, v29\n\t"
    "v_smfmac_f32_16x16x64_bf16 v[20:23], v[24:27], v[136:143], v6\n\t"
    "v_max3_u32 v60, v60, v30, v31\n\t"
    "v_max_u32 v60, v60, v61\n\t"
    "v_cmp_eq_u32 vcc, -1, v60\n\t"
    "s_cbranch_vccnz 1f\n\t"
    "2:\n\t"
    "s_waitcnt lgkmcnt(0)\n\t"
    "s_barrier\n\t"
    "ds_read_b128 v[32:35], v5 offset:0\n\t"
    "ds_read_b128 v[36:39], v5 offset:1024\n\t"
    "v_smfmac_f32_16x16x64_bf16 v[16:19], v[28:31], v[144:151], v6\n\t"
    "ds_read_b128 v[40:43], v5 offset:2048\n\t"
    "ds_read_b128 v[44:47], v5 offset:3072\n\t"
    "v_smfmac_f32_16x16x64_bf16 v[20:23], v[28:31], v[152:159], v6\n\t"
    "s_waitcnt lgkmcnt(3)\n\t"
    "v_smfmac_f32_16x16x64_bf16 v[16:19], v[32:35], v[64:71], v6\n\t"
    "ds_read_b128 v[48:51], v5 offset:6144\n\t"
    "v_smfmac_f32_16x16x64_bf16 v[20:23], v[32:35], v[72:79], v6\n\t"
    "ds_read_b128 v[52:55], v5 offset:7168\n\t"
    "s_waitcnt lgkmcnt(4)\n\t"
    "v_smfmac_f32_16x16x64_bf16 v[16:19], v[36:39], v[80:87], v6\n\t"
    "ds_read_b128 v[24:27], v5 offset:8192\n\t"
    "v_smfmac_f32_16x16x64_bf16 v[20:23], v[36:39], v[88:95], v6\n\t"
    "ds_read_b128 v[28:31], v5 offset:9216\n\t"
    "s_waitcnt lgkmcnt(5)\n\t"
    "v_smfmac_f32_16x16x64_bf16 v[16:19], v[40:43], v[96:103], v6\n\t"
    "ds_read_b128 v[56:59], v5 offset:10240\n\t"
    "v_smfmac_f32_16x16x64_bf16 v[20:23], v[40:43], v[104:111], v6\n\t"
    "ds_read_b128 v[60:63], v5 offset:11264\n\t"
    "ds_read_b128 v[36:39], v10 offset:35840\n\t"
    "s_waitcnt lgkmcnt(7)\n\t"
    "v_smfmac_f32_16x16x64_bf16 v[16:19], v[44:47], v[112:119], v6\n\t"
    "ds_read_b128 v[40:43], v10 offset:36864\n\t"
    "v_smfmac_f32_16x16x64_bf16 v[20:23], v[44:47], v[120:127], v6\n\t"
    "ds_read_b128 v[44:47], v10 offset:37888\n\t"
    "s_waitcnt lgkmcnt(8)\n\t"
    "v_smfmac_f32_16x16x64_bf16 v[16:19], v[48:51], v[160:167], v6\n\t"
    "v_smfmac_f32_16x16x64_bf16 v[20:23], v[48:51], v[168:175], v6\n\t"
    "ds_read_b128 v[48:51], v10 offset:38912\n\t"
    "s_waitcnt lgkmcnt(8)\n\t"
    "v_smfmac_f32_16x16x64_bf16 v[16:19], v[52:55], v[176:183], v6\n\t"
    "v_smfmac_f32_16x16x64_bf16 v[20:23], v[52:55], v[184:191], v6\n\t"
    "ds_read_b128 v[52:55], v10 offset:39936\n\t"
    "s_waitcnt lgkmcnt(8)\n\t"
    "v_smfmac_f32_16x16x64_bf16 v[16:19], v[24:27], v[192:199], v6\n\t"
    "v_smfmac_f32_16x16x64_bf16 v[20:23], v[24:27], v[200:207], v6\n\t"
    "ds_read_b128 v[24:27], v10 offset:32768\n\t"
    "s_waitcnt lgkmcnt(8)\n\t"
    "v_smfmac_f32_16x16x64_bf16 v[16:19], v[28:31], v[208:215], v6\n\t"
    "ds_read_b32 v8, v15 offset:29184\n\t"
    "ds_read_b128 v[32:35], v5 offset:26624\n\t"
    "s_setprio 3\n\t"
    "s_waitcnt lgkmcnt(9)\n\t"
    "v_smfmac_f32_16x16x64_bf16 v[16:19], v[56:59], v[224:231], v6\n\t"
    "s_waitcnt lgkmcnt(8)\n\t"
    "v_smfmac_f32_16x16x64_bf16 v[16:19], v[60:63], v[240:247], v6\n\t"
    "v_smfmac_f32_16x16x64_bf16 v[20:23], v[28:31], v[216:223], v6\n\t"
    "s_waitcnt lgkmcnt(0)\n\t"
    "21:\n\t"
    "v_cmp_ne_u32 vcc, %[sn], v8\n\t"
    "s_cbranch_vccnz 22f\n\t"
    "v_mul_f32 v32, %[lsc], v32\n\t"
    "v_mul_f32 v33, %[lsc], v33\n\t"
    "v_smfmac_f32_16x16x64_bf16 v[20:23], v[56:59], v[232:239], v6\n\t"
    "v_mul_f32 v34, %[lsc], v34\n\t"
    "v_mul_f32 v35, %[lsc], v35\n\t"
    "v_add_f32 v16, v16, v18\n\t"
    "v_add_f32 v17, v17, v19\n\t"
    "v_smfmac_f32_16x16x64_bf16 v[20:23], v[60:63], v[248:255], v6\n\t"
    "v_fma_f32 v16, v16, %[lsc], v32\n\t"
    "v_fma_f32 v17, v17, %[lsc], v33\n\t"
    "v_cvt_pk_bf16_f32 v16, v16, v17\n\t"
    "ds_write_b32 v15, v16 offset:17408\n\t"
    "s_mov_b32 %[bg], %[big]\n\t"
    "s_nop 2\n\t"
    "v_add_f32 v20, v20, v22\n\t"
    "v_add_f32 v21, v21, v23\n\t"
    "v_fma_f32 v20, v20, %[lsc], v34\n\t"
    "v_fma_f32 v21, v21, %[lsc], v35\n\t"
    "v_cvt_pk_bf16_f32 v20, v20, v21\n\t"
    "ds_write_b32 v15, v20 offset:17664\n\t"
    "ds_read_b128 v[28:31], v10 offset:33792\n\t"
    "ds_read_b128 v[32:35], v10 offset:34816\n\t"
    "s_waitcnt lgkmcnt(2)\n\t"
    "s_barrier\n\t"
    "ds_read_b64_tr_b16 v[16:17], v7 offset:16384\n\t"
    "ds_read_b64_tr_b16 v[18:19], v7 offset:16448\n\t"
    "ds_read_b64_tr_b16 v[20:21], v7 offset:16896\n\t"
    "ds_read_b64_tr_b16 v[22:23], v7 offset:16960\n\t"
    "ds_read_b64_tr_b16 v[56:57], v7 offset:17408\n\t"
    "ds_read_b64_tr_b16 v[58:59], v7 offset:17472\n\t"
    "ds_read_b64_tr_b16 v[60:61], v7 offset:17920\n\t"
    "ds_read_b64_tr_b16 v[62:63], v7 offset:17984\n\t"
    "global_load_dwordx4 v[6:9], v5, %[vs] sc1\n\t"
    "global_load_dwordx4 v[10:13], v5, %[vs] offset:1024 sc1\n\t"
    "s_waitcnt lgkmcnt(6)\n\t"
    "v_mfma_f32_16x16x32_bf16 v[24:27], v[24:27], v[16:19], 0\n\t"
    "v_mfma_f32_16x16x32_bf16 v[28:31], v[28:31], v[16:19], 0\n\t"
    "s_waitcnt lgkmcnt(4)\n\t"
    "v_mfma_f32_16x16x32_bf16 v[24:27], v[32:35], v[20:23], v[24:27]\n\t"
    "v_mfma_f32_16x16x32_bf16 v[28:31], v[36:39], v[20:23], v[28:31]\n\t"
    "v_xor_b32 v39, 32, v14\n\t"
    "ds_read_b64_tr_b16 v[32:33], v14 offset:45056\n\t"
    "ds_read_b64_tr_b16 v[34:35], v14 offset:46080\n\t"
    "ds_read_b64_tr_b16 v[36:37], v39 offset:45056\n\t"
    "ds_read_b64_tr_b16 v[38:39], v39 offset:46080\n\t"
    "s_waitcnt lgkmcnt(6)\n\t"
    "v_mfma_f32_16x16x32_bf16 v[24:27], v[40:43], v[56:59], v[24:27]\n\t"
    "v_mfma_f32_16x16x32_bf16 v[28:31], v[44:47], v[56:59], v[28:31]\n\t"
    "s_waitcnt lgkmcnt(4)\n\t"
    "v_mfma_f32_16x16x32_bf16 v[24:27], v[48:51], v[60:63], v[24:27]\n\t"
    "v_mfma_f32_16x16x32_bf16 v[28:31], v[52:55], v[60:63], v[28:31]\n\t"
    "v_mov_b32 v52, %[one]\n\t"
    "v_mov_b32 v53, %[one]\n\t"
    "v_mov_b32 v54, %[one]\n\t"
    "v_mov_b32 v55, %[one]\n\t"
    "v_mov_b32 v45, 0\n\t"
    "v_mov_b32 v47, 0\n\t"
    "v_mov_b32 v48, %[one]\n\t"
    "v_max3_f32 v14, |v24|, |v25|, |v26|\n\t"
    "v_max3_f32 v40, |v27|, |v28|, |v29|\n\t"
    "v_max3_f32 v14, v14, |v30|, |v31|\n\t"
    "v_max_f32 v14, v14, v40\n\t"
    "v_cmp_nge_f32 vcc, %[big], v14\n\t"
    "v_mov_b32 v14, 0\n\t"
    "s_cbranch_vccnz 12f\n\t"
    "13:\n\t"
    "v_exp_f32 v24, v24\n\t"
    "v_exp_f32 v25, v25\n\t"
    "v_exp_f32 v26, v26\n\t"
    "v_exp_f32 v27, v27\n\t"
    "v_exp_f32 v28, v28\n\t"
    "v_exp_f32 v29, v29\n\t"
    "v_exp_f32 v30, v30\n\t"
    "v_exp_f32 v31, v31\n\t"
    "v_lshrrev_b32 v46, 2, v5\n\t"
    "global_load_dword v44, v46, %[v2] sc1\n\t"
    "global_load_dword v46, v46, %[v2] offset:256 sc1\n\t"
    "v_cvt_pk_bf16_f32 v40, v24, v25\n\t"
    "v_cvt_pk_bf16_f32 v41, v26, v27\n\t"
    "v_cvt_pk_bf16_f32 v42, v28, v29\n\t"
    "v_cvt_pk_bf16_f32 v43, v30, v31\n\t"
    "v_mov_b32 v49, 0\n\t"
    "v_mov_b32 v51, 0\n\t"
    "s_waitcnt lgkmcnt(0)\n\t"
    "v_mfma_f32_16x16x32_bf16 v[52:55], v[52:55], v[40:43], 0\n\t"
    "v_mfma_f32_16x16x32_bf16 v[24:27], v[32:35], v[40:43], 0\n\t"
    "v_mfma_f32_16x16x32_bf16 v[28:31], v[36:39], v[40:43], 0\n\t"
    "s_waitcnt vmcnt(3)\n\t"
    "5:\n\t"
    "v_mfma_f32_16x16x32_bf16 v[32:35], v[6:9], v[16:19], 0\n\t"
    "v_mfma_f32_16x16x32_bf16 v[36:39], v[6:9], v[20:23], 0\n\t"
    "s_waitcnt vmcnt(2)\n\t"
    "v_mfma_f32_16x16x32_bf16 v[32:35], v[10:13], v[56:59], v[32:35]\n\t"
    "v_mfma_f32_16x16x32_bf16 v[36:39], v[10:13], v[60:63], v[36:39]\n\t"
    "v_max3_u32 v40, v6, v7, v8\n\t"
    "v_max3_u32 v41, v9, v10, v11\n\t"
    "v_max3_u32 v40, v40, v12, v13\n\t"
    "v_max_u32 v40, v40, v41\n\t"
    "v_cmp_eq_u32 vcc, -1, v40\n\t"
    "s_cbranch_vccnz 4f\n\t"
    "6:\n\t"
    "s_nop 0\n\t"
    "s_nop 0\n\t"
    "v_add_f32 v32, v32, v38\n\t"
    "v_add_f32 v33, v33, v39\n\t"
    "v_exp_f32 v34, v32\n\t"
    "v_max_f32 v40, v32, v33\n\t"
    "v_exp_f32 v35, v33\n\t"
    "v_cmp_nge_f32 vcc, %[bg], v40\n\t"
    "s_cbranch_vccnz 10f\n\t"
    "11:\n\t"
    "v_cvt_pk_bf16_f32 v50, v34, v35\n\t"
    "s_nop 1\n\t"
    "v_mfma_f32_16x16x16_bf16 v[52:55], v[48:49], v[50:51], v[52:55]\n\t"
    "17:\n\t"
    "s_waitcnt vmcnt(0) lgkmcnt(0)\n\t"
    "v_mfma_f32_16x16x16_bf16 v[32:35], v[44:45], v[50:51], v[24:27]\n\t"
    "v_mfma_f32_16x16x16_bf16 v[36:39], v[46:47], v[50:51], v[28:31]\n\t"
    "v_max_u32 v40, v44, v46\n\t"
    "v_cmp_eq_u32 vcc, -1, v40\n\t"
    "s_memrealtime s[36:37]\n\t"
    "s_nop 1\n\t"
    "v_rcp_f32 v42, v52\n\t"
    "s_cbranch_vccnz 18f\n\t"
    "19:\n\t"
    "v_pk_mul_f32 v[32:33], v[32:33], v[42:43] op_sel_hi:[1,0]\n\t"
    "v_pk_mul_f32 v[34:35], v[34:35], v[42:43] op_sel_hi:[1,0]\n\t"
    "v_pk_mul_f32 v[36:37], v[36:37], v[42:43] op_sel_hi:[1,0]\n\t"
    "v_pk_mul_f32 v[38:39], v[38:39], v[42:43] op_sel_hi:[1,0]\n\t"
    "v_cvt_pk_bf16_f32 v24, v32, v33\n\t"
    "v_cvt_pk_bf16_f32 v25, v34, v35\n\t"
    "v_cvt_pk_bf16_f32 v26, v36, v37\n\t"
    "v_cvt_pk_bf16_f32 v27, v38, v39\n\t"
    "v_min_u32 v24, -2, v24\n\t"
    "v_min_u32 v25, -2, v25\n\t"
    "v_min_u32 v26, -2, v26\n\t"
    "v_min_u32 v27, -2, v27\n\t"
    "global_store_dwordx4 v4, v[24:27], %[od] sc1\n\t"
    "v_lshlrev_b32 v40, 10, v5\n\t"
    "s_waitcnt lgkmcnt(0)\n\t"
    "s_and_b32 s36, s36, 0xffffff\n\t"
    "s_or_b32 s36, s36, %[sn]\n\t"
    "v_mov_b32 v41, s36\n\t"
    "s_mov_b64 exec, 7\n\t"
    "global_store_dword v40, v41, %[db] offset:0 sc1\n\t"
    "s_mov_b64 exec, -1\n\t"
    "ds_write_b32 v15, v44 offset:19456\n\t"
    "ds_write_b32 v15, v46 offset:19712\n\t"
    "s_setprio 1\n\t"
    "s_waitcnt lgkmcnt(0)\n\t"
    "s_barrier\n\t"
    "s_waitcnt lgkmcnt(0)\n\t"
    "s_branch 9f\n\t"
    "1:\n\t"
    "s_add_u32 %[sp], %[sp], 1\n\t"
    "s_cmp_ge_u32 %[sp], %[cap]\n\t"
    "s_cbranch_scc1 7f\n\t"
    "global_load_dwordx4 v[24:27], v5, %[ag] sc1\n\t"
    "global_load_dwordx4 v[28:31], v5, %[ag] offset:1024 sc1\n\t"
    "s_waitcnt lgkmcnt(0)\n\t"
    "s_memtime %[tc]\n\t"
    "s_nop 7\n\t"
    "s_branch 3b\n\t"
    "7:\n\t"
    "s_mov_b32 %[cap], 0\n\t"
    "s_branch 2b\n\t"
    "4:\n\t"
    "s_add_u32 %[sp], %[sp], 0x10000\n\t"
    "s_lshr_b32 %[st], %[sp], 16\n\t"
    "s_cmp_ge_u32 %[st], %[cap]\n\t"
    "s_cbranch_scc1 8f\n\t"
    "global_load_dwordx4 v[6:9], v5, %[vs] sc1\n\t"
    "global_load_dwordx4 v[10:13], v5, %[vs] offset:1024 sc1\n\t"
    "s_waitcnt vmcnt(0) lgkmcnt(0)\n\t"
    "s_branch 5b\n\t"
    "8:\n\t"
    "s_mov_b32 %[cap], 0\n\t"
    "s_branch 6b\n\t"
    "18:\n\t"
    "s_add_u32 %[sp], %[sp], 0x10000\n\t"
    "s_lshr_b32 %[st], %[sp], 16\n\t"
    "s_cmp_ge_u32 %[st], %[cap]\n\t"
    "s_cbranch_scc1 20f\n\t"
    "v_lshrrev_b32 v46, 2, v5\n\t"
    "global_load_dword v44, v46, %[v2] sc1\n\t"
    "global_load_dword v46, v46, %[v2] offset:256 sc1\n\t"
    "s_waitcnt vmcnt(0) lgkmcnt(0)\n\t"
    "s_branch 17b\n\t"
    "20:\n\t"
    "s_mov_b32 %[cap], 0\n\t"
    "s_branch 19b\n\t"
    "22:\n\t"
    "ds_read_b32 v8, v15 offset:29184\n\t"
    "ds_read_b128 v[32:35], v5 offset:26624\n\t"
    "s_waitcnt lgkmcnt(0)\n\t"
    "s_branch 21b\n\t"
    "10:\n\t"
    "v_max_f32 v40, v40, v14\n\t"
    "v_mov_b32 v42, v40\n\t"
    "s_nop 1\n\t"
    "v_permlane16_swap_b32 v40, v42\n\t"
    "v_max_f32 v40, v40, v42\n\t"
    "v_mov_b32 v42, v40\n\t"
    "s_nop 1\n\t"
    "v_permlane32_swap_b32 v40, v42\n\t"
    "v_max_f32 v40, v40, v42\n\t"
    "v_sub_f32 v34, v32, v40\n\t"
    "v_sub_f32 v35, v33, v40\n\t"
    "v_sub_f32 v40, v14, v40\n\t"
    "v_exp_f32 v40, v40\n\t"
    "v_exp_f32 v34, v34\n\t"
    "v_exp_f32 v35, v35\n\t"
    "v_mul_f32 v24, v24, v40\n\t"
    "v_mul_f32 v25, v25, v40\n\t"
    "v_mul_f32 v26, v26, v40\n\t"
    "v_mul_f32 v27, v27, v40\n\t"
    "v_mul_f32 v28, v28, v40\n\t"
    "v_mul_f32 v29, v29, v40\n\t"
    "v_mul_f32 v30, v30, v40\n\t"
    "v_mul_f32 v31, v31, v40\n\t"
    "v_mul_f32 v52, v52, v40\n\t"
    "v_mul_f32 v53, v53, v40\n\t"
    "v_mul_f32 v54, v54, v40\n\t"
    "v_mul_f32 v55, v55, v40\n\t"
    "s_branch 11b\n\t"
    "12:\n\t"
    "v_max3_f32 v14, v24, v25, v26\n\t"
    "v_max3_f32 v40, v27, v28, v29\n\t"
    "v_max3_f32 v14, v14, v30, v31\n\t"
    "v_max3_f32 v14, v14, v40, %[neg]\n\t"
    "v_mov_b32 v40, v14\n\t"
    "s_nop 1\n\t"
    "v_permlane16_swap_b32 v14, v40\n\t"
    "v_max_f32 v14, v14, v40\n\t"
    "v_mov_b32 v40, v14\n\t"
    "s_nop 1\n\t"
    "v_permlane32_swap_b32 v14, v40\n\t"
    "v_max_f32 v14, v14, v40\n\t"
    "v_sub_f32 v24, v24, v14\n\t"
    "v_sub_f32 v25, v25, v14\n\t"
    "v_sub_f32 v26, v26, v14\n\t"
    "v_sub_f32 v27, v27, v14\n\t"
    "v_sub_f32 v28, v28, v14\n\t"
    "v_sub_f32 v29, v29, v14\n\t"
    "v_sub_f32 v30, v30, v14\n\t"
    "v_sub_f32 v31, v31, v14\n\t"
    "s_mov_b32 %[bg], 0x7fc00000\n\t"
    "s_branch 13b\n\t"
    "9:"
    : [tc] "=&s"(tc), [sp] "=&s"(sp), [cap] "+s"(cap), [st] "=&s"(st), [bg] "=&s"(bg)
    : [ag] "s"(ag), [c44] "s"(0x4444u), [lsc] "s"(LSC), [sn] "s"(sn), [pb] "s"(pb), [vs] "s"(vs), [od] "s"(od), [neg] "s"(-1.0e38f), [one] "s"(0x3F803F80u), [big] "s"(24.0f), [v2] "s"(v2), [db] "s"(db), "{v[64:95]}"(W.w0), "{v[96:127]}"(W.w1), "{v[128:159]}"(W.w2), "{v[160:191]}"(W.w3), "{v[192:223]}"(W.w4), "{v[224:255]}"(W.w5)
    : "v4", "v5", "v6", "v7", "v8", "v9", "v10", "v11", "v12", "v13", "v14", "v15", "v16", "v17", "v18", "v19", "v20", "v21", "v22", "v23", "v24", "v25", "v26", "v27", "v28", "v29", "v30", "v31", "v32", "v33", "v34", "v35", "v36", "v37", "v38", "v39", "v40", "v41", "v42", "v43", "v44", "v45", "v46", "v47", "v48", "v49", "v50", "v51", "v52", "v53", "v54", "v55", "v56", "v57", "v58", "v59", "v60", "v61", "v62", "v63", "vcc", "scc", "m0", "memory", "s36", "s37", "s38");
}
template <> __device__ __forceinline__ void qk_step<3>(const QKW& W, const char* ag, const char* vs, const char* v2, const char* db, const char* od, unsigned pb, unsigned sn, unsigned& cap, unsigned& sp,
    unsigned long long& tc, unsigned long long* ts, unsigned& d0, unsigned& d1) {
  unsigned st, bg;
  asm volatile("v_mbcnt_lo_u32_b32 v60, -1, 0\n\t"
    "v_mbcnt_hi_u32_b32 v60, -1, v60\n\t"
    "v_lshlrev_b32 v5, 4, v60\n\t"
    "s_mov_b32 %[sp], 0\n\t"
    "s_setprio 2\n\t"
    "global_load_dwordx4 v[24:27], v5, %[ag] sc1\n\t"
    "global_load_dwordx4 v[28:31], v5, %[ag] offset:1024 sc1\n\t"
    "s_memtime %[tc]\n\t"
    "v_lshrrev_b32 v32, 4, v60\n\t"
    "v_and_b32 v33, 15, v60\n\t"
    "v_lshrrev_b32 v34, 2, v33\n\t"
    "v_and_b32 v35, 3, v60\n\t"
    "v_and_b32 v61, 1, v60\n\t"
    "v_lshlrev_b32 v61, 3, v61\n\t"
    "v_lshl_or_b32 v7, v34, 4, v61\n\t"
    "v_lshl_or_b32 v7, v32, 7, v7\n\t"
    "v_and_b32 v10, 2, v34\n\t"
    "v_xor_b32 v10, v10, v32\n\t"
    "v_lshl_or_b32 v10, v10, 4, %[pb]\n\t"
    "v_lshl_or_b32 v10, v33, 6, v10\n\t"
    "v_and_b32 v14, 32, v60\n\t"
    "v_lshl_or_b32 v14, v35, 3, v14\n\t"
    "v_lshrrev_b32 v34, 2, v60\n\t"
    "v_lshl_or_b32 v14, v34, 6, v14\n\t"
    "v_or_b32 v14, %[pb], v14\n\t"
    "v_and_b32 v61, 3, v60\n\t"
    "v_lshlrev_b32 v61, 6, v61\n\t"
    "v_lshl_or_b32 v4, v32, 4, v61\n\t"
    "v_and_b32 v61, 4, v60\n\t"
    "v_lshl_or_b32 v4, v61, 10, v4\n\t"
    "v_and_b32 v61, 8, v60\n\t"
    "v_lshl_or_b32 v4, v61, 14, v4\n\t"
    "v_or_b32 v4, 768, v4\n\t"
    "v_and_b32 v61, 2, v60\n\t"
    "v_mul_u32_u24 v61, 0x5555, v61\n\t"
    "v_or_b32 v6, %[c44], v61\n\t"
    "v_lshlrev_b32 v61, 2, v32\n\t"
    "v_lshl_or_b32 v15, v33, 4, v61\n\t"
    "3:\n\t"
    "v_mov_b32 v16, 0\n\t"
    "v_mov_b32 v17, 0\n\t"
    "v_mov_b32 v18, 0\n\t"
    "v_mov_b32 v19, 0\n\t"
    "v_mov_b32 v20, 0\n\t"
    "v_mov_b32 v21, 0\n\t"
    "v_mov_b32 v22, 0\n\t"
    "v_mov_b32 v23, 0\n\t"
    "s_waitcnt vmcnt(0) lgkmcnt(0)\n\t"
    "33:\n\t"
    "ds_write_b128 v5, v[24:27] offset:6144\n\t"
    "ds_write_b128 v5, v[28:31] offset:7168\n\t"
    "v_smfmac_f32_16x16x64_bf16 v[16:19], v[24:27], v[160:167], v6\n\t"
    "v_max3_u32 v60, v24, v25, v26\n\t"
    "v_max3_u32 v61, v27, v28, v29\n\t"
    "v_smfmac_f32_16x16x64_bf16 v[20:23], v[24:27], v[168:175], v6\n\t"
    "v_max3_u32 v60, v60, v30, v31\n\t"
    "v_max_u32 v60, v60, v61\n\t"
    "v_cmp_eq_u32 vcc, -1, v60\n\t"
    "s_cbranch_vccnz 1f\n\t"
    "2:\n\t"
    "s_waitcnt lgkmcnt(0)\n\t"
    "s_barrier\n\t"
    "ds_read_b128 v[32:35], v5 offset:0\n\t"
    "ds_read_b128 v[36:39], v5 offset:1024\n\t"
    "v_smfmac_f32_16x16x64_bf16 v[16:19], v[28:31], v[176:183], v6\n\t"
    "ds_read_b128 v[40:43], v5 offset:2048\n\t"
    "ds_read_b128 v[44:47], v5 offset:3072\n\t"
    "v_smfmac_f32_16x16x64_bf16 v[20:23], v[28:31], v[184:191], v6\n\t"
    "s_waitcnt lgkmcnt(3)\n\t"
    "v_smfmac_f32_16x16x64_bf16 v[16:19], v[32:35], v[64:71], v6\n\t"
    "ds_read_b128 v[48:51], v5 offset:4096\n\t"
    "v_smfmac_f32_16x16x64_bf16 v[20:23], v[32:35], v[72:79], v6\n\t"
    "ds_read_b128 v[52:55], v5 offset:5120\n\t"
    "s_waitcnt lgkmcnt(4)\n\t"
    "v_smfmac_f32_16x16x64_bf16 v[16:19], v[36:39], v[80:87], v6\n\t"
    "ds_read_b128 v[24:27], v5 offset:8192\n\t"
    "v_smfmac_f32_16x16x64_bf16 v[20:23], v[36:39], v[88:95], v6\n\t"
    "ds_read_b128 v[28:31], v5 offset:9216\n\t"
    "s_waitcnt lgkmcnt(5)\n\t"
    "v_smfmac_f32_16x16x64_bf16 v[16:19], v[40:43], v[96:103], v6\n\t"
    "ds_read_b128 v[56:59], v5 offset:10240\n\t"
    "v_smfmac_f32_16x16x64_bf16 v[20:23], v[40:43], v[104:111], v6\n\t"
    "ds_read_b128 v[60:63], v5 offset:11264\n\t"
    "ds_read_b128 v[36:39], v10 offset:35840\n\t"
    "s_waitcnt lgkmcnt(7)\n\t"
    "v_smfmac_f32_16x16x64_bf16 v[16:19], v[44:47], v[112:119], v6\n\t"
    "ds_read_b128 v[40:43], v10 offset:36864\n\t"
    "v_smfmac_f32_16x16x64_bf16 v[20:23], v[44:47], v[120:127], v6\n\t"
    "ds_read_b128 v[44:47], v10 offset:37888\n\t"
    "s_waitcnt lgkmcnt(8)\n\t"
    "v_smfmac_f32_16x16x64_bf16 v[16:19], v[48:51], v[128:135], v6\n\t"
    "v_smfmac_f32_16x16x64_bf16 v[20:23], v[48:51], v[136:143], v6\n\t"
    "ds_read_b128 v[48:51], v10 offset:38912\n\t"
    "s_waitcnt lgkmcnt(8)\n\t"
    "v_smfmac_f32_16x16x64_bf16 v[16:19], v[52:55], v[144:151], v6\n\t"
    "v_smfmac_f32_16x16x64_bf16 v[20:23], v[52:55], v[152:159], v6\n\t"
    "ds_read_b128 v[52:55], v10 offset:39936\n\t"
    "s_waitcnt lgkmcnt(8)\n\t"
    "v_smfmac_f32_16x16x64_bf16 v[16:19], v[24:27], v[192:199], v6\n\t"
    "v_smfmac_f32_16x16x64_bf16 v[20:23], v[24:27], v[200:207], v6\n\t"
    "ds_read_b128 v[24:27], v10 offset:32768\n\t"
    "s_waitcnt lgkmcnt(8)\n\t"
    "v_smfmac_f32_16x16x64_bf16 v[16:19], v[28:31], v[208:215], v6\n\t"
    "ds_read_b32 v8, v15 offset:29440\n\t"
    "ds_read_b128 v[32:35], v5 offset:27648\n\t"
    "s_setprio 3\n\t"
    "s_waitcnt lgkmcnt(9)\n\t"
    "v_smfmac_f32_16x16x64_bf16 v[16:19], v[56:59], v[224:231], v6\n\t"
    "s_waitcnt lgkmcnt(8)\n\t"
    "v_smfmac_f32_16x16x64_bf16 v[16:19], v[60:63], v[240:247], v6\n\t"
    "v_smfmac_f32_16x16x64_bf16 v[20:23], v[28:31], v[216:223], v6\n\t"
    "s_waitcnt lgkmcnt(0)\n\t"
    "21:\n\t"
    "v_cmp_ne_u32 vcc, %[sn], v8\n\t"
    "s_cbranch_vccnz 22f\n\t"
    "v_mul_f32 v32, %[lsc], v32\n\t"
    "v_mul_f32 v33, %[lsc], v33\n\t"
    "v_smfmac_f32_16x16x64_bf16 v[20:23], v[56:59], v[232:239], v6\n\t"
    "v_mul_f32 v34, %[lsc], v34\n\t"
    "v_mul_f32 v35, %[lsc], v35\n\t"
    "v_add_f32 v16, v16, v18\n\t"
    "v_add_f32 v17, v17, v19\n\t"
    "v_smfmac_f32_16x16x64_bf16 v[20:23], v[60:63], v[248:255], v6\n\t"
    "v_fma_f32 v16, v16, %[lsc], v32\n\t"
    "v_fma_f32 v17, v17, %[lsc], v33\n\t"
    "v_cvt_pk_bf16_f32 v16, v16, v17\n\t"
    "ds_write_b32 v15, v16 offset:17920\n\t"
    "s_mov_b32 %[bg], %[big]\n\t"
    "s_nop 2\n\t"
    "v_add_f32 v20, v20, v22\n\t"
    "v_add_f32 v21, v21, v23\n\t"
    "v_fma_f32 v20, v20, %[lsc], v34\n\t"
    "v_fma_f32 v21, v21, %[lsc], v35\n\t"
    "v_cvt_pk_bf16_f32 v20, v20, v21\n\t"
    "ds_write_b32 v15, v20 offset:18176\n\t"
    "ds_read_b128 v[28:31], v10 offset:33792\n\t"
    "ds_read_b128 v[32:35], v10 offset:34816\n\t"
    "s_waitcnt lgkmcnt(2)\n\t"
    "s_barrier\n\t"
    "ds_read_b64_tr_b16 v[16:17], v7 offset:16384\n\t"
    "ds_read_b64_tr_b16 v[18:19], v7 offset:16448\n\t"
    "ds_read_b64_tr_b16 v[20:21], v7 offset:16896\n\t"
    "ds_read_b64_tr_b16 v[22:23], v7 offset:16960\n\t"
    "ds_read_b64_tr_b16 v[56:57], v7 offset:17408\n\t"
    "ds_read_b64_tr_b16 v[58:59], v7 offset:17472\n\t"
    "ds_read_b64_tr_b16 v[60:61], v7 offset:17920\n\t"
    "ds_read_b64_tr_b16 v[62:63], v7 offset:17984\n\t"
    "global_load_dwordx4 v[6:9], v5, %[vs] sc1\n\t"
    "global_load_dwordx4 v[10:13], v5, %[vs] offset:1024 sc1\n\t"
    "s_waitcnt lgkmcnt(6)\n\t"
    "v_mfma_f32_16x16x32_bf16 v[24:27], v[24:27], v[16:19], 0\n\t"
    "v_mfma_f32_16x16x32_bf16 v[28:31], v[28:31], v[16:19], 0\n\t"
    "s_waitcnt lgkmcnt(4)\n\t"
    "v_mfma_f32_16x16x32_bf16 v[24:27], v[32:35], v[20:23], v[24:27]\n\t"
    "v_mfma_f32_16x16x32_bf16 v[28:31], v[36:39], v[20:23], v[28:31]\n\t"
    "v_xor_b32 v39, 32, v14\n\t"
    "ds_read_b64_tr_b16 v[32:33], v14 offset:47104\n\t"
    "ds_read_b64_tr_b16 v[34:35], v14 offset:48128\n\t"
    "ds_read_b64_tr_b16 v[36:37], v39 offset:47104\n\t"
    "ds_read_b64_tr_b16 v[38:39], v39 offset:48128\n\t"
    "s_waitcnt lgkmcnt(6)\n\t"
    "v_mfma_f32_16x16x32_bf16 v[24:27], v[40:43], v[56:59], v[24:27]\n\t"
    "v_mfma_f32_16x16x32_bf16 v[28:31], v[44:47], v[56:59], v[28:31]\n\t"
    "s_waitcnt lgkmcnt(4)\n\t"
    "v_mfma_f32_16x16x32_bf16 v[24:27], v[48:51], v[60:63], v[24:27]\n\t"
    "v_mfma_f32_16x16x32_bf16 v[28:31], v[52:55], v[60:63], v[28:31]\n\t"
    "v_mov_b32 v52, %[one]\n\t"
    "v_mov_b32 v53, %[one]\n\t"
    "v_mov_b32 v54, %[one]\n\t"
    "v_mov_b32 v55, %[one]\n\t"
    "v_mov_b32 v45, 0\n\t"
    "v_mov_b32 v47, 0\n\t"
    "v_mov_b32 v48, %[one]\n\t"
    "v_max3_f32 v14, |v24|, |v25|, |v26|\n\t"
    "v_max3_f32 v40, |v27|, |v28|, |v29|\n\t"
    "v_max3_f32 v14, v14, |v30|, |v31|\n\t"
    "v_max_f32 v14, v14, v40\n\t"
    "v_cmp_nge_f32 vcc, %[big], v14\n\t"
    "v_mov_b32 v14, 0\n\t"
    "s_cbranch_vccnz 12f\n\t"
    "13:\n\t"
    "v_exp_f32 v24, v24\n\t"
    "v_exp_f32 v25, v25\n\t"
    "v_exp_f32 v26, v26\n\t"
    "v_exp_f32 v27, v27\n\t"
    "v_exp_f32 v28, v28\n\t"
    "v_exp_f32 v29, v29\n\t"
    "v_exp_f32 v30, v30\n\t"
    "v_exp_f32 v31, v31\n\t"
    "v_lshrrev_b32 v46, 2, v5\n\t"
    "global_load_dword v44, v46, %[v2] sc1\n\t"
    "global_load_dword v46, v46, %[v2] offset:256 sc1\n\t"
    "v_cvt_pk_bf16_f32 v40, v24, v25\n\t"
    "v_cvt_pk_bf16_f32 v41, v26, v27\n\t"
    "v_cvt_pk_bf16_f32 v42, v28, v29\n\t"
    "v_cvt_pk_bf16_f32 v43, v30, v31\n\t"
    "v_mov_b32 v49, 0\n\t"
    "v_mov_b32 v51, 0\n\t"
    "s_waitcnt lgkmcnt(0)\n\t"
    "v_mfma_f32_16x16x32_bf16 v[52:55], v[52:55], v[40:43], 0\n\t"
    "v_mfma_f32_16x16x32_bf16 v[24:27], v[32:35], v[40:43], 0\n\t"
    "v_mfma_f32_16x16x32_bf16 v[28:31], v[36:39], v[40:43], 0\n\t"
    "s_waitcnt vmcnt(3)\n\t"
    "5:\n\t"
    "v_mfma_f32_16x16x32_bf16 v[32:35], v[6:9], v[16:19], 0\n\t"
    "v_mfma_f32_16x16x32_bf16 v[36:39], v[6:9], v[20:23], 0\n\t"
    "s_waitcnt vmcnt(2)\n\t"
    "v_mfma_f32_16x16x32_bf16 v[32:35], v[10:13], v[56:59], v[32:35]\n\t"
    "v_mfma_f32_16x16x32_bf16 v[36:39], v[10:13], v[60:63], v[36:39]\n\t"
    "v_max3_u32 v40, v6, v7, v8\n\t"
    "v_max3_u32 v41, v9, v10, v11\n\t"
    "v_max3_u32 v40, v40, v12, v13\n\t"
    "v_max_u32 v40, v40, v41\n\t"
    "v_cmp_eq_u32 vcc, -1, v40\n\t"
    "s_cbranch_vccnz 4f\n\t"
    "6:\n\t"
    "s_nop 0\n\t"
    "s_nop 0\n\t"
    "v_add_f32 v32, v32, v38\n\t"
    "v_add_f32 v33, v33, v39\n\t"
    "v_exp_f32 v34, v32\n\t"
    "v_max_f32 v40, v32, v33\n\t"
    "v_exp_f32 v35, v33\n\t"
    "v_cmp_nge_f32 vcc, %[bg], v40\n\t"
    "s_cbranch_vccnz 10f\n\t"
    "11:\n\t"
    "v_cvt_pk_bf16_f32 v50, v34, v35\n\t"
    "s_nop 1\n\t"
    "v_mfma_f32_16x16x16_bf16 v[52:55], v[48:49], v[50:51], v[52:55]\n\t"
    "17:\n\t"
    "s_waitcnt vmcnt(0) lgkmcnt(0)\n\t"
    "v_mfma_f32_16x16x16_bf16 v[32:35], v[44:45], v[50:51], v[24:27]\n\t"
    "v_mfma_f32_16x16x16_bf16 v[36:39], v[46:47], v[50:51], v[28:31]\n\t"
    "v_max_u32 v40, v44, v46\n\t"
    "v_cmp_eq_u32 vcc, -1, v40\n\t"
    "s_memrealtime s[36:37]\n\t"
    "s_nop 1\n\t"
    "v_rcp_f32 v42, v52\n\t"
    "s_cbranch_vccnz 18f\n\t"
    "19:\n\t"
    "v_pk_mul_f32 v[32:33], v[32:33], v[42:43] op_sel_hi:[1,0]\n\t"
    "v_pk_mul_f32 v[34:35], v[34:35], v[42:43] op_sel_hi:[1,0]\n\t"
    "v_pk_mul_f32 v[36:37], v[36:37], v[42:43] op_sel_hi:[1,0]\n\t"
    "v_pk_mul_f32 v[38:39], v[38:39], v[42:43] op_sel_hi:[1,0]\n\t"
    "v_cvt_pk_bf16_f32 v24, v32, v33\n\t"
    "v_cvt_pk_bf16_f32 v25, v34, v35\n\t"
    "v_cvt_pk_bf16_f32 v26, v36, v37\n\t"
    "v_cvt_pk_bf16_f32 v27, v38, v39\n\t"
    "v_min_u32 v24, -2, v24\n\t"
    "v_min_u32 v25, -2, v25\n\t"
    "v_min_u32 v26, -2, v26\n\t"
    "v_min_u32 v27, -2, v27\n\t"
    "global_store_dwordx4 v4, v[24:27], %[od] sc1\n\t"
    "v_lshlrev_b32 v40, 10, v5\n\t"
    "s_waitcnt lgkmcnt(0)\n\t"
    "s_and_b32 s36, s36, 0xffffff\n\t"
    "s_or_b32 s36, s36, %[sn]\n\t"
    "v_mov_b32 v41, s36\n\t"
    "s_mov_b64 exec, 7\n\t"
    "global_store_dword v40, v41, %[db] offset:8 sc1\n\t"
    "s_mov_b64 exec, -1\n\t"
    "ds_write_b32 v15, v44 offset:19968\n\t"
    "ds_write_b32 v15, v46 offset:20224\n\t"
    "s_setprio 1\n\t"
    "s_waitcnt lgkmcnt(0)\n\t"
    "s_barrier\n\t"
    "s_waitcnt lgkmcnt(0)\n\t"
    "s_branch 9f\n\t"
    "1:\n\t"
    "s_add_u32 %[sp], %[sp], 1\n\t"
    "s_cmp_ge_u32 %[sp], %[cap]\n\t"
    "s_cbranch_scc1 7f\n\t"
    "global_load_dwordx4 v[24:27], v5, %[ag] sc1\n\t"
    "global_load_dwordx4 v[28:31], v5, %[ag] offset:1024 sc1\n\t"
    "s_waitcnt lgkmcnt(0)\n\t"
    "s_memtime %[tc]\n\t"
    "s_nop 7\n\t"
    "s_branch 3b\n\t"
    "7:\n\t"
    "s_mov_b32 %[cap], 0\n\t"
    "s_branch 2b\n\t"
    "4:\n\t"
    "s_add_u32 %[sp], %[sp], 0x10000\n\t"
    "s_lshr_b32 %[st], %[sp], 16\n\t"
    "s_cmp_ge_u32 %[st], %[cap]\n\t"
    "s_cbranch_scc1 8f\n\t"
    "global_load_dwordx4 v[6:9], v5, %[vs] sc1\n\t"
    "global_load_dwordx4 v[10:13], v5, %[vs] offset:1024 sc1\n\t"
    "s_waitcnt vmcnt(0) lgkmcnt(0)\n\t"
    "s_branch 5b\n\t"
    "8:\n\t"
    "s_mov_b32 %[cap], 0\n\t"
    "s_branch 6b\n\t"
    "18:\n\t"
    "s_add_u32 %[sp], %[sp], 0x10000\n\t"
    "s_lshr_b32 %[st], %[sp], 16\n\t"
    "s_cmp_ge_u32 %[st], %[cap]\n\t"
    "s_cbranch_scc1 20f\n\t"
    "v_lshrrev_b32 v46, 2, v5\n\t"
    "global_load_dword v44, v46, %[v2] sc1\n\t"
    "global_load_dword v46, v46, %[v2] offset:256 sc1\n\t"
    "s_waitcnt vmcnt(0) lgkmcnt(0)\n\t"
    "s_branch 17b\n\t"
    "20:\n\t"
    "s_mov_b32 %[cap], 0\n\t"
    "s_branch 19b\n\t"
    "22:\n\t"
    "ds_read_b32 v8, v15 offset:29440\n\t"
    "ds_read_b128 v[32:35], v5 offset:27648\n\t"
    "s_waitcnt lgkmcnt(0)\n\t"
    "s_branch 21b\n\t"
    "10:\n\t"
    "v_max_f32 v40, v40, v14\n\t"
    "v_mov_b32 v42, v40\n\t"
    "s_nop 1\n\t"
    "v_permlane16_swap_b32 v40, v42\n\t"
    "v_max_f32 v40, v40, v42\n\t"
    "v_mov_b32 v42, v40\n\t"
    "s_nop 1\n\t"
    "v_permlane32_swap_b32 v40, v42\n\t"
    "v_max_f32 v40, v40, v42\n\t"
    "v_sub_f32 v34, v32, v40\n\t"
    "v_sub_f32 v35, v33, v40\n\t"
    "v_sub_f32 v40, v14, v40\n\t"
    "v_exp_f32 v40, v40\n\t"
    "v_exp_f32 v34, v34\n\t"
    "v_exp_f32 v35, v35\n\t"
    "v_mul_f32 v24, v24, v40\n\t"
    "v_mul_f32 v25, v25, v40\n\t"
    "v_mul_f32 v26, v26, v40\n\t"
    "v_mul_f32 v27, v27, v40\n\t"
    "v_mul_f32 v28, v28, v40\n\t"
    "v_mul_f32 v29, v29, v40\n\t"
    "v_mul_f32 v30, v30, v40\n\t"
    "v_mul_f32 v31, v31, v40\n\t"
    "v_mul_f32 v52, v52, v40\n\t"
    "v_mul_f32 v53, v53, v40\n\t"
    "v_mul_f32 v54, v54, v40\n\t"
    "v_mul_f32 v55, v55, v40\n\t"
    "s_branch 11b\n\t"
    "12:\n\t"
    "v_max3_f32 v14, v24, v25, v26\n\t"
    "v_max3_f32 v40, v27, v28, v29\n\t"
    "v_max3_f32 v14, v14, v30, v31\n\t"
    "v_max3_f32 v14, v14, v40, %[neg]\n\t"
    "v_mov_b32 v40, v14\n\t"
    "s_nop 1\n\t"
    "v_permlane16_swap_b32 v14, v40\n\t"
    "v_max_f32 v14, v14, v40\n\t"
    "v_mov_b32 v40, v14\n\t"
    "s_nop 1\n\t"
    "v_permlane32_swap_b32 v14, v40\n\t"
    "v_max_f32 v14, v14, v40\n\t"
    "v_sub_f32 v24, v24, v14\n\t"
    "v_sub_f32 v25, v25, v14\n\t"
    "v_sub_f32 v26, v26, v14\n\t"
    "v_sub_f32 v27, v27, v14\n\t"
    "v_sub_f32 v28, v28, v14\n\t"
    "v_sub_f32 v29, v29, v14\n\t"
    "v_sub_f32 v30, v30, v14\n\t"
    "v_sub_f32 v31, v31, v14\n\t"
    "s_mov_b32 %[bg], 0x7fc00000\n\t"
    "s_branch 13b\n\t"
    "9:"
    : [tc] "=&s"(tc), [sp] "=&s"(sp), [cap] "+s"(cap), [st] "=&s"(st), [bg] "=&s"(bg)
    : [ag] "s"(ag), [c44] "s"(0x4444u), [lsc] "s"(LSC), [sn] "s"(sn), [pb] "s"(pb), [vs] "s"(vs), [od] "s"(od), [neg] "s"(-1.0e38f), [one] "s"(0x3F803F80u), [big] "s"(24.0f), [v2] "s"(v2), [db] "s"(db), "{v[64:95]}"(W.w0), "{v[96:127]}"(W.w1), "{v[128:159]}"(W.w2), "{v[160:191]}"(W.w3), "{v[192:223]}"(W.w4), "{v[224:255]}"(W.w5)
    : "v4", "v5", "v6", "v7", "v8", "v9", "v10", "v11", "v12", "v13", "v14", "v15", "v16", "v17", "v18", "v19", "v20", "v21", "v22", "v23", "v24", "v25", "v26", "v27", "v28", "v29", "v30", "v31", "v32", "v33", "v34", "v35", "v36", "v37", "v38", "v39", "v40", "v41", "v42", "v43", "v44", "v45", "v46", "v47", "v48", "v49", "v50", "v51", "v52", "v53", "v54", "v55", "v56", "v57", "v58", "v59", "v60", "v61", "v62", "v63", "vcc", "scc", "m0", "memory", "s36", "s37", "s38");
}
template <> __device__ __forceinline__ void qk_step<4>(const QKW& W, const char* ag, const char* vs, const char* v2, const char* db, const char* od, unsigned pb, unsigned sn, unsigned& cap, unsigned& sp,
    unsigned long long& tc, unsigned long long* ts, unsigned& d0, unsigned& d1) {
  unsigned st, bg;
  asm volatile("v_mbcnt_lo_u32_b32 v60, -1, 0\n\t"
    "v_mbcnt_hi_u32_b32 v60, -1, v60\n\t"
    "v_lshlrev_b32 v6, 4, v60\n\t"
    "s_mov_b32 %[sp], 0\n\t"
    "s_setprio 3\n\t"
    "global_load_dwordx4 v[24:27], v6, %[ag] sc1\n\t"
    "global_load_dwordx4 v[28:31], v6, %[ag] offset:-4096 sc1\n\t"
    "s_memtime %[tc]\n\t"
    "v_lshrrev_b32 v56, 4, v60\n\t"
    "v_and_b32 v57, 15, v60\n\t"
    "v_mov_b32 v20, 0\n\t"
    "v_mov_b32 v21, 0\n\t"
    "v_mov_b32 v22, 0\n\t"
    "v_mov_b32 v23, 0\n\t"
    "v_mov_b32 v11, %[sn]\n\t"
    "v_mov_b32 v10, -1\n\t"
    "v_and_b32 v61, 2, v60\n\t"
    "v_mul_u32_u24 v61, 0x5555, v61\n\t"
    "v_or_b32 v8, %[c44], v61\n\t"
    "v_lshlrev_b32 v61, 2, v56\n\t"
    "v_lshl_or_b32 v9, v57, 4, v61\n\t"
    "3:\n\t"
    "v_mov_b32 v12, 0\n\t"
    "v_mov_b32 v13, 0\n\t"
    "v_mov_b32 v14, 0\n\t"
    "v_mov_b32 v15, 0\n\t"
    "v_mov_b32 v16, 0\n\t"
    "v_mov_b32 v17, 0\n\t"
    "v_mov_b32 v18, 0\n\t"
    "v_mov_b32 v19, 0\n\t"
    "s_waitcnt vmcnt(0) lgkmcnt(0)\n\t"
    "33:\n\t"
    "ds_write_b128 v6, v[24:27] offset:12288\n\t"
    "ds_write_b128 v6, v[28:31] offset:8192\n\t"
    "v_smfmac_f32_16x16x64_bf16 v[12:15], v[24:27], v[64:71], v8\n\t"
    "v_max3_u32 v60, v24, v25, v26\n\t"
    "v_max3_u32 v61, v27, v28, v29\n\t"
    "v_smfmac_f32_16x16x64_bf16 v[16:19], v[24:27], v[72:79], v8\n\t"
    "v_max3_u32 v60, v60, v30, v31\n\t"
    "v_max_u32 v60, v60, v61\n\t"
    "v_cmp_eq_u32 vcc, -1, v60\n\t"
    "s_cbranch_vccnz 1f\n\t"
    "2:\n\t"
    "s_waitcnt lgkmcnt(0)\n\t"
    "s_barrier\n\t"
    "ds_read_b128 v[32:35], v6 offset:13312\n\t"
    "ds_read_b128 v[36:39], v6 offset:14336\n\t"
    "ds_read_b128 v[40:43], v6 offset:15360\n\t"
    "ds_write_b32 v9, v10 offset:18432\n\t"
    "s_waitcnt lgkmcnt(3)\n\t"
    "v_smfmac_f32_16x16x64_bf16 v[12:15], v[32:35], v[80:87], v8\n\t"
    "v_smfmac_f32_16x16x64_bf16 v[16:19], v[32:35], v[88:95], v8\n\t"
    "s_waitcnt lgkmcnt(2)\n\t"
    "v_smfmac_f32_16x16x64_bf16 v[12:15], v[36:39], v[96:103], v8\n\t"
    "v_smfmac_f32_16x16x64_bf16 v[16:19], v[36:39], v[104:111], v8\n\t"
    "s_waitcnt lgkmcnt(1)\n\t"
    "v_smfmac_f32_16x16x64_bf16 v[12:15], v[40:43], v[112:119], v8\n\t"
    "v_smfmac_f32_16x16x64_bf16 v[16:19], v[40:43], v[120:127], v8\n\t"
    "s_setprio 0\n\t"
    "s_sleep 1\n\t"
    "s_nop 6\n\t"
    "v_add_f32 v12, v12, v14\n\t"
    "v_add_f32 v13, v13, v15\n\t"
    "v_add_f32 v14, v16, v18\n\t"
    "v_add_f32 v15, v17, v19\n\t"
    "ds_write_b128 v6, v[12:15] offset:24576\n\t"
    "ds_write_b32 v9, v11 offset:28672\n\t"
    "s_setprio 0\n\t"
    "s_memrealtime s[36:37]\n\t"
    "s_waitcnt lgkmcnt(0)\n\t"
    "s_mov_b32 s38, s36\n\t"
    "s_min_u32 s37, %[d0], %[d1]\n\t"
    "s_add_u32 s36, s36, s37\n\t"
    "s_and_b32 s36, s36, 0xffffff\n\t"
    "s_or_b32 s36, s36, %[sn]\n\t"
    "v_lshlrev_b32 v10, 10, v6\n\t"
    "v_mov_b32 v5, s36\n\t"
    "s_mov_b64 exec, 15\n\t"
    "global_store_dword v10, v5, %[vs] offset:-4096 sc1\n\t"
    "s_mov_b64 exec, -1\n\t"
    "ds_read_b128 v[44:47], v6 offset:0\n\t"
    "ds_read_b128 v[48:51], v6 offset:1024\n\t"
    "ds_read_b128 v[52:55], v6 offset:2048\n\t"
    "ds_read_b128 v[56:59], v6 offset:3072\n\t"
    "ds_read_b128 v[60:63], v6 offset:4096\n\t"
    "s_sleep 1\n\t"
    "v_smfmac_f32_16x16x64_bf16 v[20:23], v[28:31], v[192:199], v8\n\t"
    "ds_read_b128 v[28:31], v6 offset:5120\n\t"
    "v_smfmac_f32_16x16x64_bf16 v[20:23], v[24:27], v[224:231], v8\n\t"
    "ds_read_b128 v[24:27], v6 offset:6144\n\t"
    "v_smfmac_f32_16x16x64_bf16 v[20:23], v[32:35], v[240:247], v8\n\t"
    "ds_read_b128 v[32:35], v6 offset:7168\n\t"
    "v_smfmac_f32_16x16x64_bf16 v[20:23], v[36:39], v[232:239], v8\n\t"
    "ds_read_b128 v[36:39], v6 offset:9216\n\t"
    "v_smfmac_f32_16x16x64_bf16 v[20:23], v[40:43], v[248:255], v8\n\t"
    "ds_read_b128 v[40:43], v6 offset:10240\n\t"
    "s_waitcnt lgkmcnt(9)\n\t"
    "v_smfmac_f32_16x16x64_bf16 v[20:23], v[44:47], v[128:135], v8\n\t"
    "ds_read_b128 v[44:47], v6 offset:11264\n\t"
    "s_barrier\n\t"
    "s_setprio 0\n\t"
    "s_waitcnt lgkmcnt(9)\n\t"
    "v_smfmac_f32_16x16x64_bf16 v[20:23], v[48:51], v[144:151], v8\n\t"
    "s_waitcnt lgkmcnt(8)\n\t"
    "v_smfmac_f32_16x16x64_bf16 v[20:23], v[52:55], v[136:143], v8\n\t"
    "s_waitcnt lgkmcnt(7)\n\t"
    "v_smfmac_f32_16x16x64_bf16 v[20:23], v[56:59], v[152:159], v8\n\t"
    "s_waitcnt lgkmcnt(6)\n\t"
    "v_smfmac_f32_16x16x64_bf16 v[20:23], v[60:63], v[160:167], v8\n\t"
    "s_waitcnt lgkmcnt(5)\n\t"
    "v_smfmac_f32_16x16x64_bf16 v[20:23], v[28:31], v[176:183], v8\n\t"
    "s_waitcnt lgkmcnt(4)\n\t"
    "v_smfmac_f32_16x16x64_bf16 v[20:23], v[24:27], v[168:175], v8\n\t"
    "s_waitcnt lgkmcnt(3)\n\t"
    "v_smfmac_f32_16x16x64_bf16 v[20:23], v[32:35], v[184:191], v8\n\t"
    "s_waitcnt lgkmcnt(2)\n\t"
    "v_smfmac_f32_16x16x64_bf16 v[20:23], v[36:39], v[208:215], v8\n\t"
    "s_waitcnt lgkmcnt(1)\n\t"
    "v_smfmac_f32_16x16x64_bf16 v[20:23], v[40:43], v[200:207], v8\n\t"
    "s_waitcnt lgkmcnt(0)\n\t"
    "v_smfmac_f32_16x16x64_bf16 v[20:23], v[44:47], v[216:223], v8\n\t"
    "s_nop 7\n\t"
    "v_add_f32 v20, v20, v22\n\t"
    "v_add_f32 v21, v21, v23\n\t"
    "v_cvt_pk_bf16_f32 v20, v20, v21\n\t"
    "v_min_u32 v20, -2, v20\n\t"
    "ds_write_b32 v9, v20 offset:18432\n\t"
    "s_waitcnt lgkmcnt(0)\n\t"
    "s_setprio 3\n\t"
    "s_barrier\n\t"
    "s_memrealtime s[36:37]\n\t"
    "s_mov_b32 %[d1], %[d0]\n\t"
    "s_waitcnt lgkmcnt(0)\n\t"
    "s_sub_u32 %[d0], s36, s38\n\t"
    "s_min_u32 %[d0], %[d0], 200\n\t"
    "s_waitcnt lgkmcnt(0)\n\t"
    "s_branch 9f\n\t"
    "1:\n\t"
    "s_add_u32 %[sp], %[sp], 1\n\t"
    "s_cmp_ge_u32 %[sp], %[cap]\n\t"
    "s_cbranch_scc1 7f\n\t"
    "global_load_dwordx4 v[24:27], v6, %[ag] sc1\n\t"
    "global_load_dwordx4 v[28:31], v6, %[ag] offset:-4096 sc1\n\t"
    "s_waitcnt lgkmcnt(0)\n\t"
    "s_memtime %[tc]\n\t"
    "s_nop 7\n\t"
    "s_branch 3b\n\t"
    "7:\n\t"
    "s_mov_b32 %[cap], 0\n\t"
    "s_branch 2b\n\t"
    "9:"
    : [tc] "=&s"(tc), [sp] "=&s"(sp), [cap] "+s"(cap), [st] "=&s"(st), [d0] "+s"(d0), [d1] "+s"(d1)
    : [ag] "s"(ag), [c44] "s"(0x4444u), [lsc] "s"(LSC), [sn] "s"(sn), [vs] "s"(vs), "{v[64:95]}"(W.w0), "{v[96:127]}"(W.w1), "{v[128:159]}"(W.w2), "{v[160:191]}"(W.w3), "{v[192:223]}"(W.w4), "{v[224:255]}"(W.w5)
    : "v5", "v6", "v7", "v8", "v9", "v10", "v11", "v12", "v13", "v14", "v15", "v16", "v17", "v18", "v19", "v20", "v21", "v22", "v23", "v24", "v25", "v26", "v27", "v28", "v29", "v30", "v31", "v32", "v33", "v34", "v35", "v36", "v37", "v38", "v39", "v40", "v41", "v42", "v43", "v44", "v45", "v46", "v47", "v48", "v49", "v50", "v51", "v52", "v53", "v54", "v55", "v56", "v57", "v58", "v59", "v60", "v61", "v62", "v63", "vcc", "scc", "m0", "memory", "s36", "s37", "s38");
}
template <> __device__ __forceinline__ void qk_step<5>(const QKW& W, const char* ag, const char* vs, const char* v2, const char* db, const char* od, unsigned pb, unsigned sn, unsigned& cap, unsigned& sp,
    unsigned long long& tc, unsigned long long* ts, unsigned& d0, unsigned& d1) {
  unsigned st, bg;
  asm volatile("v_mbcnt_lo_u32_b32 v60, -1, 0\n\t"
    "v_mbcnt_hi_u32_b32 v60, -1, v60\n\t"
    "v_lshlrev_b32 v6, 4, v60\n\t"
    "s_mov_b32 %[sp], 0\n\t"
    "s_setprio 3\n\t"
    "global_load_dwordx4 v[24:27], v6, %[ag] sc1\n\t"
    "global_load_dwordx4 v[28:31], v6, %[ag] offset:-4096 sc1\n\t"
    "s_memtime %[tc]\n\t"
    "v_lshrrev_b32 v56, 4, v60\n\t"
    "v_and_b32 v57, 15, v60\n\t"
    "v_mov_b32 v20, 0\n\t"
    "v_mov_b32 v21, 0\n\t"
    "v_mov_b32 v22, 0\n\t"
    "v_mov_b32 v23, 0\n\t"
    "v_mov_b32 v11, %[sn]\n\t"
    "v_mov_b32 v10, -1\n\t"
    "v_and_b32 v61, 2, v60\n\t"
    "v_mul_u32_u24 v61, 0x5555, v61\n\t"
    "v_or_b32 v8, %[c44], v61\n\t"
    "v_lshlrev_b32 v61, 2, v56\n\t"
    "v_lshl_or_b32 v9, v57, 4, v61\n\t"
    "3:\n\t"
    "v_mov_b32 v12, 0\n\t"
    "v_mov_b32 v13, 0\n\t"
    "v_mov_b32 v14, 0\n\t"
    "v_mov_b32 v15, 0\n\t"
    "v_mov_b32 v16, 0\n\t"
    "v_mov_b32 v17, 0\n\t"
    "v_mov_b32 v18, 0\n\t"
    "v_mov_b32 v19, 0\n\t"
    "s_waitcnt vmcnt(0) lgkmcnt(0)\n\t"
    "33:\n\t"
    "ds_write_b128 v6, v[24:27] offset:13312\n\t"
    "ds_write_b128 v6, v[28:31] offset:9216\n\t"
    "v_smfmac_f32_16x16x64_bf16 v[12:15], v[24:27], v[80:87], v8\n\t"
    "v_max3_u32 v60, v24, v25, v26\n\t"
    "v_max3_u32 v61, v27, v28, v29\n\t"
    "v_smfmac_f32_16x16x64_bf16 v[16:19], v[24:27], v[88:95], v8\n\t"
    "v_max3_u32 v60, v60, v30, v31\n\t"
    "v_max_u32 v60, v60, v61\n\t"
    "v_cmp_eq_u32 vcc, -1, v60\n\t"
    "s_cbranch_vccnz 1f\n\t"
    "2:\n\t"
    "s_waitcnt lgkmcnt(0)\n\t"
    "s_barrier\n\t"
    "ds_read_b128 v[32:35], v6 offset:12288\n\t"
    "ds_read_b128 v[36:39], v6 offset:14336\n\t"
    "ds_read_b128 v[40:43], v6 offset:15360\n\t"
    "ds_write_b32 v9, v10 offset:18688\n\t"
    "s_waitcnt lgkmcnt(3)\n\t"
    "v_smfmac_f32_16x16x64_bf16 v[12:15], v[32:35], v[64:71], v8\n\t"
    "v_smfmac_f32_16x16x64_bf16 v[16:19], v[32:35], v[72:79], v8\n\t"
    "s_waitcnt lgkmcnt(2)\n\t"
    "v_smfmac_f32_16x16x64_bf16 v[12:15], v[36:39], v[96:103], v8\n\t"
    "v_smfmac_f32_16x16x64_bf16 v[16:19], v[36:39], v[104:111], v8\n\t"
    "s_waitcnt lgkmcnt(1)\n\t"
    "v_smfmac_f32_16x16x64_bf16 v[12:15], v[40:43], v[112:119], v8\n\t"
    "v_smfmac_f32_16x16x64_bf16 v[16:19], v[40:43], v[120:127], v8\n\t"
    "s_setprio 0\n\t"
    "s_sleep 1\n\t"
    "s_nop 6\n\t"
    "v_add_f32 v12, v12, v14\n\t"
    "v_add_f32 v13, v13, v15\n\t"
    "v_add_f32 v14, v16, v18\n\t"
    "v_add_f32 v15, v17, v19\n\t"
    "ds_write_b128 v6, v[12:15] offset:25600\n\t"
    "ds_write_b32 v9, v11 offset:28928\n\t"
    "s_setprio 0\n\t"
    "ds_read_b128 v[44:47], v6 offset:0\n\t"
    "ds_read_b128 v[48:51], v6 offset:1024\n\t"
    "ds_read_b128 v[52:55], v6 offset:2048\n\t"
    "ds_read_b128 v[56:59], v6 offset:3072\n\t"
    "ds_read_b128 v[60:63], v6 offset:4096\n\t"
    "s_sleep 1\n\t"
    "v_smfmac_f32_16x16x64_bf16 v[20:23], v[28:31], v[208:215], v8\n\t"
    "ds_read_b128 v[28:31], v6 offset:5120\n\t"
    "v_smfmac_f32_16x16x64_bf16 v[20:23], v[24:27], v[240:247], v8\n\t"
    "ds_read_b128 v[24:27], v6 offset:6144\n\t"
    "v_smfmac_f32_16x16x64_bf16 v[20:23], v[32:35], v[224:231], v8\n\t"
    "ds_read_b128 v[32:35], v6 offset:7168\n\t"
    "v_smfmac_f32_16x16x64_bf16 v[20:23], v[36:39], v[232:239], v8\n\t"
    "ds_read_b128 v[36:39], v6 offset:8192\n\t"
    "v_smfmac_f32_16x16x64_bf16 v[20:23], v[40:43], v[248:255], v8\n\t"
    "ds_read_b128 v[40:43], v6 offset:10240\n\t"
    "s_waitcnt lgkmcnt(9)\n\t"
    "v_smfmac_f32_16x16x64_bf16 v[20:23], v[44:47], v[128:135], v8\n\t"
    "ds_read_b128 v[44:47], v6 offset:11264\n\t"
    "s_barrier\n\t"
    "s_setprio 0\n\t"
    "s_waitcnt lgkmcnt(9)\n\t"
    "v_smfmac_f32_16x16x64_bf16 v[20:23], v[48:51], v[144:151], v8\n\t"
    "s_waitcnt lgkmcnt(8)\n\t"
    "v_smfmac_f32_16x16x64_bf16 v[20:23], v[52:55], v[136:143], v8\n\t"
    "s_waitcnt lgkmcnt(7)\n\t"
    "v_smfmac_f32_16x16x64_bf16 v[20:23], v[56:59], v[152:159], v8\n\t"
    "s_waitcnt lgkmcnt(6)\n\t"
    "v_smfmac_f32_16x16x64_bf16 v[20:23], v[60:63], v[160:167], v8\n\t"
    "s_waitcnt lgkmcnt(5)\n\t"
    "v_smfmac_f32_16x16x64_bf16 v[20:23], v[28:31], v[176:183], v8\n\t"
    "s_waitcnt lgkmcnt(4)\n\t"
    "v_smfmac_f32_16x16x64_bf16 v[20:23], v[24:27], v[168:175], v8\n\t"
    "s_waitcnt lgkmcnt(3)\n\t"
    "v_smfmac_f32_16x16x64_bf16 v[20:23], v[32:35], v[184:191], v8\n\t"
    "s_waitcnt lgkmcnt(2)\n\t"
    "v_smfmac_f32_16x16x64_bf16 v[20:23], v[36:39], v[192:199], v8\n\t"
    "s_waitcnt lgkmcnt(1)\n\t"
    "v_smfmac_f32_16x16x64_bf16 v[20:23], v[40:43], v[200:207], v8\n\t"
    "s_waitcnt lgkmcnt(0)\n\t"
    "v_smfmac_f32_16x16x64_bf16 v[20:23], v[44:47], v[216:223], v8\n\t"
    "s_nop 7\n\t"
    "v_add_f32 v20, v20, v22\n\t"
    "v_add_f32 v21, v21, v23\n\t"
    "v_cvt_pk_bf16_f32 v20, v20, v21\n\t"
    "v_min_u32 v20, -2, v20\n\t"
    "ds_write_b32 v9, v20 offset:18688\n\t"
    "s_waitcnt lgkmcnt(0)\n\t"
    "s_setprio 0\n\t"
    "s_setprio 0\n\t"
    "s_waitcnt lgkmcnt(0)\n\t"
    "s_branch 9f\n\t"
    "1:\n\t"
    "s_add_u32 %[sp], %[sp], 1\n\t"
    "s_cmp_ge_u32 %[sp], %[cap]\n\t"
    "s_cbranch_scc1 7f\n\t"
    "global_load_dwordx4 v[24:27], v6, %[ag] sc1\n\t"
    "global_load_dwordx4 v[28:31], v6, %[ag] offset:-4096 sc1\n\t"
    "s_waitcnt lgkmcnt(0)\n\t"
    "s_memtime %[tc]\n\t"
    "s_nop 7\n\t"
    "s_branch 3b\n\t"
    "7:\n\t"
    "s_mov_b32 %[cap], 0\n\t"
    "s_branch 2b\n\t"
    "9:"
    : [tc] "=&s"(tc), [sp] "=&s"(sp), [cap] "+s"(cap), [st] "=&s"(st)
    : [ag] "s"(ag), [c44] "s"(0x4444u), [lsc] "s"(LSC), [sn] "s"(sn), "{v[64:95]}"(W.w0), "{v[96:127]}"(W.w1), "{v[128:159]}"(W.w2), "{v[160:191]}"(W.w3), "{v[192:223]}"(W.w4), "{v[224:255]}"(W.w5)
    : "v6", "v7", "v8", "v9", "v10", "v11", "v12", "v13", "v14", "v15", "v16", "v17", "v18", "v19", "v20", "v21", "v22", "v23", "v24", "v25", "v26", "v27", "v28", "v29", "v30", "v31", "v32", "v33", "v34", "v35", "v36", "v37", "v38", "v39", "v40", "v41", "v42", "v43", "v44", "v45", "v46", "v47", "v48", "v49", "v50", "v51", "v52", "v53", "v54", "v55", "v56", "v57", "v58", "v59", "v60", "v61", "v62", "v63", "vcc", "scc", "m0", "memory");
}
template <> __device__ __forceinline__ void qk_step<6>(const QKW& W, const char* ag, const char* vs, const char* v2, const char* db, const char* od, unsigned pb, unsigned sn, unsigned& cap, unsigned& sp,
    unsigned long long& tc, unsigned long long* ts, unsigned& d0, unsigned& d1) {
  unsigned st, bg;
  asm volatile("v_mbcnt_lo_u32_b32 v60, -1, 0\n\t"
    "v_mbcnt_hi_u32_b32 v60, -1, v60\n\t"
    "v_lshlrev_b32 v6, 4, v60\n\t"
    "s_mov_b32 %[sp], 0\n\t"
    "s_setprio 3\n\t"
    "global_load_dwordx4 v[24:27], v6, %[ag] sc1\n\t"
    "global_load_dwordx4 v[28:31], v6, %[ag] offset:-4096 sc1\n\t"
    "s_memtime %[tc]\n\t"
    "v_lshrrev_b32 v56, 4, v60\n\t"
    "v_and_b32 v57, 15, v60\n\t"
    "v_mov_b32 v20, 0\n\t"
    "v_mov_b32 v21, 0\n\t"
    "v_mov_b32 v22, 0\n\t"
    "v_mov_b32 v23, 0\n\t"
    "v_mov_b32 v11, %[sn]\n\t"
    "v_mov_b32 v10, -1\n\t"
    "v_and_b32 v61, 2, v60\n\t"
    "v_mul_u32_u24 v61, 0x5555, v61\n\t"
    "v_or_b32 v8, %[c44], v61\n\t"
    "v_lshlrev_b32 v61, 2, v56\n\t"
    "v_lshl_or_b32 v9, v57, 4, v61\n\t"
    "s_setprio 0\n\t"
    "s_setprio 3\n\t"
    "3:\n\t"
    "v_mov_b32 v12, 0\n\t"
    "v_mov_b32 v13, 0\n\t"
    "v_mov_b32 v14, 0\n\t"
    "v_mov_b32 v15, 0\n\t"
    "v_mov_b32 v16, 0\n\t"
    "v_mov_b32 v17, 0\n\t"
    "v_mov_b32 v18, 0\n\t"
    "v_mov_b32 v19, 0\n\t"
    "s_waitcnt vmcnt(0) lgkmcnt(0)\n\t"
    "33:\n\t"
    "ds_write_b128 v6, v[24:27] offset:14336\n\t"
    "ds_write_b128 v6, v[28:31] offset:10240\n\t"
    "v_smfmac_f32_16x16x64_bf16 v[12:15], v[24:27], v[96:103], v8\n\t"
    "v_max3_u32 v60, v24, v25, v26\n\t"
    "v_max3_u32 v61, v27, v28, v29\n\t"
    "v_smfmac_f32_16x16x64_bf16 v[16:19], v[24:27], v[104:111], v8\n\t"
    "v_max3_u32 v60, v60, v30, v31\n\t"
    "v_max_u32 v60, v60, v61\n\t"
    "v_cmp_eq_u32 vcc, -1, v60\n\t"
    "s_cbranch_vccnz 1f\n\t"
    "2:\n\t"
    "s_waitcnt lgkmcnt(0)\n\t"
    "s_barrier\n\t"
    "ds_read_b128 v[32:35], v6 offset:12288\n\t"
    "ds_read_b128 v[36:39], v6 offset:13312\n\t"
    "ds_read_b128 v[40:43], v6 offset:15360\n\t"
    "ds_write_b32 v9, v10 offset:18944\n\t"
    "s_waitcnt lgkmcnt(3)\n\t"
    "v_smfmac_f32_16x16x64_bf16 v[12:15], v[32:35], v[64:71], v8\n\t"
    "v_smfmac_f32_16x16x64_bf16 v[16:19], v[32:35], v[72:79], v8\n\t"
    "s_waitcnt lgkmcnt(2)\n\t"
    "v_smfmac_f32_16x16x64_bf16 v[12:15], v[36:39], v[80:87], v8\n\t"
    "v_smfmac_f32_16x16x64_bf16 v[16:19], v[36:39], v[88:95], v8\n\t"
    "s_waitcnt lgkmcnt(1)\n\t"
    "v_smfmac_f32_16x16x64_bf16 v[12:15], v[40:43], v[112:119], v8\n\t"
    "v_smfmac_f32_16x16x64_bf16 v[16:19], v[40:43], v[120:127], v8\n\t"
    "s_setprio 0\n\t"
    "s_sleep 1\n\t"
    "s_nop 6\n\t"
    "v_add_f32 v12, v12, v14\n\t"
    "v_add_f32 v13, v13, v15\n\t"
    "v_add_f32 v14, v16, v18\n\t"
    "v_add_f32 v15, v17, v19\n\t"
    "ds_write_b128 v6, v[12:15] offset:26624\n\t"
    "ds_write_b32 v9, v11 offset:29184\n\t"
    "s_setprio 0\n\t"
    "ds_read_b128 v[44:47], v6 offset:0\n\t"
    "ds_read_b128 v[48:51], v6 offset:1024\n\t"
    "ds_read_b128 v[52:55], v6 offset:2048\n\t"
    "ds_read_b128 v[56:59], v6 offset:3072\n\t"
    "ds_read_b128 v[60:63], v6 offset:4096\n\t"
    "s_sleep 1\n\t"
    "v_smfmac_f32_16x16x64_bf16 v[20:23], v[28:31], v[200:207], v8\n\t"
    "ds_read_b128 v[28:31], v6 offset:5120\n\t"
    "v_smfmac_f32_16x16x64_bf16 v[20:23], v[24:27], v[232:239], v8\n\t"
    "ds_read_b128 v[24:27], v6 offset:6144\n\t"
    "v_smfmac_f32_16x16x64_bf16 v[20:23], v[32:35], v[224:231], v8\n\t"
    "ds_read_b128 v[32:35], v6 offset:7168\n\t"
    "v_smfmac_f32_16x16x64_bf16 v[20:23], v[36:39], v[240:247], v8\n\t"
    "ds_read_b128 v[36:39], v6 offset:8192\n\t"
    "v_smfmac_f32_16x16x64_bf16 v[20:23], v[40:43], v[248:255], v8\n\t"
    "ds_read_b128 v[40:43], v6 offset:9216\n\t"
    "s_waitcnt lgkmcnt(9)\n\t"
    "v_smfmac_f32_16x16x64_bf16 v[20:23], v[44:47], v[128:135], v8\n\t"
    "ds_read_b128 v[44:47], v6 offset:11264\n\t"
    "s_barrier\n\t"
    "s_setprio 0\n\t"
    "s_waitcnt lgkmcnt(9)\n\t"
    "v_smfmac_f32_16x16x64_bf16 v[20:23], v[48:51], v[144:151], v8\n\t"
    "s_waitcnt lgkmcnt(8)\n\t"
    "v_smfmac_f32_16x16x64_bf16 v[20:23], v[52:55], v[136:143], v8\n\t"
    "s_waitcnt lgkmcnt(7)\n\t"
    "v_smfmac_f32_16x16x64_bf16 v[20:23], v[56:59], v[152:159], v8\n\t"
    "s_waitcnt lgkmcnt(6)\n\t"
    "v_smfmac_f32_16x16x64_bf16 v[20:23], v[60:63], v[160:167], v8\n\t"
    "s_waitcnt lgkmcnt(5)\n\t"
    "v_smfmac_f32_16x16x64_bf16 v[20:23], v[28:31], v[176:183], v8\n\t"
    "s_waitcnt lgkmcnt(4)\n\t"
    "v_smfmac_f32_16x16x64_bf16 v[20:23], v[24:27], v[168:175], v8\n\t"
    "s_waitcnt lgkmcnt(3)\n\t"
    "v_smfmac_f32_16x16x64_bf16 v[20:23], v[32:35], v[184:191], v8\n\t"
    "s_waitcnt lgkmcnt(2)\n\t"
    "v_smfmac_f32_16x16x64_bf16 v[20:23], v[36:39], v[192:199], v8\n\t"
    "s_waitcnt lgkmcnt(1)\n\t"
    "v_smfmac_f32_16x16x64_bf16 v[20:23], v[40:43], v[208:215], v8\n\t"
    "s_waitcnt lgkmcnt(0)\n\t"
    "v_smfmac_f32_16x16x64_bf16 v[20:23], v[44:47], v[216:223], v8\n\t"
    "s_nop 7\n\t"
    "v_add_f32 v20, v20, v22\n\t"
    "v_add_f32 v21, v21, v23\n\t"
    "v_cvt_pk_bf16_f32 v20, v20, v21\n\t"
    "v_min_u32 v20, -2, v20\n\t"
    "ds_write_b32 v9, v20 offset:18944\n\t"
    "s_waitcnt lgkmcnt(0)\n\t"
    "s_setprio 0\n\t"
    "s_setprio 0\n\t"
    "s_waitcnt lgkmcnt(0)\n\t"
    "s_branch 9f\n\t"
    "1:\n\t"
    "s_add_u32 %[sp], %[sp], 1\n\t"
    "s_cmp_ge_u32 %[sp], %[cap]\n\t"
    "s_cbranch_scc1 7f\n\t"
    "global_load_dwordx4 v[24:27], v6, %[ag] sc1\n\t"
    "global_load_dwordx4 v[28:31], v6, %[ag] offset:-4096 sc1\n\t"
    "s_waitcnt lgkmcnt(0)\n\t"
    "s_memtime %[tc]\n\t"
    "s_nop 7\n\t"
    "s_branch 3b\n\t"
    "7:\n\t"
    "s_mov_b32 %[cap], 0\n\t"
    "s_branch 2b\n\t"
    "9:"
    : [tc] "=&s"(tc), [sp] "=&s"(sp), [cap] "+s"(cap), [st] "=&s"(st)
    : [ag] "s"(ag), [c44] "s"(0x4444u), [lsc] "s"(LSC), [sn] "s"(sn), "{v[64:95]}"(W.w0), "{v[96:127]}"(W.w1), "{v[128:159]}"(W.w2), "{v[160:191]}"(W.w3), "{v[192:223]}"(W.w4), "{v[224:255]}"(W.w5)
    : "v6", "v7", "v8", "v9", "v10", "v11", "v12", "v13", "v14", "v15", "v16", "v17", "v18", "v19", "v20", "v21", "v22", "v23", "v24", "v25", "v26", "v27", "v28", "v29", "v30", "v31", "v32", "v33", "v34", "v35", "v36", "v37", "v38", "v39", "v40", "v41", "v42", "v43", "v44", "v45", "v46", "v47", "v48", "v49", "v50", "v51", "v52", "v53", "v54", "v55", "v56", "v57", "v58", "v59", "v60", "v61", "v62", "v63", "vcc", "scc", "m0", "memory");
}
template <> __device__ __forceinline__ void qk_step<7>(const QKW& W, const char* ag, const char* vs, const char* v2, const char* db, const char* od, unsigned pb, unsigned sn, unsigned& cap, unsigned& sp,
    unsigned long long& tc, unsigned long long* ts, unsigned& d0, unsigned& d1) {
  unsigned st, bg;
  asm volatile("v_mbcnt_lo_u32_b32 v60, -1, 0\n\t"
    "v_mbcnt_hi_u32_b32 v60, -1, v60\n\t"
    "v_lshlrev_b32 v6, 4, v60\n\t"
    "s_mov_b32 %[sp], 0\n\t"
    "s_setprio 3\n\t"
    "global_load_dwordx4 v[24:27], v6, %[ag] sc1\n\t"
    "global_load_dwordx4 v[28:31], v6, %[ag] offset:-4096 sc1\n\t"
    "s_memtime %[tc]\n\t"
    "v_lshrrev_b32 v56, 4, v60\n\t"
    "v_and_b32 v57, 15, v60\n\t"
    "v_mov_b32 v20, 0\n\t"
    "v_mov_b32 v21, 0\n\t"
    "v_mov_b32 v22, 0\n\t"
    "v_mov_b32 v23, 0\n\t"
    "v_mov_b32 v11, %[sn]\n\t"
    "v_mov_b32 v10, -1\n\t"
    "v_and_b32 v61, 2, v60\n\t"
    "v_mul_u32_u24 v61, 0x5555, v61\n\t"
    "v_or_b32 v8, %[c44], v61\n\t"
    "v_lshlrev_b32 v61, 2, v56\n\t"
    "v_lshl_or_b32 v9, v57, 4, v61\n\t"
    "s_setprio 0\n\t"
    "s_setprio 3\n\t"
    "3:\n\t"
    "v_mov_b32 v12, 0\n\t"
    "v_mov_b32 v13, 0\n\t"
    "v_mov_b32 v14, 0\n\t"
    "v_mov_b32 v15, 0\n\t"
    "v_mov_b32 v16, 0\n\t"
    "v_mov_b32 v17, 0\n\t"
    "v_mov_b32 v18, 0\n\t"
    "v_mov_b32 v19, 0\n\t"
    "s_waitcnt vmcnt(0) lgkmcnt(0)\n\t"
    "33:\n\t"
    "ds_write_b128 v6, v[24:27] offset:15360\n\t"
    "ds_write_b128 v6, v[28:31] offset:11264\n\t"
    "v_smfmac_f32_16x16x64_bf16 v[12:15], v[24:27], v[112:119], v8\n\t"
    "v_max3_u32 v60, v24, v25, v26\n\t"
    "v_max3_u32 v61, v27, v28, v29\n\t"
    "v_smfmac_f32_16x16x64_bf16 v[16:19], v[24:27], v[120:127], v8\n\t"
    "v_max3_u32 v60, v60, v30, v31\n\t"
    "v_max_u32 v60, v60, v61\n\t"
    "v_cmp_eq_u32 vcc, -1, v60\n\t"
    "s_cbranch_vccnz 1f\n\t"
    "2:\n\t"
    "s_waitcnt lgkmcnt(0)\n\t"
    "s_barrier\n\t"
    "ds_read_b128 v[32:35], v6 offset:12288\n\t"
    "ds_read_b128 v[36:39], v6 offset:13312\n\t"
    "ds_read_b128 v[40:43], v6 offset:14336\n\t"
    "ds_write_b32 v9, v10 offset:19200\n\t"
    "s_waitcnt lgkmcnt(3)\n\t"
    "v_smfmac_f32_16x16x64_bf16 v[12:15], v[32:35], v[64:71], v8\n\t"
    "v_smfmac_f32_16x16x64_bf16 v[16:19], v[32:35], v[72:79], v8\n\t"
    "s_waitcnt lgkmcnt(2)\n\t"
    "v_smfmac_f32_16x16x64_bf16 v[12:15], v[36:39], v[80:87], v8\n\t"
    "v_smfmac_f32_16x16x64_bf16 v[16:19], v[36:39], v[88:95], v8\n\t"
    "s_waitcnt lgkmcnt(1)\n\t"
    "v_smfmac_f32_16x16x64_bf16 v[12:15], v[40:43], v[96:103], v8\n\t"
    "v_smfmac_f32_16x16x64_bf16 v[16:19], v[40:43], v[104:111], v8\n\t"
    "s_setprio 0\n\t"
    "s_sleep 1\n\t"
    "s_nop 6\n\t"
    "v_add_f32 v12, v12, v14\n\t"
    "v_add_f32 v13, v13, v15\n\t"
    "v_add_f32 v14, v16, v18\n\t"
    "v_add_f32 v15, v17, v19\n\t"
    "ds_write_b128 v6, v[12:15] offset:27648\n\t"
    "ds_write_b32 v9, v11 offset:29440\n\t"
    "s_setprio 0\n\t"
    "v_lshrrev_b32 v10, 24, v11\n\t"
    "v_add_u32 v10, -1, v10\n\t"
    "s_mov_b64 exec, 1\n\t"
    "global_store_dword v6, v10, %[vs] sc1\n\t"
    "s_mov_b64 exec, -1\n\t"
    "ds_read_b128 v[44:47], v6 offset:0\n\t"
    "ds_read_b128 v[48:51], v6 offset:1024\n\t"
    "ds_read_b128 v[52:55], v6 offset:2048\n\t"
    "ds_read_b128 v[56:59], v6 offset:3072\n\t"
    "ds_read_b128 v[60:63], v6 offset:4096\n\t"
    "s_sleep 1\n\t"
    "v_smfmac_f32_16x16x64_bf16 v[20:23], v[28:31], v[216:223], v8\n\t"
    "ds_read_b128 v[28:31], v6 offset:5120\n\t"
    "v_smfmac_f32_16x16x64_bf16 v[20:23], v[24:27], v[248:255], v8\n\t"
    "ds_read_b128 v[24:27], v6 offset:6144\n\t"
    "v_smfmac_f32_16x16x64_bf16 v[20:23], v[32:35], v[224:231], v8\n\t"
    "ds_read_b128 v[32:35], v6 offset:7168\n\t"
    "v_smfmac_f32_16x16x64_bf16 v[20:23], v[36:39], v[240:247], v8\n\t"
    "ds_read_b128 v[36:39], v6 offset:8192\n\t"
    "v_smfmac_f32_16x16x64_bf16 v[20:23], v[40:43], v[232:239], v8\n\t"
    "ds_read_b128 v[40:43], v6 offset:9216\n\t"
    "s_waitcnt lgkmcnt(9)\n\t"
    "v_smfmac_f32_16x16x64_bf16 v[20:23], v[44:47], v[128:135], v8\n\t"
    "ds_read_b128 v[44:47], v6 offset:10240\n\t"
    "s_barrier\n\t"
    "s_setprio 0\n\t"
    "s_waitcnt lgkmcnt(9)\n\t"
    "v_smfmac_f32_16x16x64_bf16 v[20:23], v[48:51], v[144:151], v8\n\t"
    "s_waitcnt lgkmcnt(8)\n\t"
    "v_smfmac_f32_16x16x64_bf16 v[20:23], v[52:55], v[136:143], v8\n\t"
    "s_waitcnt lgkmcnt(7)\n\t"
    "v_smfmac_f32_16x16x64_bf16 v[20:23], v[56:59], v[152:159], v8\n\t"
    "s_waitcnt lgkmcnt(6)\n\t"
    "v_smfmac_f32_16x16x64_bf16 v[20:23], v[60:63], v[160:167], v8\n\t"
    "s_waitcnt lgkmcnt(5)\n\t"
    "v_smfmac_f32_16x16x64_bf16 v[20:23], v[28:31], v[176:183], v8\n\t"
    "s_waitcnt lgkmcnt(4)\n\t"
    "v_smfmac_f32_16x16x64_bf16 v[20:23], v[24:27], v[168:175], v8\n\t"
    "s_waitcnt lgkmcnt(3)\n\t"
    "v_smfmac_f32_16x16x64_bf16 v[20:23], v[32:35], v[184:191], v8\n\t"
    "s_waitcnt lgkmcnt(2)\n\t"
    "v_smfmac_f32_16x16x64_bf16 v[20:23], v[36:39], v[192:199], v8\n\t"
    "s_waitcnt lgkmcnt(1)\n\t"
    "v_smfmac_f32_16x16x64_bf16 v[20:23], v[40:43], v[208:215], v8\n\t"
    "s_waitcnt lgkmcnt(0)\n\t"
    "v_smfmac_f32_16x16x64_bf16 v[20:23], v[44:47], v[200:207], v8\n\t"
    "s_nop 7\n\t"
    "v_add_f32 v20, v20, v22\n\t"
    "v_add_f32 v21, v21, v23\n\t"
    "v_cvt_pk_bf16_f32 v20, v20, v21\n\t"
    "v_min_u32 v20, -2, v20\n\t"
    "ds_write_b32 v9, v20 offset:19200\n\t"
    "s_waitcnt lgkmcnt(0)\n\t"
    "s_setprio 0\n\t"
    "s_waitcnt lgkmcnt(0)\n\t"
    "s_branch 9f\n\t"
    "1:\n\t"
    "s_add_u32 %[sp], %[sp], 1\n\t"
    "s_cmp_ge_u32 %[sp], %[cap]\n\t"
    "s_cbranch_scc1 7f\n\t"
    "global_load_dwordx4 v[24:27], v6, %[ag] sc1\n\t"
    "global_load_dwordx4 v[28:31], v6, %[ag] offset:-4096 sc1\n\t"
    "s_waitcnt lgkmcnt(0)\n\t"
    "s_memtime %[tc]\n\t"
    "s_nop 7\n\t"
    "s_branch 3b\n\t"
    "7:\n\t"
    "s_mov_b32 %[cap], 0\n\t"
    "s_branch 2b\n\t"
    "9:"
    : [tc] "=&s"(tc), [sp] "=&s"(sp), [cap] "+s"(cap), [st] "=&s"(st)
    : [ag] "s"(ag), [c44] "s"(0x4444u), [lsc] "s"(LSC), [sn] "s"(sn), [vs] "s"(vs), "{v[64:95]}"(W.w0), "{v[96:127]}"(W.w1), "{v[128:159]}"(W.w2), "{v[160:191]}"(W.w3), "{v[192:223]}"(W.w4), "{v[224:255]}"(W.w5)
    : "v6", "v7", "v8", "v9", "v10", "v11", "v12", "v13", "v14", "v15", "v16", "v17", "v18", "v19", "v20", "v21", "v22", "v23", "v24", "v25", "v26", "v27", "v28", "v29", "v30", "v31", "v32", "v33", "v34", "v35", "v36", "v37", "v38", "v39", "v40", "v41", "v42", "v43", "v44", "v45", "v46", "v47", "v48", "v49", "v50", "v51", "v52", "v53", "v54", "v55", "v56", "v57", "v58", "v59", "v60", "v61", "v62", "v63", "vcc", "scc", "m0", "memory");
}
template <int W_> __device__ __forceinline__ void seg_attn(const QKW& W, unsigned pb, const char* od, unsigned long long* ts) { }
template <> __device__ __forceinline__ void seg_attn<0>(const QKW& W, unsigned pb, const char* od, unsigned long long* ts) {
  asm volatile("v_mbcnt_lo_u32_b32 v32, -1, 0\n\t"
    "v_mbcnt_hi_u32_b32 v32, -1, v32\n\t"
    "v_lshlrev_b32 v35, 4, v32\n\t"
    "ds_read_b128 v[24:27], v35 offset:20480\n\t"
    "ds_read_b128 v[28:31], v35 offset:21504\n\t"
    "v_lshrrev_b32 v33, 4, v32\n\t"
    "v_and_b32 v34, 15, v32\n\t"
    "v_lshrrev_b32 v4, 2, v34\n\t"
    "v_and_b32 v4, 2, v4\n\t"
    "v_xor_b32 v4, v33, v4\n\t"
    "v_lshl_or_b32 v4, v4, 4, %[pb]\n\t"
    "v_lshl_or_b32 v4, v34, 6, v4\n\t"
    "ds_read_b128 v[8:11], v4 offset:49152\n\t"
    "ds_read_b128 v[12:15], v4 offset:51200\n\t"
    "ds_read_b128 v[16:19], v4 offset:53248\n\t"
    "ds_read_b128 v[20:23], v4 offset:55296\n\t"
    "ds_read_b128 v[40:43], v4 offset:32768\n\t"
    "ds_read_b128 v[44:47], v4 offset:34816\n\t"
    "ds_read_b128 v[48:51], v4 offset:36864\n\t"
    "ds_read_b128 v[52:55], v4 offset:38912\n\t"
    "v_lshlrev_b32 v35, 2, v33\n\t"
    "v_sub_u32 v35, v34, v35\n\t"
    "v_ashrrev_i32 v60, 31, v35\n\t"
    "v_and_b32 v60, %[neg], v60\n\t"
    "v_add_u32 v61, -1, v35\n\t"
    "v_ashrrev_i32 v61, 31, v61\n\t"
    "v_and_b32 v61, %[neg], v61\n\t"
    "v_add_u32 v62, -2, v35\n\t"
    "v_ashrrev_i32 v62, 31, v62\n\t"
    "v_and_b32 v62, %[neg], v62\n\t"
    "v_add_u32 v63, -3, v35\n\t"
    "v_ashrrev_i32 v63, 31, v63\n\t"
    "v_and_b32 v63, %[neg], v63\n\t"
    "s_waitcnt lgkmcnt(6)\n\t"
    "v_mfma_f32_16x16x32_bf16 v[32:35], v[24:27], v[8:11], 0\n\t"
    "v_mfma_f32_16x16x32_bf16 v[36:39], v[24:27], v[12:15], 0\n\t"
    "s_waitcnt lgkmcnt(4)\n\t"
    "v_mfma_f32_16x16x32_bf16 v[32:35], v[28:31], v[16:19], v[32:35]\n\t"
    "v_mfma_f32_16x16x32_bf16 v[36:39], v[28:31], v[20:23], v[36:39]\n\t"
    "s_waitcnt lgkmcnt(3)\n\t"
    "v_mfma_f32_16x16x32_bf16 v[60:63], v[40:43], v[8:11], v[60:63]\n\t"
    "s_waitcnt lgkmcnt(2)\n\t"
    "v_mfma_f32_16x16x32_bf16 v[60:63], v[44:47], v[12:15], v[60:63]\n\t"
    "v_mbcnt_lo_u32_b32 v7, -1, 0\n\t"
    "v_mbcnt_hi_u32_b32 v7, -1, v7\n\t"
    "s_waitcnt lgkmcnt(1)\n\t"
    "v_mfma_f32_16x16x32_bf16 v[60:63], v[48:51], v[16:19], v[60:63]\n\t"
    "v_lshrrev_b32 v5, 4, v7\n\t"
    "v_and_b32 v5, 2, v5\n\t"
    "s_waitcnt lgkmcnt(0)\n\t"
    "v_mfma_f32_16x16x32_bf16 v[60:63], v[52:55], v[20:23], v[60:63]\n\t"
    "v_xor_b32 v5, v5, v7\n\t"
    "v_lshl_or_b32 v5, v5, 4, %[pb]\n\t"
    "ds_read_b64_tr_b16 v[48:49], v5 offset:40960\n\t"
    "ds_read_b64_tr_b16 v[52:53], v5 offset:40968\n\t"
    "v_add_f32 v32, v32, v38\n\t"
    "v_add_f32 v33, v33, v39\n\t"
    "v_and_b32 v6, 12, v7\n\t"
    "v_and_b32 v4, 3, v7\n\t"
    "v_lshl_add_u32 v4, v6, 1, v4\n\t"
    "v_lshrrev_b32 v6, 4, v7\n\t"
    "v_lshl_add_u32 v4, v4, 2, v6\n\t"
    "v_lshlrev_b32 v4, 2, v4\n\t"
    "v_add_u32 v4, 18432, v4\n\t"
    "ds_read_b64_tr_b16 v[40:41], v5 offset:43008\n\t"
    "ds_read_b64_tr_b16 v[44:45], v5 offset:43016\n\t"
    "ds_read2_b32 v[36:37], v4 offset0:0 offset1:16\n\t"
    "ds_read2_b32 v[38:39], v4 offset0:128 offset1:144\n\t"
    "v_and_b32 v4, 7, v7\n\t"
    "v_xor_b32 v6, v6, v4\n\t"
    "v_max3_f32 v34, v32, v33, %[lo]\n\t"
    "v_max3_f32 v34, v34, v60, v61\n\t"
    "v_max3_f32 v34, v34, v62, v63\n\t"
    "v_mov_b32 v35, v34\n\t"
    "v_lshlrev_b32 v6, 4, v6\n\t"
    "v_and_b32 v4, 15, v7\n\t"
    "v_permlane16_swap_b32 v34, v35\n\t"
    "v_max_f32 v34, v34, v35\n\t"
    "v_mov_b32 v35, v34\n\t"
    "v_lshl_or_b32 v6, v4, 7, v6\n\t"
    "v_xor_b32 v4, 64, v6\n\t"
    "v_permlane32_swap_b32 v34, v35\n\t"
    "v_max_f32 v34, v34, v35\n\t"
    "v_mul_f32 v34, %[nl], v34\n\t"
    "v_fma_f32 v32, v32, %[lsc], v34\n\t"
    "v_fma_f32 v33, v33, %[lsc], v34\n\t"
    "v_fma_f32 v60, v60, %[lsc], v34\n\t"
    "v_fma_f32 v61, v61, %[lsc], v34\n\t"
    "v_fma_f32 v62, v62, %[lsc], v34\n\t"
    "v_fma_f32 v63, v63, %[lsc], v34\n\t"
    "v_exp_f32 v60, v60\n\t"
    "v_exp_f32 v61, v61\n\t"
    "v_exp_f32 v62, v62\n\t"
    "v_exp_f32 v63, v63\n\t"
    "v_exp_f32 v32, v32\n\t"
    "v_exp_f32 v33, v33\n\t"
    "v_cvt_pk_bf16_f32 v24, v60, v61\n\t"
    "v_cvt_pk_bf16_f32 v25, v62, v63\n\t"
    "v_cvt_pk_bf16_f32 v28, v32, v33\n\t"
    "s_waitcnt lgkmcnt(0)\n\t"
    "v_mfma_f32_16x16x16_bf16 v[8:11], v[48:49], v[24:25], 0\n\t"
    "v_mov_b32 v56, %[one]\n\t"
    "v_mov_b32 v57, %[one]\n\t"
    "v_mfma_f32_16x16x16_bf16 v[12:15], v[52:53], v[24:25], 0\n\t"
    "v_mov_b32 v58, %[one]\n\t"
    "v_mov_b32 v59, %[one]\n\t"
    "v_mfma_f32_16x16x16_bf16 v[16:19], v[40:41], v[24:25], 0\n\t"
    "v_mov_b32 v29, 0\n\t"
    "v_mov_b32 v30, 0\n\t"
    "v_mfma_f32_16x16x16_bf16 v[60:63], v[56:57], v[24:25], 0\n\t"
    "v_mov_b32 v31, v28\n\t"
    "v_mfma_f32_16x16x16_bf16 v[20:23], v[44:45], v[24:25], 0\n\t"
    "v_mfma_f32_16x16x16_bf16 v[8:11], v[36:37], v[28:29], v[8:11]\n\t"
    "v_mfma_f32_16x16x16_bf16 v[12:15], v[36:37], v[30:31], v[12:15]\n\t"
    "s_nop 1\n\t"
    "v_mfma_f32_16x16x16_bf16 v[16:19], v[38:39], v[28:29], v[16:19]\n\t"
    "v_mfma_f32_16x16x16_bf16 v[60:63], v[56:57], v[28:29], v[60:63]\n\t"
    "s_nop 1\n\t"
    "v_mfma_f32_16x16x16_bf16 v[20:23], v[38:39], v[30:31], v[20:23]\n\t"
    "s_nop 4\n\t"
    "v_rcp_f32 v60, v60\n\t"
    "s_nop 0\n\t"
    "v_pk_mul_f32 v[8:9], v[8:9], v[60:61] op_sel_hi:[1,0]\n\t"
    "v_pk_mul_f32 v[10:11], v[10:11], v[60:61] op_sel_hi:[1,0]\n\t"
    "v_pk_mul_f32 v[12:13], v[12:13], v[60:61] op_sel_hi:[1,0]\n\t"
    "v_pk_mul_f32 v[14:15], v[14:15], v[60:61] op_sel_hi:[1,0]\n\t"
    "v_cvt_pk_bf16_f32 v24, v8, v9\n\t"
    "v_cvt_pk_bf16_f32 v25, v10, v11\n\t"
    "v_cvt_pk_bf16_f32 v26, v12, v13\n\t"
    "v_cvt_pk_bf16_f32 v27, v14, v15\n\t"
    "global_store_dwordx4 v6, v[24:27], %[od] offset:0 sc1\n\t"
    "v_pk_mul_f32 v[16:17], v[16:17], v[60:61] op_sel_hi:[1,0]\n\t"
    "v_pk_mul_f32 v[18:19], v[18:19], v[60:61] op_sel_hi:[1,0]\n\t"
    "v_pk_mul_f32 v[20:21], v[20:21], v[60:61] op_sel_hi:[1,0]\n\t"
    "v_pk_mul_f32 v[22:23], v[22:23], v[60:61] op_sel_hi:[1,0]\n\t"
    "v_cvt_pk_bf16_f32 v28, v16, v17\n\t"
    "v_cvt_pk_bf16_f32 v29, v18, v19\n\t"
    "v_cvt_pk_bf16_f32 v30, v20, v21\n\t"
    "v_cvt_pk_bf16_f32 v31, v22, v23\n\t"
    "global_store_dwordx4 v4, v[28:31], %[od] offset:0 sc1\n\t"
    "s_waitcnt lgkmcnt(0)\n\t"
    "s_nop 0"
    :
    : [pb] "s"(pb), [od] "s"(od), [neg] "s"(-3.0e38f), [lo] "s"(-1.0e38f), [one] "s"(0x3F803F80u), [lsc] "s"(LSC), [nl] "s"(-LSC), "{v[64:95]}"(W.w0), "{v[96:127]}"(W.w1), "{v[128:159]}"(W.w2), "{v[160:191]}"(W.w3), "{v[192:223]}"(W.w4), "{v[224:255]}"(W.w5)
    : "v4", "v5", "v6", "v7", "v8", "v9", "v10", "v11", "v12", "v13", "v14", "v15", "v16", "v17", "v18", "v19", "v20", "v21", "v22", "v23", "v24", "v25", "v26", "v27", "v28", "v29", "v30", "v31", "v32", "v33", "v34", "v35", "v36", "v37", "v38", "v39", "v40", "v41", "v42", "v43", "v44", "v45", "v46", "v47", "v48", "v49", "v50", "v51", "v52", "v53", "v54", "v55", "v56", "v57", "v58", "v59", "v60", "v61", "v62", "v63", "memory");
}
template <> __device__ __forceinline__ void seg_attn<1>(const QKW& W, unsigned pb, const char* od, unsigned long long* ts) {
  asm volatile("v_mbcnt_lo_u32_b32 v32, -1, 0\n\t"
    "v_mbcnt_hi_u32_b32 v32, -1, v32\n\t"
    "v_lshlrev_b32 v35, 4, v32\n\t"
    "ds_read_b128 v[24:27], v35 offset:20480\n\t"
    "ds_read_b128 v[28:31], v35 offset:21504\n\t"
    "v_lshrrev_b32 v33, 4, v32\n\t"
    "v_and_b32 v34, 15, v32\n\t"
    "v_lshrrev_b32 v4, 2, v34\n\t"
    "v_and_b32 v4, 2, v4\n\t"
    "v_xor_b32 v4, v33, v4\n\t"
    "v_lshl_or_b32 v4, v4, 4, %[pb]\n\t"
    "v_lshl_or_b32 v4, v34, 6, v4\n\t"
    "ds_read_b128 v[8:11], v4 offset:50176\n\t"
    "ds_read_b128 v[12:15], v4 offset:52224\n\t"
    "ds_read_b128 v[16:19], v4 offset:54272\n\t"
    "ds_read_b128 v[20:23], v4 offset:56320\n\t"
    "ds_read_b128 v[40:43], v4 offset:32768\n\t"
    "ds_read_b128 v[44:47], v4 offset:34816\n\t"
    "ds_read_b128 v[48:51], v4 offset:36864\n\t"
    "ds_read_b128 v[52:55], v4 offset:38912\n\t"
    "v_lshlrev_b32 v35, 2, v33\n\t"
    "v_sub_u32 v35, v34, v35\n\t"
    "v_ashrrev_i32 v60, 31, v35\n\t"
    "v_and_b32 v60, %[neg], v60\n\t"
    "v_add_u32 v61, -1, v35\n\t"
    "v_ashrrev_i32 v61, 31, v61\n\t"
    "v_and_b32 v61, %[neg], v61\n\t"
    "v_add_u32 v62, -2, v35\n\t"
    "v_ashrrev_i32 v62, 31, v62\n\t"
    "v_and_b32 v62, %[neg], v62\n\t"
    "v_add_u32 v63, -3, v35\n\t"
    "v_ashrrev_i32 v63, 31, v63\n\t"
    "v_and_b32 v63, %[neg], v63\n\t"
    "s_waitcnt lgkmcnt(6)\n\t"
    "v_mfma_f32_16x16x32_bf16 v[32:35], v[24:27], v[8:11], 0\n\t"
    "v_mfma_f32_16x16x32_bf16 v[36:39], v[24:27], v[12:15], 0\n\t"
    "s_waitcnt lgkmcnt(4)\n\t"
    "v_mfma_f32_16x16x32_bf16 v[32:35], v[28:31], v[16:19], v[32:35]\n\t"
    "v_mfma_f32_16x16x32_bf16 v[36:39], v[28:31], v[20:23], v[36:39]\n\t"
    "ds_read_b128 v[24:27], v4 offset:33792\n\t"
    "ds_read_b128 v[28:31], v4 offset:35840\n\t"
    "s_waitcnt lgkmcnt(5)\n\t"
    "v_mfma_f32_16x16x32_bf16 v[56:59], v[40:43], v[8:11], 0\n\t"
    "s_waitcnt lgkmcnt(4)\n\t"
    "v_mfma_f32_16x16x32_bf16 v[56:59], v[44:47], v[12:15], v[56:59]\n\t"
    "ds_read_b128 v[40:43], v4 offset:37888\n\t"
    "ds_read_b128 v[44:47], v4 offset:39936\n\t"
    "v_mbcnt_lo_u32_b32 v7, -1, 0\n\t"
    "v_mbcnt_hi_u32_b32 v7, -1, v7\n\t"
    "s_waitcnt lgkmcnt(5)\n\t"
    "v_mfma_f32_16x16x32_bf16 v[56:59], v[48:51], v[16:19], v[56:59]\n\t"
    "v_lshrrev_b32 v5, 4, v7\n\t"
    "v_and_b32 v5, 2, v5\n\t"
    "s_waitcnt lgkmcnt(4)\n\t"
    "v_mfma_f32_16x16x32_bf16 v[56:59], v[52:55], v[20:23], v[56:59]\n\t"
    "v_xor_b32 v5, v5, v7\n\t"
    "v_lshl_or_b32 v5, v5, 4, %[pb]\n\t"
    "ds_read_b64_tr_b16 v[48:49], v5 offset:40960\n\t"
    "ds_read_b64_tr_b16 v[50:51], v5 offset:41984\n\t"
    "ds_read_b64_tr_b16 v[52:53], v5 offset:40968\n\t"
    "ds_read_b64_tr_b16 v[54:55], v5 offset:41992\n\t"
    "v_add_f32 v32, v32, v38\n\t"
    "v_add_f32 v33, v33, v39\n\t"
    "s_waitcnt lgkmcnt(7)\n\t"
    "v_mfma_f32_16x16x32_bf16 v[60:63], v[24:27], v[8:11], v[60:63]\n\t"
    "v_and_b32 v6, 12, v7\n\t"
    "v_and_b32 v4, 3, v7\n\t"
    "s_waitcnt lgkmcnt(6)\n\t"
    "v_mfma_f32_16x16x32_bf16 v[60:63], v[28:31], v[12:15], v[60:63]\n\t"
    "v_lshl_add_u32 v4, v6, 1, v4\n\t"
    "v_lshrrev_b32 v6, 4, v7\n\t"
    "s_waitcnt lgkmcnt(5)\n\t"
    "v_mfma_f32_16x16x32_bf16 v[60:63], v[40:43], v[16:19], v[60:63]\n\t"
    "v_lshl_add_u32 v4, v4, 2, v6\n\t"
    "v_lshlrev_b32 v4, 2, v4\n\t"
    "s_waitcnt lgkmcnt(4)\n\t"
    "v_mfma_f32_16x16x32_bf16 v[60:63], v[44:47], v[20:23], v[60:63]\n\t"
    "v_add_u32 v4, 18432, v4\n\t"
    "ds_read_b64_tr_b16 v[40:41], v5 offset:43008\n\t"
    "ds_read_b64_tr_b16 v[42:43], v5 offset:44032\n\t"
    "ds_read_b64_tr_b16 v[44:45], v5 offset:43016\n\t"
    "ds_read_b64_tr_b16 v[46:47], v5 offset:44040\n\t"
    "ds_read2_b32 v[36:37], v4 offset0:0 offset1:16\n\t"
    "ds_read2_b32 v[38:39], v4 offset0:128 offset1:144\n\t"
    "v_and_b32 v4, 7, v7\n\t"
    "v_xor_b32 v6, v6, v4\n\t"
    "v_max3_f32 v34, v32, v33, %[lo]\n\t"
    "v_max3_f32 v34, v34, v56, v57\n\t"
    "v_max3_f32 v34, v34, v58, v59\n\t"
    "v_max3_f32 v34, v34, v60, v61\n\t"
    "v_max3_f32 v34, v34, v62, v63\n\t"
    "v_mov_b32 v35, v34\n\t"
    "v_lshlrev_b32 v6, 4, v6\n\t"
    "v_and_b32 v4, 15, v7\n\t"
    "v_permlane16_swap_b32 v34, v35\n\t"
    "v_max_f32 v34, v34, v35\n\t"
    "v_mov_b32 v35, v34\n\t"
    "v_lshl_or_b32 v6, v4, 7, v6\n\t"
    "v_xor_b32 v4, 64, v6\n\t"
    "v_permlane32_swap_b32 v34, v35\n\t"
    "v_max_f32 v34, v34, v35\n\t"
    "v_mul_f32 v34, %[nl], v34\n\t"
    "v_fma_f32 v32, v32, %[lsc], v34\n\t"
    "v_fma_f32 v33, v33, %[lsc], v34\n\t"
    "v_fma_f32 v56, v56, %[lsc], v34\n\t"
    "v_fma_f32 v57, v57, %[lsc], v34\n\t"
    "v_fma_f32 v58, v58, %[lsc], v34\n\t"
    "v_fma_f32 v59, v59, %[lsc], v34\n\t"
    "v_fma_f32 v60, v60, %[lsc], v34\n\t"
    "v_fma_f32 v61, v61, %[lsc], v34\n\t"
    "v_fma_f32 v62, v62, %[lsc], v34\n\t"
    "v_fma_f32 v63, v63, %[lsc], v34\n\t"
    "v_exp_f32 v56, v56\n\t"
    "v_exp_f32 v57, v57\n\t"
    "v_exp_f32 v58, v58\n\t"
    "v_exp_f32 v59, v59\n\t"
    "v_exp_f32 v60, v60\n\t"
    "v_exp_f32 v61, v61\n\t"
    "v_exp_f32 v62, v62\n\t"
    "v_exp_f32 v63, v63\n\t"
    "v_exp_f32 v32, v32\n\t"
    "v_exp_f32 v33, v33\n\t"
    "v_cvt_pk_bf16_f32 v24, v56, v57\n\t"
    "v_cvt_pk_bf16_f32 v25, v58, v59\n\t"
    "v_cvt_pk_bf16_f32 v26, v60, v61\n\t"
    "v_cvt_pk_bf16_f32 v27, v62, v63\n\t"
    "v_cvt_pk_bf16_f32 v28, v32, v33\n\t"
    "s_waitcnt lgkmcnt(0)\n\t"
    "v_mfma_f32_16x16x32_bf16 v[8:11], v[48:51], v[24:27], 0\n\t"
    "v_mov_b32 v56, %[one]\n\t"
    "v_mov_b32 v57, %[one]\n\t"
    "v_mfma_f32_16x16x32_bf16 v[12:15], v[52:55], v[24:27], 0\n\t"
    "v_mov_b32 v58, %[one]\n\t"
    "v_mov_b32 v59, %[one]\n\t"
    "v_mfma_f32_16x16x32_bf16 v[16:19], v[40:43], v[24:27], 0\n\t"
    "v_mov_b32 v29, 0\n\t"
    "v_mov_b32 v30, 0\n\t"
    "v_mfma_f32_16x16x32_bf16 v[60:63], v[56:59], v[24:27], 0\n\t"
    "v_mov_b32 v31, v28\n\t"
    "v_mfma_f32_16x16x32_bf16 v[20:23], v[44:47], v[24:27], 0\n\t"
    "v_mfma_f32_16x16x16_bf16 v[8:11], v[36:37], v[28:29], v[8:11]\n\t"
    "v_mfma_f32_16x16x16_bf16 v[12:15], v[36:37], v[30:31], v[12:15]\n\t"
    "s_nop 1\n\t"
    "v_mfma_f32_16x16x16_bf16 v[16:19], v[38:39], v[28:29], v[16:19]\n\t"
    "v_mfma_f32_16x16x16_bf16 v[60:63], v[56:57], v[28:29], v[60:63]\n\t"
    "s_nop 1\n\t"
    "v_mfma_f32_16x16x16_bf16 v[20:23], v[38:39], v[30:31], v[20:23]\n\t"
    "s_nop 4\n\t"
    "v_rcp_f32 v60, v60\n\t"
    "s_nop 0\n\t"
    "v_pk_mul_f32 v[8:9], v[8:9], v[60:61] op_sel_hi:[1,0]\n\t"
    "v_pk_mul_f32 v[10:11], v[10:11], v[60:61] op_sel_hi:[1,0]\n\t"
    "v_pk_mul_f32 v[12:13], v[12:13], v[60:61] op_sel_hi:[1,0]\n\t"
    "v_pk_mul_f32 v[14:15], v[14:15], v[60:61] op_sel_hi:[1,0]\n\t"
    "v_cvt_pk_bf16_f32 v24, v8, v9\n\t"
    "v_cvt_pk_bf16_f32 v25, v10, v11\n\t"
    "v_cvt_pk_bf16_f32 v26, v12, v13\n\t"
    "v_cvt_pk_bf16_f32 v27, v14, v15\n\t"
    "global_store_dwordx4 v6, v[24:27], %[od] offset:2048 sc1\n\t"
    "v_pk_mul_f32 v[16:17], v[16:17], v[60:61] op_sel_hi:[1,0]\n\t"
    "v_pk_mul_f32 v[18:19], v[18:19], v[60:61] op_sel_hi:[1,0]\n\t"
    "v_pk_mul_f32 v[20:21], v[20:21], v[60:61] op_sel_hi:[1,0]\n\t"
    "v_pk_mul_f32 v[22:23], v[22:23], v[60:61] op_sel_hi:[1,0]\n\t"
    "v_cvt_pk_bf16_f32 v28, v16, v17\n\t"
    "v_cvt_pk_bf16_f32 v29, v18, v19\n\t"
    "v_cvt_pk_bf16_f32 v30, v20, v21\n\t"
    "v_cvt_pk_bf16_f32 v31, v22, v23\n\t"
    "global_store_dwordx4 v4, v[28:31], %[od] offset:2048 sc1\n\t"
    "s_waitcnt lgkmcnt(0)\n\t"
    "s_nop 0"
    :
    : [pb] "s"(pb), [od] "s"(od), [neg] "s"(-3.0e38f), [lo] "s"(-1.0e38f), [one] "s"(0x3F803F80u), [lsc] "s"(LSC), [nl] "s"(-LSC), "{v[64:95]}"(W.w0), "{v[96:127]}"(W.w1), "{v[128:159]}"(W.w2), "{v[160:191]}"(W.w3), "{v[192:223]}"(W.w4), "{v[224:255]}"(W.w5)
    : "v4", "v5", "v6", "v7", "v8", "v9", "v10", "v11", "v12", "v13", "v14", "v15", "v16", "v17", "v18", "v19", "v20", "v21", "v22", "v23", "v24", "v25", "v26", "v27", "v28", "v29", "v30", "v31", "v32", "v33", "v34", "v35", "v36", "v37", "v38", "v39", "v40", "v41", "v42", "v43", "v44", "v45", "v46", "v47", "v48", "v49", "v50", "v51", "v52", "v53", "v54", "v55", "v56", "v57", "v58", "v59", "v60", "v61", "v62", "v63", "memory");
}
template <> __device__ __forceinline__ void seg_attn<2>(const QKW& W, unsigned pb, const char* od, unsigned long long* ts) {
  asm volatile("v_mbcnt_lo_u32_b32 v32, -1, 0\n\t"
    "v_mbcnt_hi_u32_b32 v32, -1, v32\n\t"
    "v_lshlrev_b32 v35, 4, v32\n\t"
    "ds_read_b128 v[24:27], v35 offset:20480\n\t"
    "ds_read_b128 v[28:31], v35 offset:21504\n\t"
    "v_lshrrev_b32 v33, 4, v32\n\t"
    "v_and_b32 v34, 15, v32\n\t"
    "v_lshrrev_b32 v4, 2, v34\n\t"
    "v_and_b32 v4, 2, v4\n\t"
    "v_xor_b32 v4, v33, v4\n\t"
    "v_lshl_or_b32 v4, v4, 4, %[pb]\n\t"
    "v_lshl_or_b32 v4, v34, 6, v4\n\t"
    "ds_read_b128 v[8:11], v4 offset:49152\n\t"
    "ds_read_b128 v[12:15], v4 offset:51200\n\t"
    "ds_read_b128 v[16:19], v4 offset:53248\n\t"
    "ds_read_b128 v[20:23], v4 offset:55296\n\t"
    "ds_read_b128 v[40:43], v4 offset:32768\n\t"
    "ds_read_b128 v[44:47], v4 offset:34816\n\t"
    "ds_read_b128 v[48:51], v4 offset:36864\n\t"
    "ds_read_b128 v[52:55], v4 offset:38912\n\t"
    "v_lshlrev_b32 v35, 2, v33\n\t"
    "v_sub_u32 v35, v34, v35\n\t"
    "v_ashrrev_i32 v60, 31, v35\n\t"
    "v_and_b32 v60, %[neg], v60\n\t"
    "v_add_u32 v61, -1, v35\n\t"
    "v_ashrrev_i32 v61, 31, v61\n\t"
    "v_and_b32 v61, %[neg], v61\n\t"
    "v_add_u32 v62, -2, v35\n\t"
    "v_ashrrev_i32 v62, 31, v62\n\t"
    "v_and_b32 v62, %[neg], v62\n\t"
    "v_add_u32 v63, -3, v35\n\t"
    "v_ashrrev_i32 v63, 31, v63\n\t"
    "v_and_b32 v63, %[neg], v63\n\t"
    "s_waitcnt lgkmcnt(6)\n\t"
    "v_mfma_f32_16x16x32_bf16 v[32:35], v[24:27], v[8:11], 0\n\t"
    "v_mfma_f32_16x16x32_bf16 v[36:39], v[24:27], v[12:15], 0\n\t"
    "s_waitcnt lgkmcnt(4)\n\t"
    "v_mfma_f32_16x16x32_bf16 v[32:35], v[28:31], v[16:19], v[32:35]\n\t"
    "v_mfma_f32_16x16x32_bf16 v[36:39], v[28:31], v[20:23], v[36:39]\n\t"
    "s_waitcnt lgkmcnt(3)\n\t"
    "v_mfma_f32_16x16x32_bf16 v[60:63], v[40:43], v[8:11], v[60:63]\n\t"
    "s_waitcnt lgkmcnt(2)\n\t"
    "v_mfma_f32_16x16x32_bf16 v[60:63], v[44:47], v[12:15], v[60:63]\n\t"
    "v_mbcnt_lo_u32_b32 v7, -1, 0\n\t"
    "v_mbcnt_hi_u32_b32 v7, -1, v7\n\t"
    "s_waitcnt lgkmcnt(1)\n\t"
    "v_mfma_f32_16x16x32_bf16 v[60:63], v[48:51], v[16:19], v[60:63]\n\t"
    "v_lshrrev_b32 v5, 4, v7\n\t"
    "v_and_b32 v5, 2, v5\n\t"
    "s_waitcnt lgkmcnt(0)\n\t"
    "v_mfma_f32_16x16x32_bf16 v[60:63], v[52:55], v[20:23], v[60:63]\n\t"
    "v_xor_b32 v5, v5, v7\n\t"
    "v_lshl_or_b32 v5, v5, 4, %[pb]\n\t"
    "ds_read_b64_tr_b16 v[48:49], v5 offset:45056\n\t"
    "ds_read_b64_tr_b16 v[52:53], v5 offset:45064\n\t"
    "v_add_f32 v32, v32, v38\n\t"
    "v_add_f32 v33, v33, v39\n\t"
    "v_and_b32 v6, 12, v7\n\t"
    "v_and_b32 v4, 3, v7\n\t"
    "v_lshl_add_u32 v4, v6, 1, v4\n\t"
    "v_lshrrev_b32 v6, 4, v7\n\t"
    "v_lshl_add_u32 v4, v4, 2, v6\n\t"
    "v_lshlrev_b32 v4, 2, v4\n\t"
    "v_add_u32 v4, 19456, v4\n\t"
    "ds_read_b64_tr_b16 v[40:41], v5 offset:47104\n\t"
    "ds_read_b64_tr_b16 v[44:45], v5 offset:47112\n\t"
    "ds_read2_b32 v[36:37], v4 offset0:0 offset1:16\n\t"
    "ds_read2_b32 v[38:39], v4 offset0:128 offset1:144\n\t"
    "v_and_b32 v4, 7, v7\n\t"
    "v_xor_b32 v6, v6, v4\n\t"
    "v_max3_f32 v34, v32, v33, %[lo]\n\t"
    "v_max3_f32 v34, v34, v60, v61\n\t"
    "v_max3_f32 v34, v34, v62, v63\n\t"
    "v_mov_b32 v35, v34\n\t"
    "v_lshlrev_b32 v6, 4, v6\n\t"
    "v_and_b32 v4, 15, v7\n\t"
    "v_permlane16_swap_b32 v34, v35\n\t"
    "v_max_f32 v34, v34, v35\n\t"
    "v_mov_b32 v35, v34\n\t"
    "v_lshl_or_b32 v6, v4, 7, v6\n\t"
    "v_xor_b32 v4, 64, v6\n\t"
    "v_permlane32_swap_b32 v34, v35\n\t"
    "v_max_f32 v34, v34, v35\n\t"
    "v_mul_f32 v34, %[nl], v34\n\t"
    "v_fma_f32 v32, v32, %[lsc], v34\n\t"
    "v_fma_f32 v33, v33, %[lsc], v34\n\t"
    "v_fma_f32 v60, v60, %[lsc], v34\n\t"
    "v_fma_f32 v61, v61, %[lsc], v34\n\t"
    "v_fma_f32 v62, v62, %[lsc], v34\n\t"
    "v_fma_f32 v63, v63, %[lsc], v34\n\t"
    "v_exp_f32 v60, v60\n\t"
    "v_exp_f32 v61, v61\n\t"
    "v_exp_f32 v62, v62\n\t"
    "v_exp_f32 v63, v63\n\t"
    "v_exp_f32 v32, v32\n\t"
    "v_exp_f32 v33, v33\n\t"
    "v_cvt_pk_bf16_f32 v24, v60, v61\n\t"
    "v_cvt_pk_bf16_f32 v25, v62, v63\n\t"
    "v_cvt_pk_bf16_f32 v28, v32, v33\n\t"
    "s_waitcnt lgkmcnt(0)\n\t"
    "v_mfma_f32_16x16x16_bf16 v[8:11], v[48:49], v[24:25], 0\n\t"
    "v_mov_b32 v56, %[one]\n\t"
    "v_mov_b32 v57, %[one]\n\t"
    "v_mfma_f32_16x16x16_bf16 v[12:15], v[52:53], v[24:25], 0\n\t"
    "v_mov_b32 v58, %[one]\n\t"
    "v_mov_b32 v59, %[one]\n\t"
    "v_mfma_f32_16x16x16_bf16 v[16:19], v[40:41], v[24:25], 0\n\t"
    "v_mov_b32 v29, 0\n\t"
    "v_mov_b32 v30, 0\n\t"
    "v_mfma_f32_16x16x16_bf16 v[60:63], v[56:57], v[24:25], 0\n\t"
    "v_mov_b32 v31, v28\n\t"
    "v_mfma_f32_16x16x16_bf16 v[20:23], v[44:45], v[24:25], 0\n\t"
    "v_mfma_f32_16x16x16_bf16 v[8:11], v[36:37], v[28:29], v[8:11]\n\t"
    "v_mfma_f32_16x16x16_bf16 v[12:15], v[36:37], v[30:31], v[12:15]\n\t"
    "s_nop 1\n\t"
    "v_mfma_f32_16x16x16_bf16 v[16:19], v[38:39], v[28:29], v[16:19]\n\t"
    "v_mfma_f32_16x16x16_bf16 v[60:63], v[56:57], v[28:29], v[60:63]\n\t"
    "s_nop 1\n\t"
    "v_mfma_f32_16x16x16_bf16 v[20:23], v[38:39], v[30:31], v[20:23]\n\t"
    "s_nop 4\n\t"
    "v_rcp_f32 v60, v60\n\t"
    "s_nop 0\n\t"
    "v_pk_mul_f32 v[8:9], v[8:9], v[60:61] op_sel_hi:[1,0]\n\t"
    "v_pk_mul_f32 v[10:11], v[10:11], v[60:61] op_sel_hi:[1,0]\n\t"
    "v_pk_mul_f32 v[12:13], v[12:13], v[60:61] op_sel_hi:[1,0]\n\t"
    "v_pk_mul_f32 v[14:15], v[14:15], v[60:61] op_sel_hi:[1,0]\n\t"
    "v_cvt_pk_bf16_f32 v24, v8, v9\n\t"
    "v_cvt_pk_bf16_f32 v25, v10, v11\n\t"
    "v_cvt_pk_bf16_f32 v26, v12, v13\n\t"
    "v_cvt_pk_bf16_f32 v27, v14, v15\n\t"
    "global_store_dwordx4 v6, v[24:27], %[od] offset:0 sc1\n\t"
    "v_pk_mul_f32 v[16:17], v[16:17], v[60:61] op_sel_hi:[1,0]\n\t"
    "v_pk_mul_f32 v[18:19], v[18:19], v[60:61] op_sel_hi:[1,0]\n\t"
    "v_pk_mul_f32 v[20:21], v[20:21], v[60:61] op_sel_hi:[1,0]\n\t"
    "v_pk_mul_f32 v[22:23], v[22:23], v[60:61] op_sel_hi:[1,0]\n\t"
    "v_cvt_pk_bf16_f32 v28, v16, v17\n\t"
    "v_cvt_pk_bf16_f32 v29, v18, v19\n\t"
    "v_cvt_pk_bf16_f32 v30, v20, v21\n\t"
    "v_cvt_pk_bf16_f32 v31, v22, v23\n\t"
    "global_store_dwordx4 v4, v[28:31], %[od] offset:0 sc1\n\t"
    "s_waitcnt lgkmcnt(0)\n\t"
    "s_nop 0"
    :
    : [pb] "s"(pb), [od] "s"(od), [neg] "s"(-3.0e38f), [lo] "s"(-1.0e38f), [one] "s"(0x3F803F80u), [lsc] "s"(LSC), [nl] "s"(-LSC), "{v[64:95]}"(W.w0), "{v[96:127]}"(W.w1), "{v[128:159]}"(W.w2), "{v[160:191]}"(W.w3), "{v[192:223]}"(W.w4), "{v[224:255]}"(W.w5)
    : "v4", "v5", "v6", "v7", "v8", "v9", "v10", "v11", "v12", "v13", "v14", "v15", "v16", "v17", "v18", "v19", "v20", "v21", "v22", "v23", "v24", "v25", "v26", "v27", "v28", "v29", "v30", "v31", "v32", "v33", "v34", "v35", "v36", "v37", "v38", "v39", "v40", "v41", "v42", "v43", "v44", "v45", "v46", "v47", "v48", "v49", "v50", "v51", "v52", "v53", "v54", "v55", "v56", "v57", "v58", "v59", "v60", "v61", "v62", "v63", "memory");
}
template <> __device__ __forceinline__ void seg_attn<3>(const QKW& W, unsigned pb, const char* od, unsigned long long* ts) {
  asm volatile("v_mbcnt_lo_u32_b32 v32, -1, 0\n\t"
    "v_mbcnt_hi_u32_b32 v32, -1, v32\n\t"
    "v_lshlrev_b32 v35, 4, v32\n\t"
    "ds_read_b128 v[24:27], v35 offset:20480\n\t"
    "ds_read_b128 v[28:31], v35 offset:21504\n\t"
    "v_lshrrev_b32 v33, 4, v32\n\t"
    "v_and_b32 v34, 15, v32\n\t"
    "v_lshrrev_b32 v4, 2, v34\n\t"
    "v_and_b32 v4, 2, v4\n\t"
    "v_xor_b32 v4, v33, v4\n\t"
    "v_lshl_or_b32 v4, v4, 4, %[pb]\n\t"
    "v_lshl_or_b32 v4, v34, 6, v4\n\t"
    "ds_read_b128 v[8:11], v4 offset:50176\n\t"
    "ds_read_b128 v[12:15], v4 offset:52224\n\t"
    "ds_read_b128 v[16:19], v4 offset:54272\n\t"
    "ds_read_b128 v[20:23], v4 offset:56320\n\t"
    "ds_read_b128 v[40:43], v4 offset:32768\n\t"
    "ds_read_b128 v[44:47], v4 offset:34816\n\t"
    "ds_read_b128 v[48:51], v4 offset:36864\n\t"
    "ds_read_b128 v[52:55], v4 offset:38912\n\t"
    "v_lshlrev_b32 v35, 2, v33\n\t"
    "v_sub_u32 v35, v34, v35\n\t"
    "v_ashrrev_i32 v60, 31, v35\n\t"
    "v_and_b32 v60, %[neg], v60\n\t"
    "v_add_u32 v61, -1, v35\n\t"
    "v_ashrrev_i32 v61, 31, v61\n\t"
    "v_and_b32 v61, %[neg], v61\n\t"
    "v_add_u32 v62, -2, v35\n\t"
    "v_ashrrev_i32 v62, 31, v62\n\t"
    "v_and_b32 v62, %[neg], v62\n\t"
    "v_add_u32 v63, -3, v35\n\t"
    "v_ashrrev_i32 v63, 31, v63\n\t"
    "v_and_b32 v63, %[neg], v63\n\t"
    "s_waitcnt lgkmcnt(6)\n\t"
    "v_mfma_f32_16x16x32_bf16 v[32:35], v[24:27], v[8:11], 0\n\t"
    "v_mfma_f32_16x16x32_bf16 v[36:39], v[24:27], v[12:15], 0\n\t"
    "s_waitcnt lgkmcnt(4)\n\t"
    "v_mfma_f32_16x16x32_bf16 v[32:35], v[28:31], v[16:19], v[32:35]\n\t"
    "v_mfma_f32_16x16x32_bf16 v[36:39], v[28:31], v[20:23], v[36:39]\n\t"
    "ds_read_b128 v[24:27], v4 offset:33792\n\t"
    "ds_read_b128 v[28:31], v4 offset:35840\n\t"
    "s_waitcnt lgkmcnt(5)\n\t"
    "v_mfma_f32_16x16x32_bf16 v[56:59], v[40:43], v[8:11], 0\n\t"
    "s_waitcnt lgkmcnt(4)\n\t"
    "v_mfma_f32_16x16x32_bf16 v[56:59], v[44:47], v[12:15], v[56:59]\n\t"
    "ds_read_b128 v[40:43], v4 offset:37888\n\t"
    "ds_read_b128 v[44:47], v4 offset:39936\n\t"
    "v_mbcnt_lo_u32_b32 v7, -1, 0\n\t"
    "v_mbcnt_hi_u32_b32 v7, -1, v7\n\t"
    "s_waitcnt lgkmcnt(5)\n\t"
    "v_mfma_f32_16x16x32_bf16 v[56:59], v[48:51], v[16:19], v[56:59]\n\t"
    "v_lshrrev_b32 v5, 4, v7\n\t"
    "v_and_b32 v5, 2, v5\n\t"
    "s_waitcnt lgkmcnt(4)\n\t"
    "v_mfma_f32_16x16x32_bf16 v[56:59], v[52:55], v[20:23], v[56:59]\n\t"
    "v_xor_b32 v5, v5, v7\n\t"
    "v_lshl_or_b32 v5, v5, 4, %[pb]\n\t"
    "ds_read_b64_tr_b16 v[48:49], v5 offset:45056\n\t"
    "ds_read_b64_tr_b16 v[50:51], v5 offset:46080\n\t"
    "ds_read_b64_tr_b16 v[52:53], v5 offset:45064\n\t"
    "ds_read_b64_tr_b16 v[54:55], v5 offset:46088\n\t"
    "v_add_f32 v32, v32, v38\n\t"
    "v_add_f32 v33, v33, v39\n\t"
    "s_waitcnt lgkmcnt(7)\n\t"
    "v_mfma_f32_16x16x32_bf16 v[60:63], v[24:27], v[8:11], v[60:63]\n\t"
    "v_and_b32 v6, 12, v7\n\t"
    "v_and_b32 v4, 3, v7\n\t"
    "s_waitcnt lgkmcnt(6)\n\t"
    "v_mfma_f32_16x16x32_bf16 v[60:63], v[28:31], v[12:15], v[60:63]\n\t"
    "v_lshl_add_u32 v4, v6, 1, v4\n\t"
    "v_lshrrev_b32 v6, 4, v7\n\t"
    "s_waitcnt lgkmcnt(5)\n\t"
    "v_mfma_f32_16x16x32_bf16 v[60:63], v[40:43], v[16:19], v[60:63]\n\t"
    "v_lshl_add_u32 v4, v4, 2, v6\n\t"
    "v_lshlrev_b32 v4, 2, v4\n\t"
    "s_waitcnt lgkmcnt(4)\n\t"
    "v_mfma_f32_16x16x32_bf16 v[60:63], v[44:47], v[20:23], v[60:63]\n\t"
    "v_add_u32 v4, 19456, v4\n\t"
    "ds_read_b64_tr_b16 v[40:41], v5 offset:47104\n\t"
    "ds_read_b64_tr_b16 v[42:43], v5 offset:48128\n\t"
    "ds_read_b64_tr_b16 v[44:45], v5 offset:47112\n\t"
    "ds_read_b64_tr_b16 v[46:47], v5 offset:48136\n\t"
    "ds_read2_b32 v[36:37], v4 offset0:0 offset1:16\n\t"
    "ds_read2_b32 v[38:39], v4 offset0:128 offset1:144\n\t"
    "v_and_b32 v4, 7, v7\n\t"
    "v_xor_b32 v6, v6, v4\n\t"
    "v_max3_f32 v34, v32, v33, %[lo]\n\t"
    "v_max3_f32 v34, v34, v56, v57\n\t"
    "v_max3_f32 v34, v34, v58, v59\n\t"
    "v_max3_f32 v34, v34, v60, v61\n\t"
    "v_max3_f32 v34, v34, v62, v63\n\t"
    "v_mov_b32 v35, v34\n\t"
    "v_lshlrev_b32 v6, 4, v6\n\t"
    "v_and_b32 v4, 15, v7\n\t"
    "v_permlane16_swap_b32 v34, v35\n\t"
    "v_max_f32 v34, v34, v35\n\t"
    "v_mov_b32 v35, v34\n\t"
    "v_lshl_or_b32 v6, v4, 7, v6\n\t"
    "v_xor_b32 v4, 64, v6\n\t"
    "v_permlane32_swap_b32 v34, v35\n\t"
    "v_max_f32 v34, v34, v35\n\t"
    "v_mul_f32 v34, %[nl], v34\n\t"
    "v_fma_f32 v32, v32, %[lsc], v34\n\t"
    "v_fma_f32 v33, v33, %[lsc], v34\n\t"
    "v_fma_f32 v56, v56, %[lsc], v34\n\t"
    "v_fma_f32 v57, v57, %[lsc], v34\n\t"
    "v_fma_f32 v58, v58, %[lsc], v34\n\t"
    "v_fma_f32 v59, v59, %[lsc], v34\n\t"
    "v_fma_f32 v60, v60, %[lsc], v34\n\t"
    "v_fma_f32 v61, v61, %[lsc], v34\n\t"
    "v_fma_f32 v62, v62, %[lsc], v34\n\t"
    "v_fma_f32 v63, v63, %[lsc], v34\n\t"
    "v_exp_f32 v56, v56\n\t"
    "v_exp_f32 v57, v57\n\t"
    "v_exp_f32 v58, v58\n\t"
    "v_exp_f32 v59, v59\n\t"
    "v_exp_f32 v60, v60\n\t"
    "v_exp_f32 v61, v61\n\t"
    "v_exp_f32 v62, v62\n\t"
    "v_exp_f32 v63, v63\n\t"
    "v_exp_f32 v32, v32\n\t"
    "v_exp_f32 v33, v33\n\t"
    "v_cvt_pk_bf16_f32 v24, v56, v57\n\t"
    "v_cvt_pk_bf16_f32 v25, v58, v59\n\t"
    "v_cvt_pk_bf16_f32 v26, v60, v61\n\t"
    "v_cvt_pk_bf16_f32 v27, v62, v63\n\t"
    "v_cvt_pk_bf16_f32 v28, v32, v33\n\t"
    "s_waitcnt lgkmcnt(0)\n\t"
    "v_mfma_f32_16x16x32_bf16 v[8:11], v[48:51], v[24:27], 0\n\t"
    "v_mov_b32 v56, %[one]\n\t"
    "v_mov_b32 v57, %[one]\n\t"
    "v_mfma_f32_16x16x32_bf16 v[12:15], v[52:55], v[24:27], 0\n\t"
    "v_mov_b32 v58, %[one]\n\t"
    "v_mov_b32 v59, %[one]\n\t"
    "v_mfma_f32_16x16x32_bf16 v[16:19], v[40:43], v[24:27], 0\n\t"
    "v_mov_b32 v29, 0\n\t"
    "v_mov_b32 v30, 0\n\t"
    "v_mfma_f32_16x16x32_bf16 v[60:63], v[56:59], v[24:27], 0\n\t"
    "v_mov_b32 v31, v28\n\t"
    "v_mfma_f32_16x16x32_bf16 v[20:23], v[44:47], v[24:27], 0\n\t"
    "v_mfma_f32_16x16x16_bf16 v[8:11], v[36:37], v[28:29], v[8:11]\n\t"
    "v_mfma_f32_16x16x16_bf16 v[12:15], v[36:37], v[30:31], v[12:15]\n\t"
    "s_nop 1\n\t"
    "v_mfma_f32_16x16x16_bf16 v[16:19], v[38:39], v[28:29], v[16:19]\n\t"
    "v_mfma_f32_16x16x16_bf16 v[60:63], v[56:57], v[28:29], v[60:63]\n\t"
    "s_nop 1\n\t"
    "v_mfma_f32_16x16x16_bf16 v[20:23], v[38:39], v[30:31], v[20:23]\n\t"
    "s_nop 4\n\t"
    "v_rcp_f32 v60, v60\n\t"
    "s_nop 0\n\t"
    "v_pk_mul_f32 v[8:9], v[8:9], v[60:61] op_sel_hi:[1,0]\n\t"
    "v_pk_mul_f32 v[10:11], v[10:11], v[60:61] op_sel_hi:[1,0]\n\t"
    "v_pk_mul_f32 v[12:13], v[12:13], v[60:61] op_sel_hi:[1,0]\n\t"
    "v_pk_mul_f32 v[14:15], v[14:15], v[60:61] op_sel_hi:[1,0]\n\t"
    "v_cvt_pk_bf16_f32 v24, v8, v9\n\t"
    "v_cvt_pk_bf16_f32 v25, v10, v11\n\t"
    "v_cvt_pk_bf16_f32 v26, v12, v13\n\t"
    "v_cvt_pk_bf16_f32 v27, v14, v15\n\t"
    "global_store_dwordx4 v6, v[24:27], %[od] offset:2048 sc1\n\t"
    "v_pk_mul_f32 v[16:17], v[16:17], v[60:61] op_sel_hi:[1,0]\n\t"
    "v_pk_mul_f32 v[18:19], v[18:19], v[60:61] op_sel_hi:[1,0]\n\t"
    "v_pk_mul_f32 v[20:21], v[20:21], v[60:61] op_sel_hi:[1,0]\n\t"
    "v_pk_mul_f32 v[22:23], v[22:23], v[60:61] op_sel_hi:[1,0]\n\t"
    "v_cvt_pk_bf16_f32 v28, v16, v17\n\t"
    "v_cvt_pk_bf16_f32 v29, v18, v19\n\t"
    "v_cvt_pk_bf16_f32 v30, v20, v21\n\t"
    "v_cvt_pk_bf16_f32 v31, v22, v23\n\t"
    "global_store_dwordx4 v4, v[28:31], %[od] offset:2048 sc1\n\t"
    "s_waitcnt lgkmcnt(0)\n\t"
    "s_nop 0"
    :
    : [pb] "s"(pb), [od] "s"(od), [neg] "s"(-3.0e38f), [lo] "s"(-1.0e38f), [one] "s"(0x3F803F80u), [lsc] "s"(LSC), [nl] "s"(-LSC), "{v[64:95]}"(W.w0), "{v[96:127]}"(W.w1), "{v[128:159]}"(W.w2), "{v[160:191]}"(W.w3), "{v[192:223]}"(W.w4), "{v[224:255]}"(W.w5)
    : "v4", "v5", "v6", "v7", "v8", "v9", "v10", "v11", "v12", "v13", "v14", "v15", "v16", "v17", "v18", "v19", "v20", "v21", "v22", "v23", "v24", "v25", "v26", "v27", "v28", "v29", "v30", "v31", "v32", "v33", "v34", "v35", "v36", "v37", "v38", "v39", "v40", "v41", "v42", "v43", "v44", "v45", "v46", "v47", "v48", "v49", "v50", "v51", "v52", "v53", "v54", "v55", "v56", "v57", "v58", "v59", "v60", "v61", "v62", "v63", "memory");
}
template <int I> __device__ __forceinline__ void qk_wload(u32x32& w, unsigned a0, unsigned a1);
template <> __device__ __forceinline__ void qk_wload<0>(u32x32& w, unsigned a0, unsigned a1) {
  asm volatile("ds_read_b64_tr_b16 v[64:65], %1 offset:0\n\t"
    "ds_read_b64_tr_b16 v[66:67], %1 offset:512\n\t"
    "ds_read_b64_tr_b16 v[68:69], %1 offset:8192\n\t"
    "ds_read_b64_tr_b16 v[70:71], %1 offset:8704\n\t"
    "ds_read_b64_tr_b16 v[72:73], %2 offset:0\n\t"
    "ds_read_b64_tr_b16 v[74:75], %2 offset:512\n\t"
    "ds_read_b64_tr_b16 v[76:77], %2 offset:8192\n\t"
    "ds_read_b64_tr_b16 v[78:79], %2 offset:8704\n\t"
    "s_waitcnt lgkmcnt(0)\n\t"
    "ds_read_b64_tr_b16 v[80:81], %1 offset:16384\n\t"
    "ds_read_b64_tr_b16 v[82:83], %1 offset:16896\n\t"
    "ds_read_b64_tr_b16 v[84:85], %1 offset:24576\n\t"
    "ds_read_b64_tr_b16 v[86:87], %1 offset:25088\n\t"
    "ds_read_b64_tr_b16 v[88:89], %2 offset:16384\n\t"
    "ds_read_b64_tr_b16 v[90:91], %2 offset:16896\n\t"
    "ds_read_b64_tr_b16 v[92:93], %2 offset:24576\n\t"
    "ds_read_b64_tr_b16 v[94:95], %2 offset:25088\n\t"
    "s_waitcnt lgkmcnt(0)"
    : "=&{v[64:95]}"(w) : "v"(a0), "v"(a1) : "memory");
}
template <> __device__ __forceinline__ void qk_wload<1>(u32x32& w, unsigned a0, unsigned a1) {
  asm volatile("ds_read_b64_tr_b16 v[96:97], %1 offset:0\n\t"
    "ds_read_b64_tr_b16 v[98:99], %1 offset:512\n\t"
    "ds_read_b64_tr_b16 v[100:101], %1 offset:8192\n\t"
    "ds_read_b64_tr_b16 v[102:103], %1 offset:8704\n\t"
    "ds_read_b64_tr_b16 v[104:105], %2 offset:0\n\t"
    "ds_read_b64_tr_b16 v[106:107], %2 offset:512\n\t"
    "ds_read_b64_tr_b16 v[108:109], %2 offset:8192\n\t"
    "ds_read_b64_tr_b16 v[110:111], %2 offset:8704\n\t"
    "s_waitcnt lgkmcnt(0)\n\t"
    "ds_read_b64_tr_b16 v[112:113], %1 offset:16384\n\t"
    "ds_read_b64_tr_b16 v[114:115], %1 offset:16896\n\t"
    "ds_read_b64_tr_b16 v[116:117], %1 offset:24576\n\t"
    "ds_read_b64_tr_b16 v[118:119], %1 offset:25088\n\t"
    "ds_read_b64_tr_b16 v[120:121], %2 offset:16384\n\t"
    "ds_read_b64_tr_b16 v[122:123], %2 offset:16896\n\t"
    "ds_read_b64_tr_b16 v[124:125], %2 offset:24576\n\t"
    "ds_read_b64_tr_b16 v[126:127], %2 offset:25088\n\t"
    "s_waitcnt lgkmcnt(0)"
    : "=&{v[96:127]}"(w) : "v"(a0), "v"(a1) : "memory");
}
template <> __device__ __forceinline__ void qk_wload<2>(u32x32& w, unsigned a0, unsigned a1) {
  asm volatile("ds_read_b64_tr_b16 v[128:129], %1 offset:0\n\t"
    "ds_read_b64_tr_b16 v[130:131], %1 offset:512\n\t"
    "ds_read_b64_tr_b16 v[132:133], %1 offset:8192\n\t"
    "ds_read_b64_tr_b16 v[134:135], %1 offset:8704\n\t"
    "ds_read_b64_tr_b16 v[136:137], %2 offset:0\n\t"
    "ds_read_b64_tr_b16 v[138:139], %2 offset:512\n\t"
    "ds_read_b64_tr_b16 v[140:141], %2 offset:8192\n\t"
    "ds_read_b64_tr_b16 v[142:143], %2 offset:8704\n\t"
    "s_waitcnt lgkmcnt(0)\n\t"
    "ds_read_b64_tr_b16 v[144:145], %1 offset:16384\n\t"
    "ds_read_b64_tr_b16 v[146:147], %1 offset:16896\n\t"
    "ds_read_b64_tr_b16 v[148:149], %1 offset:24576\n\t"
    "ds_read_b64_tr_b16 v[150:151], %1 offset:25088\n\t"
    "ds_read_b64_tr_b16 v[152:153], %2 offset:16384\n\t"
    "ds_read_b64_tr_b16 v[154:155], %2 offset:16896\n\t"
    "ds_read_b64_tr_b16 v[156:157], %2 offset:24576\n\t"
    "ds_read_b64_tr_b16 v[158:159], %2 offset:25088\n\t"
    "s_waitcnt lgkmcnt(0)"
    : "=&{v[128:159]}"(w) : "v"(a0), "v"(a1) : "memory");
}
template <> __device__ __forceinline__ void qk_wload<3>(u32x32& w, unsigned a0, unsigned a1) {
  asm volatile("ds_read_b64_tr_b16 v[160:161], %1 offset:0\n\t"
    "ds_read_b64_tr_b16 v[162:163], %1 offset:512\n\t"
    "ds_read_b64_tr_b16 v[164:165], %1 offset:8192\n\t"
    "ds_read_b64_tr_b16 v[166:167], %1 offset:8704\n\t"
    "ds_read_b64_tr_b16 v[168:169], %2 offset:0\n\t"
    "ds_read_b64_tr_b16 v[170:171], %2 offset:512\n\t"
    "ds_read_b64_tr_b16 v[172:173], %2 offset:8192\n\t"
    "ds_read_b64_tr_b16 v[174:175], %2 offset:8704\n\t"
    "s_waitcnt lgkmcnt(0)\n\t"
    "ds_read_b64_tr_b16 v[176:177], %1 offset:16384\n\t"
    "ds_read_b64_tr_b16 v[178:179], %1 offset:16896\n\t"
    "ds_read_b64_tr_b16 v[180:181], %1 offset:24576\n\t"
    "ds_read_b64_tr_b16 v[182:183], %1 offset:25088\n\t"
    "ds_read_b64_tr_b16 v[184:185], %2 offset:16384\n\t"
    "ds_read_b64_tr_b16 v[186:187], %2 offset:16896\n\t"
    "ds_read_b64_tr_b16 v[188:189], %2 offset:24576\n\t"
    "ds_read_b64_tr_b16 v[190:191], %2 offset:25088\n\t"
    "s_waitcnt lgkmcnt(0)"
    : "=&{v[160:191]}"(w) : "v"(a0), "v"(a1) : "memory");
}
template <> __device__ __forceinline__ void qk_wload<4>(u32x32& w, unsigned a0, unsigned a1) {
  asm volatile("ds_read_b64_tr_b16 v[192:193], %1 offset:0\n\t"
    "ds_read_b64_tr_b16 v[194:195], %1 offset:512\n\t"
    "ds_read_b64_tr_b16 v[196:197], %1 offset:8192\n\t"
    "ds_read_b64_tr_b16 v[198:199], %1 offset:8704\n\t"
    "ds_read_b64_tr_b16 v[200:201], %2 offset:0\n\t"
    "ds_read_b64_tr_b16 v[202:203], %2 offset:512\n\t"
    "ds_read_b64_tr_b16 v[204:205], %2 offset:8192\n\t"
    "ds_read_b64_tr_b16 v[206:207], %2 offset:8704\n\t"
    "s_waitcnt lgkmcnt(0)\n\t"
    "ds_read_b64_tr_b16 v[208:209], %1 offset:16384\n\t"
    "ds_read_b64_tr_b16 v[210:211], %1 offset:16896\n\t"
    "ds_read_b64_tr_b16 v[212:213], %1 offset:24576\n\t"
    "ds_read_b64_tr_b16 v[214:215], %1 offset:25088\n\t"
    "ds_read_b64_tr_b16 v[216:217], %2 offset:16384\n\t"
    "ds_read_b64_tr_b16 v[218:219], %2 offset:16896\n\t"
    "ds_read_b64_tr_b16 v[220:221], %2 offset:24576\n\t"
    "ds_read_b64_tr_b16 v[222:223], %2 offset:25088\n\t"
    "s_waitcnt lgkmcnt(0)"
    : "=&{v[192:223]}"(w) : "v"(a0), "v"(a1) : "memory");
}
template <> __device__ __forceinline__ void qk_wload<5>(u32x32& w, unsigned a0, unsigned a1) {
  asm volatile("ds_read_b64_tr_b16 v[224:225], %1 offset:0\n\t"
    "ds_read_b64_tr_b16 v[226:227], %1 offset:512\n\t"
    "ds_read_b64_tr_b16 v[228:229], %1 offset:8192\n\t"
    "ds_read_b64_tr_b16 v[230:231], %1 offset:8704\n\t"
    "ds_read_b64_tr_b16 v[232:233], %2 offset:0\n\t"
    "ds_read_b64_tr_b16 v[234:235], %2 offset:512\n\t"
    "ds_read_b64_tr_b16 v[236:237], %2 offset:8192\n\t"
    "ds_read_b64_tr_b16 v[238:239], %2 offset:8704\n\t"
    "s_waitcnt lgkmcnt(0)\n\t"
    "ds_read_b64_tr_b16 v[240:241], %1 offset:16384\n\t"
    "ds_read_b64_tr_b16 v[242:243], %1 offset:16896\n\t"
    "ds_read_b64_tr_b16 v[244:245], %1 offset:24576\n\t"
    "ds_read_b64_tr_b16 v[246:247], %1 offset:25088\n\t"
    "ds_read_b64_tr_b16 v[248:249], %2 offset:16384\n\t"
    "ds_read_b64_tr_b16 v[250:251], %2 offset:16896\n\t"
    "ds_read_b64_tr_b16 v[252:253], %2 offset:24576\n\t"
    "ds_read_b64_tr_b16 v[254:255], %2 offset:25088\n\t"
    "s_waitcnt lgkmcnt(0)"
    : "=&{v[224:255]}"(w) : "v"(a0), "v"(a1) : "memory");
}
#define OREP 2
#define AREP 2
#define BELL1 0
#define RINGA 1
constexpr int V_P1 = 131072, V_PV = 149632, V_P2 = 0;
struct VRW { u32x32 k0, k1, o0, o1, o2, o3; };
__device__ __forceinline__ void vr_v(const VRW& W, const char* ag, const char* kd, const char* vd, unsigned wl, unsigned wa, unsigned wb, unsigned ra, unsigned rb, unsigned& cap, unsigned& sp, unsigned long long& tc, unsigned long long* ts) {
  asm volatile("s_mov_b32 %[sp], 0\n\t"
    "v_mbcnt_lo_u32_b32 v20, -1, 0\n\t"
    "v_mbcnt_hi_u32_b32 v20, -1, v20\n\t"
    "v_lshlrev_b32 v21, 4, v20\n\t"
    "s_nop 0\n\t"
    "global_load_dwordx4 v[24:27], v21, %[ag] sc1\n\t"
    "global_load_dwordx4 v[28:31], v21, %[ag] offset:1024 sc1\n\t"
    "s_memtime %[tc]\n\t"
    "v_lshl_add_u32 v6, v20, 4, %[wl]\n\t"
    "ds_read_b128 v[32:35], v6 offset:0\n\t"
    "ds_read_b128 v[36:39], v6 offset:1024\n\t"
    "ds_read_b128 v[40:43], v6 offset:2048\n\t"
    "ds_read_b128 v[44:47], v6 offset:3072\n\t"
    "ds_read_b128 v[48:51], v6 offset:4096\n\t"
    "ds_read_b128 v[52:55], v6 offset:5120\n\t"
    "ds_read_b128 v[56:59], v6 offset:6144\n\t"
    "ds_read_b128 v[60:63], v6 offset:7168\n\t"
    "s_setprio 3\n\t"
    "v_and_b32 v6, 2, v20\n\t"
    "v_mul_u32_u24 v6, 0x5555, v6\n\t"
    "v_or_b32 v4, %[c44], v6\n\t"
    "v_and_b32 v6, 15, v20\n\t"
    "v_lshl_add_u32 v5, v6, 2, %[wa]\n\t"
    "v_and_b32 v6, 48, v20\n\t"
    "v_mad_u32_u24 v5, v6, 36, v5\n\t"
    "v_and_b32 v6, 32, v20\n\t"
    "v_mul_u32_u24 v7, 18, v6\n\t"
    "v_and_b32 v6, 4, v20\n\t"
    "v_mul_u32_u24 v6, 72, v6\n\t"
    "v_add_u32 v7, v7, v6\n\t"
    "v_and_b32 v6, 16, v20\n\t"
    "v_lshl_add_u32 v7, v6, 3, v7\n\t"
    "v_and_b32 v6, 3, v20\n\t"
    "v_lshl_add_u32 v7, v6, 3, v7\n\t"
    "v_and_b32 v6, 8, v20\n\t"
    "v_mul_u32_u24 v6, 1160, v6\n\t"
    "v_add3_u32 v7, v7, v6, %[ra]\n\t"
    "3:\n\t"
    "v_mov_b32 v8, 0\n\t"
    "v_mov_b32 v9, 0\n\t"
    "v_mov_b32 v10, 0\n\t"
    "v_mov_b32 v11, 0\n\t"
    "v_mov_b32 v12, 0\n\t"
    "v_mov_b32 v13, 0\n\t"
    "v_mov_b32 v14, 0\n\t"
    "v_mov_b32 v15, 0\n\t"
    "v_mov_b32 v16, 0\n\t"
    "v_mov_b32 v17, 0\n\t"
    "v_mov_b32 v18, 0\n\t"
    "v_mov_b32 v19, 0\n\t"
    "v_mov_b32 v20, 0\n\t"
    "v_mov_b32 v21, 0\n\t"
    "v_mov_b32 v22, 0\n\t"
    "v_mov_b32 v23, 0\n\t"
    "s_waitcnt vmcnt(0) lgkmcnt(0)\n\t"
    "v_smfmac_f32_16x16x64_bf16 v[8:11], v[24:27], v[64:71], v4\n\t"
    "v_smfmac_f32_16x16x64_bf16 v[8:11], v[28:31], v[72:79], v4\n\t"
    "v_max3_u32 v6, v24, v25, v26\n\t"
    "v_max3_u32 v6, v6, v27, v28\n\t"
    "v_smfmac_f32_16x16x64_bf16 v[12:15], v[24:27], v[80:87], v4\n\t"
    "v_max3_u32 v6, v6, v29, v30\n\t"
    "v_max_u32 v6, v6, v31\n\t"
    "v_smfmac_f32_16x16x64_bf16 v[12:15], v[28:31], v[88:95], v4\n\t"
    "v_cmp_eq_u32 vcc, -1, v6\n\t"
    "s_cbranch_vccnz 1f\n\t"
    "2:\n\t"
    "v_smfmac_f32_16x16x64_bf16 v[16:19], v[24:27], v[96:103], v4\n\t"
    "v_smfmac_f32_16x16x64_bf16 v[16:19], v[28:31], v[104:111], v4\n\t"
    "v_add_f32 v8, v8, v10\n\t"
    "v_add_f32 v9, v9, v11\n\t"
    "ds_write2_b32 v5, v8, v9 offset0:0 offset1:72\n\t"
    "v_smfmac_f32_16x16x64_bf16 v[20:23], v[24:27], v[112:119], v4\n\t"
    "v_smfmac_f32_16x16x64_bf16 v[20:23], v[28:31], v[120:127], v4\n\t"
    "v_add_f32 v12, v12, v14\n\t"
    "v_add_f32 v13, v13, v15\n\t"
    "ds_write2_b32 v5, v12, v13 offset0:16 offset1:88\n\t"
    "v_add_f32 v16, v16, v18\n\t"
    "v_add_f32 v17, v17, v19\n\t"
    "ds_write2_b32 v5, v16, v17 offset0:32 offset1:104\n\t"
    "s_nop 1\n\t"
    "v_add_f32 v20, v20, v22\n\t"
    "v_add_f32 v21, v21, v23\n\t"
    "ds_write2_b32 v5, v20, v21 offset0:48 offset1:120\n\t"
    "v_mov_b32 v8, 0\n\t"
    "v_mov_b32 v9, 0\n\t"
    "v_mov_b32 v10, 0\n\t"
    "v_mov_b32 v11, 0\n\t"
    "v_mov_b32 v12, 0\n\t"
    "v_mov_b32 v13, 0\n\t"
    "v_mov_b32 v14, 0\n\t"
    "v_mov_b32 v15, 0\n\t"
    "v_mbcnt_lo_u32_b32 v5, -1, 0\n\t"
    "v_mbcnt_hi_u32_b32 v5, -1, v5\n\t"
    "s_waitcnt lgkmcnt(0)\n\t"
    "s_barrier\n\t"
    "ds_read_b64 v[16:17], v7 offset:0\n\t"
    "ds_read_b64 v[18:19], v7 offset:2304\n\t"
    "ds_read_b64 v[20:21], v7 offset:4608\n\t"
    "ds_read_b64 v[22:23], v7 offset:6912\n\t"
    "v_smfmac_f32_16x16x64_bf16 v[8:11], v[24:27], v[32:39], v4\n\t"
    "v_smfmac_f32_16x16x64_bf16 v[8:11], v[28:31], v[40:47], v4\n\t"
    "v_smfmac_f32_16x16x64_bf16 v[12:15], v[24:27], v[48:55], v4\n\t"
    "v_smfmac_f32_16x16x64_bf16 v[12:15], v[28:31], v[56:63], v4\n\t"
    "v_and_b32 v6, 48, v5\n\t"
    "v_and_b32 v7, 7, v5\n\t"
    "v_lshlrev_b32 v7, 2, v7\n\t"
    "v_lshl_or_b32 v7, v6, 1, v7\n\t"
    "s_waitcnt lgkmcnt(2)\n\t"
    "v_pk_add_f32 v[16:17], v[16:17], v[18:19]\n\t"
    "s_waitcnt lgkmcnt(0)\n\t"
    "v_pk_add_f32 v[20:21], v[20:21], v[22:23]\n\t"
    "v_pk_add_f32 v[16:17], v[16:17], v[20:21]\n\t"
    "s_nop 1\n\t"
    "v_add_f32_dpp v16, v16, v16 row_ror:8 row_mask:0xf bank_mask:0xf\n\t"
    "v_add_f32_dpp v17, v17, v17 row_ror:8 row_mask:0xf bank_mask:0xf\n\t"
    "v_cvt_pk_bf16_f32 v6, v16, v17\n\t"
    "v_min_u32 v6, -2, v6\n\t"
    "s_mov_b64 exec, %[em]\n\t"
    "global_store_dword v7, v6, %[kd] sc1\n\t"
    "s_mov_b64 exec, -1\n\t"
    "v_lshl_add_u32 v16, v5, 3, %[wb]\n\t"
    "v_add_f32 v8, v8, v10\n\t"
    "v_add_f32 v9, v9, v11\n\t"
    "v_add_f32 v12, v12, v14\n\t"
    "v_add_f32 v13, v13, v15\n\t"
    "ds_write2st64_b64 v16, v[8:9], v[12:13] offset1:1\n\t"
    "v_and_b32 v6, 8, v5\n\t"
    "v_mul_u32_u24 v6, 528, v6\n\t"
    "v_lshl_add_u32 v5, v7, 1, v6\n\t"
    "v_add_u32 v5, %[rb], v5\n\t"
    "s_cmp_ge_u32 %[wb], 153728\n\t"
    "s_waitcnt lgkmcnt(0)\n\t"
    "s_barrier\n\t"
    "s_cbranch_scc1 8f\n\t"
    "ds_read_b64 v[16:17], v5 offset:0\n\t"
    "ds_read_b64 v[18:19], v5 offset:1024\n\t"
    "ds_read_b64 v[20:21], v5 offset:2048\n\t"
    "ds_read_b64 v[22:23], v5 offset:3072\n\t"
    "s_waitcnt lgkmcnt(2)\n\t"
    "v_pk_add_f32 v[16:17], v[16:17], v[18:19]\n\t"
    "s_waitcnt lgkmcnt(0)\n\t"
    "v_pk_add_f32 v[20:21], v[20:21], v[22:23]\n\t"
    "v_pk_add_f32 v[16:17], v[16:17], v[20:21]\n\t"
    "s_nop 1\n\t"
    "v_add_f32_dpp v16, v16, v16 row_ror:8 row_mask:0xf bank_mask:0xf\n\t"
    "v_add_f32_dpp v17, v17, v17 row_ror:8 row_mask:0xf bank_mask:0xf\n\t"
    "v_cvt_pk_bf16_f32 v6, v16, v17\n\t"
    "v_min_u32 v6, -2, v6\n\t"
    "s_mov_b64 exec, %[em]\n\t"
    "global_store_dword v7, v6, %[vd] sc1\n\t"
    "s_mov_b64 exec, -1\n\t"
    "8:\n\t"
    "s_setprio 2\n\t"
    "s_waitcnt lgkmcnt(0)\n\t"
    "s_branch 9f\n\t"
    "1:\n\t"
    "s_add_u32 %[sp], %[sp], 1\n\t"
    "s_cmp_ge_u32 %[sp], %[cap]\n\t"
    "s_cbranch_scc1 7f\n\t"
    "v_mbcnt_lo_u32_b32 v20, -1, 0\n\t"
    "v_mbcnt_hi_u32_b32 v20, -1, v20\n\t"
    "v_lshlrev_b32 v21, 4, v20\n\t"
    "s_nop 0\n\t"
    "global_load_dwordx4 v[24:27], v21, %[ag] sc1\n\t"
    "global_load_dwordx4 v[28:31], v21, %[ag] offset:1024 sc1\n\t"
    "s_waitcnt lgkmcnt(0)\n\t"
    "s_memtime %[tc]\n\t"
    "s_nop 7\n\t"
    "s_branch 3b\n\t"
    "7:\n\t"
    "s_mov_b32 %[cap], 0\n\t"
    "s_branch 2b\n\t"
    "9:"
    : [tc] "=&s"(tc), [sp] "=&s"(sp), [cap] "+s"(cap)
    : [ag] "s"(ag), [kd] "s"(kd), [vd] "s"(vd), [wl] "s"(wl), [wa] "s"(wa), [wb] "s"(wb), [ra] "s"(ra), [rb] "s"(rb), [c44] "s"(0x4444u), [em] "s"(0x00FF00FF00FF00FFull), "{v[64:95]}"(W.k0), "{v[96:127]}"(W.k1), "{v[128:159]}"(W.o0), "{v[160:191]}"(W.o1), "{v[192:223]}"(W.o2), "{v[224:255]}"(W.o3)
    : "v4", "v5", "v6", "v7", "v8", "v9", "v10", "v11", "v12", "v13", "v14", "v15", "v16", "v17", "v18", "v19", "v20", "v21", "v22", "v23", "v24", "v25", "v26", "v27", "v28", "v29", "v30", "v31", "v32", "v33", "v34", "v35", "v36", "v37", "v38", "v39", "v40", "v41", "v42", "v43", "v44", "v45", "v46", "v47", "v48", "v49", "v50", "v51", "v52", "v53", "v54", "v55", "v56", "v57", "v58", "v59", "v60", "v61", "v62", "v63", "vcc", "scc", "memory");
}
__device__ __forceinline__ void vr_w0(const VRW& W, const char* ag, const char* vd, unsigned wl, unsigned wa, unsigned ra, unsigned& cap, unsigned& sp, unsigned long long& tc, unsigned long long* ts) {
  unsigned st;
  asm volatile("s_mov_b32 %[sp], 0\n\t"
    "v_mbcnt_lo_u32_b32 v20, -1, 0\n\t"
    "v_mbcnt_hi_u32_b32 v20, -1, v20\n\t"
    "v_lshlrev_b32 v21, 4, v20\n\t"
    "s_nop 0\n\t"
    "global_load_dwordx4 v[24:27], v21, %[ag] sc1\n\t"
    "global_load_dwordx4 v[28:31], v21, %[ag] offset:1024 sc1\n\t"
    "s_memtime %[tc]\n\t"
    "v_lshl_add_u32 v6, v20, 4, %[wl]\n\t"
    "ds_read_b128 v[32:35], v6 offset:0\n\t"
    "ds_read_b128 v[36:39], v6 offset:1024\n\t"
    "ds_read_b128 v[40:43], v6 offset:2048\n\t"
    "ds_read_b128 v[44:47], v6 offset:3072\n\t"
    "ds_read_b128 v[48:51], v6 offset:4096\n\t"
    "ds_read_b128 v[52:55], v6 offset:5120\n\t"
    "ds_read_b128 v[56:59], v6 offset:6144\n\t"
    "ds_read_b128 v[60:63], v6 offset:7168\n\t"
    "s_setprio 3\n\t"
    "v_and_b32 v6, 2, v20\n\t"
    "v_mul_u32_u24 v6, 0x5555, v6\n\t"
    "v_or_b32 v4, %[c44], v6\n\t"
    "v_and_b32 v6, 15, v20\n\t"
    "v_lshl_add_u32 v5, v6, 2, %[wa]\n\t"
    "v_and_b32 v6, 48, v20\n\t"
    "v_mad_u32_u24 v5, v6, 52, v5\n\t"
    "v_mul_u32_u24 v7, 52, v6\n\t"
    "v_and_b32 v6, 4, v20\n\t"
    "v_mul_u32_u24 v6, 104, v6\n\t"
    "v_add_u32 v7, v7, v6\n\t"
    "v_and_b32 v6, 8, v20\n\t"
    "v_lshl_add_u32 v7, v6, 4, v7\n\t"
    "v_and_b32 v6, 3, v20\n\t"
    "v_lshl_add_u32 v7, v6, 3, v7\n\t"
    "v_add_u32 v7, %[ra], v7\n\t"
    "3:\n\t"
    "v_mov_b32 v8, 0\n\t"
    "v_mov_b32 v9, 0\n\t"
    "v_mov_b32 v10, 0\n\t"
    "v_mov_b32 v11, 0\n\t"
    "v_mov_b32 v12, 0\n\t"
    "v_mov_b32 v13, 0\n\t"
    "v_mov_b32 v14, 0\n\t"
    "v_mov_b32 v15, 0\n\t"
    "v_mov_b32 v16, 0\n\t"
    "v_mov_b32 v17, 0\n\t"
    "v_mov_b32 v18, 0\n\t"
    "v_mov_b32 v19, 0\n\t"
    "v_mov_b32 v20, 0\n\t"
    "v_mov_b32 v21, 0\n\t"
    "v_mov_b32 v22, 0\n\t"
    "v_mov_b32 v23, 0\n\t"
    "s_waitcnt vmcnt(0) lgkmcnt(0)\n\t"
    "v_smfmac_f32_16x16x64_bf16 v[8:11], v[24:27], v[32:39], v4\n\t"
    "v_smfmac_f32_16x16x64_bf16 v[8:11], v[28:31], v[40:47], v4\n\t"
    "v_mov_b32 v32, 0\n\t"
    "v_mov_b32 v33, 0\n\t"
    "v_smfmac_f32_16x16x64_bf16 v[12:15], v[24:27], v[48:55], v4\n\t"
    "v_mov_b32 v34, 0\n\t"
    "v_mov_b32 v35, 0\n\t"
    "v_smfmac_f32_16x16x64_bf16 v[12:15], v[28:31], v[56:63], v4\n\t"
    "v_mov_b32 v36, 0\n\t"
    "v_mov_b32 v37, 0\n\t"
    "v_smfmac_f32_16x16x64_bf16 v[16:19], v[24:27], v[64:71], v4\n\t"
    "v_mov_b32 v38, 0\n\t"
    "v_mov_b32 v39, 0\n\t"
    "v_smfmac_f32_16x16x64_bf16 v[16:19], v[28:31], v[72:79], v4\n\t"
    "v_add_f32 v8, v8, v10\n\t"
    "v_add_f32 v9, v9, v11\n\t"
    "ds_write2_b32 v5, v8, v9 offset0:64 offset1:168\n\t"
    "v_smfmac_f32_16x16x64_bf16 v[20:23], v[24:27], v[80:87], v4\n\t"
    "v_smfmac_f32_16x16x64_bf16 v[20:23], v[28:31], v[88:95], v4\n\t"
    "v_add_f32 v12, v12, v14\n\t"
    "v_add_f32 v13, v13, v15\n\t"
    "ds_write2_b32 v5, v12, v13 offset0:80 offset1:184\n\t"
    "v_smfmac_f32_16x16x64_bf16 v[32:35], v[24:27], v[96:103], v4\n\t"
    "v_smfmac_f32_16x16x64_bf16 v[32:35], v[28:31], v[104:111], v4\n\t"
    "v_add_f32 v16, v16, v18\n\t"
    "v_add_f32 v17, v17, v19\n\t"
    "ds_write2_b32 v5, v16, v17 offset0:0 offset1:104\n\t"
    "v_smfmac_f32_16x16x64_bf16 v[36:39], v[24:27], v[112:119], v4\n\t"
    "v_smfmac_f32_16x16x64_bf16 v[36:39], v[28:31], v[120:127], v4\n\t"
    "v_max3_u32 v6, v24, v25, v26\n\t"
    "v_add_f32 v20, v20, v22\n\t"
    "v_add_f32 v21, v21, v23\n\t"
    "ds_write2_b32 v5, v20, v21 offset0:16 offset1:120\n\t"
    "v_add_f32 v32, v32, v34\n\t"
    "v_add_f32 v33, v33, v35\n\t"
    "ds_write2_b32 v5, v32, v33 offset0:32 offset1:136\n\t"
    "v_max3_u32 v6, v6, v27, v28\n\t"
    "v_add_f32 v36, v36, v38\n\t"
    "v_add_f32 v37, v37, v39\n\t"
    "ds_write2_b32 v5, v36, v37 offset0:48 offset1:152\n\t"
    "v_max3_u32 v6, v6, v29, v30\n\t"
    "v_max_u32 v6, v6, v31\n\t"
    "v_cmp_eq_u32 vcc, -1, v6\n\t"
    "v_mbcnt_lo_u32_b32 v48, -1, 0\n\t"
    "v_mbcnt_hi_u32_b32 v48, -1, v48\n\t"
    "v_lshlrev_b32 v48, 2, v48\n\t"
    "s_cbranch_vccnz 1f\n\t"
    "2:\n\t"
    "s_waitcnt lgkmcnt(0)\n\t"
    "s_barrier\n\t"
    "ds_read_b64 v[8:9], v7 offset:0\n\t"
    "ds_read_b64 v[10:11], v7 offset:3328\n\t"
    "ds_read_b64 v[12:13], v7 offset:6656\n\t"
    "ds_read_b64 v[14:15], v7 offset:9984\n\t"
    "ds_read_b64 v[16:17], v7 offset:13376\n\t"
    "ds_read_b64 v[18:19], v7 offset:16704\n\t"
    "ds_read_b64 v[20:21], v7 offset:20032\n\t"
    "ds_read_b64 v[22:23], v7 offset:23360\n\t"
    "s_waitcnt lgkmcnt(6)\n\t"
    "v_pk_add_f32 v[8:9], v[8:9], v[10:11]\n\t"
    "s_waitcnt lgkmcnt(4)\n\t"
    "v_pk_add_f32 v[12:13], v[12:13], v[14:15]\n\t"
    "v_pk_add_f32 v[8:9], v[8:9], v[12:13]\n\t"
    "s_waitcnt lgkmcnt(2)\n\t"
    "v_pk_add_f32 v[16:17], v[16:17], v[18:19]\n\t"
    "s_waitcnt lgkmcnt(0)\n\t"
    "v_pk_add_f32 v[20:21], v[20:21], v[22:23]\n\t"
    "v_pk_add_f32 v[16:17], v[16:17], v[20:21]\n\t"
    "v_pk_add_f32 v[8:9], v[8:9], v[16:17]\n\t"
    "v_cvt_pk_bf16_f32 v6, v8, v9\n\t"
    "v_min_u32 v6, -2, v6\n\t"
    "global_store_dword v48, v6, %[vd] sc1\n\t"
    "s_setprio 2\n\t"
    "s_waitcnt lgkmcnt(0)\n\t"
    "s_branch 9f\n\t"
    "1:\n\t"
    "s_add_u32 %[sp], %[sp], 1\n\t"
    "s_cmp_ge_u32 %[sp], %[cap]\n\t"
    "s_cbranch_scc1 7f\n\t"
    "s_waitcnt lgkmcnt(0)\n\t"
    "v_mbcnt_lo_u32_b32 v20, -1, 0\n\t"
    "v_mbcnt_hi_u32_b32 v20, -1, v20\n\t"
    "v_lshlrev_b32 v21, 4, v20\n\t"
    "s_nop 0\n\t"
    "global_load_dwordx4 v[24:27], v21, %[ag] sc1\n\t"
    "global_load_dwordx4 v[28:31], v21, %[ag] offset:1024 sc1\n\t"
    "s_memtime %[tc]\n\t"
    "v_lshl_add_u32 v6, v20, 4, %[wl]\n\t"
    "ds_read_b128 v[32:35], v6 offset:0\n\t"
    "ds_read_b128 v[36:39], v6 offset:1024\n\t"
    "ds_read_b128 v[40:43], v6 offset:2048\n\t"
    "ds_read_b128 v[44:47], v6 offset:3072\n\t"
    "ds_read_b128 v[48:51], v6 offset:4096\n\t"
    "ds_read_b128 v[52:55], v6 offset:5120\n\t"
    "ds_read_b128 v[56:59], v6 offset:6144\n\t"
    "ds_read_b128 v[60:63], v6 offset:7168\n\t"
    "s_branch 3b\n\t"
    "7:\n\t"
    "s_mov_b32 %[cap], 0\n\t"
    "s_branch 2b\n\t"
    "9:"
    : [tc] "=&s"(tc), [sp] "=&s"(sp), [cap] "+s"(cap), [st] "=&s"(st)
    : [ag] "s"(ag), [vd] "s"(vd), [wl] "s"(wl), [wa] "s"(wa), [ra] "s"(ra), [c44] "s"(0x4444u), [em] "s"(0x00FF00FF00FF00FFull), "{v[64:95]}"(W.k0), "{v[96:127]}"(W.k1), "{v[128:159]}"(W.o0), "{v[160:191]}"(W.o1), "{v[192:223]}"(W.o2), "{v[224:255]}"(W.o3)
    : "v4", "v5", "v6", "v7", "v8", "v9", "v10", "v11", "v12", "v13", "v14", "v15", "v16", "v17", "v18", "v19", "v20", "v21", "v22", "v23", "v24", "v25", "v26", "v27", "v28", "v29", "v30", "v31", "v32", "v33", "v34", "v35", "v36", "v37", "v38", "v39", "v40", "v41", "v42", "v43", "v44", "v45", "v46", "v47", "v48", "v49", "v50", "v51", "v52", "v53", "v54", "v55", "v56", "v57", "v58", "v59", "v60", "v61", "v62", "v63", "vcc", "scc", "memory");
}
__device__ __forceinline__ void vr_w1(const VRW& W, const char* ag, const char* vd, unsigned wl, unsigned wa, unsigned ra, unsigned& cap, unsigned& sp, unsigned long long& tc, unsigned long long* ts) {
  unsigned st;
  asm volatile("s_mov_b32 %[sp], 0\n\t"
    "v_mbcnt_lo_u32_b32 v20, -1, 0\n\t"
    "v_mbcnt_hi_u32_b32 v20, -1, v20\n\t"
    "v_lshlrev_b32 v21, 4, v20\n\t"
    "s_nop 0\n\t"
    "global_load_dwordx4 v[24:27], v21, %[ag] sc1\n\t"
    "global_load_dwordx4 v[28:31], v21, %[ag] offset:1024 sc1\n\t"
    "s_memtime %[tc]\n\t"
    "v_lshl_add_u32 v6, v20, 4, %[wl]\n\t"
    "ds_read_b128 v[32:35], v6 offset:0\n\t"
    "ds_read_b128 v[36:39], v6 offset:1024\n\t"
    "ds_read_b128 v[40:43], v6 offset:2048\n\t"
    "ds_read_b128 v[44:47], v6 offset:3072\n\t"
    "ds_read_b128 v[48:51], v6 offset:4096\n\t"
    "ds_read_b128 v[52:55], v6 offset:5120\n\t"
    "ds_read_b128 v[56:59], v6 offset:6144\n\t"
    "ds_read_b128 v[60:63], v6 offset:7168\n\t"
    "s_setprio 3\n\t"
    "v_and_b32 v6, 2, v20\n\t"
    "v_mul_u32_u24 v6, 0x5555, v6\n\t"
    "v_or_b32 v4, %[c44], v6\n\t"
    "v_and_b32 v6, 15, v20\n\t"
    "v_lshl_add_u32 v5, v6, 2, %[wa]\n\t"
    "v_and_b32 v6, 48, v20\n\t"
    "v_mad_u32_u24 v5, v6, 52, v5\n\t"
    "v_and_b32 v6, 32, v20\n\t"
    "v_mul_u32_u24 v7, 26, v6\n\t"
    "v_and_b32 v6, 16, v20\n\t"
    "v_lshl_add_u32 v7, v6, 1, v7\n\t"
    "v_and_b32 v6, 7, v20\n\t"
    "v_lshl_add_u32 v7, v6, 2, v7\n\t"
    "v_and_b32 v6, 8, v20\n\t"
    "v_mul_u32_u24 v6, 1672, v6\n\t"
    "v_add_u32 v7, v7, v6\n\t"
    "v_add_u32 v7, %[ra], v7\n\t"
    "s_setprio 0\n\t"
    "s_setprio 3\n\t"
    "3:\n\t"
    "v_mov_b32 v8, 0\n\t"
    "v_mov_b32 v9, 0\n\t"
    "v_mov_b32 v10, 0\n\t"
    "v_mov_b32 v11, 0\n\t"
    "v_mov_b32 v12, 0\n\t"
    "v_mov_b32 v13, 0\n\t"
    "v_mov_b32 v14, 0\n\t"
    "v_mov_b32 v15, 0\n\t"
    "v_mov_b32 v16, 0\n\t"
    "v_mov_b32 v17, 0\n\t"
    "v_mov_b32 v18, 0\n\t"
    "v_mov_b32 v19, 0\n\t"
    "v_mov_b32 v20, 0\n\t"
    "v_mov_b32 v21, 0\n\t"
    "v_mov_b32 v22, 0\n\t"
    "v_mov_b32 v23, 0\n\t"
    "s_waitcnt vmcnt(0) lgkmcnt(0)\n\t"
    "v_smfmac_f32_16x16x64_bf16 v[8:11], v[24:27], v[32:39], v4\n\t"
    "v_smfmac_f32_16x16x64_bf16 v[8:11], v[28:31], v[40:47], v4\n\t"
    "v_mov_b32 v32, 0\n\t"
    "v_mov_b32 v33, 0\n\t"
    "v_smfmac_f32_16x16x64_bf16 v[12:15], v[24:27], v[48:55], v4\n\t"
    "v_mov_b32 v34, 0\n\t"
    "v_mov_b32 v35, 0\n\t"
    "v_smfmac_f32_16x16x64_bf16 v[12:15], v[28:31], v[56:63], v4\n\t"
    "v_mov_b32 v36, 0\n\t"
    "v_mov_b32 v37, 0\n\t"
    "v_smfmac_f32_16x16x64_bf16 v[16:19], v[24:27], v[64:71], v4\n\t"
    "v_mov_b32 v38, 0\n\t"
    "v_mov_b32 v39, 0\n\t"
    "v_smfmac_f32_16x16x64_bf16 v[16:19], v[28:31], v[72:79], v4\n\t"
    "v_add_f32 v8, v8, v10\n\t"
    "v_add_f32 v9, v9, v11\n\t"
    "ds_write2_b32 v5, v8, v9 offset0:64 offset1:168\n\t"
    "v_smfmac_f32_16x16x64_bf16 v[20:23], v[24:27], v[80:87], v4\n\t"
    "v_smfmac_f32_16x16x64_bf16 v[20:23], v[28:31], v[88:95], v4\n\t"
    "v_add_f32 v12, v12, v14\n\t"
    "v_add_f32 v13, v13, v15\n\t"
    "ds_write2_b32 v5, v12, v13 offset0:80 offset1:184\n\t"
    "v_smfmac_f32_16x16x64_bf16 v[32:35], v[24:27], v[96:103], v4\n\t"
    "v_smfmac_f32_16x16x64_bf16 v[32:35], v[28:31], v[104:111], v4\n\t"
    "v_add_f32 v16, v16, v18\n\t"
    "v_add_f32 v17, v17, v19\n\t"
    "ds_write2_b32 v5, v16, v17 offset0:0 offset1:104\n\t"
    "v_smfmac_f32_16x16x64_bf16 v[36:39], v[24:27], v[112:119], v4\n\t"
    "v_smfmac_f32_16x16x64_bf16 v[36:39], v[28:31], v[120:127], v4\n\t"
    "v_max3_u32 v6, v24, v25, v26\n\t"
    "v_add_f32 v20, v20, v22\n\t"
    "v_add_f32 v21, v21, v23\n\t"
    "ds_write2_b32 v5, v20, v21 offset0:16 offset1:120\n\t"
    "v_add_f32 v32, v32, v34\n\t"
    "v_add_f32 v33, v33, v35\n\t"
    "ds_write2_b32 v5, v32, v33 offset0:32 offset1:136\n\t"
    "v_max3_u32 v6, v6, v27, v28\n\t"
    "v_add_f32 v36, v36, v38\n\t"
    "v_add_f32 v37, v37, v39\n\t"
    "ds_write2_b32 v5, v36, v37 offset0:48 offset1:152\n\t"
    "v_max3_u32 v6, v6, v29, v30\n\t"
    "v_max_u32 v6, v6, v31\n\t"
    "v_cmp_eq_u32 vcc, -1, v6\n\t"
    "v_mbcnt_lo_u32_b32 v48, -1, 0\n\t"
    "v_mbcnt_hi_u32_b32 v48, -1, v48\n\t"
    "v_and_b32 v6, 48, v48\n\t"
    "v_and_b32 v48, 7, v48\n\t"
    "v_lshlrev_b32 v48, 2, v48\n\t"
    "v_lshl_or_b32 v48, v6, 1, v48\n\t"
    "s_cbranch_vccnz 1f\n\t"
    "2:\n\t"
    "s_waitcnt lgkmcnt(0)\n\t"
    "s_barrier\n\t"
    "ds_read2_b32 v[8:9], v7 offset1:104\n\t"
    "v_add_u32 v7, 3328, v7\n\t"
    "ds_read2_b32 v[10:11], v7 offset1:104\n\t"
    "v_add_u32 v7, 3328, v7\n\t"
    "ds_read2_b32 v[12:13], v7 offset1:104\n\t"
    "v_add_u32 v7, 3328, v7\n\t"
    "ds_read2_b32 v[14:15], v7 offset1:104\n\t"
    "s_waitcnt lgkmcnt(2)\n\t"
    "v_pk_add_f32 v[8:9], v[8:9], v[10:11]\n\t"
    "s_waitcnt lgkmcnt(0)\n\t"
    "v_pk_add_f32 v[12:13], v[12:13], v[14:15]\n\t"
    "v_pk_add_f32 v[8:9], v[8:9], v[12:13]\n\t"
    "s_nop 1\n\t"
    "v_add_f32_dpp v8, v8, v8 row_ror:8 row_mask:0xf bank_mask:0xf\n\t"
    "v_add_f32_dpp v9, v9, v9 row_ror:8 row_mask:0xf bank_mask:0xf\n\t"
    "v_cvt_pk_bf16_f32 v6, v8, v9\n\t"
    "v_min_u32 v6, -2, v6\n\t"
    "s_mov_b64 exec, %[em]\n\t"
    "global_store_dword v48, v6, %[vd] sc1\n\t"
    "s_mov_b64 exec, -1\n\t"
    "s_setprio 2\n\t"
    "s_waitcnt lgkmcnt(0)\n\t"
    "s_branch 9f\n\t"
    "1:\n\t"
    "s_add_u32 %[sp], %[sp], 1\n\t"
    "s_cmp_ge_u32 %[sp], %[cap]\n\t"
    "s_cbranch_scc1 7f\n\t"
    "s_waitcnt lgkmcnt(0)\n\t"
    "v_mbcnt_lo_u32_b32 v20, -1, 0\n\t"
    "v_mbcnt_hi_u32_b32 v20, -1, v20\n\t"
    "v_lshlrev_b32 v21, 4, v20\n\t"
    "s_nop 0\n\t"
    "global_load_dwordx4 v[24:27], v21, %[ag] sc1\n\t"
    "global_load_dwordx4 v[28:31], v21, %[ag] offset:1024 sc1\n\t"
    "s_memtime %[tc]\n\t"
    "v_lshl_add_u32 v6, v20, 4, %[wl]\n\t"
    "ds_read_b128 v[32:35], v6 offset:0\n\t"
    "ds_read_b128 v[36:39], v6 offset:1024\n\t"
    "ds_read_b128 v[40:43], v6 offset:2048\n\t"
    "ds_read_b128 v[44:47], v6 offset:3072\n\t"
    "ds_read_b128 v[48:51], v6 offset:4096\n\t"
    "ds_read_b128 v[52:55], v6 offset:5120\n\t"
    "ds_read_b128 v[56:59], v6 offset:6144\n\t"
    "ds_read_b128 v[60:63], v6 offset:7168\n\t"
    "s_branch 3b\n\t"
    "7:\n\t"
    "s_mov_b32 %[cap], 0\n\t"
    "s_branch 2b\n\t"
    "9:"
    : [tc] "=&s"(tc), [sp] "=&s"(sp), [cap] "+s"(cap), [st] "=&s"(st)
    : [ag] "s"(ag), [vd] "s"(vd), [wl] "s"(wl), [wa] "s"(wa), [ra] "s"(ra), [c44] "s"(0x4444u), [em] "s"(0x00FF00FF00FF00FFull), "{v[64:95]}"(W.k0), "{v[96:127]}"(W.k1), "{v[128:159]}"(W.o0), "{v[160:191]}"(W.o1), "{v[192:223]}"(W.o2), "{v[224:255]}"(W.o3)
    : "v4", "v5", "v6", "v7", "v8", "v9", "v10", "v11", "v12", "v13", "v14", "v15", "v16", "v17", "v18", "v19", "v20", "v21", "v22", "v23", "v24", "v25", "v26", "v27", "v28", "v29", "v30", "v31", "v32", "v33", "v34", "v35", "v36", "v37", "v38", "v39", "v40", "v41", "v42", "v43", "v44", "v45", "v46", "v47", "v48", "v49", "v50", "v51", "v52", "v53", "v54", "v55", "v56", "v57", "v58", "v59", "v60", "v61", "v62", "v63", "vcc", "scc", "memory");
}
__device__ __forceinline__ void vr_r(const VRW& W, const char* os, const char* ad, unsigned wa, unsigned ra, unsigned& cap, unsigned& sp, unsigned long long* ts) {
  asm volatile("v_mbcnt_lo_u32_b32 v4, -1, 0\n\t"
    "v_mbcnt_hi_u32_b32 v4, -1, v4\n\t"
    "v_lshlrev_b32 v5, 4, v4\n\t"
    "global_load_dwordx4 v[12:15], v5, %[os] offset:0 sc1\n\t"
    "global_load_dwordx4 v[16:19], v5, %[os] offset:1024 sc1\n\t"
    "global_load_dwordx4 v[20:23], v5, %[os] offset:2048 sc1\n\t"
    "global_load_dwordx4 v[24:27], v5, %[os] offset:3072 sc1\n\t"
    "s_mov_b32 %[sp], 0\n\t"
    "v_and_b32 v10, 1, v4\n\t"
    "v_mul_u32_u24 v10, 0xaaaa, v10\n\t"
    "v_or_b32 v6, %[c44], v10\n\t"
    "v_and_b32 v10, 15, v4\n\t"
    "v_lshl_add_u32 v7, v10, 2, %[wa]\n\t"
    "v_and_b32 v10, 48, v4\n\t"
    "v_mad_u32_u24 v7, v10, 12, v7\n\t"
    "v_and_b32 v10, 32, v4\n\t"
    "v_mul_u32_u24 v8, 12, v10\n\t"
    "v_and_b32 v10, 4, v4\n\t"
    "v_mad_u32_u24 v8, v10, 48, v8\n\t"
    "v_and_b32 v10, 16, v4\n\t"
    "v_lshl_add_u32 v8, v10, 1, v8\n\t"
    "v_and_b32 v10, 3, v4\n\t"
    "v_lshl_add_u32 v8, v10, 3, v8\n\t"
    "v_and_b32 v10, 8, v4\n\t"
    "v_lshlrev_b32 v9, 12, v10\n\t"
    "v_mul_u32_u24 v10, 400, v10\n\t"
    "v_add3_u32 v8, v8, v10, %[ra]\n\t"
    "v_and_b32 v10, 48, v4\n\t"
    "v_lshl_or_b32 v9, v10, 1, v9\n\t"
    "v_and_b32 v10, 7, v4\n\t"
    "v_lshl_or_b32 v9, v10, 2, v9\n\t"
    "3:\n\t"
    "v_mov_b32 v28, 0\n\t"
    "v_mov_b32 v29, 0\n\t"
    "v_mov_b32 v30, 0\n\t"
    "v_mov_b32 v31, 0\n\t"
    "v_mov_b32 v32, 0\n\t"
    "v_mov_b32 v33, 0\n\t"
    "v_mov_b32 v34, 0\n\t"
    "v_mov_b32 v35, 0\n\t"
    "v_mov_b32 v36, 0\n\t"
    "v_mov_b32 v37, 0\n\t"
    "v_mov_b32 v38, 0\n\t"
    "v_mov_b32 v39, 0\n\t"
    "v_mov_b32 v40, 0\n\t"
    "v_mov_b32 v41, 0\n\t"
    "v_mov_b32 v42, 0\n\t"
    "v_mov_b32 v43, 0\n\t"
    "v_mov_b32 v10, 0\n\t"
    "s_waitcnt vmcnt(3)\n\t"
    "v_smfmac_f32_16x16x64_bf16 v[28:31], v[12:15], v[128:135], v6\n\t"
    "v_max3_u32 v10, v10, v12, v13\n\t"
    "v_smfmac_f32_16x16x64_bf16 v[32:35], v[12:15], v[136:143], v6\n\t"
    "v_max3_u32 v10, v10, v14, v15\n\t"
    "v_smfmac_f32_16x16x64_bf16 v[36:39], v[12:15], v[144:151], v6\n\t"
    "v_smfmac_f32_16x16x64_bf16 v[40:43], v[12:15], v[152:159], v6\n\t"
    "s_waitcnt vmcnt(2)\n\t"
    "v_smfmac_f32_16x16x64_bf16 v[28:31], v[16:19], v[160:167], v6\n\t"
    "v_max3_u32 v10, v10, v16, v17\n\t"
    "v_smfmac_f32_16x16x64_bf16 v[32:35], v[16:19], v[168:175], v6\n\t"
    "v_max3_u32 v10, v10, v18, v19\n\t"
    "v_smfmac_f32_16x16x64_bf16 v[36:39], v[16:19], v[176:183], v6\n\t"
    "v_smfmac_f32_16x16x64_bf16 v[40:43], v[16:19], v[184:191], v6\n\t"
    "s_waitcnt vmcnt(1)\n\t"
    "v_smfmac_f32_16x16x64_bf16 v[28:31], v[20:23], v[192:199], v6\n\t"
    "v_max3_u32 v10, v10, v20, v21\n\t"
    "v_smfmac_f32_16x16x64_bf16 v[32:35], v[20:23], v[200:207], v6\n\t"
    "v_max3_u32 v10, v10, v22, v23\n\t"
    "v_smfmac_f32_16x16x64_bf16 v[36:39], v[20:23], v[208:215], v6\n\t"
    "v_smfmac_f32_16x16x64_bf16 v[40:43], v[20:23], v[216:223], v6\n\t"
    "s_waitcnt vmcnt(0)\n\t"
    "v_smfmac_f32_16x16x64_bf16 v[28:31], v[24:27], v[224:231], v6\n\t"
    "v_max3_u32 v10, v10, v24, v25\n\t"
    "v_smfmac_f32_16x16x64_bf16 v[32:35], v[24:27], v[232:239], v6\n\t"
    "v_max3_u32 v10, v10, v26, v27\n\t"
    "v_smfmac_f32_16x16x64_bf16 v[36:39], v[24:27], v[240:247], v6\n\t"
    "v_cmp_eq_u32 vcc, -1, v10\n\t"
    "v_smfmac_f32_16x16x64_bf16 v[40:43], v[24:27], v[248:255], v6\n\t"
    "s_cbranch_vccnz 4f\n\t"
    "5:\n\t"
    "s_cmp_ge_u32 %[wa], 1536\n\t"
    "s_nop 1\n\t"
    "v_add_f32 v28, v28, v29\n\t"
    "v_add_f32 v32, v32, v33\n\t"
    "v_add_f32 v36, v38, v39\n\t"
    "v_add_f32 v28, v28, v36\n\t"
    "v_add_f32 v40, v42, v43\n\t"
    "v_add_f32 v32, v32, v40\n\t"
    "ds_write2_b32 v7, v28, v32 offset1:16\n\t"
    "s_waitcnt lgkmcnt(0)\n\t"
    "s_barrier\n\t"
    "s_cbranch_scc1 9f\n\t"
    "ds_read_b64 v[44:45], v8 offset:0\n\t"
    "ds_read_b64 v[46:47], v8 offset:768\n\t"
    "ds_read_b64 v[48:49], v8 offset:1536\n\t"
    "ds_read_b64 v[50:51], v8 offset:2304\n\t"
    "s_waitcnt lgkmcnt(2)\n\t"
    "v_pk_add_f32 v[44:45], v[44:45], v[46:47]\n\t"
    "s_waitcnt lgkmcnt(0)\n\t"
    "v_pk_add_f32 v[48:49], v[48:49], v[50:51]\n\t"
    "v_pk_add_f32 v[44:45], v[44:45], v[48:49]\n\t"
    "s_nop 1\n\t"
    "v_add_f32_dpp v44, v44, v44 row_ror:8 row_mask:0xf bank_mask:0xf\n\t"
    "v_add_f32_dpp v45, v45, v45 row_ror:8 row_mask:0xf bank_mask:0xf\n\t"
    "v_cvt_pk_bf16_f32 v10, v44, v45\n\t"
    "v_min_u32 v10, -2, v10\n\t"
    "global_store_dword v9, v10, %[ad] sc1\n\t"
    "s_waitcnt lgkmcnt(0)\n\t"
    "s_branch 9f\n\t"
    "4:\n\t"
    "s_add_u32 %[sp], %[sp], 1\n\t"
    "s_cmp_ge_u32 %[sp], %[cap]\n\t"
    "s_cbranch_scc1 7f\n\t"
    "global_load_dwordx4 v[12:15], v5, %[os] offset:0 sc1\n\t"
    "global_load_dwordx4 v[16:19], v5, %[os] offset:1024 sc1\n\t"
    "global_load_dwordx4 v[20:23], v5, %[os] offset:2048 sc1\n\t"
    "global_load_dwordx4 v[24:27], v5, %[os] offset:3072 sc1\n\t"
    "s_nop 3\n\t"
    "s_branch 3b\n\t"
    "7:\n\t"
    "s_mov_b32 %[cap], 0\n\t"
    "s_branch 5b\n\t"
    "9:"
    : [sp] "=&s"(sp), [cap] "+s"(cap)
    : [os] "s"(os), [ad] "s"(ad), [wa] "s"(wa), [ra] "s"(ra), [c44] "s"(0x4444u), "{v[64:95]}"(W.k0), "{v[96:127]}"(W.k1), "{v[128:159]}"(W.o0), "{v[160:191]}"(W.o1), "{v[192:223]}"(W.o2), "{v[224:255]}"(W.o3)
    : "v4", "v5", "v6", "v7", "v8", "v9", "v10", "v11", "v12", "v13", "v14", "v15", "v16", "v17", "v18", "v19", "v20", "v21", "v22", "v23", "v24", "v25", "v26", "v27", "v28", "v29", "v30", "v31", "v32", "v33", "v34", "v35", "v36", "v37", "v38", "v39", "v40", "v41", "v42", "v43", "v44", "v45", "v46", "v47", "v48", "v49", "v50", "v51", "v52", "v53", "v54", "v55", "v56", "v57", "v58", "v59", "vcc", "scc", "memory");
}
__device__ __forceinline__ void kr_load_k0(VRW& W, unsigned a) {
  asm volatile("ds_read_b64_tr_b16 v[64:65], %[a] offset:0\n\t"
    "ds_read_b64_tr_b16 v[66:67], %[a] offset:256\n\t"
    "ds_read_b64_tr_b16 v[68:69], %[a] offset:4096\n\t"
    "ds_read_b64_tr_b16 v[70:71], %[a] offset:4352\n\t"
    "ds_read_b64_tr_b16 v[80:81], %[a] offset:32\n\t"
    "ds_read_b64_tr_b16 v[82:83], %[a] offset:288\n\t"
    "ds_read_b64_tr_b16 v[84:85], %[a] offset:4128\n\t"
    "ds_read_b64_tr_b16 v[86:87], %[a] offset:4384\n\t"
    "s_waitcnt lgkmcnt(0)\n\t"
    "ds_read_b64_tr_b16 v[96:97], %[a] offset:64\n\t"
    "ds_read_b64_tr_b16 v[98:99], %[a] offset:320\n\t"
    "ds_read_b64_tr_b16 v[100:101], %[a] offset:4160\n\t"
    "ds_read_b64_tr_b16 v[102:103], %[a] offset:4416\n\t"
    "ds_read_b64_tr_b16 v[112:113], %[a] offset:96\n\t"
    "ds_read_b64_tr_b16 v[114:115], %[a] offset:352\n\t"
    "ds_read_b64_tr_b16 v[116:117], %[a] offset:4192\n\t"
    "ds_read_b64_tr_b16 v[118:119], %[a] offset:4448\n\t"
    "s_waitcnt lgkmcnt(0)"
    : "=&{v[64:95]}"(W.k0), "=&{v[96:127]}"(W.k1) : [a] "v"(a) : "memory");
}
__device__ __forceinline__ void kr_load_k1(VRW& W, unsigned a) {
  asm volatile("ds_read_b64_tr_b16 v[72:73], %[a] offset:0\n\t"
    "ds_read_b64_tr_b16 v[74:75], %[a] offset:256\n\t"
    "ds_read_b64_tr_b16 v[76:77], %[a] offset:4096\n\t"
    "ds_read_b64_tr_b16 v[78:79], %[a] offset:4352\n\t"
    "ds_read_b64_tr_b16 v[88:89], %[a] offset:32\n\t"
    "ds_read_b64_tr_b16 v[90:91], %[a] offset:288\n\t"
    "ds_read_b64_tr_b16 v[92:93], %[a] offset:4128\n\t"
    "ds_read_b64_tr_b16 v[94:95], %[a] offset:4384\n\t"
    "s_waitcnt lgkmcnt(0)\n\t"
    "ds_read_b64_tr_b16 v[104:105], %[a] offset:64\n\t"
    "ds_read_b64_tr_b16 v[106:107], %[a] offset:320\n\t"
    "ds_read_b64_tr_b16 v[108:109], %[a] offset:4160\n\t"
    "ds_read_b64_tr_b16 v[110:111], %[a] offset:4416\n\t"
    "ds_read_b64_tr_b16 v[120:121], %[a] offset:96\n\t"
    "ds_read_b64_tr_b16 v[122:123], %[a] offset:352\n\t"
    "ds_read_b64_tr_b16 v[124:125], %[a] offset:4192\n\t"
    "ds_read_b64_tr_b16 v[126:127], %[a] offset:4448\n\t"
    "s_waitcnt lgkmcnt(0)"
    : "+&{v[64:95]}"(W.k0), "+&{v[96:127]}"(W.k1) : [a] "v"(a) : "memory");
}
__device__ __forceinline__ void kr_load_o0(VRW& W, unsigned a) {
  asm volatile("ds_read_b64_tr_b16 v[128:129], %[a] offset:0\n\t"
    "ds_read_b64_tr_b16 v[130:131], %[a] offset:128\n\t"
    "ds_read_b64_tr_b16 v[132:133], %[a] offset:1024\n\t"
    "ds_read_b64_tr_b16 v[134:135], %[a] offset:1152\n\t"
    "ds_read_b64_tr_b16 v[136:137], %[a] offset:32\n\t"
    "ds_read_b64_tr_b16 v[138:139], %[a] offset:160\n\t"
    "ds_read_b64_tr_b16 v[140:141], %[a] offset:1056\n\t"
    "ds_read_b64_tr_b16 v[142:143], %[a] offset:1184\n\t"
    "s_waitcnt lgkmcnt(0)\n\t"
    "ds_read_b64_tr_b16 v[144:145], %[a] offset:4096\n\t"
    "ds_read_b64_tr_b16 v[146:147], %[a] offset:4224\n\t"
    "ds_read_b64_tr_b16 v[148:149], %[a] offset:5120\n\t"
    "ds_read_b64_tr_b16 v[150:151], %[a] offset:5248\n\t"
    "ds_read_b64_tr_b16 v[152:153], %[a] offset:4128\n\t"
    "ds_read_b64_tr_b16 v[154:155], %[a] offset:4256\n\t"
    "ds_read_b64_tr_b16 v[156:157], %[a] offset:5152\n\t"
    "ds_read_b64_tr_b16 v[158:159], %[a] offset:5280\n\t"
    "s_waitcnt lgkmcnt(0)"
    : "=&{v[128:159]}"(W.o0) : [a] "v"(a) : "memory");
}
__device__ __forceinline__ void kr_load_o1(VRW& W, unsigned a) {
  asm volatile("ds_read_b64_tr_b16 v[160:161], %[a] offset:0\n\t"
    "ds_read_b64_tr_b16 v[162:163], %[a] offset:128\n\t"
    "ds_read_b64_tr_b16 v[164:165], %[a] offset:1024\n\t"
    "ds_read_b64_tr_b16 v[166:167], %[a] offset:1152\n\t"
    "ds_read_b64_tr_b16 v[168:169], %[a] offset:32\n\t"
    "ds_read_b64_tr_b16 v[170:171], %[a] offset:160\n\t"
    "ds_read_b64_tr_b16 v[172:173], %[a] offset:1056\n\t"
    "ds_read_b64_tr_b16 v[174:175], %[a] offset:1184\n\t"
    "s_waitcnt lgkmcnt(0)\n\t"
    "ds_read_b64_tr_b16 v[176:177], %[a] offset:4096\n\t"
    "ds_read_b64_tr_b16 v[178:179], %[a] offset:4224\n\t"
    "ds_read_b64_tr_b16 v[180:181], %[a] offset:5120\n\t"
    "ds_read_b64_tr_b16 v[182:183], %[a] offset:5248\n\t"
    "ds_read_b64_tr_b16 v[184:185], %[a] offset:4128\n\t"
    "ds_read_b64_tr_b16 v[186:187], %[a] offset:4256\n\t"
    "ds_read_b64_tr_b16 v[188:189], %[a] offset:5152\n\t"
    "ds_read_b64_tr_b16 v[190:191], %[a] offset:5280\n\t"
    "s_waitcnt lgkmcnt(0)"
    : "=&{v[160:191]}"(W.o1) : [a] "v"(a) : "memory");
}
__device__ __forceinline__ void kr_load_o2(VRW& W, unsigned a) {
  asm volatile("ds_read_b64_tr_b16 v[192:193], %[a] offset:0\n\t"
    "ds_read_b64_tr_b16 v[194:195], %[a] offset:128\n\t"
    "ds_read_b64_tr_b16 v[196:197], %[a] offset:1024\n\t"
    "ds_read_b64_tr_b16 v[198:199], %[a] offset:1152\n\t"
    "ds_read_b64_tr_b16 v[200:201], %[a] offset:32\n\t"
    "ds_read_b64_tr_b16 v[202:203], %[a] offset:160\n\t"
    "ds_read_b64_tr_b16 v[204:205], %[a] offset:1056\n\t"
    "ds_read_b64_tr_b16 v[206:207], %[a] offset:1184\n\t"
    "s_waitcnt lgkmcnt(0)\n\t"
    "ds_read_b64_tr_b16 v[208:209], %[a] offset:4096\n\t"
    "ds_read_b64_tr_b16 v[210:211], %[a] offset:4224\n\t"
    "ds_read_b64_tr_b16 v[212:213], %[a] offset:5120\n\t"
    "ds_read_b64_tr_b16 v[214:215], %[a] offset:5248\n\t"
    "ds_read_b64_tr_b16 v[216:217], %[a] offset:4128\n\t"
    "ds_read_b64_tr_b16 v[218:219], %[a] offset:4256\n\t"
    "ds_read_b64_tr_b16 v[220:221], %[a] offset:5152\n\t"
    "ds_read_b64_tr_b16 v[222:223], %[a] offset:5280\n\t"
    "s_waitcnt lgkmcnt(0)"
    : "=&{v[192:223]}"(W.o2) : [a] "v"(a) : "memory");
}
__device__ __forceinline__ void kr_load_o3(VRW& W, unsigned a) {
  asm volatile("ds_read_b64_tr_b16 v[224:225], %[a] offset:0\n\t"
    "ds_read_b64_tr_b16 v[226:227], %[a] offset:128\n\t"
    "ds_read_b64_tr_b16 v[228:229], %[a] offset:1024\n\t"
    "ds_read_b64_tr_b16 v[230:231], %[a] offset:1152\n\t"
    "ds_read_b64_tr_b16 v[232:233], %[a] offset:32\n\t"
    "ds_read_b64_tr_b16 v[234:235], %[a] offset:160\n\t"
    "ds_read_b64_tr_b16 v[236:237], %[a] offset:1056\n\t"
    "ds_read_b64_tr_b16 v[238:239], %[a] offset:1184\n\t"
    "s_waitcnt lgkmcnt(0)\n\t"
    "ds_read_b64_tr_b16 v[240:241], %[a] offset:4096\n\t"
    "ds_read_b64_tr_b16 v[242:243], %[a] offset:4224\n\t"
    "ds_read_b64_tr_b16 v[244:245], %[a] offset:5120\n\t"
    "ds_read_b64_tr_b16 v[246:247], %[a] offset:5248\n\t"
    "ds_read_b64_tr_b16 v[248:249], %[a] offset:4128\n\t"
    "ds_read_b64_tr_b16 v[250:251], %[a] offset:4256\n\t"
    "ds_read_b64_tr_b16 v[252:253], %[a] offset:5152\n\t"
    "ds_read_b64_tr_b16 v[254:255], %[a] offset:5280\n\t"
    "s_waitcnt lgkmcnt(0)"
    : "=&{v[224:255]}"(W.o3) : [a] "v"(a) : "memory");
}
#define XPTRS(l) const unsigned par_ = ini_resolve(ws, l, tag); char* xch_ = xch_base(ws); \
  const char* xb = uni(xch_ + ((size_t)3 << 21) + (((par_ >> 1) & 1) << 13)); const char* ob = uni(xch_ + ((par_ & 1) << 13)); \
  unsigned* cnt = (unsigned*)(xch_ + ((size_t)4 << 21) + ((par_ >> 2) << 13))
__device__ __forceinline__ void qk_body(char* smem, int wave_id_, int xg, int h, int wg, const u16* __restrict__ Wq, const u16* __restrict__ Wv,
                                        char* ws, unsigned* tmo, unsigned long long tag) {
  const int wave = WAVE_ID;
  const int ty = wave >> 2, w4 = wave & 3;
  const char* wq = uni((const char*)(Wq + h * DH)); const char* wv = uni((const char*)(Wv + h * DH));
  auto stage2 = [&](int i) {
    const int tid = wave * 64 + lane_id();
    if (i < 6) {
      stage_proj<false>(wq + (size_t)i * 128 * HD * 2, Q_RING + ((2 * i) & 3) * 32768, tid, wave);
      if (i < 2) stage_proj<false>(wq + (size_t)(i + 6) * 128 * HD * 2, Q_RING + ((2 * i + 1) & 3) * 32768, tid, wave);
      else stage_proj<true>(wv + (size_t)(i - 2) * 256 * HD * 2, Q_RING + ((2 * i + 1) & 3) * 32768, tid, wave);
    }
  };
  QKW W;
  delay64<QDL>();
  stage2(0); stage2(1);
#define WLD(i, w) do { if ((i) < 5) WAIT_V(8); else WAIT_V(0); \
    WBAR(); { const int l_ = lane_id(); const unsigned o_ = Q_RING + ty * 32768 + ((i) & 1) * 65536; const bool v_ = ty && (i) >= 2; \
      qk_wload<i>(w, proj_base(v_ ? w4 : 2 * w4, l_) + o_, proj_base(v_ ? w4 + 4 : 2 * w4 + 1, l_) + o_); } \
    WBAR(); stage2((i) + 2); if (wave == 0) STS(ws, wg, 1 + (i)); if ((i) == 4 && wave == 7) ini_request(ws, 30720); } while (0)
  WLD(0, W.w0); WLD(1, W.w1); WLD(2, W.w2); WLD(3, W.w3); WLD(4, W.w4); WLD(5, W.w5);
#undef WLD
  XPTRS(smem + 30720);
  if (opaque_tid() == 64) st4_sc1(cnt + (PQKV + h) * 32 + 3, 1u);
  const char* oh = ob + (size_t)(h >> 2) * 16384 + (h & 3) * 1024;
  auto rearm = [&](int ps) {
    unsigned z = SENT; asm volatile("" : "+v"(z));
    const unsigned l16 = lane_id() * 16;
#pragma unroll
    for (int r = 0; r < 2 * OREP; ++r) st16s(oh + (size_t)(ps * 4 + (r >> 1)) * 131072, (r & 1) * 4096u + l16, u32x4{z, z, z, z});
  };
  const char* dbw = xb + (size_t)48 * 16384 + 4096 + h * 128;
  if (wave == 4) *(volatile LAS u32x4*)(smem + Q_TM) = u32x4{0u, 0u, 0u, (unsigned)__builtin_amdgcn_s_memrealtime()};
  if (wave == 6) *(u32x4*)(smem + Q_PQ + 4096 + lane_id() * 16) = u32x4{0u, 0u, 0u, 0u};
  BAR();
  if (wave == 0) STS(ws, wg, 0);
  unsigned avail = 0;
  auto kvq = [&](int s) { return ws + (wave == 5 ? OFF_K : (wave == 6 ? OFF_V : OFF_Q)) + ((size_t)s * NH + h) * 8192; };
  auto stage_kv = [&](int s, int buf) {
    if (avail <= (unsigned)s) {
      for (int spins = 0;; ++spins) {
        avail = qkv_avail<0>(cnt + (PQKV + h) * 32);
        if (avail > (unsigned)s) break;
        __builtin_amdgcn_s_sleep(8);
        if (give_up(spins, tmo, 5)) break;
      }
    }
    const unsigned lane = lane_id(), v0 = (lane ^ ((lane >> 4) & 2)) * 16;
    const char* src = uni(kvq(s));
#pragma unroll
    for (int i = 0; i < 4; ++i) glds16x2s<0>(src + i * 2048, v0, Q_K + buf * 32768 + (wave - 5) * 8192 + i * 2048);
  };
  auto loop = [&](auto w_c) __attribute__((always_inline)) {
    constexpr int WV = decltype(w_c)::value;
    if (WV >= 5 && !DRY) stage_kv(0, 0);
    unsigned t0 = 0, cap = 60000, d0 = D_0, d1 = D_0;
    int dq = 0, e0 = D_Q, e1 = D_Q;
    unsigned tp = 0;
    int sl = DRY ? 2 : 0;
    unsigned fc = 0;
    for (int s = -DRY; s < NSEG; ++s) {
      TSDECL;
      const bool dry = DRY && s < 0;
      const char* dump = ws + OFF_DUMP + 262144;
      const char* ag = xb + (size_t)(((dry ? 0 : sl * 4) + (AREP == 4 ? xg : xg >> 1)) * 2 + (WV >> 2)) * 16384 + (WV < 4 ? WV * 2048 : WV * 1024);
      const int nl = sl == 2 ? 0 : sl + 1;
      const unsigned pb = sl * 32768;
      const char* vs = dry ? (WV < 4 ? ag : dump) : WV == 4 ? dbw : WV == 7 ? (const char*)(cnt + (PROG + h) * 32) : xb + (size_t)(24 + sl * 8 + (h >> 2)) * 16384 + (h & 3) * 2048;
      const char* v2 = dry ? ag : xb + (size_t)(52 + sl * 4 + (h >> 3)) * 16384 + (h & 7) * 1024 + (WV & 1) * 512;
      const char* od = dry ? dump : oh + (size_t)sl * 524288;
      const unsigned sn = (unsigned)(dry ? 255 : s + 1) << 24;
      if (s > 0) wait_rt(t0, dq);
      TS(0);
      unsigned long long tc; unsigned sp;
      qk_step<WV>(W, ag, vs, v2, dry ? dump : dbw, od, pb, sn, cap, sp, tc, nullptr, d0, d1);
      if (dry) d0 = D_0;
      dq = (sp & 0xFFFFu) ? min(dq + DQU, DQM) : max(dq - DQD, 0);
      if (WV >= 5) {
        if (RAE && WV == RAW && s > 0 && s < NSEG - 1) rearm(sl == 0 ? 2 : sl - 1);
        WBAR();
        delay64<E_S>();
        if (s + 1 < NSEG) stage_kv(s + 1, nl);
      }
      if (!RAE && WV == RAW && s > 0 && s < NSEG - 1) rearm(sl == 0 ? 2 : sl - 1);
      unsigned long long* tsp = nullptr;
      seg_attn<WV>(W, pb, dry ? dump : ws + OFF_O + ((size_t)s * 64 + 2 * h + (WV >> 1)) * 4096, tsp);
      if (WV == 1) TSFLUSH(0x8000);
      if (WV < 4 && s >= BEARLY && lane_id() == 0 && (__hip_atomic_fetch_add((LAS unsigned*)(smem + Q_TM + 8), 1u, __ATOMIC_RELAXED, __HIP_MEMORY_SCOPE_WORKGROUP) & 3u) == 3u)
        st4_sc1(cnt + (PROG + h) * 32 + 1, (unsigned)(s + 1));
      if (WV == 6 && s == ARMW) wait_arm(cnt, tmo);
      if (s < NSEG - 1 && !dry) {
        if (WV == 4) {
          delay64<E_B>();
          u32x2 v;
          for (unsigned spins = 0;; ++spins) {
            const int lane = lane_id();
            asm volatile("s_nop 4\n\tglobal_load_dwordx2 %0, %1, %2 sc1\n\ts_waitcnt vmcnt(0)" : "=&v"(v) : "v"((lane & 31) * 128 + (lane >> 5) * 8), "s"(xb + (size_t)(48 + xg) * 16384 + 4096) : "memory");
            if (__all((v.x >> 24) > (unsigned)s) || spins >= cap) break;
          }
          const unsigned now = (unsigned)__builtin_amdgcn_s_memrealtime();
          const unsigned age = rowmin16((now - v.x) & 0xFFFFFFu);
          const unsigned tb = __builtin_amdgcn_readfirstlane(now - min(min(min(rdl(age, 0), rdl(age, 16)), min(rdl(age, 32), rdl(age, 48))), 500u));
          t0 = tb + min(e0, e1);
          *(volatile LAS u32x2*)(smem + Q_TM) = u32x2{(unsigned)(s + 1), t0};
          if (ADQ && s > 1 && __all((v.y >> 24) == (unsigned)s)) {
            const unsigned ag2 = rowmin16((now - v.y) & 0xFFFFFFu);
            e1 = e0;
            e0 = __builtin_amdgcn_readfirstlane(min(max((int)(now - min(min(rdl(ag2, 0), rdl(ag2, 16)), min(rdl(ag2, 32), rdl(ag2, 48))) - tp) + D_V, D_Q - DQM), D_Q + DQM));
          }
          tp = tb;
          asm volatile("" : "+s"(t0), "+s"(e0), "+s"(e1), "+s"(tp));
          TS(14);
          TSFLUSH(0x4000);
        } else {
          for (;;) {
            const u32x2 v = *(volatile LAS u32x2*)(smem + Q_TM);
            t0 = __builtin_amdgcn_readfirstlane(v.y);
            if (__builtin_amdgcn_readfirstlane(v.x) == (unsigned)(s + 1)) break;
            __builtin_amdgcn_s_sleep(1);
          }
        }
      }
      sl = sl == 2 ? 0 : sl + 1;
    }
    WAIT_V(0);
    WBAR();
    if (WV == 7 && lane_id() == 0) st4_sc1(cnt + (PROG + h) * 32, (unsigned)NSEG);
    if (cap == 0 && lane_id() == 0) st4_sc1(tmo, 1u);
    FCS();
  };
  switch (wave) {
    case 0: loop(std::integral_constant<int, 0>{}); break;
    case 1: loop(std::integral_constant<int, 1>{}); break;
    case 2: loop(std::integral_constant<int, 2>{}); break;
    case 3: loop(std::integral_constant<int, 3>{}); break;
    case 4: loop(std::integral_constant<int, 4>{}); break;
    case 5: loop(std::integral_constant<int, 5>{}); break;
    case 6: loop(std::integral_constant<int, 6>{}); break;
    default: loop(std::integral_constant<int, 7>{}); break;
  }
  __builtin_amdgcn_s_setprio(0);
}

__device__ __forceinline__ void vr_body(char* smem, int wave_id_, int xg, int h, int c, int wg, const u16* __restrict__ Wk, const u16* __restrict__ Wv,
                                        const u16* __restrict__ Wo, char* ws, unsigned* tmo, unsigned long long tag) {
  const int wave = WAVE_ID;
  const unsigned po = (unsigned)(h >> 4) * 16384u + ((h >> 1) & 7) * 1024 + (h & 1) * 512 + c * 128;
  const unsigned ko_ = (unsigned)(24 + (h >> 2)) * 16384u + (h & 3) * 2048 + c * 1024;
  const unsigned vo_ = (unsigned)(52 + (h >> 3)) * 16384u + (h & 7) * 1024 + c * 512;
  const int hg = (wave + ROTW * ((2 * h + c) & 3)) & 7;
  VRW W;
  {
    const int lane = lane_id();
    const int g = lane >> 4, q4 = (lane & 15) >> 2, p4 = lane & 3, fr = 4 * g + (q4 & 1) + 32 * (q4 >> 1);
    const unsigned lw = wave * 16384;
    const char* sk = uni((const char*)(Wk + (size_t)(128 * wave) * HD + h * DH + 64 * c));
    const char* sv_ = uni((const char*)(Wv + (size_t)(128 * wave) * HD + h * DH + 64 + 32 * c));
    const char* so = uni((const char*)(Wo + (size_t)(512 * hg) * DM + 32 * h));
    const int kp = lane >> 2, sr = 32 * (kp >> 3) + 16 * ((kp >> 2) & 1) + (kp & 3);
    auto issue = [&](int p) {
#pragma unroll
      for (int i = 0; i < 8; ++i) {
        const unsigned d = lw + (p & 1) * 8192 + i * 1024;
        if (p < 2) gldsw(sk + (size_t)(8 * p + i) * 8 * HD * 2, (unsigned)((lane >> 3) * HD * 2 + (lane & 7) * 16), d);
        else if (p < 6 && (NTM & 8)) gldsw(so + (size_t)(128 * (p - 2) + 64 * (i & 1) + 4 * (i >> 1)) * DM * 2, (unsigned)(sr * DM * 2 + p4 * 16), d);
        else if (p < 6) glds16s<0>(so + (size_t)(128 * (p - 2) + 64 * (i & 1) + 4 * (i >> 1)) * DM * 2, (unsigned)(sr * DM * 2 + p4 * 16), d);
        else if (NTM & 16) gldsw(sv_ + (size_t)i * 16 * HD * 2, (unsigned)(kp * HD * 2 + p4 * 16), d);
        else glds16s<0>(sv_ + (size_t)i * 16 * HD * 2, (unsigned)(kp * HD * 2 + p4 * 16), d);
      }
    };
#define VTS(p) do { } while (0)
    const int fm = 16 * (g >> 1) + 4 * (g & 1) + (q4 & 1) + 8 * (q4 >> 1);
    const unsigned ak = lw + fm * 128 + p4 * 8, ao = lw + fr * 64 + p4 * 8;
#define KB() do { if (KFB) WBAR(); } while (0)
    issue(2); KB(); issue(3);
    WAIT_V(8); VTS(1); kr_load_o0(W, ao); KB(); issue(4);
    WAIT_V(8); VTS(2); kr_load_o1(W, ao + 8192); KB(); issue(5);
    WAIT_V(8); VTS(3); kr_load_o2(W, ao); KB(); issue(0);
    WAIT_V(8); VTS(4); kr_load_o3(W, ao + 8192); KB(); issue(1);
    WAIT_V(8); VTS(5); kr_load_k0(W, ak); KB(); issue(6);
    WAIT_V(8); VTS(6); kr_load_k1(W, ak + 8192);
    if (wave == 0) ini_request(ws, 163328);
    WAIT_V(0);
    u32x8 x[4];
#pragma unroll
    for (int j = 0; j < 4; ++j) {
      const unsigned a = lw + fm * 64 + p4 * 8 + (j & 1) * 4096 + (j >> 1) * 32;
      const u32x2 t0 = trread(smem, a), t1 = trread(smem, a + 128), t2 = trread(smem, a + 2048), t3 = trread(smem, a + 2176);
      x[j] = u32x8{t0.x, t0.y, t1.x, t1.y, t2.x, t2.y, t3.x, t3.y};
    }
    char* d = smem + lw + 8192 + lane * 16;
#pragma unroll
    for (int j = 0; j < 4; ++j) { *(u32x4*)(d + j * 2048) = u32x4{x[j][0], x[j][1], x[j][2], x[j][3]}; *(u32x4*)(d + j * 2048 + 1024) = u32x4{x[j][4], x[j][5], x[j][6], x[j][7]}; }
    VTS(7);
  }
  BAR();
  XPTRS(smem + 163328);
  (void)cnt;
  auto rearm = [&](int ps) {
    const int lane = lane_id();
    unsigned z = SENT; asm volatile("" : "+v"(z));
    const u32x4 sv = u32x4{z, z, z, z};
    static_assert(AREP == 2, "");
    st16s(xb, lane < 32 ? (unsigned)(ps * 8 + (lane >> 4) * 2) * 16384u + po + ((lane >> 3) & 1) * 256 + (lane & 7) * 16 : (unsigned)ps * 65536u + vo_ + (lane & 31) * 16, sv);
    st16s(xb, (unsigned)ps * 131072u + ko_ + lane * 16, sv);
  };
  if (wave == 0) STS(ws, wg, 0);
  __builtin_amdgcn_s_setprio(2);
  auto loop = [&](auto cl_c) __attribute__((always_inline)) {
    constexpr int CL = decltype(cl_c)::value;
    unsigned cap = 60000, tr = 0, fc = 0, tm = 0;
    int da = D_A, dd = D_O;
    int sl = 0;
    for (int s = 0; s < NSEG; ++s) {
      TSDECL;
      const int sn = sl == 2 ? 0 : sl + 1;
      const char* ag = xb + (size_t)((sl * 4 + (AREP == 4 ? xg : xg >> 1)) * 2 + (wave >> 2)) * 16384 + (wave & 3) * 2048;
      const char* kd = xb + (size_t)sl * 131072 + ko_ + wave * 128;
      const char* vd = xb + (size_t)sl * 65536 + vo_ + (wave & 3) * 128;
      const char* os = ob + (size_t)((sl * 4 + ORSEL) * 8 + hg) * 16384 + c * 4096;
      const char* ad = xb + (size_t)(sn * 8) * 16384 + po + (wave & 1) * 256;
      if (s > 0) { if (wave < AREP) delay64<E_AG - E_W>(); else delay64<E_AG>(); }
      TS(0);
      unsigned long long tc; unsigned sp;
      unsigned long long* tsp = nullptr;
      if (KV3) (CL ? vr_w1 : vr_w0)(W, ag, CL ? vd : kd + wave * 128, wave * 16384 + 8192, V_P1 + wave * 3328 + (wave >> 2) * 64,
                                    CL ? V_P1 + 256 + ((wave >> 1) & 1) * 64 + (wave & 1) * 1664 : V_P1 + wave * 32, cap, sp, tc, tsp);
      else vr_v(W, ag, kd, vd, wave * 16384 + 8192, V_P1 + wave * 2304 + (wave >> 2) * 64, V_PV + wave * 1024 + (wave >> 2) * 128, V_P1 + (wave >> 1) * 32 + (wave & 1) * 1152,
           V_PV + (wave & 3) * 256, cap, sp, tc, tsp);
      TN(12, sp + 1);
      if (s > 3 && sp) fc += 1u;
      da = sp ? min(da + DQU, D_A + DQM) : max(da - DQD, D_A);
      if (s == NSEG - 1) break;
      if (ADQ && wave == 0 && s > 0) { const int l_ = lane_id(); if (l_ < 3) st4s(xb, (unsigned)(48 + l_) * 16384u + 4096 + h * 128 + c * 8 + 4, (tm & 0xFFFFFFu) | (unsigned)s << 24); }
      if (wave == 7 && s > 0) rearm(sl == 0 ? 2 : sl - 1);
      wait_clk((unsigned)tc, E_O);
      TS(3);
      const unsigned tb = bell_time(xb + (size_t)(48 + xg) * 16384 + hg * 512, (lane_id() & 3) * 128, (unsigned)(s + 1), cap);
      tr = tb;
      wait_rt(tb, dd);
      TS(4);
      vr_r(W, os, ad, V_P2 + wave * 768 + (wave >> 2) * 128, V_P2 + (wave & 1) * 64, cap, sp, tsp);
      if (ADQ && wave == 0) tm = (unsigned)__builtin_amdgcn_s_memrealtime();
      if (s > 3 && sp) fc += 0x10000u;
      dd = sp ? min(dd + DOU, D_O + DOM) : max(dd - DQD, D_O);
      TN(14, (sp & 0xFFFF) + 1);
      if (wave == 1) TSFLUSH(0x6078);
      sl = sn;
    }
    if (cap == 0 && lane_id() == 0) st4_sc1(tmo, 2u);
    FCS();
  };
  if (KV3 && wave >= 4) loop(std::integral_constant<int, 1>{}); else loop(std::integral_constant<int, 0>{});
  __builtin_amdgcn_s_setprio(0);
}
#define NCS 6
__device__ __forceinline__ int calibrate(char* ws, int i) {
  const int t = lane_id(), ci = (i & 15) % 3, w = (i >> 4) * 4 + (i & 15) / 3, chunk = ci == 0 ? 0 : ci + 2;
  char* base = xch_base(ws) + ((size_t)chunk << 21) + 127 * 16384 + w * 512;
  const char* sb[2] = {uni(base + (w & 1) * 8192), uni(base + (~w & 1) * 8192)};
  if ((t & 31) < 24) st16_sc1(base + (t >> 5) * 8192 + (t & 31) * 16, u32x4{SENT, SENT, SENT, SENT});
  unsigned v, best[2] = {1u << 30, 1u << 30};
  asm volatile("s_load_dword %0, %1, 384 glc\n\ts_waitcnt vmcnt(0) lgkmcnt(0)" : "=&s"(v) : "s"(sb[0]) : "memory");
#pragma unroll
  for (int r = 0; r < NCS; ++r) {
    unsigned long long t0, t1;
    asm volatile("s_memtime %0\n\ts_waitcnt lgkmcnt(0)\n\ts_load_dword %2, %3, %4 glc\n\ts_waitcnt lgkmcnt(0)\n\ts_memtime %1\n\ts_waitcnt lgkmcnt(0)"
                 : "=&s"(t0), "=&s"(t1), "=&s"(v) : "s"(sb[r & 1]), "i"((r >> 1) * 128) : "memory");
    best[r & 1] = min(best[r & 1], (unsigned)(t1 - t0));
  }
  return (w & 1) ? (int)best[0] - (int)best[1] : (int)best[1] - (int)best[0];
}
__device__ __forceinline__ void init_sums(char* smem, int t, int i, const u16* __restrict__ cvec, const u16* __restrict__ wmod) {
  const int cg = t & 15, r0 = t >> 4, k0 = (i >> 4) * 256 + r0 * 16;
  u32x4 w[16];
#pragma unroll
  for (int j = 0; j < 16; ++j) w[j] = ldnt<1>(wmod + (size_t)(k0 + j) * (6 * DM) + (i & 15) * 128 + cg * 8);
  const u32x4 c0 = *(const u32x4*)(cvec + k0), c1 = *(const u32x4*)(cvec + k0 + 8);
  f32x2 acc[4] = {f32x2{0, 0}, f32x2{0, 0}, f32x2{0, 0}, f32x2{0, 0}};
#pragma unroll
  for (int j = 0; j < 16; ++j) {
    const unsigned cw = j < 8 ? c0[(j >> 1) & 3] : c1[(j >> 1) & 3];
    const float c = (j & 1) ? bfhi(cw) : bflo(cw), sv = bfr(c / (1.0f + __expf(-c)));
#pragma unroll
    for (int e = 0; e < 4; ++e) acc[e] = pkfma(f32x2{sv, sv}, f32x2{bflo(w[j][e]), bfhi(w[j][e])}, acc[e]);
  }
  float* red = (float*)smem;
  *(f32x4*)(red + r0 * 128 + cg * 8) = f32x4{acc[0][0], acc[0][1], acc[1][0], acc[1][1]};
  *(f32x4*)(red + r0 * 128 + cg * 8 + 4) = f32x4{acc[2][0], acc[2][1], acc[3][0], acc[3][1]};
}
__device__ __forceinline__ void init_role(char* smem, int wave_id_, int i, const u16* __restrict__ mem0, char* ws, unsigned long long tag, unsigned vote) {
  const int t = opaque_tid();
  char* xch = xch_base(ws);
  if (t < 128) {
    const float* red = (const float*)smem;
    float s = 0;
#pragma unroll
    for (int r = 0; r < 16; ++r) s += red[r * 128 + t];
    st4_sc1(ws + OFF_MOD + ((i >> 4) * (2 * DM) + (i & 15) * 128 + t) * 4, __builtin_bit_cast(unsigned, s));
  }
  static_assert(OREP == 2 && AREP == 2, "");
  for (int b = i; b < 105; b += 64) {
    if (b < 101) {
      char* p = b < 48 ? xch + (size_t)((b >> 4) * 32 + (b & 15)) * 16384
              : (b < 56 ? xch + ((size_t)3 << 21) + (size_t)(8 + ((b - 48) >> 2) * 8 + (b & 3)) * 16384
              : (b < 96 ? xch + ((size_t)3 << 21) + (size_t)(b - 56 + 24) * 16384 : xch + ((size_t)4 << 21) + (size_t)(b - 96) * 16384));
      const unsigned v = b < 80 || (b >= 84 && b < 96) ? SENT : 0u;
#pragma unroll
      for (int j = 0; j < 2; ++j) st16_sc1(p + j * 8192 + t * 16, u32x4{v, v, v, v});
    } else {
      const int rep = (b - 101) & 1, par = (b - 101) >> 1;
      if (b == 101 && t == 0) st4_sc1(ws + OFF_TMO, 0u);
#pragma unroll
      for (int j = 0; j < 2; ++j) {
        const int u = t + 512 * j, lane = u & 63, g = lane >> 4, hf = (lane >> 1) & 1, row = ((lane >> 1) & 6) + (lane & 1);
        u32x4 v = *(const u32x4*)(mem0 + row * DM + 64 * (u >> 6) + 16 * g + 8 * hf);
        v.x = nosent(v.x); v.y = nosent(v.y); v.z = nosent(v.z); v.w = nosent(v.w);
        st16_sc1(xch + ((size_t)3 << 21) + (size_t)(rep * 2 + j) * 16384 + par * 8192 + t * 16, v);
      }
    }
  }
  if (wave_id_ == 7) IS(4);
  WAIT_V(0);
  BAR();
  if (wave_id_ == 7) IS(5);
  if (t == 0) st8_sc1(ws + OFF_INI + i * 128, u32x2{(unsigned)tag, ((unsigned)(tag >> 48) << 16) | vote});
}
#define GEMMX gemm7
#define CPF 136
#define PFD 2
#define PFW 0
#define NIMM 1
enum { T_NONE, T_QV, T_KR, T_WO, T_F1, T_F2, T_F2C, T_FIN, T_LN, T_SA };
__device__ __attribute__((aligned(16))) const unsigned ROLES[256][6] = {
  {0x1, 0x0, 0x0, 0x0, 0x0, 0x0},
  {0x11, 0x0, 0x0, 0x0, 0x0, 0x0},
  {0x21, 0x0, 0x0, 0x0, 0x0, 0x0},
  {0x31, 0x0, 0x0, 0x0, 0x0, 0x0},
  {0x41, 0x0, 0x0, 0x0, 0x0, 0x0},
  {0x51, 0x0, 0x0, 0x0, 0x0, 0x0},
  {0x61, 0x0, 0x0, 0x0, 0x0, 0x0},
  {0x71, 0x0, 0x0, 0x0, 0x0, 0x0},
  {0x81, 0x0, 0x0, 0x0, 0x0, 0x0},
  {0x91, 0x0, 0x0, 0x0, 0x0, 0x0},
  {0xa1, 0x0, 0x0, 0x0, 0x0, 0x0},
  {0xb1, 0x0, 0x0, 0x0, 0x0, 0x0},
  {0x2, 0x0, 0x0, 0x0, 0x0, 0x0},
  {0x12, 0x0, 0x0, 0x0, 0x0, 0x0},
  {0x22, 0x0, 0x0, 0x0, 0x0, 0x0},
  {0x32, 0x0, 0x0, 0x0, 0x0, 0x0},
  {0x42, 0x0, 0x0, 0x0, 0x0, 0x0},
  {0x52, 0x0, 0x0, 0x0, 0x0, 0x0},
  {0x62, 0x0, 0x0, 0x0, 0x0, 0x0},
  {0x72, 0x0, 0x0, 0x0, 0x0, 0x0},
  {0x82, 0x0, 0x0, 0x0, 0x0, 0x0},
  {0x92, 0x0, 0x0, 0x0, 0x0, 0x0},
  {0xa2, 0x0, 0x0, 0x0, 0x0, 0x0},
  {0xb2, 0x0, 0x0, 0x0, 0x0, 0x0},
  {0xc2, 0x0, 0x0, 0x0, 0x0, 0x0},
  {0xd2, 0x0, 0x0, 0x0, 0x0, 0x0},
  {0xe2, 0x0, 0x0, 0x0, 0x0, 0x0},
  {0xf2, 0x0, 0x0, 0x0, 0x0, 0x0},
  {0x102, 0x0, 0x0, 0x0, 0x0, 0x0},
  {0x112, 0x0, 0x0, 0x0, 0x0, 0x0},
  {0x122, 0x0, 0x0, 0x0, 0x0, 0x0},
  {0x132, 0x0, 0x0, 0x0, 0x0, 0x0},
  {0xc1, 0x0, 0x0, 0x0, 0x0, 0x0},
  {0xd1, 0x0, 0x0, 0x0, 0x0, 0x0},
  {0xe1, 0x0, 0x0, 0x0, 0x0, 0x0},
  {0xf1, 0x0, 0x0, 0x0, 0x0, 0x0},
  {0x101, 0x0, 0x0, 0x0, 0x0, 0x0},
  {0x111, 0x0, 0x0, 0x0, 0x0, 0x0},
  {0x121, 0x0, 0x0, 0x0, 0x0, 0x0},
  {0x131, 0x0, 0x0, 0x0, 0x0, 0x0},
  {0x142, 0x0, 0x0, 0x0, 0x0, 0x0},
  {0x152, 0x0, 0x0, 0x0, 0x0, 0x0},
  {0x162, 0x0, 0x0, 0x0, 0x0, 0x0},
  {0x172, 0x0, 0x0, 0x0, 0x0, 0x0},
  {0x182, 0x0, 0x0, 0x0, 0x0, 0x0},
  {0x192, 0x0, 0x0, 0x0, 0x0, 0x0},
  {0x1a2, 0x0, 0x0, 0x0, 0x0, 0x0},
  {0x1b2, 0x0, 0x0, 0x0, 0x0, 0x0},
  {0x1c2, 0x0, 0x0, 0x0, 0x0, 0x0},
  {0x1d2, 0x0, 0x0, 0x0, 0x0, 0x0},
  {0x1e2, 0x0, 0x0, 0x0, 0x0, 0x0},
  {0x1f2, 0x0, 0x0, 0x0, 0x0, 0x0},
  {0x202, 0x0, 0x0, 0x0, 0x0, 0x0},
  {0x212, 0x0, 0x0, 0x0, 0x0, 0x0},
  {0x222, 0x0, 0x0, 0x0, 0x0, 0x0},
  {0x232, 0x0, 0x0, 0x0, 0x0, 0x0},
  {0x242, 0x0, 0x0, 0x0, 0x0, 0x0},
  {0x252, 0x0, 0x0, 0x0, 0x0, 0x0},
  {0x262, 0x0, 0x0, 0x0, 0x0, 0x0},
  {0x272, 0x0, 0x0, 0x0, 0x0, 0x0},
  {0x282, 0x0, 0x0, 0x0, 0x0, 0x0},
  {0x292, 0x0, 0x0, 0x0, 0x0, 0x0},
  {0x2a2, 0x0, 0x0, 0x0, 0x0, 0x0},
  {0x2b2, 0x0, 0x0, 0x0, 0x0, 0x0},
  {0x141, 0x0, 0x0, 0x0, 0x0, 0x0},
  {0x151, 0x0, 0x0, 0x0, 0x0, 0x0},
  {0x161, 0x0, 0x0, 0x0, 0x0, 0x0},
  {0x171, 0x0, 0x0, 0x0, 0x0, 0x0},
  {0x181, 0x0, 0x0, 0x0, 0x0, 0x0},
  {0x191, 0x0, 0x0, 0x0, 0x0, 0x0},
  {0x1a1, 0x0, 0x0, 0x0, 0x0, 0x0},
  {0x1b1, 0x0, 0x0, 0x0, 0x0, 0x0},
  {0x1c1, 0x0, 0x0, 0x0, 0x0, 0x0},
  {0x1d1, 0x0, 0x0, 0x0, 0x0, 0x0},
  {0x1e1, 0x0, 0x0, 0x0, 0x0, 0x0},
  {0x1f1, 0x0, 0x0, 0x0, 0x0, 0x0},
  {0x2c2, 0x0, 0x0, 0x0, 0x0, 0x0},
  {0x2d2, 0x0, 0x0, 0x0, 0x0, 0x0},
  {0x2e2, 0x0, 0x0, 0x0, 0x0, 0x0},
  {0x2f2, 0x0, 0x0, 0x0, 0x0, 0x0},
  {0x302, 0x0, 0x0, 0x0, 0x0, 0x0},
  {0x312, 0x0, 0x0, 0x0, 0x0, 0x0},
  {0x322, 0x0, 0x0, 0x0, 0x0, 0x0},
  {0x332, 0x0, 0x0, 0x0, 0x0, 0x0},
  {0x342, 0x0, 0x0, 0x0, 0x0, 0x0},
  {0x352, 0x0, 0x0, 0x0, 0x0, 0x0},
  {0x362, 0x0, 0x0, 0x0, 0x0, 0x0},
  {0x372, 0x0, 0x0, 0x0, 0x0, 0x0},
  {0x382, 0x0, 0x0, 0x0, 0x0, 0x0},
  {0x392, 0x0, 0x0, 0x0, 0x0, 0x0},
  {0x3a2, 0x0, 0x0, 0x0, 0x0, 0x0},
  {0x3b2, 0x0, 0x0, 0x0, 0x0, 0x0},
  {0x3c2, 0x0, 0x0, 0x0, 0x0, 0x0},
  {0x3d2, 0x0, 0x0, 0x0, 0x0, 0x0},
  {0x3e2, 0x0, 0x0, 0x0, 0x0, 0x0},
  {0x3f2, 0x0, 0x0, 0x0, 0x0, 0x0},
  {0x100005, 0x2e0000, 0x0, 0x0, 0x0, 0x0},
  {0x100015, 0x2e0004, 0x0, 0x0, 0x0, 0x0},
  {0x100025, 0x2e0008, 0x0, 0x0, 0x0, 0x0},
  {0x100035, 0x2e000c, 0x0, 0x0, 0x0, 0x0},
  {0x100045, 0x2e0010, 0x0, 0x0, 0x0, 0x0},
  {0x100055, 0x2e0014, 0x0, 0x0, 0x0, 0x0},
  {0x100065, 0x2e0018, 0x0, 0x0, 0x0, 0x0},
  {0x200037, 0x2e001c, 0x140291e, 0x0, 0x0, 0x0},
  {0x100075, 0x2e0020, 0x0, 0x0, 0x0, 0x0},
  {0x100085, 0x2e0024, 0x0, 0x0, 0x0, 0x0},
  {0x100095, 0x2e0028, 0x0, 0x0, 0x0, 0x0},
  {0x1000a5, 0x2e002c, 0x0, 0x0, 0x0, 0x0},
  {0x1000b5, 0x2e0030, 0x0, 0x0, 0x0, 0x0},
  {0x1000c5, 0x2e0034, 0x0, 0x0, 0x0, 0x0},
  {0x1000d5, 0x2e0038, 0x0, 0x0, 0x0, 0x0},
  {0x200077, 0x2e003c, 0x140293e, 0x0, 0x0, 0x0},
  {0x1000e5, 0x2e0040, 0x0, 0x0, 0x0, 0x0},
  {0x1000f5, 0x2e0044, 0x0, 0x0, 0x0, 0x0},
  {0x100105, 0x2e0048, 0x0, 0x0, 0x0, 0x0},
  {0x100115, 0x2e004c, 0x0, 0x0, 0x0, 0x0},
  {0x100125, 0x2e0050, 0x0, 0x0, 0x0, 0x0},
  {0x100135, 0x2e0054, 0x0, 0x0, 0x0, 0x0},
  {0x100145, 0x2e0058, 0x0, 0x0, 0x0, 0x0},
  {0x2000b7, 0x2e005c, 0x140295e, 0x0, 0x0, 0x0},
  {0x100155, 0x2e0060, 0x0, 0x0, 0x0, 0x0},
  {0x100165, 0x2e0064, 0x0, 0x0, 0x0, 0x0},
  {0x100175, 0x2e0068, 0x0, 0x0, 0x0, 0x0},
  {0x100006, 0x2e006c, 0x0, 0x0, 0x0, 0x0},
  {0x100016, 0x2e0070, 0x0, 0x0, 0x0, 0x0},
  {0x100026, 0x2e0074, 0x0, 0x0, 0x0, 0x0},
  {0x100036, 0x2e0078, 0x0, 0x0, 0x0, 0x0},
  {0x2000f7, 0x2e007c, 0x140297e, 0x0, 0x0, 0x0},
  {0x100034, 0x5402902, 0x0, 0x0, 0x0, 0x0},
  {0x1000b4, 0x5402906, 0x0, 0x0, 0x0, 0x0},
  {0x100004, 0x540290a, 0x0, 0x0, 0x0, 0x0},
  {0x100014, 0x540290e, 0x0, 0x0, 0x0, 0x0},
  {0x100003, 0x5402e00, 0x0, 0x0, 0x0, 0x0},
  {0x100013, 0x5402e04, 0x0, 0x0, 0x0, 0x0},
  {0x100023, 0x5402e08, 0x0, 0x0, 0x0, 0x0},
  {0x100033, 0x5402e0c, 0x0, 0x0, 0x0, 0x0},
  {0x200007, 0x350001, 0x1402912, 0x0, 0x0, 0x0},
  {0x200017, 0x350005, 0x1402916, 0x0, 0x0, 0x0},
  {0x200027, 0x350009, 0x1402919, 0x0, 0x0, 0x0},
  {0x200000, 0x35000d, 0x140291a, 0x0, 0x0, 0x0},
  {0x200000, 0x350011, 0x140291d, 0x0, 0x0, 0x0},
  {0x200000, 0x400015, 0x1402e18, 0x0, 0x0, 0x0},
  {0x100024, 0x290019, 0x0, 0x0, 0x0, 0x0},
  {0x100044, 0x29001d, 0x0, 0x0, 0x0, 0x0},
  {0x100054, 0x290002, 0x0, 0x0, 0x0, 0x0},
  {0x100064, 0x290006, 0x0, 0x0, 0x0, 0x0},
  {0x100074, 0x29000a, 0x0, 0x0, 0x0, 0x0},
  {0x100084, 0x29000e, 0x0, 0x0, 0x0, 0x0},
  {0x100094, 0x290012, 0x0, 0x0, 0x0, 0x0},
  {0x1000a4, 0x290016, 0x0, 0x0, 0x0, 0x0},
  {0x1000c4, 0x29001a, 0x0, 0x0, 0x0, 0x0},
  {0x1000d4, 0x29001e, 0x0, 0x0, 0x0, 0x0},
  {0x101043, 0x1403501, 0x0, 0x0, 0x0, 0x0},
  {0x102053, 0x140350d, 0x0, 0x0, 0x0, 0x0},
  {0x103063, 0x1403505, 0x0, 0x0, 0x0, 0x0},
  {0x104073, 0x1403509, 0x0, 0x0, 0x0, 0x0},
  {0x105008, 0x1402e10, 0x0, 0x0, 0x0, 0x0},
  {0x106088, 0x1403511, 0x0, 0x0, 0x0, 0x0},
  {0x107018, 0x1402e14, 0x0, 0x0, 0x0, 0x0},
  {0x108098, 0x1402e1c, 0x0, 0x0, 0x0, 0x0},
  {0x100134, 0x5402922, 0x0, 0x0, 0x0, 0x0},
  {0x1001b4, 0x5402926, 0x0, 0x0, 0x0, 0x0},
  {0x1000e4, 0x540292a, 0x0, 0x0, 0x0, 0x0},
  {0x1000f4, 0x540292e, 0x0, 0x0, 0x0, 0x0},
  {0x100083, 0x5402e20, 0x0, 0x0, 0x0, 0x0},
  {0x100093, 0x5402e24, 0x0, 0x0, 0x0, 0x0},
  {0x1000a3, 0x5402e28, 0x0, 0x0, 0x0, 0x0},
  {0x1000b3, 0x5402e2c, 0x0, 0x0, 0x0, 0x0},
  {0x200047, 0x350021, 0x1402932, 0x0, 0x0, 0x0},
  {0x200057, 0x350025, 0x1402936, 0x0, 0x0, 0x0},
  {0x200067, 0x350029, 0x1402939, 0x0, 0x0, 0x0},
  {0x200000, 0x35002d, 0x140293a, 0x0, 0x0, 0x0},
  {0x200000, 0x350031, 0x140293d, 0x0, 0x0, 0x0},
  {0x200000, 0x400035, 0x1402e38, 0x0, 0x0, 0x0},
  {0x100104, 0x290039, 0x0, 0x0, 0x0, 0x0},
  {0x100114, 0x29003d, 0x0, 0x0, 0x0, 0x0},
  {0x100124, 0x290022, 0x0, 0x0, 0x0, 0x0},
  {0x100144, 0x290026, 0x0, 0x0, 0x0, 0x0},
  {0x100154, 0x29002a, 0x0, 0x0, 0x0, 0x0},
  {0x100164, 0x29002e, 0x0, 0x0, 0x0, 0x0},
  {0x100174, 0x290032, 0x0, 0x0, 0x0, 0x0},
  {0x100184, 0x290036, 0x0, 0x0, 0x0, 0x0},
  {0x100194, 0x29003a, 0x0, 0x0, 0x0, 0x0},
  {0x1001a4, 0x29003e, 0x0, 0x0, 0x0, 0x0},
  {0x1090c3, 0x1403521, 0x0, 0x0, 0x0, 0x0},
  {0x10a0d3, 0x140352d, 0x0, 0x0, 0x0, 0x0},
  {0x10b0e3, 0x1403525, 0x0, 0x0, 0x0, 0x0},
  {0x10c0f3, 0x1403529, 0x0, 0x0, 0x0, 0x0},
  {0x10d028, 0x1402e30, 0x0, 0x0, 0x0, 0x0},
  {0x10e0a8, 0x1403531, 0x0, 0x0, 0x0, 0x0},
  {0x10f038, 0x1402e34, 0x0, 0x0, 0x0, 0x0},
  {0x1100b8, 0x1402e3c, 0x0, 0x0, 0x0, 0x0},
  {0x100234, 0x5402942, 0x0, 0x0, 0x0, 0x0},
  {0x1001c4, 0x5402946, 0x0, 0x0, 0x0, 0x0},
  {0x1001d4, 0x540294a, 0x0, 0x0, 0x0, 0x0},
  {0x1001e4, 0x540294e, 0x0, 0x0, 0x0, 0x0},
  {0x100103, 0x5402e40, 0x0, 0x0, 0x0, 0x0},
  {0x100113, 0x5402e44, 0x0, 0x0, 0x0, 0x0},
  {0x100123, 0x5402e48, 0x0, 0x0, 0x0, 0x0},
  {0x100133, 0x5402e4c, 0x0, 0x0, 0x0, 0x0},
  {0x200087, 0x350041, 0x1402952, 0x0, 0x0, 0x0},
  {0x200097, 0x350045, 0x1402956, 0x0, 0x0, 0x0},
  {0x2000a7, 0x350049, 0x1402959, 0x0, 0x0, 0x0},
  {0x200000, 0x35004d, 0x140295a, 0x0, 0x0, 0x0},
  {0x200000, 0x350051, 0x140295d, 0x0, 0x0, 0x0},
  {0x200000, 0x400055, 0x1402e58, 0x0, 0x0, 0x0},
  {0x1001f4, 0x290059, 0x0, 0x0, 0x0, 0x0},
  {0x100204, 0x29005d, 0x0, 0x0, 0x0, 0x0},
  {0x100214, 0x290042, 0x0, 0x0, 0x0, 0x0},
  {0x100224, 0x290046, 0x0, 0x0, 0x0, 0x0},
  {0x100244, 0x29004a, 0x0, 0x0, 0x0, 0x0},
  {0x100254, 0x29004e, 0x0, 0x0, 0x0, 0x0},
  {0x100264, 0x290052, 0x0, 0x0, 0x0, 0x0},
  {0x100274, 0x290056, 0x0, 0x0, 0x0, 0x0},
  {0x100284, 0x29005a, 0x0, 0x0, 0x0, 0x0},
  {0x100294, 0x29005e, 0x0, 0x0, 0x0, 0x0},
  {0x111143, 0x1403541, 0x0, 0x0, 0x0, 0x0},
  {0x112153, 0x140354d, 0x0, 0x0, 0x0, 0x0},
  {0x113163, 0x1403545, 0x0, 0x0, 0x0, 0x0},
  {0x114173, 0x1403549, 0x0, 0x0, 0x0, 0x0},
  {0x115048, 0x1402e50, 0x0, 0x0, 0x0, 0x0},
  {0x1160c8, 0x1403551, 0x0, 0x0, 0x0, 0x0},
  {0x117058, 0x1402e54, 0x0, 0x0, 0x0, 0x0},
  {0x1180d8, 0x1402e5c, 0x0, 0x0, 0x0, 0x0},
  {0x1002b4, 0x5402962, 0x0, 0x0, 0x0, 0x0},
  {0x100334, 0x5402966, 0x0, 0x0, 0x0, 0x0},
  {0x1002a4, 0x540296a, 0x0, 0x0, 0x0, 0x0},
  {0x1002c4, 0x540296e, 0x0, 0x0, 0x0, 0x0},
  {0x100183, 0x5402e60, 0x0, 0x0, 0x0, 0x0},
  {0x100193, 0x5402e64, 0x0, 0x0, 0x0, 0x0},
  {0x1001a3, 0x5402e68, 0x0, 0x0, 0x0, 0x0},
  {0x1001b3, 0x5402e6c, 0x0, 0x0, 0x0, 0x0},
  {0x2000c7, 0x350061, 0x1402972, 0x0, 0x0, 0x0},
  {0x2000d7, 0x350065, 0x1402976, 0x0, 0x0, 0x0},
  {0x2000e7, 0x350069, 0x1402979, 0x0, 0x0, 0x0},
  {0x200000, 0x35006d, 0x140297a, 0x0, 0x0, 0x0},
  {0x200000, 0x350071, 0x140297d, 0x0, 0x0, 0x0},
  {0x200000, 0x400075, 0x1402e78, 0x0, 0x0, 0x0},
  {0x1002d4, 0x290079, 0x0, 0x0, 0x0, 0x0},
  {0x1002e4, 0x29007d, 0x0, 0x0, 0x0, 0x0},
  {0x1002f4, 0x290062, 0x0, 0x0, 0x0, 0x0},
  {0x100304, 0x290066, 0x0, 0x0, 0x0, 0x0},
  {0x100314, 0x29006a, 0x0, 0x0, 0x0, 0x0},
  {0x100324, 0x29006e, 0x0, 0x0, 0x0, 0x0},
  {0x100344, 0x290072, 0x0, 0x0, 0x0, 0x0},
  {0x100354, 0x290076, 0x0, 0x0, 0x0, 0x0},
  {0x100364, 0x29007a, 0x0, 0x0, 0x0, 0x0},
  {0x100374, 0x29007e, 0x0, 0x0, 0x0, 0x0},
  {0x1191c3, 0x1403561, 0x0, 0x0, 0x0, 0x0},
  {0x11a1d3, 0x140356d, 0x0, 0x0, 0x0, 0x0},
  {0x11b1e3, 0x1403565, 0x0, 0x0, 0x0, 0x0},
  {0x11c1f3, 0x1403569, 0x0, 0x0, 0x0, 0x0},
  {0x11d068, 0x1402e70, 0x0, 0x0, 0x0, 0x0},
  {0x11e0e8, 0x1403571, 0x0, 0x0, 0x0, 0x0},
  {0x11f078, 0x1402e74, 0x0, 0x0, 0x0, 0x0},
  {0x1200f8, 0x1402e7c, 0x0, 0x0, 0x0, 0x0},
};
constexpr unsigned NQV = 0xc080c;
__device__ __attribute__((aligned(16))) const unsigned PFT[8][128] = {
  {0x40000f, 0x40000e, 0x0, 0x0, 0x41000f, 0x41000e, 0x0, 0x0, 0x42000f, 0x42000e, 0x0, 0x0, 0x43000f, 0x43000e, 0x0, 0x0, 0x44000f, 0x40002d, 0x0, 0x0, 0x45000f, 0x41002d, 0x0, 0x0, 0x46000f, 0x42002d, 0x0, 0x0, 0x47000f, 0x43002d, 0x0, 0x0, 0x48000f, 0x40002e, 0x0, 0x0, 0x49000f, 0x41002e, 0x0, 0x0, 0x4a000f, 0x42002e, 0x0, 0x0, 0x4b000f, 0x43002e, 0x0, 0x0, 0x4c000f, 0x40004d, 0x0, 0x0, 0x4d000f, 0x41004d, 0x0, 0x0, 0x4e000f, 0x42004d, 0x0, 0x0, 0x4f000f, 0x43004d, 0x0, 0x0, 0x40000c, 0x40004e, 0x0, 0x0, 0x41000c, 0x41004e, 0x0, 0x0, 0x42000c, 0x42004e, 0x0, 0x0, 0x43000c, 0x43004e, 0x0, 0x0, 0x40002c, 0x400003, 0x0, 0x0, 0x41002c, 0x400007, 0x0, 0x0, 0x42002c, 0x40000b, 0x0, 0x0, 0x43002c, 0x400013, 0x0, 0x0, 0x40004c, 0x420003, 0x0, 0x0, 0x41004c, 0x420007, 0x0, 0x0, 0x42004c, 0x42000b, 0x0, 0x0, 0x43004c, 0x420013, 0x0, 0x0, 0x40000d, 0x440003, 0x0, 0x0, 0x41000d, 0x440007, 0x0, 0x0, 0x42000d, 0x44000b, 0x0, 0x0, 0x43000d, 0x440013, 0x0, 0x0},
  {0x40006c, 0x400017, 0x0, 0x0, 0x41006c, 0x40001b, 0x0, 0x0, 0x42006c, 0x40001f, 0x0, 0x0, 0x43006c, 0x400023, 0x0, 0x0, 0x40008c, 0x400027, 0x0, 0x0, 0x41008c, 0x40002b, 0x0, 0x0, 0x42008c, 0x420017, 0x0, 0x0, 0x43008c, 0x42001b, 0x0, 0x0, 0x40006d, 0x42001f, 0x0, 0x0, 0x41006d, 0x420023, 0x0, 0x0, 0x42006d, 0x420027, 0x0, 0x0, 0x43006d, 0x42002b, 0x0, 0x0, 0x40006e, 0x440017, 0x0, 0x0, 0x41006e, 0x44001b, 0x0, 0x0, 0x42006e, 0x44001f, 0x0, 0x0, 0x43006e, 0x440023, 0x0, 0x0, 0x40008d, 0x440027, 0x0, 0x0, 0x41008d, 0x44002b, 0x0, 0x0, 0x42008d, 0x460017, 0x0, 0x0, 0x43008d, 0x46001b, 0x0, 0x0, 0x40008e, 0x46001f, 0x0, 0x0, 0x41008e, 0x460023, 0x0, 0x0, 0x42008e, 0x460027, 0x0, 0x0, 0x43008e, 0x46002b, 0x0, 0x0, 0x4000ad, 0x480017, 0x0, 0x0, 0x4100ad, 0x48001b, 0x0, 0x0, 0x4200ad, 0x48001f, 0x0, 0x0, 0x4300ad, 0x480023, 0x0, 0x0, 0x4000ae, 0x480027, 0x0, 0x0, 0x4100ae, 0x48002b, 0x0, 0x0, 0x4200ae, 0x4a0017, 0x0, 0x0, 0x4300ae, 0x4a001b, 0x0, 0x0},
  {0x40002f, 0x4000ce, 0x0, 0x0, 0x41002f, 0x4100ce, 0x0, 0x0, 0x42002f, 0x4200ce, 0x0, 0x0, 0x43002f, 0x4300ce, 0x0, 0x0, 0x44002f, 0x4000ed, 0x0, 0x0, 0x45002f, 0x4100ed, 0x0, 0x0, 0x46002f, 0x4200ed, 0x0, 0x0, 0x47002f, 0x4300ed, 0x0, 0x0, 0x48002f, 0x4000ee, 0x0, 0x0, 0x49002f, 0x4100ee, 0x0, 0x0, 0x4a002f, 0x4200ee, 0x0, 0x0, 0x4b002f, 0x4300ee, 0x0, 0x0, 0x4c002f, 0x4000fe, 0x0, 0x0, 0x4d002f, 0x4100fe, 0x0, 0x0, 0x4e002f, 0x4200fe, 0x0, 0x0, 0x4f002f, 0x4300fe, 0x0, 0x0, 0x4000ac, 0x4000fd, 0x0, 0x0, 0x4100ac, 0x4100fd, 0x0, 0x0, 0x4200ac, 0x4200fd, 0x0, 0x0, 0x4300ac, 0x4300fd, 0x0, 0x0, 0x4000cc, 0x40003f, 0x0, 0x0, 0x4100cc, 0x41003f, 0x0, 0x0, 0x4200cc, 0x42003f, 0x0, 0x0, 0x4300cc, 0x43003f, 0x0, 0x0, 0x4000ec, 0x44003f, 0x0, 0x0, 0x4100ec, 0x45003f, 0x0, 0x0, 0x4200ec, 0x46003f, 0x0, 0x0, 0x4300ec, 0x47003f, 0x0, 0x0, 0x4000cd, 0x48003f, 0x0, 0x0, 0x4100cd, 0x49003f, 0x0, 0x0, 0x4200cd, 0x4a003f, 0x0, 0x0, 0x4300cd, 0x4b003f, 0x0, 0x0},
  {0x40000c, 0x0, 0x0, 0x0, 0x41000c, 0x0, 0x0, 0x0, 0x42000c, 0x0, 0x0, 0x0, 0x43000c, 0x0, 0x0, 0x0, 0x40002c, 0x0, 0x0, 0x0, 0x41002c, 0x0, 0x0, 0x0, 0x42002c, 0x0, 0x0, 0x0, 0x43002c, 0x0, 0x0, 0x0, 0x40004c, 0x0, 0x0, 0x0, 0x41004c, 0x0, 0x0, 0x0, 0x42004c, 0x0, 0x0, 0x0, 0x43004c, 0x0, 0x0, 0x0, 0x40006c, 0x0, 0x0, 0x0, 0x41006c, 0x0, 0x0, 0x0, 0x42006c, 0x0, 0x0, 0x0, 0x43006c, 0x0, 0x0, 0x0, 0x40008c, 0x0, 0x0, 0x0, 0x41008c, 0x0, 0x0, 0x0, 0x42008c, 0x0, 0x0, 0x0, 0x43008c, 0x0, 0x0, 0x0, 0x4000ac, 0x0, 0x0, 0x0, 0x4100ac, 0x0, 0x0, 0x0, 0x4200ac, 0x0, 0x0, 0x0, 0x4300ac, 0x0, 0x0, 0x0, 0x4000cc, 0x0, 0x0, 0x0, 0x4100cc, 0x0, 0x0, 0x0, 0x4200cc, 0x0, 0x0, 0x0, 0x4300cc, 0x0, 0x0, 0x0, 0x4000ec, 0x0, 0x0, 0x0, 0x4100ec, 0x0, 0x0, 0x0, 0x4200ec, 0x0, 0x0, 0x0, 0x4300ec, 0x0, 0x0, 0x0},
  {0x0, 0x0, 0x0, 0x0, 0x0, 0x0, 0x0, 0x0, 0x0, 0x0, 0x0, 0x0, 0x0, 0x0, 0x0, 0x0, 0x0, 0x0, 0x0, 0x0, 0x0, 0x0, 0x0, 0x0, 0x0, 0x0, 0x0, 0x0, 0x0, 0x0, 0x0, 0x0, 0x40000d, 0x0, 0x0, 0x0, 0x41000d, 0x0, 0x0, 0x0, 0x42000d, 0x0, 0x0, 0x0, 0x43000d, 0x0, 0x0, 0x0, 0x40002d, 0x0, 0x0, 0x0, 0x41002d, 0x0, 0x0, 0x0, 0x42002d, 0x0, 0x0, 0x0, 0x43002d, 0x0, 0x0, 0x0, 0x40000e, 0x0, 0x0, 0x0, 0x41000e, 0x0, 0x0, 0x0, 0x42000e, 0x0, 0x0, 0x0, 0x43000e, 0x0, 0x0, 0x0, 0x40002e, 0x0, 0x0, 0x0, 0x41002e, 0x0, 0x0, 0x0, 0x42002e, 0x0, 0x0, 0x0, 0x43002e, 0x0, 0x0, 0x0, 0x0, 0x0, 0x0, 0x0, 0x0, 0x0, 0x0, 0x0, 0x0, 0x0, 0x0, 0x0, 0x0, 0x0, 0x0, 0x0, 0x0, 0x0, 0x0, 0x0, 0x0, 0x0, 0x0, 0x0, 0x0, 0x0, 0x0, 0x0, 0x0, 0x0, 0x0, 0x0},
  {0x0, 0x0, 0x0, 0x0, 0x0, 0x0, 0x0, 0x0, 0x0, 0x0, 0x0, 0x0, 0x0, 0x0, 0x0, 0x0, 0x0, 0x0, 0x0, 0x0, 0x0, 0x0, 0x0, 0x0, 0x0, 0x0, 0x0, 0x0, 0x0, 0x0, 0x0, 0x0, 0x40004d, 0x0, 0x0, 0x0, 0x41004d, 0x0, 0x0, 0x0, 0x42004d, 0x0, 0x0, 0x0, 0x43004d, 0x0, 0x0, 0x0, 0x40006d, 0x0, 0x0, 0x0, 0x41006d, 0x0, 0x0, 0x0, 0x42006d, 0x0, 0x0, 0x0, 0x43006d, 0x0, 0x0, 0x0, 0x40004e, 0x0, 0x0, 0x0, 0x41004e, 0x0, 0x0, 0x0, 0x42004e, 0x0, 0x0, 0x0, 0x43004e, 0x0, 0x0, 0x0, 0x40006e, 0x0, 0x0, 0x0, 0x41006e, 0x0, 0x0, 0x0, 0x42006e, 0x0, 0x0, 0x0, 0x43006e, 0x0, 0x0, 0x0, 0x0, 0x0, 0x0, 0x0, 0x0, 0x0, 0x0, 0x0, 0x0, 0x0, 0x0, 0x0, 0x0, 0x0, 0x0, 0x0, 0x0, 0x0, 0x0, 0x0, 0x0, 0x0, 0x0, 0x0, 0x0, 0x0, 0x0, 0x0, 0x0, 0x0, 0x0, 0x0},
  {0x0, 0x0, 0x0, 0x0, 0x0, 0x0, 0x0, 0x0, 0x0, 0x0, 0x0, 0x0, 0x0, 0x0, 0x0, 0x0, 0x0, 0x0, 0x0, 0x0, 0x0, 0x0, 0x0, 0x0, 0x0, 0x0, 0x0, 0x0, 0x0, 0x0, 0x0, 0x0, 0x40008d, 0x0, 0x0, 0x0, 0x41008d, 0x0, 0x0, 0x0, 0x42008d, 0x0, 0x0, 0x0, 0x43008d, 0x0, 0x0, 0x0, 0x4000ad, 0x0, 0x0, 0x0, 0x4100ad, 0x0, 0x0, 0x0, 0x4200ad, 0x0, 0x0, 0x0, 0x4300ad, 0x0, 0x0, 0x0, 0x40008e, 0x0, 0x0, 0x0, 0x41008e, 0x0, 0x0, 0x0, 0x42008e, 0x0, 0x0, 0x0, 0x43008e, 0x0, 0x0, 0x0, 0x4000ae, 0x0, 0x0, 0x0, 0x4100ae, 0x0, 0x0, 0x0, 0x4200ae, 0x0, 0x0, 0x0, 0x4300ae, 0x0, 0x0, 0x0, 0x0, 0x0, 0x0, 0x0, 0x0, 0x0, 0x0, 0x0, 0x0, 0x0, 0x0, 0x0, 0x0, 0x0, 0x0, 0x0, 0x0, 0x0, 0x0, 0x0, 0x0, 0x0, 0x0, 0x0, 0x0, 0x0, 0x0, 0x0, 0x0, 0x0, 0x0, 0x0},
  {0x0, 0x0, 0x0, 0x0, 0x0, 0x0, 0x0, 0x0, 0x0, 0x0, 0x0, 0x0, 0x0, 0x0, 0x0, 0x0, 0x0, 0x0, 0x0, 0x0, 0x0, 0x0, 0x0, 0x0, 0x0, 0x0, 0x0, 0x0, 0x0, 0x0, 0x0, 0x0, 0x4000cd, 0x0, 0x0, 0x0, 0x4100cd, 0x0, 0x0, 0x0, 0x4200cd, 0x0, 0x0, 0x0, 0x4300cd, 0x0, 0x0, 0x0, 0x4000ed, 0x0, 0x0, 0x0, 0x4100ed, 0x0, 0x0, 0x0, 0x4200ed, 0x0, 0x0, 0x0, 0x4300ed, 0x0, 0x0, 0x0, 0x4000ce, 0x0, 0x0, 0x0, 0x4100ce, 0x0, 0x0, 0x0, 0x4200ce, 0x0, 0x0, 0x0, 0x4300ce, 0x0, 0x0, 0x0, 0x4000ee, 0x0, 0x0, 0x0, 0x4100ee, 0x0, 0x0, 0x0, 0x4200ee, 0x0, 0x0, 0x0, 0x4300ee, 0x0, 0x0, 0x0, 0x0, 0x0, 0x0, 0x0, 0x0, 0x0, 0x0, 0x0, 0x0, 0x0, 0x0, 0x0, 0x0, 0x0, 0x0, 0x0, 0x0, 0x0, 0x0, 0x0, 0x0, 0x0, 0x0, 0x0, 0x0, 0x0, 0x0, 0x0, 0x0, 0x0, 0x0, 0x0},
};
__global__ void __launch_bounds__(512, 2) k_mega(const u16* __restrict__ Wq, const u16* __restrict__ Wk, const u16* __restrict__ Wv,
                                                 const u16* __restrict__ Wo, const u16* __restrict__ w1, const u16* __restrict__ w3,
                                                 const u16* __restrict__ w2, const u16* __restrict__ x, const u16* __restrict__ cvec,
                                                 const u16* __restrict__ wmod, const u16* __restrict__ mem0, char* ws, u16* out) {
  extern __shared__ __attribute__((aligned(16))) char smem[];
  int wave_id_ = __builtin_amdgcn_readfirstlane((int)threadIdx.x >> 6);
  asm volatile("" : "+s"(wave_id_));
  unsigned* tmo = (unsigned*)(ws + OFF_TMO);
  unsigned long long* mts = nullptr;
  const float* mod = (const float*)(ws + OFF_MOD);
  volatile LAS unsigned* tslot = (volatile LAS unsigned*)(smem + L_SLOT + 768);
  unsigned xcc; asm volatile("s_getreg_b32 %0, hwreg(HW_REG_XCC_ID)" : "=s"(xcc)); xcc &= 7;
  auto ini_of = [](int sl) { return sl >= 128 && ((sl + 8) & 31) < 16 ? ((sl >> 5) - 4) * 16 + (sl & 15) : -1; };
  const int guess = xcc * 32 + ((blockIdx.x >> 3) & 31);
  if (wave_id_ == 3) {
    const char* pc; asm volatile("s_getpc_b64 %0" : "=s"(pc));
    const unsigned o = (unsigned)(lane_id() * 32 + ((blockIdx.x >> 3) & 31)) * 128;
    if (o < CPF * 1024) { const unsigned v = *(const unsigned*)(pc + o); asm volatile("" :: "v"(v)); }
  }
  if (wave_id_ >= 4 && ini_of(guess) >= 0) { init_sums(smem, opaque_tid() - 256, ini_of(guess), cvec, wmod); if (wave_id_ == 7) IS(3); }
  u32x2 rt_; u32x4 pfe;
  unsigned long long tag;
  asm volatile("s_load_dwordx2 %0, %3, %6\n\ts_load_dwordx2 %1, %4, 0\n\ts_load_dwordx4 %2, %5, 0\n\ts_waitcnt lgkmcnt(0)" : "=&s"(tag), "=&s"(rt_), "=&s"(pfe)
               : "s"(ws), "s"(&ROLES[guess][0]), "s"(&PFT[xcc][((blockIdx.x >> 3) & 31) * 4]), "i"((int)OFF_EP) : "memory");
  tag = (tag ^ (tag >> 31) ^ 0x9E3779B97F4A7C15ull) * 0xBF58476D1CE4E5B9ull; tag = (tag ^ (tag >> 29)) * 0x94D049BB133111EBull; tag ^= tag >> 32;
  if (wave_id_ == 0) {
    if (lane_id() == 0) {
      const unsigned long long g = tag; IS(0);
      unsigned sl = 0xFFFFu;
      for (int i = 0; i < 256; ++i)
        if (__hip_atomic_exchange((unsigned long long*)(ws + OFF_CLM) + ((guess + i) & 255), g, __ATOMIC_RELAXED, __HIP_MEMORY_SCOPE_AGENT) != g) { sl = (guess + i) & 255; break; }
      IS(1);
      tslot[0] = sl;
    }
  } else if (wave_id_ == 1) {
    const int ig = ini_of(guess);
    tslot[1] = ig >= 0 && (ig & 15) < 12 ? (unsigned)calibrate(ws, ig) : 0u;
    IS(2);
  } else if (ini_of(guess) >= 0) {
    if (wave_id_ < 4) { const unsigned v = *(const unsigned*)(mem0 + (opaque_tid() - 128) * 64); asm volatile("" :: "v"(v)); }
  } else if (wave_id_ >= 4) {
    delay64<PFD>();
#pragma unroll
    for (int i = 0; i < 4; ++i) {
      const unsigned e = pfe[i];
      const int t = opaque_tid() - 256, m = e & 3, wide = (e >> 21) & 1, row = ((e >> 8) & 8191) + (wide ? t >> 4 : t), cl = ((e >> 2) & 63) + (wide ? t & 15 : 0);
      const u16* p = (m == 0 ? Wq : (m == 1 ? Wk : (m == 2 ? Wv : Wo))) + (size_t)row * (m == 3 ? DM : HD) + cl * 64;
      if (e) asm volatile("s_mov_b32 m0, %1\n\ts_nop 0\n\tglobal_load_lds_dword %0, off" :: "v"(p), "s"((rt_.x & 15) == T_QV ? 31232 : L_GB + 8192) : "memory", "m0");
    }
    if (PFW) WAIT_V(0);
  }
  BAR();
  const int slot = (int)__builtin_amdgcn_readfirstlane(tslot[0]);
  const int cd = (int)__builtin_amdgcn_readfirstlane(tslot[1]);
  if (slot != guess) { rt_ = *(const u32x2*)ROLES[slot & 255]; WAIT_V(0); }
  BAR();
  asm volatile("" : "+s"(wave_id_));
  if (slot >= 256) return;
  const int xg = slot >> 5;
  const unsigned rw = rt_[0];
  const int role = rw & 15, idx = (rw >> 4) & 255;
  if (role == T_QV || role == T_KR) {
    const int wg = role == T_QV ? (idx >> 3) * 32 + (idx & 7) : (idx >> 4) * 32 + 8 + (idx & 15);
    if (role == T_QV) qk_body(smem, wave_id_, xg, idx, wg, Wq, Wv, ws, tmo, tag);
    else vr_body(smem, wave_id_, xg, idx >> 1, idx & 1, wg, Wk, Wv, Wo, ws, tmo, tag);
    return;
  }
  unsigned* cnt = (unsigned*)(xch_base(ws) + ((size_t)4 << 21));
  const int ini = ini_of(slot);
  if (ini >= 0) {
    if (slot != guess) { if (wave_id_ < 4) init_sums(smem, opaque_tid(), ini, cvec, wmod); BAR(); }
    init_role(smem, wave_id_, ini, mem0, ws, tag, slot != guess || cd == 0 ? 0u : ((cd > 0) == !(xcc & 4) ? 1u : 2u));
    if ((rw >> 12) & 255) l1_role(smem, wave_id_, ((rw >> 12) & 255) - 1, x, mod, cvec, wmod, ws + OFF_H1, cnt, tmo, mts, tag);
    else cnt += (ini_resolve1(smem, wave_id_, ws, tag) >> 2) << 11;
  }
  const int ntask = (rw >> 20) & 15;
  for (int it = 0; it < ntask; ++it) {
    const unsigned tk = it == 0 ? rt_[1] : ROLES[slot][1 + it];
    const int m = tk & 3, h = (tk >> 2) & 31, bo = (tk >> 8) & 255, ns = ((tk >> 16) & 255) - bo, part = (tk >> 24) & 3;
    const int lane = lane_id();
    const Dep d{uni((const char*)cnt), (unsigned)(PL1 + (lane & 31)) * 128, 8u, (unsigned)-bo};
    unsigned par = 0;
    if (wave_id_ == 0) {
      if ((tk >> 26) & 1) wait_k0(cnt, h, tmo);
    }
    BAR();
    gemm7<R_QKV>(smem, wave_id_, (m == 0 ? Wq : (m == 1 ? Wk : Wv)) + h * DH, nullptr, HD, ws + OFF_H1 + (size_t)bo * 65536, 65536, d,
                    ws + (m == 0 ? OFF_Q : (m == 1 ? OFF_K : OFF_V)) + h * 8192 + (size_t)bo * 32 * 8192, 32 * 8192,
                    cnt + (PQKV + h) * 32 + 4 * part + m, tmo, part ? 319 : (h >> 3) * 32 + m * 8 + (h & 7), mts, false,
                    bo ? 0 : NIMM, ns, bo ? ns : 0, bo * 257, tag, &par);
    cnt += (par >> 2) << 11;
  }
  __syncthreads();
  const char* cb = uni((const char*)cnt);
  if (role == T_WO || role == T_F2) {
    const bool wo = role == T_WO;
    const int ks = idx >> 3, cn = idx & 7;
    const int lane = lane_id();
    const Dep d{cb, (unsigned)(wo ? PROG + 8 * ks + (lane & 7) : PF1 + 16 * ks + (lane & 15)) * 128, 1u, 0u};
    gemm7<R_PART>(smem, wave_id_, (wo ? Wo : w2) + (size_t)ks * 1024 * DM + cn * 128, nullptr, DM, ws + (wo ? OFF_O : OFF_G) + ks * 16 * 4096, (wo ? 64 : 56) * 4096, d,
                  ws + (wo ? OFF_P : OFF_P2) + (ks * DM + cn * 128) * (TLP ? 64 : 2), (size_t)32 * PROW, cnt + ((wo ? PWO : PF2) + idx) * 32, tmo,
                  wo ? 128 + ks * 32 + cn : 128 + (idx / 7) * 32 + 22 + idx % 7, mts, !wo);
  } else if (role == T_F2C) {
    const int lane = lane_id();
    const Dep d{cb, (unsigned)(PF1 + 48 + (lane & 7)) * 128, 1u, 0u};
    gemm7<R_PART2>(smem, wave_id_, w2 + (size_t)3072 * DM + idx * 256, nullptr, DM, ws + OFF_G + 48 * 4096, 56 * 4096, d,
                   ws + OFF_P2 + (3 * DM + idx * 256) * (TLP ? 64 : 2), (size_t)32 * PROW, cnt + (PF2 + 24 + idx) * 32, tmo, 128 + 96 + 25 + idx, mts, true);
  } else if (role == T_F1) {
    const int lane = lane_id();
    const Dep d{cb, (unsigned)(PLN + (lane & 15)) * 128, 2u, (unsigned)((lane >> 3) & 1)};
    gemm7<R_F1>(smem, wave_id_, w1 + 64 * idx, w3 + 64 * idx, FF, ws + OFF_H2, 65536, d, ws + OFF_G + idx * 4096, 56 * 4096, cnt + (PF1 + idx) * 32, tmo,
                128 + (idx / 14) * 32 + 8 + idx % 14, mts, false);
  } else if (role == T_FIN) {
    fin_role(smem, wave_id_, idx, mod, ws + OFF_P2, (const u16*)(ws + OFF_X1), out, cnt, tmo, mts);
    if (idx == 15 && opaque_tid() == 0) st8_sc1(ws + OFF_EP, u32x2{(unsigned)tag, (unsigned)(tag >> 32)});
  } else if (role == T_LN) {
    row_role(smem, wave_id_, idx & 7, idx >> 3, mod, ws + OFF_P, x, (u16*)(ws + OFF_X1), ws + OFF_H2, cnt, tmo, 256 + ((idx >> 1) & 3), mts);
  }
}

extern "C" void kernel_launch(void* const* d_in, const int* in_sizes, int n_in, void* d_out, int out_size, void* d_ws, size_t ws_size,
                              hipStream_t stream) {
  const u16* x    = (const u16*)d_in[0];
  const u16* mem0 = (const u16*)d_in[1];
  const u16* Wq   = (const u16*)d_in[2];
  const u16* Wk   = (const u16*)d_in[3];
  const u16* Wv   = (const u16*)d_in[4];
  const u16* Wo   = (const u16*)d_in[5];
  const u16* w1   = (const u16*)d_in[8];
  const u16* w3   = (const u16*)d_in[9];
  const u16* w2   = (const u16*)d_in[10];
  const u16* c    = (const u16*)d_in[11];
  const u16* wmod = (const u16*)d_in[12];
#define WSHIFT 0
  char* ws = (char*)d_ws + ((size_t)WSHIFT << 20);
  hipFuncSetAttribute((const void*)k_mega, hipFuncAttributeMaxDynamicSharedMemorySize, MEGA_LDS);
  k_mega<<<256, 512, MEGA_LDS, stream>>>(Wq, Wk, Wv, Wo, w1, w3, w2, x, c, wmod, mem0, ws, (u16*)d_out);
}
```

```cpp
#include <hip/hip_runtime.h>
#include <hip/hip_bf16.h>
#include <stdint.h>
#include <type_traits>
#pragma clang diagnostic ignored "-Winline-asm"
#pragma clang diagnostic ignored "-Wunused-value"

typedef unsigned short u16;
typedef short s16x4 __attribute__((ext_vector_type(4)));
typedef __bf16 bf16x8 __attribute__((ext_vector_type(8)));
typedef float f32x4 __attribute__((ext_vector_type(4)));
typedef float f32x2 __attribute__((ext_vector_type(2)));
typedef unsigned u32x4 __attribute__((ext_vector_type(4)));
typedef unsigned u32x2 __attribute__((ext_vector_type(2)));
#define LAS __attribute__((address_space(3)))

constexpr int SEQ = 2048, DM = 1024, NH = 32, DH = 128, HD = 4096, NMEM = 8, SEG = 32, NSEG = 64, FF = 3584;
constexpr unsigned SENT = 0xFFFFFFFFu;

__device__ __forceinline__ int lane_id() { int l; asm volatile("v_mbcnt_lo_u32_b32 %0, -1, 0\n\tv_mbcnt_hi_u32_b32 %0, -1, %0" : "=v"(l)); return l; }
#define WAVE_ID wave_id_
#define opaque_tid() (WAVE_ID * 64 + lane_id())
__device__ __forceinline__ float bf2f(u16 v) { return __builtin_bit_cast(float, (unsigned)v << 16); }
__device__ __forceinline__ float bflo(unsigned v) { return __builtin_bit_cast(float, v << 16); }
__device__ __forceinline__ float bfhi(unsigned v) { return __builtin_bit_cast(float, v & 0xffff0000u); }
__device__ __forceinline__ float bfr(float x) { return (float)(__bf16)x; }
__device__ __forceinline__ unsigned pack2(float a, float b) {
  typedef __bf16 bf16x2 __attribute__((ext_vector_type(2)));
  bf16x2 v; v[0] = (__bf16)a; v[1] = (__bf16)b; return __builtin_bit_cast(unsigned, v);
}
__device__ __forceinline__ unsigned nosent(unsigned v) { return v == SENT ? 0x7FC07FC0u : v; }
__device__ __forceinline__ bf16x8 asbf8(u32x4 v) { return __builtin_bit_cast(bf16x8, v); }
__device__ __forceinline__ s16x4 ass4(u32x2 v) { return __builtin_bit_cast(s16x4, v); }
__device__ __forceinline__ u32x2 trread(const char* lds_base, unsigned off) {
  s16x4 v = __builtin_amdgcn_ds_read_tr16_b64_v4i16((LAS s16x4*)(lds_base + off));
  return __builtin_bit_cast(u32x2, v);
}
__device__ __forceinline__ void glds16(const void* gsrc, unsigned lds_dst) {
  unsigned keep;
  asm volatile("s_mov_b32 %0, m0\n\ts_mov_b32 m0, %2\n\ts_nop 0\n\tglobal_load_lds_dwordx4 %1, off\n\ts_mov_b32 m0, %0"
               : "=&s"(keep) : "v"(gsrc), "s"(lds_dst) : "memory");
}
#define CNT_ " nt"
#define WNT_ " nt"
__device__ __forceinline__ void glds16_w(const void* gsrc, unsigned lds_dst) {
  unsigned keep;
  asm volatile("s_mov_b32 %0, m0\n\ts_mov_b32 m0, %2\n\ts_nop 0\n\tglobal_load_lds_dwordx4 %1, off" CNT_ "\n\ts_mov_b32 m0, %0"
               : "=&s"(keep) : "v"(gsrc), "s"(lds_dst) : "memory");
}
__device__ __forceinline__ void glds16_sc1(const void* gsrc, unsigned lds_dst) {
  unsigned keep;
  asm volatile("s_mov_b32 %0, m0\n\ts_mov_b32 m0, %2\n\ts_nop 0\n\tglobal_load_lds_dwordx4 %1, off sc1\n\ts_mov_b32 m0, %0"
               : "=&s"(keep) : "v"(gsrc), "s"(lds_dst) : "memory");
}
#define NTM 1
template <int B> __device__ __forceinline__ u32x4 ldnt(const void* p) { if (NTM & B) return __builtin_nontemporal_load((const u32x4*)p); else return *(const u32x4*)p; }
#define MFMA32(a, b, c) __builtin_amdgcn_mfma_f32_16x16x32_bf16((a), (b), (c), 0, 0, 0)

__device__ __forceinline__ void st16_sc1(void* p, u32x4 v) { asm volatile("global_store_dwordx4 %0, %1, off sc1\n\ts_nop 1" :: "v"(p), "v"(v) : "memory"); }
__device__ __forceinline__ void st8_sc1(void* p, u32x2 v) { asm volatile("global_store_dwordx2 %0, %1, off sc1\n\ts_nop 1" :: "v"(p), "v"(v) : "memory"); }
template <bool WT> __device__ __forceinline__ void st8(void* p, u32x2 v) { if (WT) st8_sc1(p, v); else *(u32x2*)p = v; }
template <bool WT> __device__ __forceinline__ void st16(void* p, u32x4 v) { if (WT) st16_sc1(p, v); else *(u32x4*)p = v; }
__device__ __forceinline__ u32x4 ld16_sc1(const void* p) {
  u32x4 r; asm volatile("global_load_dwordx4 %0, %1, off sc1\n\ts_waitcnt vmcnt(0)" : "=&v"(r) : "v"(p) : "memory"); return r;
}
__device__ __forceinline__ void ld16x4_sc1(const void* p, u32x4& a, u32x4& b, u32x4& c, u32x4& d) {
  asm volatile("global_load_dwordx4 %0, %4, off sc1\n\tglobal_load_dwordx4 %1, %4, off offset:1024 sc1\n\t"
               "global_load_dwordx4 %2, %4, off offset:2048 sc1\n\tglobal_load_dwordx4 %3, %4, off offset:3072 sc1\n\ts_waitcnt vmcnt(0)"
               : "=&v"(a), "=&v"(b), "=&v"(c), "=&v"(d) : "v"(p) : "memory");
}
typedef __amdgpu_buffer_rsrc_t rsrc_t;
__device__ __forceinline__ rsrc_t mkrs(const void* p) { return __builtin_amdgcn_make_buffer_rsrc((void*)p, 0, 0x7FFFFFFF, 0x00020000); }
__device__ __forceinline__ u32x4 bld16(rsrc_t r, unsigned off) { return __builtin_amdgcn_raw_buffer_load_b128(r, off, 0, 16); }
__device__ __forceinline__ u32x2 bld8(rsrc_t r, unsigned off) { return __builtin_amdgcn_raw_buffer_load_b64(r, off, 0, 16); }
__device__ __forceinline__ unsigned mx4(u32x4 v) { return max(max(v.x, v.y), max(v.z, v.w)); }
__device__ __forceinline__ bool ok4(u32x4 v) { return v.x != SENT && v.y != SENT && v.z != SENT && v.w != SENT; }
constexpr int SPIN_CAP = 2000000;
__device__ __forceinline__ bool give_up(int spins, unsigned* tmo, unsigned code) {
  if ((spins & 4095) != 4095) return false;
  if (spins > SPIN_CAP) { __hip_atomic_store(tmo, code, __ATOMIC_RELAXED, __HIP_MEMORY_SCOPE_AGENT); return true; }
  return __hip_atomic_load(tmo, __ATOMIC_RELAXED, __HIP_MEMORY_SCOPE_AGENT) != 0u;
}
template <bool ACQ = true>
__device__ __forceinline__ void wait_cnt(unsigned* c, unsigned n, unsigned* tmo) {
  for (int spins = 0;; ++spins) {
    const unsigned v = __hip_atomic_load(c, __ATOMIC_RELAXED, __HIP_MEMORY_SCOPE_AGENT);
    if (v >= n) break;
    __builtin_amdgcn_s_sleep(8);
    if (give_up(spins, tmo, 5)) break;
  }
  if (ACQ) {
    __builtin_amdgcn_fence(__ATOMIC_ACQUIRE, "agent");
    asm volatile("s_waitcnt vmcnt(0)" ::: "memory");
  }
}

__device__ __forceinline__ const char* uni(const char* p) {
  const unsigned long long v = (unsigned long long)p;
  const unsigned lo = __builtin_amdgcn_readfirstlane((unsigned)v), hi = __builtin_amdgcn_readfirstlane((unsigned)(v >> 32));
  return (const char*)(((unsigned long long)hi << 32) | lo);
}
#define TSCLK "s_memrealtime"
#define TC(k) do { } while (0)
#define TS(k) do { } while (0)
#define TSDECL do { } while (0)
#define TSFLUSH(mask) do { } while (0)

constexpr size_t OFF_MOD  = 0;
constexpr size_t OFF_MOD2 = 65536;
constexpr size_t OFF_TMO  = 98304;
constexpr size_t OFF_EP   = 98304 + 256;
constexpr size_t OFF_CLM  = 102400;
constexpr size_t OFF_INI  = 131072;
constexpr size_t OFF_H1   = 262144;
constexpr size_t OFF_Q    = OFF_H1 + (size_t)SEQ * DM * 2;
constexpr size_t OFF_K    = OFF_Q + (size_t)SEQ * HD * 2;
constexpr size_t OFF_V    = OFF_K + (size_t)SEQ * HD * 2;
constexpr size_t OFF_O    = OFF_V + (size_t)SEQ * HD * 2;
constexpr size_t OFF_X1   = OFF_O + (size_t)SEQ * HD * 2;
constexpr size_t OFF_H2   = OFF_X1 + (size_t)SEQ * DM * 2;
constexpr size_t OFF_G    = OFF_H2 + (size_t)SEQ * DM * 2;
constexpr size_t OFF_XCH  = (size_t)100 << 20;
constexpr size_t OFF_P    = (size_t)112 << 20;
constexpr size_t OFF_P2   = (size_t)128 << 20;
constexpr size_t OFF_DUMP = (size_t)144 << 20;
constexpr int PROW = 4 * DM * 2;
#define TLQ 1
#define TLP 1
static_assert(OFF_G + (size_t)SEQ * FF * 2 <= OFF_XCH, "layout");
__device__ __forceinline__ char* xch_base(char* ws) { return (char*)(((size_t)(ws + OFF_XCH) + ((2u << 20) - 1)) & ~(size_t)((2u << 20) - 1)); }
constexpr int PROG = 16;
constexpr int PQKV = 64;
constexpr int PL1 = 96;
constexpr int PARM = 128;
constexpr int PWO = 192;
constexpr int PLN = 224;
constexpr int PF2 = 320;
constexpr int PF1 = 448;
#define BEARLY 54
#define BEG BEARLY
#define BEH BEARLY
#define ARMW 51
#define LAZY 0
static_assert(BEH <= BEARLY && BEG <= BEARLY && BEH % 2 == 0, "");
constexpr int L_RED = 131072;
constexpr int L_SLOT = L_RED + 16384;
constexpr int L_GB = L_SLOT + 1024;
constexpr int MEGA_LDS = 163840;
static_assert(BEARLY % 2 == 0, "");
static_assert(L_GB + 12288 + 2048 <= MEGA_LDS, "lds");
__device__ __forceinline__ int bswz(int row) { return (((row >> 1) & 1) | (((row >> 3) & 1) << 1)) << 1; }
template <int OFF, bool SC1 = false>
__device__ __forceinline__ void glds16s(const char* sbase, unsigned voff, unsigned lds_base) {
  unsigned keep;
  if (SC1)
    asm volatile("s_mov_b32 %0, m0\n\ts_add_u32 m0, %3, %4\n\ts_nop 0\n\tglobal_load_lds_dwordx4 %1, %2 sc1\n\ts_mov_b32 m0, %0"
                 : "=&s"(keep) : "v"(voff), "s"(sbase), "s"(lds_base), "i"(OFF) : "memory", "scc");
  else
    asm volatile("s_mov_b32 %0, m0\n\ts_add_u32 m0, %3, %4\n\ts_nop 0\n\tglobal_load_lds_dwordx4 %1, %2\n\ts_mov_b32 m0, %0"
                 : "=&s"(keep) : "v"(voff), "s"(sbase), "s"(lds_base), "i"(OFF) : "memory", "scc");
}
template <int OFF>
__device__ __forceinline__ void glds16x2s(const char* sbase, unsigned voff, unsigned lds_base) {
  unsigned keep;
  asm volatile("s_mov_b32 %0, m0\n\ts_add_u32 m0, %3, %4\n\ts_nop 0\n\tglobal_load_lds_dwordx4 %1, %2 sc1\n\tglobal_load_lds_dwordx4 %1, %2 offset:1024 sc1\n\ts_mov_b32 m0, %0"
               : "=&s"(keep) : "v"(voff), "s"(sbase), "s"(lds_base), "i"(OFF) : "memory", "scc");
}
__device__ __forceinline__ void st16s(const char* sb, unsigned vo, u32x4 v) { asm volatile("global_store_dwordx4 %0, %1, %2 sc1\n\ts_nop 1" :: "v"(vo), "v"(v), "s"(sb) : "memory"); }
__device__ __forceinline__ void head_to_lds(const char* src, int lane, unsigned lds) {
  if (TLQ) {
    const int c = (lane & 15) ^ (lane >> 4);
    const unsigned v0 = (unsigned)((c >> 2) * 2048 + (lane >> 4) * 64 + (c & 3) * 16);
#pragma unroll
    for (int i = 0; i < 8; ++i) glds16s<0, true>(src + i * 256, v0 ^ ((i & 3) * 2048), lds + i * 1024);
  } else {
#pragma unroll
    for (int i = 0; i < 4; ++i) glds16x2s<0>(src + i * 2048, (unsigned)lane * 16, lds + i * 2048);
  }
}
#define WAIT_V(n) asm volatile("s_waitcnt vmcnt(" #n ")" ::: "memory")
#define BAR() do { asm volatile("s_waitcnt lgkmcnt(0)" ::: "memory"); __builtin_amdgcn_s_barrier(); asm volatile("" ::: "memory"); } while (0)
__device__ __forceinline__ void st4_sc1(void* p, unsigned v) { asm volatile("global_store_dword %0, %1, off sc1" :: "v"(p), "v"(v) : "memory"); }
__device__ __forceinline__ unsigned ld4_sc1(const void* p) {
  unsigned r; asm volatile("global_load_dword %0, %1, off sc1\n\ts_waitcnt vmcnt(0)" : "=&v"(r) : "v"(p) : "memory"); return r;
}
template <int CTRL> __device__ __forceinline__ unsigned dppu(unsigned x) { return (unsigned)__builtin_amdgcn_update_dpp(0, (int)x, CTRL, 0xf, 0xf, false); }
__device__ __forceinline__ unsigned rowmin16(unsigned v) {
  v = min(v, dppu<0x128>(v)); v = min(v, dppu<0x124>(v)); v = min(v, dppu<0x122>(v)); v = min(v, dppu<0x121>(v));
  return v;
}
__device__ __forceinline__ unsigned rdl(unsigned v, int l) { return (unsigned)__builtin_amdgcn_readlane((int)v, l); }
constexpr float SCALE = 0.08838834764831845f;
constexpr float LOG2E = 1.4426950408889634f;
template <int N> __device__ __forceinline__ void delay64() {
  if (N >= 16) { __builtin_amdgcn_s_sleep(15); delay64<(N >= 16 ? N - 15 : 0)>(); } else if (N > 0) __builtin_amdgcn_s_sleep(N);
}
__device__ __forceinline__ void sw16(float x, float& a, float& b) {
  a = x; b = x; asm volatile("s_nop 1\n\tv_permlane16_swap_b32 %0, %1" : "+v"(a), "+v"(b));
}
__device__ __forceinline__ void sw32(float x, float& a, float& b) {
  a = x; b = x; asm volatile("s_nop 1\n\tv_permlane32_swap_b32 %0, %1" : "+v"(a), "+v"(b));
}
__device__ __forceinline__ float red16_32_max(float x) {
  float a, b; sw16(x, a, b); x = fmaxf(a, b); sw32(x, a, b); return fmaxf(a, b);
}
__device__ __forceinline__ float red16_32_add(float x) {
  float a, b; sw16(x, a, b); x = a + b; sw32(x, a, b); return a + b;
}
__device__ __forceinline__ unsigned cvtpk(float a, float b) { unsigned r; asm("v_cvt_pk_bf16_f32 %0, %1, %2" : "=v"(r) : "v"(a), "v"(b)); return r; }
__device__ __forceinline__ float max3(float a, float b, float c) { float r; asm("v_max3_f32 %0, %1, %2, %3" : "=v"(r) : "v"(a), "v"(b), "v"(c)); return r; }
__device__ __forceinline__ float max2(float a, float b) { float r; asm("v_max_f32 %0, %1, %2" : "=v"(r) : "v"(a), "v"(b)); return r; }
__device__ __forceinline__ unsigned ror8u(unsigned x) { return (unsigned)__builtin_amdgcn_update_dpp(0, (int)x, 0x128, 0xf, 0xf, false); }
__device__ __forceinline__ float ror8(float x) { return __builtin_bit_cast(float, ror8u(__builtin_bit_cast(unsigned, x))); }
__device__ __forceinline__ float add_ror8(float x) {
  return x + __builtin_bit_cast(float, __builtin_amdgcn_update_dpp(0, __builtin_bit_cast(int, x), 0x128, 0xf, 0xf, false));
}

#define RSL 4
__device__ __forceinline__ void ini_request(const char* ws, unsigned lds) {
  const char* p = uni(ws + OFF_INI);
  asm volatile("s_mov_b32 m0, %2\n\ts_nop 3\n\tglobal_load_lds_dword %0, %1 sc1\n\ts_add_u32 m0, %2, 252\n\ts_nop 0\n\tglobal_load_lds_dword %0, %1 offset:4 sc1"
               :: "v"(lane_id() * 128), "s"(p), "s"(lds) : "memory", "m0", "scc");
}
__device__ __forceinline__ unsigned ini_resolve(const char* ws, const char* l, unsigned long long tag) {
  const int lane = lane_id();
  u32x2 v = u32x2{~(unsigned)tag, 0u};
  if (l) v = u32x2{*(const volatile LAS unsigned*)(l + lane * 4), *(const volatile LAS unsigned*)(l + 256 + lane * 4)};
  for (int spins = 0; !__all(v.x == (unsigned)tag && (v.y >> 16) == (unsigned)(tag >> 48)) && spins < SPIN_CAP; ++spins) {
    if (spins) __builtin_amdgcn_s_sleep(RSL);
    asm volatile("global_load_dwordx2 %0, %1, off sc1\n\ts_waitcnt vmcnt(0)" : "=&v"(v) : "v"(ws + OFF_INI + lane * 128) : "memory");
  }
  const unsigned long long up = __ballot((v.y & 3u) == 1u), dn = __ballot((v.y & 3u) == 2u);
  unsigned par = 0;
#pragma unroll
  for (int c = 0; c < 3; ++c) { const unsigned long long m = (0x249ull << c) * 0x0001000100010001ull; par |= (__popcll(up & m) > __popcll(dn & m) ? 0u : 1u) << c; }
  par = __builtin_amdgcn_readfirstlane(par);
  asm volatile("" : "+s"(par));
  return par;
}
__device__ __forceinline__ unsigned ini_resolve1(char* smem, int wave_id_, const char* ws, unsigned long long tag) {
  volatile LAS unsigned* w = (volatile LAS unsigned*)(smem + L_SLOT + 800);
  if (WAVE_ID == 1) *w = ini_resolve(ws, nullptr, tag);
  BAR();
  return __builtin_amdgcn_readfirstlane(*w);
}
template <int M0> __device__ __forceinline__ unsigned qkv_avail(const unsigned* line) {
  const unsigned v = ld4_sc1(line + (lane_id() & 15));
  unsigned r = NSEG;
#pragma unroll
  for (int m = M0; m < 3; ++m) {
    unsigned a = rdl(v, m);
#pragma unroll
    for (int p = 1; p < 3; ++p) { const unsigned w = rdl(v, 4 * p + m); if (a >= (w >> 8)) a = max(a, w & 255u); }
    r = min(r, a);
  }
  return r;
}
__device__ __forceinline__ void wait_l1(const unsigned* cnt, unsigned n, unsigned* tmo) {
  const int lane = lane_id();
  for (int spins = 0;; ++spins) {
    const unsigned v = ld4_sc1(cnt + (PL1 + (lane & 31)) * 32);
    if (__all(v >= n)) break;
    __builtin_amdgcn_s_sleep(8);
    if (give_up(spins, tmo, 12)) break;
  }
}
constexpr int NARM = 16;
__device__ __forceinline__ void wait_arm(const unsigned* cnt, unsigned* tmo) {
  for (int spins = 0;; ++spins) {
    if (__all(ld4_sc1(cnt + (PARM + min(lane_id(), NARM - 1)) * 32) != 0u)) break;
    __builtin_amdgcn_s_sleep(8);
    if (give_up(spins, tmo, 12)) break;
  }
}
#define GKV 1
__device__ __forceinline__ void wait_k0(const unsigned* cnt, int h, unsigned* tmo, bool kv = true) {
  for (int spins = 0;; ++spins) {
    const int l = lane_id();
    const unsigned v = ld4_sc1(cnt + (PQKV + h) * 32 + (l == 0 || !kv ? 3 : 1 + (l & 1)));
    if (__all(v >= (l == 0 ? 1u : (unsigned)GKV))) break;
    __builtin_amdgcn_s_sleep(32);
    if (give_up(spins, tmo, 13)) break;
  }
}
#define IS(k) do { } while (0)
#define KTON 0
#define WCLK(slot, b) do { } while (0)
#define WTM(slot, b, k) do { } while (0)
#define WTS(slot, b, k) do { } while (0)
#define KTS(k_) do { } while (0)
#define STS(ws_, slot_, k_) do { } while (0)
typedef __bf16 bf16x2 __attribute__((ext_vector_type(2)));
__device__ __forceinline__ f32x2 pkfma(f32x2 a, f32x2 b, f32x2 c) { return __builtin_elementwise_fma(a, b, c); }
__device__ __forceinline__ float wavesum(float v) {
  v += __builtin_bit_cast(float, dppu<0x128>(__builtin_bit_cast(unsigned, v))); v += __builtin_bit_cast(float, dppu<0x124>(__builtin_bit_cast(unsigned, v)));
  v += __builtin_bit_cast(float, dppu<0x122>(__builtin_bit_cast(unsigned, v))); v += __builtin_bit_cast(float, dppu<0x121>(__builtin_bit_cast(unsigned, v)));
  return red16_32_add(v);
}
__device__ __forceinline__ void bst16(rsrc_t r, unsigned off, unsigned soff, u32x4 v) { __builtin_amdgcn_raw_buffer_store_b128(v, r, off + soff, 0, 16); }
__device__ __forceinline__ float adds(unsigned v, unsigned sel, float c) { return __builtin_amdgcn_fdot2_f32_bf16(__builtin_bit_cast(bf16x2, v), __builtin_bit_cast(bf16x2, sel), c, false); }
__device__ __forceinline__ void sum4(const u32x4 (&p)[4], f32x2 (&a)[4]) {
  unsigned slo = 0x00003F80u, shi = 0x3F800000u;
  asm("" : "+s"(slo), "+s"(shi));
#pragma unroll
  for (int e = 0; e < 4; ++e) {
    float lo = bflo(p[0][e]), hi = bfhi(p[0][e]);
#pragma unroll
    for (int q = 1; q < 4; ++q) { lo = adds(p[q][e], slo, lo); hi = adds(p[q][e], shi, hi); }
    a[e] = f32x2{lo, hi};
  }
}
__device__ __forceinline__ u32x4 norm8(const f32x2 (&v)[4], const char* mt, float mb, float rden) {
  const f32x4 h0 = *(const f32x4*)(mt + 4096), h1 = *(const f32x4*)(mt + 4096 + 1024), c0 = *(const f32x4*)(mt + 8192), c1 = *(const f32x4*)(mt + 8192 + 1024);
  const f32x2 m2 = f32x2{mb, mb}, d2 = f32x2{rden, rden};
  const f32x2 r0 = pkfma(v[0] - m2, f32x2{c0[0], c0[1]} * d2, f32x2{h0[0], h0[1]}), r1 = pkfma(v[1] - m2, f32x2{c0[2], c0[3]} * d2, f32x2{h0[2], h0[3]});
  const f32x2 r2 = pkfma(v[2] - m2, f32x2{c1[0], c1[1]} * d2, f32x2{h1[0], h1[1]}), r3 = pkfma(v[3] - m2, f32x2{c1[2], c1[3]} * d2, f32x2{h1[2], h1[3]});
  return u32x4{pack2(r0[0], r0[1]), pack2(r1[0], r1[1]), pack2(r2[0], r2[1]), pack2(r3[0], r3[1])};
}
__device__ __forceinline__ bool fin_half(const char* P, const u16* res, const char* gbt, u16* __restrict__ o1, int row, int hf, int lane, bool chk) {
  const rsrc_t rp = mkrs(P), rr = mkrs(res);
  const unsigned po = (unsigned)((row >> 5) * 32 * PROW + (hf * 16 + (lane >> 2)) * 2048 + (row & 31) * 64 + (lane & 3) * 16), ro = (unsigned)(row * DM * 2 + hf * 1024 + lane * 16);
  u32x4 p[4];
#pragma unroll
  for (int q = 0; q < 4; ++q) p[q] = bld16(rp, po + q * 65536);
  const u32x4 rv = bld16(rr, ro);
  if (chk && !__all(max(max(max(mx4(p[0]), mx4(p[1])), max(mx4(p[2]), mx4(p[3]))), mx4(rv)) != SENT)) return false;
  f32x2 a[4];
  sum4(p, a);
  const u32x4 gv = *(const u32x4*)(gbt + hf * 1024 + lane * 16);
  u32x4 o;
#pragma unroll
  for (int e = 0; e < 4; ++e) { const f32x2 s = pkfma(f32x2{bflo(gv[e]), bfhi(gv[e])}, a[e], f32x2{bflo(rv[e]), bfhi(rv[e])}); o[e] = pack2(s[0], s[1]); }
  st16_sc1(o1 + (size_t)row * DM + hf * 512 + lane * 8, o);
  return true;
}
__device__ __forceinline__ void ln_half(const u32x4 (&p)[4], u32x4 rv, const f32x4 (&tb)[6], volatile LAS float* xs, const char* x1, unsigned xo, const char* h2, unsigned ho,
                                        int wave, int lane, unsigned* ann, unsigned n) {
  f32x2 a[4], v[4];
  sum4(p, a);
  u32x4 o;
#pragma unroll
  for (int e = 0; e < 4; ++e) {
    const f32x2 s = pkfma(f32x2{tb[e >> 1][2 * (e & 1)], tb[e >> 1][2 * (e & 1) + 1]}, a[e], f32x2{bflo(rv[e]), bfhi(rv[e])});
    o[e] = pack2(s[0], s[1]);
    v[e] = f32x2{bflo(o[e]), bfhi(o[e])};
  }
  const float c = __builtin_bit_cast(float, __builtin_amdgcn_readfirstlane(__builtin_bit_cast(int, v[0][0])));
  f32x2 s2 = f32x2{0, 0}, q2 = s2;
#pragma unroll
  for (int i = 0; i < 4; ++i) { const f32x2 d = v[i] - f32x2{c, c}; s2 += d; q2 = pkfma(d, d, q2); }
  float s = s2[0] + s2[1], q = q2[0] + q2[1];
#define DA(x, k) x += __builtin_bit_cast(float, dppu<k>(__builtin_bit_cast(unsigned, x)))
  DA(s, 0x128); DA(q, 0x128); DA(s, 0x124); DA(q, 0x124); DA(s, 0x122); DA(q, 0x122); DA(s, 0x121); DA(q, 0x121);
#undef DA
  {
    asm volatile("s_nop 1\n\tv_permlane16_swap_b32 %0, %1" : "+v"(s), "+v"(q));
    s += q; q = s;
    asm volatile("s_nop 1\n\tv_permlane32_swap_b32 %0, %1" : "+v"(s), "+v"(q));
    s += q;
  }
  if (lane < 48 && (lane & 15) == 0) xs[wave * 4 + (lane >> 4)] = lane == 32 ? c : s;
  st16s(x1, xo, o);
  BAR();
  if (ann && lane == 0) st8_sc1(ann, u32x2{n - 1u, n});
  const f32x4 u0 = *(const volatile LAS f32x4*)(xs + (wave & 6) * 4), u1 = *(const volatile LAS f32x4*)(xs + (wave | 1) * 4);
  const float m0 = u0[0] * (2.0f / DM), m1 = u1[0] * (2.0f / DM);
  const float dm = (u0[2] - u1[2]) + (m0 - m1);
  const float mean = ((u0[2] + m0) + (u1[2] + m1)) * 0.5f;
  const float qq = ((u0[1] - u0[0] * m0) + (u1[1] - u1[0] * m1)) + dm * dm * (DM / 4);
  const float mb = bfr(mean), vb = bfr(fmaxf(qq, 0.0f) * (1.0f / DM));
  const float rden = __builtin_amdgcn_rcpf(bfr(__builtin_amdgcn_sqrtf(bfr(vb + 1e-5f))));
  const f32x2 m2 = f32x2{mb, mb}, d2 = f32x2{rden, rden};
  u32x4 r;
#pragma unroll
  for (int e = 0; e < 4; ++e) {
    const f32x2 t = pkfma(v[e] - m2, f32x2{tb[4 + (e >> 1)][2 * (e & 1)], tb[4 + (e >> 1)][2 * (e & 1) + 1]} * d2, f32x2{tb[2 + (e >> 1)][2 * (e & 1)], tb[2 + (e >> 1)][2 * (e & 1) + 1]});
    r[e] = pack2(t[0], t[1]);
  }
  st16s(h2, ho, r);
}
__device__ __forceinline__ void row_table(char* tab, const float* mod, const unsigned* cnt, unsigned* tmo, int tid) {
  wait_l1(cnt, 9u, tmo);
  const rsrc_t rm = mkrs((const char*)mod + OFF_MOD2);
#pragma unroll
  for (int i = 0; i < 2; ++i) {
    const int u = tid + 512 * i, a = u >> 8, c = u & 255;
    f32x4 v = __builtin_bit_cast(f32x4, bld16(rm, (unsigned)u * 16));
    v = f32x4{bfr(v[0]), bfr(v[1]), bfr(v[2]), bfr(v[3])};
    if (a == 2) v = f32x4{bfr(1.0f + v[0]), bfr(1.0f + v[1]), bfr(1.0f + v[2]), bfr(1.0f + v[3])};
    if (a < 3) *(f32x4*)(tab + a * 4096 + ((c >> 7) * 2 + (c & 1)) * 1024 + ((c >> 1) & 63) * 16) = v;
    else *(u32x2*)(tab + 12288 + c * 8) = u32x2{pack2(v[0], v[1]), pack2(v[2], v[3])};
  }
  __syncthreads();
}
#define LS(i) do { } while (0)
#define ARMP 2
#define ARM0 10
#define LNA 1
#define LNP 1
#define LN1 44
#define LNSL 1
__device__ __forceinline__ void row_role(char* smem, int wave_id_, int grp, int set, const float* mod, const char* P, const u16* x, u16* x1, char* h2c,
                                         unsigned* cnt, unsigned* tmo, int slot, unsigned long long* mts) {
  const int tid = opaque_tid(), lane = tid & 63, wave = WAVE_ID;
  if (tid < 8) *(volatile LAS unsigned*)(smem + 16384 + 256 + tid * 4) = 0u;
  row_table(smem, mod, cnt, tmo, tid);
  static_assert(TLP, "");
  const int hf = wave & 1, r4 = 4 * grp + (wave >> 1);
  const unsigned pw2 = (unsigned)(PWO + ((lane >> 2) & 3) * 8 + hf * 4 + (lane & 3)) * 128 + ((lane >> 4) & 1) * 4;
  auto rdy = [&](unsigned v, int bb, bool early) { const unsigned long long m = __ballot(v > (unsigned)bb); return ((early ? m | m >> 16 : m) & 0xFFFFull) == 0xFFFFull; };
  unsigned* myp = cnt + (PLN + 8 * set + grp) * 32;
  unsigned pv = 0;
  u32x4 p[4], q[4], rv, rn;
  unsigned qv = 0; bool pre = false;
  bool req = false;
  const char* pb = uni(P); const char* cb = uni((const char*)cnt); const char* xs_ = uni((const char*)x);
  auto publish = [&](int n, int e = 0) {
    if (lane == 0 && (__hip_atomic_fetch_add((LAS unsigned*)(smem + 16384 + 256 + 4 * e), 1u, __ATOMIC_RELAXED, __HIP_MEMORY_SCOPE_WORKGROUP) & 7u) == 7u) {
      if (e) st8_sc1(myp, u32x2{(unsigned)n - 1u, (unsigned)n}); else st4_sc1(myp, (unsigned)n);
    }
  };
  f32x4 tb[6];
#pragma unroll
  for (int i = 0; i < 6; ++i) tb[i] = *(const f32x4*)(smem + (i >> 1) * 4096 + hf * 2048 + (i & 1) * 1024 + lane * 16);
  const unsigned po = (unsigned)((hf * 16 + (lane >> 2)) * 2048 + r4 * 64 + (lane & 3) * 16), xo = (unsigned)(r4 * DM * 2 + hf * 1024 + lane * 16);
  const unsigned ho = (unsigned)(r4 * 128 + (((lane & 7) ^ (r4 & 7)) << 4) + (lane >> 3) * 4096 + hf * 32768);
  const rsrc_t rx = mkrs(xs_);
  rn = __builtin_amdgcn_raw_buffer_load_b128(rx, xo, set * 65536, 0);
  for (int bb = set; bb < NSEG; bb += 2) {
    if (pre) {
      asm volatile("s_waitcnt vmcnt(0)" : "+v"(q[0]), "+v"(q[1]), "+v"(q[2]), "+v"(q[3]), "+v"(qv) :: "memory");
      p[0] = q[0]; p[1] = q[1]; p[2] = q[2]; p[3] = q[3]; pv = qv;
    } else {
    if (LN1 && bb >= LN1) {
      volatile LAS unsigned* fw = (volatile LAS unsigned*)(smem + 16384 + 272);
      if (wave == 0) {
        for (int spins = 0;; ++spins) {
          const unsigned long long m = __ballot(ld4_sc1(cnt + (PWO + (lane & 31)) * 32 + (lane >> 5)) > (unsigned)bb);
          if ((unsigned)(m | m >> 32) == 0xFFFFFFFFu || give_up(spins, tmo, 11)) break;
          __builtin_amdgcn_s_sleep(LNSL);
        }
        *fw = (unsigned)bb;
      } else while (__builtin_amdgcn_readfirstlane(*fw) != (unsigned)bb) { }
    } else
    for (int spins = 0; !rdy(pv, bb, true); ++spins) {
      pv = ld4_sc1((const char*)cnt + pw2);
      if (rdy(pv, bb, true)) break;
      __builtin_amdgcn_s_sleep(LNSL);
      if (give_up(spins, tmo, 11)) break;
    }
    for (int spins = 0;; ++spins) {
      const bool safe = bb < BEARLY || rdy(pv, bb, false);
      WTM(slot, bb, 0);
      LS(0);
      asm volatile("s_nop 4\n\tglobal_load_dwordx4 %0, %5, %9 sc1\n\tglobal_load_dwordx4 %1, %6, %9 sc1\n\tglobal_load_dwordx4 %2, %7, %9 sc1\n\tglobal_load_dwordx4 %3, %8, %9 sc1\n\t"
                   "global_load_dword %4, %10, %11 sc1\n\ts_waitcnt vmcnt(0)"
                   : "=&v"(p[0]), "=&v"(p[1]), "=&v"(p[2]), "=&v"(p[3]), "=&v"(pv)
                   : "v"(po), "v"(po + 65536), "v"(po + 131072), "v"(po + 196608), "s"(pb + (size_t)bb * 32 * PROW), "v"(pw2), "s"(cb) : "memory");
      if (safe || __all(max(max(mx4(p[0]), mx4(p[1])), max(mx4(p[2]), mx4(p[3]))) != SENT) || give_up(spins, tmo, 11)) break;
    }
    }
    asm volatile("v_mov_b32 %0, %4\n\tv_mov_b32 %1, %5\n\tv_mov_b32 %2, %6\n\tv_mov_b32 %3, %7" : "=&v"(rv.x), "=&v"(rv.y), "=&v"(rv.z), "=&v"(rv.w) : "v"(rn.x), "v"(rn.y), "v"(rn.z), "v"(rn.w) : "memory");
    if (bb + 2 < NSEG) rn = __builtin_amdgcn_raw_buffer_load_b128(rx, xo, (bb + 2) * 65536, 0);
    LS(1);
    if (req && bb < BEH + 2) publish(bb >> 1);
    pre = LNP && bb + 2 < LN1 && rdy(pv, bb + 2, false);
    if (pre) {
      const rsrc_t rp = mkrs(pb + (size_t)(bb + 2) * 32 * PROW);
#pragma unroll
      for (int i = 0; i < 4; ++i) q[i] = bld16(rp, po + i * 65536);
      qv = __builtin_amdgcn_raw_buffer_load_b32(mkrs(cb), pw2, 0, 16);
    }
    ln_half(p, rv, tb, (volatile LAS float*)(smem + 16384 + ((bb >> 1) & 1) * 128), uni((const char*)x1) + (size_t)bb * 65536, xo, uni(h2c) + (size_t)bb * 65536, ho, wave, lane,
            LNA && bb >= BEH && wave == 7 ? myp : nullptr, (unsigned)(bb >> 1) + 1u);
    LS(2);
    {
      constexpr int NJ = ((NSEG - BEARLY) * 3 + ARMP - 1) / ARMP;
      static_assert(BEG == BEARLY && BEH == BEARLY && ARM0 + 2 * NJ + 6 <= ARMW, "");
      const int j0 = (bb - ARM0) >> 1, o = (8 * set + grp) * 8192 + tid * 16;
      for (int j = j0 * ARMP; bb >= ARM0 && j < (j0 + 1) * ARMP && j < (NSEG - BEARLY) * 3; ++j) {
        const int ab = BEARLY + j / 3;
        char* w0 = (char*)tmo - OFF_TMO;
        const u32x4 z = u32x4{SENT, SENT, SENT, SENT};
        char* p0 = w0 + (j % 3 == 0 ? OFF_O : (j % 3 == 1 ? OFF_P : OFF_P2)) + (size_t)ab * 262144 + o;
        st16_sc1(p0, z); st16_sc1(p0 + 131072, z);
        if (j % 3 == 2) st16_sc1(w0 + (set ? OFF_H2 : OFF_X1) + (size_t)ab * 65536 + (o & 65535), z);
        else if ((j % 3) * 131072 + o < 229376) st16_sc1(w0 + OFF_G + (size_t)ab * 229376 + (j % 3) * 131072 + o, z);
      }
      if (bb >= ARM0 && j0 == NJ && tid == 0) st4_sc1(cnt + (PARM + 8 * set + grp) * 32, 1u);
    }
    if (bb >= BEH && !LNA) publish((bb >> 1) + 1, 1);
    req = bb + 2 < NSEG && (bb >= BEH || rdy(pv, bb + 2, true));
    if (!req) {
      if (bb + 2 < NSEG) pv = ld4_sc1((const char*)cnt + pw2);
      WAIT_V(0);
      LS(3);
      publish((bb >> 1) + 1);
    }
    WTM(slot, bb, 2);
  }
}
#define L1G 0
__device__ __forceinline__ void l1_role(char* smem, int wave_id_, int c, const u16* __restrict__ x, const float* __restrict__ mod, const u16* __restrict__ cvec,
                                        const u16* __restrict__ wmod, char* __restrict__ h1c, unsigned*& cnt, unsigned* tmo, unsigned long long* mts, unsigned long long tag) {
  const int tid = opaque_tid(), lane = tid & 63, wave = WAVE_ID;
  u32x4 xr[8][2];
  const u16* src = x + (size_t)(8 * c + wave) * DM + lane * 8;
  xr[0][0] = ldnt<2>(src); xr[0][1] = ldnt<2>(src + 512);
  {
    const float sva = bf2f(cvec[tid]), svb = bf2f(cvec[tid + 512]);
    cnt += (ini_resolve1(smem, wave_id_, (const char*)mod - OFF_MOD, tag) >> 2) << 11;
    if (wave == 7) IS(6);
    f32x4 m[4];
    const rsrc_t rm = mkrs(mod);
#pragma unroll
    for (int k = 0; k < 4; ++k) m[k] = __builtin_bit_cast(f32x4, bld16(rm, (unsigned)(k * (2 * DM) + tid * 4) * 4));
    f32x4 v = (m[0] + m[1]) + (m[2] + m[3]);
    v = f32x4{bfr(v[0]), bfr(v[1]), bfr(v[2]), bfr(v[3])};
    const int a = tid >> 8, cc = tid & 255;
    if (a) v = f32x4{bfr(1.0f + v[0]), bfr(1.0f + v[1]), bfr(1.0f + v[2]), bfr(1.0f + v[3])};
    *(f32x4*)(smem + 4096 + a * 4096 + ((cc >> 7) * 2 + (cc & 1)) * 1024 + ((cc >> 1) & 63) * 16) = v;
    float* sv = (float*)(smem + 12288);
    sv[tid] = bfr(sva / (1.0f + __expf(-sva))); sv[tid + 512] = bfr(svb / (1.0f + __expf(-svb)));
  }
  __syncthreads();
  if (wave == 7) IS(7);
#pragma unroll
  for (int p = 0; p < 8; ++p) {
    f32x2 v[2][4];
#pragma unroll
    for (int i = 0; i < 2; ++i)
#pragma unroll
      for (int e = 0; e < 4; ++e) v[i][e] = f32x2{bflo(xr[p][i][e]), bfhi(xr[p][i][e])};
    const f32x2 s2 = ((v[0][0] + v[0][1]) + (v[0][2] + v[0][3])) + ((v[1][0] + v[1][1]) + (v[1][2] + v[1][3]));
    const float mean = wavesum(s2[0] + s2[1]) * (1.0f / DM);
    f32x2 q2 = f32x2{0, 0};
#pragma unroll
    for (int i = 0; i < 8; ++i) { const f32x2 d = v[i >> 2][i & 3] - f32x2{mean, mean}; q2 = pkfma(d, d, q2); }
    const float q = wavesum(q2[0] + q2[1]);
    if (p == 0 && wave == 0) STS((char*)tmo - OFF_TMO, 128 + (c >> 3) * 32 + 24 + (c & 7), 0);
    const float mb = bfr(mean), vb = bfr(q * (1.0f / DM));
    const float rden = 1.0f / bfr(sqrtf(bfr(vb + 1e-5f)));
    const int row = 256 * p + 8 * c + wave, rb = row & 31;
    char* dst = h1c + (size_t)(row >> 5) * 65536 + (lane >> 3) * 4096 + rb * 128 + (((lane & 7) ^ (rb & 7)) << 4);
    const char* ml = smem + lane * 16;
#pragma unroll
    for (int i = 0; i < 2; ++i) st16_sc1(dst + i * 32768, norm8(v[i], ml + i * 2048, mb, rden));
    WAIT_V(0);
    __builtin_amdgcn_s_barrier();
    if (tid == 0) st4_sc1(cnt + (PL1 + c) * 32, (unsigned)(p + 1));
    if (p == 0) {
      if (L1G) wait_k0(cnt, c, tmo, L1G == 1);
#pragma unroll
      for (int pp = 1; pp < 8; ++pp) { xr[pp][0] = ldnt<2>(src + (size_t)pp * 256 * DM); xr[pp][1] = ldnt<2>(src + (size_t)pp * 256 * DM + 512); }
    }
    if (wave == 0) WTS(c, p, 0);
    if (p == 0 && wave == 0) STS((char*)tmo - OFF_TMO, 128 + (c >> 3) * 32 + 24 + (c & 7), 1);
  }
  wait_k0(cnt, c, tmo);
  {
    const int cg = tid & 15, r0 = tid >> 4;
    const u16* wp = wmod + (size_t)r0 * (6 * DM) + 2 * DM + 128 * c + cg * 8;
    const float* sv = (const float*)(smem + 12288) + r0;
    f32x2 acc[4] = {f32x2{0, 0}, f32x2{0, 0}, f32x2{0, 0}, f32x2{0, 0}};
#pragma unroll 1
    for (int o = 0; o < 4; ++o) {
      u32x4 w[8];
#pragma unroll
      for (int i = 0; i < 8; ++i) w[i] = ldnt<1>(wp + (size_t)(o * 8 + i) * 32 * (6 * DM));
#pragma unroll
      for (int i = 0; i < 8; ++i) {
        const float s = sv[(o * 8 + i) * 32];
#pragma unroll
        for (int e = 0; e < 4; ++e) acc[e] = pkfma(f32x2{s, s}, f32x2{bflo(w[i][e]), bfhi(w[i][e])}, acc[e]);
      }
    }
    float* red = (float*)(smem + 16384);
    *(f32x4*)(red + r0 * 128 + cg * 8) = f32x4{acc[0][0], acc[0][1], acc[1][0], acc[1][1]};
    *(f32x4*)(red + r0 * 128 + cg * 8 + 4) = f32x4{acc[2][0], acc[2][1], acc[3][0], acc[3][1]};
    __syncthreads();
    if (tid < 128) {
      float s = 0;
#pragma unroll
      for (int r = 0; r < 32; ++r) s += red[r * 128 + tid];
      st4_sc1((char*)mod + OFF_MOD2 + (128 * c + tid) * 4, __builtin_bit_cast(unsigned, s));
    }
    WAIT_V(0);
    __builtin_amdgcn_s_barrier();
    if (tid == 0) st4_sc1(cnt + (PL1 + c) * 32, 9u);
  }
}
enum { R_QKV = 0, R_PART = 1, R_F1 = 2, R_PART2 = 3 };
struct Dep {
  const char* base; unsigned voff; unsigned mul, add;
};
__device__ __forceinline__ unsigned dep_eval(unsigned v, const Dep& d) {
  v = rowmin16(v * d.mul + d.add);
  return min(min(rdl(v, 0), rdl(v, 16)), min(rdl(v, 32), rdl(v, 48)));
}
typedef unsigned u32x32 __attribute__((ext_vector_type(32)));
typedef unsigned u32x16 __attribute__((ext_vector_type(16)));
__device__ __forceinline__ void fast_ld0(const u32x32 (&W)[4], u32x16& pd, unsigned ea, unsigned da, int& b, int& lf, int cp, unsigned sa, unsigned mu, unsigned ad,
    unsigned rw, unsigned ab, const char* p1, const char* p0, unsigned l1, unsigned l0, const unsigned (&av)[8]) {
  asm volatile("s_setprio 0\n\t"
    "s_waitcnt vmcnt(12)\n\t"
    "s_barrier\n\t"
    "ds_read_b128 v[96:99], %[ea] offset:0\n\t"
    "ds_read_b128 v[100:103], %[ea] offset:2048\n\t"
    "ds_read_b128 v[104:107], %[da] offset:0\n\t"
    "ds_read_b128 v[108:111], %[da] offset:2048\n\t"
    "ds_read_b128 v[112:115], %[ea] offset:4096\n\t"
    "ds_read_b128 v[116:119], %[ea] offset:6144\n\t"
    "s_branch 2f\n\t"
    "1:\n\t"
    "s_waitcnt vmcnt(12)\n\t"
    "s_nop 1\n\t"
    "s_barrier\n\t"
    "2:\n\t"
    "s_waitcnt lgkmcnt(4)\n\t"
    "v_mfma_f32_16x16x32_bf16 v[64:67], v[128:131], v[96:99], 0\n\t"
    "ds_read_b128 v[120:123], %[da] offset:4096\n\t"
    "s_add_u32 m0, %[l1], 0\n\t"
    "ds_write_b128 %[rw], v[80:83] offset:0\n\t"
    "v_mfma_f32_16x16x32_bf16 v[68:71], v[132:135], v[96:99], 0\n\t"
    "ds_read_b128 v[124:127], %[da] offset:6144\n\t"
    "global_load_lds_dwordx4 %[av0], s[36:37] sc1\n\t"
    "ds_write_b128 %[rw], v[84:87] offset:1024\n\t"
    "v_mfma_f32_16x16x32_bf16 v[72:75], v[128:131], v[100:103], 0\n\t"
    "s_add_u32 m0, %[l1], 32768\n\t"
    "ds_write_b128 %[rw], v[88:91] offset:2048\n\t"
    "v_mfma_f32_16x16x32_bf16 v[76:79], v[132:135], v[100:103], 0\n\t"
    "global_load_lds_dwordx4 %[av1], s[36:37] sc1\n\t"
    "ds_write_b128 %[rw], v[92:95] offset:3072\n\t"
    "s_waitcnt lgkmcnt(8)\n\t"
    "v_mfma_f32_16x16x32_bf16 v[64:67], v[136:139], v[104:107], v[64:67]\n\t"
    "ds_read_b128 v[96:99], %[ea] offset:8192\n\t"
    "s_add_u32 m0, %[l1], 4096\n\t"
    "v_mfma_f32_16x16x32_bf16 v[68:71], v[140:143], v[104:107], v[68:71]\n\t"
    "ds_read_b128 v[100:103], %[ea] offset:10240\n\t"
    "global_load_lds_dwordx4 %[av2], s[36:37] sc1\n\t"
    "v_mfma_f32_16x16x32_bf16 v[72:75], v[136:139], v[108:111], v[72:75]\n\t"
    "s_add_u32 m0, %[l1], 36864\n\t"
    "v_mfma_f32_16x16x32_bf16 v[76:79], v[140:143], v[108:111], v[76:79]\n\t"
    "global_load_lds_dwordx4 %[av3], s[36:37] sc1\n\t"
    "s_waitcnt lgkmcnt(8)\n\t"
    "v_mfma_f32_16x16x32_bf16 v[64:67], v[144:147], v[112:115], v[64:67]\n\t"
    "ds_read_b128 v[104:107], %[da] offset:8192\n\t"
    "s_add_u32 m0, %[l1], 8192\n\t"
    "v_mfma_f32_16x16x32_bf16 v[68:71], v[148:151], v[112:115], v[68:71]\n\t"
    "ds_read_b128 v[108:111], %[da] offset:10240\n\t"
    "global_load_lds_dwordx4 %[av4], s[36:37] sc1\n\t"
    "v_mfma_f32_16x16x32_bf16 v[72:75], v[144:147], v[116:119], v[72:75]\n\t"
    "s_add_u32 m0, %[l1], 40960\n\t"
    "v_mfma_f32_16x16x32_bf16 v[76:79], v[148:151], v[116:119], v[76:79]\n\t"
    "global_load_lds_dwordx4 %[av5], s[36:37] sc1\n\t"
    "s_waitcnt lgkmcnt(7)\n\t"
    "v_mfma_f32_16x16x32_bf16 v[64:67], v[152:155], v[120:123], v[64:67]\n\t"
    "ds_read_b128 v[112:115], %[ea] offset:12288\n\t"
    "s_add_u32 m0, %[l1], 12288\n\t"
    "v_mfma_f32_16x16x32_bf16 v[68:71], v[156:159], v[120:123], v[68:71]\n\t"
    "ds_read_b128 v[116:119], %[ea] offset:14336\n\t"
    "global_load_lds_dwordx4 %[av6], s[36:37] sc1\n\t"
    "v_mfma_f32_16x16x32_bf16 v[72:75], v[152:155], v[124:127], v[72:75]\n\t"
    "s_add_u32 m0, %[l1], 45056\n\t"
    "v_mfma_f32_16x16x32_bf16 v[76:79], v[156:159], v[124:127], v[76:79]\n\t"
    "global_load_lds_dwordx4 %[av7], s[36:37] sc1\n\t"
    "s_waitcnt lgkmcnt(4)\n\t"
    "v_mfma_f32_16x16x32_bf16 v[64:67], v[160:163], v[96:99], v[64:67]\n\t"
    "ds_read_b128 v[120:123], %[da] offset:12288\n\t"
    "v_mfma_f32_16x16x32_bf16 v[68:71], v[164:167], v[96:99], v[68:71]\n\t"
    "ds_read_b128 v[124:127], %[da] offset:14336\n\t"
    "v_mfma_f32_16x16x32_bf16 v[72:75], v[160:163], v[100:103], v[72:75]\n\t"
    "v_mfma_f32_16x16x32_bf16 v[76:79], v[164:167], v[100:103], v[76:79]\n\t"
    "s_waitcnt lgkmcnt(4)\n\t"
    "v_mfma_f32_16x16x32_bf16 v[64:67], v[168:171], v[104:107], v[64:67]\n\t"
    "ds_read_b128 v[96:99], %[ea] offset:16384\n\t"
    "v_mfma_f32_16x16x32_bf16 v[68:71], v[172:175], v[104:107], v[68:71]\n\t"
    "ds_read_b128 v[100:103], %[ea] offset:18432\n\t"
    "v_mfma_f32_16x16x32_bf16 v[72:75], v[168:171], v[108:111], v[72:75]\n\t"
    "v_mfma_f32_16x16x32_bf16 v[76:79], v[172:175], v[108:111], v[76:79]\n\t"
    "s_waitcnt lgkmcnt(4)\n\t"
    "v_mfma_f32_16x16x32_bf16 v[64:67], v[176:179], v[112:115], v[64:67]\n\t"
    "ds_read_b128 v[104:107], %[da] offset:16384\n\t"
    "v_mfma_f32_16x16x32_bf16 v[68:71], v[180:183], v[112:115], v[68:71]\n\t"
    "ds_read_b128 v[108:111], %[da] offset:18432\n\t"
    "v_mfma_f32_16x16x32_bf16 v[72:75], v[176:179], v[116:119], v[72:75]\n\t"
    "v_mfma_f32_16x16x32_bf16 v[76:79], v[180:183], v[116:119], v[76:79]\n\t"
    "s_waitcnt lgkmcnt(4)\n\t"
    "v_mfma_f32_16x16x32_bf16 v[64:67], v[184:187], v[120:123], v[64:67]\n\t"
    "ds_read_b128 v[112:115], %[ea] offset:20480\n\t"
    "v_mfma_f32_16x16x32_bf16 v[68:71], v[188:191], v[120:123], v[68:71]\n\t"
    "ds_read_b128 v[116:119], %[ea] offset:22528\n\t"
    "v_mfma_f32_16x16x32_bf16 v[72:75], v[184:187], v[124:127], v[72:75]\n\t"
    "v_mfma_f32_16x16x32_bf16 v[76:79], v[188:191], v[124:127], v[76:79]\n\t"
    "s_waitcnt vmcnt(12)\n\t"
    "s_barrier\n\t"
    "s_waitcnt lgkmcnt(4)\n\t"
    "v_mfma_f32_16x16x32_bf16 v[64:67], v[192:195], v[96:99], v[64:67]\n\t"
    "ds_read_b128 v[120:123], %[da] offset:20480\n\t"
    "s_add_u32 m0, %[l0], 0\n\t"
    "v_mfma_f32_16x16x32_bf16 v[68:71], v[196:199], v[96:99], v[68:71]\n\t"
    "ds_read_b128 v[124:127], %[da] offset:22528\n\t"
    "global_load_lds_dwordx4 %[av0], s[38:39] sc1\n\t"
    "v_mfma_f32_16x16x32_bf16 v[72:75], v[192:195], v[100:103], v[72:75]\n\t"
    "s_add_u32 m0, %[l0], 32768\n\t"
    "v_mfma_f32_16x16x32_bf16 v[76:79], v[196:199], v[100:103], v[76:79]\n\t"
    "global_load_lds_dwordx4 %[av1], s[38:39] sc1\n\t"
    "s_waitcnt lgkmcnt(4)\n\t"
    "v_mfma_f32_16x16x32_bf16 v[64:67], v[200:203], v[104:107], v[64:67]\n\t"
    "ds_read_b128 v[96:99], %[ea] offset:24576\n\t"
    "s_add_u32 m0, %[l0], 4096\n\t"
    "v_mfma_f32_16x16x32_bf16 v[68:71], v[204:207], v[104:107], v[68:71]\n\t"
    "ds_read_b128 v[100:103], %[ea] offset:26624\n\t"
    "global_load_lds_dwordx4 %[av2], s[38:39] sc1\n\t"
    "v_mfma_f32_16x16x32_bf16 v[72:75], v[200:203], v[108:111], v[72:75]\n\t"
    "s_add_u32 m0, %[l0], 36864\n\t"
    "v_mfma_f32_16x16x32_bf16 v[76:79], v[204:207], v[108:111], v[76:79]\n\t"
    "global_load_lds_dwordx4 %[av3], s[38:39] sc1\n\t"
    "s_add_u32 s36, s36, %[ab]\n\t"
    "s_addc_u32 s37, s37, 0\n\t"
    "s_waitcnt lgkmcnt(4)\n\t"
    "v_mfma_f32_16x16x32_bf16 v[64:67], v[208:211], v[112:115], v[64:67]\n\t"
    "ds_read_b128 v[104:107], %[da] offset:24576\n\t"
    "s_add_u32 m0, %[l0], 8192\n\t"
    "v_mfma_f32_16x16x32_bf16 v[68:71], v[212:215], v[112:115], v[68:71]\n\t"
    "ds_read_b128 v[108:111], %[da] offset:26624\n\t"
    "global_load_lds_dwordx4 %[av4], s[38:39] sc1\n\t"
    "v_mfma_f32_16x16x32_bf16 v[72:75], v[208:211], v[116:119], v[72:75]\n\t"
    "ds_read_b32 v39, %[sa]\n\t"
    "s_add_u32 m0, %[l0], 40960\n\t"
    "v_mfma_f32_16x16x32_bf16 v[76:79], v[212:215], v[116:119], v[76:79]\n\t"
    "v_xor_b32 %[sa], 0x100, %[sa]\n\t"
    "global_load_lds_dwordx4 %[av5], s[38:39] sc1\n\t"
    "s_xor_b32 %[l1], %[l1], 0x10000\n\t"
    "s_waitcnt lgkmcnt(5)\n\t"
    "v_mfma_f32_16x16x32_bf16 v[64:67], v[216:219], v[120:123], v[64:67]\n\t"
    "ds_read_b128 v[112:115], %[ea] offset:28672\n\t"
    "s_add_u32 m0, %[l0], 12288\n\t"
    "v_mfma_f32_16x16x32_bf16 v[68:71], v[220:223], v[120:123], v[68:71]\n\t"
    "ds_read_b128 v[116:119], %[ea] offset:30720\n\t"
    "global_load_lds_dwordx4 %[av6], s[38:39] sc1\n\t"
    "v_mfma_f32_16x16x32_bf16 v[72:75], v[216:219], v[124:127], v[72:75]\n\t"
    "v_xor_b32 %[ea], 0x10000, %[ea]\n\t"
    "s_add_u32 m0, %[l0], 45056\n\t"
    "v_mfma_f32_16x16x32_bf16 v[76:79], v[220:223], v[124:127], v[76:79]\n\t"
    "global_load_lds_dwordx4 %[av7], s[38:39] sc1\n\t"
    "s_waitcnt lgkmcnt(5)\n\t"
    "v_mfma_f32_16x16x32_bf16 v[64:67], v[224:227], v[96:99], v[64:67]\n\t"
    "ds_read_b128 v[120:123], %[da] offset:28672\n\t"
    "v_mfma_f32_16x16x32_bf16 v[68:71], v[228:231], v[96:99], v[68:71]\n\t"
    "ds_read_b128 v[124:127], %[da] offset:30720\n\t"
    "v_mfma_f32_16x16x32_bf16 v[72:75], v[224:227], v[100:103], v[72:75]\n\t"
    "v_xor_b32 %[da], 0x10000, %[da]\n\t"
    "v_mfma_f32_16x16x32_bf16 v[76:79], v[228:231], v[100:103], v[76:79]\n\t"
    "s_waitcnt lgkmcnt(5)\n\t"
    "v_mfma_f32_16x16x32_bf16 v[64:67], v[232:235], v[104:107], v[64:67]\n\t"
    "ds_read_b128 v[96:99], %[ea] offset:0\n\t"
    "v_mfma_f32_16x16x32_bf16 v[68:71], v[236:239], v[104:107], v[68:71]\n\t"
    "ds_read_b128 v[100:103], %[ea] offset:2048\n\t"
    "v_mfma_f32_16x16x32_bf16 v[72:75], v[232:235], v[108:111], v[72:75]\n\t"
    "v_mfma_f32_16x16x32_bf16 v[76:79], v[236:239], v[108:111], v[76:79]\n\t"
    "s_waitcnt lgkmcnt(4)\n\t"
    "v_mfma_f32_16x16x32_bf16 v[64:67], v[240:243], v[112:115], v[64:67]\n\t"
    "ds_read_b128 v[104:107], %[da] offset:0\n\t"
    "v_mad_u32_u24 v39, v39, %[mu], %[ad]\n\t"
    "v_mfma_f32_16x16x32_bf16 v[68:71], v[244:247], v[112:115], v[68:71]\n\t"
    "ds_read_b128 v[108:111], %[da] offset:2048\n\t"
    "v_cmp_ge_i32 vcc, %[lf], v39\n\t"
    "s_cmp_eq_u64 vcc, 0\n\t"
    "s_addc_u32 %[lf], %[lf], 0\n\t"
    "v_mfma_f32_16x16x32_bf16 v[72:75], v[240:243], v[116:119], v[72:75]\n\t"
    "v_mfma_f32_16x16x32_bf16 v[76:79], v[244:247], v[116:119], v[76:79]\n\t"
    "v_cmp_ge_i32 vcc, %[lf], v39\n\t"
    "s_cmp_eq_u64 vcc, 0\n\t"
    "s_addc_u32 %[lf], %[lf], 0\n\t"
    "s_waitcnt lgkmcnt(4)\n\t"
    "v_mfma_f32_16x16x32_bf16 v[80:83], v[248:251], v[120:123], v[64:67]\n\t"
    "ds_read_b128 v[112:115], %[ea] offset:4096\n\t"
    "s_min_i32 %[lf], %[lf], %[cp]\n\t"
    "s_add_u32 %[b], %[b], 1\n\t"
    "v_mfma_f32_16x16x32_bf16 v[84:87], v[252:255], v[120:123], v[68:71]\n\t"
    "ds_read_b128 v[116:119], %[ea] offset:6144\n\t"
    "v_mfma_f32_16x16x32_bf16 v[88:91], v[248:251], v[124:127], v[72:75]\n\t"
    "v_mfma_f32_16x16x32_bf16 v[92:95], v[252:255], v[124:127], v[76:79]\n\t"
    "s_add_u32 s38, s38, %[ab]\n\t"
    "s_addc_u32 s39, s39, 0\n\t"
    "s_xor_b32 %[l0], %[l0], 0x10000\n\t"
    "s_cmp_lt_i32 %[b], %[lf]\n\t"
    "s_cbranch_scc1 1b\n\t"
    "s_waitcnt lgkmcnt(0)\n\t"
    "s_nop 4"
    : "=&{v[80:95]}"(pd), [ea] "+&v"(ea), [da] "+&v"(da), [b] "+&s"(b), [lf] "+&s"(lf), "+{s[36:37]}"(p1), "+{s[38:39]}"(p0), [sa] "+&v"(sa), [l1] "+&s"(l1), [l0] "+&s"(l0)
    : [rw] "v"(rw), [ab] "s"(ab), [cp] "s"(cp), [mu] "s"(mu), [ad] "v"(ad), "{v[128:159]}"(W[0]), "{v[160:191]}"(W[1]), "{v[192:223]}"(W[2]), "{v[224:255]}"(W[3]), [av0] "v"(av[0]), [av1] "v"(av[1]), [av2] "v"(av[2]), [av3] "v"(av[3]), [av4] "v"(av[4]), [av5] "v"(av[5]), [av6] "v"(av[6]), [av7] "v"(av[7])
    : "v39", "v64", "v65", "v66", "v67", "v68", "v69", "v70", "v71", "v72", "v73", "v74", "v75", "v76", "v77", "v78", "v79", "v96", "v97", "v98", "v99", "v100", "v101", "v102", "v103", "v104", "v105", "v106", "v107", "v108", "v109", "v110", "v111", "v112", "v113", "v114", "v115", "v116", "v117", "v118", "v119", "v120", "v121", "v122", "v123", "v124", "v125", "v126", "v127", "vcc", "scc", "m0", "memory");
}
__device__ __forceinline__ void fast_ld2(const u32x32 (&W)[4], u32x16& pd, unsigned ea, unsigned da, int& b, int& lf, int cp, unsigned sa, unsigned mu, unsigned ad,
    unsigned rw, unsigned ab, const char* p1, const char* p0, unsigned l1, unsigned l0, const unsigned (&av)[8]) {
  asm volatile("s_setprio 0\n\t"
    "s_waitcnt vmcnt(6)\n\t"
    "s_barrier\n\t"
    "ds_read_b128 v[96:99], %[ea] offset:0\n\t"
    "ds_read_b128 v[100:103], %[ea] offset:2048\n\t"
    "ds_read_b128 v[104:107], %[da] offset:0\n\t"
    "ds_read_b128 v[108:111], %[da] offset:2048\n\t"
    "ds_read_b128 v[112:115], %[ea] offset:4096\n\t"
    "ds_read_b128 v[116:119], %[ea] offset:6144\n\t"
    "s_branch 2f\n\t"
    "1:\n\t"
    "s_waitcnt vmcnt(6)\n\t"
    "s_nop 1\n\t"
    "s_barrier\n\t"
    "2:\n\t"
    "s_waitcnt lgkmcnt(4)\n\t"
    "v_mfma_f32_16x16x32_bf16 v[64:67], v[128:131], v[96:99], 0\n\t"
    "ds_read_b128 v[120:123], %[da] offset:4096\n\t"
    "s_add_u32 m0, %[l1], 0\n\t"
    "ds_write_b128 %[rw], v[80:83] offset:0\n\t"
    "v_mfma_f32_16x16x32_bf16 v[68:71], v[132:135], v[96:99], 0\n\t"
    "ds_read_b128 v[124:127], %[da] offset:6144\n\t"
    "global_load_lds_dwordx4 %[av0], s[36:37] sc1\n\t"
    "ds_write_b128 %[rw], v[84:87] offset:1024\n\t"
    "v_mfma_f32_16x16x32_bf16 v[72:75], v[128:131], v[100:103], 0\n\t"
    "ds_write_b128 %[rw], v[88:91] offset:2048\n\t"
    "v_mfma_f32_16x16x32_bf16 v[76:79], v[132:135], v[100:103], 0\n\t"
    "ds_write_b128 %[rw], v[92:95] offset:3072\n\t"
    "s_waitcnt lgkmcnt(8)\n\t"
    "v_mfma_f32_16x16x32_bf16 v[64:67], v[136:139], v[104:107], v[64:67]\n\t"
    "ds_read_b128 v[96:99], %[ea] offset:8192\n\t"
    "s_add_u32 m0, %[l1], 4096\n\t"
    "v_mfma_f32_16x16x32_bf16 v[68:71], v[140:143], v[104:107], v[68:71]\n\t"
    "ds_read_b128 v[100:103], %[ea] offset:10240\n\t"
    "global_load_lds_dwordx4 %[av2], s[36:37] sc1\n\t"
    "v_mfma_f32_16x16x32_bf16 v[72:75], v[136:139], v[108:111], v[72:75]\n\t"
    "v_mfma_f32_16x16x32_bf16 v[76:79], v[140:143], v[108:111], v[76:79]\n\t"
    "s_waitcnt lgkmcnt(8)\n\t"
    "v_mfma_f32_16x16x32_bf16 v[64:67], v[144:147], v[112:115], v[64:67]\n\t"
    "ds_read_b128 v[104:107], %[da] offset:8192\n\t"
    "s_add_u32 m0, %[l1], 8192\n\t"
    "v_mfma_f32_16x16x32_bf16 v[68:71], v[148:151], v[112:115], v[68:71]\n\t"
    "ds_read_b128 v[108:111], %[da] offset:10240\n\t"
    "global_load_lds_dwordx4 %[av4], s[36:37] sc1\n\t"
    "v_mfma_f32_16x16x32_bf16 v[72:75], v[144:147], v[116:119], v[72:75]\n\t"
    "v_mfma_f32_16x16x32_bf16 v[76:79], v[148:151], v[116:119], v[76:79]\n\t"
    "s_waitcnt lgkmcnt(7)\n\t"
    "v_mfma_f32_16x16x32_bf16 v[64:67], v[152:155], v[120:123], v[64:67]\n\t"
    "ds_read_b128 v[112:115], %[ea] offset:12288\n\t"
    "s_add_u32 m0, %[l1], 12288\n\t"
    "v_mfma_f32_16x16x32_bf16 v[68:71], v[156:159], v[120:123], v[68:71]\n\t"
    "ds_read_b128 v[116:119], %[ea] offset:14336\n\t"
    "global_load_lds_dwordx4 %[av6], s[36:37] sc1\n\t"
    "v_mfma_f32_16x16x32_bf16 v[72:75], v[152:155], v[124:127], v[72:75]\n\t"
    "v_mfma_f32_16x16x32_bf16 v[76:79], v[156:159], v[124:127], v[76:79]\n\t"
    "s_waitcnt lgkmcnt(4)\n\t"
    "v_mfma_f32_16x16x32_bf16 v[64:67], v[160:163], v[96:99], v[64:67]\n\t"
    "ds_read_b128 v[120:123], %[da] offset:12288\n\t"
    "v_mfma_f32_16x16x32_bf16 v[68:71], v[164:167], v[96:99], v[68:71]\n\t"
    "ds_read_b128 v[124:127], %[da] offset:14336\n\t"
    "v_mfma_f32_16x16x32_bf16 v[72:75], v[160:163], v[100:103], v[72:75]\n\t"
    "v_mfma_f32_16x16x32_bf16 v[76:79], v[164:167], v[100:103], v[76:79]\n\t"
    "s_waitcnt lgkmcnt(4)\n\t"
    "v_mfma_f32_16x16x32_bf16 v[64:67], v[168:171], v[104:107], v[64:67]\n\t"
    "ds_read_b128 v[96:99], %[ea] offset:16384\n\t"
    "v_mfma_f32_16x16x32_bf16 v[68:71], v[172:175], v[104:107], v[68:71]\n\t"
    "ds_read_b128 v[100:103], %[ea] offset:18432\n\t"
    "v_mfma_f32_16x16x32_bf16 v[72:75], v[168:171], v[108:111], v[72:75]\n\t"
    "v_mfma_f32_16x16x32_bf16 v[76:79], v[172:175], v[108:111], v[76:79]\n\t"
    "s_waitcnt lgkmcnt(4)\n\t"
    "v_mfma_f32_16x16x32_bf16 v[64:67], v[176:179], v[112:115], v[64:67]\n\t"
    "ds_read_b128 v[104:107], %[da] offset:16384\n\t"
    "v_mfma_f32_16x16x32_bf16 v[68:71], v[180:183], v[112:115], v[68:71]\n\t"
    "ds_read_b128 v[108:111], %[da] offset:18432\n\t"
    "v_mfma_f32_16x16x32_bf16 v[72:75], v[176:179], v[116:119], v[72:75]\n\t"
    "v_mfma_f32_16x16x32_bf16 v[76:79], v[180:183], v[116:119], v[76:79]\n\t"
    "s_waitcnt lgkmcnt(4)\n\t"
    "v_mfma_f32_16x16x32_bf16 v[64:67], v[184:187], v[120:123], v[64:67]\n\t"
    "ds_read_b128 v[112:115], %[ea] offset:20480\n\t"
    "v_mfma_f32_16x16x32_bf16 v[68:71], v[188:191], v[120:123], v[68:71]\n\t"
    "ds_read_b128 v[116:119], %[ea] offset:22528\n\t"
    "v_mfma_f32_16x16x32_bf16 v[72:75], v[184:187], v[124:127], v[72:75]\n\t"
    "v_mfma_f32_16x16x32_bf16 v[76:79], v[188:191], v[124:127], v[76:79]\n\t"
    "s_waitcnt vmcnt(6)\n\t"
    "s_barrier\n\t"
    "s_waitcnt lgkmcnt(4)\n\t"
    "v_mfma_f32_16x16x32_bf16 v[64:67], v[192:195], v[96:99], v[64:67]\n\t"
    "ds_read_b128 v[120:123], %[da] offset:20480\n\t"
    "s_add_u32 m0, %[l0], 0\n\t"
    "v_mfma_f32_16x16x32_bf16 v[68:71], v[196:199], v[96:99], v[68:71]\n\t"
    "ds_read_b128 v[124:127], %[da] offset:22528\n\t"
    "global_load_lds_dwordx4 %[av0], s[38:39] sc1\n\t"
    "v_mfma_f32_16x16x32_bf16 v[72:75], v[192:195], v[100:103], v[72:75]\n\t"
    "v_mfma_f32_16x16x32_bf16 v[76:79], v[196:199], v[100:103], v[76:79]\n\t"
    "s_waitcnt lgkmcnt(4)\n\t"
    "v_mfma_f32_16x16x32_bf16 v[64:67], v[200:203], v[104:107], v[64:67]\n\t"
    "ds_read_b128 v[96:99], %[ea] offset:24576\n\t"
    "s_add_u32 m0, %[l0], 4096\n\t"
    "v_mfma_f32_16x16x32_bf16 v[68:71], v[204:207], v[104:107], v[68:71]\n\t"
    "ds_read_b128 v[100:103], %[ea] offset:26624\n\t"
    "global_load_lds_dwordx4 %[av2], s[38:39] sc1\n\t"
    "v_mfma_f32_16x16x32_bf16 v[72:75], v[200:203], v[108:111], v[72:75]\n\t"
    "v_mfma_f32_16x16x32_bf16 v[76:79], v[204:207], v[108:111], v[76:79]\n\t"
    "s_add_u32 s36, s36, %[ab]\n\t"
    "s_addc_u32 s37, s37, 0\n\t"
    "s_waitcnt lgkmcnt(4)\n\t"
    "v_mfma_f32_16x16x32_bf16 v[64:67], v[208:211], v[112:115], v[64:67]\n\t"
    "ds_read_b128 v[104:107], %[da] offset:24576\n\t"
    "s_add_u32 m0, %[l0], 8192\n\t"
    "v_mfma_f32_16x16x32_bf16 v[68:71], v[212:215], v[112:115], v[68:71]\n\t"
    "ds_read_b128 v[108:111], %[da] offset:26624\n\t"
    "global_load_lds_dwordx4 %[av4], s[38:39] sc1\n\t"
    "v_mfma_f32_16x16x32_bf16 v[72:75], v[208:211], v[116:119], v[72:75]\n\t"
    "ds_read_b32 v39, %[sa]\n\t"
    "v_mfma_f32_16x16x32_bf16 v[76:79], v[212:215], v[116:119], v[76:79]\n\t"
    "v_xor_b32 %[sa], 0x100, %[sa]\n\t"
    "s_xor_b32 %[l1], %[l1], 0x10000\n\t"
    "s_waitcnt lgkmcnt(5)\n\t"
    "v_mfma_f32_16x16x32_bf16 v[64:67], v[216:219], v[120:123], v[64:67]\n\t"
    "ds_read_b128 v[112:115], %[ea] offset:28672\n\t"
    "s_add_u32 m0, %[l0], 12288\n\t"
    "v_mfma_f32_16x16x32_bf16 v[68:71], v[220:223], v[120:123], v[68:71]\n\t"
    "ds_read_b128 v[116:119], %[ea] offset:30720\n\t"
    "global_load_lds_dwordx4 %[av6], s[38:39] sc1\n\t"
    "v_mfma_f32_16x16x32_bf16 v[72:75], v[216:219], v[124:127], v[72:75]\n\t"
    "v_xor_b32 %[ea], 0x10000, %[ea]\n\t"
    "v_mfma_f32_16x16x32_bf16 v[76:79], v[220:223], v[124:127], v[76:79]\n\t"
    "s_waitcnt lgkmcnt(5)\n\t"
    "v_mfma_f32_16x16x32_bf16 v[64:67], v[224:227], v[96:99], v[64:67]\n\t"
    "ds_read_b128 v[120:123], %[da] offset:28672\n\t"
    "v_mfma_f32_16x16x32_bf16 v[68:71], v[228:231], v[96:99], v[68:71]\n\t"
    "ds_read_b128 v[124:127], %[da] offset:30720\n\t"
    "v_mfma_f32_16x16x32_bf16 v[72:75], v[224:227], v[100:103], v[72:75]\n\t"
    "v_xor_b32 %[da], 0x10000, %[da]\n\t"
    "v_mfma_f32_16x16x32_bf16 v[76:79], v[228:231], v[100:103], v[76:79]\n\t"
    "s_waitcnt lgkmcnt(5)\n\t"
    "v_mfma_f32_16x16x32_bf16 v[64:67], v[232:235], v[104:107], v[64:67]\n\t"
    "ds_read_b128 v[96:99], %[ea] offset:0\n\t"
    "v_mfma_f32_16x16x32_bf16 v[68:71], v[236:239], v[104:107], v[68:71]\n\t"
    "ds_read_b128 v[100:103], %[ea] offset:2048\n\t"
    "v_mfma_f32_16x16x32_bf16 v[72:75], v[232:235], v[108:111], v[72:75]\n\t"
    "v_mfma_f32_16x16x32_bf16 v[76:79], v[236:239], v[108:111], v[76:79]\n\t"
    "s_waitcnt lgkmcnt(4)\n\t"
    "v_mfma_f32_16x16x32_bf16 v[64:67], v[240:243], v[112:115], v[64:67]\n\t"
    "ds_read_b128 v[104:107], %[da] offset:0\n\t"
    "v_mad_u32_u24 v39, v39, %[mu], %[ad]\n\t"
    "v_mfma_f32_16x16x32_bf16 v[68:71], v[244:247], v[112:115], v[68:71]\n\t"
    "ds_read_b128 v[108:111], %[da] offset:2048\n\t"
    "v_cmp_ge_i32 vcc, %[lf], v39\n\t"
    "s_cmp_eq_u64 vcc, 0\n\t"
    "s_addc_u32 %[lf], %[lf], 0\n\t"
    "v_mfma_f32_16x16x32_bf16 v[72:75], v[240:243], v[116:119], v[72:75]\n\t"
    "v_mfma_f32_16x16x32_bf16 v[76:79], v[244:247], v[116:119], v[76:79]\n\t"
    "v_cmp_ge_i32 vcc, %[lf], v39\n\t"
    "s_cmp_eq_u64 vcc, 0\n\t"
    "s_addc_u32 %[lf], %[lf], 0\n\t"
    "s_waitcnt lgkmcnt(4)\n\t"
    "v_mfma_f32_16x16x32_bf16 v[80:83], v[248:251], v[120:123], v[64:67]\n\t"
    "ds_read_b128 v[112:115], %[ea] offset:4096\n\t"
    "s_min_i32 %[lf], %[lf], %[cp]\n\t"
    "s_add_u32 %[b], %[b], 1\n\t"
    "v_mfma_f32_16x16x32_bf16 v[84:87], v[252:255], v[120:123], v[68:71]\n\t"
    "ds_read_b128 v[116:119], %[ea] offset:6144\n\t"
    "v_mfma_f32_16x16x32_bf16 v[88:91], v[248:251], v[124:127], v[72:75]\n\t"
    "v_mfma_f32_16x16x32_bf16 v[92:95], v[252:255], v[124:127], v[76:79]\n\t"
    "s_add_u32 s38, s38, %[ab]\n\t"
    "s_addc_u32 s39, s39, 0\n\t"
    "s_xor_b32 %[l0], %[l0], 0x10000\n\t"
    "s_cmp_lt_i32 %[b], %[lf]\n\t"
    "s_cbranch_scc1 1b\n\t"
    "s_waitcnt lgkmcnt(0)\n\t"
    "s_nop 4"
    : "=&{v[80:95]}"(pd), [ea] "+&v"(ea), [da] "+&v"(da), [b] "+&s"(b), [lf] "+&s"(lf), "+{s[36:37]}"(p1), "+{s[38:39]}"(p0), [sa] "+&v"(sa), [l1] "+&s"(l1), [l0] "+&s"(l0)
    : [rw] "v"(rw), [ab] "s"(ab), [cp] "s"(cp), [mu] "s"(mu), [ad] "v"(ad), "{v[128:159]}"(W[0]), "{v[160:191]}"(W[1]), "{v[192:223]}"(W[2]), "{v[224:255]}"(W[3]), [av0] "v"(av[0]), [av1] "v"(av[1]), [av2] "v"(av[2]), [av3] "v"(av[3]), [av4] "v"(av[4]), [av5] "v"(av[5]), [av6] "v"(av[6]), [av7] "v"(av[7])
    : "v39", "v64", "v65", "v66", "v67", "v68", "v69", "v70", "v71", "v72", "v73", "v74", "v75", "v76", "v77", "v78", "v79", "v96", "v97", "v98", "v99", "v100", "v101", "v102", "v103", "v104", "v105", "v106", "v107", "v108", "v109", "v110", "v111", "v112", "v113", "v114", "v115", "v116", "v117", "v118", "v119", "v120", "v121", "v122", "v123", "v124", "v125", "v126", "v127", "vcc", "scc", "m0", "memory");
}
__device__ __forceinline__ void fast_st0(const u32x32 (&W)[4], u32x16& pd, unsigned ea, unsigned da, int& b, int& lf, int cp, unsigned sa, unsigned mu, unsigned ad,
    unsigned rw, unsigned ab, const char* p1, const char* p0, unsigned co0, unsigned co1, int po, int pf, unsigned w0, unsigned sl, unsigned dv, const char* db, const char* pg) {
  unsigned st;
  asm volatile("s_barrier\n\t"
    "ds_read_b128 v[96:99], %[ea] offset:0\n\t"
    "ds_read_b128 v[100:103], %[ea] offset:2048\n\t"
    "ds_read_b128 v[104:107], %[da] offset:0\n\t"
    "ds_read_b128 v[108:111], %[da] offset:2048\n\t"
    "ds_read_b128 v[112:115], %[ea] offset:4096\n\t"
    "ds_read_b128 v[116:119], %[ea] offset:6144\n\t"
    "s_branch 2f\n\t"
    "1:\n\t"
    "s_barrier\n\t"
    "2:\n\t"
    "s_waitcnt lgkmcnt(4)\n\t"
    "v_mfma_f32_16x16x32_bf16 v[64:67], v[128:131], v[96:99], 0\n\t"
    "ds_read_b128 v[120:123], %[da] offset:4096\n\t"
    "v_mfma_f32_16x16x32_bf16 v[68:71], v[132:135], v[96:99], 0\n\t"
    "ds_read_b128 v[124:127], %[da] offset:6144\n\t"
    "v_mfma_f32_16x16x32_bf16 v[72:75], v[128:131], v[100:103], 0\n\t"
    "v_mfma_f32_16x16x32_bf16 v[76:79], v[132:135], v[100:103], 0\n\t"
    "s_waitcnt lgkmcnt(4)\n\t"
    "v_mfma_f32_16x16x32_bf16 v[64:67], v[136:139], v[104:107], v[64:67]\n\t"
    "ds_read_b128 v[96:99], %[ea] offset:8192\n\t"
    "v_mfma_f32_16x16x32_bf16 v[68:71], v[140:143], v[104:107], v[68:71]\n\t"
    "ds_read_b128 v[100:103], %[ea] offset:10240\n\t"
    "v_mfma_f32_16x16x32_bf16 v[72:75], v[136:139], v[108:111], v[72:75]\n\t"
    "v_mfma_f32_16x16x32_bf16 v[76:79], v[140:143], v[108:111], v[76:79]\n\t"
    "s_waitcnt lgkmcnt(4)\n\t"
    "v_mfma_f32_16x16x32_bf16 v[64:67], v[144:147], v[112:115], v[64:67]\n\t"
    "ds_read_b128 v[104:107], %[da] offset:8192\n\t"
    "v_mfma_f32_16x16x32_bf16 v[68:71], v[148:151], v[112:115], v[68:71]\n\t"
    "ds_read_b128 v[108:111], %[da] offset:10240\n\t"
    "v_mfma_f32_16x16x32_bf16 v[72:75], v[144:147], v[116:119], v[72:75]\n\t"
    "v_mfma_f32_16x16x32_bf16 v[76:79], v[148:151], v[116:119], v[76:79]\n\t"
    "s_waitcnt lgkmcnt(4)\n\t"
    "v_mfma_f32_16x16x32_bf16 v[64:67], v[152:155], v[120:123], v[64:67]\n\t"
    "ds_read_b128 v[112:115], %[ea] offset:12288\n\t"
    "v_mfma_f32_16x16x32_bf16 v[68:71], v[156:159], v[120:123], v[68:71]\n\t"
    "ds_read_b128 v[116:119], %[ea] offset:14336\n\t"
    "v_mfma_f32_16x16x32_bf16 v[72:75], v[152:155], v[124:127], v[72:75]\n\t"
    "v_mfma_f32_16x16x32_bf16 v[76:79], v[156:159], v[124:127], v[76:79]\n\t"
    "s_waitcnt lgkmcnt(4)\n\t"
    "v_mfma_f32_16x16x32_bf16 v[64:67], v[160:163], v[96:99], v[64:67]\n\t"
    "ds_read_b128 v[120:123], %[da] offset:12288\n\t"
    "v_mfma_f32_16x16x32_bf16 v[68:71], v[164:167], v[96:99], v[68:71]\n\t"
    "ds_read_b128 v[124:127], %[da] offset:14336\n\t"
    "v_mfma_f32_16x16x32_bf16 v[72:75], v[160:163], v[100:103], v[72:75]\n\t"
    "v_mfma_f32_16x16x32_bf16 v[76:79], v[164:167], v[100:103], v[76:79]\n\t"
    "s_waitcnt lgkmcnt(4)\n\t"
    "v_mfma_f32_16x16x32_bf16 v[64:67], v[168:171], v[104:107], v[64:67]\n\t"
    "ds_read_b128 v[96:99], %[ea] offset:16384\n\t"
    "v_mfma_f32_16x16x32_bf16 v[68:71], v[172:175], v[104:107], v[68:71]\n\t"
    "ds_read_b128 v[100:103], %[ea] offset:18432\n\t"
    "v_mfma_f32_16x16x32_bf16 v[72:75], v[168:171], v[108:111], v[72:75]\n\t"
    "v_mfma_f32_16x16x32_bf16 v[76:79], v[172:175], v[108:111], v[76:79]\n\t"
    "s_waitcnt lgkmcnt(4)\n\t"
    "v_mfma_f32_16x16x32_bf16 v[64:67], v[176:179], v[112:115], v[64:67]\n\t"
    "ds_read_b128 v[104:107], %[da] offset:16384\n\t"
    "v_mfma_f32_16x16x32_bf16 v[68:71], v[180:183], v[112:115], v[68:71]\n\t"
    "ds_read_b128 v[108:111], %[da] offset:18432\n\t"
    "v_mfma_f32_16x16x32_bf16 v[72:75], v[176:179], v[116:119], v[72:75]\n\t"
    "v_mfma_f32_16x16x32_bf16 v[76:79], v[180:183], v[116:119], v[76:79]\n\t"
    "s_waitcnt lgkmcnt(4)\n\t"
    "v_mfma_f32_16x16x32_bf16 v[64:67], v[184:187], v[120:123], v[64:67]\n\t"
    "ds_read_b128 v[112:115], %[ea] offset:20480\n\t"
    "v_mfma_f32_16x16x32_bf16 v[68:71], v[188:191], v[120:123], v[68:71]\n\t"
    "ds_read_b128 v[116:119], %[ea] offset:22528\n\t"
    "v_mfma_f32_16x16x32_bf16 v[72:75], v[184:187], v[124:127], v[72:75]\n\t"
    "v_mfma_f32_16x16x32_bf16 v[76:79], v[188:191], v[124:127], v[76:79]\n\t"
    "s_waitcnt vmcnt(0)\n\t"
    "s_barrier\n\t"
    "s_waitcnt lgkmcnt(4)\n\t"
    "v_mfma_f32_16x16x32_bf16 v[64:67], v[192:195], v[96:99], v[64:67]\n\t"
    "ds_read_b128 v[120:123], %[da] offset:20480\n\t"
    "v_mfma_f32_16x16x32_bf16 v[68:71], v[196:199], v[96:99], v[68:71]\n\t"
    "ds_read_b128 v[124:127], %[da] offset:22528\n\t"
    "v_mfma_f32_16x16x32_bf16 v[72:75], v[192:195], v[100:103], v[72:75]\n\t"
    "ds_read_b128 v[48:51], %[rw] offset:0\n\t"
    "v_mfma_f32_16x16x32_bf16 v[76:79], v[196:199], v[100:103], v[76:79]\n\t"
    "ds_read_b128 v[52:55], %[rw] offset:1024\n\t"
    "s_cmp_eq_u32 %[w0], 0\n\t"
    "s_cbranch_scc1 5f\n\t"
    "6:\n\t"
    "s_waitcnt lgkmcnt(6)\n\t"
    "v_mfma_f32_16x16x32_bf16 v[64:67], v[200:203], v[104:107], v[64:67]\n\t"
    "ds_read_b128 v[96:99], %[ea] offset:24576\n\t"
    "v_mfma_f32_16x16x32_bf16 v[68:71], v[204:207], v[104:107], v[68:71]\n\t"
    "ds_read_b128 v[100:103], %[ea] offset:26624\n\t"
    "v_mfma_f32_16x16x32_bf16 v[72:75], v[200:203], v[108:111], v[72:75]\n\t"
    "ds_read_b128 v[56:59], %[rw] offset:2048\n\t"
    "v_mfma_f32_16x16x32_bf16 v[76:79], v[204:207], v[108:111], v[76:79]\n\t"
    "ds_read_b128 v[60:63], %[rw] offset:3072\n\t"
    "s_waitcnt lgkmcnt(0)\n\t"
    "v_mfma_f32_16x16x32_bf16 v[64:67], v[208:211], v[112:115], v[64:67]\n\t"
    "ds_read_b128 v[104:107], %[da] offset:24576\n\t"
    "v_add_f32 v48, v48, v80\n\t"
    "v_add_f32 v49, v49, v81\n\t"
    "v_mfma_f32_16x16x32_bf16 v[68:71], v[212:215], v[112:115], v[68:71]\n\t"
    "ds_read_b128 v[108:111], %[da] offset:26624\n\t"
    "v_add_f32 v50, v50, v82\n\t"
    "v_mfma_f32_16x16x32_bf16 v[72:75], v[208:211], v[116:119], v[72:75]\n\t"
    "ds_read_b32 v39, %[sa]\n\t"
    "v_add_f32 v51, v51, v83\n\t"
    "v_mfma_f32_16x16x32_bf16 v[76:79], v[212:215], v[116:119], v[76:79]\n\t"
    "v_xor_b32 %[sa], 0x100, %[sa]\n\t"
    "v_add_f32 v52, v52, v84\n\t"
    "v_add_f32 v53, v53, v85\n\t"
    "v_mfma_f32_16x16x32_bf16 v[64:67], v[216:219], v[120:123], v[64:67]\n\t"
    "ds_read_b128 v[112:115], %[ea] offset:28672\n\t"
    "v_add_f32 v54, v54, v86\n\t"
    "v_mfma_f32_16x16x32_bf16 v[68:71], v[220:223], v[120:123], v[68:71]\n\t"
    "ds_read_b128 v[116:119], %[ea] offset:30720\n\t"
    "v_add_f32 v55, v55, v87\n\t"
    "v_mfma_f32_16x16x32_bf16 v[72:75], v[216:219], v[124:127], v[72:75]\n\t"
    "v_xor_b32 %[ea], 0x10000, %[ea]\n\t"
    "v_add_f32 v56, v56, v88\n\t"
    "v_add_f32 v57, v57, v89\n\t"
    "v_mfma_f32_16x16x32_bf16 v[76:79], v[220:223], v[124:127], v[76:79]\n\t"
    "v_add_f32 v58, v58, v90\n\t"
    "v_mfma_f32_16x16x32_bf16 v[64:67], v[224:227], v[96:99], v[64:67]\n\t"
    "ds_read_b128 v[120:123], %[da] offset:28672\n\t"
    "v_add_f32 v59, v59, v91\n\t"
    "v_mfma_f32_16x16x32_bf16 v[68:71], v[228:231], v[96:99], v[68:71]\n\t"
    "ds_read_b128 v[124:127], %[da] offset:30720\n\t"
    "v_add_f32 v60, v60, v92\n\t"
    "v_mfma_f32_16x16x32_bf16 v[72:75], v[224:227], v[100:103], v[72:75]\n\t"
    "v_xor_b32 %[da], 0x10000, %[da]\n\t"
    "v_add_f32 v61, v61, v93\n\t"
    "v_add_f32 v62, v62, v94\n\t"
    "v_mfma_f32_16x16x32_bf16 v[76:79], v[228:231], v[100:103], v[76:79]\n\t"
    "v_add_f32 v63, v63, v95\n\t"
    "s_waitcnt lgkmcnt(5)\n\t"
    "v_mfma_f32_16x16x32_bf16 v[64:67], v[232:235], v[104:107], v[64:67]\n\t"
    "ds_read_b128 v[96:99], %[ea] offset:0\n\t"
    "v_cvt_pk_bf16_f32 v40, v48, v49\n\t"
    "v_mfma_f32_16x16x32_bf16 v[68:71], v[236:239], v[104:107], v[68:71]\n\t"
    "ds_read_b128 v[100:103], %[ea] offset:2048\n\t"
    "v_cvt_pk_bf16_f32 v41, v50, v51\n\t"
    "v_cvt_pk_bf16_f32 v42, v52, v53\n\t"
    "v_mfma_f32_16x16x32_bf16 v[72:75], v[232:235], v[108:111], v[72:75]\n\t"
    "v_cvt_pk_bf16_f32 v43, v54, v55\n\t"
    "v_mfma_f32_16x16x32_bf16 v[76:79], v[236:239], v[108:111], v[76:79]\n\t"
    "global_store_dwordx4 %[co0], v[40:43], s[36:37] sc1\n\t"
    "s_waitcnt lgkmcnt(4)\n\t"
    "v_mfma_f32_16x16x32_bf16 v[64:67], v[240:243], v[112:115], v[64:67]\n\t"
    "ds_read_b128 v[104:107], %[da] offset:0\n\t"
    "v_mad_u32_u24 v39, v39, %[mu], %[ad]\n\t"
    "v_cvt_pk_bf16_f32 v44, v56, v57\n\t"
    "v_cvt_pk_bf16_f32 v45, v58, v59\n\t"
    "v_mfma_f32_16x16x32_bf16 v[68:71], v[244:247], v[112:115], v[68:71]\n\t"
    "ds_read_b128 v[108:111], %[da] offset:2048\n\t"
    "v_cmp_ge_i32 vcc, %[lf], v39\n\t"
    "s_cmp_eq_u64 vcc, 0\n\t"
    "s_addc_u32 %[lf], %[lf], 0\n\t"
    "v_cvt_pk_bf16_f32 v46, v60, v61\n\t"
    "v_mfma_f32_16x16x32_bf16 v[72:75], v[240:243], v[116:119], v[72:75]\n\t"
    "v_cvt_pk_bf16_f32 v47, v62, v63\n\t"
    "v_mfma_f32_16x16x32_bf16 v[76:79], v[244:247], v[116:119], v[76:79]\n\t"
    "v_cmp_ge_i32 vcc, %[lf], v39\n\t"
    "s_cmp_eq_u64 vcc, 0\n\t"
    "s_addc_u32 %[lf], %[lf], 0\n\t"
    "global_store_dwordx4 %[co1], v[44:47], s[36:37] sc1\n\t"
    "s_waitcnt lgkmcnt(4)\n\t"
    "v_mfma_f32_16x16x32_bf16 v[80:83], v[248:251], v[120:123], v[64:67]\n\t"
    "ds_read_b128 v[112:115], %[ea] offset:4096\n\t"
    "s_min_i32 %[lf], %[lf], %[cp]\n\t"
    "s_add_u32 %[b], %[b], 1\n\t"
    "v_mfma_f32_16x16x32_bf16 v[84:87], v[252:255], v[120:123], v[68:71]\n\t"
    "ds_read_b128 v[116:119], %[ea] offset:6144\n\t"
    "v_mfma_f32_16x16x32_bf16 v[88:91], v[248:251], v[124:127], v[72:75]\n\t"
    "v_mfma_f32_16x16x32_bf16 v[92:95], v[252:255], v[124:127], v[76:79]\n\t"
    "s_mov_b64 s[36:37], s[38:39]\n\t"
    "s_add_u32 s38, s38, %[ab]\n\t"
    "s_addc_u32 s39, s39, 0\n\t"
    "s_cmp_lt_i32 %[b], %[lf]\n\t"
    "s_cbranch_scc1 1b\n\t"
    "s_waitcnt lgkmcnt(0)\n\t"
    "s_nop 4\n\t"
    "s_branch 9f\n\t"
    "5:\n\t"
    "s_add_u32 %[st], %[b], %[po]\n\t"
    "s_max_i32 %[st], %[st], %[p0]\n\t"
    "v_mov_b32 v39, %[st]\n\t"
    "v_mov_b32 v38, 0\n\t"
    "s_mov_b64 exec, 1\n\t"
    "global_store_dword v38, v39, %[pg] sc1\n\t"
    "s_mov_b64 exec, -1\n\t"
    "s_mov_b32 %[p0], %[p1]\n\t"
    "s_xor_b32 %[sl], %[sl], 0x100\n\t"
    "s_cmp_ge_i32 %[lf], %[cp]\n\t"
    "s_cbranch_scc1 6b\n\t"
    "s_mov_b32 m0, %[sl]\n\t"
    "s_nop 0\n\t"
    "global_load_lds_dword %[dv], %[db] sc1\n\t"
    "s_branch 6b\n\t"
    "9:"
    : "=&{v[80:95]}"(pd), [ea] "+&v"(ea), [da] "+&v"(da), [b] "+&s"(b), [lf] "+&s"(lf), "+{s[36:37]}"(p1), "+{s[38:39]}"(p0), [sa] "+&v"(sa), [st] "=&s"(st), [sl] "+&s"(sl), [p0] "+&s"(pf)
    : [rw] "v"(rw), [ab] "s"(ab), [cp] "s"(cp), [mu] "s"(mu), [ad] "v"(ad), "{v[128:159]}"(W[0]), "{v[160:191]}"(W[1]), "{v[192:223]}"(W[2]), "{v[224:255]}"(W[3]), [co0] "v"(co0), [w0] "s"(w0), [dv] "v"(dv), [db] "s"(db), [pg] "s"(pg), [po] "s"(po - 1), [p1] "s"(b + po), [co1] "v"(co1)
    : "v38", "v39", "v40", "v41", "v42", "v43", "v44", "v45", "v46", "v47", "v48", "v49", "v50", "v51", "v52", "v53", "v54", "v55", "v56", "v57", "v58", "v59", "v60", "v61", "v62", "v63", "v64", "v65", "v66", "v67", "v68", "v69", "v70", "v71", "v72", "v73", "v74", "v75", "v76", "v77", "v78", "v79", "v96", "v97", "v98", "v99", "v100", "v101", "v102", "v103", "v104", "v105", "v106", "v107", "v108", "v109", "v110", "v111", "v112", "v113", "v114", "v115", "v116", "v117", "v118", "v119", "v120", "v121", "v122", "v123", "v124", "v125", "v126", "v127", "vcc", "scc", "m0", "memory");
}
__device__ __forceinline__ void fast_st1(const u32x32 (&W)[4], u32x16& pd, unsigned ea, unsigned da, int& b, int& lf, int cp, unsigned sa, unsigned mu, unsigned ad,
    unsigned rw, unsigned ab, const char* p1, const char* p0, unsigned co0, unsigned co1, int po, int pf, unsigned w0, unsigned sl, unsigned dv, const char* db, const char* pg) {
  unsigned st;
  asm volatile("s_barrier\n\t"
    "ds_read_b128 v[96:99], %[ea] offset:0\n\t"
    "ds_read_b128 v[100:103], %[ea] offset:2048\n\t"
    "ds_read_b128 v[104:107], %[da] offset:0\n\t"
    "ds_read_b128 v[108:111], %[da] offset:2048\n\t"
    "ds_read_b128 v[112:115], %[ea] offset:4096\n\t"
    "ds_read_b128 v[116:119], %[ea] offset:6144\n\t"
    "s_branch 2f\n\t"
    "1:\n\t"
    "s_barrier\n\t"
    "2:\n\t"
    "s_waitcnt lgkmcnt(4)\n\t"
    "v_mfma_f32_16x16x32_bf16 v[64:67], v[128:131], v[96:99], 0\n\t"
    "ds_read_b128 v[120:123], %[da] offset:4096\n\t"
    "v_mfma_f32_16x16x32_bf16 v[68:71], v[132:135], v[96:99], 0\n\t"
    "ds_read_b128 v[124:127], %[da] offset:6144\n\t"
    "v_mfma_f32_16x16x32_bf16 v[72:75], v[128:131], v[100:103], 0\n\t"
    "v_mfma_f32_16x16x32_bf16 v[76:79], v[132:135], v[100:103], 0\n\t"
    "s_waitcnt lgkmcnt(4)\n\t"
    "v_mfma_f32_16x16x32_bf16 v[64:67], v[136:139], v[104:107], v[64:67]\n\t"
    "ds_read_b128 v[96:99], %[ea] offset:8192\n\t"
    "v_mfma_f32_16x16x32_bf16 v[68:71], v[140:143], v[104:107], v[68:71]\n\t"
    "ds_read_b128 v[100:103], %[ea] offset:10240\n\t"
    "v_mfma_f32_16x16x32_bf16 v[72:75], v[136:139], v[108:111], v[72:75]\n\t"
    "v_mfma_f32_16x16x32_bf16 v[76:79], v[140:143], v[108:111], v[76:79]\n\t"
    "s_waitcnt lgkmcnt(4)\n\t"
    "v_mfma_f32_16x16x32_bf16 v[64:67], v[144:147], v[112:115], v[64:67]\n\t"
    "ds_read_b128 v[104:107], %[da] offset:8192\n\t"
    "v_mfma_f32_16x16x32_bf16 v[68:71], v[148:151], v[112:115], v[68:71]\n\t"
    "ds_read_b128 v[108:111], %[da] offset:10240\n\t"
    "v_mfma_f32_16x16x32_bf16 v[72:75], v[144:147], v[116:119], v[72:75]\n\t"
    "v_mfma_f32_16x16x32_bf16 v[76:79], v[148:151], v[116:119], v[76:79]\n\t"
    "s_waitcnt lgkmcnt(4)\n\t"
    "v_mfma_f32_16x16x32_bf16 v[64:67], v[152:155], v[120:123], v[64:67]\n\t"
    "ds_read_b128 v[112:115], %[ea] offset:12288\n\t"
    "v_mfma_f32_16x16x32_bf16 v[68:71], v[156:159], v[120:123], v[68:71]\n\t"
    "ds_read_b128 v[116:119], %[ea] offset:14336\n\t"
    "v_mfma_f32_16x16x32_bf16 v[72:75], v[152:155], v[124:127], v[72:75]\n\t"
    "v_mfma_f32_16x16x32_bf16 v[76:79], v[156:159], v[124:127], v[76:79]\n\t"
    "s_waitcnt lgkmcnt(4)\n\t"
    "v_mfma_f32_16x16x32_bf16 v[64:67], v[160:163], v[96:99], v[64:67]\n\t"
    "ds_read_b128 v[120:123], %[da] offset:12288\n\t"
    "v_mfma_f32_16x16x32_bf16 v[68:71], v[164:167], v[96:99], v[68:71]\n\t"
    "ds_read_b128 v[124:127], %[da] offset:14336\n\t"
    "v_mfma_f32_16x16x32_bf16 v[72:75], v[160:163], v[100:103], v[72:75]\n\t"
    "v_mfma_f32_16x16x32_bf16 v[76:79], v[164:167], v[100:103], v[76:79]\n\t"
    "s_waitcnt lgkmcnt(4)\n\t"
    "v_mfma_f32_16x16x32_bf16 v[64:67], v[168:171], v[104:107], v[64:67]\n\t"
    "ds_read_b128 v[96:99], %[ea] offset:16384\n\t"
    "v_mfma_f32_16x16x32_bf16 v[68:71], v[172:175], v[104:107], v[68:71]\n\t"
    "ds_read_b128 v[100:103], %[ea] offset:18432\n\t"
    "v_mfma_f32_16x16x32_bf16 v[72:75], v[168:171], v[108:111], v[72:75]\n\t"
    "v_mfma_f32_16x16x32_bf16 v[76:79], v[172:175], v[108:111], v[76:79]\n\t"
    "s_waitcnt lgkmcnt(4)\n\t"
    "v_mfma_f32_16x16x32_bf16 v[64:67], v[176:179], v[112:115], v[64:67]\n\t"
    "ds_read_b128 v[104:107], %[da] offset:16384\n\t"
    "v_mfma_f32_16x16x32_bf16 v[68:71], v[180:183], v[112:115], v[68:71]\n\t"
    "ds_read_b128 v[108:111], %[da] offset:18432\n\t"
    "v_mfma_f32_16x16x32_bf16 v[72:75], v[176:179], v[116:119], v[72:75]\n\t"
    "v_mfma_f32_16x16x32_bf16 v[76:79], v[180:183], v[116:119], v[76:79]\n\t"
    "s_waitcnt lgkmcnt(4)\n\t"
    "v_mfma_f32_16x16x32_bf16 v[64:67], v[184:187], v[120:123], v[64:67]\n\t"
    "ds_read_b128 v[112:115], %[ea] offset:20480\n\t"
    "v_mfma_f32_16x16x32_bf16 v[68:71], v[188:191], v[120:123], v[68:71]\n\t"
    "ds_read_b128 v[116:119], %[ea] offset:22528\n\t"
    "v_mfma_f32_16x16x32_bf16 v[72:75], v[184:187], v[124:127], v[72:75]\n\t"
    "v_mfma_f32_16x16x32_bf16 v[76:79], v[188:191], v[124:127], v[76:79]\n\t"
    "s_waitcnt vmcnt(0)\n\t"
    "s_barrier\n\t"
    "s_waitcnt lgkmcnt(4)\n\t"
    "v_mfma_f32_16x16x32_bf16 v[64:67], v[192:195], v[96:99], v[64:67]\n\t"
    "ds_read_b128 v[120:123], %[da] offset:20480\n\t"
    "v_mfma_f32_16x16x32_bf16 v[68:71], v[196:199], v[96:99], v[68:71]\n\t"
    "ds_read_b128 v[124:127], %[da] offset:22528\n\t"
    "v_mfma_f32_16x16x32_bf16 v[72:75], v[192:195], v[100:103], v[72:75]\n\t"
    "ds_read_b128 v[48:51], %[rw] offset:0\n\t"
    "v_mfma_f32_16x16x32_bf16 v[76:79], v[196:199], v[100:103], v[76:79]\n\t"
    "ds_read_b128 v[52:55], %[rw] offset:1024\n\t"
    "s_cmp_eq_u32 %[w0], 0\n\t"
    "s_cbranch_scc1 5f\n\t"
    "6:\n\t"
    "s_waitcnt lgkmcnt(6)\n\t"
    "v_mfma_f32_16x16x32_bf16 v[64:67], v[200:203], v[104:107], v[64:67]\n\t"
    "ds_read_b128 v[96:99], %[ea] offset:24576\n\t"
    "v_mfma_f32_16x16x32_bf16 v[68:71], v[204:207], v[104:107], v[68:71]\n\t"
    "ds_read_b128 v[100:103], %[ea] offset:26624\n\t"
    "v_mfma_f32_16x16x32_bf16 v[72:75], v[200:203], v[108:111], v[72:75]\n\t"
    "ds_read_b128 v[56:59], %[rw] offset:2048\n\t"
    "v_mfma_f32_16x16x32_bf16 v[76:79], v[204:207], v[108:111], v[76:79]\n\t"
    "ds_read_b128 v[60:63], %[rw] offset:3072\n\t"
    "s_waitcnt lgkmcnt(0)\n\t"
    "v_mfma_f32_16x16x32_bf16 v[64:67], v[208:211], v[112:115], v[64:67]\n\t"
    "ds_read_b128 v[104:107], %[da] offset:24576\n\t"
    "v_add_f32 v48, v48, v80\n\t"
    "v_add_f32 v49, v49, v81\n\t"
    "v_add_f32 v50, v50, v82\n\t"
    "v_mfma_f32_16x16x32_bf16 v[68:71], v[212:215], v[112:115], v[68:71]\n\t"
    "ds_read_b128 v[108:111], %[da] offset:26624\n\t"
    "v_add_f32 v51, v51, v83\n\t"
    "v_add_f32 v52, v52, v84\n\t"
    "v_add_f32 v53, v53, v85\n\t"
    "v_mfma_f32_16x16x32_bf16 v[72:75], v[208:211], v[116:119], v[72:75]\n\t"
    "ds_read_b32 v39, %[sa]\n\t"
    "v_add_f32 v54, v54, v86\n\t"
    "v_add_f32 v55, v55, v87\n\t"
    "v_add_f32 v56, v56, v88\n\t"
    "v_mfma_f32_16x16x32_bf16 v[76:79], v[212:215], v[116:119], v[76:79]\n\t"
    "v_xor_b32 %[sa], 0x100, %[sa]\n\t"
    "v_add_f32 v57, v57, v89\n\t"
    "v_add_f32 v58, v58, v90\n\t"
    "v_add_f32 v59, v59, v91\n\t"
    "v_mfma_f32_16x16x32_bf16 v[64:67], v[216:219], v[120:123], v[64:67]\n\t"
    "ds_read_b128 v[112:115], %[ea] offset:28672\n\t"
    "v_add_f32 v60, v60, v92\n\t"
    "v_add_f32 v61, v61, v93\n\t"
    "v_add_f32 v62, v62, v94\n\t"
    "v_mfma_f32_16x16x32_bf16 v[68:71], v[220:223], v[120:123], v[68:71]\n\t"
    "ds_read_b128 v[116:119], %[ea] offset:30720\n\t"
    "v_add_f32 v63, v63, v95\n\t"
    "v_mul_f32 v40, %[nl], v48\n\t"
    "v_mul_f32 v41, %[nl], v49\n\t"
    "v_mfma_f32_16x16x32_bf16 v[72:75], v[216:219], v[124:127], v[72:75]\n\t"
    "v_xor_b32 %[ea], 0x10000, %[ea]\n\t"
    "v_mul_f32 v42, %[nl], v50\n\t"
    "v_mul_f32 v43, %[nl], v51\n\t"
    "v_mul_f32 v44, %[nl], v56\n\t"
    "v_mfma_f32_16x16x32_bf16 v[76:79], v[220:223], v[124:127], v[76:79]\n\t"
    "v_mul_f32 v45, %[nl], v57\n\t"
    "v_mul_f32 v46, %[nl], v58\n\t"
    "v_mul_f32 v47, %[nl], v59\n\t"
    "v_mfma_f32_16x16x32_bf16 v[64:67], v[224:227], v[96:99], v[64:67]\n\t"
    "ds_read_b128 v[120:123], %[da] offset:28672\n\t"
    "v_exp_f32 v40, v40\n\t"
    "v_exp_f32 v41, v41\n\t"
    "v_exp_f32 v42, v42\n\t"
    "v_mfma_f32_16x16x32_bf16 v[68:71], v[228:231], v[96:99], v[68:71]\n\t"
    "ds_read_b128 v[124:127], %[da] offset:30720\n\t"
    "v_exp_f32 v43, v43\n\t"
    "v_exp_f32 v44, v44\n\t"
    "v_exp_f32 v45, v45\n\t"
    "v_mfma_f32_16x16x32_bf16 v[72:75], v[224:227], v[100:103], v[72:75]\n\t"
    "v_xor_b32 %[da], 0x10000, %[da]\n\t"
    "v_exp_f32 v46, v46\n\t"
    "v_exp_f32 v47, v47\n\t"
    "v_add_f32 v40, 1.0, v40\n\t"
    "v_mfma_f32_16x16x32_bf16 v[76:79], v[228:231], v[100:103], v[76:79]\n\t"
    "v_add_f32 v41, 1.0, v41\n\t"
    "v_add_f32 v42, 1.0, v42\n\t"
    "v_add_f32 v43, 1.0, v43\n\t"
    "s_waitcnt lgkmcnt(5)\n\t"
    "v_mfma_f32_16x16x32_bf16 v[64:67], v[232:235], v[104:107], v[64:67]\n\t"
    "ds_read_b128 v[96:99], %[ea] offset:0\n\t"
    "v_add_f32 v44, 1.0, v44\n\t"
    "v_add_f32 v45, 1.0, v45\n\t"
    "v_add_f32 v46, 1.0, v46\n\t"
    "v_mfma_f32_16x16x32_bf16 v[68:71], v[236:239], v[104:107], v[68:71]\n\t"
    "ds_read_b128 v[100:103], %[ea] offset:2048\n\t"
    "v_add_f32 v47, 1.0, v47\n\t"
    "v_rcp_f32 v40, v40\n\t"
    "v_rcp_f32 v41, v41\n\t"
    "v_mfma_f32_16x16x32_bf16 v[72:75], v[232:235], v[108:111], v[72:75]\n\t"
    "v_rcp_f32 v42, v42\n\t"
    "v_rcp_f32 v43, v43\n\t"
    "v_rcp_f32 v44, v44\n\t"
    "v_mfma_f32_16x16x32_bf16 v[76:79], v[236:239], v[108:111], v[76:79]\n\t"
    "v_rcp_f32 v45, v45\n\t"
    "v_rcp_f32 v46, v46\n\t"
    "v_rcp_f32 v47, v47\n\t"
    "s_waitcnt lgkmcnt(4)\n\t"
    "v_mfma_f32_16x16x32_bf16 v[64:67], v[240:243], v[112:115], v[64:67]\n\t"
    "ds_read_b128 v[104:107], %[da] offset:0\n\t"
    "v_mad_u32_u24 v39, v39, %[mu], %[ad]\n\t"
    "v_mul_f32 v40, v48, v40\n\t"
    "v_mul_f32 v41, v49, v41\n\t"
    "v_mul_f32 v42, v50, v42\n\t"
    "v_mfma_f32_16x16x32_bf16 v[68:71], v[244:247], v[112:115], v[68:71]\n\t"
    "ds_read_b128 v[108:111], %[da] offset:2048\n\t"
    "v_cmp_ge_i32 vcc, %[lf], v39\n\t"
    "s_cmp_eq_u64 vcc, 0\n\t"
    "s_addc_u32 %[lf], %[lf], 0\n\t"
    "v_mul_f32 v43, v51, v43\n\t"
    "v_mul_f32 v44, v56, v44\n\t"
    "v_mul_f32 v45, v57, v45\n\t"
    "v_mfma_f32_16x16x32_bf16 v[72:75], v[240:243], v[116:119], v[72:75]\n\t"
    "v_mul_f32 v46, v58, v46\n\t"
    "v_mul_f32 v47, v59, v47\n\t"
    "v_mul_f32 v40, v40, v52\n\t"
    "v_mfma_f32_16x16x32_bf16 v[76:79], v[244:247], v[116:119], v[76:79]\n\t"
    "v_cmp_ge_i32 vcc, %[lf], v39\n\t"
    "s_cmp_eq_u64 vcc, 0\n\t"
    "s_addc_u32 %[lf], %[lf], 0\n\t"
    "v_mul_f32 v41, v41, v53\n\t"
    "v_mul_f32 v42, v42, v54\n\t"
    "v_mul_f32 v43, v43, v55\n\t"
    "s_waitcnt lgkmcnt(4)\n\t"
    "v_mfma_f32_16x16x32_bf16 v[80:83], v[248:251], v[120:123], v[64:67]\n\t"
    "ds_read_b128 v[112:115], %[ea] offset:4096\n\t"
    "s_min_i32 %[lf], %[lf], %[cp]\n\t"
    "s_add_u32 %[b], %[b], 1\n\t"
    "v_mul_f32 v44, v44, v60\n\t"
    "v_mul_f32 v45, v45, v61\n\t"
    "v_mul_f32 v46, v46, v62\n\t"
    "v_mfma_f32_16x16x32_bf16 v[84:87], v[252:255], v[120:123], v[68:71]\n\t"
    "ds_read_b128 v[116:119], %[ea] offset:6144\n\t"
    "v_mul_f32 v47, v47, v63\n\t"
    "v_cvt_pk_bf16_f32 v48, v40, v41\n\t"
    "v_cvt_pk_bf16_f32 v49, v42, v43\n\t"
    "v_mfma_f32_16x16x32_bf16 v[88:91], v[248:251], v[124:127], v[72:75]\n\t"
    "v_cvt_pk_bf16_f32 v50, v44, v45\n\t"
    "v_cvt_pk_bf16_f32 v51, v46, v47\n\t"
    "s_nop 1\n\t"
    "v_mfma_f32_16x16x32_bf16 v[92:95], v[252:255], v[124:127], v[76:79]\n\t"
    "v_permlane16_swap_b32 v48, v50\n\t"
    "v_permlane16_swap_b32 v49, v51\n\t"
    "global_store_dwordx4 %[co0], v[48:51], s[36:37] sc1\n\t"
    "s_mov_b64 s[36:37], s[38:39]\n\t"
    "s_add_u32 s38, s38, %[ab]\n\t"
    "s_addc_u32 s39, s39, 0\n\t"
    "s_cmp_lt_i32 %[b], %[lf]\n\t"
    "s_cbranch_scc1 1b\n\t"
    "s_waitcnt lgkmcnt(0)\n\t"
    "s_nop 4\n\t"
    "s_branch 9f\n\t"
    "5:\n\t"
    "s_add_u32 %[st], %[b], %[po]\n\t"
    "s_max_i32 %[st], %[st], %[p0]\n\t"
    "v_mov_b32 v39, %[st]\n\t"
    "v_mov_b32 v38, 0\n\t"
    "s_mov_b64 exec, 1\n\t"
    "global_store_dword v38, v39, %[pg] sc1\n\t"
    "s_mov_b64 exec, -1\n\t"
    "s_mov_b32 %[p0], %[p1]\n\t"
    "s_xor_b32 %[sl], %[sl], 0x100\n\t"
    "s_cmp_ge_i32 %[lf], %[cp]\n\t"
    "s_cbranch_scc1 6b\n\t"
    "s_mov_b32 m0, %[sl]\n\t"
    "s_nop 0\n\t"
    "global_load_lds_dword %[dv], %[db] sc1\n\t"
    "s_branch 6b\n\t"
    "9:"
    : "=&{v[80:95]}"(pd), [ea] "+&v"(ea), [da] "+&v"(da), [b] "+&s"(b), [lf] "+&s"(lf), "+{s[36:37]}"(p1), "+{s[38:39]}"(p0), [sa] "+&v"(sa), [st] "=&s"(st), [sl] "+&s"(sl), [p0] "+&s"(pf)
    : [rw] "v"(rw), [ab] "s"(ab), [cp] "s"(cp), [mu] "s"(mu), [ad] "v"(ad), "{v[128:159]}"(W[0]), "{v[160:191]}"(W[1]), "{v[192:223]}"(W[2]), "{v[224:255]}"(W[3]), [co0] "v"(co0), [w0] "s"(w0), [dv] "v"(dv), [db] "s"(db), [pg] "s"(pg), [po] "s"(po - 1), [p1] "s"(b + po), [nl] "s"(-LOG2E)
    : "v38", "v39", "v40", "v41", "v42", "v43", "v44", "v45", "v46", "v47", "v48", "v49", "v50", "v51", "v52", "v53", "v54", "v55", "v56", "v57", "v58", "v59", "v60", "v61", "v62", "v63", "v64", "v65", "v66", "v67", "v68", "v69", "v70", "v71", "v72", "v73", "v74", "v75", "v76", "v77", "v78", "v79", "v96", "v97", "v98", "v99", "v100", "v101", "v102", "v103", "v104", "v105", "v106", "v107", "v108", "v109", "v110", "v111", "v112", "v113", "v114", "v115", "v116", "v117", "v118", "v119", "v120", "v121", "v122", "v123", "v124", "v125", "v126", "v127", "vcc", "scc", "m0", "memory");
}
__device__ __forceinline__ void fast_st2(const u32x32 (&W)[4], u32x16& pd, unsigned ea, unsigned da, int& b, int& lf, int cp, unsigned sa, unsigned mu, unsigned ad,
    unsigned rw, unsigned ab, const char* p1, const char* p0, unsigned co0, unsigned co1, int po, int pf, unsigned w0, unsigned sl, unsigned dv, const char* db, const char* pg) {
  unsigned st;
  asm volatile("s_barrier\n\t"
    "ds_read_b128 v[96:99], %[ea] offset:0\n\t"
    "ds_read_b128 v[100:103], %[ea] offset:2048\n\t"
    "ds_read_b128 v[104:107], %[da] offset:0\n\t"
    "ds_read_b128 v[108:111], %[da] offset:2048\n\t"
    "ds_read_b128 v[112:115], %[ea] offset:4096\n\t"
    "ds_read_b128 v[116:119], %[ea] offset:6144\n\t"
    "s_branch 2f\n\t"
    "1:\n\t"
    "s_barrier\n\t"
    "2:\n\t"
    "s_waitcnt lgkmcnt(4)\n\t"
    "v_mfma_f32_16x16x32_bf16 v[64:67], v[128:131], v[96:99], 0\n\t"
    "ds_read_b128 v[120:123], %[da] offset:4096\n\t"
    "v_mfma_f32_16x16x32_bf16 v[68:71], v[132:135], v[96:99], 0\n\t"
    "ds_read_b128 v[124:127], %[da] offset:6144\n\t"
    "v_mfma_f32_16x16x32_bf16 v[72:75], v[128:131], v[100:103], 0\n\t"
    "v_mfma_f32_16x16x32_bf16 v[76:79], v[132:135], v[100:103], 0\n\t"
    "s_waitcnt lgkmcnt(4)\n\t"
    "v_mfma_f32_16x16x32_bf16 v[64:67], v[136:139], v[104:107], v[64:67]\n\t"
    "ds_read_b128 v[96:99], %[ea] offset:8192\n\t"
    "v_mfma_f32_16x16x32_bf16 v[68:71], v[140:143], v[104:107], v[68:71]\n\t"
    "ds_read_b128 v[100:103], %[ea] offset:10240\n\t"
    "v_mfma_f32_16x16x32_bf16 v[72:75], v[136:139], v[108:111], v[72:75]\n\t"
    "v_mfma_f32_16x16x32_bf16 v[76:79], v[140:143], v[108:111], v[76:79]\n\t"
    "s_waitcnt lgkmcnt(4)\n\t"
    "v_mfma_f32_16x16x32_bf16 v[64:67], v[144:147], v[112:115], v[64:67]\n\t"
    "ds_read_b128 v[104:107], %[da] offset:8192\n\t"
    "v_mfma_f32_16x16x32_bf16 v[68:71], v[148:151], v[112:115], v[68:71]\n\t"
    "ds_read_b128 v[108:111], %[da] offset:10240\n\t"
    "v_mfma_f32_16x16x32_bf16 v[72:75], v[144:147], v[116:119], v[72:75]\n\t"
    "v_mfma_f32_16x16x32_bf16 v[76:79], v[148:151], v[116:119], v[76:79]\n\t"
    "s_waitcnt lgkmcnt(4)\n\t"
    "v_mfma_f32_16x16x32_bf16 v[64:67], v[152:155], v[120:123], v[64:67]\n\t"
    "ds_read_b128 v[112:115], %[ea] offset:12288\n\t"
    "v_mfma_f32_16x16x32_bf16 v[68:71], v[156:159], v[120:123], v[68:71]\n\t"
    "ds_read_b128 v[116:119], %[ea] offset:14336\n\t"
    "v_mfma_f32_16x16x32_bf16 v[72:75], v[152:155], v[124:127], v[72:75]\n\t"
    "v_mfma_f32_16x16x32_bf16 v[76:79], v[156:159], v[124:127], v[76:79]\n\t"
    "s_waitcnt lgkmcnt(4)\n\t"
    "v_mfma_f32_16x16x32_bf16 v[64:67], v[160:163], v[96:99], v[64:67]\n\t"
    "ds_read_b128 v[120:123], %[da] offset:12288\n\t"
    "v_mfma_f32_16x16x32_bf16 v[68:71], v[164:167], v[96:99], v[68:71]\n\t"
    "ds_read_b128 v[124:127], %[da] offset:14336\n\t"
    "v_mfma_f32_16x16x32_bf16 v[72:75], v[160:163], v[100:103], v[72:75]\n\t"
    "v_mfma_f32_16x16x32_bf16 v[76:79], v[164:167], v[100:103], v[76:79]\n\t"
    "s_waitcnt lgkmcnt(4)\n\t"
    "v_mfma_f32_16x16x32_bf16 v[64:67], v[168:171], v[104:107], v[64:67]\n\t"
    "ds_read_b128 v[96:99], %[ea] offset:16384\n\t"
    "v_mfma_f32_16x16x32_bf16 v[68:71], v[172:175], v[104:107], v[68:71]\n\t"
    "ds_read_b128 v[100:103], %[ea] offset:18432\n\t"
    "v_mfma_f32_16x16x32_bf16 v[72:75], v[168:171], v[108:111], v[72:75]\n\t"
    "v_mfma_f32_16x16x32_bf16 v[76:79], v[172:175], v[108:111], v[76:79]\n\t"
    "s_waitcnt lgkmcnt(4)\n\t"
    "v_mfma_f32_16x16x32_bf16 v[64:67], v[176:179], v[112:115], v[64:67]\n\t"
    "ds_read_b128 v[104:107], %[da] offset:16384\n\t"
    "v_mfma_f32_16x16x32_bf16 v[68:71], v[180:183], v[112:115], v[68:71]\n\t"
    "ds_read_b128 v[108:111], %[da] offset:18432\n\t"
    "v_mfma_f32_16x16x32_bf16 v[72:75], v[176:179], v[116:119], v[72:75]\n\t"
    "v_mfma_f32_16x16x32_bf16 v[76:79], v[180:183], v[116:119], v[76:79]\n\t"
    "s_waitcnt lgkmcnt(4)\n\t"
    "v_mfma_f32_16x16x32_bf16 v[64:67], v[184:187], v[120:123], v[64:67]\n\t"
    "ds_read_b128 v[112:115], %[ea] offset:20480\n\t"
    "v_mfma_f32_16x16x32_bf16 v[68:71], v[188:191], v[120:123], v[68:71]\n\t"
    "ds_read_b128 v[116:119], %[ea] offset:22528\n\t"
    "v_mfma_f32_16x16x32_bf16 v[72:75], v[184:187], v[124:127], v[72:75]\n\t"
    "v_mfma_f32_16x16x32_bf16 v[76:79], v[188:191], v[124:127], v[76:79]\n\t"
    "s_waitcnt vmcnt(0)\n\t"
    "s_barrier\n\t"
    "s_waitcnt lgkmcnt(4)\n\t"
    "v_mfma_f32_16x16x32_bf16 v[64:67], v[192:195], v[96:99], v[64:67]\n\t"
    "ds_read_b128 v[120:123], %[da] offset:20480\n\t"
    "v_mfma_f32_16x16x32_bf16 v[68:71], v[196:199], v[96:99], v[68:71]\n\t"
    "ds_read_b128 v[124:127], %[da] offset:22528\n\t"
    "v_mfma_f32_16x16x32_bf16 v[72:75], v[192:195], v[100:103], v[72:75]\n\t"
    "ds_read_b128 v[48:51], %[rw] offset:0\n\t"
    "v_mfma_f32_16x16x32_bf16 v[76:79], v[196:199], v[100:103], v[76:79]\n\t"
    "ds_read_b128 v[52:55], %[rw] offset:1024\n\t"
    "s_cmp_eq_u32 %[w0], 0\n\t"
    "s_cbranch_scc1 5f\n\t"
    "6:\n\t"
    "s_waitcnt lgkmcnt(6)\n\t"
    "v_mfma_f32_16x16x32_bf16 v[64:67], v[200:203], v[104:107], v[64:67]\n\t"
    "ds_read_b128 v[96:99], %[ea] offset:24576\n\t"
    "v_mfma_f32_16x16x32_bf16 v[68:71], v[204:207], v[104:107], v[68:71]\n\t"
    "ds_read_b128 v[100:103], %[ea] offset:26624\n\t"
    "v_mfma_f32_16x16x32_bf16 v[72:75], v[200:203], v[108:111], v[72:75]\n\t"
    "ds_read_b128 v[56:59], %[rw] offset:2048\n\t"
    "v_mfma_f32_16x16x32_bf16 v[76:79], v[204:207], v[108:111], v[76:79]\n\t"
    "ds_read_b128 v[60:63], %[rw] offset:3072\n\t"
    "s_waitcnt lgkmcnt(0)\n\t"
    "v_mfma_f32_16x16x32_bf16 v[64:67], v[208:211], v[112:115], v[64:67]\n\t"
    "ds_read_b128 v[104:107], %[da] offset:24576\n\t"
    "v_cvt_pk_bf16_f32 v40, v80, v81\n\t"
    "v_mfma_f32_16x16x32_bf16 v[68:71], v[212:215], v[112:115], v[68:71]\n\t"
    "ds_read_b128 v[108:111], %[da] offset:26624\n\t"
    "v_cvt_pk_bf16_f32 v41, v82, v83\n\t"
    "v_mfma_f32_16x16x32_bf16 v[72:75], v[208:211], v[116:119], v[72:75]\n\t"
    "ds_read_b32 v39, %[sa]\n\t"
    "v_cvt_pk_bf16_f32 v42, v84, v85\n\t"
    "v_mfma_f32_16x16x32_bf16 v[76:79], v[212:215], v[116:119], v[76:79]\n\t"
    "v_xor_b32 %[sa], 0x100, %[sa]\n\t"
    "v_cvt_pk_bf16_f32 v43, v86, v87\n\t"
    "v_mfma_f32_16x16x32_bf16 v[64:67], v[216:219], v[120:123], v[64:67]\n\t"
    "ds_read_b128 v[112:115], %[ea] offset:28672\n\t"
    "global_store_dwordx4 %[co0], v[40:43], s[36:37] sc1\n\t"
    "v_mfma_f32_16x16x32_bf16 v[68:71], v[220:223], v[120:123], v[68:71]\n\t"
    "ds_read_b128 v[116:119], %[ea] offset:30720\n\t"
    "v_cvt_pk_bf16_f32 v44, v88, v89\n\t"
    "v_mfma_f32_16x16x32_bf16 v[72:75], v[216:219], v[124:127], v[72:75]\n\t"
    "v_xor_b32 %[ea], 0x10000, %[ea]\n\t"
    "v_cvt_pk_bf16_f32 v45, v90, v91\n\t"
    "v_mfma_f32_16x16x32_bf16 v[76:79], v[220:223], v[124:127], v[76:79]\n\t"
    "v_cvt_pk_bf16_f32 v46, v92, v93\n\t"
    "v_mfma_f32_16x16x32_bf16 v[64:67], v[224:227], v[96:99], v[64:67]\n\t"
    "ds_read_b128 v[120:123], %[da] offset:28672\n\t"
    "v_cvt_pk_bf16_f32 v47, v94, v95\n\t"
    "v_mfma_f32_16x16x32_bf16 v[68:71], v[228:231], v[96:99], v[68:71]\n\t"
    "ds_read_b128 v[124:127], %[da] offset:30720\n\t"
    "global_store_dwordx4 %[co1], v[44:47], s[36:37] sc1\n\t"
    "v_mfma_f32_16x16x32_bf16 v[72:75], v[224:227], v[100:103], v[72:75]\n\t"
    "v_xor_b32 %[da], 0x10000, %[da]\n\t"
    "v_cvt_pk_bf16_f32 v48, v48, v49\n\t"
    "v_mfma_f32_16x16x32_bf16 v[76:79], v[228:231], v[100:103], v[76:79]\n\t"
    "v_cvt_pk_bf16_f32 v49, v50, v51\n\t"
    "s_waitcnt lgkmcnt(5)\n\t"
    "v_mfma_f32_16x16x32_bf16 v[64:67], v[232:235], v[104:107], v[64:67]\n\t"
    "ds_read_b128 v[96:99], %[ea] offset:0\n\t"
    "v_cvt_pk_bf16_f32 v50, v52, v53\n\t"
    "v_mfma_f32_16x16x32_bf16 v[68:71], v[236:239], v[104:107], v[68:71]\n\t"
    "ds_read_b128 v[100:103], %[ea] offset:2048\n\t"
    "v_cvt_pk_bf16_f32 v51, v54, v55\n\t"
    "v_mfma_f32_16x16x32_bf16 v[72:75], v[232:235], v[108:111], v[72:75]\n\t"
    "global_store_dwordx4 %[co0], v[48:51], s[36:37] offset:2048 sc1\n\t"
    "v_mfma_f32_16x16x32_bf16 v[76:79], v[236:239], v[108:111], v[76:79]\n\t"
    "v_cvt_pk_bf16_f32 v56, v56, v57\n\t"
    "s_waitcnt lgkmcnt(4)\n\t"
    "v_mfma_f32_16x16x32_bf16 v[64:67], v[240:243], v[112:115], v[64:67]\n\t"
    "ds_read_b128 v[104:107], %[da] offset:0\n\t"
    "v_mad_u32_u24 v39, v39, %[mu], %[ad]\n\t"
    "v_cvt_pk_bf16_f32 v57, v58, v59\n\t"
    "v_mfma_f32_16x16x32_bf16 v[68:71], v[244:247], v[112:115], v[68:71]\n\t"
    "ds_read_b128 v[108:111], %[da] offset:2048\n\t"
    "v_cmp_ge_i32 vcc, %[lf], v39\n\t"
    "s_cmp_eq_u64 vcc, 0\n\t"
    "s_addc_u32 %[lf], %[lf], 0\n\t"
    "v_cvt_pk_bf16_f32 v58, v60, v61\n\t"
    "v_mfma_f32_16x16x32_bf16 v[72:75], v[240:243], v[116:119], v[72:75]\n\t"
    "v_cvt_pk_bf16_f32 v59, v62, v63\n\t"
    "v_mfma_f32_16x16x32_bf16 v[76:79], v[244:247], v[116:119], v[76:79]\n\t"
    "v_cmp_ge_i32 vcc, %[lf], v39\n\t"
    "s_cmp_eq_u64 vcc, 0\n\t"
    "s_addc_u32 %[lf], %[lf], 0\n\t"
    "global_store_dwordx4 %[co1], v[56:59], s[36:37] offset:2048 sc1\n\t"
    "s_waitcnt lgkmcnt(4)\n\t"
    "v_mfma_f32_16x16x32_bf16 v[80:83], v[248:251], v[120:123], v[64:67]\n\t"
    "ds_read_b128 v[112:115], %[ea] offset:4096\n\t"
    "s_min_i32 %[lf], %[lf], %[cp]\n\t"
    "s_add_u32 %[b], %[b], 1\n\t"
    "v_mfma_f32_16x16x32_bf16 v[84:87], v[252:255], v[120:123], v[68:71]\n\t"
    "ds_read_b128 v[116:119], %[ea] offset:6144\n\t"
    "v_mfma_f32_16x16x32_bf16 v[88:91], v[248:251], v[124:127], v[72:75]\n\t"
    "v_mfma_f32_16x16x32_bf16 v[92:95], v[252:255], v[124:127], v[76:79]\n\t"
    "s_mov_b64 s[36:37], s[38:39]\n\t"
    "s_add_u32 s38, s38, %[ab]\n\t"
    "s_addc_u32 s39, s39, 0\n\t"
    "s_cmp_lt_i32 %[b], %[lf]\n\t"
    "s_cbranch_scc1 1b\n\t"
    "s_waitcnt lgkmcnt(0)\n\t"
    "s_nop 4\n\t"
    "s_branch 9f\n\t"
    "5:\n\t"
    "s_add_u32 %[st], %[b], %[po]\n\t"
    "s_max_i32 %[st], %[st], %[p0]\n\t"
    "v_mov_b32 v39, %[st]\n\t"
    "v_mov_b32 v38, 0\n\t"
    "s_mov_b64 exec, 1\n\t"
    "global_store_dword v38, v39, %[pg] sc1\n\t"
    "s_mov_b64 exec, -1\n\t"
    "s_mov_b32 %[p0], %[p1]\n\t"
    "s_xor_b32 %[sl], %[sl], 0x100\n\t"
    "s_cmp_ge_i32 %[lf], %[cp]\n\t"
    "s_cbranch_scc1 6b\n\t"
    "s_mov_b32 m0, %[sl]\n\t"
    "s_nop 0\n\t"
    "global_load_lds_dword %[dv], %[db] sc1\n\t"
    "s_branch 6b\n\t"
    "9:"
    : "=&{v[80:95]}"(pd), [ea] "+&v"(ea), [da] "+&v"(da), [b] "+&s"(b), [lf] "+&s"(lf), "+{s[36:37]}"(p1), "+{s[38:39]}"(p0), [sa] "+&v"(sa), [st] "=&s"(st), [sl] "+&s"(sl), [p0] "+&s"(pf)
    : [rw] "v"(rw), [ab] "s"(ab), [cp] "s"(cp), [mu] "s"(mu), [ad] "v"(ad), "{v[128:159]}"(W[0]), "{v[160:191]}"(W[1]), "{v[192:223]}"(W[2]), "{v[224:255]}"(W[3]), [co0] "v"(co0), [w0] "s"(w0), [dv] "v"(dv), [db] "s"(db), [pg] "s"(pg), [po] "s"(po - 1), [p1] "s"(b + po), [co1] "v"(co1)
    : "v38", "v39", "v40", "v41", "v42", "v43", "v44", "v45", "v46", "v47", "v48", "v49", "v50", "v51", "v52", "v53", "v54", "v55", "v56", "v57", "v58", "v59", "v60", "v61", "v62", "v63", "v64", "v65", "v66", "v67", "v68", "v69", "v70", "v71", "v72", "v73", "v74", "v75", "v76", "v77", "v78", "v79", "v96", "v97", "v98", "v99", "v100", "v101", "v102", "v103", "v104", "v105", "v106", "v107", "v108", "v109", "v110", "v111", "v112", "v113", "v114", "v115", "v116", "v117", "v118", "v119", "v120", "v121", "v122", "v123", "v124", "v125", "v126", "v127", "vcc", "scc", "m0", "memory");
}
__device__ __forceinline__ void near_ld0(const u32x32 (&W)[4], u32x16& pd, unsigned ea, unsigned da, int& b, int& lf, int cp, unsigned sa, unsigned mu, unsigned ad,
    unsigned rw, unsigned ab, const char* p1, const char* p0, unsigned l1, unsigned l0, const unsigned (&av)[8]) {
  asm volatile("s_setprio 0\n\t"
    "s_waitcnt vmcnt(4)\n\t"
    "s_barrier\n\t"
    "ds_read_b128 v[96:99], %[ea] offset:0\n\t"
    "ds_read_b128 v[100:103], %[ea] offset:2048\n\t"
    "ds_read_b128 v[104:107], %[da] offset:0\n\t"
    "ds_read_b128 v[108:111], %[da] offset:2048\n\t"
    "ds_read_b128 v[112:115], %[ea] offset:4096\n\t"
    "ds_read_b128 v[116:119], %[ea] offset:6144\n\t"
    "s_branch 2f\n\t"
    "1:\n\t"
    "s_waitcnt vmcnt(4)\n\t"
    "s_nop 1\n\t"
    "s_barrier\n\t"
    "2:\n\t"
    "s_waitcnt lgkmcnt(4)\n\t"
    "v_mfma_f32_16x16x32_bf16 v[64:67], v[128:131], v[96:99], 0\n\t"
    "ds_read_b128 v[120:123], %[da] offset:4096\n\t"
    "s_add_u32 m0, %[l1], 0\n\t"
    "ds_write_b128 %[rw], v[80:83] offset:0\n\t"
    "v_mfma_f32_16x16x32_bf16 v[68:71], v[132:135], v[96:99], 0\n\t"
    "ds_read_b128 v[124:127], %[da] offset:6144\n\t"
    "global_load_lds_dwordx4 %[av0], s[36:37] sc1\n\t"
    "ds_write_b128 %[rw], v[84:87] offset:1024\n\t"
    "v_mfma_f32_16x16x32_bf16 v[72:75], v[128:131], v[100:103], 0\n\t"
    "s_add_u32 m0, %[l1], 32768\n\t"
    "ds_write_b128 %[rw], v[88:91] offset:2048\n\t"
    "v_mfma_f32_16x16x32_bf16 v[76:79], v[132:135], v[100:103], 0\n\t"
    "global_load_lds_dwordx4 %[av1], s[36:37] sc1\n\t"
    "ds_write_b128 %[rw], v[92:95] offset:3072\n\t"
    "s_waitcnt lgkmcnt(8)\n\t"
    "v_mfma_f32_16x16x32_bf16 v[64:67], v[136:139], v[104:107], v[64:67]\n\t"
    "ds_read_b128 v[96:99], %[ea] offset:8192\n\t"
    "s_add_u32 m0, %[l1], 4096\n\t"
    "v_mfma_f32_16x16x32_bf16 v[68:71], v[140:143], v[104:107], v[68:71]\n\t"
    "ds_read_b128 v[100:103], %[ea] offset:10240\n\t"
    "global_load_lds_dwordx4 %[av2], s[36:37] sc1\n\t"
    "v_mfma_f32_16x16x32_bf16 v[72:75], v[136:139], v[108:111], v[72:75]\n\t"
    "s_add_u32 m0, %[l1], 36864\n\t"
    "v_mfma_f32_16x16x32_bf16 v[76:79], v[140:143], v[108:111], v[76:79]\n\t"
    "global_load_lds_dwordx4 %[av3], s[36:37] sc1\n\t"
    "s_waitcnt lgkmcnt(2)\n\t"
    "v_mfma_f32_16x16x32_bf16 v[64:67], v[144:147], v[112:115], v[64:67]\n\t"
    "ds_read_b128 v[104:107], %[da] offset:8192\n\t"
    "s_add_u32 m0, %[l1], 8192\n\t"
    "v_mfma_f32_16x16x32_bf16 v[68:71], v[148:151], v[112:115], v[68:71]\n\t"
    "ds_read_b128 v[108:111], %[da] offset:10240\n\t"
    "global_load_lds_dwordx4 %[av4], s[36:37] sc1\n\t"
    "v_mfma_f32_16x16x32_bf16 v[72:75], v[144:147], v[116:119], v[72:75]\n\t"
    "s_add_u32 m0, %[l1], 40960\n\t"
    "v_mfma_f32_16x16x32_bf16 v[76:79], v[148:151], v[116:119], v[76:79]\n\t"
    "global_load_lds_dwordx4 %[av5], s[36:37] sc1\n\t"
    "s_waitcnt vmcnt(6)\n\t"
    "s_barrier\n\t"
    "v_mfma_f32_16x16x32_bf16 v[64:67], v[152:155], v[120:123], v[64:67]\n\t"
    "ds_read_b128 v[112:115], %[ea] offset:12288\n\t"
    "s_add_u32 m0, %[l1], 12288\n\t"
    "v_mfma_f32_16x16x32_bf16 v[68:71], v[156:159], v[120:123], v[68:71]\n\t"
    "ds_read_b128 v[116:119], %[ea] offset:14336\n\t"
    "global_load_lds_dwordx4 %[av6], s[36:37] sc1\n\t"
    "v_mfma_f32_16x16x32_bf16 v[72:75], v[152:155], v[124:127], v[72:75]\n\t"
    "s_add_u32 m0, %[l1], 45056\n\t"
    "v_mfma_f32_16x16x32_bf16 v[76:79], v[156:159], v[124:127], v[76:79]\n\t"
    "global_load_lds_dwordx4 %[av7], s[36:37] sc1\n\t"
    "s_waitcnt lgkmcnt(4)\n\t"
    "v_mfma_f32_16x16x32_bf16 v[64:67], v[160:163], v[96:99], v[64:67]\n\t"
    "ds_read_b128 v[120:123], %[da] offset:12288\n\t"
    "s_add_u32 m0, %[l0], 0\n\t"
    "v_mfma_f32_16x16x32_bf16 v[68:71], v[164:167], v[96:99], v[68:71]\n\t"
    "ds_read_b128 v[124:127], %[da] offset:14336\n\t"
    "global_load_lds_dwordx4 %[av0], s[38:39] sc1\n\t"
    "v_mfma_f32_16x16x32_bf16 v[72:75], v[160:163], v[100:103], v[72:75]\n\t"
    "s_add_u32 m0, %[l0], 32768\n\t"
    "v_mfma_f32_16x16x32_bf16 v[76:79], v[164:167], v[100:103], v[76:79]\n\t"
    "global_load_lds_dwordx4 %[av1], s[38:39] sc1\n\t"
    "s_waitcnt lgkmcnt(4)\n\t"
    "v_mfma_f32_16x16x32_bf16 v[64:67], v[168:171], v[104:107], v[64:67]\n\t"
    "ds_read_b128 v[96:99], %[ea] offset:16384\n\t"
    "s_add_u32 m0, %[l0], 4096\n\t"
    "v_mfma_f32_16x16x32_bf16 v[68:71], v[172:175], v[104:107], v[68:71]\n\t"
    "ds_read_b128 v[100:103], %[ea] offset:18432\n\t"
    "global_load_lds_dwordx4 %[av2], s[38:39] sc1\n\t"
    "v_mfma_f32_16x16x32_bf16 v[72:75], v[168:171], v[108:111], v[72:75]\n\t"
    "s_add_u32 m0, %[l0], 36864\n\t"
    "v_mfma_f32_16x16x32_bf16 v[76:79], v[172:175], v[108:111], v[76:79]\n\t"
    "global_load_lds_dwordx4 %[av3], s[38:39] sc1\n\t"
    "s_waitcnt lgkmcnt(4)\n\t"
    "v_mfma_f32_16x16x32_bf16 v[64:67], v[176:179], v[112:115], v[64:67]\n\t"
    "ds_read_b128 v[104:107], %[da] offset:16384\n\t"
    "s_add_u32 m0, %[l0], 8192\n\t"
    "v_mfma_f32_16x16x32_bf16 v[68:71], v[180:183], v[112:115], v[68:71]\n\t"
    "ds_read_b128 v[108:111], %[da] offset:18432\n\t"
    "global_load_lds_dwordx4 %[av4], s[38:39] sc1\n\t"
    "v_mfma_f32_16x16x32_bf16 v[72:75], v[176:179], v[116:119], v[72:75]\n\t"
    "s_add_u32 m0, %[l0], 40960\n\t"
    "v_mfma_f32_16x16x32_bf16 v[76:79], v[180:183], v[116:119], v[76:79]\n\t"
    "global_load_lds_dwordx4 %[av5], s[38:39] sc1\n\t"
    "s_waitcnt lgkmcnt(4)\n\t"
    "v_mfma_f32_16x16x32_bf16 v[64:67], v[184:187], v[120:123], v[64:67]\n\t"
    "ds_read_b128 v[112:115], %[ea] offset:20480\n\t"
    "s_add_u32 m0, %[l0], 12288\n\t"
    "v_mfma_f32_16x16x32_bf16 v[68:71], v[188:191], v[120:123], v[68:71]\n\t"
    "ds_read_b128 v[116:119], %[ea] offset:22528\n\t"
    "global_load_lds_dwordx4 %[av6], s[38:39] sc1\n\t"
    "v_mfma_f32_16x16x32_bf16 v[72:75], v[184:187], v[124:127], v[72:75]\n\t"
    "s_add_u32 m0, %[l0], 45056\n\t"
    "v_mfma_f32_16x16x32_bf16 v[76:79], v[188:191], v[124:127], v[76:79]\n\t"
    "global_load_lds_dwordx4 %[av7], s[38:39] sc1\n\t"
    "s_waitcnt lgkmcnt(4)\n\t"
    "v_mfma_f32_16x16x32_bf16 v[64:67], v[192:195], v[96:99], v[64:67]\n\t"
    "ds_read_b128 v[120:123], %[da] offset:20480\n\t"
    "v_mfma_f32_16x16x32_bf16 v[68:71], v[196:199], v[96:99], v[68:71]\n\t"
    "ds_read_b128 v[124:127], %[da] offset:22528\n\t"
    "v_mfma_f32_16x16x32_bf16 v[72:75], v[192:195], v[100:103], v[72:75]\n\t"
    "v_mfma_f32_16x16x32_bf16 v[76:79], v[196:199], v[100:103], v[76:79]\n\t"
    "s_waitcnt lgkmcnt(4)\n\t"
    "v_mfma_f32_16x16x32_bf16 v[64:67], v[200:203], v[104:107], v[64:67]\n\t"
    "ds_read_b128 v[96:99], %[ea] offset:24576\n\t"
    "v_mfma_f32_16x16x32_bf16 v[68:71], v[204:207], v[104:107], v[68:71]\n\t"
    "ds_read_b128 v[100:103], %[ea] offset:26624\n\t"
    "v_mfma_f32_16x16x32_bf16 v[72:75], v[200:203], v[108:111], v[72:75]\n\t"
    "v_mfma_f32_16x16x32_bf16 v[76:79], v[204:207], v[108:111], v[76:79]\n\t"
    "s_add_u32 s36, s36, %[ab]\n\t"
    "s_addc_u32 s37, s37, 0\n\t"
    "s_waitcnt lgkmcnt(4)\n\t"
    "v_mfma_f32_16x16x32_bf16 v[64:67], v[208:211], v[112:115], v[64:67]\n\t"
    "ds_read_b128 v[104:107], %[da] offset:24576\n\t"
    "v_mfma_f32_16x16x32_bf16 v[68:71], v[212:215], v[112:115], v[68:71]\n\t"
    "ds_read_b128 v[108:111], %[da] offset:26624\n\t"
    "v_mfma_f32_16x16x32_bf16 v[72:75], v[208:211], v[116:119], v[72:75]\n\t"
    "v_mfma_f32_16x16x32_bf16 v[76:79], v[212:215], v[116:119], v[76:79]\n\t"
    "s_xor_b32 %[l1], %[l1], 0x10000\n\t"
    "s_waitcnt lgkmcnt(4)\n\t"
    "v_mfma_f32_16x16x32_bf16 v[64:67], v[216:219], v[120:123], v[64:67]\n\t"
    "ds_read_b128 v[112:115], %[ea] offset:28672\n\t"
    "v_mfma_f32_16x16x32_bf16 v[68:71], v[220:223], v[120:123], v[68:71]\n\t"
    "ds_read_b128 v[116:119], %[ea] offset:30720\n\t"
    "v_mfma_f32_16x16x32_bf16 v[72:75], v[216:219], v[124:127], v[72:75]\n\t"
    "v_xor_b32 %[ea], 0x10000, %[ea]\n\t"
    "v_mfma_f32_16x16x32_bf16 v[76:79], v[220:223], v[124:127], v[76:79]\n\t"
    "s_waitcnt vmcnt(12)\n\t"
    "s_barrier\n\t"
    "s_waitcnt lgkmcnt(4)\n\t"
    "v_mfma_f32_16x16x32_bf16 v[64:67], v[224:227], v[96:99], v[64:67]\n\t"
    "ds_read_b32 v39, %[sa]\n\t"
    "ds_read_b128 v[120:123], %[da] offset:28672\n\t"
    "v_mfma_f32_16x16x32_bf16 v[68:71], v[228:231], v[96:99], v[68:71]\n\t"
    "ds_read_b128 v[124:127], %[da] offset:30720\n\t"
    "v_mfma_f32_16x16x32_bf16 v[72:75], v[224:227], v[100:103], v[72:75]\n\t"
    "v_xor_b32 %[da], 0x10000, %[da]\n\t"
    "v_mfma_f32_16x16x32_bf16 v[76:79], v[228:231], v[100:103], v[76:79]\n\t"
    "s_waitcnt lgkmcnt(5)\n\t"
    "v_mfma_f32_16x16x32_bf16 v[64:67], v[232:235], v[104:107], v[64:67]\n\t"
    "ds_read_b128 v[96:99], %[ea] offset:0\n\t"
    "v_mfma_f32_16x16x32_bf16 v[68:71], v[236:239], v[104:107], v[68:71]\n\t"
    "ds_read_b128 v[100:103], %[ea] offset:2048\n\t"
    "v_mfma_f32_16x16x32_bf16 v[72:75], v[232:235], v[108:111], v[72:75]\n\t"
    "v_mfma_f32_16x16x32_bf16 v[76:79], v[236:239], v[108:111], v[76:79]\n\t"
    "s_waitcnt lgkmcnt(4)\n\t"
    "v_mfma_f32_16x16x32_bf16 v[64:67], v[240:243], v[112:115], v[64:67]\n\t"
    "ds_read_b128 v[104:107], %[da] offset:0\n\t"
    "v_mad_u32_u24 v39, v39, %[mu], %[ad]\n\t"
    "v_mfma_f32_16x16x32_bf16 v[68:71], v[244:247], v[112:115], v[68:71]\n\t"
    "ds_read_b128 v[108:111], %[da] offset:2048\n\t"
    "v_cmp_ge_i32 vcc, %[lf], v39\n\t"
    "s_cmp_eq_u64 vcc, 0\n\t"
    "s_addc_u32 %[lf], %[lf], 0\n\t"
    "v_mfma_f32_16x16x32_bf16 v[72:75], v[240:243], v[116:119], v[72:75]\n\t"
    "v_mfma_f32_16x16x32_bf16 v[76:79], v[244:247], v[116:119], v[76:79]\n\t"
    "v_cmp_ge_i32 vcc, %[lf], v39\n\t"
    "s_cmp_eq_u64 vcc, 0\n\t"
    "s_addc_u32 %[lf], %[lf], 0\n\t"
    "s_waitcnt lgkmcnt(4)\n\t"
    "v_mfma_f32_16x16x32_bf16 v[80:83], v[248:251], v[120:123], v[64:67]\n\t"
    "ds_read_b128 v[112:115], %[ea] offset:4096\n\t"
    "s_min_i32 %[lf], %[lf], %[cp]\n\t"
    "s_add_u32 %[b], %[b], 1\n\t"
    "v_mfma_f32_16x16x32_bf16 v[84:87], v[252:255], v[120:123], v[68:71]\n\t"
    "ds_read_b128 v[116:119], %[ea] offset:6144\n\t"
    "v_mfma_f32_16x16x32_bf16 v[88:91], v[248:251], v[124:127], v[72:75]\n\t"
    "v_mfma_f32_16x16x32_bf16 v[92:95], v[252:255], v[124:127], v[76:79]\n\t"
    "s_add_u32 s38, s38, %[ab]\n\t"
    "s_addc_u32 s39, s39, 0\n\t"
    "s_xor_b32 %[l0], %[l0], 0x10000\n\t"
    "s_cmp_lt_i32 %[b], %[lf]\n\t"
    "s_cbranch_scc1 1b\n\t"
    "s_waitcnt lgkmcnt(0)\n\t"
    "s_nop 4"
    : "=&{v[80:95]}"(pd), [ea] "+&v"(ea), [da] "+&v"(da), [b] "+&s"(b), [lf] "+&s"(lf), "+{s[36:37]}"(p1), "+{s[38:39]}"(p0), [sa] "+&v"(sa), [l1] "+&s"(l1), [l0] "+&s"(l0)
    : [rw] "v"(rw), [ab] "s"(ab), [cp] "s"(cp), [mu] "s"(mu), [ad] "v"(ad), "{v[128:159]}"(W[0]), "{v[160:191]}"(W[1]), "{v[192:223]}"(W[2]), "{v[224:255]}"(W[3]), [av0] "v"(av[0]), [av1] "v"(av[1]), [av2] "v"(av[2]), [av3] "v"(av[3]), [av4] "v"(av[4]), [av5] "v"(av[5]), [av6] "v"(av[6]), [av7] "v"(av[7])
    : "v39", "v64", "v65", "v66", "v67", "v68", "v69", "v70", "v71", "v72", "v73", "v74", "v75", "v76", "v77", "v78", "v79", "v96", "v97", "v98", "v99", "v100", "v101", "v102", "v103", "v104", "v105", "v106", "v107", "v108", "v109", "v110", "v111", "v112", "v113", "v114", "v115", "v116", "v117", "v118", "v119", "v120", "v121", "v122", "v123", "v124", "v125", "v126", "v127", "vcc", "scc", "m0", "memory");
}
__device__ __forceinline__ void near_ld2(const u32x32 (&W)[4], u32x16& pd, unsigned ea, unsigned da, int& b, int& lf, int cp, unsigned sa, unsigned mu, unsigned ad,
    unsigned rw, unsigned ab, const char* p1, const char* p0, unsigned l1, unsigned l0, const unsigned (&av)[8]) {
  asm volatile("s_setprio 0\n\t"
    "s_waitcnt vmcnt(2)\n\t"
    "s_barrier\n\t"
    "ds_read_b128 v[96:99], %[ea] offset:0\n\t"
    "ds_read_b128 v[100:103], %[ea] offset:2048\n\t"
    "ds_read_b128 v[104:107], %[da] offset:0\n\t"
    "ds_read_b128 v[108:111], %[da] offset:2048\n\t"
    "ds_read_b128 v[112:115], %[ea] offset:4096\n\t"
    "ds_read_b128 v[116:119], %[ea] offset:6144\n\t"
    "s_branch 2f\n\t"
    "1:\n\t"
    "s_waitcnt vmcnt(2)\n\t"
    "s_nop 1\n\t"
    "s_barrier\n\t"
    "2:\n\t"
    "s_waitcnt lgkmcnt(4)\n\t"
    "v_mfma_f32_16x16x32_bf16 v[64:67], v[128:131], v[96:99], 0\n\t"
    "ds_read_b128 v[120:123], %[da] offset:4096\n\t"
    "s_add_u32 m0, %[l1], 0\n\t"
    "ds_write_b128 %[rw], v[80:83] offset:0\n\t"
    "v_mfma_f32_16x16x32_bf16 v[68:71], v[132:135], v[96:99], 0\n\t"
    "ds_read_b128 v[124:127], %[da] offset:6144\n\t"
    "global_load_lds_dwordx4 %[av0], s[36:37] sc1\n\t"
    "ds_write_b128 %[rw], v[84:87] offset:1024\n\t"
    "v_mfma_f32_16x16x32_bf16 v[72:75], v[128:131], v[100:103], 0\n\t"
    "ds_write_b128 %[rw], v[88:91] offset:2048\n\t"
    "v_mfma_f32_16x16x32_bf16 v[76:79], v[132:135], v[100:103], 0\n\t"
    "ds_write_b128 %[rw], v[92:95] offset:3072\n\t"
    "s_waitcnt lgkmcnt(8)\n\t"
    "v_mfma_f32_16x16x32_bf16 v[64:67], v[136:139], v[104:107], v[64:67]\n\t"
    "ds_read_b128 v[96:99], %[ea] offset:8192\n\t"
    "s_add_u32 m0, %[l1], 4096\n\t"
    "v_mfma_f32_16x16x32_bf16 v[68:71], v[140:143], v[104:107], v[68:71]\n\t"
    "ds_read_b128 v[100:103], %[ea] offset:10240\n\t"
    "global_load_lds_dwordx4 %[av2], s[36:37] sc1\n\t"
    "v_mfma_f32_16x16x32_bf16 v[72:75], v[136:139], v[108:111], v[72:75]\n\t"
    "v_mfma_f32_16x16x32_bf16 v[76:79], v[140:143], v[108:111], v[76:79]\n\t"
    "s_waitcnt lgkmcnt(2)\n\t"
    "v_mfma_f32_16x16x32_bf16 v[64:67], v[144:147], v[112:115], v[64:67]\n\t"
    "ds_read_b128 v[104:107], %[da] offset:8192\n\t"
    "s_add_u32 m0, %[l1], 8192\n\t"
    "v_mfma_f32_16x16x32_bf16 v[68:71], v[148:151], v[112:115], v[68:71]\n\t"
    "ds_read_b128 v[108:111], %[da] offset:10240\n\t"
    "global_load_lds_dwordx4 %[av4], s[36:37] sc1\n\t"
    "v_mfma_f32_16x16x32_bf16 v[72:75], v[144:147], v[116:119], v[72:75]\n\t"
    "v_mfma_f32_16x16x32_bf16 v[76:79], v[148:151], v[116:119], v[76:79]\n\t"
    "s_waitcnt vmcnt(3)\n\t"
    "s_barrier\n\t"
    "v_mfma_f32_16x16x32_bf16 v[64:67], v[152:155], v[120:123], v[64:67]\n\t"
    "ds_read_b128 v[112:115], %[ea] offset:12288\n\t"
    "s_add_u32 m0, %[l1], 12288\n\t"
    "v_mfma_f32_16x16x32_bf16 v[68:71], v[156:159], v[120:123], v[68:71]\n\t"
    "ds_read_b128 v[116:119], %[ea] offset:14336\n\t"
    "global_load_lds_dwordx4 %[av6], s[36:37] sc1\n\t"
    "v_mfma_f32_16x16x32_bf16 v[72:75], v[152:155], v[124:127], v[72:75]\n\t"
    "v_mfma_f32_16x16x32_bf16 v[76:79], v[156:159], v[124:127], v[76:79]\n\t"
    "s_waitcnt lgkmcnt(4)\n\t"
    "v_mfma_f32_16x16x32_bf16 v[64:67], v[160:163], v[96:99], v[64:67]\n\t"
    "ds_read_b128 v[120:123], %[da] offset:12288\n\t"
    "s_add_u32 m0, %[l0], 0\n\t"
    "v_mfma_f32_16x16x32_bf16 v[68:71], v[164:167], v[96:99], v[68:71]\n\t"
    "ds_read_b128 v[124:127], %[da] offset:14336\n\t"
    "global_load_lds_dwordx4 %[av0], s[38:39] sc1\n\t"
    "v_mfma_f32_16x16x32_bf16 v[72:75], v[160:163], v[100:103], v[72:75]\n\t"
    "v_mfma_f32_16x16x32_bf16 v[76:79], v[164:167], v[100:103], v[76:79]\n\t"
    "s_waitcnt lgkmcnt(4)\n\t"
    "v_mfma_f32_16x16x32_bf16 v[64:67], v[168:171], v[104:107], v[64:67]\n\t"
    "ds_read_b128 v[96:99], %[ea] offset:16384\n\t"
    "s_add_u32 m0, %[l0], 4096\n\t"
    "v_mfma_f32_16x16x32_bf16 v[68:71], v[172:175], v[104:107], v[68:71]\n\t"
    "ds_read_b128 v[100:103], %[ea] offset:18432\n\t"
    "global_load_lds_dwordx4 %[av2], s[38:39] sc1\n\t"
    "v_mfma_f32_16x16x32_bf16 v[72:75], v[168:171], v[108:111], v[72:75]\n\t"
    "v_mfma_f32_16x16x32_bf16 v[76:79], v[172:175], v[108:111], v[76:79]\n\t"
    "s_waitcnt lgkmcnt(4)\n\t"
    "v_mfma_f32_16x16x32_bf16 v[64:67], v[176:179], v[112:115], v[64:67]\n\t"
    "ds_read_b128 v[104:107], %[da] offset:16384\n\t"
    "s_add_u32 m0, %[l0], 8192\n\t"
    "v_mfma_f32_16x16x32_bf16 v[68:71], v[180:183], v[112:115], v[68:71]\n\t"
    "ds_read_b128 v[108:111], %[da] offset:18432\n\t"
    "global_load_lds_dwordx4 %[av4], s[38:39] sc1\n\t"
    "v_mfma_f32_16x16x32_bf16 v[72:75], v[176:179], v[116:119], v[72:75]\n\t"
    "v_mfma_f32_16x16x32_bf16 v[76:79], v[180:183], v[116:119], v[76:79]\n\t"
    "s_waitcnt lgkmcnt(4)\n\t"
    "v_mfma_f32_16x16x32_bf16 v[64:67], v[184:187], v[120:123], v[64:67]\n\t"
    "ds_read_b128 v[112:115], %[ea] offset:20480\n\t"
    "s_add_u32 m0, %[l0], 12288\n\t"
    "v_mfma_f32_16x16x32_bf16 v[68:71], v[188:191], v[120:123], v[68:71]\n\t"
    "ds_read_b128 v[116:119], %[ea] offset:22528\n\t"
    "global_load_lds_dwordx4 %[av6], s[38:39] sc1\n\t"
    "v_mfma_f32_16x16x32_bf16 v[72:75], v[184:187], v[124:127], v[72:75]\n\t"
    "v_mfma_f32_16x16x32_bf16 v[76:79], v[188:191], v[124:127], v[76:79]\n\t"
    "s_waitcnt lgkmcnt(4)\n\t"
    "v_mfma_f32_16x16x32_bf16 v[64:67], v[192:195], v[96:99], v[64:67]\n\t"
    "ds_read_b128 v[120:123], %[da] offset:20480\n\t"
    "v_mfma_f32_16x16x32_bf16 v[68:71], v[196:199], v[96:99], v[68:71]\n\t"
    "ds_read_b128 v[124:127], %[da] offset:22528\n\t"
    "v_mfma_f32_16x16x32_bf16 v[72:75], v[192:195], v[100:103], v[72:75]\n\t"
    "v_mfma_f32_16x16x32_bf16 v[76:79], v[196:199], v[100:103], v[76:79]\n\t"
    "s_waitcnt lgkmcnt(4)\n\t"
    "v_mfma_f32_16x16x32_bf16 v[64:67], v[200:203], v[104:107], v[64:67]\n\t"
    "ds_read_b128 v[96:99], %[ea] offset:24576\n\t"
    "v_mfma_f32_16x16x32_bf16 v[68:71], v[204:207], v[104:107], v[68:71]\n\t"
    "ds_read_b128 v[100:103], %[ea] offset:26624\n\t"
    "v_mfma_f32_16x16x32_bf16 v[72:75], v[200:203], v[108:111], v[72:75]\n\t"
    "v_mfma_f32_16x16x32_bf16 v[76:79], v[204:207], v[108:111], v[76:79]\n\t"
    "s_add_u32 s36, s36, %[ab]\n\t"
    "s_addc_u32 s37, s37, 0\n\t"
    "s_waitcnt lgkmcnt(4)\n\t"
    "v_mfma_f32_16x16x32_bf16 v[64:67], v[208:211], v[112:115], v[64:67]\n\t"
    "ds_read_b128 v[104:107], %[da] offset:24576\n\t"
    "v_mfma_f32_16x16x32_bf16 v[68:71], v[212:215], v[112:115], v[68:71]\n\t"
    "ds_read_b128 v[108:111], %[da] offset:26624\n\t"
    "v_mfma_f32_16x16x32_bf16 v[72:75], v[208:211], v[116:119], v[72:75]\n\t"
    "v_mfma_f32_16x16x32_bf16 v[76:79], v[212:215], v[116:119], v[76:79]\n\t"
    "s_xor_b32 %[l1], %[l1], 0x10000\n\t"
    "s_waitcnt lgkmcnt(4)\n\t"
    "v_mfma_f32_16x16x32_bf16 v[64:67], v[216:219], v[120:123], v[64:67]\n\t"
    "ds_read_b128 v[112:115], %[ea] offset:28672\n\t"
    "v_mfma_f32_16x16x32_bf16 v[68:71], v[220:223], v[120:123], v[68:71]\n\t"
    "ds_read_b128 v[116:119], %[ea] offset:30720\n\t"
    "v_mfma_f32_16x16x32_bf16 v[72:75], v[216:219], v[124:127], v[72:75]\n\t"
    "v_xor_b32 %[ea], 0x10000, %[ea]\n\t"
    "v_mfma_f32_16x16x32_bf16 v[76:79], v[220:223], v[124:127], v[76:79]\n\t"
    "s_waitcnt vmcnt(6)\n\t"
    "s_barrier\n\t"
    "s_waitcnt lgkmcnt(4)\n\t"
    "v_mfma_f32_16x16x32_bf16 v[64:67], v[224:227], v[96:99], v[64:67]\n\t"
    "ds_read_b32 v39, %[sa]\n\t"
    "ds_read_b128 v[120:123], %[da] offset:28672\n\t"
    "v_mfma_f32_16x16x32_bf16 v[68:71], v[228:231], v[96:99], v[68:71]\n\t"
    "ds_read_b128 v[124:127], %[da] offset:30720\n\t"
    "v_mfma_f32_16x16x32_bf16 v[72:75], v[224:227], v[100:103], v[72:75]\n\t"
    "v_xor_b32 %[da], 0x10000, %[da]\n\t"
    "v_mfma_f32_16x16x32_bf16 v[76:79], v[228:231], v[100:103], v[76:79]\n\t"
    "s_waitcnt lgkmcnt(5)\n\t"
    "v_mfma_f32_16x16x32_bf16 v[64:67], v[232:235], v[104:107], v[64:67]\n\t"
    "ds_read_b128 v[96:99], %[ea] offset:0\n\t"
    "v_mfma_f32_16x16x32_bf16 v[68:71], v[236:239], v[104:107], v[68:71]\n\t"
    "ds_read_b128 v[100:103], %[ea] offset:2048\n\t"
    "v_mfma_f32_16x16x32_bf16 v[72:75], v[232:235], v[108:111], v[72:75]\n\t"
    "v_mfma_f32_16x16x32_bf16 v[76:79], v[236:239], v[108:111], v[76:79]\n\t"
    "s_waitcnt lgkmcnt(4)\n\t"
    "v_mfma_f32_16x16x32_bf16 v[64:67], v[240:243], v[112:115], v[64:67]\n\t"
    "ds_read_b128 v[104:107], %[da] offset:0\n\t"
    "v_mad_u32_u24 v39, v39, %[mu], %[ad]\n\t"
    "v_mfma_f32_16x16x32_bf16 v[68:71], v[244:247], v[112:115], v[68:71]\n\t"
    "ds_read_b128 v[108:111], %[da] offset:2048\n\t"
    "v_cmp_ge_i32 vcc, %[lf], v39\n\t"
    "s_cmp_eq_u64 vcc, 0\n\t"
    "s_addc_u32 %[lf], %[lf], 0\n\t"
    "v_mfma_f32_16x16x32_bf16 v[72:75], v[240:243], v[116:119], v[72:75]\n\t"
    "v_mfma_f32_16x16x32_bf16 v[76:79], v[244:247], v[116:119], v[76:79]\n\t"
    "v_cmp_ge_i32 vcc, %[lf], v39\n\t"
    "s_cmp_eq_u64 vcc, 0\n\t"
    "s_addc_u32 %[lf], %[lf], 0\n\t"
    "s_waitcnt lgkmcnt(4)\n\t"
    "v_mfma_f32_16x16x32_bf16 v[80:83], v[248:251], v[120:123], v[64:67]\n\t"
    "ds_read_b128 v[112:115], %[ea] offset:4096\n\t"
    "s_min_i32 %[lf], %[lf], %[cp]\n\t"
    "s_add_u32 %[b], %[b], 1\n\t"
    "v_mfma_f32_16x16x32_bf16 v[84:87], v[252:255], v[120:123], v[68:71]\n\t"
    "ds_read_b128 v[116:119], %[ea] offset:6144\n\t"
    "v_mfma_f32_16x16x32_bf16 v[88:91], v[248:251], v[124:127], v[72:75]\n\t"
    "v_mfma_f32_16x16x32_bf16 v[92:95], v[252:255], v[124:127], v[76:79]\n\t"
    "s_add_u32 s38, s38, %[ab]\n\t"
    "s_addc_u32 s39, s39, 0\n\t"
    "s_xor_b32 %[l0], %[l0], 0x10000\n\t"
    "s_cmp_lt_i32 %[b], %[lf]\n\t"
    "s_cbranch_scc1 1b\n\t"
    "s_waitcnt lgkmcnt(0)\n\t"
    "s_nop 4"
    : "=&{v[80:95]}"(pd), [ea] "+&v"(ea), [da] "+&v"(da), [b] "+&s"(b), [lf] "+&s"(lf), "+{s[36:37]}"(p1), "+{s[38:39]}"(p0), [sa] "+&v"(sa), [l1] "+&s"(l1), [l0] "+&s"(l0)
    : [rw] "v"(rw), [ab] "s"(ab), [cp] "s"(cp), [mu] "s"(mu), [ad] "v"(ad), "{v[128:159]}"(W[0]), "{v[160:191]}"(W[1]), "{v[192:223]}"(W[2]), "{v[224:255]}"(W[3]), [av0] "v"(av[0]), [av1] "v"(av[1]), [av2] "v"(av[2]), [av3] "v"(av[3]), [av4] "v"(av[4]), [av5] "v"(av[5]), [av6] "v"(av[6]), [av7] "v"(av[7])
    : "v39", "v64", "v65", "v66", "v67", "v68", "v69", "v70", "v71", "v72", "v73", "v74", "v75", "v76", "v77", "v78", "v79", "v96", "v97", "v98", "v99", "v100", "v101", "v102", "v103", "v104", "v105", "v106", "v107", "v108", "v109", "v110", "v111", "v112", "v113", "v114", "v115", "v116", "v117", "v118", "v119", "v120", "v121", "v122", "v123", "v124", "v125", "v126", "v127", "vcc", "scc", "m0", "memory");
}
__device__ __forceinline__ void near_st0(const u32x32 (&W)[4], u32x16& pd, unsigned ea, unsigned da, int& b, int& lf, int cp, unsigned sa, unsigned mu, unsigned ad,
    unsigned rw, unsigned ab, const char* p1, const char* p0, unsigned co0, unsigned co1, int po, int pf, unsigned w0, unsigned sl, unsigned dv, const char* db, const char* pg) {
  unsigned st;
  asm volatile("s_waitcnt vmcnt(0)\n\t"
    "s_barrier\n\t"
    "ds_read_b128 v[96:99], %[ea] offset:0\n\t"
    "ds_read_b128 v[100:103], %[ea] offset:2048\n\t"
    "ds_read_b128 v[104:107], %[da] offset:0\n\t"
    "ds_read_b128 v[108:111], %[da] offset:2048\n\t"
    "ds_read_b128 v[112:115], %[ea] offset:4096\n\t"
    "ds_read_b128 v[116:119], %[ea] offset:6144\n\t"
    "s_branch 2f\n\t"
    "1:\n\t"
    "s_waitcnt vmcnt(0)\n\t"
    "s_barrier\n\t"
    "2:\n\t"
    "s_waitcnt lgkmcnt(4)\n\t"
    "v_mfma_f32_16x16x32_bf16 v[64:67], v[128:131], v[96:99], 0\n\t"
    "ds_read_b128 v[120:123], %[da] offset:4096\n\t"
    "v_mfma_f32_16x16x32_bf16 v[68:71], v[132:135], v[96:99], 0\n\t"
    "ds_read_b128 v[124:127], %[da] offset:6144\n\t"
    "v_mfma_f32_16x16x32_bf16 v[72:75], v[128:131], v[100:103], 0\n\t"
    "v_mfma_f32_16x16x32_bf16 v[76:79], v[132:135], v[100:103], 0\n\t"
    "s_cmp_eq_u32 %[w0], 0\n\t"
    "s_cbranch_scc1 5f\n\t"
    "6:\n\t"
    "s_waitcnt lgkmcnt(4)\n\t"
    "v_mfma_f32_16x16x32_bf16 v[64:67], v[136:139], v[104:107], v[64:67]\n\t"
    "ds_read_b128 v[96:99], %[ea] offset:8192\n\t"
    "v_mfma_f32_16x16x32_bf16 v[68:71], v[140:143], v[104:107], v[68:71]\n\t"
    "ds_read_b128 v[100:103], %[ea] offset:10240\n\t"
    "v_mfma_f32_16x16x32_bf16 v[72:75], v[136:139], v[108:111], v[72:75]\n\t"
    "v_mfma_f32_16x16x32_bf16 v[76:79], v[140:143], v[108:111], v[76:79]\n\t"
    "s_waitcnt lgkmcnt(4)\n\t"
    "v_mfma_f32_16x16x32_bf16 v[64:67], v[144:147], v[112:115], v[64:67]\n\t"
    "ds_read_b128 v[104:107], %[da] offset:8192\n\t"
    "v_mfma_f32_16x16x32_bf16 v[68:71], v[148:151], v[112:115], v[68:71]\n\t"
    "ds_read_b128 v[108:111], %[da] offset:10240\n\t"
    "v_mfma_f32_16x16x32_bf16 v[72:75], v[144:147], v[116:119], v[72:75]\n\t"
    "v_mfma_f32_16x16x32_bf16 v[76:79], v[148:151], v[116:119], v[76:79]\n\t"
    "s_barrier\n\t"
    "s_waitcnt lgkmcnt(4)\n\t"
    "v_mfma_f32_16x16x32_bf16 v[64:67], v[152:155], v[120:123], v[64:67]\n\t"
    "ds_read_b128 v[112:115], %[ea] offset:12288\n\t"
    "v_mfma_f32_16x16x32_bf16 v[68:71], v[156:159], v[120:123], v[68:71]\n\t"
    "ds_read_b128 v[116:119], %[ea] offset:14336\n\t"
    "v_mfma_f32_16x16x32_bf16 v[72:75], v[152:155], v[124:127], v[72:75]\n\t"
    "ds_read_b128 v[48:51], %[rw] offset:0\n\t"
    "v_mfma_f32_16x16x32_bf16 v[76:79], v[156:159], v[124:127], v[76:79]\n\t"
    "ds_read_b128 v[52:55], %[rw] offset:1024\n\t"
    "s_waitcnt lgkmcnt(6)\n\t"
    "v_mfma_f32_16x16x32_bf16 v[64:67], v[160:163], v[96:99], v[64:67]\n\t"
    "ds_read_b128 v[120:123], %[da] offset:12288\n\t"
    "v_mfma_f32_16x16x32_bf16 v[68:71], v[164:167], v[96:99], v[68:71]\n\t"
    "ds_read_b128 v[124:127], %[da] offset:14336\n\t"
    "v_mfma_f32_16x16x32_bf16 v[72:75], v[160:163], v[100:103], v[72:75]\n\t"
    "ds_read_b128 v[56:59], %[rw] offset:2048\n\t"
    "v_mfma_f32_16x16x32_bf16 v[76:79], v[164:167], v[100:103], v[76:79]\n\t"
    "ds_read_b128 v[60:63], %[rw] offset:3072\n\t"
    "s_waitcnt lgkmcnt(0)\n\t"
    "v_mfma_f32_16x16x32_bf16 v[64:67], v[168:171], v[104:107], v[64:67]\n\t"
    "ds_read_b128 v[96:99], %[ea] offset:16384\n\t"
    "v_add_f32 v48, v48, v80\n\t"
    "v_add_f32 v49, v49, v81\n\t"
    "v_mfma_f32_16x16x32_bf16 v[68:71], v[172:175], v[104:107], v[68:71]\n\t"
    "ds_read_b128 v[100:103], %[ea] offset:18432\n\t"
    "v_add_f32 v50, v50, v82\n\t"
    "v_mfma_f32_16x16x32_bf16 v[72:75], v[168:171], v[108:111], v[72:75]\n\t"
    "v_add_f32 v51, v51, v83\n\t"
    "v_mfma_f32_16x16x32_bf16 v[76:79], v[172:175], v[108:111], v[76:79]\n\t"
    "v_add_f32 v52, v52, v84\n\t"
    "v_add_f32 v53, v53, v85\n\t"
    "v_mfma_f32_16x16x32_bf16 v[64:67], v[176:179], v[112:115], v[64:67]\n\t"
    "ds_read_b128 v[104:107], %[da] offset:16384\n\t"
    "v_add_f32 v54, v54, v86\n\t"
    "v_mfma_f32_16x16x32_bf16 v[68:71], v[180:183], v[112:115], v[68:71]\n\t"
    "ds_read_b128 v[108:111], %[da] offset:18432\n\t"
    "v_add_f32 v55, v55, v87\n\t"
    "v_mfma_f32_16x16x32_bf16 v[72:75], v[176:179], v[116:119], v[72:75]\n\t"
    "v_add_f32 v56, v56, v88\n\t"
    "v_add_f32 v57, v57, v89\n\t"
    "v_mfma_f32_16x16x32_bf16 v[76:79], v[180:183], v[116:119], v[76:79]\n\t"
    "v_add_f32 v58, v58, v90\n\t"
    "v_mfma_f32_16x16x32_bf16 v[64:67], v[184:187], v[120:123], v[64:67]\n\t"
    "ds_read_b128 v[112:115], %[ea] offset:20480\n\t"
    "v_add_f32 v59, v59, v91\n\t"
    "v_mfma_f32_16x16x32_bf16 v[68:71], v[188:191], v[120:123], v[68:71]\n\t"
    "ds_read_b128 v[116:119], %[ea] offset:22528\n\t"
    "v_add_f32 v60, v60, v92\n\t"
    "v_mfma_f32_16x16x32_bf16 v[72:75], v[184:187], v[124:127], v[72:75]\n\t"
    "v_add_f32 v61, v61, v93\n\t"
    "v_add_f32 v62, v62, v94\n\t"
    "v_mfma_f32_16x16x32_bf16 v[76:79], v[188:191], v[124:127], v[76:79]\n\t"
    "v_add_f32 v63, v63, v95\n\t"
    "s_waitcnt lgkmcnt(4)\n\t"
    "v_mfma_f32_16x16x32_bf16 v[64:67], v[192:195], v[96:99], v[64:67]\n\t"
    "ds_read_b128 v[120:123], %[da] offset:20480\n\t"
    "v_cvt_pk_bf16_f32 v40, v48, v49\n\t"
    "v_mfma_f32_16x16x32_bf16 v[68:71], v[196:199], v[96:99], v[68:71]\n\t"
    "ds_read_b128 v[124:127], %[da] offset:22528\n\t"
    "v_cvt_pk_bf16_f32 v41, v50, v51\n\t"
    "v_cvt_pk_bf16_f32 v42, v52, v53\n\t"
    "v_mfma_f32_16x16x32_bf16 v[72:75], v[192:195], v[100:103], v[72:75]\n\t"
    "v_cvt_pk_bf16_f32 v43, v54, v55\n\t"
    "v_mfma_f32_16x16x32_bf16 v[76:79], v[196:199], v[100:103], v[76:79]\n\t"
    "global_store_dwordx4 %[co0], v[40:43], s[36:37] sc1\n\t"
    "s_waitcnt lgkmcnt(4)\n\t"
    "v_mfma_f32_16x16x32_bf16 v[64:67], v[200:203], v[104:107], v[64:67]\n\t"
    "ds_read_b128 v[96:99], %[ea] offset:24576\n\t"
    "v_cvt_pk_bf16_f32 v44, v56, v57\n\t"
    "v_cvt_pk_bf16_f32 v45, v58, v59\n\t"
    "v_mfma_f32_16x16x32_bf16 v[68:71], v[204:207], v[104:107], v[68:71]\n\t"
    "ds_read_b128 v[100:103], %[ea] offset:26624\n\t"
    "v_cvt_pk_bf16_f32 v46, v60, v61\n\t"
    "v_mfma_f32_16x16x32_bf16 v[72:75], v[200:203], v[108:111], v[72:75]\n\t"
    "v_cvt_pk_bf16_f32 v47, v62, v63\n\t"
    "v_mfma_f32_16x16x32_bf16 v[76:79], v[204:207], v[108:111], v[76:79]\n\t"
    "global_store_dwordx4 %[co1], v[44:47], s[36:37] sc1\n\t"
    "s_waitcnt lgkmcnt(4)\n\t"
    "v_mfma_f32_16x16x32_bf16 v[64:67], v[208:211], v[112:115], v[64:67]\n\t"
    "ds_read_b128 v[104:107], %[da] offset:24576\n\t"
    "v_mfma_f32_16x16x32_bf16 v[68:71], v[212:215], v[112:115], v[68:71]\n\t"
    "ds_read_b128 v[108:111], %[da] offset:26624\n\t"
    "v_mfma_f32_16x16x32_bf16 v[72:75], v[208:211], v[116:119], v[72:75]\n\t"
    "v_mfma_f32_16x16x32_bf16 v[76:79], v[212:215], v[116:119], v[76:79]\n\t"
    "s_waitcnt lgkmcnt(4)\n\t"
    "v_mfma_f32_16x16x32_bf16 v[64:67], v[216:219], v[120:123], v[64:67]\n\t"
    "ds_read_b128 v[112:115], %[ea] offset:28672\n\t"
    "v_mfma_f32_16x16x32_bf16 v[68:71], v[220:223], v[120:123], v[68:71]\n\t"
    "ds_read_b128 v[116:119], %[ea] offset:30720\n\t"
    "v_mfma_f32_16x16x32_bf16 v[72:75], v[216:219], v[124:127], v[72:75]\n\t"
    "v_xor_b32 %[ea], 0x10000, %[ea]\n\t"
    "v_mfma_f32_16x16x32_bf16 v[76:79], v[220:223], v[124:127], v[76:79]\n\t"
    "s_waitcnt vmcnt(2)\n\t"
    "s_barrier\n\t"
    "s_waitcnt lgkmcnt(4)\n\t"
    "v_mfma_f32_16x16x32_bf16 v[64:67], v[224:227], v[96:99], v[64:67]\n\t"
    "ds_read_b32 v39, %[sa]\n\t"
    "ds_read_b128 v[120:123], %[da] offset:28672\n\t"
    "v_mfma_f32_16x16x32_bf16 v[68:71], v[228:231], v[96:99], v[68:71]\n\t"
    "ds_read_b128 v[124:127], %[da] offset:30720\n\t"
    "v_mfma_f32_16x16x32_bf16 v[72:75], v[224:227], v[100:103], v[72:75]\n\t"
    "v_xor_b32 %[da], 0x10000, %[da]\n\t"
    "v_mfma_f32_16x16x32_bf16 v[76:79], v[228:231], v[100:103], v[76:79]\n\t"
    "s_waitcnt lgkmcnt(5)\n\t"
    "v_mfma_f32_16x16x32_bf16 v[64:67], v[232:235], v[104:107], v[64:67]\n\t"
    "ds_read_b128 v[96:99], %[ea] offset:0\n\t"
    "v_mfma_f32_16x16x32_bf16 v[68:71], v[236:239], v[104:107], v[68:71]\n\t"
    "ds_read_b128 v[100:103], %[ea] offset:2048\n\t"
    "v_mfma_f32_16x16x32_bf16 v[72:75], v[232:235], v[108:111], v[72:75]\n\t"
    "v_mfma_f32_16x16x32_bf16 v[76:79], v[236:239], v[108:111], v[76:79]\n\t"
    "s_waitcnt lgkmcnt(4)\n\t"
    "v_mfma_f32_16x16x32_bf16 v[64:67], v[240:243], v[112:115], v[64:67]\n\t"
    "ds_read_b128 v[104:107], %[da] offset:0\n\t"
    "v_mad_u32_u24 v39, v39, %[mu], %[ad]\n\t"
    "v_mfma_f32_16x16x32_bf16 v[68:71], v[244:247], v[112:115], v[68:71]\n\t"
    "ds_read_b128 v[108:111], %[da] offset:2048\n\t"
    "v_cmp_ge_i32 vcc, %[lf], v39\n\t"
    "s_cmp_eq_u64 vcc, 0\n\t"
    "s_addc_u32 %[lf], %[lf], 0\n\t"
    "v_mfma_f32_16x16x32_bf16 v[72:75], v[240:243], v[116:119], v[72:75]\n\t"
    "v_mfma_f32_16x16x32_bf16 v[76:79], v[244:247], v[116:119], v[76:79]\n\t"
    "v_cmp_ge_i32 vcc, %[lf], v39\n\t"
    "s_cmp_eq_u64 vcc, 0\n\t"
    "s_addc_u32 %[lf], %[lf], 0\n\t"
    "s_waitcnt lgkmcnt(4)\n\t"
    "v_mfma_f32_16x16x32_bf16 v[80:83], v[248:251], v[120:123], v[64:67]\n\t"
    "ds_read_b128 v[112:115], %[ea] offset:4096\n\t"
    "s_min_i32 %[lf], %[lf], %[cp]\n\t"
    "s_add_u32 %[b], %[b], 1\n\t"
    "v_mfma_f32_16x16x32_bf16 v[84:87], v[252:255], v[120:123], v[68:71]\n\t"
    "ds_read_b128 v[116:119], %[ea] offset:6144\n\t"
    "v_mfma_f32_16x16x32_bf16 v[88:91], v[248:251], v[124:127], v[72:75]\n\t"
    "v_mfma_f32_16x16x32_bf16 v[92:95], v[252:255], v[124:127], v[76:79]\n\t"
    "s_mov_b64 s[36:37], s[38:39]\n\t"
    "s_add_u32 s38, s38, %[ab]\n\t"
    "s_addc_u32 s39, s39, 0\n\t"
    "s_cmp_lt_i32 %[b], %[lf]\n\t"
    "s_cbranch_scc1 1b\n\t"
    "s_waitcnt lgkmcnt(0)\n\t"
    "s_nop 4\n\t"
    "s_branch 9f\n\t"
    "5:\n\t"
    "s_add_u32 %[st], %[b], %[po]\n\t"
    "s_max_i32 %[st], %[st], %[p0]\n\t"
    "v_mov_b32 v39, %[st]\n\t"
    "v_mov_b32 v38, 0\n\t"
    "s_mov_b64 exec, 1\n\t"
    "global_store_dword v38, v39, %[pg] sc1\n\t"
    "s_mov_b64 exec, -1\n\t"
    "s_mov_b32 %[p0], %[p1]\n\t"
    "s_cmp_ge_i32 %[lf], %[cp]\n\t"
    "s_cbranch_scc1 6b\n\t"
    "s_mov_b32 m0, %[sl]\n\t"
    "s_nop 0\n\t"
    "global_load_lds_dword %[dv], %[db] sc1\n\t"
    "s_branch 6b\n\t"
    "9:"
    : "=&{v[80:95]}"(pd), [ea] "+&v"(ea), [da] "+&v"(da), [b] "+&s"(b), [lf] "+&s"(lf), "+{s[36:37]}"(p1), "+{s[38:39]}"(p0), [sa] "+&v"(sa), [st] "=&s"(st), [sl] "+&s"(sl), [p0] "+&s"(pf)
    : [rw] "v"(rw), [ab] "s"(ab), [cp] "s"(cp), [mu] "s"(mu), [ad] "v"(ad), "{v[128:159]}"(W[0]), "{v[160:191]}"(W[1]), "{v[192:223]}"(W[2]), "{v[224:255]}"(W[3]), [co0] "v"(co0), [w0] "s"(w0), [dv] "v"(dv), [db] "s"(db), [pg] "s"(pg), [po] "s"(po - 1), [p1] "s"(b + po), [co1] "v"(co1)
    : "v38", "v39", "v40", "v41", "v42", "v43", "v44", "v45", "v46", "v47", "v48", "v49", "v50", "v51", "v52", "v53", "v54", "v55", "v56", "v57", "v58", "v59", "v60", "v61", "v62", "v63", "v64", "v65", "v66", "v67", "v68", "v69", "v70", "v71", "v72", "v73", "v74", "v75", "v76", "v77", "v78", "v79", "v96", "v97", "v98", "v99", "v100", "v101", "v102", "v103", "v104", "v105", "v106", "v107", "v108", "v109", "v110", "v111", "v112", "v113", "v114", "v115", "v116", "v117", "v118", "v119", "v120", "v121", "v122", "v123", "v124", "v125", "v126", "v127", "vcc", "scc", "m0", "memory");
}
__device__ __forceinline__ void near_st1(const u32x32 (&W)[4], u32x16& pd, unsigned ea, unsigned da, int& b, int& lf, int cp, unsigned sa, unsigned mu, unsigned ad,
    unsigned rw, unsigned ab, const char* p1, const char* p0, unsigned co0, unsigned co1, int po, int pf, unsigned w0, unsigned sl, unsigned dv, const char* db, const char* pg) {
  unsigned st;
  asm volatile("s_waitcnt vmcnt(0)\n\t"
    "s_barrier\n\t"
    "ds_read_b128 v[96:99], %[ea] offset:0\n\t"
    "ds_read_b128 v[100:103], %[ea] offset:2048\n\t"
    "ds_read_b128 v[104:107], %[da] offset:0\n\t"
    "ds_read_b128 v[108:111], %[da] offset:2048\n\t"
    "ds_read_b128 v[112:115], %[ea] offset:4096\n\t"
    "ds_read_b128 v[116:119], %[ea] offset:6144\n\t"
    "s_branch 2f\n\t"
    "1:\n\t"
    "s_waitcnt vmcnt(0)\n\t"
    "s_barrier\n\t"
    "2:\n\t"
    "s_waitcnt lgkmcnt(4)\n\t"
    "v_mfma_f32_16x16x32_bf16 v[64:67], v[128:131], v[96:99], 0\n\t"
    "ds_read_b128 v[120:123], %[da] offset:4096\n\t"
    "v_mfma_f32_16x16x32_bf16 v[68:71], v[132:135], v[96:99], 0\n\t"
    "ds_read_b128 v[124:127], %[da] offset:6144\n\t"
    "v_mfma_f32_16x16x32_bf16 v[72:75], v[128:131], v[100:103], 0\n\t"
    "v_mfma_f32_16x16x32_bf16 v[76:79], v[132:135], v[100:103], 0\n\t"
    "s_cmp_eq_u32 %[w0], 0\n\t"
    "s_cbranch_scc1 5f\n\t"
    "6:\n\t"
    "s_waitcnt lgkmcnt(4)\n\t"
    "v_mfma_f32_16x16x32_bf16 v[64:67], v[136:139], v[104:107], v[64:67]\n\t"
    "ds_read_b128 v[96:99], %[ea] offset:8192\n\t"
    "v_mfma_f32_16x16x32_bf16 v[68:71], v[140:143], v[104:107], v[68:71]\n\t"
    "ds_read_b128 v[100:103], %[ea] offset:10240\n\t"
    "v_mfma_f32_16x16x32_bf16 v[72:75], v[136:139], v[108:111], v[72:75]\n\t"
    "v_mfma_f32_16x16x32_bf16 v[76:79], v[140:143], v[108:111], v[76:79]\n\t"
    "s_waitcnt lgkmcnt(4)\n\t"
    "v_mfma_f32_16x16x32_bf16 v[64:67], v[144:147], v[112:115], v[64:67]\n\t"
    "ds_read_b128 v[104:107], %[da] offset:8192\n\t"
    "v_mfma_f32_16x16x32_bf16 v[68:71], v[148:151], v[112:115], v[68:71]\n\t"
    "ds_read_b128 v[108:111], %[da] offset:10240\n\t"
    "v_mfma_f32_16x16x32_bf16 v[72:75], v[144:147], v[116:119], v[72:75]\n\t"
    "v_mfma_f32_16x16x32_bf16 v[76:79], v[148:151], v[116:119], v[76:79]\n\t"
    "s_barrier\n\t"
    "s_waitcnt lgkmcnt(4)\n\t"
    "v_mfma_f32_16x16x32_bf16 v[64:67], v[152:155], v[120:123], v[64:67]\n\t"
    "ds_read_b128 v[112:115], %[ea] offset:12288\n\t"
    "v_mfma_f32_16x16x32_bf16 v[68:71], v[156:159], v[120:123], v[68:71]\n\t"
    "ds_read_b128 v[116:119], %[ea] offset:14336\n\t"
    "v_mfma_f32_16x16x32_bf16 v[72:75], v[152:155], v[124:127], v[72:75]\n\t"
    "ds_read_b128 v[48:51], %[rw] offset:0\n\t"
    "v_mfma_f32_16x16x32_bf16 v[76:79], v[156:159], v[124:127], v[76:79]\n\t"
    "ds_read_b128 v[52:55], %[rw] offset:1024\n\t"
    "s_waitcnt lgkmcnt(6)\n\t"
    "v_mfma_f32_16x16x32_bf16 v[64:67], v[160:163], v[96:99], v[64:67]\n\t"
    "ds_read_b128 v[120:123], %[da] offset:12288\n\t"
    "v_mfma_f32_16x16x32_bf16 v[68:71], v[164:167], v[96:99], v[68:71]\n\t"
    "ds_read_b128 v[124:127], %[da] offset:14336\n\t"
    "v_mfma_f32_16x16x32_bf16 v[72:75], v[160:163], v[100:103], v[72:75]\n\t"
    "ds_read_b128 v[56:59], %[rw] offset:2048\n\t"
    "v_mfma_f32_16x16x32_bf16 v[76:79], v[164:167], v[100:103], v[76:79]\n\t"
    "ds_read_b128 v[60:63], %[rw] offset:3072\n\t"
    "s_waitcnt lgkmcnt(0)\n\t"
    "v_mfma_f32_16x16x32_bf16 v[64:67], v[168:171], v[104:107], v[64:67]\n\t"
    "ds_read_b128 v[96:99], %[ea] offset:16384\n\t"
    "v_add_f32 v48, v48, v80\n\t"
    "v_add_f32 v49, v49, v81\n\t"
    "v_add_f32 v50, v50, v82\n\t"
    "v_mfma_f32_16x16x32_bf16 v[68:71], v[172:175], v[104:107], v[68:71]\n\t"
    "ds_read_b128 v[100:103], %[ea] offset:18432\n\t"
    "v_add_f32 v51, v51, v83\n\t"
    "v_add_f32 v52, v52, v84\n\t"
    "v_add_f32 v53, v53, v85\n\t"
    "v_mfma_f32_16x16x32_bf16 v[72:75], v[168:171], v[108:111], v[72:75]\n\t"
    "v_add_f32 v54, v54, v86\n\t"
    "v_add_f32 v55, v55, v87\n\t"
    "v_add_f32 v56, v56, v88\n\t"
    "v_mfma_f32_16x16x32_bf16 v[76:79], v[172:175], v[108:111], v[76:79]\n\t"
    "v_add_f32 v57, v57, v89\n\t"
    "v_add_f32 v58, v58, v90\n\t"
    "v_add_f32 v59, v59, v91\n\t"
    "v_mfma_f32_16x16x32_bf16 v[64:67], v[176:179], v[112:115], v[64:67]\n\t"
    "ds_read_b128 v[104:107], %[da] offset:16384\n\t"
    "v_add_f32 v60, v60, v92\n\t"
    "v_add_f32 v61, v61, v93\n\t"
    "v_add_f32 v62, v62, v94\n\t"
    "v_mfma_f32_16x16x32_bf16 v[68:71], v[180:183], v[112:115], v[68:71]\n\t"
    "ds_read_b128 v[108:111], %[da] offset:18432\n\t"
    "v_add_f32 v63, v63, v95\n\t"
    "v_mul_f32 v40, %[nl], v48\n\t"
    "v_mul_f32 v41, %[nl], v49\n\t"
    "v_mfma_f32_16x16x32_bf16 v[72:75], v[176:179], v[116:119], v[72:75]\n\t"
    "v_mul_f32 v42, %[nl], v50\n\t"
    "v_mul_f32 v43, %[nl], v51\n\t"
    "v_mul_f32 v44, %[nl], v56\n\t"
    "v_mfma_f32_16x16x32_bf16 v[76:79], v[180:183], v[116:119], v[76:79]\n\t"
    "v_mul_f32 v45, %[nl], v57\n\t"
    "v_mul_f32 v46, %[nl], v58\n\t"
    "v_mul_f32 v47, %[nl], v59\n\t"
    "v_mfma_f32_16x16x32_bf16 v[64:67], v[184:187], v[120:123], v[64:67]\n\t"
    "ds_read_b128 v[112:115], %[ea] offset:20480\n\t"
    "v_exp_f32 v40, v40\n\t"
    "v_exp_f32 v41, v41\n\t"
    "v_exp_f32 v42, v42\n\t"
    "v_mfma_f32_16x16x32_bf16 v[68:71], v[188:191], v[120:123], v[68:71]\n\t"
    "ds_read_b128 v[116:119], %[ea] offset:22528\n\t"
    "v_exp_f32 v43, v43\n\t"
    "v_exp_f32 v44, v44\n\t"
    "v_exp_f32 v45, v45\n\t"
    "v_mfma_f32_16x16x32_bf16 v[72:75], v[184:187], v[124:127], v[72:75]\n\t"
    "v_exp_f32 v46, v46\n\t"
    "v_exp_f32 v47, v47\n\t"
    "v_add_f32 v40, 1.0, v40\n\t"
    "v_mfma_f32_16x16x32_bf16 v[76:79], v[188:191], v[124:127], v[76:79]\n\t"
    "v_add_f32 v41, 1.0, v41\n\t"
    "v_add_f32 v42, 1.0, v42\n\t"
    "v_add_f32 v43, 1.0, v43\n\t"
    "s_waitcnt lgkmcnt(4)\n\t"
    "v_mfma_f32_16x16x32_bf16 v[64:67], v[192:195], v[96:99], v[64:67]\n\t"
    "ds_read_b128 v[120:123], %[da] offset:20480\n\t"
    "v_add_f32 v44, 1.0, v44\n\t"
    "v_add_f32 v45, 1.0, v45\n\t"
    "v_add_f32 v46, 1.0, v46\n\t"
    "v_mfma_f32_16x16x32_bf16 v[68:71], v[196:199], v[96:99], v[68:71]\n\t"
    "ds_read_b128 v[124:127], %[da] offset:22528\n\t"
    "v_add_f32 v47, 1.0, v47\n\t"
    "v_rcp_f32 v40, v40\n\t"
    "v_rcp_f32 v41, v41\n\t"
    "v_mfma_f32_16x16x32_bf16 v[72:75], v[192:195], v[100:103], v[72:75]\n\t"
    "v_rcp_f32 v42, v42\n\t"
    "v_rcp_f32 v43, v43\n\t"
    "v_rcp_f32 v44, v44\n\t"
    "v_mfma_f32_16x16x32_bf16 v[76:79], v[196:199], v[100:103], v[76:79]\n\t"
    "v_rcp_f32 v45, v45\n\t"
    "v_rcp_f32 v46, v46\n\t"
    "v_rcp_f32 v47, v47\n\t"
    "s_waitcnt lgkmcnt(4)\n\t"
    "v_mfma_f32_16x16x32_bf16 v[64:67], v[200:203], v[104:107], v[64:67]\n\t"
    "ds_read_b128 v[96:99], %[ea] offset:24576\n\t"
    "v_mul_f32 v40, v48, v40\n\t"
    "v_mul_f32 v41, v49, v41\n\t"
    "v_mul_f32 v42, v50, v42\n\t"
    "v_mfma_f32_16x16x32_bf16 v[68:71], v[204:207], v[104:107], v[68:71]\n\t"
    "ds_read_b128 v[100:103], %[ea] offset:26624\n\t"
    "v_mul_f32 v43, v51, v43\n\t"
    "v_mul_f32 v44, v56, v44\n\t"
    "v_mul_f32 v45, v57, v45\n\t"
    "v_mfma_f32_16x16x32_bf16 v[72:75], v[200:203], v[108:111], v[72:75]\n\t"
    "v_mul_f32 v46, v58, v46\n\t"
    "v_mul_f32 v47, v59, v47\n\t"
    "v_mul_f32 v40, v40, v52\n\t"
    "v_mfma_f32_16x16x32_bf16 v[76:79], v[204:207], v[108:111], v[76:79]\n\t"
    "v_mul_f32 v41, v41, v53\n\t"
    "v_mul_f32 v42, v42, v54\n\t"
    "v_mul_f32 v43, v43, v55\n\t"
    "s_waitcnt lgkmcnt(4)\n\t"
    "v_mfma_f32_16x16x32_bf16 v[64:67], v[208:211], v[112:115], v[64:67]\n\t"
    "ds_read_b128 v[104:107], %[da] offset:24576\n\t"
    "v_mul_f32 v44, v44, v60\n\t"
    "v_mul_f32 v45, v45, v61\n\t"
    "v_mul_f32 v46, v46, v62\n\t"
    "v_mfma_f32_16x16x32_bf16 v[68:71], v[212:215], v[112:115], v[68:71]\n\t"
    "ds_read_b128 v[108:111], %[da] offset:26624\n\t"
    "v_mul_f32 v47, v47, v63\n\t"
    "v_cvt_pk_bf16_f32 v48, v40, v41\n\t"
    "v_cvt_pk_bf16_f32 v49, v42, v43\n\t"
    "v_mfma_f32_16x16x32_bf16 v[72:75], v[208:211], v[116:119], v[72:75]\n\t"
    "v_cvt_pk_bf16_f32 v50, v44, v45\n\t"
    "v_cvt_pk_bf16_f32 v51, v46, v47\n\t"
    "s_nop 1\n\t"
    "v_mfma_f32_16x16x32_bf16 v[76:79], v[212:215], v[116:119], v[76:79]\n\t"
    "v_permlane16_swap_b32 v48, v50\n\t"
    "v_permlane16_swap_b32 v49, v51\n\t"
    "global_store_dwordx4 %[co0], v[48:51], s[36:37] sc1\n\t"
    "s_waitcnt lgkmcnt(4)\n\t"
    "v_mfma_f32_16x16x32_bf16 v[64:67], v[216:219], v[120:123], v[64:67]\n\t"
    "ds_read_b128 v[112:115], %[ea] offset:28672\n\t"
    "v_mfma_f32_16x16x32_bf16 v[68:71], v[220:223], v[120:123], v[68:71]\n\t"
    "ds_read_b128 v[116:119], %[ea] offset:30720\n\t"
    "v_mfma_f32_16x16x32_bf16 v[72:75], v[216:219], v[124:127], v[72:75]\n\t"
    "v_xor_b32 %[ea], 0x10000, %[ea]\n\t"
    "v_mfma_f32_16x16x32_bf16 v[76:79], v[220:223], v[124:127], v[76:79]\n\t"
    "s_waitcnt vmcnt(1)\n\t"
    "s_barrier\n\t"
    "s_waitcnt lgkmcnt(4)\n\t"
    "v_mfma_f32_16x16x32_bf16 v[64:67], v[224:227], v[96:99], v[64:67]\n\t"
    "ds_read_b32 v39, %[sa]\n\t"
    "ds_read_b128 v[120:123], %[da] offset:28672\n\t"
    "v_mfma_f32_16x16x32_bf16 v[68:71], v[228:231], v[96:99], v[68:71]\n\t"
    "ds_read_b128 v[124:127], %[da] offset:30720\n\t"
    "v_mfma_f32_16x16x32_bf16 v[72:75], v[224:227], v[100:103], v[72:75]\n\t"
    "v_xor_b32 %[da], 0x10000, %[da]\n\t"
    "v_mfma_f32_16x16x32_bf16 v[76:79], v[228:231], v[100:103], v[76:79]\n\t"
    "s_waitcnt lgkmcnt(5)\n\t"
    "v_mfma_f32_16x16x32_bf16 v[64:67], v[232:235], v[104:107], v[64:67]\n\t"
    "ds_read_b128 v[96:99], %[ea] offset:0\n\t"
    "v_mfma_f32_16x16x32_bf16 v[68:71], v[236:239], v[104:107], v[68:71]\n\t"
    "ds_read_b128 v[100:103], %[ea] offset:2048\n\t"
    "v_mfma_f32_16x16x32_bf16 v[72:75], v[232:235], v[108:111], v[72:75]\n\t"
    "v_mfma_f32_16x16x32_bf16 v[76:79], v[236:239], v[108:111], v[76:79]\n\t"
    "s_waitcnt lgkmcnt(4)\n\t"
    "v_mfma_f32_16x16x32_bf16 v[64:67], v[240:243], v[112:115], v[64:67]\n\t"
    "ds_read_b128 v[104:107], %[da] offset:0\n\t"
    "v_mad_u32_u24 v39, v39, %[mu], %[ad]\n\t"
    "v_mfma_f32_16x16x32_bf16 v[68:71], v[244:247], v[112:115], v[68:71]\n\t"
    "ds_read_b128 v[108:111], %[da] offset:2048\n\t"
    "v_cmp_ge_i32 vcc, %[lf], v39\n\t"
    "s_cmp_eq_u64 vcc, 0\n\t"
    "s_addc_u32 %[lf], %[lf], 0\n\t"
    "v_mfma_f32_16x16x32_bf16 v[72:75], v[240:243], v[116:119], v[72:75]\n\t"
    "v_mfma_f32_16x16x32_bf16 v[76:79], v[244:247], v[116:119], v[76:79]\n\t"
    "v_cmp_ge_i32 vcc, %[lf], v39\n\t"
    "s_cmp_eq_u64 vcc, 0\n\t"
    "s_addc_u32 %[lf], %[lf], 0\n\t"
    "s_waitcnt lgkmcnt(4)\n\t"
    "v_mfma_f32_16x16x32_bf16 v[80:83], v[248:251], v[120:123], v[64:67]\n\t"
    "ds_read_b128 v[112:115], %[ea] offset:4096\n\t"
    "s_min_i32 %[lf], %[lf], %[cp]\n\t"
    "s_add_u32 %[b], %[b], 1\n\t"
    "v_mfma_f32_16x16x32_bf16 v[84:87], v[252:255], v[120:123], v[68:71]\n\t"
    "ds_read_b128 v[116:119], %[ea] offset:6144\n\t"
    "v_mfma_f32_16x16x32_bf16 v[88:91], v[248:251], v[124:127], v[72:75]\n\t"
    "v_mfma_f32_16x16x32_bf16 v[92:95], v[252:255], v[124:127], v[76:79]\n\t"
    "s_mov_b64 s[36:37], s[38:39]\n\t"
    "s_add_u32 s38, s38, %[ab]\n\t"
    "s_addc_u32 s39, s39, 0\n\t"
    "s_cmp_lt_i32 %[b], %[lf]\n\t"
    "s_cbranch_scc1 1b\n\t"
    "s_waitcnt lgkmcnt(0)\n\t"
    "s_nop 4\n\t"
    "s_branch 9f\n\t"
    "5:\n\t"
    "s_add_u32 %[st], %[b], %[po]\n\t"
    "s_max_i32 %[st], %[st], %[p0]\n\t"
    "v_mov_b32 v39, %[st]\n\t"
    "v_mov_b32 v38, 0\n\t"
    "s_mov_b64 exec, 1\n\t"
    "global_store_dword v38, v39, %[pg] sc1\n\t"
    "s_mov_b64 exec, -1\n\t"
    "s_mov_b32 %[p0], %[p1]\n\t"
    "s_cmp_ge_i32 %[lf], %[cp]\n\t"
    "s_cbranch_scc1 6b\n\t"
    "s_mov_b32 m0, %[sl]\n\t"
    "s_nop 0\n\t"
    "global_load_lds_dword %[dv], %[db] sc1\n\t"
    "s_branch 6b\n\t"
    "9:"
    : "=&{v[80:95]}"(pd), [ea] "+&v"(ea), [da] "+&v"(da), [b] "+&s"(b), [lf] "+&s"(lf), "+{s[36:37]}"(p1), "+{s[38:39]}"(p0), [sa] "+&v"(sa), [st] "=&s"(st), [sl] "+&s"(sl), [p0] "+&s"(pf)
    : [rw] "v"(rw), [ab] "s"(ab), [cp] "s"(cp), [mu] "s"(mu), [ad] "v"(ad), "{v[128:159]}"(W[0]), "{v[160:191]}"(W[1]), "{v[192:223]}"(W[2]), "{v[224:255]}"(W[3]), [co0] "v"(co0), [w0] "s"(w0), [dv] "v"(dv), [db] "s"(db), [pg] "s"(pg), [po] "s"(po - 1), [p1] "s"(b + po), [nl] "s"(-LOG2E)
    : "v38", "v39", "v40", "v41", "v42", "v43", "v44", "v45", "v46", "v47", "v48", "v49", "v50", "v51", "v52", "v53", "v54", "v55", "v56", "v57", "v58", "v59", "v60", "v61", "v62", "v63", "v64", "v65", "v66", "v67", "v68", "v69", "v70", "v71", "v72", "v73", "v74", "v75", "v76", "v77", "v78", "v79", "v96", "v97", "v98", "v99", "v100", "v101", "v102", "v103", "v104", "v105", "v106", "v107", "v108", "v109", "v110", "v111", "v112", "v113", "v114", "v115", "v116", "v117", "v118", "v119", "v120", "v121", "v122", "v123", "v124", "v125", "v126", "v127", "vcc", "scc", "m0", "memory");
}
__device__ __forceinline__ void near_st2(const u32x32 (&W)[4], u32x16& pd, unsigned ea, unsigned da, int& b, int& lf, int cp, unsigned sa, unsigned mu, unsigned ad,
    unsigned rw, unsigned ab, const char* p1, const char* p0, unsigned co0, unsigned co1, int po, int pf, unsigned w0, unsigned sl, unsigned dv, const char* db, const char* pg) {
  unsigned st;
  asm volatile("s_waitcnt vmcnt(0)\n\t"
    "s_barrier\n\t"
    "ds_read_b128 v[96:99], %[ea] offset:0\n\t"
    "ds_read_b128 v[100:103], %[ea] offset:2048\n\t"
    "ds_read_b128 v[104:107], %[da] offset:0\n\t"
    "ds_read_b128 v[108:111], %[da] offset:2048\n\t"
    "ds_read_b128 v[112:115], %[ea] offset:4096\n\t"
    "ds_read_b128 v[116:119], %[ea] offset:6144\n\t"
    "s_branch 2f\n\t"
    "1:\n\t"
    "s_waitcnt vmcnt(0)\n\t"
    "s_barrier\n\t"
    "2:\n\t"
    "s_waitcnt lgkmcnt(4)\n\t"
    "v_mfma_f32_16x16x32_bf16 v[64:67], v[128:131], v[96:99], 0\n\t"
    "ds_read_b128 v[120:123], %[da] offset:4096\n\t"
    "v_mfma_f32_16x16x32_bf16 v[68:71], v[132:135], v[96:99], 0\n\t"
    "ds_read_b128 v[124:127], %[da] offset:6144\n\t"
    "v_mfma_f32_16x16x32_bf16 v[72:75], v[128:131], v[100:103], 0\n\t"
    "v_mfma_f32_16x16x32_bf16 v[76:79], v[132:135], v[100:103], 0\n\t"
    "s_cmp_eq_u32 %[w0], 0\n\t"
    "s_cbranch_scc1 5f\n\t"
    "6:\n\t"
    "s_waitcnt lgkmcnt(4)\n\t"
    "v_mfma_f32_16x16x32_bf16 v[64:67], v[136:139], v[104:107], v[64:67]\n\t"
    "ds_read_b128 v[96:99], %[ea] offset:8192\n\t"
    "v_mfma_f32_16x16x32_bf16 v[68:71], v[140:143], v[104:107], v[68:71]\n\t"
    "ds_read_b128 v[100:103], %[ea] offset:10240\n\t"
    "v_mfma_f32_16x16x32_bf16 v[72:75], v[136:139], v[108:111], v[72:75]\n\t"
    "v_mfma_f32_16x16x32_bf16 v[76:79], v[140:143], v[108:111], v[76:79]\n\t"
    "s_waitcnt lgkmcnt(4)\n\t"
    "v_mfma_f32_16x16x32_bf16 v[64:67], v[144:147], v[112:115], v[64:67]\n\t"
    "ds_read_b128 v[104:107], %[da] offset:8192\n\t"
    "v_mfma_f32_16x16x32_bf16 v[68:71], v[148:151], v[112:115], v[68:71]\n\t"
    "ds_read_b128 v[108:111], %[da] offset:10240\n\t"
    "v_mfma_f32_16x16x32_bf16 v[72:75], v[144:147], v[116:119], v[72:75]\n\t"
    "v_mfma_f32_16x16x32_bf16 v[76:79], v[148:151], v[116:119], v[76:79]\n\t"
    "s_barrier\n\t"
    "s_waitcnt lgkmcnt(4)\n\t"
    "v_mfma_f32_16x16x32_bf16 v[64:67], v[152:155], v[120:123], v[64:67]\n\t"
    "ds_read_b128 v[112:115], %[ea] offset:12288\n\t"
    "v_mfma_f32_16x16x32_bf16 v[68:71], v[156:159], v[120:123], v[68:71]\n\t"
    "ds_read_b128 v[116:119], %[ea] offset:14336\n\t"
    "v_mfma_f32_16x16x32_bf16 v[72:75], v[152:155], v[124:127], v[72:75]\n\t"
    "ds_read_b128 v[48:51], %[rw] offset:0\n\t"
    "v_mfma_f32_16x16x32_bf16 v[76:79], v[156:159], v[124:127], v[76:79]\n\t"
    "ds_read_b128 v[52:55], %[rw] offset:1024\n\t"
    "s_waitcnt lgkmcnt(6)\n\t"
    "v_mfma_f32_16x16x32_bf16 v[64:67], v[160:163], v[96:99], v[64:67]\n\t"
    "ds_read_b128 v[120:123], %[da] offset:12288\n\t"
    "v_mfma_f32_16x16x32_bf16 v[68:71], v[164:167], v[96:99], v[68:71]\n\t"
    "ds_read_b128 v[124:127], %[da] offset:14336\n\t"
    "v_mfma_f32_16x16x32_bf16 v[72:75], v[160:163], v[100:103], v[72:75]\n\t"
    "ds_read_b128 v[56:59], %[rw] offset:2048\n\t"
    "v_mfma_f32_16x16x32_bf16 v[76:79], v[164:167], v[100:103], v[76:79]\n\t"
    "ds_read_b128 v[60:63], %[rw] offset:3072\n\t"
    "s_waitcnt lgkmcnt(0)\n\t"
    "v_mfma_f32_16x16x32_bf16 v[64:67], v[168:171], v[104:107], v[64:67]\n\t"
    "ds_read_b128 v[96:99], %[ea] offset:16384\n\t"
    "v_cvt_pk_bf16_f32 v40, v80, v81\n\t"
    "v_mfma_f32_16x16x32_bf16 v[68:71], v[172:175], v[104:107], v[68:71]\n\t"
    "ds_read_b128 v[100:103], %[ea] offset:18432\n\t"
    "v_cvt_pk_bf16_f32 v41, v82, v83\n\t"
    "v_mfma_f32_16x16x32_bf16 v[72:75], v[168:171], v[108:111], v[72:75]\n\t"
    "v_cvt_pk_bf16_f32 v42, v84, v85\n\t"
    "v_mfma_f32_16x16x32_bf16 v[76:79], v[172:175], v[108:111], v[76:79]\n\t"
    "v_cvt_pk_bf16_f32 v43, v86, v87\n\t"
    "v_mfma_f32_16x16x32_bf16 v[64:67], v[176:179], v[112:115], v[64:67]\n\t"
    "ds_read_b128 v[104:107], %[da] offset:16384\n\t"
    "global_store_dwordx4 %[co0], v[40:43], s[36:37] sc1\n\t"
    "v_mfma_f32_16x16x32_bf16 v[68:71], v[180:183], v[112:115], v[68:71]\n\t"
    "ds_read_b128 v[108:111], %[da] offset:18432\n\t"
    "v_cvt_pk_bf16_f32 v44, v88, v89\n\t"
    "v_mfma_f32_16x16x32_bf16 v[72:75], v[176:179], v[116:119], v[72:75]\n\t"
    "v_cvt_pk_bf16_f32 v45, v90, v91\n\t"
    "v_mfma_f32_16x16x32_bf16 v[76:79], v[180:183], v[116:119], v[76:79]\n\t"
    "v_cvt_pk_bf16_f32 v46, v92, v93\n\t"
    "v_mfma_f32_16x16x32_bf16 v[64:67], v[184:187], v[120:123], v[64:67]\n\t"
    "ds_read_b128 v[112:115], %[ea] offset:20480\n\t"
    "v_cvt_pk_bf16_f32 v47, v94, v95\n\t"
    "v_mfma_f32_16x16x32_bf16 v[68:71], v[188:191], v[120:123], v[68:71]\n\t"
    "ds_read_b128 v[116:119], %[ea] offset:22528\n\t"
    "global_store_dwordx4 %[co1], v[44:47], s[36:37] sc1\n\t"
    "v_mfma_f32_16x16x32_bf16 v[72:75], v[184:187], v[124:127], v[72:75]\n\t"
    "v_cvt_pk_bf16_f32 v48, v48, v49\n\t"
    "v_mfma_f32_16x16x32_bf16 v[76:79], v[188:191], v[124:127], v[76:79]\n\t"
    "v_cvt_pk_bf16_f32 v49, v50, v51\n\t"
    "s_waitcnt lgkmcnt(4)\n\t"
    "v_mfma_f32_16x16x32_bf16 v[64:67], v[192:195], v[96:99], v[64:67]\n\t"
    "ds_read_b128 v[120:123], %[da] offset:20480\n\t"
    "v_cvt_pk_bf16_f32 v50, v52, v53\n\t"
    "v_mfma_f32_16x16x32_bf16 v[68:71], v[196:199], v[96:99], v[68:71]\n\t"
    "ds_read_b128 v[124:127], %[da] offset:22528\n\t"
    "v_cvt_pk_bf16_f32 v51, v54, v55\n\t"
    "v_mfma_f32_16x16x32_bf16 v[72:75], v[192:195], v[100:103], v[72:75]\n\t"
    "global_store_dwordx4 %[co0], v[48:51], s[36:37] offset:2048 sc1\n\t"
    "v_mfma_f32_16x16x32_bf16 v[76:79], v[196:199], v[100:103], v[76:79]\n\t"
    "v_cvt_pk_bf16_f32 v56, v56, v57\n\t"
    "s_waitcnt lgkmcnt(4)\n\t"
    "v_mfma_f32_16x16x32_bf16 v[64:67], v[200:203], v[104:107], v[64:67]\n\t"
    "ds_read_b128 v[96:99], %[ea] offset:24576\n\t"
    "v_cvt_pk_bf16_f32 v57, v58, v59\n\t"
    "v_mfma_f32_16x16x32_bf16 v[68:71], v[204:207], v[104:107], v[68:71]\n\t"
    "ds_read_b128 v[100:103], %[ea] offset:26624\n\t"
    "v_cvt_pk_bf16_f32 v58, v60, v61\n\t"
    "v_mfma_f32_16x16x32_bf16 v[72:75], v[200:203], v[108:111], v[72:75]\n\t"
    "v_cvt_pk_bf16_f32 v59, v62, v63\n\t"
    "v_mfma_f32_16x16x32_bf16 v[76:79], v[204:207], v[108:111], v[76:79]\n\t"
    "global_store_dwordx4 %[co1], v[56:59], s[36:37] offset:2048 sc1\n\t"
    "s_waitcnt lgkmcnt(4)\n\t"
    "v_mfma_f32_16x16x32_bf16 v[64:67], v[208:211], v[112:115], v[64:67]\n\t"
    "ds_read_b128 v[104:107], %[da] offset:24576\n\t"
    "v_mfma_f32_16x16x32_bf16 v[68:71], v[212:215], v[112:115], v[68:71]\n\t"
    "ds_read_b128 v[108:111], %[da] offset:26624\n\t"
    "v_mfma_f32_16x16x32_bf16 v[72:75], v[208:211], v[116:119], v[72:75]\n\t"
    "v_mfma_f32_16x16x32_bf16 v[76:79], v[212:215], v[116:119], v[76:79]\n\t"
    "s_waitcnt lgkmcnt(4)\n\t"
    "v_mfma_f32_16x16x32_bf16 v[64:67], v[216:219], v[120:123], v[64:67]\n\t"
    "ds_read_b128 v[112:115], %[ea] offset:28672\n\t"
    "v_mfma_f32_16x16x32_bf16 v[68:71], v[220:223], v[120:123], v[68:71]\n\t"
    "ds_read_b128 v[116:119], %[ea] offset:30720\n\t"
    "v_mfma_f32_16x16x32_bf16 v[72:75], v[216:219], v[124:127], v[72:75]\n\t"
    "v_xor_b32 %[ea], 0x10000, %[ea]\n\t"
    "v_mfma_f32_16x16x32_bf16 v[76:79], v[220:223], v[124:127], v[76:79]\n\t"
    "s_waitcnt vmcnt(4)\n\t"
    "s_barrier\n\t"
    "s_waitcnt lgkmcnt(4)\n\t"
    "v_mfma_f32_16x16x32_bf16 v[64:67], v[224:227], v[96:99], v[64:67]\n\t"
    "ds_read_b32 v39, %[sa]\n\t"
    "ds_read_b128 v[120:123], %[da] offset:28672\n\t"
    "v_mfma_f32_16x16x32_bf16 v[68:71], v[228:231], v[96:99], v[68:71]\n\t"
    "ds_read_b128 v[124:127], %[da] offset:30720\n\t"
    "v_mfma_f32_16x16x32_bf16 v[72:75], v[224:227], v[100:103], v[72:75]\n\t"
    "v_xor_b32 %[da], 0x10000, %[da]\n\t"
    "v_mfma_f32_16x16x32_bf16 v[76:79], v[228:231], v[100:103], v[76:79]\n\t"
    "s_waitcnt lgkmcnt(5)\n\t"
    "v_mfma_f32_16x16x32_bf16 v[64:67], v[232:235], v[104:107], v[64:67]\n\t"
    "ds_read_b128 v[96:99], %[ea] offset:0\n\t"
    "v_mfma_f32_16x16x32_bf16 v[68:71], v[236:239], v[104:107], v[68:71]\n\t"
    "ds_read_b128 v[100:103], %[ea] offset:2048\n\t"
    "v_mfma_f32_16x16x32_bf16 v[72:75], v[232:235], v[108:111], v[72:75]\n\t"
    "v_mfma_f32_16x16x32_bf16 v[76:79], v[236:239], v[108:111], v[76:79]\n\t"
    "s_waitcnt lgkmcnt(4)\n\t"
    "v_mfma_f32_16x16x32_bf16 v[64:67], v[240:243], v[112:115], v[64:67]\n\t"
    "ds_read_b128 v[104:107], %[da] offset:0\n\t"
    "v_mad_u32_u24 v39, v39, %[mu], %[ad]\n\t"
    "v_mfma_f32_16x16x32_bf16 v[68:71], v[244:247], v[112:115], v[68:71]\n\t"
    "ds_read_b128 v[108:111], %[da] offset:2048\n\t"
    "v_cmp_ge_i32 vcc, %[lf], v39\n\t"
    "s_cmp_eq_u64 vcc, 0\n\t"
    "s_addc_u32 %[lf], %[lf], 0\n\t"
    "v_mfma_f32_16x16x32_bf16 v[72:75], v[240:243], v[116:119], v[72:75]\n\t"
    "v_mfma_f32_16x16x32_bf16 v[76:79], v[244:247], v[116:119], v[76:79]\n\t"
    "v_cmp_ge_i32 vcc, %[lf], v39\n\t"
    "s_cmp_eq_u64 vcc, 0\n\t"
    "s_addc_u32 %[lf], %[lf], 0\n\t"
    "s_waitcnt lgkmcnt(4)\n\t"
    "v_mfma_f32_16x16x32_bf16 v[80:83], v[248:251], v[120:123], v[64:67]\n\t"
    "ds_read_b128 v[112:115], %[ea] offset:4096\n\t"
    "s_min_i32 %[lf], %[lf], %[cp]\n\t"
    "s_add_u32 %[b], %[b], 1\n\t"
    "v_mfma_f32_16x16x32_bf16 v[84:87], v[252:255], v[120:123], v[68:71]\n\t"
    "ds_read_b128 v[116:119], %[ea] offset:6144\n\t"
    "v_mfma_f32_16x16x32_bf16 v[88:91], v[248:251], v[124:127], v[72:75]\n\t"
    "v_mfma_f32_16x16x32_bf16 v[92:95], v[252:255], v[124:127], v[76:79]\n\t"
    "s_mov_b64 s[36:37], s[38:39]\n\t"
    "s_add_u32 s38, s38, %[ab]\n\t"
    "s_addc_u32 s39, s39, 0\n\t"
    "s_cmp_lt_i32 %[b], %[lf]\n\t"
    "s_cbranch_scc1 1b\n\t"
    "s_waitcnt lgkmcnt(0)\n\t"
    "s_nop 4\n\t"
    "s_branch 9f\n\t"
    "5:\n\t"
    "s_add_u32 %[st], %[b], %[po]\n\t"
    "s_max_i32 %[st], %[st], %[p0]\n\t"
    "v_mov_b32 v39, %[st]\n\t"
    "v_mov_b32 v38, 0\n\t"
    "s_mov_b64 exec, 1\n\t"
    "global_store_dword v38, v39, %[pg] sc1\n\t"
    "s_mov_b64 exec, -1\n\t"
    "s_mov_b32 %[p0], %[p1]\n\t"
    "s_cmp_ge_i32 %[lf], %[cp]\n\t"
    "s_cbranch_scc1 6b\n\t"
    "s_mov_b32 m0, %[sl]\n\t"
    "s_nop 0\n\t"
    "global_load_lds_dword %[dv], %[db] sc1\n\t"
    "s_branch 6b\n\t"
    "9:"
    : "=&{v[80:95]}"(pd), [ea] "+&v"(ea), [da] "+&v"(da), [b] "+&s"(b), [lf] "+&s"(lf), "+{s[36:37]}"(p1), "+{s[38:39]}"(p0), [sa] "+&v"(sa), [st] "=&s"(st), [sl] "+&s"(sl), [p0] "+&s"(pf)
    : [rw] "v"(rw), [ab] "s"(ab), [cp] "s"(cp), [mu] "s"(mu), [ad] "v"(ad), "{v[128:159]}"(W[0]), "{v[160:191]}"(W[1]), "{v[192:223]}"(W[2]), "{v[224:255]}"(W[3]), [co0] "v"(co0), [w0] "s"(w0), [dv] "v"(dv), [db] "s"(db), [pg] "s"(pg), [po] "s"(po - 1), [p1] "s"(b + po), [co1] "v"(co1)
    : "v38", "v39", "v40", "v41", "v42", "v43", "v44", "v45", "v46", "v47", "v48", "v49", "v50", "v51", "v52", "v53", "v54", "v55", "v56", "v57", "v58", "v59", "v60", "v61", "v62", "v63", "v64", "v65", "v66", "v67", "v68", "v69", "v70", "v71", "v72", "v73", "v74", "v75", "v76", "v77", "v78", "v79", "v96", "v97", "v98", "v99", "v100", "v101", "v102", "v103", "v104", "v105", "v106", "v107", "v108", "v109", "v110", "v111", "v112", "v113", "v114", "v115", "v116", "v117", "v118", "v119", "v120", "v121", "v122", "v123", "v124", "v125", "v126", "v127", "vcc", "scc", "m0", "memory");
}
__device__ __forceinline__ void lean_ld0(const u32x32 (&W)[4], u32x16& pd, unsigned ea, unsigned da, unsigned x, unsigned lb, const char* s0, const char* s1, const unsigned (&av)[8], unsigned sa, unsigned mu, unsigned ad, int th, unsigned s2, unsigned dv, const char* db) {
  asm volatile("s_setprio 2\n\t"
    "ds_read_b128 v[96:99], %[ea] offset:0\n\t"
    "ds_read_b128 v[100:103], %[ea] offset:2048\n\t"
    "ds_read_b128 v[104:107], %[da] offset:0\n\t"
    "ds_read_b128 v[108:111], %[da] offset:2048\n\t"
    "ds_read_b128 v[112:115], %[ea] offset:4096\n\t"
    "ds_read_b128 v[116:119], %[ea] offset:6144\n\t"
    "s_waitcnt lgkmcnt(4)\n\t"
    "v_mfma_f32_16x16x32_bf16 v[80:83], v[128:131], v[96:99], 0\n\t"
    "ds_read_b128 v[120:123], %[da] offset:4096\n\t"
    "v_mfma_f32_16x16x32_bf16 v[84:87], v[132:135], v[96:99], 0\n\t"
    "ds_read_b128 v[124:127], %[da] offset:6144\n\t"
    "v_mfma_f32_16x16x32_bf16 v[88:91], v[128:131], v[100:103], 0\n\t"
    "v_mfma_f32_16x16x32_bf16 v[92:95], v[132:135], v[100:103], 0\n\t"
    "s_waitcnt vmcnt(24)\n\t"
    "s_cmp_lt_u32 %[x], 2\n\t"
    "s_cbranch_scc0 41f\n\t"
    "s_waitcnt vmcnt(8)\n\t"
    "41:\n\t"
    "s_barrier\n\t"
    "s_waitcnt lgkmcnt(4)\n\t"
    "v_mfma_f32_16x16x32_bf16 v[80:83], v[136:139], v[104:107], v[80:83]\n\t"
    "ds_read_b128 v[96:99], %[ea] offset:8192\n\t"
    "v_mfma_f32_16x16x32_bf16 v[84:87], v[140:143], v[104:107], v[84:87]\n\t"
    "ds_read_b128 v[100:103], %[ea] offset:10240\n\t"
    "v_mfma_f32_16x16x32_bf16 v[88:91], v[136:139], v[108:111], v[88:91]\n\t"
    "v_mfma_f32_16x16x32_bf16 v[92:95], v[140:143], v[108:111], v[92:95]\n\t"
    "s_waitcnt lgkmcnt(4)\n\t"
    "v_mfma_f32_16x16x32_bf16 v[80:83], v[144:147], v[112:115], v[80:83]\n\t"
    "ds_read_b128 v[104:107], %[da] offset:8192\n\t"
    "v_mfma_f32_16x16x32_bf16 v[84:87], v[148:151], v[112:115], v[84:87]\n\t"
    "ds_read_b128 v[108:111], %[da] offset:10240\n\t"
    "v_mfma_f32_16x16x32_bf16 v[88:91], v[144:147], v[116:119], v[88:91]\n\t"
    "v_mfma_f32_16x16x32_bf16 v[92:95], v[148:151], v[116:119], v[92:95]\n\t"
    "s_waitcnt lgkmcnt(4)\n\t"
    "v_mfma_f32_16x16x32_bf16 v[80:83], v[152:155], v[120:123], v[80:83]\n\t"
    "ds_read_b128 v[112:115], %[ea] offset:12288\n\t"
    "v_mfma_f32_16x16x32_bf16 v[84:87], v[156:159], v[120:123], v[84:87]\n\t"
    "ds_read_b128 v[116:119], %[ea] offset:14336\n\t"
    "v_mfma_f32_16x16x32_bf16 v[88:91], v[152:155], v[124:127], v[88:91]\n\t"
    "v_mfma_f32_16x16x32_bf16 v[92:95], v[156:159], v[124:127], v[92:95]\n\t"
    "s_waitcnt lgkmcnt(4)\n\t"
    "v_mfma_f32_16x16x32_bf16 v[80:83], v[160:163], v[96:99], v[80:83]\n\t"
    "ds_read_b128 v[120:123], %[da] offset:12288\n\t"
    "v_mfma_f32_16x16x32_bf16 v[84:87], v[164:167], v[96:99], v[84:87]\n\t"
    "ds_read_b128 v[124:127], %[da] offset:14336\n\t"
    "v_mfma_f32_16x16x32_bf16 v[88:91], v[160:163], v[100:103], v[88:91]\n\t"
    "v_mfma_f32_16x16x32_bf16 v[92:95], v[164:167], v[100:103], v[92:95]\n\t"
    "s_waitcnt vmcnt(20)\n\t"
    "s_cmp_lt_u32 %[x], 2\n\t"
    "s_cbranch_scc0 45f\n\t"
    "s_waitcnt vmcnt(4)\n\t"
    "45:\n\t"
    "s_barrier\n\t"
    "s_waitcnt lgkmcnt(4)\n\t"
    "v_mfma_f32_16x16x32_bf16 v[80:83], v[168:171], v[104:107], v[80:83]\n\t"
    "ds_read_b128 v[96:99], %[ea] offset:16384\n\t"
    "v_mfma_f32_16x16x32_bf16 v[84:87], v[172:175], v[104:107], v[84:87]\n\t"
    "ds_read_b128 v[100:103], %[ea] offset:18432\n\t"
    "v_mfma_f32_16x16x32_bf16 v[88:91], v[168:171], v[108:111], v[88:91]\n\t"
    "v_mfma_f32_16x16x32_bf16 v[92:95], v[172:175], v[108:111], v[92:95]\n\t"
    "s_waitcnt lgkmcnt(4)\n\t"
    "v_mfma_f32_16x16x32_bf16 v[80:83], v[176:179], v[112:115], v[80:83]\n\t"
    "ds_read_b128 v[104:107], %[da] offset:16384\n\t"
    "v_mfma_f32_16x16x32_bf16 v[84:87], v[180:183], v[112:115], v[84:87]\n\t"
    "ds_read_b128 v[108:111], %[da] offset:18432\n\t"
    "v_mfma_f32_16x16x32_bf16 v[88:91], v[176:179], v[116:119], v[88:91]\n\t"
    "v_mfma_f32_16x16x32_bf16 v[92:95], v[180:183], v[116:119], v[92:95]\n\t"
    "s_waitcnt lgkmcnt(4)\n\t"
    "v_mfma_f32_16x16x32_bf16 v[80:83], v[184:187], v[120:123], v[80:83]\n\t"
    "ds_read_b128 v[112:115], %[ea] offset:20480\n\t"
    "v_mfma_f32_16x16x32_bf16 v[84:87], v[188:191], v[120:123], v[84:87]\n\t"
    "ds_read_b128 v[116:119], %[ea] offset:22528\n\t"
    "v_mfma_f32_16x16x32_bf16 v[88:91], v[184:187], v[124:127], v[88:91]\n\t"
    "v_mfma_f32_16x16x32_bf16 v[92:95], v[188:191], v[124:127], v[92:95]\n\t"
    "s_waitcnt lgkmcnt(4)\n\t"
    "v_mfma_f32_16x16x32_bf16 v[80:83], v[192:195], v[96:99], v[80:83]\n\t"
    "ds_read_b128 v[120:123], %[da] offset:20480\n\t"
    "v_mfma_f32_16x16x32_bf16 v[84:87], v[196:199], v[96:99], v[84:87]\n\t"
    "ds_read_b128 v[124:127], %[da] offset:22528\n\t"
    "v_mfma_f32_16x16x32_bf16 v[88:91], v[192:195], v[100:103], v[88:91]\n\t"
    "v_mfma_f32_16x16x32_bf16 v[92:95], v[196:199], v[100:103], v[92:95]\n\t"
    "s_waitcnt vmcnt(16)\n\t"
    "s_cmp_lt_u32 %[x], 2\n\t"
    "s_cbranch_scc0 49f\n\t"
    "s_waitcnt vmcnt(0)\n\t"
    "49:\n\t"
    "s_barrier\n\t"
    "ds_read_b32 v79, %[sa]\n\t"
    "s_waitcnt lgkmcnt(5)\n\t"
    "v_mfma_f32_16x16x32_bf16 v[80:83], v[200:203], v[104:107], v[80:83]\n\t"
    "ds_read_b128 v[96:99], %[ea] offset:24576\n\t"
    "v_mfma_f32_16x16x32_bf16 v[84:87], v[204:207], v[104:107], v[84:87]\n\t"
    "ds_read_b128 v[100:103], %[ea] offset:26624\n\t"
    "v_mfma_f32_16x16x32_bf16 v[88:91], v[200:203], v[108:111], v[88:91]\n\t"
    "v_mfma_f32_16x16x32_bf16 v[92:95], v[204:207], v[108:111], v[92:95]\n\t"
    "s_waitcnt lgkmcnt(5)\n\t"
    "v_mfma_f32_16x16x32_bf16 v[80:83], v[208:211], v[112:115], v[80:83]\n\t"
    "ds_read_b128 v[104:107], %[da] offset:24576\n\t"
    "v_mfma_f32_16x16x32_bf16 v[84:87], v[212:215], v[112:115], v[84:87]\n\t"
    "ds_read_b128 v[108:111], %[da] offset:26624\n\t"
    "v_mfma_f32_16x16x32_bf16 v[88:91], v[208:211], v[116:119], v[88:91]\n\t"
    "v_mfma_f32_16x16x32_bf16 v[92:95], v[212:215], v[116:119], v[92:95]\n\t"
    "s_cmp_eq_u32 %[s2], 0\n\t"
    "s_cbranch_scc1 7f\n\t"
    "s_mov_b32 m0, %[s2]\n\t"
    "s_nop 0\n\t"
    "global_load_lds_dword %[dv], %[db] sc1\n\t"
    "7:\n\t"
    "s_waitcnt lgkmcnt(4)\n\t"
    "v_mfma_f32_16x16x32_bf16 v[80:83], v[216:219], v[120:123], v[80:83]\n\t"
    "ds_read_b128 v[112:115], %[ea] offset:28672\n\t"
    "v_mfma_f32_16x16x32_bf16 v[84:87], v[220:223], v[120:123], v[84:87]\n\t"
    "ds_read_b128 v[116:119], %[ea] offset:30720\n\t"
    "v_mfma_f32_16x16x32_bf16 v[88:91], v[216:219], v[124:127], v[88:91]\n\t"
    "v_mad_u32_u24 v79, v79, %[mu], %[ad]\n\t"
    "v_cmp_ge_i32 vcc, %[th], v79\n\t"
    "v_mfma_f32_16x16x32_bf16 v[92:95], v[220:223], v[124:127], v[92:95]\n\t"
    "s_cbranch_vccz 5f\n\t"
    "s_waitcnt lgkmcnt(4)\n\t"
    "v_mfma_f32_16x16x32_bf16 v[80:83], v[224:227], v[96:99], v[80:83]\n\t"
    "ds_read_b128 v[120:123], %[da] offset:28672\n\t"
    "v_mfma_f32_16x16x32_bf16 v[84:87], v[228:231], v[96:99], v[84:87]\n\t"
    "ds_read_b128 v[124:127], %[da] offset:30720\n\t"
    "v_mfma_f32_16x16x32_bf16 v[88:91], v[224:227], v[100:103], v[88:91]\n\t"
    "v_mfma_f32_16x16x32_bf16 v[92:95], v[228:231], v[100:103], v[92:95]\n\t"
    "s_waitcnt lgkmcnt(4)\n\t"
    "v_mfma_f32_16x16x32_bf16 v[80:83], v[232:235], v[104:107], v[80:83]\n\t"
    "v_mfma_f32_16x16x32_bf16 v[84:87], v[236:239], v[104:107], v[84:87]\n\t"
    "v_mfma_f32_16x16x32_bf16 v[88:91], v[232:235], v[108:111], v[88:91]\n\t"
    "v_mfma_f32_16x16x32_bf16 v[92:95], v[236:239], v[108:111], v[92:95]\n\t"
    "s_waitcnt lgkmcnt(2)\n\t"
    "v_mfma_f32_16x16x32_bf16 v[80:83], v[240:243], v[112:115], v[80:83]\n\t"
    "v_mfma_f32_16x16x32_bf16 v[84:87], v[244:247], v[112:115], v[84:87]\n\t"
    "v_mfma_f32_16x16x32_bf16 v[88:91], v[240:243], v[116:119], v[88:91]\n\t"
    "v_mfma_f32_16x16x32_bf16 v[92:95], v[244:247], v[116:119], v[92:95]\n\t"
    "s_waitcnt lgkmcnt(0)\n\t"
    "v_mfma_f32_16x16x32_bf16 v[80:83], v[248:251], v[120:123], v[80:83]\n\t"
    "v_mfma_f32_16x16x32_bf16 v[84:87], v[252:255], v[120:123], v[84:87]\n\t"
    "v_mfma_f32_16x16x32_bf16 v[88:91], v[248:251], v[124:127], v[88:91]\n\t"
    "v_mfma_f32_16x16x32_bf16 v[92:95], v[252:255], v[124:127], v[92:95]\n\t"
    "s_branch 6f\n\t"
    "5:\n\t"
    "s_waitcnt lgkmcnt(4)\n\t"
    "v_mfma_f32_16x16x32_bf16 v[80:83], v[224:227], v[96:99], v[80:83]\n\t"
    "ds_read_b128 v[120:123], %[da] offset:28672\n\t"
    "s_add_u32 m0, %[lb], 0\n\t"
    "s_nop 0\n\t"
    "global_load_lds_dwordx4 %[av0], %[s0] sc1\n\t"
    "v_mfma_f32_16x16x32_bf16 v[84:87], v[228:231], v[96:99], v[84:87]\n\t"
    "ds_read_b128 v[124:127], %[da] offset:30720\n\t"
    "s_add_u32 m0, %[lb], 32768\n\t"
    "s_nop 0\n\t"
    "global_load_lds_dwordx4 %[av1], %[s0] sc1\n\t"
    "v_mfma_f32_16x16x32_bf16 v[88:91], v[224:227], v[100:103], v[88:91]\n\t"
    "s_add_u32 m0, %[lb], 4096\n\t"
    "s_nop 0\n\t"
    "global_load_lds_dwordx4 %[av2], %[s0] sc1\n\t"
    "v_mfma_f32_16x16x32_bf16 v[92:95], v[228:231], v[100:103], v[92:95]\n\t"
    "s_add_u32 m0, %[lb], 36864\n\t"
    "s_nop 0\n\t"
    "global_load_lds_dwordx4 %[av3], %[s0] sc1\n\t"
    "s_waitcnt lgkmcnt(4)\n\t"
    "v_mfma_f32_16x16x32_bf16 v[80:83], v[232:235], v[104:107], v[80:83]\n\t"
    "s_add_u32 m0, %[lb], 8192\n\t"
    "s_nop 0\n\t"
    "global_load_lds_dwordx4 %[av4], %[s0] sc1\n\t"
    "v_mfma_f32_16x16x32_bf16 v[84:87], v[236:239], v[104:107], v[84:87]\n\t"
    "s_add_u32 m0, %[lb], 40960\n\t"
    "s_nop 0\n\t"
    "global_load_lds_dwordx4 %[av5], %[s0] sc1\n\t"
    "v_mfma_f32_16x16x32_bf16 v[88:91], v[232:235], v[108:111], v[88:91]\n\t"
    "s_add_u32 m0, %[lb], 12288\n\t"
    "s_nop 0\n\t"
    "global_load_lds_dwordx4 %[av6], %[s0] sc1\n\t"
    "v_mfma_f32_16x16x32_bf16 v[92:95], v[236:239], v[108:111], v[92:95]\n\t"
    "s_add_u32 m0, %[lb], 45056\n\t"
    "s_nop 0\n\t"
    "global_load_lds_dwordx4 %[av7], %[s0] sc1\n\t"
    "s_waitcnt lgkmcnt(2)\n\t"
    "v_mfma_f32_16x16x32_bf16 v[80:83], v[240:243], v[112:115], v[80:83]\n\t"
    "s_add_u32 m0, %[lb], 16384\n\t"
    "s_nop 0\n\t"
    "global_load_lds_dwordx4 %[av0], %[s1] sc1\n\t"
    "v_mfma_f32_16x16x32_bf16 v[84:87], v[244:247], v[112:115], v[84:87]\n\t"
    "s_add_u32 m0, %[lb], 49152\n\t"
    "s_nop 0\n\t"
    "global_load_lds_dwordx4 %[av1], %[s1] sc1\n\t"
    "v_mfma_f32_16x16x32_bf16 v[88:91], v[240:243], v[116:119], v[88:91]\n\t"
    "s_add_u32 m0, %[lb], 20480\n\t"
    "s_nop 0\n\t"
    "global_load_lds_dwordx4 %[av2], %[s1] sc1\n\t"
    "v_mfma_f32_16x16x32_bf16 v[92:95], v[244:247], v[116:119], v[92:95]\n\t"
    "s_add_u32 m0, %[lb], 53248\n\t"
    "s_nop 0\n\t"
    "global_load_lds_dwordx4 %[av3], %[s1] sc1\n\t"
    "s_waitcnt lgkmcnt(0)\n\t"
    "v_mfma_f32_16x16x32_bf16 v[80:83], v[248:251], v[120:123], v[80:83]\n\t"
    "s_add_u32 m0, %[lb], 24576\n\t"
    "s_nop 0\n\t"
    "global_load_lds_dwordx4 %[av4], %[s1] sc1\n\t"
    "v_mfma_f32_16x16x32_bf16 v[84:87], v[252:255], v[120:123], v[84:87]\n\t"
    "s_add_u32 m0, %[lb], 57344\n\t"
    "s_nop 0\n\t"
    "global_load_lds_dwordx4 %[av5], %[s1] sc1\n\t"
    "v_mfma_f32_16x16x32_bf16 v[88:91], v[248:251], v[124:127], v[88:91]\n\t"
    "s_add_u32 m0, %[lb], 28672\n\t"
    "s_nop 0\n\t"
    "global_load_lds_dwordx4 %[av6], %[s1] sc1\n\t"
    "v_mfma_f32_16x16x32_bf16 v[92:95], v[252:255], v[124:127], v[92:95]\n\t"
    "s_add_u32 m0, %[lb], 61440\n\t"
    "s_nop 0\n\t"
    "global_load_lds_dwordx4 %[av7], %[s1] sc1\n\t"
    "6:\n\t"
    "s_nop 6\n\t"
    "s_branch 9f\n\t"
    "3:\n\t"
    "9:"
    : "=&{v[80:95]}"(pd)
    : [ea] "v"(ea), [da] "v"(da), [x] "s"(x), "{v[128:159]}"(W[0]), "{v[160:191]}"(W[1]), "{v[192:223]}"(W[2]), "{v[224:255]}"(W[3]), [lb] "s"(lb), [s0] "s"(s0), [s1] "s"(s1), [av0] "v"(av[0]), [av1] "v"(av[1]), [av2] "v"(av[2]), [av3] "v"(av[3]), [av4] "v"(av[4]), [av5] "v"(av[5]), [av6] "v"(av[6]), [av7] "v"(av[7]), [sa] "v"(sa), [mu] "s"(mu), [ad] "v"(ad), [th] "s"(th), [s2] "s"(s2), [dv] "v"(dv), [db] "s"(db)
    : "v96", "v97", "v98", "v99", "v100", "v101", "v102", "v103", "v104", "v105", "v106", "v107", "v108", "v109", "v110", "v111", "v112", "v113", "v114", "v115", "v116", "v117", "v118", "v119", "v120", "v121", "v122", "v123", "v124", "v125", "v126", "v127", "v79", "vcc", "scc", "m0", "memory");
}
__device__ __forceinline__ void lean_dm0(const u32x32 (&W)[4], u32x16& pd, unsigned ea, unsigned da, unsigned x, unsigned lb, const char* s0, const char* s1, const unsigned (&av)[8]) {
  asm volatile("s_setprio 2\n\t"
    "ds_read_b128 v[96:99], %[ea] offset:0\n\t"
    "ds_read_b128 v[100:103], %[ea] offset:2048\n\t"
    "ds_read_b128 v[104:107], %[da] offset:0\n\t"
    "ds_read_b128 v[108:111], %[da] offset:2048\n\t"
    "ds_read_b128 v[112:115], %[ea] offset:4096\n\t"
    "ds_read_b128 v[116:119], %[ea] offset:6144\n\t"
    "s_waitcnt lgkmcnt(4)\n\t"
    "v_mfma_f32_16x16x32_bf16 v[80:83], v[128:131], v[96:99], 0\n\t"
    "ds_read_b128 v[120:123], %[da] offset:4096\n\t"
    "s_add_u32 m0, %[lb], 0\n\t"
    "v_mfma_f32_16x16x32_bf16 v[84:87], v[132:135], v[96:99], 0\n\t"
    "ds_read_b128 v[124:127], %[da] offset:6144\n\t"
    "global_load_lds_dwordx4 %[av0], %[s0] sc1\n\t"
    "v_mfma_f32_16x16x32_bf16 v[88:91], v[128:131], v[100:103], 0\n\t"
    "s_add_u32 m0, %[lb], 32768\n\t"
    "v_mfma_f32_16x16x32_bf16 v[92:95], v[132:135], v[100:103], 0\n\t"
    "global_load_lds_dwordx4 %[av1], %[s0] sc1\n\t"
    "s_waitcnt vmcnt(10)\n\t"
    "s_barrier\n\t"
    "s_waitcnt lgkmcnt(4)\n\t"
    "v_mfma_f32_16x16x32_bf16 v[80:83], v[136:139], v[104:107], v[80:83]\n\t"
    "ds_read_b128 v[96:99], %[ea] offset:8192\n\t"
    "s_add_u32 m0, %[lb], 4096\n\t"
    "v_mfma_f32_16x16x32_bf16 v[84:87], v[140:143], v[104:107], v[84:87]\n\t"
    "ds_read_b128 v[100:103], %[ea] offset:10240\n\t"
    "global_load_lds_dwordx4 %[av2], %[s0] sc1\n\t"
    "v_mfma_f32_16x16x32_bf16 v[88:91], v[136:139], v[108:111], v[88:91]\n\t"
    "s_add_u32 m0, %[lb], 36864\n\t"
    "v_mfma_f32_16x16x32_bf16 v[92:95], v[140:143], v[108:111], v[92:95]\n\t"
    "global_load_lds_dwordx4 %[av3], %[s0] sc1\n\t"
    "s_waitcnt lgkmcnt(4)\n\t"
    "v_mfma_f32_16x16x32_bf16 v[80:83], v[144:147], v[112:115], v[80:83]\n\t"
    "ds_read_b128 v[104:107], %[da] offset:8192\n\t"
    "s_add_u32 m0, %[lb], 8192\n\t"
    "v_mfma_f32_16x16x32_bf16 v[84:87], v[148:151], v[112:115], v[84:87]\n\t"
    "ds_read_b128 v[108:111], %[da] offset:10240\n\t"
    "global_load_lds_dwordx4 %[av4], %[s0] sc1\n\t"
    "v_mfma_f32_16x16x32_bf16 v[88:91], v[144:147], v[116:119], v[88:91]\n\t"
    "s_add_u32 m0, %[lb], 40960\n\t"
    "v_mfma_f32_16x16x32_bf16 v[92:95], v[148:151], v[116:119], v[92:95]\n\t"
    "global_load_lds_dwordx4 %[av5], %[s0] sc1\n\t"
    "s_waitcnt lgkmcnt(4)\n\t"
    "v_mfma_f32_16x16x32_bf16 v[80:83], v[152:155], v[120:123], v[80:83]\n\t"
    "ds_read_b128 v[112:115], %[ea] offset:12288\n\t"
    "s_add_u32 m0, %[lb], 12288\n\t"
    "v_mfma_f32_16x16x32_bf16 v[84:87], v[156:159], v[120:123], v[84:87]\n\t"
    "ds_read_b128 v[116:119], %[ea] offset:14336\n\t"
    "global_load_lds_dwordx4 %[av6], %[s0] sc1\n\t"
    "v_mfma_f32_16x16x32_bf16 v[88:91], v[152:155], v[124:127], v[88:91]\n\t"
    "s_add_u32 m0, %[lb], 45056\n\t"
    "v_mfma_f32_16x16x32_bf16 v[92:95], v[156:159], v[124:127], v[92:95]\n\t"
    "global_load_lds_dwordx4 %[av7], %[s0] sc1\n\t"
    "s_waitcnt lgkmcnt(4)\n\t"
    "v_mfma_f32_16x16x32_bf16 v[80:83], v[160:163], v[96:99], v[80:83]\n\t"
    "ds_read_b128 v[120:123], %[da] offset:12288\n\t"
    "s_add_u32 m0, %[lb], 16384\n\t"
    "v_mfma_f32_16x16x32_bf16 v[84:87], v[164:167], v[96:99], v[84:87]\n\t"
    "ds_read_b128 v[124:127], %[da] offset:14336\n\t"
    "global_load_lds_dwordx4 %[av0], %[s1] sc1\n\t"
    "v_mfma_f32_16x16x32_bf16 v[88:91], v[160:163], v[100:103], v[88:91]\n\t"
    "s_add_u32 m0, %[lb], 49152\n\t"
    "v_mfma_f32_16x16x32_bf16 v[92:95], v[164:167], v[100:103], v[92:95]\n\t"
    "global_load_lds_dwordx4 %[av1], %[s1] sc1\n\t"
    "s_waitcnt vmcnt(14)\n\t"
    "s_barrier\n\t"
    "s_waitcnt lgkmcnt(4)\n\t"
    "v_mfma_f32_16x16x32_bf16 v[80:83], v[168:171], v[104:107], v[80:83]\n\t"
    "ds_read_b128 v[96:99], %[ea] offset:16384\n\t"
    "s_add_u32 m0, %[lb], 20480\n\t"
    "v_mfma_f32_16x16x32_bf16 v[84:87], v[172:175], v[104:107], v[84:87]\n\t"
    "ds_read_b128 v[100:103], %[ea] offset:18432\n\t"
    "global_load_lds_dwordx4 %[av2], %[s1] sc1\n\t"
    "v_mfma_f32_16x16x32_bf16 v[88:91], v[168:171], v[108:111], v[88:91]\n\t"
    "s_add_u32 m0, %[lb], 53248\n\t"
    "v_mfma_f32_16x16x32_bf16 v[92:95], v[172:175], v[108:111], v[92:95]\n\t"
    "global_load_lds_dwordx4 %[av3], %[s1] sc1\n\t"
    "s_waitcnt lgkmcnt(4)\n\t"
    "v_mfma_f32_16x16x32_bf16 v[80:83], v[176:179], v[112:115], v[80:83]\n\t"
    "ds_read_b128 v[104:107], %[da] offset:16384\n\t"
    "s_add_u32 m0, %[lb], 24576\n\t"
    "v_mfma_f32_16x16x32_bf16 v[84:87], v[180:183], v[112:115], v[84:87]\n\t"
    "ds_read_b128 v[108:111], %[da] offset:18432\n\t"
    "global_load_lds_dwordx4 %[av4], %[s1] sc1\n\t"
    "v_mfma_f32_16x16x32_bf16 v[88:91], v[176:179], v[116:119], v[88:91]\n\t"
    "s_add_u32 m0, %[lb], 57344\n\t"
    "v_mfma_f32_16x16x32_bf16 v[92:95], v[180:183], v[116:119], v[92:95]\n\t"
    "global_load_lds_dwordx4 %[av5], %[s1] sc1\n\t"
    "s_waitcnt lgkmcnt(4)\n\t"
    "v_mfma_f32_16x16x32_bf16 v[80:83], v[184:187], v[120:123], v[80:83]\n\t"
    "ds_read_b128 v[112:115], %[ea] offset:20480\n\t"
    "s_add_u32 m0, %[lb], 28672\n\t"
    "v_mfma_f32_16x16x32_bf16 v[84:87], v[188:191], v[120:123], v[84:87]\n\t"
    "ds_read_b128 v[116:119], %[ea] offset:22528\n\t"
    "global_load_lds_dwordx4 %[av6], %[s1] sc1\n\t"
    "v_mfma_f32_16x16x32_bf16 v[88:91], v[184:187], v[124:127], v[88:91]\n\t"
    "s_add_u32 m0, %[lb], 61440\n\t"
    "v_mfma_f32_16x16x32_bf16 v[92:95], v[188:191], v[124:127], v[92:95]\n\t"
    "global_load_lds_dwordx4 %[av7], %[s1] sc1\n\t"
    "s_waitcnt lgkmcnt(4)\n\t"
    "v_mfma_f32_16x16x32_bf16 v[80:83], v[192:195], v[96:99], v[80:83]\n\t"
    "ds_read_b128 v[120:123], %[da] offset:20480\n\t"
    "v_mfma_f32_16x16x32_bf16 v[84:87], v[196:199], v[96:99], v[84:87]\n\t"
    "ds_read_b128 v[124:127], %[da] offset:22528\n\t"
    "v_mfma_f32_16x16x32_bf16 v[88:91], v[192:195], v[100:103], v[88:91]\n\t"
    "v_mfma_f32_16x16x32_bf16 v[92:95], v[196:199], v[100:103], v[92:95]\n\t"
    "s_waitcnt vmcnt(16)\n\t"
    "s_barrier\n\t"
    "s_waitcnt lgkmcnt(4)\n\t"
    "v_mfma_f32_16x16x32_bf16 v[80:83], v[200:203], v[104:107], v[80:83]\n\t"
    "ds_read_b128 v[96:99], %[ea] offset:24576\n\t"
    "v_mfma_f32_16x16x32_bf16 v[84:87], v[204:207], v[104:107], v[84:87]\n\t"
    "ds_read_b128 v[100:103], %[ea] offset:26624\n\t"
    "v_mfma_f32_16x16x32_bf16 v[88:91], v[200:203], v[108:111], v[88:91]\n\t"
    "v_mfma_f32_16x16x32_bf16 v[92:95], v[204:207], v[108:111], v[92:95]\n\t"
    "s_waitcnt lgkmcnt(4)\n\t"
    "v_mfma_f32_16x16x32_bf16 v[80:83], v[208:211], v[112:115], v[80:83]\n\t"
    "ds_read_b128 v[104:107], %[da] offset:24576\n\t"
    "v_mfma_f32_16x16x32_bf16 v[84:87], v[212:215], v[112:115], v[84:87]\n\t"
    "ds_read_b128 v[108:111], %[da] offset:26624\n\t"
    "v_mfma_f32_16x16x32_bf16 v[88:91], v[208:211], v[116:119], v[88:91]\n\t"
    "v_mfma_f32_16x16x32_bf16 v[92:95], v[212:215], v[116:119], v[92:95]\n\t"
    "s_waitcnt lgkmcnt(4)\n\t"
    "v_mfma_f32_16x16x32_bf16 v[80:83], v[216:219], v[120:123], v[80:83]\n\t"
    "ds_read_b128 v[112:115], %[ea] offset:28672\n\t"
    "v_mfma_f32_16x16x32_bf16 v[84:87], v[220:223], v[120:123], v[84:87]\n\t"
    "ds_read_b128 v[116:119], %[ea] offset:30720\n\t"
    "v_mfma_f32_16x16x32_bf16 v[88:91], v[216:219], v[124:127], v[88:91]\n\t"
    "v_mfma_f32_16x16x32_bf16 v[92:95], v[220:223], v[124:127], v[92:95]\n\t"
    "s_waitcnt lgkmcnt(4)\n\t"
    "v_mfma_f32_16x16x32_bf16 v[80:83], v[224:227], v[96:99], v[80:83]\n\t"
    "ds_read_b128 v[120:123], %[da] offset:28672\n\t"
    "v_mfma_f32_16x16x32_bf16 v[84:87], v[228:231], v[96:99], v[84:87]\n\t"
    "ds_read_b128 v[124:127], %[da] offset:30720\n\t"
    "v_mfma_f32_16x16x32_bf16 v[88:91], v[224:227], v[100:103], v[88:91]\n\t"
    "v_mfma_f32_16x16x32_bf16 v[92:95], v[228:231], v[100:103], v[92:95]\n\t"
    "s_waitcnt lgkmcnt(4)\n\t"
    "v_mfma_f32_16x16x32_bf16 v[80:83], v[232:235], v[104:107], v[80:83]\n\t"
    "v_mfma_f32_16x16x32_bf16 v[84:87], v[236:239], v[104:107], v[84:87]\n\t"
    "v_mfma_f32_16x16x32_bf16 v[88:91], v[232:235], v[108:111], v[88:91]\n\t"
    "v_mfma_f32_16x16x32_bf16 v[92:95], v[236:239], v[108:111], v[92:95]\n\t"
    "s_waitcnt lgkmcnt(2)\n\t"
    "v_mfma_f32_16x16x32_bf16 v[80:83], v[240:243], v[112:115], v[80:83]\n\t"
    "v_mfma_f32_16x16x32_bf16 v[84:87], v[244:247], v[112:115], v[84:87]\n\t"
    "v_mfma_f32_16x16x32_bf16 v[88:91], v[240:243], v[116:119], v[88:91]\n\t"
    "v_mfma_f32_16x16x32_bf16 v[92:95], v[244:247], v[116:119], v[92:95]\n\t"
    "s_waitcnt lgkmcnt(0)\n\t"
    "v_mfma_f32_16x16x32_bf16 v[80:83], v[248:251], v[120:123], v[80:83]\n\t"
    "v_mfma_f32_16x16x32_bf16 v[84:87], v[252:255], v[120:123], v[84:87]\n\t"
    "v_mfma_f32_16x16x32_bf16 v[88:91], v[248:251], v[124:127], v[88:91]\n\t"
    "v_mfma_f32_16x16x32_bf16 v[92:95], v[252:255], v[124:127], v[92:95]\n\t"
    "s_nop 6\n\t"
    "s_branch 9f\n\t"
    "3:\n\t"
    "9:"
    : "=&{v[80:95]}"(pd)
    : [ea] "v"(ea), [da] "v"(da), [x] "s"(x), "{v[128:159]}"(W[0]), "{v[160:191]}"(W[1]), "{v[192:223]}"(W[2]), "{v[224:255]}"(W[3]), [lb] "s"(lb), [s0] "s"(s0), [s1] "s"(s1), [av0] "v"(av[0]), [av1] "v"(av[1]), [av2] "v"(av[2]), [av3] "v"(av[3]), [av4] "v"(av[4]), [av5] "v"(av[5]), [av6] "v"(av[6]), [av7] "v"(av[7])
    : "v96", "v97", "v98", "v99", "v100", "v101", "v102", "v103", "v104", "v105", "v106", "v107", "v108", "v109", "v110", "v111", "v112", "v113", "v114", "v115", "v116", "v117", "v118", "v119", "v120", "v121", "v122", "v123", "v124", "v125", "v126", "v127", "vcc", "scc", "m0", "memory");
}
__device__ __forceinline__ void lean_ld1(const u32x32 (&W)[4], u32x16& pd, unsigned ea, unsigned da, unsigned x, unsigned lb, const char* s0, const char* s1, const unsigned (&av)[8], unsigned sa, unsigned mu, unsigned ad, int th, unsigned s2, unsigned dv, const char* db) {
  asm volatile("s_setprio 2\n\t"
    "ds_read_b128 v[96:99], %[ea] offset:0\n\t"
    "ds_read_b128 v[100:103], %[ea] offset:2048\n\t"
    "ds_read_b128 v[104:107], %[da] offset:0\n\t"
    "ds_read_b128 v[108:111], %[da] offset:2048\n\t"
    "ds_read_b128 v[112:115], %[ea] offset:4096\n\t"
    "ds_read_b128 v[116:119], %[ea] offset:6144\n\t"
    "s_waitcnt lgkmcnt(4)\n\t"
    "v_mfma_f32_16x16x32_bf16 v[80:83], v[128:131], v[96:99], 0\n\t"
    "ds_read_b128 v[120:123], %[da] offset:4096\n\t"
    "v_mfma_f32_16x16x32_bf16 v[84:87], v[132:135], v[96:99], 0\n\t"
    "ds_read_b128 v[124:127], %[da] offset:6144\n\t"
    "v_mfma_f32_16x16x32_bf16 v[88:91], v[128:131], v[100:103], 0\n\t"
    "v_mfma_f32_16x16x32_bf16 v[92:95], v[132:135], v[100:103], 0\n\t"
    "s_waitcnt vmcnt(12)\n\t"
    "s_cmp_lt_u32 %[x], 2\n\t"
    "s_cbranch_scc0 41f\n\t"
    "s_waitcnt vmcnt(4)\n\t"
    "41:\n\t"
    "s_barrier\n\t"
    "s_waitcnt lgkmcnt(4)\n\t"
    "v_mfma_f32_16x16x32_bf16 v[80:83], v[136:139], v[104:107], v[80:83]\n\t"
    "ds_read_b128 v[96:99], %[ea] offset:8192\n\t"
    "v_mfma_f32_16x16x32_bf16 v[84:87], v[140:143], v[104:107], v[84:87]\n\t"
    "ds_read_b128 v[100:103], %[ea] offset:10240\n\t"
    "v_mfma_f32_16x16x32_bf16 v[88:91], v[136:139], v[108:111], v[88:91]\n\t"
    "v_mfma_f32_16x16x32_bf16 v[92:95], v[140:143], v[108:111], v[92:95]\n\t"
    "s_waitcnt lgkmcnt(4)\n\t"
    "v_mfma_f32_16x16x32_bf16 v[80:83], v[144:147], v[112:115], v[80:83]\n\t"
    "ds_read_b128 v[104:107], %[da] offset:8192\n\t"
    "v_mfma_f32_16x16x32_bf16 v[84:87], v[148:151], v[112:115], v[84:87]\n\t"
    "ds_read_b128 v[108:111], %[da] offset:10240\n\t"
    "v_mfma_f32_16x16x32_bf16 v[88:91], v[144:147], v[116:119], v[88:91]\n\t"
    "v_mfma_f32_16x16x32_bf16 v[92:95], v[148:151], v[116:119], v[92:95]\n\t"
    "s_waitcnt lgkmcnt(4)\n\t"
    "v_mfma_f32_16x16x32_bf16 v[80:83], v[152:155], v[120:123], v[80:83]\n\t"
    "ds_read_b128 v[112:115], %[ea] offset:12288\n\t"
    "v_mfma_f32_16x16x32_bf16 v[84:87], v[156:159], v[120:123], v[84:87]\n\t"
    "ds_read_b128 v[116:119], %[ea] offset:14336\n\t"
    "v_mfma_f32_16x16x32_bf16 v[88:91], v[152:155], v[124:127], v[88:91]\n\t"
    "v_mfma_f32_16x16x32_bf16 v[92:95], v[156:159], v[124:127], v[92:95]\n\t"
    "s_waitcnt lgkmcnt(4)\n\t"
    "v_mfma_f32_16x16x32_bf16 v[80:83], v[160:163], v[96:99], v[80:83]\n\t"
    "ds_read_b128 v[120:123], %[da] offset:12288\n\t"
    "v_mfma_f32_16x16x32_bf16 v[84:87], v[164:167], v[96:99], v[84:87]\n\t"
    "ds_read_b128 v[124:127], %[da] offset:14336\n\t"
    "v_mfma_f32_16x16x32_bf16 v[88:91], v[160:163], v[100:103], v[88:91]\n\t"
    "v_mfma_f32_16x16x32_bf16 v[92:95], v[164:167], v[100:103], v[92:95]\n\t"
    "s_waitcnt vmcnt(10)\n\t"
    "s_cmp_lt_u32 %[x], 2\n\t"
    "s_cbranch_scc0 45f\n\t"
    "s_waitcnt vmcnt(2)\n\t"
    "45:\n\t"
    "s_barrier\n\t"
    "s_waitcnt lgkmcnt(4)\n\t"
    "v_mfma_f32_16x16x32_bf16 v[80:83], v[168:171], v[104:107], v[80:83]\n\t"
    "ds_read_b128 v[96:99], %[ea] offset:16384\n\t"
    "v_mfma_f32_16x16x32_bf16 v[84:87], v[172:175], v[104:107], v[84:87]\n\t"
    "ds_read_b128 v[100:103], %[ea] offset:18432\n\t"
    "v_mfma_f32_16x16x32_bf16 v[88:91], v[168:171], v[108:111], v[88:91]\n\t"
    "v_mfma_f32_16x16x32_bf16 v[92:95], v[172:175], v[108:111], v[92:95]\n\t"
    "s_waitcnt lgkmcnt(4)\n\t"
    "v_mfma_f32_16x16x32_bf16 v[80:83], v[176:179], v[112:115], v[80:83]\n\t"
    "ds_read_b128 v[104:107], %[da] offset:16384\n\t"
    "v_mfma_f32_16x16x32_bf16 v[84:87], v[180:183], v[112:115], v[84:87]\n\t"
    "ds_read_b128 v[108:111], %[da] offset:18432\n\t"
    "v_mfma_f32_16x16x32_bf16 v[88:91], v[176:179], v[116:119], v[88:91]\n\t"
    "v_mfma_f32_16x16x32_bf16 v[92:95], v[180:183], v[116:119], v[92:95]\n\t"
    "s_waitcnt lgkmcnt(4)\n\t"
    "v_mfma_f32_16x16x32_bf16 v[80:83], v[184:187], v[120:123], v[80:83]\n\t"
    "ds_read_b128 v[112:115], %[ea] offset:20480\n\t"
    "v_mfma_f32_16x16x32_bf16 v[84:87], v[188:191], v[120:123], v[84:87]\n\t"
    "ds_read_b128 v[116:119], %[ea] offset:22528\n\t"
    "v_mfma_f32_16x16x32_bf16 v[88:91], v[184:187], v[124:127], v[88:91]\n\t"
    "v_mfma_f32_16x16x32_bf16 v[92:95], v[188:191], v[124:127], v[92:95]\n\t"
    "s_waitcnt lgkmcnt(4)\n\t"
    "v_mfma_f32_16x16x32_bf16 v[80:83], v[192:195], v[96:99], v[80:83]\n\t"
    "ds_read_b128 v[120:123], %[da] offset:20480\n\t"
    "v_mfma_f32_16x16x32_bf16 v[84:87], v[196:199], v[96:99], v[84:87]\n\t"
    "ds_read_b128 v[124:127], %[da] offset:22528\n\t"
    "v_mfma_f32_16x16x32_bf16 v[88:91], v[192:195], v[100:103], v[88:91]\n\t"
    "v_mfma_f32_16x16x32_bf16 v[92:95], v[196:199], v[100:103], v[92:95]\n\t"
    "s_waitcnt vmcnt(8)\n\t"
    "s_cmp_lt_u32 %[x], 2\n\t"
    "s_cbranch_scc0 49f\n\t"
    "s_waitcnt vmcnt(0)\n\t"
    "49:\n\t"
    "s_barrier\n\t"
    "ds_read_b32 v79, %[sa]\n\t"
    "s_waitcnt lgkmcnt(5)\n\t"
    "v_mfma_f32_16x16x32_bf16 v[80:83], v[200:203], v[104:107], v[80:83]\n\t"
    "ds_read_b128 v[96:99], %[ea] offset:24576\n\t"
    "v_mfma_f32_16x16x32_bf16 v[84:87], v[204:207], v[104:107], v[84:87]\n\t"
    "ds_read_b128 v[100:103], %[ea] offset:26624\n\t"
    "v_mfma_f32_16x16x32_bf16 v[88:91], v[200:203], v[108:111], v[88:91]\n\t"
    "v_mfma_f32_16x16x32_bf16 v[92:95], v[204:207], v[108:111], v[92:95]\n\t"
    "s_waitcnt lgkmcnt(5)\n\t"
    "v_mfma_f32_16x16x32_bf16 v[80:83], v[208:211], v[112:115], v[80:83]\n\t"
    "ds_read_b128 v[104:107], %[da] offset:24576\n\t"
    "v_mfma_f32_16x16x32_bf16 v[84:87], v[212:215], v[112:115], v[84:87]\n\t"
    "ds_read_b128 v[108:111], %[da] offset:26624\n\t"
    "v_mfma_f32_16x16x32_bf16 v[88:91], v[208:211], v[116:119], v[88:91]\n\t"
    "v_mfma_f32_16x16x32_bf16 v[92:95], v[212:215], v[116:119], v[92:95]\n\t"
    "s_cmp_eq_u32 %[s2], 0\n\t"
    "s_cbranch_scc1 7f\n\t"
    "s_mov_b32 m0, %[s2]\n\t"
    "s_nop 0\n\t"
    "global_load_lds_dword %[dv], %[db] sc1\n\t"
    "7:\n\t"
    "s_waitcnt lgkmcnt(4)\n\t"
    "v_mfma_f32_16x16x32_bf16 v[80:83], v[216:219], v[120:123], v[80:83]\n\t"
    "ds_read_b128 v[112:115], %[ea] offset:28672\n\t"
    "v_mfma_f32_16x16x32_bf16 v[84:87], v[220:223], v[120:123], v[84:87]\n\t"
    "ds_read_b128 v[116:119], %[ea] offset:30720\n\t"
    "v_mfma_f32_16x16x32_bf16 v[88:91], v[216:219], v[124:127], v[88:91]\n\t"
    "v_mad_u32_u24 v79, v79, %[mu], %[ad]\n\t"
    "v_cmp_ge_i32 vcc, %[th], v79\n\t"
    "v_mfma_f32_16x16x32_bf16 v[92:95], v[220:223], v[124:127], v[92:95]\n\t"
    "s_cbranch_vccz 5f\n\t"
    "s_waitcnt lgkmcnt(4)\n\t"
    "v_mfma_f32_16x16x32_bf16 v[80:83], v[224:227], v[96:99], v[80:83]\n\t"
    "ds_read_b128 v[120:123], %[da] offset:28672\n\t"
    "v_mfma_f32_16x16x32_bf16 v[84:87], v[228:231], v[96:99], v[84:87]\n\t"
    "ds_read_b128 v[124:127], %[da] offset:30720\n\t"
    "v_mfma_f32_16x16x32_bf16 v[88:91], v[224:227], v[100:103], v[88:91]\n\t"
    "v_mfma_f32_16x16x32_bf16 v[92:95], v[228:231], v[100:103], v[92:95]\n\t"
    "s_waitcnt lgkmcnt(4)\n\t"
    "v_mfma_f32_16x16x32_bf16 v[80:83], v[232:235], v[104:107], v[80:83]\n\t"
    "v_mfma_f32_16x16x32_bf16 v[84:87], v[236:239], v[104:107], v[84:87]\n\t"
    "v_mfma_f32_16x16x32_bf16 v[88:91], v[232:235], v[108:111], v[88:91]\n\t"
    "v_mfma_f32_16x16x32_bf16 v[92:95], v[236:239], v[108:111], v[92:95]\n\t"
    "s_waitcnt lgkmcnt(2)\n\t"
    "v_mfma_f32_16x16x32_bf16 v[80:83], v[240:243], v[112:115], v[80:83]\n\t"
    "v_mfma_f32_16x16x32_bf16 v[84:87], v[244:247], v[112:115], v[84:87]\n\t"
    "v_mfma_f32_16x16x32_bf16 v[88:91], v[240:243], v[116:119], v[88:91]\n\t"
    "v_mfma_f32_16x16x32_bf16 v[92:95], v[244:247], v[116:119], v[92:95]\n\t"
    "s_waitcnt lgkmcnt(0)\n\t"
    "v_mfma_f32_16x16x32_bf16 v[80:83], v[248:251], v[120:123], v[80:83]\n\t"
    "v_mfma_f32_16x16x32_bf16 v[84:87], v[252:255], v[120:123], v[84:87]\n\t"
    "v_mfma_f32_16x16x32_bf16 v[88:91], v[248:251], v[124:127], v[88:91]\n\t"
    "v_mfma_f32_16x16x32_bf16 v[92:95], v[252:255], v[124:127], v[92:95]\n\t"
    "s_branch 6f\n\t"
    "5:\n\t"
    "s_waitcnt lgkmcnt(4)\n\t"
    "v_mfma_f32_16x16x32_bf16 v[80:83], v[224:227], v[96:99], v[80:83]\n\t"
    "ds_read_b128 v[120:123], %[da] offset:28672\n\t"
    "s_add_u32 m0, %[lb], 0\n\t"
    "s_nop 0\n\t"
    "global_load_lds_dwordx4 %[av0], %[s0] sc1\n\t"
    "v_mfma_f32_16x16x32_bf16 v[84:87], v[228:231], v[96:99], v[84:87]\n\t"
    "ds_read_b128 v[124:127], %[da] offset:30720\n\t"
    "v_mfma_f32_16x16x32_bf16 v[88:91], v[224:227], v[100:103], v[88:91]\n\t"
    "s_add_u32 m0, %[lb], 4096\n\t"
    "s_nop 0\n\t"
    "global_load_lds_dwordx4 %[av2], %[s0] sc1\n\t"
    "v_mfma_f32_16x16x32_bf16 v[92:95], v[228:231], v[100:103], v[92:95]\n\t"
    "s_waitcnt lgkmcnt(4)\n\t"
    "v_mfma_f32_16x16x32_bf16 v[80:83], v[232:235], v[104:107], v[80:83]\n\t"
    "s_add_u32 m0, %[lb], 8192\n\t"
    "s_nop 0\n\t"
    "global_load_lds_dwordx4 %[av4], %[s0] sc1\n\t"
    "v_mfma_f32_16x16x32_bf16 v[84:87], v[236:239], v[104:107], v[84:87]\n\t"
    "v_mfma_f32_16x16x32_bf16 v[88:91], v[232:235], v[108:111], v[88:91]\n\t"
    "s_add_u32 m0, %[lb], 12288\n\t"
    "s_nop 0\n\t"
    "global_load_lds_dwordx4 %[av6], %[s0] sc1\n\t"
    "v_mfma_f32_16x16x32_bf16 v[92:95], v[236:239], v[108:111], v[92:95]\n\t"
    "s_waitcnt lgkmcnt(2)\n\t"
    "v_mfma_f32_16x16x32_bf16 v[80:83], v[240:243], v[112:115], v[80:83]\n\t"
    "s_add_u32 m0, %[lb], 16384\n\t"
    "s_nop 0\n\t"
    "global_load_lds_dwordx4 %[av0], %[s1] sc1\n\t"
    "v_mfma_f32_16x16x32_bf16 v[84:87], v[244:247], v[112:115], v[84:87]\n\t"
    "v_mfma_f32_16x16x32_bf16 v[88:91], v[240:243], v[116:119], v[88:91]\n\t"
    "s_add_u32 m0, %[lb], 20480\n\t"
    "s_nop 0\n\t"
    "global_load_lds_dwordx4 %[av2], %[s1] sc1\n\t"
    "v_mfma_f32_16x16x32_bf16 v[92:95], v[244:247], v[116:119], v[92:95]\n\t"
    "s_waitcnt lgkmcnt(0)\n\t"
    "v_mfma_f32_16x16x32_bf16 v[80:83], v[248:251], v[120:123], v[80:83]\n\t"
    "s_add_u32 m0, %[lb], 24576\n\t"
    "s_nop 0\n\t"
    "global_load_lds_dwordx4 %[av4], %[s1] sc1\n\t"
    "v_mfma_f32_16x16x32_bf16 v[84:87], v[252:255], v[120:123], v[84:87]\n\t"
    "v_mfma_f32_16x16x32_bf16 v[88:91], v[248:251], v[124:127], v[88:91]\n\t"
    "s_add_u32 m0, %[lb], 28672\n\t"
    "s_nop 0\n\t"
    "global_load_lds_dwordx4 %[av6], %[s1] sc1\n\t"
    "v_mfma_f32_16x16x32_bf16 v[92:95], v[252:255], v[124:127], v[92:95]\n\t"
    "6:\n\t"
    "s_nop 6\n\t"
    "s_branch 9f\n\t"
    "3:\n\t"
    "9:"
    : "=&{v[80:95]}"(pd)
    : [ea] "v"(ea), [da] "v"(da), [x] "s"(x), "{v[128:159]}"(W[0]), "{v[160:191]}"(W[1]), "{v[192:223]}"(W[2]), "{v[224:255]}"(W[3]), [lb] "s"(lb), [s0] "s"(s0), [s1] "s"(s1), [av0] "v"(av[0]), [av1] "v"(av[1]), [av2] "v"(av[2]), [av3] "v"(av[3]), [av4] "v"(av[4]), [av5] "v"(av[5]), [av6] "v"(av[6]), [av7] "v"(av[7]), [sa] "v"(sa), [mu] "s"(mu), [ad] "v"(ad), [th] "s"(th), [s2] "s"(s2), [dv] "v"(dv), [db] "s"(db)
    : "v96", "v97", "v98", "v99", "v100", "v101", "v102", "v103", "v104", "v105", "v106", "v107", "v108", "v109", "v110", "v111", "v112", "v113", "v114", "v115", "v116", "v117", "v118", "v119", "v120", "v121", "v122", "v123", "v124", "v125", "v126", "v127", "v79", "vcc", "scc", "m0", "memory");
}
__device__ __forceinline__ void lean_dm1(const u32x32 (&W)[4], u32x16& pd, unsigned ea, unsigned da, unsigned x, unsigned lb, const char* s0, const char* s1, const unsigned (&av)[8]) {
  asm volatile("s_setprio 2\n\t"
    "ds_read_b128 v[96:99], %[ea] offset:0\n\t"
    "ds_read_b128 v[100:103], %[ea] offset:2048\n\t"
    "ds_read_b128 v[104:107], %[da] offset:0\n\t"
    "ds_read_b128 v[108:111], %[da] offset:2048\n\t"
    "ds_read_b128 v[112:115], %[ea] offset:4096\n\t"
    "ds_read_b128 v[116:119], %[ea] offset:6144\n\t"
    "s_waitcnt lgkmcnt(4)\n\t"
    "v_mfma_f32_16x16x32_bf16 v[80:83], v[128:131], v[96:99], 0\n\t"
    "ds_read_b128 v[120:123], %[da] offset:4096\n\t"
    "s_add_u32 m0, %[lb], 0\n\t"
    "v_mfma_f32_16x16x32_bf16 v[84:87], v[132:135], v[96:99], 0\n\t"
    "ds_read_b128 v[124:127], %[da] offset:6144\n\t"
    "global_load_lds_dwordx4 %[av0], %[s0] sc1\n\t"
    "v_mfma_f32_16x16x32_bf16 v[88:91], v[128:131], v[100:103], 0\n\t"
    "v_mfma_f32_16x16x32_bf16 v[92:95], v[132:135], v[100:103], 0\n\t"
    "s_waitcnt vmcnt(5)\n\t"
    "s_barrier\n\t"
    "s_waitcnt lgkmcnt(4)\n\t"
    "v_mfma_f32_16x16x32_bf16 v[80:83], v[136:139], v[104:107], v[80:83]\n\t"
    "ds_read_b128 v[96:99], %[ea] offset:8192\n\t"
    "s_add_u32 m0, %[lb], 4096\n\t"
    "v_mfma_f32_16x16x32_bf16 v[84:87], v[140:143], v[104:107], v[84:87]\n\t"
    "ds_read_b128 v[100:103], %[ea] offset:10240\n\t"
    "global_load_lds_dwordx4 %[av2], %[s0] sc1\n\t"
    "v_mfma_f32_16x16x32_bf16 v[88:91], v[136:139], v[108:111], v[88:91]\n\t"
    "v_mfma_f32_16x16x32_bf16 v[92:95], v[140:143], v[108:111], v[92:95]\n\t"
    "s_waitcnt lgkmcnt(4)\n\t"
    "v_mfma_f32_16x16x32_bf16 v[80:83], v[144:147], v[112:115], v[80:83]\n\t"
    "ds_read_b128 v[104:107], %[da] offset:8192\n\t"
    "s_add_u32 m0, %[lb], 8192\n\t"
    "v_mfma_f32_16x16x32_bf16 v[84:87], v[148:151], v[112:115], v[84:87]\n\t"
    "ds_read_b128 v[108:111], %[da] offset:10240\n\t"
    "global_load_lds_dwordx4 %[av4], %[s0] sc1\n\t"
    "v_mfma_f32_16x16x32_bf16 v[88:91], v[144:147], v[116:119], v[88:91]\n\t"
    "v_mfma_f32_16x16x32_bf16 v[92:95], v[148:151], v[116:119], v[92:95]\n\t"
    "s_waitcnt lgkmcnt(4)\n\t"
    "v_mfma_f32_16x16x32_bf16 v[80:83], v[152:155], v[120:123], v[80:83]\n\t"
    "ds_read_b128 v[112:115], %[ea] offset:12288\n\t"
    "s_add_u32 m0, %[lb], 12288\n\t"
    "v_mfma_f32_16x16x32_bf16 v[84:87], v[156:159], v[120:123], v[84:87]\n\t"
    "ds_read_b128 v[116:119], %[ea] offset:14336\n\t"
    "global_load_lds_dwordx4 %[av6], %[s0] sc1\n\t"
    "v_mfma_f32_16x16x32_bf16 v[88:91], v[152:155], v[124:127], v[88:91]\n\t"
    "v_mfma_f32_16x16x32_bf16 v[92:95], v[156:159], v[124:127], v[92:95]\n\t"
    "s_waitcnt lgkmcnt(4)\n\t"
    "v_mfma_f32_16x16x32_bf16 v[80:83], v[160:163], v[96:99], v[80:83]\n\t"
    "ds_read_b128 v[120:123], %[da] offset:12288\n\t"
    "s_add_u32 m0, %[lb], 16384\n\t"
    "v_mfma_f32_16x16x32_bf16 v[84:87], v[164:167], v[96:99], v[84:87]\n\t"
    "ds_read_b128 v[124:127], %[da] offset:14336\n\t"
    "global_load_lds_dwordx4 %[av0], %[s1] sc1\n\t"
    "v_mfma_f32_16x16x32_bf16 v[88:91], v[160:163], v[100:103], v[88:91]\n\t"
    "v_mfma_f32_16x16x32_bf16 v[92:95], v[164:167], v[100:103], v[92:95]\n\t"
    "s_waitcnt vmcnt(7)\n\t"
    "s_barrier\n\t"
    "s_waitcnt lgkmcnt(4)\n\t"
    "v_mfma_f32_16x16x32_bf16 v[80:83], v[168:171], v[104:107], v[80:83]\n\t"
    "ds_read_b128 v[96:99], %[ea] offset:16384\n\t"
    "s_add_u32 m0, %[lb], 20480\n\t"
    "v_mfma_f32_16x16x32_bf16 v[84:87], v[172:175], v[104:107], v[84:87]\n\t"
    "ds_read_b128 v[100:103], %[ea] offset:18432\n\t"
    "global_load_lds_dwordx4 %[av2], %[s1] sc1\n\t"
    "v_mfma_f32_16x16x32_bf16 v[88:91], v[168:171], v[108:111], v[88:91]\n\t"
    "v_mfma_f32_16x16x32_bf16 v[92:95], v[172:175], v[108:111], v[92:95]\n\t"
    "s_waitcnt lgkmcnt(4)\n\t"
    "v_mfma_f32_16x16x32_bf16 v[80:83], v[176:179], v[112:115], v[80:83]\n\t"
    "ds_read_b128 v[104:107], %[da] offset:16384\n\t"
    "s_add_u32 m0, %[lb], 24576\n\t"
    "v_mfma_f32_16x16x32_bf16 v[84:87], v[180:183], v[112:115], v[84:87]\n\t"
    "ds_read_b128 v[108:111], %[da] offset:18432\n\t"
    "global_load_lds_dwordx4 %[av4], %[s1] sc1\n\t"
    "v_mfma_f32_16x16x32_bf16 v[88:91], v[176:179], v[116:119], v[88:91]\n\t"
    "v_mfma_f32_16x16x32_bf16 v[92:95], v[180:183], v[116:119], v[92:95]\n\t"
    "s_waitcnt lgkmcnt(4)\n\t"
    "v_mfma_f32_16x16x32_bf16 v[80:83], v[184:187], v[120:123], v[80:83]\n\t"
    "ds_read_b128 v[112:115], %[ea] offset:20480\n\t"
    "s_add_u32 m0, %[lb], 28672\n\t"
    "v_mfma_f32_16x16x32_bf16 v[84:87], v[188:191], v[120:123], v[84:87]\n\t"
    "ds_read_b128 v[116:119], %[ea] offset:22528\n\t"
    "global_load_lds_dwordx4 %[av6], %[s1] sc1\n\t"
    "v_mfma_f32_16x16x32_bf16 v[88:91], v[184:187], v[124:127], v[88:91]\n\t"
    "v_mfma_f32_16x16x32_bf16 v[92:95], v[188:191], v[124:127], v[92:95]\n\t"
    "s_waitcnt lgkmcnt(4)\n\t"
    "v_mfma_f32_16x16x32_bf16 v[80:83], v[192:195], v[96:99], v[80:83]\n\t"
    "ds_read_b128 v[120:123], %[da] offset:20480\n\t"
    "v_mfma_f32_16x16x32_bf16 v[84:87], v[196:199], v[96:99], v[84:87]\n\t"
    "ds_read_b128 v[124:127], %[da] offset:22528\n\t"
    "v_mfma_f32_16x16x32_bf16 v[88:91], v[192:195], v[100:103], v[88:91]\n\t"
    "v_mfma_f32_16x16x32_bf16 v[92:95], v[196:199], v[100:103], v[92:95]\n\t"
    "s_waitcnt vmcnt(8)\n\t"
    "s_barrier\n\t"
    "s_waitcnt lgkmcnt(4)\n\t"
    "v_mfma_f32_16x16x32_bf16 v[80:83], v[200:203], v[104:107], v[80:83]\n\t"
    "ds_read_b128 v[96:99], %[ea] offset:24576\n\t"
    "v_mfma_f32_16x16x32_bf16 v[84:87], v[204:207], v[104:107], v[84:87]\n\t"
    "ds_read_b128 v[100:103], %[ea] offset:26624\n\t"
    "v_mfma_f32_16x16x32_bf16 v[88:91], v[200:203], v[108:111], v[88:91]\n\t"
    "v_mfma_f32_16x16x32_bf16 v[92:95], v[204:207], v[108:111], v[92:95]\n\t"
    "s_waitcnt lgkmcnt(4)\n\t"
    "v_mfma_f32_16x16x32_bf16 v[80:83], v[208:211], v[112:115], v[80:83]\n\t"
    "ds_read_b128 v[104:107], %[da] offset:24576\n\t"
    "v_mfma_f32_16x16x32_bf16 v[84:87], v[212:215], v[112:115], v[84:87]\n\t"
    "ds_read_b128 v[108:111], %[da] offset:26624\n\t"
    "v_mfma_f32_16x16x32_bf16 v[88:91], v[208:211], v[116:119], v[88:91]\n\t"
    "v_mfma_f32_16x16x32_bf16 v[92:95], v[212:215], v[116:119], v[92:95]\n\t"
    "s_waitcnt lgkmcnt(4)\n\t"
    "v_mfma_f32_16x16x32_bf16 v[80:83], v[216:219], v[120:123], v[80:83]\n\t"
    "ds_read_b128 v[112:115], %[ea] offset:28672\n\t"
    "v_mfma_f32_16x16x32_bf16 v[84:87], v[220:223], v[120:123], v[84:87]\n\t"
    "ds_read_b128 v[116:119], %[ea] offset:30720\n\t"
    "v_mfma_f32_16x16x32_bf16 v[88:91], v[216:219], v[124:127], v[88:91]\n\t"
    "v_mfma_f32_16x16x32_bf16 v[92:95], v[220:223], v[124:127], v[92:95]\n\t"
    "s_waitcnt lgkmcnt(4)\n\t"
    "v_mfma_f32_16x16x32_bf16 v[80:83], v[224:227], v[96:99], v[80:83]\n\t"
    "ds_read_b128 v[120:123], %[da] offset:28672\n\t"
    "v_mfma_f32_16x16x32_bf16 v[84:87], v[228:231], v[96:99], v[84:87]\n\t"
    "ds_read_b128 v[124:127], %[da] offset:30720\n\t"
    "v_mfma_f32_16x16x32_bf16 v[88:91], v[224:227], v[100:103], v[88:91]\n\t"
    "v_mfma_f32_16x16x32_bf16 v[92:95], v[228:231], v[100:103], v[92:95]\n\t"
    "s_waitcnt lgkmcnt(4)\n\t"
    "v_mfma_f32_16x16x32_bf16 v[80:83], v[232:235], v[104:107], v[80:83]\n\t"
    "v_mfma_f32_16x16x32_bf16 v[84:87], v[236:239], v[104:107], v[84:87]\n\t"
    "v_mfma_f32_16x16x32_bf16 v[88:91], v[232:235], v[108:111], v[88:91]\n\t"
    "v_mfma_f32_16x16x32_bf16 v[92:95], v[236:239], v[108:111], v[92:95]\n\t"
    "s_waitcnt lgkmcnt(2)\n\t"
    "v_mfma_f32_16x16x32_bf16 v[80:83], v[240:243], v[112:115], v[80:83]\n\t"
    "v_mfma_f32_16x16x32_bf16 v[84:87], v[244:247], v[112:115], v[84:87]\n\t"
    "v_mfma_f32_16x16x32_bf16 v[88:91], v[240:243], v[116:119], v[88:91]\n\t"
    "v_mfma_f32_16x16x32_bf16 v[92:95], v[244:247], v[116:119], v[92:95]\n\t"
    "s_waitcnt lgkmcnt(0)\n\t"
    "v_mfma_f32_16x16x32_bf16 v[80:83], v[248:251], v[120:123], v[80:83]\n\t"
    "v_mfma_f32_16x16x32_bf16 v[84:87], v[252:255], v[120:123], v[84:87]\n\t"
    "v_mfma_f32_16x16x32_bf16 v[88:91], v[248:251], v[124:127], v[88:91]\n\t"
    "v_mfma_f32_16x16x32_bf16 v[92:95], v[252:255], v[124:127], v[92:95]\n\t"
    "s_nop 6\n\t"
    "s_branch 9f\n\t"
    "3:\n\t"
    "9:"
    : "=&{v[80:95]}"(pd)
    : [ea] "v"(ea), [da] "v"(da), [x] "s"(x), "{v[128:159]}"(W[0]), "{v[160:191]}"(W[1]), "{v[192:223]}"(W[2]), "{v[224:255]}"(W[3]), [lb] "s"(lb), [s0] "s"(s0), [s1] "s"(s1), [av0] "v"(av[0]), [av1] "v"(av[1]), [av2] "v"(av[2]), [av3] "v"(av[3]), [av4] "v"(av[4]), [av5] "v"(av[5]), [av6] "v"(av[6]), [av7] "v"(av[7])
    : "v96", "v97", "v98", "v99", "v100", "v101", "v102", "v103", "v104", "v105", "v106", "v107", "v108", "v109", "v110", "v111", "v112", "v113", "v114", "v115", "v116", "v117", "v118", "v119", "v120", "v121", "v122", "v123", "v124", "v125", "v126", "v127", "vcc", "scc", "m0", "memory");
}
__device__ __forceinline__ void lean_st0(const u32x32 (&W)[4], u32x16& pd, unsigned ea, unsigned da, unsigned x, unsigned pv, unsigned cn, unsigned sl, unsigned dv, const char* db, const char* pg) {
  unsigned st;
  asm volatile("ds_read_b128 v[96:99], %[ea] offset:0\n\t"
    "ds_read_b128 v[100:103], %[ea] offset:2048\n\t"
    "ds_read_b128 v[104:107], %[da] offset:0\n\t"
    "ds_read_b128 v[108:111], %[da] offset:2048\n\t"
    "ds_read_b128 v[112:115], %[ea] offset:4096\n\t"
    "ds_read_b128 v[116:119], %[ea] offset:6144\n\t"
    "s_waitcnt lgkmcnt(4)\n\t"
    "v_mfma_f32_16x16x32_bf16 v[80:83], v[128:131], v[96:99], 0\n\t"
    "ds_read_b128 v[120:123], %[da] offset:4096\n\t"
    "v_mfma_f32_16x16x32_bf16 v[84:87], v[132:135], v[96:99], 0\n\t"
    "ds_read_b128 v[124:127], %[da] offset:6144\n\t"
    "v_mfma_f32_16x16x32_bf16 v[88:91], v[128:131], v[100:103], 0\n\t"
    "v_mfma_f32_16x16x32_bf16 v[92:95], v[132:135], v[100:103], 0\n\t"
    "s_barrier\n\t"
    "s_waitcnt lgkmcnt(4)\n\t"
    "v_mfma_f32_16x16x32_bf16 v[80:83], v[136:139], v[104:107], v[80:83]\n\t"
    "ds_read_b128 v[96:99], %[ea] offset:8192\n\t"
    "v_mfma_f32_16x16x32_bf16 v[84:87], v[140:143], v[104:107], v[84:87]\n\t"
    "ds_read_b128 v[100:103], %[ea] offset:10240\n\t"
    "v_mfma_f32_16x16x32_bf16 v[88:91], v[136:139], v[108:111], v[88:91]\n\t"
    "v_mfma_f32_16x16x32_bf16 v[92:95], v[140:143], v[108:111], v[92:95]\n\t"
    "s_waitcnt lgkmcnt(4)\n\t"
    "v_mfma_f32_16x16x32_bf16 v[80:83], v[144:147], v[112:115], v[80:83]\n\t"
    "ds_read_b128 v[104:107], %[da] offset:8192\n\t"
    "v_mfma_f32_16x16x32_bf16 v[84:87], v[148:151], v[112:115], v[84:87]\n\t"
    "ds_read_b128 v[108:111], %[da] offset:10240\n\t"
    "v_mfma_f32_16x16x32_bf16 v[88:91], v[144:147], v[116:119], v[88:91]\n\t"
    "v_mfma_f32_16x16x32_bf16 v[92:95], v[148:151], v[116:119], v[92:95]\n\t"
    "s_waitcnt lgkmcnt(4)\n\t"
    "v_mfma_f32_16x16x32_bf16 v[80:83], v[152:155], v[120:123], v[80:83]\n\t"
    "ds_read_b128 v[112:115], %[ea] offset:12288\n\t"
    "v_mfma_f32_16x16x32_bf16 v[84:87], v[156:159], v[120:123], v[84:87]\n\t"
    "ds_read_b128 v[116:119], %[ea] offset:14336\n\t"
    "v_mfma_f32_16x16x32_bf16 v[88:91], v[152:155], v[124:127], v[88:91]\n\t"
    "v_mfma_f32_16x16x32_bf16 v[92:95], v[156:159], v[124:127], v[92:95]\n\t"
    "s_bitcmp1_b32 %[x], 2\n\t"
    "s_cbranch_scc1 7f\n\t"
    "8:\n\t"
    "s_waitcnt lgkmcnt(4)\n\t"
    "v_mfma_f32_16x16x32_bf16 v[80:83], v[160:163], v[96:99], v[80:83]\n\t"
    "ds_read_b128 v[120:123], %[da] offset:12288\n\t"
    "v_mfma_f32_16x16x32_bf16 v[84:87], v[164:167], v[96:99], v[84:87]\n\t"
    "ds_read_b128 v[124:127], %[da] offset:14336\n\t"
    "v_mfma_f32_16x16x32_bf16 v[88:91], v[160:163], v[100:103], v[88:91]\n\t"
    "v_mfma_f32_16x16x32_bf16 v[92:95], v[164:167], v[100:103], v[92:95]\n\t"
    "s_barrier\n\t"
    "s_waitcnt lgkmcnt(4)\n\t"
    "v_mfma_f32_16x16x32_bf16 v[80:83], v[168:171], v[104:107], v[80:83]\n\t"
    "ds_read_b128 v[96:99], %[ea] offset:16384\n\t"
    "v_mfma_f32_16x16x32_bf16 v[84:87], v[172:175], v[104:107], v[84:87]\n\t"
    "ds_read_b128 v[100:103], %[ea] offset:18432\n\t"
    "v_mfma_f32_16x16x32_bf16 v[88:91], v[168:171], v[108:111], v[88:91]\n\t"
    "v_mfma_f32_16x16x32_bf16 v[92:95], v[172:175], v[108:111], v[92:95]\n\t"
    "s_waitcnt lgkmcnt(4)\n\t"
    "v_mfma_f32_16x16x32_bf16 v[80:83], v[176:179], v[112:115], v[80:83]\n\t"
    "ds_read_b128 v[104:107], %[da] offset:16384\n\t"
    "v_mfma_f32_16x16x32_bf16 v[84:87], v[180:183], v[112:115], v[84:87]\n\t"
    "ds_read_b128 v[108:111], %[da] offset:18432\n\t"
    "v_mfma_f32_16x16x32_bf16 v[88:91], v[176:179], v[116:119], v[88:91]\n\t"
    "v_mfma_f32_16x16x32_bf16 v[92:95], v[180:183], v[116:119], v[92:95]\n\t"
    "s_waitcnt lgkmcnt(4)\n\t"
    "v_mfma_f32_16x16x32_bf16 v[80:83], v[184:187], v[120:123], v[80:83]\n\t"
    "ds_read_b128 v[112:115], %[ea] offset:20480\n\t"
    "v_mfma_f32_16x16x32_bf16 v[84:87], v[188:191], v[120:123], v[84:87]\n\t"
    "ds_read_b128 v[116:119], %[ea] offset:22528\n\t"
    "v_mfma_f32_16x16x32_bf16 v[88:91], v[184:187], v[124:127], v[88:91]\n\t"
    "v_mfma_f32_16x16x32_bf16 v[92:95], v[188:191], v[124:127], v[92:95]\n\t"
    "s_waitcnt lgkmcnt(4)\n\t"
    "v_mfma_f32_16x16x32_bf16 v[80:83], v[192:195], v[96:99], v[80:83]\n\t"
    "ds_read_b128 v[120:123], %[da] offset:20480\n\t"
    "v_mfma_f32_16x16x32_bf16 v[84:87], v[196:199], v[96:99], v[84:87]\n\t"
    "ds_read_b128 v[124:127], %[da] offset:22528\n\t"
    "v_mfma_f32_16x16x32_bf16 v[88:91], v[192:195], v[100:103], v[88:91]\n\t"
    "v_mfma_f32_16x16x32_bf16 v[92:95], v[196:199], v[100:103], v[92:95]\n\t"
    "s_barrier\n\t"
    "s_and_b32 %[st], %[x], 3\n\t"
    "s_cbranch_scc1 3f\n\t"
    "4:\n\t"
    "s_waitcnt lgkmcnt(4)\n\t"
    "v_mfma_f32_16x16x32_bf16 v[80:83], v[200:203], v[104:107], v[80:83]\n\t"
    "ds_read_b128 v[96:99], %[ea] offset:24576\n\t"
    "v_mfma_f32_16x16x32_bf16 v[84:87], v[204:207], v[104:107], v[84:87]\n\t"
    "ds_read_b128 v[100:103], %[ea] offset:26624\n\t"
    "v_mfma_f32_16x16x32_bf16 v[88:91], v[200:203], v[108:111], v[88:91]\n\t"
    "v_mfma_f32_16x16x32_bf16 v[92:95], v[204:207], v[108:111], v[92:95]\n\t"
    "s_waitcnt lgkmcnt(4)\n\t"
    "v_mfma_f32_16x16x32_bf16 v[80:83], v[208:211], v[112:115], v[80:83]\n\t"
    "ds_read_b128 v[104:107], %[da] offset:24576\n\t"
    "v_mfma_f32_16x16x32_bf16 v[84:87], v[212:215], v[112:115], v[84:87]\n\t"
    "ds_read_b128 v[108:111], %[da] offset:26624\n\t"
    "v_mfma_f32_16x16x32_bf16 v[88:91], v[208:211], v[116:119], v[88:91]\n\t"
    "v_mfma_f32_16x16x32_bf16 v[92:95], v[212:215], v[116:119], v[92:95]\n\t"
    "s_waitcnt lgkmcnt(4)\n\t"
    "v_mfma_f32_16x16x32_bf16 v[80:83], v[216:219], v[120:123], v[80:83]\n\t"
    "ds_read_b128 v[112:115], %[ea] offset:28672\n\t"
    "v_mfma_f32_16x16x32_bf16 v[84:87], v[220:223], v[120:123], v[84:87]\n\t"
    "ds_read_b128 v[116:119], %[ea] offset:30720\n\t"
    "v_mfma_f32_16x16x32_bf16 v[88:91], v[216:219], v[124:127], v[88:91]\n\t"
    "v_mfma_f32_16x16x32_bf16 v[92:95], v[220:223], v[124:127], v[92:95]\n\t"
    "s_waitcnt lgkmcnt(4)\n\t"
    "v_mfma_f32_16x16x32_bf16 v[80:83], v[224:227], v[96:99], v[80:83]\n\t"
    "ds_read_b128 v[120:123], %[da] offset:28672\n\t"
    "v_mfma_f32_16x16x32_bf16 v[84:87], v[228:231], v[96:99], v[84:87]\n\t"
    "ds_read_b128 v[124:127], %[da] offset:30720\n\t"
    "v_mfma_f32_16x16x32_bf16 v[88:91], v[224:227], v[100:103], v[88:91]\n\t"
    "v_mfma_f32_16x16x32_bf16 v[92:95], v[228:231], v[100:103], v[92:95]\n\t"
    "s_waitcnt lgkmcnt(4)\n\t"
    "v_mfma_f32_16x16x32_bf16 v[80:83], v[232:235], v[104:107], v[80:83]\n\t"
    "v_mfma_f32_16x16x32_bf16 v[84:87], v[236:239], v[104:107], v[84:87]\n\t"
    "v_mfma_f32_16x16x32_bf16 v[88:91], v[232:235], v[108:111], v[88:91]\n\t"
    "v_mfma_f32_16x16x32_bf16 v[92:95], v[236:239], v[108:111], v[92:95]\n\t"
    "s_waitcnt lgkmcnt(2)\n\t"
    "v_mfma_f32_16x16x32_bf16 v[80:83], v[240:243], v[112:115], v[80:83]\n\t"
    "v_mfma_f32_16x16x32_bf16 v[84:87], v[244:247], v[112:115], v[84:87]\n\t"
    "v_mfma_f32_16x16x32_bf16 v[88:91], v[240:243], v[116:119], v[88:91]\n\t"
    "v_mfma_f32_16x16x32_bf16 v[92:95], v[244:247], v[116:119], v[92:95]\n\t"
    "s_waitcnt lgkmcnt(0)\n\t"
    "v_mfma_f32_16x16x32_bf16 v[80:83], v[248:251], v[120:123], v[80:83]\n\t"
    "v_mfma_f32_16x16x32_bf16 v[84:87], v[252:255], v[120:123], v[84:87]\n\t"
    "v_mfma_f32_16x16x32_bf16 v[88:91], v[248:251], v[124:127], v[88:91]\n\t"
    "v_mfma_f32_16x16x32_bf16 v[92:95], v[252:255], v[124:127], v[92:95]\n\t"
    "s_nop 6\n\t"
    "s_branch 9f\n\t"
    "3:\n\t"
    "s_bitcmp0_b32 %[x], 0\n\t"
    "s_cbranch_scc1 5f\n\t"
    "7:\n\t"
    "v_mov_b32 v76, %[cn]\n\t"
    "v_mov_b32 v77, 1\n\t"
    "v_mov_b32 v78, %[pv]\n\t"
    "v_mov_b32 v79, 0\n\t"
    "s_waitcnt vmcnt(0)\n\t"
    "s_mov_b64 exec, 1\n\t"
    "ds_add_rtn_u32 v77, v76, v77\n\t"
    "s_waitcnt lgkmcnt(0)\n\t"
    "v_readfirstlane_b32 %[st], v77\n\t"
    "s_and_b32 %[st], %[st], 3\n\t"
    "s_cmp_lg_u32 %[st], 3\n\t"
    "s_cbranch_scc1 6f\n\t"
    "global_store_dword v79, v78, %[pg] sc1\n\t"
    "6:\n\t"
    "s_mov_b64 exec, -1\n\t"
    "s_bitcmp1_b32 %[x], 2\n\t"
    "s_cbranch_scc1 8b\n\t"
    "5:\n\t"
    "s_bitcmp0_b32 %[x], 1\n\t"
    "s_cbranch_scc1 4b\n\t"
    "s_mov_b32 m0, %[sl]\n\t"
    "s_nop 0\n\t"
    "global_load_lds_dword %[dv], %[db] sc1\n\t"
    "s_branch 4b\n\t"
    "9:"
    : "=&{v[80:95]}"(pd), [st] "=&s"(st)
    : [ea] "v"(ea), [da] "v"(da), [x] "s"(x), "{v[128:159]}"(W[0]), "{v[160:191]}"(W[1]), "{v[192:223]}"(W[2]), "{v[224:255]}"(W[3]), [pv] "s"(pv), [cn] "s"(cn), [sl] "s"(sl), [dv] "v"(dv), [db] "s"(db), [pg] "s"(pg)
    : "v96", "v97", "v98", "v99", "v100", "v101", "v102", "v103", "v104", "v105", "v106", "v107", "v108", "v109", "v110", "v111", "v112", "v113", "v114", "v115", "v116", "v117", "v118", "v119", "v120", "v121", "v122", "v123", "v124", "v125", "v126", "v127", "v76", "v77", "v78", "v79", "vcc", "scc", "m0", "memory");
}
#define LEAN_QB 1
#define PXB 2
__device__ __forceinline__ void paced_ld0(const u32x32 (&W)[4], unsigned ea, unsigned da, int& b, int ns, unsigned ab, unsigned rw, unsigned fa,
    const char* p0, const char* p1, unsigned lb, int& cap, int bx, int wm, unsigned rq, unsigned mu, unsigned ad, unsigned dv, const char* db, unsigned sl, unsigned sa, const unsigned (&av)[8]) {
  unsigned st;
  asm volatile("s_setprio 2\n\t"
    "v_mov_b32 v77, %[wm]\n\t"
    "s_cmp_lg_u32 %[rq], 0\n\t"
    "s_cbranch_scc1 3f\n\t"
    "2:\n\t"
    "v_cmp_ge_i32 vcc, %[b], v77\n\t"
    "s_cbranch_vccz 4f\n\t"
    "21:\n\t"
    "s_cmp_eq_u32 %[sl], 0\n\t"
    "s_cbranch_scc1 71f\n\t"
    "s_mov_b32 m0, %[sl]\n\t"
    "s_nop 0\n\t"
    "global_load_lds_dword %[dv], %[db] sc1\n\t"
    "71:\n\t"
    "s_sub_u32 %[cap], %[cap], 1\n\t"
    "s_max_i32 %[cap], %[cap], 0\n\t"
    "s_waitcnt vmcnt(1)\n\t"
    "ds_read2_b32 v[78:79], %[sa] offset1:32\n\t"
    "s_waitcnt lgkmcnt(0)\n\t"
    "v_max_u32 v78, v78, v79\n\t"
    "v_mad_u32_u24 v78, v78, %[mu], %[ad]\n\t"
    "v_max_i32 v77, v77, v78\n\t"
    "v_cmp_ge_i32 vcc, %[b], v77\n\t"
    "s_cbranch_vccz 4f\n\t"
    "s_sleep 1\n\t"
    "s_cmp_lg_u32 %[cap], 0\n\t"
    "s_cbranch_scc1 21b\n\t"
    "4:\n\t"
    "s_add_u32 m0, %[lb], 0\n\t"
    "s_nop 0\n\t"
    "global_load_lds_dwordx4 %[av0], s[36:37] sc1\n\t"
    "s_add_u32 m0, %[lb], 32768\n\t"
    "s_nop 0\n\t"
    "global_load_lds_dwordx4 %[av1], s[36:37] sc1\n\t"
    "s_add_u32 m0, %[lb], 4096\n\t"
    "s_nop 0\n\t"
    "global_load_lds_dwordx4 %[av2], s[36:37] sc1\n\t"
    "s_add_u32 m0, %[lb], 36864\n\t"
    "s_nop 0\n\t"
    "global_load_lds_dwordx4 %[av3], s[36:37] sc1\n\t"
    "s_add_u32 m0, %[lb], 8192\n\t"
    "s_nop 0\n\t"
    "global_load_lds_dwordx4 %[av4], s[36:37] sc1\n\t"
    "s_add_u32 m0, %[lb], 40960\n\t"
    "s_nop 0\n\t"
    "global_load_lds_dwordx4 %[av5], s[36:37] sc1\n\t"
    "s_add_u32 m0, %[lb], 12288\n\t"
    "s_nop 0\n\t"
    "global_load_lds_dwordx4 %[av6], s[36:37] sc1\n\t"
    "s_add_u32 m0, %[lb], 45056\n\t"
    "s_nop 0\n\t"
    "global_load_lds_dwordx4 %[av7], s[36:37] sc1\n\t"
    "s_add_u32 m0, %[lb], 16384\n\t"
    "s_nop 0\n\t"
    "global_load_lds_dwordx4 %[av0], s[38:39] sc1\n\t"
    "s_add_u32 m0, %[lb], 49152\n\t"
    "s_nop 0\n\t"
    "global_load_lds_dwordx4 %[av1], s[38:39] sc1\n\t"
    "s_add_u32 m0, %[lb], 20480\n\t"
    "s_nop 0\n\t"
    "global_load_lds_dwordx4 %[av2], s[38:39] sc1\n\t"
    "s_add_u32 m0, %[lb], 53248\n\t"
    "s_nop 0\n\t"
    "global_load_lds_dwordx4 %[av3], s[38:39] sc1\n\t"
    "s_add_u32 m0, %[lb], 24576\n\t"
    "s_nop 0\n\t"
    "global_load_lds_dwordx4 %[av4], s[38:39] sc1\n\t"
    "s_add_u32 m0, %[lb], 57344\n\t"
    "s_nop 0\n\t"
    "global_load_lds_dwordx4 %[av5], s[38:39] sc1\n\t"
    "s_add_u32 m0, %[lb], 28672\n\t"
    "s_nop 0\n\t"
    "global_load_lds_dwordx4 %[av6], s[38:39] sc1\n\t"
    "s_add_u32 m0, %[lb], 61440\n\t"
    "s_nop 0\n\t"
    "global_load_lds_dwordx4 %[av7], s[38:39] sc1\n\t"
    "s_add_u32 s36, s36, %[ab]\n\t"
    "s_addc_u32 s37, s37, 0\n\t"
    "s_add_u32 s38, s38, %[ab]\n\t"
    "s_addc_u32 s39, s39, 0\n\t"
    "s_xor_b32 %[lb], %[lb], 0x10000\n\t"
    "3:\n\t"
    "v_min_i32 v77, %[ns], v77\n\t"
    "s_cmp_eq_u32 %[sl], 0\n\t"
    "s_cbranch_scc1 31f\n\t"
    "s_add_u32 %[st], %[b], 2\n\t"
    "v_cmp_ge_i32 vcc, %[st], v77\n\t"
    "s_cmp_ge_i32 %[st], %[bx]\n\t"
    "s_cbranch_scc1 31f\n\t"
    "s_cbranch_vccnz 31f\n\t"
    "s_add_u32 %[st], %[b], 1\n\t"
    "v_mov_b32 v74, %[st]\n\t"
    "v_mov_b32 v75, %[st]\n\t"
    "ds_write2_b64 %[fa], v[74:75], v[74:75] offset0:4 offset1:5\n\t"
    "31:\n\t"
    "s_add_u32 %[st], %[b], 1\n\t"
    "v_cmp_ge_i32 vcc, %[st], v77\n\t"
    "s_waitcnt vmcnt(12) lgkmcnt(0)\n\t"
    "s_barrier\n\t"
    "ds_read2_b32 v[74:75], %[fa] offset0:4 offset1:8\n\t"
    "ds_read_b128 v[96:99], %[ea] offset:0\n\t"
    "ds_read_b128 v[100:103], %[ea] offset:2048\n\t"
    "ds_read_b128 v[104:107], %[da] offset:0\n\t"
    "ds_read_b128 v[108:111], %[da] offset:2048\n\t"
    "ds_read_b128 v[112:115], %[ea] offset:4096\n\t"
    "ds_read_b128 v[116:119], %[ea] offset:6144\n\t"
    "s_cbranch_vccz 5f\n\t"
    "s_waitcnt lgkmcnt(4)\n\t"
    "v_mfma_f32_16x16x32_bf16 v[80:83], v[128:131], v[96:99], 0\n\t"
    "ds_read_b128 v[120:123], %[da] offset:4096\n\t"
    "v_mfma_f32_16x16x32_bf16 v[84:87], v[132:135], v[96:99], 0\n\t"
    "ds_read_b128 v[124:127], %[da] offset:6144\n\t"
    "v_readfirstlane_b32 %[st], v74\n\t"
    "v_mfma_f32_16x16x32_bf16 v[88:91], v[128:131], v[100:103], 0\n\t"
    "v_mfma_f32_16x16x32_bf16 v[92:95], v[132:135], v[100:103], 0\n\t"
    "s_cmp_lg_u32 %[st], 0\n\t"
    "s_cbranch_scc1 9f\n\t"
    "s_waitcnt vmcnt(8)\n\t"
    "s_barrier\n\t"
    "s_waitcnt lgkmcnt(4)\n\t"
    "v_mfma_f32_16x16x32_bf16 v[80:83], v[136:139], v[104:107], v[80:83]\n\t"
    "ds_read_b128 v[96:99], %[ea] offset:8192\n\t"
    "v_mfma_f32_16x16x32_bf16 v[84:87], v[140:143], v[104:107], v[84:87]\n\t"
    "ds_read_b128 v[100:103], %[ea] offset:10240\n\t"
    "v_mfma_f32_16x16x32_bf16 v[88:91], v[136:139], v[108:111], v[88:91]\n\t"
    "v_mfma_f32_16x16x32_bf16 v[92:95], v[140:143], v[108:111], v[92:95]\n\t"
    "s_waitcnt lgkmcnt(4)\n\t"
    "v_mfma_f32_16x16x32_bf16 v[80:83], v[144:147], v[112:115], v[80:83]\n\t"
    "ds_read_b128 v[104:107], %[da] offset:8192\n\t"
    "v_mfma_f32_16x16x32_bf16 v[84:87], v[148:151], v[112:115], v[84:87]\n\t"
    "ds_read_b128 v[108:111], %[da] offset:10240\n\t"
    "v_mfma_f32_16x16x32_bf16 v[88:91], v[144:147], v[116:119], v[88:91]\n\t"
    "v_mfma_f32_16x16x32_bf16 v[92:95], v[148:151], v[116:119], v[92:95]\n\t"
    "s_waitcnt lgkmcnt(4)\n\t"
    "v_mfma_f32_16x16x32_bf16 v[80:83], v[152:155], v[120:123], v[80:83]\n\t"
    "ds_read_b128 v[112:115], %[ea] offset:12288\n\t"
    "v_mfma_f32_16x16x32_bf16 v[84:87], v[156:159], v[120:123], v[84:87]\n\t"
    "ds_read_b128 v[116:119], %[ea] offset:14336\n\t"
    "v_mfma_f32_16x16x32_bf16 v[88:91], v[152:155], v[124:127], v[88:91]\n\t"
    "v_mfma_f32_16x16x32_bf16 v[92:95], v[156:159], v[124:127], v[92:95]\n\t"
    "s_waitcnt lgkmcnt(4)\n\t"
    "v_mfma_f32_16x16x32_bf16 v[80:83], v[160:163], v[96:99], v[80:83]\n\t"
    "ds_read_b128 v[120:123], %[da] offset:12288\n\t"
    "v_mfma_f32_16x16x32_bf16 v[84:87], v[164:167], v[96:99], v[84:87]\n\t"
    "ds_read_b128 v[124:127], %[da] offset:14336\n\t"
    "v_mfma_f32_16x16x32_bf16 v[88:91], v[160:163], v[100:103], v[88:91]\n\t"
    "v_mfma_f32_16x16x32_bf16 v[92:95], v[164:167], v[100:103], v[92:95]\n\t"
    "s_waitcnt vmcnt(4)\n\t"
    "s_barrier\n\t"
    "s_waitcnt lgkmcnt(4)\n\t"
    "v_mfma_f32_16x16x32_bf16 v[80:83], v[168:171], v[104:107], v[80:83]\n\t"
    "ds_read_b128 v[96:99], %[ea] offset:16384\n\t"
    "v_mfma_f32_16x16x32_bf16 v[84:87], v[172:175], v[104:107], v[84:87]\n\t"
    "ds_read_b128 v[100:103], %[ea] offset:18432\n\t"
    "v_mfma_f32_16x16x32_bf16 v[88:91], v[168:171], v[108:111], v[88:91]\n\t"
    "v_mfma_f32_16x16x32_bf16 v[92:95], v[172:175], v[108:111], v[92:95]\n\t"
    "s_waitcnt lgkmcnt(4)\n\t"
    "v_mfma_f32_16x16x32_bf16 v[80:83], v[176:179], v[112:115], v[80:83]\n\t"
    "ds_read_b128 v[104:107], %[da] offset:16384\n\t"
    "v_mfma_f32_16x16x32_bf16 v[84:87], v[180:183], v[112:115], v[84:87]\n\t"
    "ds_read_b128 v[108:111], %[da] offset:18432\n\t"
    "v_mfma_f32_16x16x32_bf16 v[88:91], v[176:179], v[116:119], v[88:91]\n\t"
    "v_mfma_f32_16x16x32_bf16 v[92:95], v[180:183], v[116:119], v[92:95]\n\t"
    "s_waitcnt lgkmcnt(4)\n\t"
    "v_mfma_f32_16x16x32_bf16 v[80:83], v[184:187], v[120:123], v[80:83]\n\t"
    "ds_read_b128 v[112:115], %[ea] offset:20480\n\t"
    "v_mfma_f32_16x16x32_bf16 v[84:87], v[188:191], v[120:123], v[84:87]\n\t"
    "ds_read_b128 v[116:119], %[ea] offset:22528\n\t"
    "v_mfma_f32_16x16x32_bf16 v[88:91], v[184:187], v[124:127], v[88:91]\n\t"
    "v_mfma_f32_16x16x32_bf16 v[92:95], v[188:191], v[124:127], v[92:95]\n\t"
    "s_waitcnt lgkmcnt(4)\n\t"
    "v_mfma_f32_16x16x32_bf16 v[80:83], v[192:195], v[96:99], v[80:83]\n\t"
    "ds_read_b128 v[120:123], %[da] offset:20480\n\t"
    "v_mfma_f32_16x16x32_bf16 v[84:87], v[196:199], v[96:99], v[84:87]\n\t"
    "ds_read_b128 v[124:127], %[da] offset:22528\n\t"
    "v_mfma_f32_16x16x32_bf16 v[88:91], v[192:195], v[100:103], v[88:91]\n\t"
    "v_mfma_f32_16x16x32_bf16 v[92:95], v[196:199], v[100:103], v[92:95]\n\t"
    "s_waitcnt vmcnt(0)\n\t"
    "s_barrier\n\t"
    "s_waitcnt lgkmcnt(4)\n\t"
    "v_mfma_f32_16x16x32_bf16 v[80:83], v[200:203], v[104:107], v[80:83]\n\t"
    "ds_read_b128 v[96:99], %[ea] offset:24576\n\t"
    "v_mfma_f32_16x16x32_bf16 v[84:87], v[204:207], v[104:107], v[84:87]\n\t"
    "ds_read_b128 v[100:103], %[ea] offset:26624\n\t"
    "v_mfma_f32_16x16x32_bf16 v[88:91], v[200:203], v[108:111], v[88:91]\n\t"
    "v_mfma_f32_16x16x32_bf16 v[92:95], v[204:207], v[108:111], v[92:95]\n\t"
    "s_cmp_eq_u32 %[sl], 0\n\t"
    "s_cbranch_scc1 72f\n\t"
    "s_mov_b32 m0, %[sl]\n\t"
    "s_nop 0\n\t"
    "global_load_lds_dword %[dv], %[db] sc1\n\t"
    "72:\n\t"
    "s_waitcnt lgkmcnt(4)\n\t"
    "v_mfma_f32_16x16x32_bf16 v[80:83], v[208:211], v[112:115], v[80:83]\n\t"
    "ds_read_b128 v[104:107], %[da] offset:24576\n\t"
    "v_mfma_f32_16x16x32_bf16 v[84:87], v[212:215], v[112:115], v[84:87]\n\t"
    "ds_read_b128 v[108:111], %[da] offset:26624\n\t"
    "v_mfma_f32_16x16x32_bf16 v[88:91], v[208:211], v[116:119], v[88:91]\n\t"
    "v_mfma_f32_16x16x32_bf16 v[92:95], v[212:215], v[116:119], v[92:95]\n\t"
    "s_waitcnt lgkmcnt(4)\n\t"
    "v_mfma_f32_16x16x32_bf16 v[80:83], v[216:219], v[120:123], v[80:83]\n\t"
    "ds_read_b128 v[112:115], %[ea] offset:28672\n\t"
    "v_mfma_f32_16x16x32_bf16 v[84:87], v[220:223], v[120:123], v[84:87]\n\t"
    "ds_read_b128 v[116:119], %[ea] offset:30720\n\t"
    "v_mfma_f32_16x16x32_bf16 v[88:91], v[216:219], v[124:127], v[88:91]\n\t"
    "v_mfma_f32_16x16x32_bf16 v[92:95], v[220:223], v[124:127], v[92:95]\n\t"
    "s_waitcnt lgkmcnt(4)\n\t"
    "v_mfma_f32_16x16x32_bf16 v[80:83], v[224:227], v[96:99], v[80:83]\n\t"
    "ds_read_b128 v[120:123], %[da] offset:28672\n\t"
    "v_mfma_f32_16x16x32_bf16 v[84:87], v[228:231], v[96:99], v[84:87]\n\t"
    "ds_read_b128 v[124:127], %[da] offset:30720\n\t"
    "v_mfma_f32_16x16x32_bf16 v[88:91], v[224:227], v[100:103], v[88:91]\n\t"
    "v_mfma_f32_16x16x32_bf16 v[92:95], v[228:231], v[100:103], v[92:95]\n\t"
    "s_waitcnt lgkmcnt(4)\n\t"
    "v_mfma_f32_16x16x32_bf16 v[80:83], v[232:235], v[104:107], v[80:83]\n\t"
    "v_mfma_f32_16x16x32_bf16 v[84:87], v[236:239], v[104:107], v[84:87]\n\t"
    "v_mfma_f32_16x16x32_bf16 v[88:91], v[232:235], v[108:111], v[88:91]\n\t"
    "v_xor_b32 %[ea], 0x10000, %[ea]\n\t"
    "v_mfma_f32_16x16x32_bf16 v[92:95], v[236:239], v[108:111], v[92:95]\n\t"
    "v_xor_b32 %[da], 0x10000, %[da]\n\t"
    "s_cmp_eq_u32 %[sl], 0\n\t"
    "s_cbranch_scc1 73f\n\t"
    "s_mov_b32 m0, %[sl]\n\t"
    "s_nop 0\n\t"
    "global_load_lds_dword %[dv], %[db] sc1\n\t"
    "73:\n\t"
    "s_waitcnt lgkmcnt(2)\n\t"
    "v_mfma_f32_16x16x32_bf16 v[80:83], v[240:243], v[112:115], v[80:83]\n\t"
    "v_mfma_f32_16x16x32_bf16 v[84:87], v[244:247], v[112:115], v[84:87]\n\t"
    "v_mfma_f32_16x16x32_bf16 v[88:91], v[240:243], v[116:119], v[88:91]\n\t"
    "s_add_u32 %[b], %[b], 1\n\t"
    "v_mfma_f32_16x16x32_bf16 v[92:95], v[244:247], v[116:119], v[92:95]\n\t"
    "s_waitcnt lgkmcnt(0)\n\t"
    "v_mfma_f32_16x16x32_bf16 v[80:83], v[248:251], v[120:123], v[80:83]\n\t"
    "v_mfma_f32_16x16x32_bf16 v[84:87], v[252:255], v[120:123], v[84:87]\n\t"
    "v_mfma_f32_16x16x32_bf16 v[88:91], v[248:251], v[124:127], v[88:91]\n\t"
    "v_mfma_f32_16x16x32_bf16 v[92:95], v[252:255], v[124:127], v[92:95]\n\t"
    "s_nop 4\n\t"
    "ds_write_b128 %[rw], v[80:83] offset:0\n\t"
    "ds_write_b128 %[rw], v[84:87] offset:1024\n\t"
    "ds_write_b128 %[rw], v[88:91] offset:2048\n\t"
    "ds_write_b128 %[rw], v[92:95] offset:3072\n\t"
    "s_waitcnt vmcnt(1)\n\t"
    "ds_read2_b32 v[78:79], %[sa] offset1:32\n\t"
    "s_waitcnt lgkmcnt(0)\n\t"
    "v_max_u32 v78, v78, v79\n\t"
    "v_mad_u32_u24 v78, v78, %[mu], %[ad]\n\t"
    "v_max_i32 v77, v77, v78\n\t"
    "v_readfirstlane_b32 %[st], v75\n\t"
    "v_mov_b32 v76, %[b]\n\t"
    "s_cmp_eq_u32 %[st], %[b]\n\t"
    "s_cbranch_scc1 32f\n\t"
    "v_cmp_ge_i32 vcc, %[b], v77\n\t"
    "s_cmp_ge_i32 %[b], %[ns]\n\t"
    "s_cbranch_scc1 6f\n\t"
    "s_cbranch_vccnz 6f\n\t"
    "v_or_b32 v76, 0x100, v76\n\t"
    "ds_write_b32 %[fa], v76\n\t"
    "s_branch 4b\n\t"
    "6:\n\t"
    "ds_write_b32 %[fa], v76\n\t"
    "s_cbranch_scc0 21b\n\t"
    "s_waitcnt lgkmcnt(0)\n\t"
    "s_barrier\n\t"
    "s_branch 9f\n\t"
    "5:\n\t"
    "s_waitcnt lgkmcnt(4)\n\t"
    "v_mfma_f32_16x16x32_bf16 v[80:83], v[128:131], v[96:99], 0\n\t"
    "ds_read_b128 v[120:123], %[da] offset:4096\n\t"
    "s_add_u32 m0, %[lb], 0\n\t"
    "v_mfma_f32_16x16x32_bf16 v[84:87], v[132:135], v[96:99], 0\n\t"
    "ds_read_b128 v[124:127], %[da] offset:6144\n\t"
    "global_load_lds_dwordx4 %[av0], s[36:37] sc1\n\t"
    "v_readfirstlane_b32 %[st], v74\n\t"
    "v_mfma_f32_16x16x32_bf16 v[88:91], v[128:131], v[100:103], 0\n\t"
    "s_add_u32 m0, %[lb], 32768\n\t"
    "v_mfma_f32_16x16x32_bf16 v[92:95], v[132:135], v[100:103], 0\n\t"
    "global_load_lds_dwordx4 %[av1], s[36:37] sc1\n\t"
    "s_cmp_lg_u32 %[st], 0\n\t"
    "s_cbranch_scc1 9f\n\t"
    "s_waitcnt vmcnt(10)\n\t"
    "s_barrier\n\t"
    "s_waitcnt lgkmcnt(4)\n\t"
    "v_mfma_f32_16x16x32_bf16 v[80:83], v[136:139], v[104:107], v[80:83]\n\t"
    "ds_read_b128 v[96:99], %[ea] offset:8192\n\t"
    "s_add_u32 m0, %[lb], 4096\n\t"
    "v_mfma_f32_16x16x32_bf16 v[84:87], v[140:143], v[104:107], v[84:87]\n\t"
    "ds_read_b128 v[100:103], %[ea] offset:10240\n\t"
    "global_load_lds_dwordx4 %[av2], s[36:37] sc1\n\t"
    "v_mfma_f32_16x16x32_bf16 v[88:91], v[136:139], v[108:111], v[88:91]\n\t"
    "s_add_u32 m0, %[lb], 36864\n\t"
    "v_mfma_f32_16x16x32_bf16 v[92:95], v[140:143], v[108:111], v[92:95]\n\t"
    "global_load_lds_dwordx4 %[av3], s[36:37] sc1\n\t"
    "s_waitcnt lgkmcnt(4)\n\t"
    "v_mfma_f32_16x16x32_bf16 v[80:83], v[144:147], v[112:115], v[80:83]\n\t"
    "ds_read_b128 v[104:107], %[da] offset:8192\n\t"
    "s_add_u32 m0, %[lb], 8192\n\t"
    "v_mfma_f32_16x16x32_bf16 v[84:87], v[148:151], v[112:115], v[84:87]\n\t"
    "ds_read_b128 v[108:111], %[da] offset:10240\n\t"
    "global_load_lds_dwordx4 %[av4], s[36:37] sc1\n\t"
    "v_mfma_f32_16x16x32_bf16 v[88:91], v[144:147], v[116:119], v[88:91]\n\t"
    "s_add_u32 m0, %[lb], 40960\n\t"
    "v_mfma_f32_16x16x32_bf16 v[92:95], v[148:151], v[116:119], v[92:95]\n\t"
    "global_load_lds_dwordx4 %[av5], s[36:37] sc1\n\t"
    "s_waitcnt lgkmcnt(4)\n\t"
    "v_mfma_f32_16x16x32_bf16 v[80:83], v[152:155], v[120:123], v[80:83]\n\t"
    "ds_read_b128 v[112:115], %[ea] offset:12288\n\t"
    "s_add_u32 m0, %[lb], 12288\n\t"
    "v_mfma_f32_16x16x32_bf16 v[84:87], v[156:159], v[120:123], v[84:87]\n\t"
    "ds_read_b128 v[116:119], %[ea] offset:14336\n\t"
    "global_load_lds_dwordx4 %[av6], s[36:37] sc1\n\t"
    "v_mfma_f32_16x16x32_bf16 v[88:91], v[152:155], v[124:127], v[88:91]\n\t"
    "s_add_u32 m0, %[lb], 45056\n\t"
    "v_mfma_f32_16x16x32_bf16 v[92:95], v[156:159], v[124:127], v[92:95]\n\t"
    "global_load_lds_dwordx4 %[av7], s[36:37] sc1\n\t"
    "s_waitcnt lgkmcnt(4)\n\t"
    "v_mfma_f32_16x16x32_bf16 v[80:83], v[160:163], v[96:99], v[80:83]\n\t"
    "ds_read_b128 v[120:123], %[da] offset:12288\n\t"
    "s_add_u32 m0, %[lb], 16384\n\t"
    "v_mfma_f32_16x16x32_bf16 v[84:87], v[164:167], v[96:99], v[84:87]\n\t"
    "ds_read_b128 v[124:127], %[da] offset:14336\n\t"
    "global_load_lds_dwordx4 %[av0], s[38:39] sc1\n\t"
    "v_mfma_f32_16x16x32_bf16 v[88:91], v[160:163], v[100:103], v[88:91]\n\t"
    "s_add_u32 m0, %[lb], 49152\n\t"
    "v_mfma_f32_16x16x32_bf16 v[92:95], v[164:167], v[100:103], v[92:95]\n\t"
    "global_load_lds_dwordx4 %[av1], s[38:39] sc1\n\t"
    "s_waitcnt vmcnt(14)\n\t"
    "s_barrier\n\t"
    "s_waitcnt lgkmcnt(4)\n\t"
    "v_mfma_f32_16x16x32_bf16 v[80:83], v[168:171], v[104:107], v[80:83]\n\t"
    "ds_read_b128 v[96:99], %[ea] offset:16384\n\t"
    "s_add_u32 m0, %[lb], 20480\n\t"
    "v_mfma_f32_16x16x32_bf16 v[84:87], v[172:175], v[104:107], v[84:87]\n\t"
    "ds_read_b128 v[100:103], %[ea] offset:18432\n\t"
    "global_load_lds_dwordx4 %[av2], s[38:39] sc1\n\t"
    "v_mfma_f32_16x16x32_bf16 v[88:91], v[168:171], v[108:111], v[88:91]\n\t"
    "s_add_u32 m0, %[lb], 53248\n\t"
    "v_mfma_f32_16x16x32_bf16 v[92:95], v[172:175], v[108:111], v[92:95]\n\t"
    "global_load_lds_dwordx4 %[av3], s[38:39] sc1\n\t"
    "s_waitcnt lgkmcnt(4)\n\t"
    "v_mfma_f32_16x16x32_bf16 v[80:83], v[176:179], v[112:115], v[80:83]\n\t"
    "ds_read_b128 v[104:107], %[da] offset:16384\n\t"
    "s_add_u32 m0, %[lb], 24576\n\t"
    "v_mfma_f32_16x16x32_bf16 v[84:87], v[180:183], v[112:115], v[84:87]\n\t"
    "ds_read_b128 v[108:111], %[da] offset:18432\n\t"
    "global_load_lds_dwordx4 %[av4], s[38:39] sc1\n\t"
    "v_mfma_f32_16x16x32_bf16 v[88:91], v[176:179], v[116:119], v[88:91]\n\t"
    "s_add_u32 m0, %[lb], 57344\n\t"
    "v_mfma_f32_16x16x32_bf16 v[92:95], v[180:183], v[116:119], v[92:95]\n\t"
    "global_load_lds_dwordx4 %[av5], s[38:39] sc1\n\t"
    "s_waitcnt lgkmcnt(4)\n\t"
    "v_mfma_f32_16x16x32_bf16 v[80:83], v[184:187], v[120:123], v[80:83]\n\t"
    "ds_read_b128 v[112:115], %[ea] offset:20480\n\t"
    "s_add_u32 m0, %[lb], 28672\n\t"
    "v_mfma_f32_16x16x32_bf16 v[84:87], v[188:191], v[120:123], v[84:87]\n\t"
    "ds_read_b128 v[116:119], %[ea] offset:22528\n\t"
    "global_load_lds_dwordx4 %[av6], s[38:39] sc1\n\t"
    "v_mfma_f32_16x16x32_bf16 v[88:91], v[184:187], v[124:127], v[88:91]\n\t"
    "s_add_u32 m0, %[lb], 61440\n\t"
    "v_mfma_f32_16x16x32_bf16 v[92:95], v[188:191], v[124:127], v[92:95]\n\t"
    "global_load_lds_dwordx4 %[av7], s[38:39] sc1\n\t"
    "s_waitcnt lgkmcnt(4)\n\t"
    "v_mfma_f32_16x16x32_bf16 v[80:83], v[192:195], v[96:99], v[80:83]\n\t"
    "ds_read_b128 v[120:123], %[da] offset:20480\n\t"
    "v_mfma_f32_16x16x32_bf16 v[84:87], v[196:199], v[96:99], v[84:87]\n\t"
    "ds_read_b128 v[124:127], %[da] offset:22528\n\t"
    "v_mfma_f32_16x16x32_bf16 v[88:91], v[192:195], v[100:103], v[88:91]\n\t"
    "v_mfma_f32_16x16x32_bf16 v[92:95], v[196:199], v[100:103], v[92:95]\n\t"
    "s_add_u32 s36, s36, %[ab]\n\t"
    "s_addc_u32 s37, s37, 0\n\t"
    "s_add_u32 s38, s38, %[ab]\n\t"
    "s_addc_u32 s39, s39, 0\n\t"
    "s_xor_b32 %[lb], %[lb], 0x10000\n\t"
    "s_waitcnt vmcnt(16)\n\t"
    "s_barrier\n\t"
    "s_waitcnt lgkmcnt(4)\n\t"
    "v_mfma_f32_16x16x32_bf16 v[80:83], v[200:203], v[104:107], v[80:83]\n\t"
    "ds_read_b128 v[96:99], %[ea] offset:24576\n\t"
    "v_mfma_f32_16x16x32_bf16 v[84:87], v[204:207], v[104:107], v[84:87]\n\t"
    "ds_read_b128 v[100:103], %[ea] offset:26624\n\t"
    "v_mfma_f32_16x16x32_bf16 v[88:91], v[200:203], v[108:111], v[88:91]\n\t"
    "v_mfma_f32_16x16x32_bf16 v[92:95], v[204:207], v[108:111], v[92:95]\n\t"
    "s_cmp_eq_u32 %[sl], 0\n\t"
    "s_cbranch_scc1 74f\n\t"
    "s_mov_b32 m0, %[sl]\n\t"
    "s_nop 0\n\t"
    "global_load_lds_dword %[dv], %[db] sc1\n\t"
    "74:\n\t"
    "s_waitcnt lgkmcnt(4)\n\t"
    "v_mfma_f32_16x16x32_bf16 v[80:83], v[208:211], v[112:115], v[80:83]\n\t"
    "ds_read_b128 v[104:107], %[da] offset:24576\n\t"
    "v_mfma_f32_16x16x32_bf16 v[84:87], v[212:215], v[112:115], v[84:87]\n\t"
    "ds_read_b128 v[108:111], %[da] offset:26624\n\t"
    "v_mfma_f32_16x16x32_bf16 v[88:91], v[208:211], v[116:119], v[88:91]\n\t"
    "v_mfma_f32_16x16x32_bf16 v[92:95], v[212:215], v[116:119], v[92:95]\n\t"
    "s_waitcnt lgkmcnt(4)\n\t"
    "v_mfma_f32_16x16x32_bf16 v[80:83], v[216:219], v[120:123], v[80:83]\n\t"
    "ds_read_b128 v[112:115], %[ea] offset:28672\n\t"
    "v_mfma_f32_16x16x32_bf16 v[84:87], v[220:223], v[120:123], v[84:87]\n\t"
    "ds_read_b128 v[116:119], %[ea] offset:30720\n\t"
    "v_mfma_f32_16x16x32_bf16 v[88:91], v[216:219], v[124:127], v[88:91]\n\t"
    "v_mfma_f32_16x16x32_bf16 v[92:95], v[220:223], v[124:127], v[92:95]\n\t"
    "s_waitcnt lgkmcnt(4)\n\t"
    "v_mfma_f32_16x16x32_bf16 v[80:83], v[224:227], v[96:99], v[80:83]\n\t"
    "ds_read_b128 v[120:123], %[da] offset:28672\n\t"
    "v_mfma_f32_16x16x32_bf16 v[84:87], v[228:231], v[96:99], v[84:87]\n\t"
    "ds_read_b128 v[124:127], %[da] offset:30720\n\t"
    "v_mfma_f32_16x16x32_bf16 v[88:91], v[224:227], v[100:103], v[88:91]\n\t"
    "v_mfma_f32_16x16x32_bf16 v[92:95], v[228:231], v[100:103], v[92:95]\n\t"
    "s_waitcnt lgkmcnt(4)\n\t"
    "v_mfma_f32_16x16x32_bf16 v[80:83], v[232:235], v[104:107], v[80:83]\n\t"
    "v_mfma_f32_16x16x32_bf16 v[84:87], v[236:239], v[104:107], v[84:87]\n\t"
    "v_mfma_f32_16x16x32_bf16 v[88:91], v[232:235], v[108:111], v[88:91]\n\t"
    "v_xor_b32 %[ea], 0x10000, %[ea]\n\t"
    "v_mfma_f32_16x16x32_bf16 v[92:95], v[236:239], v[108:111], v[92:95]\n\t"
    "v_xor_b32 %[da], 0x10000, %[da]\n\t"
    "s_waitcnt lgkmcnt(2)\n\t"
    "v_mfma_f32_16x16x32_bf16 v[80:83], v[240:243], v[112:115], v[80:83]\n\t"
    "v_mfma_f32_16x16x32_bf16 v[84:87], v[244:247], v[112:115], v[84:87]\n\t"
    "v_mfma_f32_16x16x32_bf16 v[88:91], v[240:243], v[116:119], v[88:91]\n\t"
    "s_add_u32 %[b], %[b], 1\n\t"
    "v_mfma_f32_16x16x32_bf16 v[92:95], v[244:247], v[116:119], v[92:95]\n\t"
    "s_waitcnt lgkmcnt(0)\n\t"
    "v_mfma_f32_16x16x32_bf16 v[80:83], v[248:251], v[120:123], v[80:83]\n\t"
    "v_mfma_f32_16x16x32_bf16 v[84:87], v[252:255], v[120:123], v[84:87]\n\t"
    "v_mfma_f32_16x16x32_bf16 v[88:91], v[248:251], v[124:127], v[88:91]\n\t"
    "v_mfma_f32_16x16x32_bf16 v[92:95], v[252:255], v[124:127], v[92:95]\n\t"
    "v_mov_b32 v76, %[b]\n\t"
    "v_or_b32 v76, 0x100, v76\n\t"
    "s_nop 2\n\t"
    "ds_write_b128 %[rw], v[80:83] offset:0\n\t"
    "ds_write_b128 %[rw], v[84:87] offset:1024\n\t"
    "ds_write_b128 %[rw], v[88:91] offset:2048\n\t"
    "ds_write_b128 %[rw], v[92:95] offset:3072\n\t"
    "ds_write_b32 %[fa], v76\n\t"
    "v_readfirstlane_b32 %[st], v75\n\t"
    "s_waitcnt vmcnt(0)\n\t"
    "ds_read2_b32 v[78:79], %[sa] offset1:32\n\t"
    "s_waitcnt lgkmcnt(0)\n\t"
    "v_max_u32 v78, v78, v79\n\t"
    "v_mad_u32_u24 v78, v78, %[mu], %[ad]\n\t"
    "v_max_i32 v77, v77, v78\n\t"
    "s_cmp_lg_u32 %[st], %[b]\n\t"
    "s_cbranch_scc1 3b\n\t"
    "s_barrier\n\t"
    "s_branch 9f\n\t"
    "32:\n\t"
    "ds_write_b32 %[fa], v76\n\t"
    "s_add_u32 m0, %[lb], 0\n\t"
    "s_nop 0\n\t"
    "global_load_lds_dwordx4 %[av0], s[36:37] sc1\n\t"
    "s_add_u32 m0, %[lb], 32768\n\t"
    "s_nop 0\n\t"
    "global_load_lds_dwordx4 %[av1], s[36:37] sc1\n\t"
    "s_add_u32 m0, %[lb], 4096\n\t"
    "s_nop 0\n\t"
    "global_load_lds_dwordx4 %[av2], s[36:37] sc1\n\t"
    "s_add_u32 m0, %[lb], 36864\n\t"
    "s_nop 0\n\t"
    "global_load_lds_dwordx4 %[av3], s[36:37] sc1\n\t"
    "s_add_u32 m0, %[lb], 8192\n\t"
    "s_nop 0\n\t"
    "global_load_lds_dwordx4 %[av4], s[36:37] sc1\n\t"
    "s_add_u32 m0, %[lb], 40960\n\t"
    "s_nop 0\n\t"
    "global_load_lds_dwordx4 %[av5], s[36:37] sc1\n\t"
    "s_add_u32 m0, %[lb], 12288\n\t"
    "s_nop 0\n\t"
    "global_load_lds_dwordx4 %[av6], s[36:37] sc1\n\t"
    "s_add_u32 m0, %[lb], 45056\n\t"
    "s_nop 0\n\t"
    "global_load_lds_dwordx4 %[av7], s[36:37] sc1\n\t"
    "s_add_u32 m0, %[lb], 16384\n\t"
    "s_nop 0\n\t"
    "global_load_lds_dwordx4 %[av0], s[38:39] sc1\n\t"
    "s_add_u32 m0, %[lb], 49152\n\t"
    "s_nop 0\n\t"
    "global_load_lds_dwordx4 %[av1], s[38:39] sc1\n\t"
    "s_add_u32 m0, %[lb], 20480\n\t"
    "s_nop 0\n\t"
    "global_load_lds_dwordx4 %[av2], s[38:39] sc1\n\t"
    "s_add_u32 m0, %[lb], 53248\n\t"
    "s_nop 0\n\t"
    "global_load_lds_dwordx4 %[av3], s[38:39] sc1\n\t"
    "s_add_u32 m0, %[lb], 24576\n\t"
    "s_nop 0\n\t"
    "global_load_lds_dwordx4 %[av4], s[38:39] sc1\n\t"
    "s_add_u32 m0, %[lb], 57344\n\t"
    "s_nop 0\n\t"
    "global_load_lds_dwordx4 %[av5], s[38:39] sc1\n\t"
    "s_add_u32 m0, %[lb], 28672\n\t"
    "s_nop 0\n\t"
    "global_load_lds_dwordx4 %[av6], s[38:39] sc1\n\t"
    "s_add_u32 m0, %[lb], 61440\n\t"
    "s_nop 0\n\t"
    "global_load_lds_dwordx4 %[av7], s[38:39] sc1\n\t"
    "s_add_u32 s36, s36, %[ab]\n\t"
    "s_addc_u32 s37, s37, 0\n\t"
    "s_add_u32 s38, s38, %[ab]\n\t"
    "s_addc_u32 s39, s39, 0\n\t"
    "s_xor_b32 %[lb], %[lb], 0x10000\n\t"
    "s_waitcnt lgkmcnt(0)\n\t"
    "s_barrier\n\t"
    "9:\n\t"
    "s_waitcnt lgkmcnt(0)\n\t"
    "s_nop 7"
    : [ea] "+&v"(ea), [da] "+&v"(da), [b] "+&s"(b), [st] "=&s"(st), "+{s[36:37]}"(p0), "+{s[38:39]}"(p1), [lb] "+&s"(lb), [cap] "+&s"(cap)
    : [ns] "s"(ns), [ab] "s"(ab), [rw] "v"(rw), [fa] "v"(fa), "{v[128:159]}"(W[0]), "{v[160:191]}"(W[1]), "{v[192:223]}"(W[2]), "{v[224:255]}"(W[3]), [bx] "s"(bx), [wm] "s"(wm), [rq] "s"(rq), [mu] "s"(mu), [ad] "v"(ad), [dv] "v"(dv), [db] "s"(db), [sl] "s"(sl), [sa] "v"(sa), [av0] "v"(av[0]), [av1] "v"(av[1]), [av2] "v"(av[2]), [av3] "v"(av[3]), [av4] "v"(av[4]), [av5] "v"(av[5]), [av6] "v"(av[6]), [av7] "v"(av[7])
    : "v74", "v75", "v76", "v77", "v78", "v79", "v80", "v81", "v82", "v83", "v84", "v85", "v86", "v87", "v88", "v89", "v90", "v91", "v92", "v93", "v94", "v95", "v96", "v97", "v98", "v99", "v100", "v101", "v102", "v103", "v104", "v105", "v106", "v107", "v108", "v109", "v110", "v111", "v112", "v113", "v114", "v115", "v116", "v117", "v118", "v119", "v120", "v121", "v122", "v123", "v124", "v125", "v126", "v127", "vcc", "scc", "m0", "memory");
}
__device__ __forceinline__ void paced_ld2(const u32x32 (&W)[4], unsigned ea, unsigned da, int& b, int ns, unsigned ab, unsigned rw, unsigned fa,
    const char* p0, const char* p1, unsigned lb, int& cap, int bx, int wm, unsigned rq, unsigned mu, unsigned ad, unsigned dv, const char* db, unsigned sl, unsigned sa, const unsigned (&av)[8]) {
  unsigned st;
  asm volatile("s_setprio 2\n\t"
    "v_mov_b32 v77, %[wm]\n\t"
    "s_cmp_lg_u32 %[rq], 0\n\t"
    "s_cbranch_scc1 3f\n\t"
    "2:\n\t"
    "v_cmp_ge_i32 vcc, %[b], v77\n\t"
    "s_cbranch_vccz 4f\n\t"
    "21:\n\t"
    "s_cmp_eq_u32 %[sl], 0\n\t"
    "s_cbranch_scc1 71f\n\t"
    "s_mov_b32 m0, %[sl]\n\t"
    "s_nop 0\n\t"
    "global_load_lds_dword %[dv], %[db] sc1\n\t"
    "71:\n\t"
    "s_sub_u32 %[cap], %[cap], 1\n\t"
    "s_max_i32 %[cap], %[cap], 0\n\t"
    "s_waitcnt vmcnt(1)\n\t"
    "ds_read2_b32 v[78:79], %[sa] offset1:32\n\t"
    "s_waitcnt lgkmcnt(0)\n\t"
    "v_max_u32 v78, v78, v79\n\t"
    "v_mad_u32_u24 v78, v78, %[mu], %[ad]\n\t"
    "v_max_i32 v77, v77, v78\n\t"
    "v_cmp_ge_i32 vcc, %[b], v77\n\t"
    "s_cbranch_vccz 4f\n\t"
    "s_sleep 1\n\t"
    "s_cmp_lg_u32 %[cap], 0\n\t"
    "s_cbranch_scc1 21b\n\t"
    "4:\n\t"
    "s_add_u32 m0, %[lb], 0\n\t"
    "s_nop 0\n\t"
    "global_load_lds_dwordx4 %[av0], s[36:37] sc1\n\t"
    "s_add_u32 m0, %[lb], 4096\n\t"
    "s_nop 0\n\t"
    "global_load_lds_dwordx4 %[av2], s[36:37] sc1\n\t"
    "s_add_u32 m0, %[lb], 8192\n\t"
    "s_nop 0\n\t"
    "global_load_lds_dwordx4 %[av4], s[36:37] sc1\n\t"
    "s_add_u32 m0, %[lb], 12288\n\t"
    "s_nop 0\n\t"
    "global_load_lds_dwordx4 %[av6], s[36:37] sc1\n\t"
    "s_add_u32 m0, %[lb], 16384\n\t"
    "s_nop 0\n\t"
    "global_load_lds_dwordx4 %[av0], s[38:39] sc1\n\t"
    "s_add_u32 m0, %[lb], 20480\n\t"
    "s_nop 0\n\t"
    "global_load_lds_dwordx4 %[av2], s[38:39] sc1\n\t"
    "s_add_u32 m0, %[lb], 24576\n\t"
    "s_nop 0\n\t"
    "global_load_lds_dwordx4 %[av4], s[38:39] sc1\n\t"
    "s_add_u32 m0, %[lb], 28672\n\t"
    "s_nop 0\n\t"
    "global_load_lds_dwordx4 %[av6], s[38:39] sc1\n\t"
    "s_add_u32 s36, s36, %[ab]\n\t"
    "s_addc_u32 s37, s37, 0\n\t"
    "s_add_u32 s38, s38, %[ab]\n\t"
    "s_addc_u32 s39, s39, 0\n\t"
    "s_xor_b32 %[lb], %[lb], 0x10000\n\t"
    "3:\n\t"
    "v_min_i32 v77, %[ns], v77\n\t"
    "s_cmp_eq_u32 %[sl], 0\n\t"
    "s_cbranch_scc1 31f\n\t"
    "s_add_u32 %[st], %[b], 2\n\t"
    "v_cmp_ge_i32 vcc, %[st], v77\n\t"
    "s_cmp_ge_i32 %[st], %[bx]\n\t"
    "s_cbranch_scc1 31f\n\t"
    "s_cbranch_vccnz 31f\n\t"
    "s_add_u32 %[st], %[b], 1\n\t"
    "v_mov_b32 v74, %[st]\n\t"
    "v_mov_b32 v75, %[st]\n\t"
    "ds_write2_b64 %[fa], v[74:75], v[74:75] offset0:4 offset1:5\n\t"
    "31:\n\t"
    "s_add_u32 %[st], %[b], 1\n\t"
    "v_cmp_ge_i32 vcc, %[st], v77\n\t"
    "s_waitcnt vmcnt(6) lgkmcnt(0)\n\t"
    "s_barrier\n\t"
    "ds_read2_b32 v[74:75], %[fa] offset0:4 offset1:8\n\t"
    "ds_read_b128 v[96:99], %[ea] offset:0\n\t"
    "ds_read_b128 v[100:103], %[ea] offset:2048\n\t"
    "ds_read_b128 v[104:107], %[da] offset:0\n\t"
    "ds_read_b128 v[108:111], %[da] offset:2048\n\t"
    "ds_read_b128 v[112:115], %[ea] offset:4096\n\t"
    "ds_read_b128 v[116:119], %[ea] offset:6144\n\t"
    "s_cbranch_vccz 5f\n\t"
    "s_waitcnt lgkmcnt(4)\n\t"
    "v_mfma_f32_16x16x32_bf16 v[80:83], v[128:131], v[96:99], 0\n\t"
    "ds_read_b128 v[120:123], %[da] offset:4096\n\t"
    "v_mfma_f32_16x16x32_bf16 v[84:87], v[132:135], v[96:99], 0\n\t"
    "ds_read_b128 v[124:127], %[da] offset:6144\n\t"
    "v_readfirstlane_b32 %[st], v74\n\t"
    "v_mfma_f32_16x16x32_bf16 v[88:91], v[128:131], v[100:103], 0\n\t"
    "v_mfma_f32_16x16x32_bf16 v[92:95], v[132:135], v[100:103], 0\n\t"
    "s_cmp_lg_u32 %[st], 0\n\t"
    "s_cbranch_scc1 9f\n\t"
    "s_waitcnt vmcnt(4)\n\t"
    "s_barrier\n\t"
    "s_waitcnt lgkmcnt(4)\n\t"
    "v_mfma_f32_16x16x32_bf16 v[80:83], v[136:139], v[104:107], v[80:83]\n\t"
    "ds_read_b128 v[96:99], %[ea] offset:8192\n\t"
    "v_mfma_f32_16x16x32_bf16 v[84:87], v[140:143], v[104:107], v[84:87]\n\t"
    "ds_read_b128 v[100:103], %[ea] offset:10240\n\t"
    "v_mfma_f32_16x16x32_bf16 v[88:91], v[136:139], v[108:111], v[88:91]\n\t"
    "v_mfma_f32_16x16x32_bf16 v[92:95], v[140:143], v[108:111], v[92:95]\n\t"
    "s_waitcnt lgkmcnt(4)\n\t"
    "v_mfma_f32_16x16x32_bf16 v[80:83], v[144:147], v[112:115], v[80:83]\n\t"
    "ds_read_b128 v[104:107], %[da] offset:8192\n\t"
    "v_mfma_f32_16x16x32_bf16 v[84:87], v[148:151], v[112:115], v[84:87]\n\t"
    "ds_read_b128 v[108:111], %[da] offset:10240\n\t"
    "v_mfma_f32_16x16x32_bf16 v[88:91], v[144:147], v[116:119], v[88:91]\n\t"
    "v_mfma_f32_16x16x32_bf16 v[92:95], v[148:151], v[116:119], v[92:95]\n\t"
    "s_waitcnt lgkmcnt(4)\n\t"
    "v_mfma_f32_16x16x32_bf16 v[80:83], v[152:155], v[120:123], v[80:83]\n\t"
    "ds_read_b128 v[112:115], %[ea] offset:12288\n\t"
    "v_mfma_f32_16x16x32_bf16 v[84:87], v[156:159], v[120:123], v[84:87]\n\t"
    "ds_read_b128 v[116:119], %[ea] offset:14336\n\t"
    "v_mfma_f32_16x16x32_bf16 v[88:91], v[152:155], v[124:127], v[88:91]\n\t"
    "v_mfma_f32_16x16x32_bf16 v[92:95], v[156:159], v[124:127], v[92:95]\n\t"
    "s_waitcnt lgkmcnt(4)\n\t"
    "v_mfma_f32_16x16x32_bf16 v[80:83], v[160:163], v[96:99], v[80:83]\n\t"
    "ds_read_b128 v[120:123], %[da] offset:12288\n\t"
    "v_mfma_f32_16x16x32_bf16 v[84:87], v[164:167], v[96:99], v[84:87]\n\t"
    "ds_read_b128 v[124:127], %[da] offset:14336\n\t"
    "v_mfma_f32_16x16x32_bf16 v[88:91], v[160:163], v[100:103], v[88:91]\n\t"
    "v_mfma_f32_16x16x32_bf16 v[92:95], v[164:167], v[100:103], v[92:95]\n\t"
    "s_waitcnt vmcnt(2)\n\t"
    "s_barrier\n\t"
    "s_waitcnt lgkmcnt(4)\n\t"
    "v_mfma_f32_16x16x32_bf16 v[80:83], v[168:171], v[104:107], v[80:83]\n\t"
    "ds_read_b128 v[96:99], %[ea] offset:16384\n\t"
    "v_mfma_f32_16x16x32_bf16 v[84:87], v[172:175], v[104:107], v[84:87]\n\t"
    "ds_read_b128 v[100:103], %[ea] offset:18432\n\t"
    "v_mfma_f32_16x16x32_bf16 v[88:91], v[168:171], v[108:111], v[88:91]\n\t"
    "v_mfma_f32_16x16x32_bf16 v[92:95], v[172:175], v[108:111], v[92:95]\n\t"
    "s_waitcnt lgkmcnt(4)\n\t"
    "v_mfma_f32_16x16x32_bf16 v[80:83], v[176:179], v[112:115], v[80:83]\n\t"
    "ds_read_b128 v[104:107], %[da] offset:16384\n\t"
    "v_mfma_f32_16x16x32_bf16 v[84:87], v[180:183], v[112:115], v[84:87]\n\t"
    "ds_read_b128 v[108:111], %[da] offset:18432\n\t"
    "v_mfma_f32_16x16x32_bf16 v[88:91], v[176:179], v[116:119], v[88:91]\n\t"
    "v_mfma_f32_16x16x32_bf16 v[92:95], v[180:183], v[116:119], v[92:95]\n\t"
    "s_waitcnt lgkmcnt(4)\n\t"
    "v_mfma_f32_16x16x32_bf16 v[80:83], v[184:187], v[120:123], v[80:83]\n\t"
    "ds_read_b128 v[112:115], %[ea] offset:20480\n\t"
    "v_mfma_f32_16x16x32_bf16 v[84:87], v[188:191], v[120:123], v[84:87]\n\t"
    "ds_read_b128 v[116:119], %[ea] offset:22528\n\t"
    "v_mfma_f32_16x16x32_bf16 v[88:91], v[184:187], v[124:127], v[88:91]\n\t"
    "v_mfma_f32_16x16x32_bf16 v[92:95], v[188:191], v[124:127], v[92:95]\n\t"
    "s_waitcnt lgkmcnt(4)\n\t"
    "v_mfma_f32_16x16x32_bf16 v[80:83], v[192:195], v[96:99], v[80:83]\n\t"
    "ds_read_b128 v[120:123], %[da] offset:20480\n\t"
    "v_mfma_f32_16x16x32_bf16 v[84:87], v[196:199], v[96:99], v[84:87]\n\t"
    "ds_read_b128 v[124:127], %[da] offset:22528\n\t"
    "v_mfma_f32_16x16x32_bf16 v[88:91], v[192:195], v[100:103], v[88:91]\n\t"
    "v_mfma_f32_16x16x32_bf16 v[92:95], v[196:199], v[100:103], v[92:95]\n\t"
    "s_waitcnt vmcnt(0)\n\t"
    "s_barrier\n\t"
    "s_waitcnt lgkmcnt(4)\n\t"
    "v_mfma_f32_16x16x32_bf16 v[80:83], v[200:203], v[104:107], v[80:83]\n\t"
    "ds_read_b128 v[96:99], %[ea] offset:24576\n\t"
    "v_mfma_f32_16x16x32_bf16 v[84:87], v[204:207], v[104:107], v[84:87]\n\t"
    "ds_read_b128 v[100:103], %[ea] offset:26624\n\t"
    "v_mfma_f32_16x16x32_bf16 v[88:91], v[200:203], v[108:111], v[88:91]\n\t"
    "v_mfma_f32_16x16x32_bf16 v[92:95], v[204:207], v[108:111], v[92:95]\n\t"
    "s_cmp_eq_u32 %[sl], 0\n\t"
    "s_cbranch_scc1 72f\n\t"
    "s_mov_b32 m0, %[sl]\n\t"
    "s_nop 0\n\t"
    "global_load_lds_dword %[dv], %[db] sc1\n\t"
    "72:\n\t"
    "s_waitcnt lgkmcnt(4)\n\t"
    "v_mfma_f32_16x16x32_bf16 v[80:83], v[208:211], v[112:115], v[80:83]\n\t"
    "ds_read_b128 v[104:107], %[da] offset:24576\n\t"
    "v_mfma_f32_16x16x32_bf16 v[84:87], v[212:215], v[112:115], v[84:87]\n\t"
    "ds_read_b128 v[108:111], %[da] offset:26624\n\t"
    "v_mfma_f32_16x16x32_bf16 v[88:91], v[208:211], v[116:119], v[88:91]\n\t"
    "v_mfma_f32_16x16x32_bf16 v[92:95], v[212:215], v[116:119], v[92:95]\n\t"
    "s_waitcnt lgkmcnt(4)\n\t"
    "v_mfma_f32_16x16x32_bf16 v[80:83], v[216:219], v[120:123], v[80:83]\n\t"
    "ds_read_b128 v[112:115], %[ea] offset:28672\n\t"
    "v_mfma_f32_16x16x32_bf16 v[84:87], v[220:223], v[120:123], v[84:87]\n\t"
    "ds_read_b128 v[116:119], %[ea] offset:30720\n\t"
    "v_mfma_f32_16x16x32_bf16 v[88:91], v[216:219], v[124:127], v[88:91]\n\t"
    "v_mfma_f32_16x16x32_bf16 v[92:95], v[220:223], v[124:127], v[92:95]\n\t"
    "s_waitcnt lgkmcnt(4)\n\t"
    "v_mfma_f32_16x16x32_bf16 v[80:83], v[224:227], v[96:99], v[80:83]\n\t"
    "ds_read_b128 v[120:123], %[da] offset:28672\n\t"
    "v_mfma_f32_16x16x32_bf16 v[84:87], v[228:231], v[96:99], v[84:87]\n\t"
    "ds_read_b128 v[124:127], %[da] offset:30720\n\t"
    "v_mfma_f32_16x16x32_bf16 v[88:91], v[224:227], v[100:103], v[88:91]\n\t"
    "v_mfma_f32_16x16x32_bf16 v[92:95], v[228:231], v[100:103], v[92:95]\n\t"
    "s_waitcnt lgkmcnt(4)\n\t"
    "v_mfma_f32_16x16x32_bf16 v[80:83], v[232:235], v[104:107], v[80:83]\n\t"
    "v_mfma_f32_16x16x32_bf16 v[84:87], v[236:239], v[104:107], v[84:87]\n\t"
    "v_mfma_f32_16x16x32_bf16 v[88:91], v[232:235], v[108:111], v[88:91]\n\t"
    "v_xor_b32 %[ea], 0x10000, %[ea]\n\t"
    "v_mfma_f32_16x16x32_bf16 v[92:95], v[236:239], v[108:111], v[92:95]\n\t"
    "v_xor_b32 %[da], 0x10000, %[da]\n\t"
    "s_cmp_eq_u32 %[sl], 0\n\t"
    "s_cbranch_scc1 73f\n\t"
    "s_mov_b32 m0, %[sl]\n\t"
    "s_nop 0\n\t"
    "global_load_lds_dword %[dv], %[db] sc1\n\t"
    "73:\n\t"
    "s_waitcnt lgkmcnt(2)\n\t"
    "v_mfma_f32_16x16x32_bf16 v[80:83], v[240:243], v[112:115], v[80:83]\n\t"
    "v_mfma_f32_16x16x32_bf16 v[84:87], v[244:247], v[112:115], v[84:87]\n\t"
    "v_mfma_f32_16x16x32_bf16 v[88:91], v[240:243], v[116:119], v[88:91]\n\t"
    "s_add_u32 %[b], %[b], 1\n\t"
    "v_mfma_f32_16x16x32_bf16 v[92:95], v[244:247], v[116:119], v[92:95]\n\t"
    "s_waitcnt lgkmcnt(0)\n\t"
    "v_mfma_f32_16x16x32_bf16 v[80:83], v[248:251], v[120:123], v[80:83]\n\t"
    "v_mfma_f32_16x16x32_bf16 v[84:87], v[252:255], v[120:123], v[84:87]\n\t"
    "v_mfma_f32_16x16x32_bf16 v[88:91], v[248:251], v[124:127], v[88:91]\n\t"
    "v_mfma_f32_16x16x32_bf16 v[92:95], v[252:255], v[124:127], v[92:95]\n\t"
    "s_nop 4\n\t"
    "ds_write_b128 %[rw], v[80:83] offset:0\n\t"
    "ds_write_b128 %[rw], v[84:87] offset:1024\n\t"
    "ds_write_b128 %[rw], v[88:91] offset:2048\n\t"
    "ds_write_b128 %[rw], v[92:95] offset:3072\n\t"
    "s_waitcnt vmcnt(1)\n\t"
    "ds_read2_b32 v[78:79], %[sa] offset1:32\n\t"
    "s_waitcnt lgkmcnt(0)\n\t"
    "v_max_u32 v78, v78, v79\n\t"
    "v_mad_u32_u24 v78, v78, %[mu], %[ad]\n\t"
    "v_max_i32 v77, v77, v78\n\t"
    "v_readfirstlane_b32 %[st], v75\n\t"
    "v_mov_b32 v76, %[b]\n\t"
    "s_cmp_eq_u32 %[st], %[b]\n\t"
    "s_cbranch_scc1 32f\n\t"
    "v_cmp_ge_i32 vcc, %[b], v77\n\t"
    "s_cmp_ge_i32 %[b], %[ns]\n\t"
    "s_cbranch_scc1 6f\n\t"
    "s_cbranch_vccnz 6f\n\t"
    "v_or_b32 v76, 0x100, v76\n\t"
    "ds_write_b32 %[fa], v76\n\t"
    "s_branch 4b\n\t"
    "6:\n\t"
    "ds_write_b32 %[fa], v76\n\t"
    "s_cbranch_scc0 21b\n\t"
    "s_waitcnt lgkmcnt(0)\n\t"
    "s_barrier\n\t"
    "s_branch 9f\n\t"
    "5:\n\t"
    "s_waitcnt lgkmcnt(4)\n\t"
    "v_mfma_f32_16x16x32_bf16 v[80:83], v[128:131], v[96:99], 0\n\t"
    "ds_read_b128 v[120:123], %[da] offset:4096\n\t"
    "s_add_u32 m0, %[lb], 0\n\t"
    "v_mfma_f32_16x16x32_bf16 v[84:87], v[132:135], v[96:99], 0\n\t"
    "ds_read_b128 v[124:127], %[da] offset:6144\n\t"
    "global_load_lds_dwordx4 %[av0], s[36:37] sc1\n\t"
    "v_readfirstlane_b32 %[st], v74\n\t"
    "v_mfma_f32_16x16x32_bf16 v[88:91], v[128:131], v[100:103], 0\n\t"
    "v_mfma_f32_16x16x32_bf16 v[92:95], v[132:135], v[100:103], 0\n\t"
    "s_cmp_lg_u32 %[st], 0\n\t"
    "s_cbranch_scc1 9f\n\t"
    "s_waitcnt vmcnt(5)\n\t"
    "s_barrier\n\t"
    "s_waitcnt lgkmcnt(4)\n\t"
    "v_mfma_f32_16x16x32_bf16 v[80:83], v[136:139], v[104:107], v[80:83]\n\t"
    "ds_read_b128 v[96:99], %[ea] offset:8192\n\t"
    "s_add_u32 m0, %[lb], 4096\n\t"
    "v_mfma_f32_16x16x32_bf16 v[84:87], v[140:143], v[104:107], v[84:87]\n\t"
    "ds_read_b128 v[100:103], %[ea] offset:10240\n\t"
    "global_load_lds_dwordx4 %[av2], s[36:37] sc1\n\t"
    "v_mfma_f32_16x16x32_bf16 v[88:91], v[136:139], v[108:111], v[88:91]\n\t"
    "v_mfma_f32_16x16x32_bf16 v[92:95], v[140:143], v[108:111], v[92:95]\n\t"
    "s_waitcnt lgkmcnt(4)\n\t"
    "v_mfma_f32_16x16x32_bf16 v[80:83], v[144:147], v[112:115], v[80:83]\n\t"
    "ds_read_b128 v[104:107], %[da] offset:8192\n\t"
    "s_add_u32 m0, %[lb], 8192\n\t"
    "v_mfma_f32_16x16x32_bf16 v[84:87], v[148:151], v[112:115], v[84:87]\n\t"
    "ds_read_b128 v[108:111], %[da] offset:10240\n\t"
    "global_load_lds_dwordx4 %[av4], s[36:37] sc1\n\t"
    "v_mfma_f32_16x16x32_bf16 v[88:91], v[144:147], v[116:119], v[88:91]\n\t"
    "v_mfma_f32_16x16x32_bf16 v[92:95], v[148:151], v[116:119], v[92:95]\n\t"
    "s_waitcnt lgkmcnt(4)\n\t"
    "v_mfma_f32_16x16x32_bf16 v[80:83], v[152:155], v[120:123], v[80:83]\n\t"
    "ds_read_b128 v[112:115], %[ea] offset:12288\n\t"
    "s_add_u32 m0, %[lb], 12288\n\t"
    "v_mfma_f32_16x16x32_bf16 v[84:87], v[156:159], v[120:123], v[84:87]\n\t"
    "ds_read_b128 v[116:119], %[ea] offset:14336\n\t"
    "global_load_lds_dwordx4 %[av6], s[36:37] sc1\n\t"
    "v_mfma_f32_16x16x32_bf16 v[88:91], v[152:155], v[124:127], v[88:91]\n\t"
    "v_mfma_f32_16x16x32_bf16 v[92:95], v[156:159], v[124:127], v[92:95]\n\t"
    "s_waitcnt lgkmcnt(4)\n\t"
    "v_mfma_f32_16x16x32_bf16 v[80:83], v[160:163], v[96:99], v[80:83]\n\t"
    "ds_read_b128 v[120:123], %[da] offset:12288\n\t"
    "s_add_u32 m0, %[lb], 16384\n\t"
    "v_mfma_f32_16x16x32_bf16 v[84:87], v[164:167], v[96:99], v[84:87]\n\t"
    "ds_read_b128 v[124:127], %[da] offset:14336\n\t"
    "global_load_lds_dwordx4 %[av0], s[38:39] sc1\n\t"
    "v_mfma_f32_16x16x32_bf16 v[88:91], v[160:163], v[100:103], v[88:91]\n\t"
    "v_mfma_f32_16x16x32_bf16 v[92:95], v[164:167], v[100:103], v[92:95]\n\t"
    "s_waitcnt vmcnt(7)\n\t"
    "s_barrier\n\t"
    "s_waitcnt lgkmcnt(4)\n\t"
    "v_mfma_f32_16x16x32_bf16 v[80:83], v[168:171], v[104:107], v[80:83]\n\t"
    "ds_read_b128 v[96:99], %[ea] offset:16384\n\t"
    "s_add_u32 m0, %[lb], 20480\n\t"
    "v_mfma_f32_16x16x32_bf16 v[84:87], v[172:175], v[104:107], v[84:87]\n\t"
    "ds_read_b128 v[100:103], %[ea] offset:18432\n\t"
    "global_load_lds_dwordx4 %[av2], s[38:39] sc1\n\t"
    "v_mfma_f32_16x16x32_bf16 v[88:91], v[168:171], v[108:111], v[88:91]\n\t"
    "v_mfma_f32_16x16x32_bf16 v[92:95], v[172:175], v[108:111], v[92:95]\n\t"
    "s_waitcnt lgkmcnt(4)\n\t"
    "v_mfma_f32_16x16x32_bf16 v[80:83], v[176:179], v[112:115], v[80:83]\n\t"
    "ds_read_b128 v[104:107], %[da] offset:16384\n\t"
    "s_add_u32 m0, %[lb], 24576\n\t"
    "v_mfma_f32_16x16x32_bf16 v[84:87], v[180:183], v[112:115], v[84:87]\n\t"
    "ds_read_b128 v[108:111], %[da] offset:18432\n\t"
    "global_load_lds_dwordx4 %[av4], s[38:39] sc1\n\t"
    "v_mfma_f32_16x16x32_bf16 v[88:91], v[176:179], v[116:119], v[88:91]\n\t"
    "v_mfma_f32_16x16x32_bf16 v[92:95], v[180:183], v[116:119], v[92:95]\n\t"
    "s_waitcnt lgkmcnt(4)\n\t"
    "v_mfma_f32_16x16x32_bf16 v[80:83], v[184:187], v[120:123], v[80:83]\n\t"
    "ds_read_b128 v[112:115], %[ea] offset:20480\n\t"
    "s_add_u32 m0, %[lb], 28672\n\t"
    "v_mfma_f32_16x16x32_bf16 v[84:87], v[188:191], v[120:123], v[84:87]\n\t"
    "ds_read_b128 v[116:119], %[ea] offset:22528\n\t"
    "global_load_lds_dwordx4 %[av6], s[38:39] sc1\n\t"
    "v_mfma_f32_16x16x32_bf16 v[88:91], v[184:187], v[124:127], v[88:91]\n\t"
    "v_mfma_f32_16x16x32_bf16 v[92:95], v[188:191], v[124:127], v[92:95]\n\t"
    "s_waitcnt lgkmcnt(4)\n\t"
    "v_mfma_f32_16x16x32_bf16 v[80:83], v[192:195], v[96:99], v[80:83]\n\t"
    "ds_read_b128 v[120:123], %[da] offset:20480\n\t"
    "v_mfma_f32_16x16x32_bf16 v[84:87], v[196:199], v[96:99], v[84:87]\n\t"
    "ds_read_b128 v[124:127], %[da] offset:22528\n\t"
    "v_mfma_f32_16x16x32_bf16 v[88:91], v[192:195], v[100:103], v[88:91]\n\t"
    "v_mfma_f32_16x16x32_bf16 v[92:95], v[196:199], v[100:103], v[92:95]\n\t"
    "s_add_u32 s36, s36, %[ab]\n\t"
    "s_addc_u32 s37, s37, 0\n\t"
    "s_add_u32 s38, s38, %[ab]\n\t"
    "s_addc_u32 s39, s39, 0\n\t"
    "s_xor_b32 %[lb], %[lb], 0x10000\n\t"
    "s_waitcnt vmcnt(8)\n\t"
    "s_barrier\n\t"
    "s_waitcnt lgkmcnt(4)\n\t"
    "v_mfma_f32_16x16x32_bf16 v[80:83], v[200:203], v[104:107], v[80:83]\n\t"
    "ds_read_b128 v[96:99], %[ea] offset:24576\n\t"
    "v_mfma_f32_16x16x32_bf16 v[84:87], v[204:207], v[104:107], v[84:87]\n\t"
    "ds_read_b128 v[100:103], %[ea] offset:26624\n\t"
    "v_mfma_f32_16x16x32_bf16 v[88:91], v[200:203], v[108:111], v[88:91]\n\t"
    "v_mfma_f32_16x16x32_bf16 v[92:95], v[204:207], v[108:111], v[92:95]\n\t"
    "s_cmp_eq_u32 %[sl], 0\n\t"
    "s_cbranch_scc1 74f\n\t"
    "s_mov_b32 m0, %[sl]\n\t"
    "s_nop 0\n\t"
    "global_load_lds_dword %[dv], %[db] sc1\n\t"
    "74:\n\t"
    "s_waitcnt lgkmcnt(4)\n\t"
    "v_mfma_f32_16x16x32_bf16 v[80:83], v[208:211], v[112:115], v[80:83]\n\t"
    "ds_read_b128 v[104:107], %[da] offset:24576\n\t"
    "v_mfma_f32_16x16x32_bf16 v[84:87], v[212:215], v[112:115], v[84:87]\n\t"
    "ds_read_b128 v[108:111], %[da] offset:26624\n\t"
    "v_mfma_f32_16x16x32_bf16 v[88:91], v[208:211], v[116:119], v[88:91]\n\t"
    "v_mfma_f32_16x16x32_bf16 v[92:95], v[212:215], v[116:119], v[92:95]\n\t"
    "s_waitcnt lgkmcnt(4)\n\t"
    "v_mfma_f32_16x16x32_bf16 v[80:83], v[216:219], v[120:123], v[80:83]\n\t"
    "ds_read_b128 v[112:115], %[ea] offset:28672\n\t"
    "v_mfma_f32_16x16x32_bf16 v[84:87], v[220:223], v[120:123], v[84:87]\n\t"
    "ds_read_b128 v[116:119], %[ea] offset:30720\n\t"
    "v_mfma_f32_16x16x32_bf16 v[88:91], v[216:219], v[124:127], v[88:91]\n\t"
    "v_mfma_f32_16x16x32_bf16 v[92:95], v[220:223], v[124:127], v[92:95]\n\t"
    "s_waitcnt lgkmcnt(4)\n\t"
    "v_mfma_f32_16x16x32_bf16 v[80:83], v[224:227], v[96:99], v[80:83]\n\t"
    "ds_read_b128 v[120:123], %[da] offset:28672\n\t"
    "v_mfma_f32_16x16x32_bf16 v[84:87], v[228:231], v[96:99], v[84:87]\n\t"
    "ds_read_b128 v[124:127], %[da] offset:30720\n\t"
    "v_mfma_f32_16x16x32_bf16 v[88:91], v[224:227], v[100:103], v[88:91]\n\t"
    "v_mfma_f32_16x16x32_bf16 v[92:95], v[228:231], v[100:103], v[92:95]\n\t"
    "s_waitcnt lgkmcnt(4)\n\t"
    "v_mfma_f32_16x16x32_bf16 v[80:83], v[232:235], v[104:107], v[80:83]\n\t"
    "v_mfma_f32_16x16x32_bf16 v[84:87], v[236:239], v[104:107], v[84:87]\n\t"
    "v_mfma_f32_16x16x32_bf16 v[88:91], v[232:235], v[108:111], v[88:91]\n\t"
    "v_xor_b32 %[ea], 0x10000, %[ea]\n\t"
    "v_mfma_f32_16x16x32_bf16 v[92:95], v[236:239], v[108:111], v[92:95]\n\t"
    "v_xor_b32 %[da], 0x10000, %[da]\n\t"
    "s_waitcnt lgkmcnt(2)\n\t"
    "v_mfma_f32_16x16x32_bf16 v[80:83], v[240:243], v[112:115], v[80:83]\n\t"
    "v_mfma_f32_16x16x32_bf16 v[84:87], v[244:247], v[112:115], v[84:87]\n\t"
    "v_mfma_f32_16x16x32_bf16 v[88:91], v[240:243], v[116:119], v[88:91]\n\t"
    "s_add_u32 %[b], %[b], 1\n\t"
    "v_mfma_f32_16x16x32_bf16 v[92:95], v[244:247], v[116:119], v[92:95]\n\t"
    "s_waitcnt lgkmcnt(0)\n\t"
    "v_mfma_f32_16x16x32_bf16 v[80:83], v[248:251], v[120:123], v[80:83]\n\t"
    "v_mfma_f32_16x16x32_bf16 v[84:87], v[252:255], v[120:123], v[84:87]\n\t"
    "v_mfma_f32_16x16x32_bf16 v[88:91], v[248:251], v[124:127], v[88:91]\n\t"
    "v_mfma_f32_16x16x32_bf16 v[92:95], v[252:255], v[124:127], v[92:95]\n\t"
    "v_mov_b32 v76, %[b]\n\t"
    "v_or_b32 v76, 0x100, v76\n\t"
    "s_nop 2\n\t"
    "ds_write_b128 %[rw], v[80:83] offset:0\n\t"
    "ds_write_b128 %[rw], v[84:87] offset:1024\n\t"
    "ds_write_b128 %[rw], v[88:91] offset:2048\n\t"
    "ds_write_b128 %[rw], v[92:95] offset:3072\n\t"
    "ds_write_b32 %[fa], v76\n\t"
    "v_readfirstlane_b32 %[st], v75\n\t"
    "s_waitcnt vmcnt(0)\n\t"
    "ds_read2_b32 v[78:79], %[sa] offset1:32\n\t"
    "s_waitcnt lgkmcnt(0)\n\t"
    "v_max_u32 v78, v78, v79\n\t"
    "v_mad_u32_u24 v78, v78, %[mu], %[ad]\n\t"
    "v_max_i32 v77, v77, v78\n\t"
    "s_cmp_lg_u32 %[st], %[b]\n\t"
    "s_cbranch_scc1 3b\n\t"
    "s_barrier\n\t"
    "s_branch 9f\n\t"
    "32:\n\t"
    "ds_write_b32 %[fa], v76\n\t"
    "s_add_u32 m0, %[lb], 0\n\t"
    "s_nop 0\n\t"
    "global_load_lds_dwordx4 %[av0], s[36:37] sc1\n\t"
    "s_add_u32 m0, %[lb], 4096\n\t"
    "s_nop 0\n\t"
    "global_load_lds_dwordx4 %[av2], s[36:37] sc1\n\t"
    "s_add_u32 m0, %[lb], 8192\n\t"
    "s_nop 0\n\t"
    "global_load_lds_dwordx4 %[av4], s[36:37] sc1\n\t"
    "s_add_u32 m0, %[lb], 12288\n\t"
    "s_nop 0\n\t"
    "global_load_lds_dwordx4 %[av6], s[36:37] sc1\n\t"
    "s_add_u32 m0, %[lb], 16384\n\t"
    "s_nop 0\n\t"
    "global_load_lds_dwordx4 %[av0], s[38:39] sc1\n\t"
    "s_add_u32 m0, %[lb], 20480\n\t"
    "s_nop 0\n\t"
    "global_load_lds_dwordx4 %[av2], s[38:39] sc1\n\t"
    "s_add_u32 m0, %[lb], 24576\n\t"
    "s_nop 0\n\t"
    "global_load_lds_dwordx4 %[av4], s[38:39] sc1\n\t"
    "s_add_u32 m0, %[lb], 28672\n\t"
    "s_nop 0\n\t"
    "global_load_lds_dwordx4 %[av6], s[38:39] sc1\n\t"
    "s_add_u32 s36, s36, %[ab]\n\t"
    "s_addc_u32 s37, s37, 0\n\t"
    "s_add_u32 s38, s38, %[ab]\n\t"
    "s_addc_u32 s39, s39, 0\n\t"
    "s_xor_b32 %[lb], %[lb], 0x10000\n\t"
    "s_waitcnt lgkmcnt(0)\n\t"
    "s_barrier\n\t"
    "9:\n\t"
    "s_waitcnt lgkmcnt(0)\n\t"
    "s_nop 7"
    : [ea] "+&v"(ea), [da] "+&v"(da), [b] "+&s"(b), [st] "=&s"(st), "+{s[36:37]}"(p0), "+{s[38:39]}"(p1), [lb] "+&s"(lb), [cap] "+&s"(cap)
    : [ns] "s"(ns), [ab] "s"(ab), [rw] "v"(rw), [fa] "v"(fa), "{v[128:159]}"(W[0]), "{v[160:191]}"(W[1]), "{v[192:223]}"(W[2]), "{v[224:255]}"(W[3]), [bx] "s"(bx), [wm] "s"(wm), [rq] "s"(rq), [mu] "s"(mu), [ad] "v"(ad), [dv] "v"(dv), [db] "s"(db), [sl] "s"(sl), [sa] "v"(sa), [av0] "v"(av[0]), [av1] "v"(av[1]), [av2] "v"(av[2]), [av3] "v"(av[3]), [av4] "v"(av[4]), [av5] "v"(av[5]), [av6] "v"(av[6]), [av7] "v"(av[7])
    : "v74", "v75", "v76", "v77", "v78", "v79", "v80", "v81", "v82", "v83", "v84", "v85", "v86", "v87", "v88", "v89", "v90", "v91", "v92", "v93", "v94", "v95", "v96", "v97", "v98", "v99", "v100", "v101", "v102", "v103", "v104", "v105", "v106", "v107", "v108", "v109", "v110", "v111", "v112", "v113", "v114", "v115", "v116", "v117", "v118", "v119", "v120", "v121", "v122", "v123", "v124", "v125", "v126", "v127", "vcc", "scc", "m0", "memory");
}
__device__ __forceinline__ void paced_st0(const u32x32 (&W)[4], unsigned ea, unsigned da, int& b, int ns, unsigned ab, unsigned rw, unsigned fa,
    const char* p0, unsigned pp, int w0, int be, int bo, int bl, int po, unsigned cn, const char* pg, unsigned co0, unsigned co1, int& bnd) {
  unsigned st;
  asm volatile("1:\n\t"
    "s_barrier\n\t"
    "ds_read_b128 v[96:99], %[ea] offset:0\n\t"
    "ds_read_b128 v[100:103], %[ea] offset:2048\n\t"
    "ds_read_b128 v[104:107], %[da] offset:0\n\t"
    "ds_read_b128 v[108:111], %[da] offset:2048\n\t"
    "ds_read_b128 v[112:115], %[ea] offset:4096\n\t"
    "ds_read_b128 v[116:119], %[ea] offset:6144\n\t"
    "s_waitcnt lgkmcnt(4)\n\t"
    "v_mfma_f32_16x16x32_bf16 v[80:83], v[128:131], v[96:99], 0\n\t"
    "ds_read_b128 v[120:123], %[da] offset:4096\n\t"
    "v_mfma_f32_16x16x32_bf16 v[84:87], v[132:135], v[96:99], 0\n\t"
    "ds_read_b128 v[124:127], %[da] offset:6144\n\t"
    "v_mfma_f32_16x16x32_bf16 v[88:91], v[128:131], v[100:103], 0\n\t"
    "v_mfma_f32_16x16x32_bf16 v[92:95], v[132:135], v[100:103], 0\n\t"
    "s_barrier\n\t"
    "s_waitcnt lgkmcnt(4)\n\t"
    "v_mfma_f32_16x16x32_bf16 v[80:83], v[136:139], v[104:107], v[80:83]\n\t"
    "ds_read_b128 v[96:99], %[ea] offset:8192\n\t"
    "v_mfma_f32_16x16x32_bf16 v[84:87], v[140:143], v[104:107], v[84:87]\n\t"
    "ds_read_b128 v[100:103], %[ea] offset:10240\n\t"
    "v_mfma_f32_16x16x32_bf16 v[88:91], v[136:139], v[108:111], v[88:91]\n\t"
    "v_mfma_f32_16x16x32_bf16 v[92:95], v[140:143], v[108:111], v[92:95]\n\t"
    "s_waitcnt lgkmcnt(4)\n\t"
    "v_mfma_f32_16x16x32_bf16 v[80:83], v[144:147], v[112:115], v[80:83]\n\t"
    "ds_read_b128 v[104:107], %[da] offset:8192\n\t"
    "v_mfma_f32_16x16x32_bf16 v[84:87], v[148:151], v[112:115], v[84:87]\n\t"
    "ds_read_b128 v[108:111], %[da] offset:10240\n\t"
    "v_mfma_f32_16x16x32_bf16 v[88:91], v[144:147], v[116:119], v[88:91]\n\t"
    "v_mfma_f32_16x16x32_bf16 v[92:95], v[148:151], v[116:119], v[92:95]\n\t"
    "s_waitcnt lgkmcnt(4)\n\t"
    "v_mfma_f32_16x16x32_bf16 v[80:83], v[152:155], v[120:123], v[80:83]\n\t"
    "ds_read_b128 v[112:115], %[ea] offset:12288\n\t"
    "v_mfma_f32_16x16x32_bf16 v[84:87], v[156:159], v[120:123], v[84:87]\n\t"
    "ds_read_b128 v[116:119], %[ea] offset:14336\n\t"
    "v_mfma_f32_16x16x32_bf16 v[88:91], v[152:155], v[124:127], v[88:91]\n\t"
    "v_mfma_f32_16x16x32_bf16 v[92:95], v[156:159], v[124:127], v[92:95]\n\t"
    "s_cmp_lg_u32 %[pp], 0\n\t"
    "s_cbranch_scc1 7f\n\t"
    "8:\n\t"
    "s_waitcnt lgkmcnt(4)\n\t"
    "v_mfma_f32_16x16x32_bf16 v[80:83], v[160:163], v[96:99], v[80:83]\n\t"
    "ds_read_b128 v[120:123], %[da] offset:12288\n\t"
    "v_mfma_f32_16x16x32_bf16 v[84:87], v[164:167], v[96:99], v[84:87]\n\t"
    "ds_read_b128 v[124:127], %[da] offset:14336\n\t"
    "v_mfma_f32_16x16x32_bf16 v[88:91], v[160:163], v[100:103], v[88:91]\n\t"
    "v_mfma_f32_16x16x32_bf16 v[92:95], v[164:167], v[100:103], v[92:95]\n\t"
    "s_barrier\n\t"
    "s_waitcnt lgkmcnt(4)\n\t"
    "v_mfma_f32_16x16x32_bf16 v[80:83], v[168:171], v[104:107], v[80:83]\n\t"
    "ds_read_b128 v[96:99], %[ea] offset:16384\n\t"
    "v_mfma_f32_16x16x32_bf16 v[84:87], v[172:175], v[104:107], v[84:87]\n\t"
    "ds_read_b128 v[100:103], %[ea] offset:18432\n\t"
    "v_mfma_f32_16x16x32_bf16 v[88:91], v[168:171], v[108:111], v[88:91]\n\t"
    "v_mfma_f32_16x16x32_bf16 v[92:95], v[172:175], v[108:111], v[92:95]\n\t"
    "s_waitcnt lgkmcnt(4)\n\t"
    "v_mfma_f32_16x16x32_bf16 v[80:83], v[176:179], v[112:115], v[80:83]\n\t"
    "ds_read_b128 v[104:107], %[da] offset:16384\n\t"
    "v_mfma_f32_16x16x32_bf16 v[84:87], v[180:183], v[112:115], v[84:87]\n\t"
    "ds_read_b128 v[108:111], %[da] offset:18432\n\t"
    "v_mfma_f32_16x16x32_bf16 v[88:91], v[176:179], v[116:119], v[88:91]\n\t"
    "v_mfma_f32_16x16x32_bf16 v[92:95], v[180:183], v[116:119], v[92:95]\n\t"
    "s_waitcnt lgkmcnt(4)\n\t"
    "v_mfma_f32_16x16x32_bf16 v[80:83], v[184:187], v[120:123], v[80:83]\n\t"
    "ds_read_b128 v[112:115], %[ea] offset:20480\n\t"
    "v_mfma_f32_16x16x32_bf16 v[84:87], v[188:191], v[120:123], v[84:87]\n\t"
    "ds_read_b128 v[116:119], %[ea] offset:22528\n\t"
    "v_mfma_f32_16x16x32_bf16 v[88:91], v[184:187], v[124:127], v[88:91]\n\t"
    "v_mfma_f32_16x16x32_bf16 v[92:95], v[188:191], v[124:127], v[92:95]\n\t"
    "s_waitcnt lgkmcnt(4)\n\t"
    "v_mfma_f32_16x16x32_bf16 v[80:83], v[192:195], v[96:99], v[80:83]\n\t"
    "ds_read_b128 v[120:123], %[da] offset:20480\n\t"
    "v_mfma_f32_16x16x32_bf16 v[84:87], v[196:199], v[96:99], v[84:87]\n\t"
    "ds_read_b128 v[124:127], %[da] offset:22528\n\t"
    "v_mfma_f32_16x16x32_bf16 v[88:91], v[192:195], v[100:103], v[88:91]\n\t"
    "v_mfma_f32_16x16x32_bf16 v[92:95], v[196:199], v[100:103], v[92:95]\n\t"
    "s_waitcnt vmcnt(0)\n\t"
    "s_barrier\n\t"
    "s_waitcnt lgkmcnt(4)\n\t"
    "v_mfma_f32_16x16x32_bf16 v[80:83], v[200:203], v[104:107], v[80:83]\n\t"
    "ds_read_b128 v[96:99], %[ea] offset:24576\n\t"
    "v_mfma_f32_16x16x32_bf16 v[84:87], v[204:207], v[104:107], v[84:87]\n\t"
    "ds_read_b128 v[100:103], %[ea] offset:26624\n\t"
    "v_mfma_f32_16x16x32_bf16 v[88:91], v[200:203], v[108:111], v[88:91]\n\t"
    "v_mfma_f32_16x16x32_bf16 v[92:95], v[204:207], v[108:111], v[92:95]\n\t"
    "s_waitcnt lgkmcnt(4)\n\t"
    "v_mfma_f32_16x16x32_bf16 v[80:83], v[208:211], v[112:115], v[80:83]\n\t"
    "ds_read_b128 v[104:107], %[da] offset:24576\n\t"
    "v_mfma_f32_16x16x32_bf16 v[84:87], v[212:215], v[112:115], v[84:87]\n\t"
    "ds_read_b128 v[108:111], %[da] offset:26624\n\t"
    "v_mfma_f32_16x16x32_bf16 v[88:91], v[208:211], v[116:119], v[88:91]\n\t"
    "v_mfma_f32_16x16x32_bf16 v[92:95], v[212:215], v[116:119], v[92:95]\n\t"
    "s_waitcnt lgkmcnt(4)\n\t"
    "v_mfma_f32_16x16x32_bf16 v[80:83], v[216:219], v[120:123], v[80:83]\n\t"
    "ds_read_b128 v[112:115], %[ea] offset:28672\n\t"
    "v_mfma_f32_16x16x32_bf16 v[84:87], v[220:223], v[120:123], v[84:87]\n\t"
    "ds_read_b128 v[116:119], %[ea] offset:30720\n\t"
    "v_mfma_f32_16x16x32_bf16 v[88:91], v[216:219], v[124:127], v[88:91]\n\t"
    "v_mfma_f32_16x16x32_bf16 v[92:95], v[220:223], v[124:127], v[92:95]\n\t"
    "s_waitcnt lgkmcnt(4)\n\t"
    "v_mfma_f32_16x16x32_bf16 v[80:83], v[224:227], v[96:99], v[80:83]\n\t"
    "ds_read_b128 v[120:123], %[da] offset:28672\n\t"
    "v_mfma_f32_16x16x32_bf16 v[84:87], v[228:231], v[96:99], v[84:87]\n\t"
    "ds_read_b128 v[124:127], %[da] offset:30720\n\t"
    "v_mfma_f32_16x16x32_bf16 v[88:91], v[224:227], v[100:103], v[88:91]\n\t"
    "v_mfma_f32_16x16x32_bf16 v[92:95], v[228:231], v[100:103], v[92:95]\n\t"
    "s_cmp_ge_i32 %[bl], %[w0]\n\t"
    "s_cbranch_scc1 57f\n\t"
    "s_add_u32 %[st], %[b], 1\n\t"
    "s_cmp_le_i32 %[st], %[bl]\n\t"
    "s_cbranch_scc1 57f\n\t"
    "s_cmp_gt_i32 %[st], %[bo]\n\t"
    "s_mov_b64 exec, 1\n\t"
    "v_mov_b32 v72, 0\n\t"
    "v_mov_b32 v74, %[st]\n\t"
    "s_cselect_b32 %[st], %[st], 0\n\t"
    "v_add_u32 v74, -1, v74\n\t"
    "v_mov_b32 v75, %[st]\n\t"
    "global_store_dwordx2 v72, v[74:75], %[pg] sc1\n\t"
    "s_mov_b64 exec, -1\n\t"
    "57:\n\t"
    "s_waitcnt lgkmcnt(4)\n\t"
    "v_mfma_f32_16x16x32_bf16 v[80:83], v[232:235], v[104:107], v[80:83]\n\t"
    "v_mfma_f32_16x16x32_bf16 v[84:87], v[236:239], v[104:107], v[84:87]\n\t"
    "v_mfma_f32_16x16x32_bf16 v[88:91], v[232:235], v[108:111], v[88:91]\n\t"
    "v_xor_b32 %[ea], 0x10000, %[ea]\n\t"
    "v_mfma_f32_16x16x32_bf16 v[92:95], v[236:239], v[108:111], v[92:95]\n\t"
    "v_xor_b32 %[da], 0x10000, %[da]\n\t"
    "s_waitcnt lgkmcnt(2)\n\t"
    "v_mfma_f32_16x16x32_bf16 v[80:83], v[240:243], v[112:115], v[80:83]\n\t"
    "v_mfma_f32_16x16x32_bf16 v[84:87], v[244:247], v[112:115], v[84:87]\n\t"
    "v_mfma_f32_16x16x32_bf16 v[88:91], v[240:243], v[116:119], v[88:91]\n\t"
    "s_add_u32 %[b], %[b], 1\n\t"
    "v_mfma_f32_16x16x32_bf16 v[92:95], v[244:247], v[116:119], v[92:95]\n\t"
    "s_waitcnt lgkmcnt(0)\n\t"
    "v_mfma_f32_16x16x32_bf16 v[80:83], v[248:251], v[120:123], v[80:83]\n\t"
    "v_mfma_f32_16x16x32_bf16 v[84:87], v[252:255], v[120:123], v[84:87]\n\t"
    "v_mfma_f32_16x16x32_bf16 v[88:91], v[248:251], v[124:127], v[88:91]\n\t"
    "v_mfma_f32_16x16x32_bf16 v[92:95], v[252:255], v[124:127], v[92:95]\n\t"
    "ds_read_b32 v38, %[fa] offset:32\n\t"
    "2:\n\t"
    "ds_read_b32 v39, %[fa]\n\t"
    "ds_read_b128 v[48:51], %[rw] offset:0\n\t"
    "ds_read_b128 v[56:59], %[rw] offset:2048\n\t"
    "ds_read_b128 v[52:55], %[rw] offset:1024\n\t"
    "ds_read_b128 v[60:63], %[rw] offset:3072\n\t"
    "s_waitcnt lgkmcnt(2)\n\t"
    "v_readfirstlane_b32 %[pp], v39\n\t"
    "s_and_b32 %[st], %[pp], 0xff\n\t"
    "s_cmp_eq_u32 %[st], %[b]\n\t"
    "s_cbranch_scc1 3f\n\t"
    "s_waitcnt lgkmcnt(0)\n\t"
    "s_branch 2b\n\t"
    "3:\n\t"
    "v_add_f32 v48, v48, v80\n\t"
    "v_add_f32 v49, v49, v81\n\t"
    "v_add_f32 v50, v50, v82\n\t"
    "v_add_f32 v51, v51, v83\n\t"
    "v_add_f32 v56, v56, v88\n\t"
    "v_add_f32 v57, v57, v89\n\t"
    "v_add_f32 v58, v58, v90\n\t"
    "v_add_f32 v59, v59, v91\n\t"
    "v_cmp_u_f32 vcc, v48, v56\n\t"
    "s_waitcnt lgkmcnt(0)\n\t"
    "v_add_f32 v52, v52, v84\n\t"
    "v_add_f32 v53, v53, v85\n\t"
    "v_add_f32 v54, v54, v86\n\t"
    "v_add_f32 v55, v55, v87\n\t"
    "v_add_f32 v60, v60, v92\n\t"
    "v_add_f32 v61, v61, v93\n\t"
    "v_add_f32 v62, v62, v94\n\t"
    "v_add_f32 v63, v63, v95\n\t"
    "s_cmp_lg_u64 vcc, 0\n\t"
    "s_cselect_b32 %[st], %[b], 0\n\t"
    "s_cmp_gt_i32 %[st], %[be]\n\t"
    "s_cbranch_scc1 4f\n\t"
    "v_cvt_pk_bf16_f32 v40, v48, v49\n\t"
    "v_cvt_pk_bf16_f32 v41, v50, v51\n\t"
    "v_cvt_pk_bf16_f32 v42, v52, v53\n\t"
    "v_cvt_pk_bf16_f32 v43, v54, v55\n\t"
    "global_store_dwordx4 %[co0], v[40:43], s[36:37] sc1\n\t"
    "v_cvt_pk_bf16_f32 v44, v56, v57\n\t"
    "v_cvt_pk_bf16_f32 v45, v58, v59\n\t"
    "v_cvt_pk_bf16_f32 v46, v60, v61\n\t"
    "v_cvt_pk_bf16_f32 v47, v62, v63\n\t"
    "global_store_dwordx4 %[co1], v[44:47], s[36:37] sc1\n\t"
    "v_readfirstlane_b32 %[st], v38\n\t"
    "s_add_u32 s36, s36, %[ab]\n\t"
    "s_addc_u32 s37, s37, 0\n\t"
    "s_cmp_eq_u32 %[st], %[b]\n\t"
    "s_cbranch_scc1 33f\n\t"
    "s_cmp_le_i32 %[b], %[bl]\n\t"
    "s_cbranch_scc1 5f\n\t"
    "s_mov_b32 %[pp], 0\n\t"
    "s_cmp_lt_i32 %[b], %[ns]\n\t"
    "s_cbranch_scc1 1b\n\t"
    "33:\n\t"
    "s_barrier\n\t"
    "s_branch 9f\n\t"
    "5:\n\t"
    "s_lshr_b32 %[pp], %[pp], 8\n\t"
    "s_cbranch_scc1 1b\n\t"
    "v_mov_b32 v72, %[cn]\n\t"
    "v_mov_b32 v73, 1\n\t"
    "s_waitcnt vmcnt(0)\n\t"
    "s_mov_b64 exec, 1\n\t"
    "ds_add_rtn_u32 v73, v72, v73\n\t"
    "s_add_u32 %[st], %[b], %[po]\n\t"
    "v_mov_b32 v75, %[st]\n\t"
    "v_add_u32 v74, 0, v75\n\t"
    "v_mov_b32 v72, 0\n\t"
    "s_waitcnt lgkmcnt(0)\n\t"
    "v_readfirstlane_b32 %[st], v73\n\t"
    "s_and_b32 %[st], %[st], 3\n\t"
    "s_cmp_lg_u32 %[st], 3\n\t"
    "s_cbranch_scc1 61f\n\t"
    "global_store_dword v72, v74, %[pg] sc1\n\t"
    "61:\n\t"
    "s_mov_b64 exec, -1\n\t"
    "s_mov_b32 %[pp], 0\n\t"
    "s_cmp_lt_i32 %[b], %[ns]\n\t"
    "s_cbranch_scc1 1b\n\t"
    "s_barrier\n\t"
    "s_branch 9f\n\t"
    "4:\n\t"
    "v_mov_b32 v39, %[b]\n\t"
    "ds_write_b32 %[fa], v39 offset:16\n\t"
    "s_waitcnt lgkmcnt(0)\n\t"
    "s_barrier\n\t"
    "s_branch 9f\n\t"
    "7:\n\t"
    "v_mov_b32 v72, %[cn]\n\t"
    "v_mov_b32 v73, 1\n\t"
    "s_waitcnt vmcnt(0)\n\t"
    "s_mov_b64 exec, 1\n\t"
    "ds_add_rtn_u32 v73, v72, v73\n\t"
    "s_add_u32 %[st], %[b], %[po]\n\t"
    "v_mov_b32 v75, %[st]\n\t"
    "v_add_u32 v74, 0, v75\n\t"
    "v_mov_b32 v72, 0\n\t"
    "s_waitcnt lgkmcnt(0)\n\t"
    "v_readfirstlane_b32 %[st], v73\n\t"
    "s_and_b32 %[st], %[st], 3\n\t"
    "s_cmp_lg_u32 %[st], 3\n\t"
    "s_cbranch_scc1 61f\n\t"
    "global_store_dword v72, v74, %[pg] sc1\n\t"
    "61:\n\t"
    "s_mov_b64 exec, -1\n\t"
    "s_mov_b32 %[pp], 0\n\t"
    "s_branch 8b\n\t"
    "9:\n\t"
    "s_waitcnt lgkmcnt(0)"
    : [ea] "+&v"(ea), [da] "+&v"(da), [b] "+&s"(b), [st] "=&s"(st), "+{s[36:37]}"(p0), [pp] "+&s"(pp)
    : [ns] "s"(ns), [ab] "s"(ab), [rw] "v"(rw), [fa] "v"(fa), "{v[128:159]}"(W[0]), "{v[160:191]}"(W[1]), "{v[192:223]}"(W[2]), "{v[224:255]}"(W[3]), [w0] "s"(w0), [be] "s"(be), [bo] "s"(bo), [bl] "s"(bl), [po] "s"(po), [cn] "s"(cn), [pg] "s"(pg), [co0] "v"(co0), [co1] "v"(co1)
    : "v38", "v39", "v40", "v41", "v42", "v43", "v44", "v45", "v46", "v47", "v48", "v49", "v50", "v51", "v52", "v53", "v54", "v55", "v56", "v57", "v58", "v59", "v60", "v61", "v62", "v63", "v72", "v73", "v74", "v75", "v80", "v81", "v82", "v83", "v84", "v85", "v86", "v87", "v88", "v89", "v90", "v91", "v92", "v93", "v94", "v95", "v96", "v97", "v98", "v99", "v100", "v101", "v102", "v103", "v104", "v105", "v106", "v107", "v108", "v109", "v110", "v111", "v112", "v113", "v114", "v115", "v116", "v117", "v118", "v119", "v120", "v121", "v122", "v123", "v124", "v125", "v126", "v127", "vcc", "scc", "m0", "memory");
}
__device__ __forceinline__ void paced_st1(const u32x32 (&W)[4], unsigned ea, unsigned da, int& b, int ns, unsigned ab, unsigned rw, unsigned fa,
    const char* p0, unsigned pp, int w0, int be, int bo, int bl, int po, unsigned cn, const char* pg, unsigned co0, unsigned co1, int& bnd) {
  unsigned st;
  asm volatile("1:\n\t"
    "s_barrier\n\t"
    "ds_read_b128 v[96:99], %[ea] offset:0\n\t"
    "ds_read_b128 v[100:103], %[ea] offset:2048\n\t"
    "ds_read_b128 v[104:107], %[da] offset:0\n\t"
    "ds_read_b128 v[108:111], %[da] offset:2048\n\t"
    "ds_read_b128 v[112:115], %[ea] offset:4096\n\t"
    "ds_read_b128 v[116:119], %[ea] offset:6144\n\t"
    "s_waitcnt lgkmcnt(4)\n\t"
    "v_mfma_f32_16x16x32_bf16 v[80:83], v[128:131], v[96:99], 0\n\t"
    "ds_read_b128 v[120:123], %[da] offset:4096\n\t"
    "v_mfma_f32_16x16x32_bf16 v[84:87], v[132:135], v[96:99], 0\n\t"
    "ds_read_b128 v[124:127], %[da] offset:6144\n\t"
    "v_mfma_f32_16x16x32_bf16 v[88:91], v[128:131], v[100:103], 0\n\t"
    "v_mfma_f32_16x16x32_bf16 v[92:95], v[132:135], v[100:103], 0\n\t"
    "s_barrier\n\t"
    "s_waitcnt lgkmcnt(4)\n\t"
    "v_mfma_f32_16x16x32_bf16 v[80:83], v[136:139], v[104:107], v[80:83]\n\t"
    "ds_read_b128 v[96:99], %[ea] offset:8192\n\t"
    "v_mfma_f32_16x16x32_bf16 v[84:87], v[140:143], v[104:107], v[84:87]\n\t"
    "ds_read_b128 v[100:103], %[ea] offset:10240\n\t"
    "v_mfma_f32_16x16x32_bf16 v[88:91], v[136:139], v[108:111], v[88:91]\n\t"
    "v_mfma_f32_16x16x32_bf16 v[92:95], v[140:143], v[108:111], v[92:95]\n\t"
    "s_waitcnt lgkmcnt(4)\n\t"
    "v_mfma_f32_16x16x32_bf16 v[80:83], v[144:147], v[112:115], v[80:83]\n\t"
    "ds_read_b128 v[104:107], %[da] offset:8192\n\t"
    "v_mfma_f32_16x16x32_bf16 v[84:87], v[148:151], v[112:115], v[84:87]\n\t"
    "ds_read_b128 v[108:111], %[da] offset:10240\n\t"
    "v_mfma_f32_16x16x32_bf16 v[88:91], v[144:147], v[116:119], v[88:91]\n\t"
    "v_mfma_f32_16x16x32_bf16 v[92:95], v[148:151], v[116:119], v[92:95]\n\t"
    "s_waitcnt lgkmcnt(4)\n\t"
    "v_mfma_f32_16x16x32_bf16 v[80:83], v[152:155], v[120:123], v[80:83]\n\t"
    "ds_read_b128 v[112:115], %[ea] offset:12288\n\t"
    "v_mfma_f32_16x16x32_bf16 v[84:87], v[156:159], v[120:123], v[84:87]\n\t"
    "ds_read_b128 v[116:119], %[ea] offset:14336\n\t"
    "v_mfma_f32_16x16x32_bf16 v[88:91], v[152:155], v[124:127], v[88:91]\n\t"
    "v_mfma_f32_16x16x32_bf16 v[92:95], v[156:159], v[124:127], v[92:95]\n\t"
    "s_cmp_lg_u32 %[pp], 0\n\t"
    "s_cbranch_scc1 7f\n\t"
    "8:\n\t"
    "s_waitcnt lgkmcnt(4)\n\t"
    "v_mfma_f32_16x16x32_bf16 v[80:83], v[160:163], v[96:99], v[80:83]\n\t"
    "ds_read_b128 v[120:123], %[da] offset:12288\n\t"
    "v_mfma_f32_16x16x32_bf16 v[84:87], v[164:167], v[96:99], v[84:87]\n\t"
    "ds_read_b128 v[124:127], %[da] offset:14336\n\t"
    "v_mfma_f32_16x16x32_bf16 v[88:91], v[160:163], v[100:103], v[88:91]\n\t"
    "v_mfma_f32_16x16x32_bf16 v[92:95], v[164:167], v[100:103], v[92:95]\n\t"
    "s_barrier\n\t"
    "s_waitcnt lgkmcnt(4)\n\t"
    "v_mfma_f32_16x16x32_bf16 v[80:83], v[168:171], v[104:107], v[80:83]\n\t"
    "ds_read_b128 v[96:99], %[ea] offset:16384\n\t"
    "v_mfma_f32_16x16x32_bf16 v[84:87], v[172:175], v[104:107], v[84:87]\n\t"
    "ds_read_b128 v[100:103], %[ea] offset:18432\n\t"
    "v_mfma_f32_16x16x32_bf16 v[88:91], v[168:171], v[108:111], v[88:91]\n\t"
    "v_mfma_f32_16x16x32_bf16 v[92:95], v[172:175], v[108:111], v[92:95]\n\t"
    "s_waitcnt lgkmcnt(4)\n\t"
    "v_mfma_f32_16x16x32_bf16 v[80:83], v[176:179], v[112:115], v[80:83]\n\t"
    "ds_read_b128 v[104:107], %[da] offset:16384\n\t"
    "v_mfma_f32_16x16x32_bf16 v[84:87], v[180:183], v[112:115], v[84:87]\n\t"
    "ds_read_b128 v[108:111], %[da] offset:18432\n\t"
    "v_mfma_f32_16x16x32_bf16 v[88:91], v[176:179], v[116:119], v[88:91]\n\t"
    "v_mfma_f32_16x16x32_bf16 v[92:95], v[180:183], v[116:119], v[92:95]\n\t"
    "s_waitcnt lgkmcnt(4)\n\t"
    "v_mfma_f32_16x16x32_bf16 v[80:83], v[184:187], v[120:123], v[80:83]\n\t"
    "ds_read_b128 v[112:115], %[ea] offset:20480\n\t"
    "v_mfma_f32_16x16x32_bf16 v[84:87], v[188:191], v[120:123], v[84:87]\n\t"
    "ds_read_b128 v[116:119], %[ea] offset:22528\n\t"
    "v_mfma_f32_16x16x32_bf16 v[88:91], v[184:187], v[124:127], v[88:91]\n\t"
    "v_mfma_f32_16x16x32_bf16 v[92:95], v[188:191], v[124:127], v[92:95]\n\t"
    "s_waitcnt lgkmcnt(4)\n\t"
    "v_mfma_f32_16x16x32_bf16 v[80:83], v[192:195], v[96:99], v[80:83]\n\t"
    "ds_read_b128 v[120:123], %[da] offset:20480\n\t"
    "v_mfma_f32_16x16x32_bf16 v[84:87], v[196:199], v[96:99], v[84:87]\n\t"
    "ds_read_b128 v[124:127], %[da] offset:22528\n\t"
    "v_mfma_f32_16x16x32_bf16 v[88:91], v[192:195], v[100:103], v[88:91]\n\t"
    "v_mfma_f32_16x16x32_bf16 v[92:95], v[196:199], v[100:103], v[92:95]\n\t"
    "s_waitcnt vmcnt(0)\n\t"
    "s_barrier\n\t"
    "s_waitcnt lgkmcnt(4)\n\t"
    "v_mfma_f32_16x16x32_bf16 v[80:83], v[200:203], v[104:107], v[80:83]\n\t"
    "ds_read_b128 v[96:99], %[ea] offset:24576\n\t"
    "v_mfma_f32_16x16x32_bf16 v[84:87], v[204:207], v[104:107], v[84:87]\n\t"
    "ds_read_b128 v[100:103], %[ea] offset:26624\n\t"
    "v_mfma_f32_16x16x32_bf16 v[88:91], v[200:203], v[108:111], v[88:91]\n\t"
    "v_mfma_f32_16x16x32_bf16 v[92:95], v[204:207], v[108:111], v[92:95]\n\t"
    "s_waitcnt lgkmcnt(4)\n\t"
    "v_mfma_f32_16x16x32_bf16 v[80:83], v[208:211], v[112:115], v[80:83]\n\t"
    "ds_read_b128 v[104:107], %[da] offset:24576\n\t"
    "v_mfma_f32_16x16x32_bf16 v[84:87], v[212:215], v[112:115], v[84:87]\n\t"
    "ds_read_b128 v[108:111], %[da] offset:26624\n\t"
    "v_mfma_f32_16x16x32_bf16 v[88:91], v[208:211], v[116:119], v[88:91]\n\t"
    "v_mfma_f32_16x16x32_bf16 v[92:95], v[212:215], v[116:119], v[92:95]\n\t"
    "s_waitcnt lgkmcnt(4)\n\t"
    "v_mfma_f32_16x16x32_bf16 v[80:83], v[216:219], v[120:123], v[80:83]\n\t"
    "ds_read_b128 v[112:115], %[ea] offset:28672\n\t"
    "v_mfma_f32_16x16x32_bf16 v[84:87], v[220:223], v[120:123], v[84:87]\n\t"
    "ds_read_b128 v[116:119], %[ea] offset:30720\n\t"
    "v_mfma_f32_16x16x32_bf16 v[88:91], v[216:219], v[124:127], v[88:91]\n\t"
    "v_mfma_f32_16x16x32_bf16 v[92:95], v[220:223], v[124:127], v[92:95]\n\t"
    "s_waitcnt lgkmcnt(4)\n\t"
    "v_mfma_f32_16x16x32_bf16 v[80:83], v[224:227], v[96:99], v[80:83]\n\t"
    "ds_read_b128 v[120:123], %[da] offset:28672\n\t"
    "v_mfma_f32_16x16x32_bf16 v[84:87], v[228:231], v[96:99], v[84:87]\n\t"
    "ds_read_b128 v[124:127], %[da] offset:30720\n\t"
    "v_mfma_f32_16x16x32_bf16 v[88:91], v[224:227], v[100:103], v[88:91]\n\t"
    "v_mfma_f32_16x16x32_bf16 v[92:95], v[228:231], v[100:103], v[92:95]\n\t"
    "s_waitcnt lgkmcnt(4)\n\t"
    "v_mfma_f32_16x16x32_bf16 v[80:83], v[232:235], v[104:107], v[80:83]\n\t"
    "v_mfma_f32_16x16x32_bf16 v[84:87], v[236:239], v[104:107], v[84:87]\n\t"
    "v_mfma_f32_16x16x32_bf16 v[88:91], v[232:235], v[108:111], v[88:91]\n\t"
    "v_xor_b32 %[ea], 0x10000, %[ea]\n\t"
    "v_mfma_f32_16x16x32_bf16 v[92:95], v[236:239], v[108:111], v[92:95]\n\t"
    "v_xor_b32 %[da], 0x10000, %[da]\n\t"
    "s_waitcnt lgkmcnt(2)\n\t"
    "v_mfma_f32_16x16x32_bf16 v[80:83], v[240:243], v[112:115], v[80:83]\n\t"
    "v_mfma_f32_16x16x32_bf16 v[84:87], v[244:247], v[112:115], v[84:87]\n\t"
    "v_mfma_f32_16x16x32_bf16 v[88:91], v[240:243], v[116:119], v[88:91]\n\t"
    "s_add_u32 %[b], %[b], 1\n\t"
    "v_mfma_f32_16x16x32_bf16 v[92:95], v[244:247], v[116:119], v[92:95]\n\t"
    "s_cmp_ge_i32 %[bl], %[w0]\n\t"
    "s_cbranch_scc1 57f\n\t"
    "s_add_u32 %[st], %[b], 0\n\t"
    "s_cmp_le_i32 %[st], %[bl]\n\t"
    "s_cbranch_scc1 57f\n\t"
    "s_cmp_gt_i32 %[st], %[bo]\n\t"
    "s_mov_b64 exec, 1\n\t"
    "v_mov_b32 v72, 0\n\t"
    "v_mov_b32 v74, %[st]\n\t"
    "s_cselect_b32 %[st], %[st], 0\n\t"
    "v_add_u32 v74, -1, v74\n\t"
    "v_mov_b32 v75, %[st]\n\t"
    "global_store_dwordx2 v72, v[74:75], %[pg] sc1\n\t"
    "s_mov_b64 exec, -1\n\t"
    "57:\n\t"
    "s_waitcnt lgkmcnt(0)\n\t"
    "v_mfma_f32_16x16x32_bf16 v[80:83], v[248:251], v[120:123], v[80:83]\n\t"
    "v_mfma_f32_16x16x32_bf16 v[84:87], v[252:255], v[120:123], v[84:87]\n\t"
    "v_mfma_f32_16x16x32_bf16 v[88:91], v[248:251], v[124:127], v[88:91]\n\t"
    "v_mfma_f32_16x16x32_bf16 v[92:95], v[252:255], v[124:127], v[92:95]\n\t"
    "ds_read_b32 v38, %[fa] offset:32\n\t"
    "2:\n\t"
    "ds_read_b32 v39, %[fa]\n\t"
    "ds_read_b128 v[48:51], %[rw] offset:0\n\t"
    "ds_read_b128 v[56:59], %[rw] offset:2048\n\t"
    "ds_read_b128 v[52:55], %[rw] offset:1024\n\t"
    "ds_read_b128 v[60:63], %[rw] offset:3072\n\t"
    "s_waitcnt lgkmcnt(2)\n\t"
    "v_readfirstlane_b32 %[pp], v39\n\t"
    "s_and_b32 %[st], %[pp], 0xff\n\t"
    "s_cmp_eq_u32 %[st], %[b]\n\t"
    "s_cbranch_scc1 3f\n\t"
    "s_waitcnt lgkmcnt(0)\n\t"
    "s_branch 2b\n\t"
    "3:\n\t"
    "v_add_f32 v48, v48, v80\n\t"
    "v_add_f32 v49, v49, v81\n\t"
    "v_add_f32 v50, v50, v82\n\t"
    "v_add_f32 v51, v51, v83\n\t"
    "v_add_f32 v56, v56, v88\n\t"
    "v_add_f32 v57, v57, v89\n\t"
    "v_add_f32 v58, v58, v90\n\t"
    "v_add_f32 v59, v59, v91\n\t"
    "v_cmp_u_f32 vcc, v48, v56\n\t"
    "s_waitcnt lgkmcnt(0)\n\t"
    "v_add_f32 v52, v52, v84\n\t"
    "v_add_f32 v53, v53, v85\n\t"
    "v_add_f32 v54, v54, v86\n\t"
    "v_add_f32 v55, v55, v87\n\t"
    "v_add_f32 v60, v60, v92\n\t"
    "v_add_f32 v61, v61, v93\n\t"
    "v_add_f32 v62, v62, v94\n\t"
    "v_add_f32 v63, v63, v95\n\t"
    "s_cmp_lg_u64 vcc, 0\n\t"
    "s_cselect_b32 %[st], %[b], 0\n\t"
    "s_cmp_gt_i32 %[st], %[be]\n\t"
    "s_cbranch_scc1 4f\n\t"
    "v_mul_f32 v40, %[nl], v48\n\t"
    "v_mul_f32 v41, %[nl], v49\n\t"
    "v_mul_f32 v42, %[nl], v50\n\t"
    "v_mul_f32 v43, %[nl], v51\n\t"
    "v_mul_f32 v44, %[nl], v56\n\t"
    "v_mul_f32 v45, %[nl], v57\n\t"
    "v_mul_f32 v46, %[nl], v58\n\t"
    "v_mul_f32 v47, %[nl], v59\n\t"
    "v_exp_f32 v40, v40\n\t"
    "v_exp_f32 v41, v41\n\t"
    "v_exp_f32 v42, v42\n\t"
    "v_exp_f32 v43, v43\n\t"
    "v_exp_f32 v44, v44\n\t"
    "v_exp_f32 v45, v45\n\t"
    "v_exp_f32 v46, v46\n\t"
    "v_exp_f32 v47, v47\n\t"
    "v_add_f32 v40, 1.0, v40\n\t"
    "v_add_f32 v41, 1.0, v41\n\t"
    "v_add_f32 v42, 1.0, v42\n\t"
    "v_add_f32 v43, 1.0, v43\n\t"
    "v_add_f32 v44, 1.0, v44\n\t"
    "v_add_f32 v45, 1.0, v45\n\t"
    "v_add_f32 v46, 1.0, v46\n\t"
    "v_add_f32 v47, 1.0, v47\n\t"
    "v_rcp_f32 v40, v40\n\t"
    "v_rcp_f32 v41, v41\n\t"
    "v_rcp_f32 v42, v42\n\t"
    "v_rcp_f32 v43, v43\n\t"
    "v_rcp_f32 v44, v44\n\t"
    "v_rcp_f32 v45, v45\n\t"
    "v_rcp_f32 v46, v46\n\t"
    "v_rcp_f32 v47, v47\n\t"
    "v_mul_f32 v40, v48, v40\n\t"
    "v_mul_f32 v41, v49, v41\n\t"
    "v_mul_f32 v42, v50, v42\n\t"
    "v_mul_f32 v43, v51, v43\n\t"
    "v_mul_f32 v44, v56, v44\n\t"
    "v_mul_f32 v45, v57, v45\n\t"
    "v_mul_f32 v46, v58, v46\n\t"
    "v_mul_f32 v47, v59, v47\n\t"
    "v_mul_f32 v40, v40, v52\n\t"
    "v_mul_f32 v41, v41, v53\n\t"
    "v_mul_f32 v42, v42, v54\n\t"
    "v_mul_f32 v43, v43, v55\n\t"
    "v_mul_f32 v44, v44, v60\n\t"
    "v_mul_f32 v45, v45, v61\n\t"
    "v_mul_f32 v46, v46, v62\n\t"
    "v_mul_f32 v47, v47, v63\n\t"
    "v_cvt_pk_bf16_f32 v48, v40, v41\n\t"
    "v_cvt_pk_bf16_f32 v49, v42, v43\n\t"
    "v_cvt_pk_bf16_f32 v50, v44, v45\n\t"
    "v_cvt_pk_bf16_f32 v51, v46, v47\n\t"
    "s_nop 1\n\t"
    "v_permlane16_swap_b32 v48, v50\n\t"
    "v_permlane16_swap_b32 v49, v51\n\t"
    "global_store_dwordx4 %[co0], v[48:51], s[36:37] sc1\n\t"
    "v_readfirstlane_b32 %[st], v38\n\t"
    "s_add_u32 s36, s36, %[ab]\n\t"
    "s_addc_u32 s37, s37, 0\n\t"
    "s_cmp_eq_u32 %[st], %[b]\n\t"
    "s_cbranch_scc1 33f\n\t"
    "s_cmp_le_i32 %[b], %[bl]\n\t"
    "s_cbranch_scc1 5f\n\t"
    "s_mov_b32 %[pp], 0\n\t"
    "s_cmp_lt_i32 %[b], %[ns]\n\t"
    "s_cbranch_scc1 1b\n\t"
    "33:\n\t"
    "s_barrier\n\t"
    "s_branch 9f\n\t"
    "5:\n\t"
    "s_lshr_b32 %[pp], %[pp], 8\n\t"
    "s_cbranch_scc1 1b\n\t"
    "v_mov_b32 v72, %[cn]\n\t"
    "v_mov_b32 v73, 1\n\t"
    "s_waitcnt vmcnt(0)\n\t"
    "s_mov_b64 exec, 1\n\t"
    "ds_add_rtn_u32 v73, v72, v73\n\t"
    "s_add_u32 %[st], %[b], %[po]\n\t"
    "v_mov_b32 v75, %[st]\n\t"
    "v_add_u32 v74, 0, v75\n\t"
    "v_mov_b32 v72, 0\n\t"
    "s_waitcnt lgkmcnt(0)\n\t"
    "v_readfirstlane_b32 %[st], v73\n\t"
    "s_and_b32 %[st], %[st], 3\n\t"
    "s_cmp_lg_u32 %[st], 3\n\t"
    "s_cbranch_scc1 61f\n\t"
    "global_store_dword v72, v74, %[pg] sc1\n\t"
    "61:\n\t"
    "s_mov_b64 exec, -1\n\t"
    "s_mov_b32 %[pp], 0\n\t"
    "s_cmp_lt_i32 %[b], %[ns]\n\t"
    "s_cbranch_scc1 1b\n\t"
    "s_barrier\n\t"
    "s_branch 9f\n\t"
    "4:\n\t"
    "v_mov_b32 v39, %[b]\n\t"
    "ds_write_b32 %[fa], v39 offset:16\n\t"
    "s_waitcnt lgkmcnt(0)\n\t"
    "s_barrier\n\t"
    "s_branch 9f\n\t"
    "7:\n\t"
    "v_mov_b32 v72, %[cn]\n\t"
    "v_mov_b32 v73, 1\n\t"
    "s_waitcnt vmcnt(0)\n\t"
    "s_mov_b64 exec, 1\n\t"
    "ds_add_rtn_u32 v73, v72, v73\n\t"
    "s_add_u32 %[st], %[b], %[po]\n\t"
    "v_mov_b32 v75, %[st]\n\t"
    "v_add_u32 v74, 0, v75\n\t"
    "v_mov_b32 v72, 0\n\t"
    "s_waitcnt lgkmcnt(0)\n\t"
    "v_readfirstlane_b32 %[st], v73\n\t"
    "s_and_b32 %[st], %[st], 3\n\t"
    "s_cmp_lg_u32 %[st], 3\n\t"
    "s_cbranch_scc1 61f\n\t"
    "global_store_dword v72, v74, %[pg] sc1\n\t"
    "61:\n\t"
    "s_mov_b64 exec, -1\n\t"
    "s_mov_b32 %[pp], 0\n\t"
    "s_branch 8b\n\t"
    "9:\n\t"
    "s_waitcnt lgkmcnt(0)"
    : [ea] "+&v"(ea), [da] "+&v"(da), [b] "+&s"(b), [st] "=&s"(st), "+{s[36:37]}"(p0), [pp] "+&s"(pp)
    : [ns] "s"(ns), [ab] "s"(ab), [rw] "v"(rw), [fa] "v"(fa), "{v[128:159]}"(W[0]), "{v[160:191]}"(W[1]), "{v[192:223]}"(W[2]), "{v[224:255]}"(W[3]), [w0] "s"(w0), [be] "s"(be), [bo] "s"(bo), [bl] "s"(bl), [po] "s"(po), [cn] "s"(cn), [pg] "s"(pg), [co0] "v"(co0), [nl] "s"(-LOG2E)
    : "v38", "v39", "v40", "v41", "v42", "v43", "v44", "v45", "v46", "v47", "v48", "v49", "v50", "v51", "v52", "v53", "v54", "v55", "v56", "v57", "v58", "v59", "v60", "v61", "v62", "v63", "v72", "v73", "v74", "v75", "v80", "v81", "v82", "v83", "v84", "v85", "v86", "v87", "v88", "v89", "v90", "v91", "v92", "v93", "v94", "v95", "v96", "v97", "v98", "v99", "v100", "v101", "v102", "v103", "v104", "v105", "v106", "v107", "v108", "v109", "v110", "v111", "v112", "v113", "v114", "v115", "v116", "v117", "v118", "v119", "v120", "v121", "v122", "v123", "v124", "v125", "v126", "v127", "vcc", "scc", "m0", "memory");
}
__device__ __forceinline__ void paced_st2(const u32x32 (&W)[4], unsigned ea, unsigned da, int& b, int ns, unsigned ab, unsigned rw, unsigned fa,
    const char* p0, unsigned pp, int w0, int be, int bo, int bl, int po, unsigned cn, const char* pg, unsigned co0, unsigned co1, int& bnd) {
  unsigned st;
  asm volatile("1:\n\t"
    "s_barrier\n\t"
    "ds_read_b128 v[96:99], %[ea] offset:0\n\t"
    "ds_read_b128 v[100:103], %[ea] offset:2048\n\t"
    "ds_read_b128 v[104:107], %[da] offset:0\n\t"
    "ds_read_b128 v[108:111], %[da] offset:2048\n\t"
    "ds_read_b128 v[112:115], %[ea] offset:4096\n\t"
    "ds_read_b128 v[116:119], %[ea] offset:6144\n\t"
    "s_waitcnt lgkmcnt(4)\n\t"
    "v_mfma_f32_16x16x32_bf16 v[80:83], v[128:131], v[96:99], 0\n\t"
    "ds_read_b128 v[120:123], %[da] offset:4096\n\t"
    "v_mfma_f32_16x16x32_bf16 v[84:87], v[132:135], v[96:99], 0\n\t"
    "ds_read_b128 v[124:127], %[da] offset:6144\n\t"
    "v_mfma_f32_16x16x32_bf16 v[88:91], v[128:131], v[100:103], 0\n\t"
    "v_mfma_f32_16x16x32_bf16 v[92:95], v[132:135], v[100:103], 0\n\t"
    "s_barrier\n\t"
    "s_waitcnt lgkmcnt(4)\n\t"
    "v_mfma_f32_16x16x32_bf16 v[80:83], v[136:139], v[104:107], v[80:83]\n\t"
    "ds_read_b128 v[96:99], %[ea] offset:8192\n\t"
    "v_mfma_f32_16x16x32_bf16 v[84:87], v[140:143], v[104:107], v[84:87]\n\t"
    "ds_read_b128 v[100:103], %[ea] offset:10240\n\t"
    "v_mfma_f32_16x16x32_bf16 v[88:91], v[136:139], v[108:111], v[88:91]\n\t"
    "v_mfma_f32_16x16x32_bf16 v[92:95], v[140:143], v[108:111], v[92:95]\n\t"
    "s_waitcnt lgkmcnt(4)\n\t"
    "v_mfma_f32_16x16x32_bf16 v[80:83], v[144:147], v[112:115], v[80:83]\n\t"
    "ds_read_b128 v[104:107], %[da] offset:8192\n\t"
    "v_mfma_f32_16x16x32_bf16 v[84:87], v[148:151], v[112:115], v[84:87]\n\t"
    "ds_read_b128 v[108:111], %[da] offset:10240\n\t"
    "v_mfma_f32_16x16x32_bf16 v[88:91], v[144:147], v[116:119], v[88:91]\n\t"
    "v_mfma_f32_16x16x32_bf16 v[92:95], v[148:151], v[116:119], v[92:95]\n\t"
    "s_waitcnt lgkmcnt(4)\n\t"
    "v_mfma_f32_16x16x32_bf16 v[80:83], v[152:155], v[120:123], v[80:83]\n\t"
    "ds_read_b128 v[112:115], %[ea] offset:12288\n\t"
    "v_mfma_f32_16x16x32_bf16 v[84:87], v[156:159], v[120:123], v[84:87]\n\t"
    "ds_read_b128 v[116:119], %[ea] offset:14336\n\t"
    "v_mfma_f32_16x16x32_bf16 v[88:91], v[152:155], v[124:127], v[88:91]\n\t"
    "v_mfma_f32_16x16x32_bf16 v[92:95], v[156:159], v[124:127], v[92:95]\n\t"
    "s_cmp_lg_u32 %[pp], 0\n\t"
    "s_cbranch_scc1 7f\n\t"
    "8:\n\t"
    "s_waitcnt lgkmcnt(4)\n\t"
    "v_mfma_f32_16x16x32_bf16 v[80:83], v[160:163], v[96:99], v[80:83]\n\t"
    "ds_read_b128 v[120:123], %[da] offset:12288\n\t"
    "v_mfma_f32_16x16x32_bf16 v[84:87], v[164:167], v[96:99], v[84:87]\n\t"
    "ds_read_b128 v[124:127], %[da] offset:14336\n\t"
    "v_mfma_f32_16x16x32_bf16 v[88:91], v[160:163], v[100:103], v[88:91]\n\t"
    "v_mfma_f32_16x16x32_bf16 v[92:95], v[164:167], v[100:103], v[92:95]\n\t"
    "s_barrier\n\t"
    "s_waitcnt lgkmcnt(4)\n\t"
    "v_mfma_f32_16x16x32_bf16 v[80:83], v[168:171], v[104:107], v[80:83]\n\t"
    "ds_read_b128 v[96:99], %[ea] offset:16384\n\t"
    "v_mfma_f32_16x16x32_bf16 v[84:87], v[172:175], v[104:107], v[84:87]\n\t"
    "ds_read_b128 v[100:103], %[ea] offset:18432\n\t"
    "v_mfma_f32_16x16x32_bf16 v[88:91], v[168:171], v[108:111], v[88:91]\n\t"
    "v_mfma_f32_16x16x32_bf16 v[92:95], v[172:175], v[108:111], v[92:95]\n\t"
    "s_waitcnt lgkmcnt(4)\n\t"
    "v_mfma_f32_16x16x32_bf16 v[80:83], v[176:179], v[112:115], v[80:83]\n\t"
    "ds_read_b128 v[104:107], %[da] offset:16384\n\t"
    "v_mfma_f32_16x16x32_bf16 v[84:87], v[180:183], v[112:115], v[84:87]\n\t"
    "ds_read_b128 v[108:111], %[da] offset:18432\n\t"
    "v_mfma_f32_16x16x32_bf16 v[88:91], v[176:179], v[116:119], v[88:91]\n\t"
    "v_mfma_f32_16x16x32_bf16 v[92:95], v[180:183], v[116:119], v[92:95]\n\t"
    "s_waitcnt lgkmcnt(4)\n\t"
    "v_mfma_f32_16x16x32_bf16 v[80:83], v[184:187], v[120:123], v[80:83]\n\t"
    "ds_read_b128 v[112:115], %[ea] offset:20480\n\t"
    "v_mfma_f32_16x16x32_bf16 v[84:87], v[188:191], v[120:123], v[84:87]\n\t"
    "ds_read_b128 v[116:119], %[ea] offset:22528\n\t"
    "v_mfma_f32_16x16x32_bf16 v[88:91], v[184:187], v[124:127], v[88:91]\n\t"
    "v_mfma_f32_16x16x32_bf16 v[92:95], v[188:191], v[124:127], v[92:95]\n\t"
    "s_waitcnt lgkmcnt(4)\n\t"
    "v_mfma_f32_16x16x32_bf16 v[80:83], v[192:195], v[96:99], v[80:83]\n\t"
    "ds_read_b128 v[120:123], %[da] offset:20480\n\t"
    "v_mfma_f32_16x16x32_bf16 v[84:87], v[196:199], v[96:99], v[84:87]\n\t"
    "ds_read_b128 v[124:127], %[da] offset:22528\n\t"
    "v_mfma_f32_16x16x32_bf16 v[88:91], v[192:195], v[100:103], v[88:91]\n\t"
    "v_mfma_f32_16x16x32_bf16 v[92:95], v[196:199], v[100:103], v[92:95]\n\t"
    "s_waitcnt vmcnt(0)\n\t"
    "s_barrier\n\t"
    "s_waitcnt lgkmcnt(4)\n\t"
    "v_mfma_f32_16x16x32_bf16 v[80:83], v[200:203], v[104:107], v[80:83]\n\t"
    "ds_read_b128 v[96:99], %[ea] offset:24576\n\t"
    "v_mfma_f32_16x16x32_bf16 v[84:87], v[204:207], v[104:107], v[84:87]\n\t"
    "ds_read_b128 v[100:103], %[ea] offset:26624\n\t"
    "v_mfma_f32_16x16x32_bf16 v[88:91], v[200:203], v[108:111], v[88:91]\n\t"
    "v_mfma_f32_16x16x32_bf16 v[92:95], v[204:207], v[108:111], v[92:95]\n\t"
    "s_waitcnt lgkmcnt(4)\n\t"
    "v_mfma_f32_16x16x32_bf16 v[80:83], v[208:211], v[112:115], v[80:83]\n\t"
    "ds_read_b128 v[104:107], %[da] offset:24576\n\t"
    "v_mfma_f32_16x16x32_bf16 v[84:87], v[212:215], v[112:115], v[84:87]\n\t"
    "ds_read_b128 v[108:111], %[da] offset:26624\n\t"
    "v_mfma_f32_16x16x32_bf16 v[88:91], v[208:211], v[116:119], v[88:91]\n\t"
    "v_mfma_f32_16x16x32_bf16 v[92:95], v[212:215], v[116:119], v[92:95]\n\t"
    "s_waitcnt lgkmcnt(4)\n\t"
    "v_mfma_f32_16x16x32_bf16 v[80:83], v[216:219], v[120:123], v[80:83]\n\t"
    "ds_read_b128 v[112:115], %[ea] offset:28672\n\t"
    "v_mfma_f32_16x16x32_bf16 v[84:87], v[220:223], v[120:123], v[84:87]\n\t"
    "ds_read_b128 v[116:119], %[ea] offset:30720\n\t"
    "v_mfma_f32_16x16x32_bf16 v[88:91], v[216:219], v[124:127], v[88:91]\n\t"
    "v_mfma_f32_16x16x32_bf16 v[92:95], v[220:223], v[124:127], v[92:95]\n\t"
    "s_waitcnt lgkmcnt(4)\n\t"
    "v_mfma_f32_16x16x32_bf16 v[80:83], v[224:227], v[96:99], v[80:83]\n\t"
    "ds_read_b128 v[120:123], %[da] offset:28672\n\t"
    "v_mfma_f32_16x16x32_bf16 v[84:87], v[228:231], v[96:99], v[84:87]\n\t"
    "ds_read_b128 v[124:127], %[da] offset:30720\n\t"
    "v_mfma_f32_16x16x32_bf16 v[88:91], v[224:227], v[100:103], v[88:91]\n\t"
    "v_mfma_f32_16x16x32_bf16 v[92:95], v[228:231], v[100:103], v[92:95]\n\t"
    "s_waitcnt lgkmcnt(4)\n\t"
    "v_mfma_f32_16x16x32_bf16 v[80:83], v[232:235], v[104:107], v[80:83]\n\t"
    "v_mfma_f32_16x16x32_bf16 v[84:87], v[236:239], v[104:107], v[84:87]\n\t"
    "v_mfma_f32_16x16x32_bf16 v[88:91], v[232:235], v[108:111], v[88:91]\n\t"
    "v_xor_b32 %[ea], 0x10000, %[ea]\n\t"
    "v_mfma_f32_16x16x32_bf16 v[92:95], v[236:239], v[108:111], v[92:95]\n\t"
    "v_xor_b32 %[da], 0x10000, %[da]\n\t"
    "s_cmp_ge_i32 %[bl], %[w0]\n\t"
    "s_cbranch_scc1 57f\n\t"
    "s_add_u32 %[st], %[b], 1\n\t"
    "s_cmp_le_i32 %[st], %[bl]\n\t"
    "s_cbranch_scc1 57f\n\t"
    "s_cmp_gt_i32 %[st], %[bo]\n\t"
    "s_mov_b64 exec, 1\n\t"
    "v_mov_b32 v72, 0\n\t"
    "v_mov_b32 v74, %[st]\n\t"
    "s_cselect_b32 %[st], %[st], 0\n\t"
    "v_add_u32 v74, -1, v74\n\t"
    "v_mov_b32 v75, %[st]\n\t"
    "global_store_dwordx2 v72, v[74:75], %[pg] sc1\n\t"
    "s_mov_b64 exec, -1\n\t"
    "57:\n\t"
    "s_waitcnt lgkmcnt(2)\n\t"
    "v_mfma_f32_16x16x32_bf16 v[80:83], v[240:243], v[112:115], v[80:83]\n\t"
    "v_mfma_f32_16x16x32_bf16 v[84:87], v[244:247], v[112:115], v[84:87]\n\t"
    "v_mfma_f32_16x16x32_bf16 v[88:91], v[240:243], v[116:119], v[88:91]\n\t"
    "s_add_u32 %[b], %[b], 1\n\t"
    "v_mfma_f32_16x16x32_bf16 v[92:95], v[244:247], v[116:119], v[92:95]\n\t"
    "s_waitcnt lgkmcnt(0)\n\t"
    "v_mfma_f32_16x16x32_bf16 v[80:83], v[248:251], v[120:123], v[80:83]\n\t"
    "v_mfma_f32_16x16x32_bf16 v[84:87], v[252:255], v[120:123], v[84:87]\n\t"
    "v_mfma_f32_16x16x32_bf16 v[88:91], v[248:251], v[124:127], v[88:91]\n\t"
    "v_mfma_f32_16x16x32_bf16 v[92:95], v[252:255], v[124:127], v[92:95]\n\t"
    "s_nop 6\n\t"
    "v_cmp_u_f32 vcc, v80, v88\n\t"
    "s_cmp_lg_u64 vcc, 0\n\t"
    "s_cselect_b32 %[st], %[b], 0\n\t"
    "s_cmp_gt_i32 %[st], %[be]\n\t"
    "s_cbranch_scc1 4f\n\t"
    "v_cvt_pk_bf16_f32 v40, v80, v81\n\t"
    "v_cvt_pk_bf16_f32 v41, v82, v83\n\t"
    "v_cvt_pk_bf16_f32 v42, v84, v85\n\t"
    "v_cvt_pk_bf16_f32 v43, v86, v87\n\t"
    "global_store_dwordx4 %[co0], v[40:43], s[36:37] sc1\n\t"
    "v_cvt_pk_bf16_f32 v44, v88, v89\n\t"
    "v_cvt_pk_bf16_f32 v45, v90, v91\n\t"
    "v_cvt_pk_bf16_f32 v46, v92, v93\n\t"
    "v_cvt_pk_bf16_f32 v47, v94, v95\n\t"
    "global_store_dwordx4 %[co1], v[44:47], s[36:37] sc1\n\t"
    "ds_read_b32 v38, %[fa] offset:32\n\t"
    "2:\n\t"
    "ds_read_b32 v39, %[fa]\n\t"
    "ds_read_b128 v[48:51], %[rw] offset:0\n\t"
    "ds_read_b128 v[52:55], %[rw] offset:1024\n\t"
    "ds_read_b128 v[56:59], %[rw] offset:2048\n\t"
    "ds_read_b128 v[60:63], %[rw] offset:3072\n\t"
    "s_waitcnt lgkmcnt(2)\n\t"
    "v_readfirstlane_b32 %[pp], v39\n\t"
    "s_and_b32 %[st], %[pp], 0xff\n\t"
    "s_cmp_eq_u32 %[st], %[b]\n\t"
    "s_cbranch_scc1 3f\n\t"
    "s_waitcnt lgkmcnt(0)\n\t"
    "s_branch 2b\n\t"
    "3:\n\t"
    "v_cvt_pk_bf16_f32 v48, v48, v49\n\t"
    "v_cvt_pk_bf16_f32 v49, v50, v51\n\t"
    "v_cvt_pk_bf16_f32 v50, v52, v53\n\t"
    "v_cvt_pk_bf16_f32 v51, v54, v55\n\t"
    "global_store_dwordx4 %[co0], v[48:51], s[36:37] offset:2048 sc1\n\t"
    "s_waitcnt lgkmcnt(0)\n\t"
    "v_cvt_pk_bf16_f32 v56, v56, v57\n\t"
    "v_cvt_pk_bf16_f32 v57, v58, v59\n\t"
    "v_cvt_pk_bf16_f32 v58, v60, v61\n\t"
    "v_cvt_pk_bf16_f32 v59, v62, v63\n\t"
    "global_store_dwordx4 %[co1], v[56:59], s[36:37] offset:2048 sc1\n\t"
    "v_readfirstlane_b32 %[st], v38\n\t"
    "s_add_u32 s36, s36, %[ab]\n\t"
    "s_addc_u32 s37, s37, 0\n\t"
    "s_cmp_eq_u32 %[st], %[b]\n\t"
    "s_cbranch_scc1 33f\n\t"
    "s_cmp_le_i32 %[b], %[bl]\n\t"
    "s_cbranch_scc1 5f\n\t"
    "s_mov_b32 %[pp], 0\n\t"
    "s_cmp_lt_i32 %[b], %[ns]\n\t"
    "s_cbranch_scc1 1b\n\t"
    "33:\n\t"
    "s_barrier\n\t"
    "s_branch 9f\n\t"
    "5:\n\t"
    "s_lshr_b32 %[pp], %[pp], 8\n\t"
    "s_cbranch_scc1 1b\n\t"
    "v_mov_b32 v72, %[cn]\n\t"
    "v_mov_b32 v73, 1\n\t"
    "s_waitcnt vmcnt(0)\n\t"
    "s_mov_b64 exec, 1\n\t"
    "ds_add_rtn_u32 v73, v72, v73\n\t"
    "s_add_u32 %[st], %[b], %[po]\n\t"
    "v_mov_b32 v75, %[st]\n\t"
    "v_add_u32 v74, 0, v75\n\t"
    "v_mov_b32 v72, 0\n\t"
    "s_waitcnt lgkmcnt(0)\n\t"
    "v_readfirstlane_b32 %[st], v73\n\t"
    "s_and_b32 %[st], %[st], 3\n\t"
    "s_cmp_lg_u32 %[st], 3\n\t"
    "s_cbranch_scc1 61f\n\t"
    "global_store_dword v72, v74, %[pg] sc1\n\t"
    "61:\n\t"
    "s_mov_b64 exec, -1\n\t"
    "s_mov_b32 %[pp], 0\n\t"
    "s_cmp_lt_i32 %[b], %[ns]\n\t"
    "s_cbranch_scc1 1b\n\t"
    "s_barrier\n\t"
    "s_branch 9f\n\t"
    "4:\n\t"
    "v_mov_b32 v39, %[b]\n\t"
    "ds_write_b32 %[fa], v39 offset:16\n\t"
    "s_waitcnt lgkmcnt(0)\n\t"
    "s_barrier\n\t"
    "s_branch 9f\n\t"
    "7:\n\t"
    "v_mov_b32 v72, %[cn]\n\t"
    "v_mov_b32 v73, 1\n\t"
    "s_waitcnt vmcnt(0)\n\t"
    "s_mov_b64 exec, 1\n\t"
    "ds_add_rtn_u32 v73, v72, v73\n\t"
    "s_add_u32 %[st], %[b], %[po]\n\t"
    "v_mov_b32 v75, %[st]\n\t"
    "v_add_u32 v74, 0, v75\n\t"
    "v_mov_b32 v72, 0\n\t"
    "s_waitcnt lgkmcnt(0)\n\t"
    "v_readfirstlane_b32 %[st], v73\n\t"
    "s_and_b32 %[st], %[st], 3\n\t"
    "s_cmp_lg_u32 %[st], 3\n\t"
    "s_cbranch_scc1 61f\n\t"
    "global_store_dword v72, v74, %[pg] sc1\n\t"
    "61:\n\t"
    "s_mov_b64 exec, -1\n\t"
    "s_mov_b32 %[pp], 0\n\t"
    "s_branch 8b\n\t"
    "9:\n\t"
    "s_waitcnt lgkmcnt(0)"
    : [ea] "+&v"(ea), [da] "+&v"(da), [b] "+&s"(b), [st] "=&s"(st), "+{s[36:37]}"(p0), [pp] "+&s"(pp)
    : [ns] "s"(ns), [ab] "s"(ab), [rw] "v"(rw), [fa] "v"(fa), "{v[128:159]}"(W[0]), "{v[160:191]}"(W[1]), "{v[192:223]}"(W[2]), "{v[224:255]}"(W[3]), [w0] "s"(w0), [be] "s"(be), [bo] "s"(bo), [bl] "s"(bl), [po] "s"(po), [cn] "s"(cn), [pg] "s"(pg), [co0] "v"(co0), [co1] "v"(co1)
    : "v38", "v39", "v40", "v41", "v42", "v43", "v44", "v45", "v46", "v47", "v48", "v49", "v50", "v51", "v52", "v53", "v54", "v55", "v56", "v57", "v58", "v59", "v60", "v61", "v62", "v63", "v72", "v73", "v74", "v75", "v80", "v81", "v82", "v83", "v84", "v85", "v86", "v87", "v88", "v89", "v90", "v91", "v92", "v93", "v94", "v95", "v96", "v97", "v98", "v99", "v100", "v101", "v102", "v103", "v104", "v105", "v106", "v107", "v108", "v109", "v110", "v111", "v112", "v113", "v114", "v115", "v116", "v117", "v118", "v119", "v120", "v121", "v122", "v123", "v124", "v125", "v126", "v127", "vcc", "scc", "m0", "memory");
}
__device__ __forceinline__ void fin_ld(const u32x16& pd, unsigned rw, unsigned fa, unsigned fv) {
  asm volatile("ds_write_b128 %[rw], v[80:83] offset:0\n\t"
    "ds_write_b128 %[rw], v[84:87] offset:1024\n\t"
    "ds_write_b128 %[rw], v[88:91] offset:2048\n\t"
    "ds_write_b128 %[rw], v[92:95] offset:3072\n\t"
    "ds_write_b32 %[fa], %[fv]"
    :
    : [rw] "v"(rw), "{v[80:95]}"(pd), [fa] "v"(fa), [fv] "v"(fv)
    : "memory");
}
__device__ __forceinline__ void fin_st0(const u32x16& pd, unsigned rw, unsigned fa, unsigned fv, const char* cb, unsigned co0, unsigned co1, int& bnd) {
  asm volatile("1:\n\t"
    "ds_read_b32 v39, %[fa]\n\t"
    "ds_read_b128 v[48:51], %[rw] offset:0\n\t"
    "ds_read_b128 v[52:55], %[rw] offset:1024\n\t"
    "ds_read_b128 v[56:59], %[rw] offset:2048\n\t"
    "ds_read_b128 v[60:63], %[rw] offset:3072\n\t"
    "s_waitcnt lgkmcnt(2)\n\t"
    "v_cmp_ne_u32 vcc, v39, %[fv]\n\t"
    "s_cbranch_vccz 2f\n\t"
    "s_waitcnt lgkmcnt(0)\n\t"
    "s_branch 1b\n\t"
    "2:\n\t"
    "v_add_f32 v48, v48, v80\n\t"
    "v_add_f32 v49, v49, v81\n\t"
    "v_add_f32 v50, v50, v82\n\t"
    "v_add_f32 v51, v51, v83\n\t"
    "v_add_f32 v52, v52, v84\n\t"
    "v_add_f32 v53, v53, v85\n\t"
    "v_add_f32 v54, v54, v86\n\t"
    "v_add_f32 v55, v55, v87\n\t"
    "v_cvt_pk_bf16_f32 v40, v48, v49\n\t"
    "v_cvt_pk_bf16_f32 v41, v50, v51\n\t"
    "v_cvt_pk_bf16_f32 v42, v52, v53\n\t"
    "v_cvt_pk_bf16_f32 v43, v54, v55\n\t"
    "global_store_dwordx4 %[co0], v[40:43], %[cb] sc1\n\t"
    "s_waitcnt lgkmcnt(0)\n\t"
    "v_add_f32 v56, v56, v88\n\t"
    "v_add_f32 v57, v57, v89\n\t"
    "v_add_f32 v58, v58, v90\n\t"
    "v_add_f32 v59, v59, v91\n\t"
    "v_add_f32 v60, v60, v92\n\t"
    "v_add_f32 v61, v61, v93\n\t"
    "v_add_f32 v62, v62, v94\n\t"
    "v_add_f32 v63, v63, v95\n\t"
    "v_cvt_pk_bf16_f32 v44, v56, v57\n\t"
    "v_cvt_pk_bf16_f32 v45, v58, v59\n\t"
    "v_cvt_pk_bf16_f32 v46, v60, v61\n\t"
    "v_cvt_pk_bf16_f32 v47, v62, v63\n\t"
    "global_store_dwordx4 %[co1], v[44:47], %[cb] sc1\n\t"
    "s_nop 1"
    :
    : [rw] "v"(rw), "{v[80:95]}"(pd), [fa] "v"(fa), [fv] "v"(fv), [cb] "s"(cb), [co0] "v"(co0), [co1] "v"(co1)
    : "v39", "v40", "v41", "v42", "v43", "v44", "v45", "v46", "v47", "v48", "v49", "v50", "v51", "v52", "v53", "v54", "v55", "v56", "v57", "v58", "v59", "v60", "v61", "v62", "v63", "vcc", "scc", "memory");
}
__device__ __forceinline__ void fin_st1(const u32x16& pd, unsigned rw, unsigned fa, unsigned fv, const char* cb, unsigned co0, unsigned co1, int& bnd) {
  asm volatile("1:\n\t"
    "ds_read_b32 v39, %[fa]\n\t"
    "ds_read_b128 v[48:51], %[rw] offset:0\n\t"
    "ds_read_b128 v[52:55], %[rw] offset:1024\n\t"
    "ds_read_b128 v[56:59], %[rw] offset:2048\n\t"
    "ds_read_b128 v[60:63], %[rw] offset:3072\n\t"
    "s_waitcnt lgkmcnt(2)\n\t"
    "v_cmp_ne_u32 vcc, v39, %[fv]\n\t"
    "s_cbranch_vccz 2f\n\t"
    "s_waitcnt lgkmcnt(0)\n\t"
    "s_branch 1b\n\t"
    "2:\n\t"
    "v_add_f32 v48, v48, v80\n\t"
    "v_add_f32 v49, v49, v81\n\t"
    "v_add_f32 v50, v50, v82\n\t"
    "v_add_f32 v51, v51, v83\n\t"
    "v_add_f32 v52, v52, v84\n\t"
    "v_add_f32 v53, v53, v85\n\t"
    "v_add_f32 v54, v54, v86\n\t"
    "v_add_f32 v55, v55, v87\n\t"
    "s_waitcnt lgkmcnt(0)\n\t"
    "v_add_f32 v56, v56, v88\n\t"
    "v_add_f32 v57, v57, v89\n\t"
    "v_add_f32 v58, v58, v90\n\t"
    "v_add_f32 v59, v59, v91\n\t"
    "v_add_f32 v60, v60, v92\n\t"
    "v_add_f32 v61, v61, v93\n\t"
    "v_add_f32 v62, v62, v94\n\t"
    "v_add_f32 v63, v63, v95\n\t"
    "v_mul_f32 v40, %[nl], v48\n\t"
    "v_mul_f32 v41, %[nl], v49\n\t"
    "v_mul_f32 v42, %[nl], v50\n\t"
    "v_mul_f32 v43, %[nl], v51\n\t"
    "v_mul_f32 v44, %[nl], v56\n\t"
    "v_mul_f32 v45, %[nl], v57\n\t"
    "v_mul_f32 v46, %[nl], v58\n\t"
    "v_mul_f32 v47, %[nl], v59\n\t"
    "v_exp_f32 v40, v40\n\t"
    "v_exp_f32 v41, v41\n\t"
    "v_exp_f32 v42, v42\n\t"
    "v_exp_f32 v43, v43\n\t"
    "v_exp_f32 v44, v44\n\t"
    "v_exp_f32 v45, v45\n\t"
    "v_exp_f32 v46, v46\n\t"
    "v_exp_f32 v47, v47\n\t"
    "v_add_f32 v40, 1.0, v40\n\t"
    "v_add_f32 v41, 1.0, v41\n\t"
    "v_add_f32 v42, 1.0, v42\n\t"
    "v_add_f32 v43, 1.0, v43\n\t"
    "v_add_f32 v44, 1.0, v44\n\t"
    "v_add_f32 v45, 1.0, v45\n\t"
    "v_add_f32 v46, 1.0, v46\n\t"
    "v_add_f32 v47, 1.0, v47\n\t"
    "v_rcp_f32 v40, v40\n\t"
    "v_rcp_f32 v41, v41\n\t"
    "v_rcp_f32 v42, v42\n\t"
    "v_rcp_f32 v43, v43\n\t"
    "v_rcp_f32 v44, v44\n\t"
    "v_rcp_f32 v45, v45\n\t"
    "v_rcp_f32 v46, v46\n\t"
    "v_rcp_f32 v47, v47\n\t"
    "v_mul_f32 v40, v48, v40\n\t"
    "v_mul_f32 v41, v49, v41\n\t"
    "v_mul_f32 v42, v50, v42\n\t"
    "v_mul_f32 v43, v51, v43\n\t"
    "v_mul_f32 v44, v56, v44\n\t"
    "v_mul_f32 v45, v57, v45\n\t"
    "v_mul_f32 v46, v58, v46\n\t"
    "v_mul_f32 v47, v59, v47\n\t"
    "v_mul_f32 v40, v40, v52\n\t"
    "v_mul_f32 v41, v41, v53\n\t"
    "v_mul_f32 v42, v42, v54\n\t"
    "v_mul_f32 v43, v43, v55\n\t"
    "v_mul_f32 v44, v44, v60\n\t"
    "v_mul_f32 v45, v45, v61\n\t"
    "v_mul_f32 v46, v46, v62\n\t"
    "v_mul_f32 v47, v47, v63\n\t"
    "v_cvt_pk_bf16_f32 v48, v40, v41\n\t"
    "v_cvt_pk_bf16_f32 v49, v42, v43\n\t"
    "v_cvt_pk_bf16_f32 v50, v44, v45\n\t"
    "v_cvt_pk_bf16_f32 v51, v46, v47\n\t"
    "s_nop 1\n\t"
    "v_permlane16_swap_b32 v48, v50\n\t"
    "v_permlane16_swap_b32 v49, v51\n\t"
    "global_store_dwordx4 %[co0], v[48:51], %[cb] sc1\n\t"
    "s_nop 1"
    :
    : [rw] "v"(rw), "{v[80:95]}"(pd), [fa] "v"(fa), [fv] "v"(fv), [cb] "s"(cb), [co0] "v"(co0), [nl] "s"(-LOG2E)
    : "v39", "v40", "v41", "v42", "v43", "v44", "v45", "v46", "v47", "v48", "v49", "v50", "v51", "v52", "v53", "v54", "v55", "v56", "v57", "v58", "v59", "v60", "v61", "v62", "v63", "vcc", "scc", "memory");
}
__device__ __forceinline__ void fin_st2(const u32x16& pd, unsigned rw, unsigned fa, unsigned fv, const char* cb, unsigned co0, unsigned co1, int& bnd) {
  asm volatile("s_nop 1\n\t"
    "v_cvt_pk_bf16_f32 v40, v80, v81\n\t"
    "v_cvt_pk_bf16_f32 v41, v82, v83\n\t"
    "v_cvt_pk_bf16_f32 v42, v84, v85\n\t"
    "v_cvt_pk_bf16_f32 v43, v86, v87\n\t"
    "global_store_dwordx4 %[co0], v[40:43], %[cb] sc1\n\t"
    "v_cvt_pk_bf16_f32 v44, v88, v89\n\t"
    "v_cvt_pk_bf16_f32 v45, v90, v91\n\t"
    "v_cvt_pk_bf16_f32 v46, v92, v93\n\t"
    "v_cvt_pk_bf16_f32 v47, v94, v95\n\t"
    "global_store_dwordx4 %[co1], v[44:47], %[cb] sc1\n\t"
    "1:\n\t"
    "ds_read_b32 v39, %[fa]\n\t"
    "ds_read_b128 v[48:51], %[rw] offset:0\n\t"
    "ds_read_b128 v[52:55], %[rw] offset:1024\n\t"
    "ds_read_b128 v[56:59], %[rw] offset:2048\n\t"
    "ds_read_b128 v[60:63], %[rw] offset:3072\n\t"
    "s_waitcnt lgkmcnt(2)\n\t"
    "v_cmp_ne_u32 vcc, v39, %[fv]\n\t"
    "s_cbranch_vccz 2f\n\t"
    "s_waitcnt lgkmcnt(0)\n\t"
    "s_branch 1b\n\t"
    "2:\n\t"
    "v_cvt_pk_bf16_f32 v48, v48, v49\n\t"
    "v_cvt_pk_bf16_f32 v49, v50, v51\n\t"
    "v_cvt_pk_bf16_f32 v50, v52, v53\n\t"
    "v_cvt_pk_bf16_f32 v51, v54, v55\n\t"
    "global_store_dwordx4 %[co0], v[48:51], %[cb] offset:2048 sc1\n\t"
    "s_waitcnt lgkmcnt(0)\n\t"
    "v_cvt_pk_bf16_f32 v56, v56, v57\n\t"
    "v_cvt_pk_bf16_f32 v57, v58, v59\n\t"
    "v_cvt_pk_bf16_f32 v58, v60, v61\n\t"
    "v_cvt_pk_bf16_f32 v59, v62, v63\n\t"
    "global_store_dwordx4 %[co1], v[56:59], %[cb] offset:2048 sc1\n\t"
    "s_nop 1"
    :
    : [rw] "v"(rw), "{v[80:95]}"(pd), [fa] "v"(fa), [fv] "v"(fv), [cb] "s"(cb), [co0] "v"(co0), [co1] "v"(co1)
    : "v39", "v40", "v41", "v42", "v43", "v44", "v45", "v46", "v47", "v48", "v49", "v50", "v51", "v52", "v53", "v54", "v55", "v56", "v57", "v58", "v59", "v60", "v61", "v62", "v63", "vcc", "scc", "memory");
}
template <int R> __device__ __forceinline__ void gw_load(u32x32& w, unsigned a0, unsigned a1);
template <> __device__ __forceinline__ void gw_load<0>(u32x32& w, unsigned a0, unsigned a1) {
  asm volatile("ds_read_b64_tr_b16 v[128:129], %1 offset:0\n\t"
    "ds_read_b64_tr_b16 v[130:131], %1 offset:512\n\t"
    "ds_read_b64_tr_b16 v[132:133], %2 offset:0\n\t"
    "ds_read_b64_tr_b16 v[134:135], %2 offset:512\n\t"
    "ds_read_b64_tr_b16 v[136:137], %1 offset:4096\n\t"
    "ds_read_b64_tr_b16 v[138:139], %1 offset:4608\n\t"
    "ds_read_b64_tr_b16 v[140:141], %2 offset:4096\n\t"
    "ds_read_b64_tr_b16 v[142:143], %2 offset:4608\n\t"
    "s_waitcnt lgkmcnt(0)\n\t"
    "ds_read_b64_tr_b16 v[144:145], %1 offset:16384\n\t"
    "ds_read_b64_tr_b16 v[146:147], %1 offset:16896\n\t"
    "ds_read_b64_tr_b16 v[148:149], %2 offset:16384\n\t"
    "ds_read_b64_tr_b16 v[150:151], %2 offset:16896\n\t"
    "ds_read_b64_tr_b16 v[152:153], %1 offset:20480\n\t"
    "ds_read_b64_tr_b16 v[154:155], %1 offset:20992\n\t"
    "ds_read_b64_tr_b16 v[156:157], %2 offset:20480\n\t"
    "ds_read_b64_tr_b16 v[158:159], %2 offset:20992\n\t"
    "s_waitcnt lgkmcnt(0)"
    : "=&{v[128:159]}"(w) : "v"(a0), "v"(a1) : "memory");
}
template <> __device__ __forceinline__ void gw_load<1>(u32x32& w, unsigned a0, unsigned a1) {
  asm volatile("ds_read_b64_tr_b16 v[160:161], %1 offset:0\n\t"
    "ds_read_b64_tr_b16 v[162:163], %1 offset:512\n\t"
    "ds_read_b64_tr_b16 v[164:165], %2 offset:0\n\t"
    "ds_read_b64_tr_b16 v[166:167], %2 offset:512\n\t"
    "ds_read_b64_tr_b16 v[168:169], %1 offset:4096\n\t"
    "ds_read_b64_tr_b16 v[170:171], %1 offset:4608\n\t"
    "ds_read_b64_tr_b16 v[172:173], %2 offset:4096\n\t"
    "ds_read_b64_tr_b16 v[174:175], %2 offset:4608\n\t"
    "s_waitcnt lgkmcnt(0)\n\t"
    "ds_read_b64_tr_b16 v[176:177], %1 offset:16384\n\t"
    "ds_read_b64_tr_b16 v[178:179], %1 offset:16896\n\t"
    "ds_read_b64_tr_b16 v[180:181], %2 offset:16384\n\t"
    "ds_read_b64_tr_b16 v[182:183], %2 offset:16896\n\t"
    "ds_read_b64_tr_b16 v[184:185], %1 offset:20480\n\t"
    "ds_read_b64_tr_b16 v[186:187], %1 offset:20992\n\t"
    "ds_read_b64_tr_b16 v[188:189], %2 offset:20480\n\t"
    "ds_read_b64_tr_b16 v[190:191], %2 offset:20992\n\t"
    "s_waitcnt lgkmcnt(0)"
    : "=&{v[160:191]}"(w) : "v"(a0), "v"(a1) : "memory");
}
template <> __device__ __forceinline__ void gw_load<2>(u32x32& w, unsigned a0, unsigned a1) {
  asm volatile("ds_read_b64_tr_b16 v[192:193], %1 offset:0\n\t"
    "ds_read_b64_tr_b16 v[194:195], %1 offset:512\n\t"
    "ds_read_b64_tr_b16 v[196:197], %2 offset:0\n\t"
    "ds_read_b64_tr_b16 v[198:199], %2 offset:512\n\t"
    "ds_read_b64_tr_b16 v[200:201], %1 offset:4096\n\t"
    "ds_read_b64_tr_b16 v[202:203], %1 offset:4608\n\t"
    "ds_read_b64_tr_b16 v[204:205], %2 offset:4096\n\t"
    "ds_read_b64_tr_b16 v[206:207], %2 offset:4608\n\t"
    "s_waitcnt lgkmcnt(0)\n\t"
    "ds_read_b64_tr_b16 v[208:209], %1 offset:16384\n\t"
    "ds_read_b64_tr_b16 v[210:211], %1 offset:16896\n\t"
    "ds_read_b64_tr_b16 v[212:213], %2 offset:16384\n\t"
    "ds_read_b64_tr_b16 v[214:215], %2 offset:16896\n\t"
    "ds_read_b64_tr_b16 v[216:217], %1 offset:20480\n\t"
    "ds_read_b64_tr_b16 v[218:219], %1 offset:20992\n\t"
    "ds_read_b64_tr_b16 v[220:221], %2 offset:20480\n\t"
    "ds_read_b64_tr_b16 v[222:223], %2 offset:20992\n\t"
    "s_waitcnt lgkmcnt(0)"
    : "=&{v[192:223]}"(w) : "v"(a0), "v"(a1) : "memory");
}
template <> __device__ __forceinline__ void gw_load<3>(u32x32& w, unsigned a0, unsigned a1) {
  asm volatile("ds_read_b64_tr_b16 v[224:225], %1 offset:0\n\t"
    "ds_read_b64_tr_b16 v[226:227], %1 offset:512\n\t"
    "ds_read_b64_tr_b16 v[228:229], %2 offset:0\n\t"
    "ds_read_b64_tr_b16 v[230:231], %2 offset:512\n\t"
    "ds_read_b64_tr_b16 v[232:233], %1 offset:4096\n\t"
    "ds_read_b64_tr_b16 v[234:235], %1 offset:4608\n\t"
    "ds_read_b64_tr_b16 v[236:237], %2 offset:4096\n\t"
    "ds_read_b64_tr_b16 v[238:239], %2 offset:4608\n\t"
    "s_waitcnt lgkmcnt(0)\n\t"
    "ds_read_b64_tr_b16 v[240:241], %1 offset:16384\n\t"
    "ds_read_b64_tr_b16 v[242:243], %1 offset:16896\n\t"
    "ds_read_b64_tr_b16 v[244:245], %2 offset:16384\n\t"
    "ds_read_b64_tr_b16 v[246:247], %2 offset:16896\n\t"
    "ds_read_b64_tr_b16 v[248:249], %1 offset:20480\n\t"
    "ds_read_b64_tr_b16 v[250:251], %1 offset:20992\n\t"
    "ds_read_b64_tr_b16 v[252:253], %2 offset:20480\n\t"
    "ds_read_b64_tr_b16 v[254:255], %2 offset:20992\n\t"
    "s_waitcnt lgkmcnt(0)"
    : "=&{v[224:255]}"(w) : "v"(a0), "v"(a1) : "memory");
}
template <int I, int E, class F> __device__ __forceinline__ void sfor(F&& f) { if constexpr (I < E) { f(std::integral_constant<int, I>{}); sfor<I + 1, E>(f); } }
#define WFB 0
template <int ROLE, int KQ, class F0, class F>
__device__ __forceinline__ void wload(char* smem, int wave_id_, const u16* W0, const u16* W1, int ldb, u32x32 (&bw)[4], int rot, F0&& hook0, F&& hook, unsigned long long* wts = nullptr) {
  constexpr int ABUF = 65536;
  const int tid = opaque_tid(), lane = tid & 63, wave = WAVE_ID;
  const int kq = KQ == 2 ? wave >> 2 : 0, nq = KQ == 2 ? wave & 3 : wave;
  const int g = lane >> 4, r16 = lane & 15, q4 = r16 >> 2, p4 = lane & 3;
  const unsigned ldsb = __builtin_amdgcn_readfirstlane((unsigned)(size_t)(LAS char*)smem);
  {
    const int srow = 8 * wave + (lane >> 3), ch = (lane & 7) ^ bswz(srow);
    const unsigned voff = (unsigned)(srow * ldb + ch * 8) * 2;
    const char* w0 = uni((const char*)W0); const char* w1 = uni((const char*)(ROLE == R_F1 ? W1 : W0 + 64));
    const size_t kb = (size_t)64 * ldb * 2;
    auto wsrc = [&](int r, int i) {
      const int kbi = KQ == 2 ? (ROLE == R_PART2 ? 0 : (i >> 2) * 8) + ((2 * r + ((i >> 1) & 1)) ^ rot) : 2 * r + (i >> 2);
      return KQ == 2 ? ((i & 1) ? w1 : w0) + kbi * kb + (ROLE == R_PART2 ? (i >> 2) * 256 : 0) : w0 + kbi * kb + (i & 3) * 128;
    };
    auto wstage = [&](int r) {
      const unsigned ld = ldsb + (r & 1) * ABUF + wave * 1024;
#pragma unroll
      for (int i = 0; i < 8; ++i) {
        unsigned keep;
        asm volatile("s_mov_b32 %0, m0\n\ts_mov_b32 m0, %3\n\ts_nop 0\n\tglobal_load_lds_dwordx4 %1, %2" WNT_ "\n\ts_mov_b32 m0, %0"
                     : "=&s"(keep) : "v"(voff), "s"(wsrc(r, i)), "s"(ld + i * 8192) : "memory");
      }
    };
    const int brow = 8 * g + q4, bs = bswz(brow);
    unsigned la[2];
#pragma unroll
    for (int j = 0; j < 2; ++j) {
      int blk, c16, sub;
      if (ROLE == R_F1) { blk = j; c16 = nq * 2 + (p4 >> 1); sub = (p4 & 1) * 8; }
      else { blk = nq >> 1; c16 = (nq & 1) * 4 + p4; sub = j * 8; }
      int set = kq;
      if (ROLE == R_PART2 && TLP) { set = nq >> 1; blk = nq & 1; c16 = kq * 4 + p4; }
      la[j] = (unsigned)((KQ == 2 ? set * 4 + blk : blk) * 8192 + brow * 128 + ((c16 ^ bs) << 4) + sub);
    }
    wstage(0); if (WFB) __builtin_amdgcn_s_barrier();
    wstage(1); if (WFB) __builtin_amdgcn_s_barrier();
    u32x4 raw[2][8];
#pragma unroll
    for (int r = 0; r < 2; ++r) {
#pragma unroll
      for (int i = 0; i < 8; ++i) asm volatile("global_load_dwordx4 %0, %1, %2" WNT_ : "=&v"(raw[r][i]) : "v"(voff), "s"(wsrc(r + 2, i)) : "memory");
      if (WFB && r == 0) __builtin_amdgcn_s_barrier();
    }
    sfor<0, 4>([&](auto r_c) __attribute__((always_inline)) {
      constexpr int r = decltype(r_c)::value;
      if (r == 0) WAIT_V(24); else if (r == 1) WAIT_V(16);
      if (r == 3) { BAR(); hook(); } else if (r < 2) { __builtin_amdgcn_s_barrier(); asm volatile("" ::: "memory"); }
      gw_load<r>(bw[r], ldsb + (r & 1) * ABUF + la[0], ldsb + (r & 1) * ABUF + la[1]);
      if (r == 1 || r == 2) {
        u32x4 (&q)[8] = raw[(r + 1) & 1];
        if (r == 1) hook0();
        if (r == 1) asm volatile("s_waitcnt vmcnt(8)" : "+v"(q[0]), "+v"(q[1]), "+v"(q[2]), "+v"(q[3]), "+v"(q[4]), "+v"(q[5]), "+v"(q[6]), "+v"(q[7]) :: "memory");
        else asm volatile("s_waitcnt vmcnt(0)" : "+v"(q[0]), "+v"(q[1]), "+v"(q[2]), "+v"(q[3]), "+v"(q[4]), "+v"(q[5]), "+v"(q[6]), "+v"(q[7]) :: "memory");
#pragma unroll
        for (int i = 0; i < 8; ++i) *(u32x4*)(smem + ((r + 1) & 1) * ABUF + wave * 1024 + i * 8192 + lane * 16) = q[i];
        if (r == 1) BAR();
      }
      if (r == 3) BAR();
    });
  }
}
#define DSR2(d0, d1, a0_, a1_, off) asm volatile("ds_read_b128 %0, %2 offset:%4\n\tds_read_b128 %1, %3 offset:%4" : "=&v"(d0), "=&v"(d1) : "v"(a0_), "v"(a1_), "i"(off))
template <int N> __device__ __forceinline__ void waitl(u32x4& a, u32x4& b) { asm volatile("s_waitcnt lgkmcnt(%2)" : "+v"(a), "+v"(b) : "i"(N)); }
__device__ __forceinline__ void glds1(const char* sbase, unsigned voff, unsigned lds) {
  unsigned keep;
  asm volatile("s_mov_b32 %0, m0\n\ts_mov_b32 m0, %3\n\ts_nop 0\n\tglobal_load_lds_dwordx4 %1, %2 sc1\n\ts_mov_b32 m0, %0"
               : "=&s"(keep) : "v"(voff), "s"(sbase), "s"(lds) : "memory");
}
#define XS(i) do { } while (0)
#define XFL(b_) do { } while (0)
#define XA_ 0
#define XF_ 0
#define XE_ 0
#define XP_ 0
#define PUBK 9
#define LATE 0
#define NOWP 2
#define NEARM 14
#define FASTM 1
#define PNEAR 2
#define LATEQ 0
#define DEFP 1
#define ROTM 0
#define ROTK 7
#define PXM 0
#define BPACE1 42
#define BPACE2 46
#define BPACE 36
#define W2 1
#define PDEEP 4
template <int ROLE>
__device__ __forceinline__ void gemm7(char* smem, int wave_id_, const u16* W0, const u16* W1, int ldb, const char* A_, size_t a_blk, Dep dep,
                                      char* C_, size_t c_blk, unsigned* myprog, unsigned* tmo, int slot, unsigned long long* mts,
                                      bool gl, int nimm = 0, int nseg = NSEG, int wm0 = 0, int pofs = 0, unsigned long long tag = 0, unsigned* par = nullptr) {
  volatile LAS unsigned* wsl = (volatile LAS unsigned*)(smem + L_SLOT + 512);
  if (opaque_tid() < 256) *(volatile LAS unsigned*)(smem + L_SLOT + opaque_tid() * 4) = 0u;
  const int wave = WAVE_ID;
  const int kq = wave >> 2, nq = wave & 3;
  const unsigned ldsb = __builtin_amdgcn_readfirstlane((unsigned)(size_t)(LAS char*)smem);
  const int rot = (ROTM >> ROLE) & 1 ? slot & ROTK : 0;
  const char* A = uni(A_) + nq * 1024;
  constexpr bool NEAR = (NEARM >> ROLE) & 1;
  constexpr bool FAST = (FASTM >> ROLE) & 1;
  constexpr int PD = NEAR ? PNEAR : PDEEP;
  constexpr bool P2C = ROLE == R_PART2;
  int pw = 0;
  int wm = wm0, nh = 0;
  auto lpoll = [&](int b, bool hs = false) {
    unsigned w;
    if (wave == 4) {
      for (int spins = 0;; ++spins) {
        unsigned v;
        if (hs) { WAIT_V(0); v = *(volatile LAS unsigned*)(smem + L_SLOT + 768 + lane_id() * 4); }
        else asm volatile("s_nop 4\n\tglobal_load_dword %0, %1, %2 sc1\n\ts_waitcnt vmcnt(0)" : "=&v"(v) : "v"(dep.voff), "s"(dep.base) : "memory");
        w = dep_eval(v, dep);
        if ((int)w > b) break;
        if (!hs) __builtin_amdgcn_s_sleep(2);
        hs = false;
        if (give_up(spins, tmo, 9)) { w = NSEG; break; }
      }
      w = w < 255u ? w : 255u;
      if (lane_id() == 0) wsl[0] = ((unsigned)(b + 1) << 8) | w;
    } else {
      for (int sp_ = 0;; ++sp_) {
        w = __builtin_amdgcn_readfirstlane(wsl[0]);
        if ((w >> 8) == (unsigned)(b + 1)) break;
        __builtin_amdgcn_s_sleep(1);
      }
      w &= 255u;
    }
    return (int)w;
  };
  auto pubw = [&](int n) {
    WAIT_V(0);
    if (lane_id() == 0) {
      const unsigned o = __hip_atomic_fetch_add((LAS unsigned*)(smem + L_SLOT + 516), 1u, __ATOMIC_RELAXED, __HIP_MEMORY_SCOPE_WORKGROUP);
      if ((o & 3u) == 3u) {
        st4_sc1(myprog, (unsigned)(n + pofs));
      }
    }
    asm volatile("s_waitcnt lgkmcnt(0)" ::: "memory");
  };
  auto hsrc = [&](int hh) { return A + (size_t)(hh >> 1) * a_blk + ((hh ^ (rot >> 2)) & 1) * 16384; };
  auto hlds = [&](int hh) { return ldsb + (unsigned)(((hh >> 1) & 1) * 65536 + (hh & 1) * 16384 + nq * 1024); };
#define POFF(i_) (((i_) & 1) * 32768 + ((i_) >> 1) * 4096)
#define PIECE(s_, l_, i_) asm volatile("s_add_u32 m0, %2, %3\n\ts_nop 0\n\tglobal_load_lds_dwordx4 %0, %1 sc1" :: "v"(av[i_]), "s"(s_), "s"(l_), "i"(POFF(i_)) : "memory", "scc", "m0")
  u32x32 BW[4];
  KTS(1);
  wload<ROLE, 2>(smem, wave_id_, W0, W1, ldb, BW, rot, [&]() {
    if (ROLE == R_QKV && nimm > 0 && wave == 7) ini_request((const char*)tmo - OFF_TMO, ldsb + L_GB);
  }, [&]() {
    if (ROLE == R_QKV && nimm > 0) {
      *par = ini_resolve((const char*)tmo - OFF_TMO, smem + L_GB, tag);
      dep.base += (*par >> 2) << 13; myprog += (*par >> 2) << 11;
    }
    if (kq) {
      if (wm0 > 0) wait_l1((const unsigned*)dep.base, 8u, tmo);
      else wm = lpoll(0);
      const unsigned l16 = lane_id() * 16;
#pragma unroll
      for (int i = 0; i < 16; ++i)
        if (!(P2C && (i & 1))) glds1(hsrc(i >> 3) + (i & 1) * 32768 + ((((i & 7) >> 1) ^ rot) & 3) * 4096, l16, hlds(i >> 3) + POFF(i & 7));
    }
    nh = 2;
  }, ROLE == R_QKV ? (unsigned long long*)((char*)tmo - OFF_TMO + ((size_t)246 << 20)) + (128 + slot) * 8 : nullptr);
  KTS(2);
  if (wm0 == 0 && !kq) wm = (int)(__builtin_amdgcn_readfirstlane(wsl[0]) & 255u);
  if (ROLE == R_QKV && opaque_tid() == 0) STS((char*)tmo - OFF_TMO, 128 + slot, 0);
  auto body = [&](auto ldr_c) __attribute__((always_inline)) {
    constexpr bool LDR = decltype(ldr_c)::value;
    if (XP_ == 0 ? !LDR : (XP_ == 1 ? LDR : false)) __builtin_amdgcn_s_setprio(1);
    unsigned av[8];
    const int lane = lane_id();
#pragma unroll
    for (int i = 0; i < 8; ++i) av[i] = lane * 16 + (P2C ? 0 : (i & 1) * 32768) + (((i >> 1) ^ rot) & 3) * 4096;
    auto issue_all = [&](int hh) {
      if (LDR) {
        const char* s_ = hsrc(hh); const unsigned l_ = hlds(hh);
#pragma unroll
        for (int i = 0; i < 8; ++i) if (!(P2C && (i & 1))) PIECE(s_, l_, i);
      }
    };
    int b = 0;
    bool hs = false, redo = false;
    int bnd = 200000;
#define BCK(k) do { } while (0)
    while (b < nseg) {
      const int ln = lane_id(), g = ln >> 4, r16 = ln & 15;
      const unsigned e0 = ldsb + (unsigned)((P2C ? 0 : kq * 32768) + (b & 1) * 65536 + (gl ? (g >> 1) * 512 + r16 * 32 + (g & 1) * 16 : r16 * 128 + ((g ^ (r16 & 7)) << 4)));
      const unsigned d0 = e0 ^ (gl ? 1024u : 64u);
      const unsigned rw = ldsb + L_RED + nq * 4096 + ln * 16;
      const unsigned co0 = ROLE == R_F1 ? (unsigned)((g & 1) * 2048 + nq * 512 + r16 * 32 + (g >> 1) * 16)
                         : ((ROLE == R_QKV ? TLQ : TLP) ? (unsigned)(nq * (P2C ? 4096 : 2048) + r16 * 64 + g * 16)
                         : (ROLE == R_QKV ? (unsigned)(r16 * 256 + (((4 * nq + g) ^ r16) << 4)) : (unsigned)(r16 * PROW + (32 * nq + 8 * g) * 2)));
      const unsigned co1 = co0 + ((ROLE == R_QKV ? TLQ : TLP) ? 1024u : (ROLE == R_QKV ? 4096u : 16u * PROW));
      u32x16 pv_;
      bool smp = false, lt0 = false;
      auto run = [&](auto ah_c) __attribute__((always_inline)) {
        constexpr int AH = decltype(ah_c)::value;
        __builtin_amdgcn_s_barrier(); asm volatile("" ::: "memory");
        for (; nh < 2 * b + AH; ++nh) issue_all(nh);
        int lf = (wm < nseg ? wm : nseg) - (AH - 1), cp = nseg - (AH - 1);
        if (!LDR) KTS(128 + b);
        const unsigned sa = ldsb + L_SLOT + ln * 4, ad = dep.add - (AH - 1);
        if constexpr (LDR) (AH == 2 ? (P2C ? near_ld2 : near_ld0) : (P2C ? fast_ld2 : fast_ld0))(BW, pv_, e0, d0, b, lf, cp, sa, dep.mul, ad, rw, (unsigned)a_blk, hsrc(2 * b + AH), hsrc(2 * b + AH + 1), hlds(2 * b + AH), hlds(2 * b + AH + 1), av);
        else {
          const char* c0_ = uni((const char*)tmo - OFF_TMO + OFF_DUMP); const char* c1_ = uni(C_ + (size_t)b * c_blk);
          const int sf = b + pofs - (pw > 0 ? 1 : 0);
          const char* pg = uni((const char*)myprog);
          (AH == 2 ? (ROLE == R_F1 ? near_st1 : (P2C ? near_st2 : near_st0)) : (ROLE == R_F1 ? fast_st1 : (P2C ? fast_st2 : fast_st0)))(BW, pv_, e0, d0, b, lf, cp, sa, dep.mul, ad, rw, (unsigned)c_blk, c0_, c1_, co0, co1, pofs, sf, (unsigned)wave,
                                                                   ldsb + L_SLOT, dep.voff, dep.base, pg);
        }
        if (!LDR) KTS(64 + b);
        wm = lf + (AH - 1) > wm ? lf + (AH - 1) : wm;
        nh = 2 * b + AH;
        --b;
        __builtin_amdgcn_s_barrier(); asm volatile("" ::: "memory");
      };
      if (FAST && (wm < nseg ? wm : nseg) >= b + PDEEP && b >= nimm && nh <= 2 * b + 3 && !redo) {
        run(std::integral_constant<int, 3>{});
      } else if (NEAR && (wm < nseg ? wm : nseg) >= b + PNEAR && b >= nimm && nh <= 2 * b + 2 && !redo) {
        run(std::integral_constant<int, 2>{});
      } else if (nh <= 2 * b + 2 && b >= (ROLE == R_QKV ? nseg - 1 : (gl ? BPACE2 : (ROLE == R_F1 ? BPACE1 : BPACE))) && !redo) {
#define PTA
        if constexpr (LDR) {
          int cap = wave == 4 ? SPIN_CAP : 8 * SPIN_CAP;
          if (hs) WAIT_V(0);
          (P2C ? paced_ld2 : paced_ld0)(BW, e0, d0, b, nseg, (unsigned)a_blk, rw, ldsb + L_SLOT + 528 + nq * 4, hsrc(nh), hsrc(nh + 1), hlds(nh), cap, ROLE == R_QKV ? 0 : BEARLY - PXM, wm, (unsigned)(nh - 2 * b),
                                        dep.mul, dep.add, dep.voff + (ROLE == R_QKV ? 0 : (ln >> 5) * 4), dep.base, wave == 4 ? ldsb + L_SLOT + 768 : 0u, ldsb + L_SLOT + 768 + (ln & 31) * 4, av PTA);
          if (cap == 0 && ln == 0) st4_sc1(tmo, 15u);
        } else (ROLE == R_F1 ? paced_st1 : (P2C ? paced_st2 : paced_st0))(BW, e0, d0, b, nseg, (unsigned)c_blk, rw, ldsb + L_SLOT + 528 + nq * 4, uni(C_ + (size_t)b * c_blk),
                                                                          __builtin_amdgcn_readfirstlane(pw > 0 ? 1u : 0u), wave == 0 ? 998 : -1, ROLE == R_QKV ? 999 : (gl ? BEG : (ROLE == R_F1 ? BEH : BEARLY)),
                                                                          ROLE == R_F1 ? BEG : BEARLY, ROLE == R_QKV ? 999 : (gl ? LAZY : (ROLE == R_F1 ? BEG : BEARLY)), pofs, ldsb + L_SLOT + 516,
                                                                          uni((const char*)myprog), co0, co1, bnd PTA);
        BCK(1);
        const int fb = (int)__builtin_amdgcn_readfirstlane(*(volatile LAS unsigned*)(smem + L_SLOT + 544 + nq * 4));
        if (fb == 0 && b < nseg) { nh = 2 * b + 2; wm = b + PXB; pw = b; hs = false; continue; }
        if (fb == 0) { if (!LDR && ROLE != R_QKV) pubw(nseg); break; }
        WAIT_V(0); BAR();
        if (LDR && ln == 0) { *(volatile LAS unsigned*)(smem + L_SLOT + 528 + nq * 4) = 0u; *(volatile LAS unsigned*)(smem + L_SLOT + 544 + nq * 4) = 0u; wsl[0] = 0u; }
        BAR();
        b = fb - 1; nh = 2 * b; wm = b; pw = 0; hs = false; redo = true;
        if (!LDR) pubw(b);
        continue;
      } else {
        const bool pl = wm <= b;
        XS(0);
        if (pl && LDR) wm = lpoll(b, hs);
        XS(1);
        const bool cold = nh < 2 * b + 2;
        { const int upto = nh == 2 * b + 3 ? 2 * b + 4 : 2 * b + 2; for (; nh < upto; ++nh) issue_all(nh); }
        smp = nh == 2 * b + 2 && b + 1 < nseg && wm0 < nseg;
        if (LDR && wave == 4 && smp)
          asm volatile("s_mov_b32 m0, %0\n\ts_nop 3\n\tglobal_load_lds_dword %1, %2 sc1" :: "s"(ldsb + L_SLOT + (b & 1) * 256), "v"(dep.voff), "s"(dep.base) : "memory", "m0");
        XS(2);
        if (LDR && LEAN_QB) { if (nh >= 2 * b + 4) { if (P2C) WAIT_V(14); else WAIT_V(28); } else { if (P2C) WAIT_V(6); else WAIT_V(12); } }
        else if (LDR) { if (nh >= 2 * b + 4) { if (P2C) WAIT_V(12); else WAIT_V(24); } else { if (P2C) WAIT_V(4); else WAIT_V(8); } }
        BAR();
        XS(3);
        if (pl && !LDR) wm = (int)(__builtin_amdgcn_readfirstlane(wsl[0]) & 255u);
        const bool dm = nh == 2 * b + 2 && wm > b + 1 && b + 1 < nseg;
        if (ROLE == R_QKV && b < 2 && opaque_tid() == 0) STS((char*)tmo - OFF_TMO, 128 + slot, 1 + b);
        lt0 = smp && !dm;
        if constexpr (LDR) {
          if (dm) (P2C ? lean_dm1 : lean_dm0)(BW, pv_, e0, d0, 0u, hlds(nh), hsrc(nh), hsrc(nh + 1), av);
          else (P2C ? lean_ld1 : lean_ld0)(BW, pv_, e0, d0, (unsigned)(nh - 2 * b - 2), hlds(nh), hsrc(nh), hsrc(nh + 1), av, ldsb + L_SLOT + (b & 1) * 256 + ln * 4, dep.mul, dep.add, LATEQ && lt0 ? b + 1 : 0x7FFFFFFF,
                                           __builtin_amdgcn_readfirstlane(lt0 && wave == 4 ? ldsb + L_SLOT + 768 : 0u), dep.voff, dep.base);
        }
        else lean_st0(BW, pv_, e0, d0, __builtin_amdgcn_readfirstlane(pw > 0 ? (cold ? 4u : 1u) : 0u), (unsigned)(pw + pofs), ldsb + L_SLOT + 516, ldsb + L_SLOT + 256,
                     dep.voff, dep.base, uni((const char*)myprog));
        if (dm) nh += 2;
        XS(5);
      }
      if constexpr (LDR) fin_ld(pv_, rw, ldsb + L_SLOT + 528 + nq * 4, (unsigned)(b + 1));
      else (ROLE == R_F1 ? fin_st1 : (P2C ? fin_st2 : fin_st0))(pv_, rw, ldsb + L_SLOT + 528 + nq * 4, (unsigned)(b + 1), uni(C_ + (size_t)b * c_blk), co0, co1, bnd);
      BCK(2);
      XS(6);
      if (smp) {
        const int w = (int)dep_eval(*(volatile LAS unsigned*)(smem + L_SLOT + (b & 1) * 256 + lane_id() * 4), dep);
        if (LATEQ && lt0 && w > b + 1) nh += 2;
        wm = w > wm ? w : wm;
      }
      bool now = (NOWP == 2 ? nh < 2 * b + 4 && wm < b + 1 + PD : wm <= b + 1) || b + 1 >= nseg || b < nimm;
      if (!LDR && now) {
        if (DEFP && b >= nimm && b + 1 < nseg && (wm > b + 1 || (__builtin_amdgcn_readfirstlane(wsl[0]) >> 8) == (unsigned)(b + 2))) now = false;
        else pubw(b + 1);
      }
      XS(7);
      XFL(b);
      pw = now ? 0 : b + 1;
      hs = W2 && lt0;
      redo = false;
      ++b;
    }
    __builtin_amdgcn_s_setprio(0);
  };
  if (kq) body(std::true_type{}); else body(std::false_type{});
#undef PIECE
#undef POFF
  WAIT_V(0);
  BAR();
  KTS(3);
}
__device__ __forceinline__ void fin_role(char* smem, int wave_id_, int fidx, const float* mod, const char* P2, const u16* x1, u16* out, unsigned* cnt, unsigned* tmo,
                                         unsigned long long* mts) {
  const int tid = opaque_tid(), lane = tid & 63, wave = WAVE_ID;
  row_table(smem + L_GB, mod, cnt, tmo, tid);
  volatile LAS unsigned* wsl = (volatile LAS unsigned*)(smem + L_SLOT + 512);
  int wf = 0, par = 0;
  for (int nf = fidx >> 3; nf < NSEG; nf += 2) {
    if (wf <= nf) {
      if (wave == 0) {
        unsigned w;
        for (int spins = 0;; ++spins) {
          u32x2 v2; asm volatile("global_load_dwordx2 %0, %1, off sc1\n\ts_waitcnt vmcnt(0)" : "=&v"(v2) : "v"(cnt + (PF2 + ((lane & 31) < 28 ? (lane & 31) : 0)) * 32) : "memory");
          const unsigned v = rowmin16(max(v2.x, v2.y));
          w = min(rdl(v, 0), rdl(v, 16));
          if ((int)w > nf) break;
          __builtin_amdgcn_s_sleep(2);
          if (give_up(spins, tmo, 9)) { w = NSEG; break; }
        }
        if (lane == 0) wsl[par] = w;
      }
      BAR();
      wf = (int)__builtin_amdgcn_readfirstlane(wsl[par]);
      par ^= 1;
    }
    int ln = lane; asm volatile("" : "+v"(ln));
    if (wave == 0) WTS(256 + 8 + fidx, nf / 2, 0);
    bool chk = nf >= BEH;
    for (int spins = 0; !fin_half(P2, x1, smem + L_GB + 12288, out, nf * 32 + 4 * (fidx & 7) + (wave >> 1), wave & 1, ln, chk); ++spins) {
      asm volatile("" ::: "memory");
      if (spins >= 3) {
        const unsigned v = ld4_sc1(cnt + (ln < 28 ? PF2 + ln : PLN + fidx) * 32);
        if (__all((ln < 28 ? v : 2 * v) > (unsigned)nf) || give_up(spins, tmo, 9)) chk = false;
      }
    }
    if (wave == 0) WTS(256 + 8 + fidx, nf / 2, 2);
  }
}
constexpr int Q_MEM = 0;
constexpr int Q_QK  = 16384;
constexpr int Q_TM  = 22528;
constexpr int Q_PQ  = 24576;
constexpr int Q_K   = 32768;
constexpr int Q_RING = 32768;
constexpr int NCHAIN = 96;
#define NQQ 6
#define NQK 4
#define E_AG 7
#define RDYI -1
#define ROTW 2
#define DQU 6
#define DQM 18
#define DQD 1
#define ORSEL (xg >> 1)
#define D_Q 96
#define D_O 3
#define D_A 0
#define WOF 1
#define E_S 0
#define SPF 0
#define STE 0
#define RAE 0
#define SPUB 62
#define QDL 0
#define KFB 1
#define E_B1 0
#define ADQ 1
#define D_V 4
#define DRY 0
#define D_0 72
#define KV3 0
static_assert(ARMW + 1 < BEARLY, "");
#define ADK 0
#define K_0 22
#define K_V 4
#define K_MIN 6
#define RAW 5
#define E_MIN 3
#define E_W 3
#define DOU 0
#define DOM 12
#define CL0 1
#define TSW 0
#define E_B 6
#define E_O 1900
#define KDEL 5
#define VPOS 0

typedef __bf16 bf16x16 __attribute__((ext_vector_type(16)));
typedef unsigned u32x8 __attribute__((ext_vector_type(8)));
__device__ __forceinline__ f32x4 SMF(u32x4 a, u32x8 b, f32x4 c, int idx) {
  return __builtin_amdgcn_smfmac_f32_16x16x64_bf16(__builtin_bit_cast(bf16x8, a), __builtin_bit_cast(bf16x16, b), c, idx, 0, 0);
}
__device__ __forceinline__ float fold32(float a, float b) {
  const auto p = __builtin_amdgcn_permlane32_swap(__float_as_uint(a), __float_as_uint(b), false, false);
  return __uint_as_float(p[0]) + __uint_as_float(p[1]);
}
__device__ __forceinline__ f32x2 rowsum(f32x4 x) { return f32x2{fold32(x[0], x[2]), fold32(x[1], x[3])}; }
__device__ __forceinline__ unsigned nosent1(unsigned v) { return v < 0xFFFFFFFEu ? v : 0xFFFFFFFEu; }
__device__ __forceinline__ void st4s(const char* sb, unsigned vo, unsigned v) { asm volatile("global_store_dword %0, %1, %2 sc1\n\ts_nop 0" :: "v"(vo), "v"(v), "s"(sb) : "memory"); }
__device__ __forceinline__ void st8s(const char* sb, unsigned vo, u32x2 v) { asm volatile("global_store_dwordx2 %0, %1, %2 sc1\n\ts_nop 1" :: "v"(vo), "v"(v), "s"(sb) : "memory"); }
__device__ __forceinline__ void gldsw(const char* sbase, unsigned voff, unsigned lds) {
  unsigned keep;
  asm volatile("s_mov_b32 %0, m0\n\ts_mov_b32 m0, %3\n\ts_nop 0\n\tglobal_load_lds_dwordx4 %1, %2" CNT_ "\n\ts_mov_b32 m0, %0"
               : "=&s"(keep) : "v"(voff), "s"(sbase), "s"(lds) : "memory");
}
#define PWC 1
#define PWL 0
__device__ __forceinline__ void wait_clk(unsigned t0, unsigned d) {
  while ((unsigned)__builtin_amdgcn_s_memtime() - t0 < d) __builtin_amdgcn_s_sleep(PWC);
}
__device__ __forceinline__ unsigned bell_time(const char* a, unsigned vo, unsigned tag, unsigned cap, unsigned v = 0) {
  for (unsigned spins = 0; !__all((v >> 24) >= tag); ++spins) {
    asm volatile("s_nop 4\n\tglobal_load_dword %0, %1, %2 sc1\n\ts_waitcnt vmcnt(0)" : "=&v"(v) : "v"(vo), "s"(a) : "memory");
    if (__all((v >> 24) >= tag) || spins >= cap) break;
  }
  const unsigned now = (unsigned)__builtin_amdgcn_s_memrealtime() + 250u;
  const unsigned age = rowmin16((now - v) & 0xFFFFFFu);
  unsigned r = __builtin_amdgcn_readfirstlane(now - min(min(min(rdl(age, 0), rdl(age, 16)), min(rdl(age, 32), rdl(age, 48))), 500u));
  asm volatile("" : "+s"(r));
  return r;
}
#define PWS 0
__device__ __forceinline__ void wait_rt(unsigned t0, int d) {
  for (;;) {
    const int r = d - (int)((unsigned)__builtin_amdgcn_s_memrealtime() - t0);
    if (r <= 0) break;
    if (PWS && r > 24) __builtin_amdgcn_s_sleep(6); else if (PWS && r > 9) __builtin_amdgcn_s_sleep(2);
  }
}
#define WBAR() do { __builtin_amdgcn_s_barrier(); asm volatile("" ::: "memory"); } while (0)
#define SB() __builtin_amdgcn_sched_barrier(0)
#define TN(k, v) do { } while (0)
template <bool HALF>
__device__ __forceinline__ void stage_proj(const char* src, unsigned lds, int tid, int wave) {
  const int row = tid >> 4, f = 2 * ((row & 1) | (((row >> 2) & 3) << 1));
#pragma unroll
  for (int j = 0; j < 4; ++j) {
    const int pc = (tid & 15) ^ f;
    gldsw(src + (size_t)j * 32 * HD * 2, (unsigned)(HALF ? (row + (pc >> 3) * 128) * HD * 2 + ((pc & 7) << 4) : row * HD * 2 + (pc << 4)), lds + (wave * 64 + 512 * j) * 16);
  }
}
__device__ __forceinline__ unsigned proj_base(int T, int lane) {
  const int g = lane >> 4, q4 = (lane & 15) >> 2, p4 = lane & 3, fs = 2 * ((q4 & 1) | ((g & 1) << 1) | ((q4 >> 1) << 2));
  return (unsigned)((16 * (g >> 1) + 4 * (g & 1) + (q4 & 1) + 8 * (q4 >> 1)) * 256 + (((2 * T) ^ fs) << 4) + p4 * 8);
}
__device__ __forceinline__ f32x4 zero4() { float z = 0.0f; asm volatile("" : "+v"(z)); return f32x4{z, z, z, z}; }
__device__ __forceinline__ unsigned lds_word(const char* p) { return __builtin_amdgcn_readfirstlane(*(volatile LAS unsigned*)p); }

#define MFMA16(a, b, c) __builtin_amdgcn_mfma_f32_16x16x16bf16_1k((a), (b), (c), 0, 0, 0)
#define UF(x) __uint_as_float(x)
__device__ __forceinline__ unsigned umax3(unsigned a, unsigned b, unsigned c) { return max(max(a, b), c); }
__device__ __forceinline__ f32x2 logit2(float a, float b) {
  return f32x2{a, b};
}
constexpr float LSC = LOG2E * SCALE;

#define FCS() do { } while (0)
struct QKW { u32x32 w0, w1, w2, w3, w4, w5; };
template <int W_> __device__ __forceinline__ void qk_step(const QKW& W, const char* ag, const char* vs, const char* v2, const char* db, const char* od, unsigned pb, unsigned sn, unsigned& cap, unsigned& sp,
    unsigned long long& tc, unsigned long long* ts, unsigned& d0, unsigned& d1);
template <> __device__ __forceinline__ void qk_step<0>(const QKW& W, const char* ag, const char* vs, const char* v2, const char* db, const char* od, unsigned pb, unsigned sn, unsigned& cap, unsigned& sp,
    unsigned long long& tc, unsigned long long* ts, unsigned& d0, unsigned& d1) {
  unsigned st, bg;
  asm volatile("v_mbcnt_lo_u32_b32 v60, -1, 0\n\t"
    "v_mbcnt_hi_u32_b32 v60, -1, v60\n\t"
    "v_lshlrev_b32 v5, 4, v60\n\t"
    "s_mov_b32 %[sp], 0\n\t"
    "s_setprio 2\n\t"
    "global_load_dwordx4 v[24:27], v5, %[ag] sc1\n\t"
    "global_load_dwordx4 v[28:31], v5, %[ag] offset:1024 sc1\n\t"
    "s_memtime %[tc]\n\t"
    "v_lshrrev_b32 v32, 4, v60\n\t"
    "v_and_b32 v33, 15, v60\n\t"
    "v_lshrrev_b32 v34, 2, v33\n\t"
    "v_and_b32 v35, 3, v60\n\t"
    "v_and_b32 v61, 1, v60\n\t"
    "v_lshlrev_b32 v61, 3, v61\n\t"
    "v_lshl_or_b32 v7, v34, 4, v61\n\t"
    "v_lshl_or_b32 v7, v32, 7, v7\n\t"
    "v_and_b32 v10, 2, v34\n\t"
    "v_xor_b32 v10, v10, v32\n\t"
    "v_lshl_or_b32 v10, v10, 4, %[pb]\n\t"
    "v_lshl_or_b32 v10, v33, 6, v10\n\t"
    "v_and_b32 v14, 32, v60\n\t"
    "v_lshl_or_b32 v14, v35, 3, v14\n\t"
    "v_lshrrev_b32 v34, 2, v60\n\t"
    "v_lshl_or_b32 v14, v34, 6, v14\n\t"
    "v_or_b32 v14, %[pb], v14\n\t"
    "v_and_b32 v61, 3, v60\n\t"
    "v_lshlrev_b32 v61, 6, v61\n\t"
    "v_lshl_or_b32 v4, v32, 4, v61\n\t"
    "v_and_b32 v61, 4, v60\n\t"
    "v_lshl_or_b32 v4, v61, 10, v4\n\t"
    "v_and_b32 v61, 8, v60\n\t"
    "v_lshl_or_b32 v4, v61, 14, v4\n\t"
    "v_or_b32 v4, 0, v4\n\t"
    "v_and_b32 v61, 2, v60\n\t"
    "v_mul_u32_u24 v61, 0x5555, v61\n\t"
    "v_or_b32 v6, %[c44], v61\n\t"
    "v_lshlrev_b32 v61, 2, v32\n\t"
    "v_lshl_or_b32 v15, v33, 4, v61\n\t"
    "3:\n\t"
    "v_mov_b32 v16, 0\n\t"
    "v_mov_b32 v17, 0\n\t"
    "v_mov_b32 v18, 0\n\t"
    "v_mov_b32 v19, 0\n\t"
    "v_mov_b32 v20, 0\n\t"
    "v_mov_b32 v21, 0\n\t"
    "v_mov_b32 v22, 0\n\t"
    "v_mov_b32 v23, 0\n\t"
    "s_waitcnt vmcnt(0) lgkmcnt(0)\n\t"
    "33:\n\t"
    "ds_write_b128 v5, v[24:27] offset:0\n\t"
    "ds_write_b128 v5, v[28:31] offset:1024\n\t"
    "v_smfmac_f32_16x16x64_bf16 v[16:19], v[24:27], v[64:71], v6\n\t"
    "v_max3_u32 v60, v24, v25, v26\n\t"
    "v_max3_u32 v61, v27, v28, v29\n\t"
    "v_smfmac_f32_16x16x64_bf16 v[20:23], v[24:27], v[72:79], v6\n\t"
    "v_max3_u32 v60, v60, v30, v31\n\t"
    "v_max_u32 v60, v60, v61\n\t"
    "v_cmp_eq_u32 vcc, -1, v60\n\t"
    "s_cbranch_vccnz 1f\n\t"
    "2:\n\t"
    "s_waitcnt lgkmcnt(0)\n\t"
    "s_barrier\n\t"
    "ds_read_b128 v[32:35], v5 offset:2048\n\t"
    "ds_read_b128 v[36:39], v5 offset:3072\n\t"
    "v_smfmac_f32_16x16x64_bf16 v[16:19], v[28:31], v[80:87], v6\n\t"
    "ds_read_b128 v[40:43], v5 offset:4096\n\t"
    "ds_read_b128 v[44:47], v5 offset:5120\n\t"
    "v_smfmac_f32_16x16x64_bf16 v[20:23], v[28:31], v[88:95], v6\n\t"
    "s_waitcnt lgkmcnt(3)\n\t"
    "v_smfmac_f32_16x16x64_bf16 v[16:19], v[32:35], v[96:103], v6\n\t"
    "ds_read_b128 v[48:51], v5 offset:6144\n\t"
    "v_smfmac_f32_16x16x64_bf16 v[20:23], v[32:35], v[104:111], v6\n\t"
    "ds_read_b128 v[52:55], v5 offset:7168\n\t"
    "s_waitcnt lgkmcnt(4)\n\t"
    "v_smfmac_f32_16x16x64_bf16 v[16:19], v[36:39], v[112:119], v6\n\t"
    "ds_read_b128 v[24:27], v5 offset:8192\n\t"
    "v_smfmac_f32_16x16x64_bf16 v[20:23], v[36:39], v[120:127], v6\n\t"
    "ds_read_b128 v[28:31], v5 offset:9216\n\t"
    "s_waitcnt lgkmcnt(5)\n\t"
    "v_smfmac_f32_16x16x64_bf16 v[16:19], v[40:43], v[128:135], v6\n\t"
    "ds_read_b128 v[56:59], v5 offset:10240\n\t"
    "v_smfmac_f32_16x16x64_bf16 v[20:23], v[40:43], v[136:143], v6\n\t"
    "ds_read_b128 v[60:63], v5 offset:11264\n\t"
    "ds_read_b128 v[36:39], v10 offset:35840\n\t"
    "s_waitcnt lgkmcnt(7)\n\t"
    "v_smfmac_f32_16x16x64_bf16 v[16:19], v[44:47], v[144:151], v6\n\t"
    "ds_read_b128 v[40:43], v10 offset:36864\n\t"
    "v_smfmac_f32_16x16x64_bf16 v[20:23], v[44:47], v[152:159], v6\n\t"
    "ds_read_b128 v[44:47], v10 offset:37888\n\t"
    "s_waitcnt lgkmcnt(8)\n\t"
    "v_smfmac_f32_16x16x64_bf16 v[16:19], v[48:51], v[160:167], v6\n\t"
    "v_smfmac_f32_16x16x64_bf16 v[20:23], v[48:51], v[168:175], v6\n\t"
    "ds_read_b128 v[48:51], v10 offset:38912\n\t"
    "s_waitcnt lgkmcnt(8)\n\t"
    "v_smfmac_f32_16x16x64_bf16 v[16:19], v[52:55], v[176:183], v6\n\t"
    "v_smfmac_f32_16x16x64_bf16 v[20:23], v[52:55], v[184:191], v6\n\t"
    "ds_read_b128 v[52:55], v10 offset:39936\n\t"
    "s_waitcnt lgkmcnt(8)\n\t"
    "v_smfmac_f32_16x16x64_bf16 v[16:19], v[24:27], v[192:199], v6\n\t"
    "v_smfmac_f32_16x16x64_bf16 v[20:23], v[24:27], v[200:207], v6\n\t"
    "ds_read_b128 v[24:27], v10 offset:32768\n\t"
    "s_waitcnt lgkmcnt(8)\n\t"
    "v_smfmac_f32_16x16x64_bf16 v[16:19], v[28:31], v[208:215], v6\n\t"
    "ds_read_b32 v8, v15 offset:28672\n\t"
    "ds_read_b128 v[32:35], v5 offset:24576\n\t"
    "s_setprio 3\n\t"
    "s_waitcnt lgkmcnt(9)\n\t"
    "v_smfmac_f32_16x16x64_bf16 v[16:19], v[56:59], v[224:231], v6\n\t"
    "s_waitcnt lgkmcnt(8)\n\t"
    "v_smfmac_f32_16x16x64_bf16 v[16:19], v[60:63], v[240:247], v6\n\t"
    "v_smfmac_f32_16x16x64_bf16 v[20:23], v[28:31], v[216:223], v6\n\t"
    "s_waitcnt lgkmcnt(0)\n\t"
    "21:\n\t"
    "v_cmp_ne_u32 vcc, %[sn], v8\n\t"
    "s_cbranch_vccnz 22f\n\t"
    "v_mul_f32 v32, %[lsc], v32\n\t"
    "v_mul_f32 v33, %[lsc], v33\n\t"
    "v_smfmac_f32_16x16x64_bf16 v[20:23], v[56:59], v[232:239], v6\n\t"
    "v_mul_f32 v34, %[lsc], v34\n\t"
    "v_mul_f32 v35, %[lsc], v35\n\t"
    "v_add_f32 v16, v16, v18\n\t"
    "v_add_f32 v17, v17, v19\n\t"
    "v_smfmac_f32_16x16x64_bf16 v[20:23], v[60:63], v[248:255], v6\n\t"
    "v_fma_f32 v16, v16, %[lsc], v32\n\t"
    "v_fma_f32 v17, v17, %[lsc], v33\n\t"
    "v_cvt_pk_bf16_f32 v16, v16, v17\n\t"
    "ds_write_b32 v15, v16 offset:16384\n\t"
    "s_mov_b32 %[bg], %[big]\n\t"
    "s_nop 2\n\t"
    "v_add_f32 v20, v20, v22\n\t"
    "v_add_f32 v21, v21, v23\n\t"
    "v_fma_f32 v20, v20, %[lsc], v34\n\t"
    "v_fma_f32 v21, v21, %[lsc], v35\n\t"
    "v_cvt_pk_bf16_f32 v20, v20, v21\n\t"
    "ds_write_b32 v15, v20 offset:16640\n\t"
    "ds_read_b128 v[28:31], v10 offset:33792\n\t"
    "ds_read_b128 v[32:35], v10 offset:34816\n\t"
    "s_waitcnt lgkmcnt(2)\n\t"
    "s_barrier\n\t"
    "ds_read_b64_tr_b16 v[16:17], v7 offset:16384\n\t"
    "ds_read_b64_tr_b16 v[18:19], v7 offset:16448\n\t"
    "ds_read_b64_tr_b16 v[20:21], v7 offset:16896\n\t"
    "ds_read_b64_tr_b16 v[22:23], v7 offset:16960\n\t"
    "ds_read_b64_tr_b16 v[56:57], v7 offset:17408\n\t"
    "ds_read_b64_tr_b16 v[58:59], v7 offset:17472\n\t"
    "ds_read_b64_tr_b16 v[60:61], v7 offset:17920\n\t"
    "ds_read_b64_tr_b16 v[62:63], v7 offset:17984\n\t"
    "global_load_dwordx4 v[6:9], v5, %[vs] sc1\n\t"
    "global_load_dwordx4 v[10:13], v5, %[vs] offset:1024 sc1\n\t"
    "s_waitcnt lgkmcnt(6)\n\t"
    "v_mfma_f32_16x16x32_bf16 v[24:27], v[24:27], v[16:19], 0\n\t"
    "v_mfma_f32_16x16x32_bf16 v[28:31], v[28:31], v[16:19], 0\n\t"
    "s_waitcnt lgkmcnt(4)\n\t"
    "v_mfma_f32_16x16x32_bf16 v[24:27], v[32:35], v[20:23], v[24:27]\n\t"
    "v_mfma_f32_16x16x32_bf16 v[28:31], v[36:39], v[20:23], v[28:31]\n\t"
    "v_xor_b32 v39, 32, v14\n\t"
    "ds_read_b64_tr_b16 v[32:33], v14 offset:40960\n\t"
    "ds_read_b64_tr_b16 v[34:35], v14 offset:41984\n\t"
    "ds_read_b64_tr_b16 v[36:37], v39 offset:40960\n\t"
    "ds_read_b64_tr_b16 v[38:39], v39 offset:41984\n\t"
    "s_waitcnt lgkmcnt(6)\n\t"
    "v_mfma_f32_16x16x32_bf16 v[24:27], v[40:43], v[56:59], v[24:27]\n\t"
    "v_mfma_f32_16x16x32_bf16 v[28:31], v[44:47], v[56:59], v[28:31]\n\t"
    "s_waitcnt lgkmcnt(4)\n\t"
    "v_mfma_f32_16x16x32_bf16 v[24:27], v[48:51], v[60:63], v[24:27]\n\t"
    "v_mfma_f32_16x16x32_bf16 v[28:31], v[52:55], v[60:63], v[28:31]\n\t"
    "v_mov_b32 v52, %[one]\n\t"
    "v_mov_b32 v53, %[one]\n\t"
    "v_mov_b32 v54, %[one]\n\t"
    "v_mov_b32 v55, %[one]\n\t"
    "v_mov_b32 v45, 0\n\t"
    "v_mov_b32 v47, 0\n\t"
    "v_mov_b32 v48, %[one]\n\t"
    "v_max3_f32 v14, |v24|, |v25|, |v26|\n\t"
    "v_max3_f32 v40, |v27|, |v28|, |v29|\n\t"
    "v_max3_f32 v14, v14, |v30|, |v31|\n\t"
    "v_max_f32 v14, v14, v40\n\t"
    "v_cmp_nge_f32 vcc, %[big], v14\n\t"
    "v_mov_b32 v14, 0\n\t"
    "s_cbranch_vccnz 12f\n\t"
    "13:\n\t"
    "v_exp_f32 v24, v24\n\t"
    "v_exp_f32 v25, v25\n\t"
    "v_exp_f32 v26, v26\n\t"
    "v_exp_f32 v27, v27\n\t"
    "v_exp_f32 v28, v28\n\t"
    "v_exp_f32 v29, v29\n\t"
    "v_exp_f32 v30, v30\n\t"
    "v_exp_f32 v31, v31\n\t"
    "v_cvt_pk_bf16_f32 v40, v24, v25\n\t"
    "v_cvt_pk_bf16_f32 v41, v26, v27\n\t"
    "v_cvt_pk_bf16_f32 v42, v28, v29\n\t"
    "v_cvt_pk_bf16_f32 v43, v30, v31\n\t"
    "v_mov_b32 v49, 0\n\t"
    "v_mov_b32 v51, 0\n\t"
    "s_waitcnt lgkmcnt(0)\n\t"
    "v_mfma_f32_16x16x32_bf16 v[52:55], v[52:55], v[40:43], 0\n\t"
    "v_mfma_f32_16x16x32_bf16 v[24:27], v[32:35], v[40:43], 0\n\t"
    "v_mfma_f32_16x16x32_bf16 v[28:31], v[36:39], v[40:43], 0\n\t"
    "s_waitcnt vmcnt(1)\n\t"
    "5:\n\t"
    "v_mfma_f32_16x16x32_bf16 v[32:35], v[6:9], v[16:19], 0\n\t"
    "v_mfma_f32_16x16x32_bf16 v[36:39], v[6:9], v[20:23], 0\n\t"
    "s_waitcnt vmcnt(0)\n\t"
    "v_mfma_f32_16x16x32_bf16 v[32:35], v[10:13], v[56:59], v[32:35]\n\t"
    "v_mfma_f32_16x16x32_bf16 v[36:39], v[10:13], v[60:63], v[36:39]\n\t"
    "ds_read_b32 v44, v15 offset:18432\n\t"
    "ds_read_b32 v46, v15 offset:18688\n\t"
    "v_max3_u32 v40, v6, v7, v8\n\t"
    "v_max3_u32 v41, v9, v10, v11\n\t"
    "v_max3_u32 v40, v40, v12, v13\n\t"
    "v_max_u32 v40, v40, v41\n\t"
    "v_cmp_eq_u32 vcc, -1, v40\n\t"
    "s_cbranch_vccnz 4f\n\t"
    "6:\n\t"
    "s_nop 0\n\t"
    "v_add_f32 v32, v32, v38\n\t"
    "v_add_f32 v33, v33, v39\n\t"
    "v_exp_f32 v34, v32\n\t"
    "v_max_f32 v40, v32, v33\n\t"
    "v_exp_f32 v35, v33\n\t"
    "v_cmp_nge_f32 vcc, %[bg], v40\n\t"
    "s_cbranch_vccnz 10f\n\t"
    "11:\n\t"
    "v_cvt_pk_bf16_f32 v50, v34, v35\n\t"
    "s_nop 1\n\t"
    "v_mfma_f32_16x16x16_bf16 v[52:55], v[48:49], v[50:51], v[52:55]\n\t"
    "17:\n\t"
    "s_waitcnt vmcnt(0) lgkmcnt(0)\n\t"
    "v_mfma_f32_16x16x16_bf16 v[32:35], v[44:45], v[50:51], v[24:27]\n\t"
    "v_mfma_f32_16x16x16_bf16 v[36:39], v[46:47], v[50:51], v[28:31]\n\t"
    "v_max_u32 v40, v44, v46\n\t"
    "v_cmp_eq_u32 vcc, -1, v40\n\t"
    "s_nop 2\n\t"
    "v_rcp_f32 v42, v52\n\t"
    "s_cbranch_vccnz 18f\n\t"
    "19:\n\t"
    "v_pk_mul_f32 v[32:33], v[32:33], v[42:43] op_sel_hi:[1,0]\n\t"
    "v_pk_mul_f32 v[34:35], v[34:35], v[42:43] op_sel_hi:[1,0]\n\t"
    "v_pk_mul_f32 v[36:37], v[36:37], v[42:43] op_sel_hi:[1,0]\n\t"
    "v_pk_mul_f32 v[38:39], v[38:39], v[42:43] op_sel_hi:[1,0]\n\t"
    "v_cvt_pk_bf16_f32 v24, v32, v33\n\t"
    "v_cvt_pk_bf16_f32 v25, v34, v35\n\t"
    "v_cvt_pk_bf16_f32 v26, v36, v37\n\t"
    "v_cvt_pk_bf16_f32 v27, v38, v39\n\t"
    "v_min_u32 v24, -2, v24\n\t"
    "v_min_u32 v25, -2, v25\n\t"
    "v_min_u32 v26, -2, v26\n\t"
    "v_min_u32 v27, -2, v27\n\t"
    "global_store_dwordx4 v4, v[24:27], %[od] sc1\n\t"
    "ds_write_b128 v5, v[6:9] offset:20480\n\t"
    "s_setprio 1\n\t"
    "s_waitcnt lgkmcnt(0)\n\t"
    "s_barrier\n\t"
    "s_waitcnt lgkmcnt(0)\n\t"
    "s_branch 9f\n\t"
    "1:\n\t"
    "s_add_u32 %[sp], %[sp], 1\n\t"
    "s_cmp_ge_u32 %[sp], %[cap]\n\t"
    "s_cbranch_scc1 7f\n\t"
    "global_load_dwordx4 v[24:27], v5, %[ag] sc1\n\t"
    "global_load_dwordx4 v[28:31], v5, %[ag] offset:1024 sc1\n\t"
    "s_waitcnt lgkmcnt(0)\n\t"
    "s_memtime %[tc]\n\t"
    "s_nop 7\n\t"
    "s_branch 3b\n\t"
    "7:\n\t"
    "s_mov_b32 %[cap], 0\n\t"
    "s_branch 2b\n\t"
    "4:\n\t"
    "s_add_u32 %[sp], %[sp], 0x10000\n\t"
    "s_lshr_b32 %[st], %[sp], 16\n\t"
    "s_cmp_ge_u32 %[st], %[cap]\n\t"
    "s_cbranch_scc1 8f\n\t"
    "global_load_dwordx4 v[6:9], v5, %[vs] sc1\n\t"
    "global_load_dwordx4 v[10:13], v5, %[vs] offset:1024 sc1\n\t"
    "s_waitcnt vmcnt(0) lgkmcnt(0)\n\t"
    "s_branch 5b\n\t"
    "8:\n\t"
    "s_mov_b32 %[cap], 0\n\t"
    "s_branch 6b\n\t"
    "18:\n\t"
    "s_add_u32 %[sp], %[sp], 0x10000\n\t"
    "s_lshr_b32 %[st], %[sp], 16\n\t"
    "s_cmp_ge_u32 %[st], %[cap]\n\t"
    "s_cbranch_scc1 20f\n\t"
    "ds_read_b32 v44, v15 offset:18432\n\t"
    "ds_read_b32 v46, v15 offset:18688\n\t"
    "s_waitcnt vmcnt(0) lgkmcnt(0)\n\t"
    "s_branch 17b\n\t"
    "20:\n\t"
    "s_mov_b32 %[cap], 0\n\t"
    "s_branch 19b\n\t"
    "22:\n\t"
    "ds_read_b32 v8, v15 offset:28672\n\t"
    "ds_read_b128 v[32:35], v5 offset:24576\n\t"
    "s_waitcnt lgkmcnt(0)\n\t"
    "s_branch 21b\n\t"
    "10:\n\t"
    "v_max_f32 v40, v40, v14\n\t"
    "v_mov_b32 v42, v40\n\t"
    "s_nop 1\n\t"
    "v_permlane16_swap_b32 v40, v42\n\t"
    "v_max_f32 v40, v40, v42\n\t"
    "v_mov_b32 v42, v40\n\t"
    "s_nop 1\n\t"
    "v_permlane32_swap_b32 v40, v42\n\t"
    "v_max_f32 v40, v40, v42\n\t"
    "v_sub_f32 v34, v32, v40\n\t"
    "v_sub_f32 v35, v33, v40\n\t"
    "v_sub_f32 v40, v14, v40\n\t"
    "v_exp_f32 v40, v40\n\t"
    "v_exp_f32 v34, v34\n\t"
    "v_exp_f32 v35, v35\n\t"
    "v_mul_f32 v24, v24, v40\n\t"
    "v_mul_f32 v25, v25, v40\n\t"
    "v_mul_f32 v26, v26, v40\n\t"
    "v_mul_f32 v27, v27, v40\n\t"
    "v_mul_f32 v28, v28, v40\n\t"
    "v_mul_f32 v29, v29, v40\n\t"
    "v_mul_f32 v30, v30, v40\n\t"
    "v_mul_f32 v31, v31, v40\n\t"
    "v_mul_f32 v52, v52, v40\n\t"
    "v_mul_f32 v53, v53, v40\n\t"
    "v_mul_f32 v54, v54, v40\n\t"
    "v_mul_f32 v55, v55, v40\n\t"
    "s_branch 11b\n\t"
    "12:\n\t"
    "v_max3_f32 v14, v24, v25, v26\n\t"
    "v_max3_f32 v40, v27, v28, v29\n\t"
    "v_max3_f32 v14, v14, v30, v31\n\t"
    "v_max3_f32 v14, v14, v40, %[neg]\n\t"
    "v_mov_b32 v40, v14\n\t"
    "s_nop 1\n\t"
    "v_permlane16_swap_b32 v14, v40\n\t"
    "v_max_f32 v14, v14, v40\n\t"
    "v_mov_b32 v40, v14\n\t"
    "s_nop 1\n\t"
    "v_permlane32_swap_b32 v14, v40\n\t"
    "v_max_f32 v14, v14, v40\n\t"
    "v_sub_f32 v24, v24, v14\n\t"
    "v_sub_f32 v25, v25, v14\n\t"
    "v_sub_f32 v26, v26, v14\n\t"
    "v_sub_f32 v27, v27, v14\n\t"
    "v_sub_f32 v28, v28, v14\n\t"
    "v_sub_f32 v29, v29, v14\n\t"
    "v_sub_f32 v30, v30, v14\n\t"
    "v_sub_f32 v31, v31, v14\n\t"
    "s_mov_b32 %[bg], 0x7fc00000\n\t"
    "s_branch 13b\n\t"
    "9:"
    : [tc] "=&s"(tc), [sp] "=&s"(sp), [cap] "+s"(cap), [st] "=&s"(st), [bg] "=&s"(bg)
    : [ag] "s"(ag), [c44] "s"(0x4444u), [lsc] "s"(LSC), [sn] "s"(sn), [pb] "s"(pb), [vs] "s"(vs), [od] "s"(od), [neg] "s"(-1.0e38f), [one] "s"(0x3F803F80u), [big] "s"(24.0f), "{v[64:95]}"(W.w0), "{v[96:127]}"(W.w1), "{v[128:159]}"(W.w2), "{v[160:191]}"(W.w3), "{v[192:223]}"(W.w4), "{v[224:255]}"(W.w5)
    : "v4", "v5", "v6", "v7", "v8", "v9", "v10", "v11", "v12", "v13", "v14", "v15", "v16", "v17", "v18", "v19", "v20", "v21", "v22", "v23", "v24", "v25", "v26", "v27", "v28", "v29", "v30", "v31", "v32", "v33", "v34", "v35", "v36", "v37", "v38", "v39", "v40", "v41", "v42", "v43", "v44", "v45", "v46", "v47", "v48", "v49", "v50", "v51", "v52", "v53", "v54", "v55", "v56", "v57", "v58", "v59", "v60", "v61", "v62", "v63", "vcc", "scc", "m0", "memory");
}
template <> __device__ __forceinline__ void qk_step<1>(const QKW& W, const char* ag, const char* vs, const char* v2, const char* db, const char* od, unsigned pb, unsigned sn, unsigned& cap, unsigned& sp,
    unsigned long long& tc, unsigned long long* ts, unsigned& d0, unsigned& d1) {
  unsigned st, bg;
  asm volatile("v_mbcnt_lo_u32_b32 v60, -1, 0\n\t"
    "v_mbcnt_hi_u32_b32 v60, -1, v60\n\t"
    "v_lshlrev_b32 v5, 4, v60\n\t"
    "s_mov_b32 %[sp], 0\n\t"
    "s_setprio 2\n\t"
    "global_load_dwordx4 v[24:27], v5, %[ag] sc1\n\t"
    "global_load_dwordx4 v[28:31], v5, %[ag] offset:1024 sc1\n\t"
    "s_memtime %[tc]\n\t"
    "v_lshrrev_b32 v32, 4, v60\n\t"
    "v_and_b32 v33, 15, v60\n\t"
    "v_lshrrev_b32 v34, 2, v33\n\t"
    "v_and_b32 v35, 3, v60\n\t"
    "v_and_b32 v61, 1, v60\n\t"
    "v_lshlrev_b32 v61, 3, v61\n\t"
    "v_lshl_or_b32 v7, v34, 4, v61\n\t"
    "v_lshl_or_b32 v7, v32, 7, v7\n\t"
    "v_and_b32 v10, 2, v34\n\t"
    "v_xor_b32 v10, v10, v32\n\t"
    "v_lshl_or_b32 v10, v10, 4, %[pb]\n\t"
    "v_lshl_or_b32 v10, v33, 6, v10\n\t"
    "v_and_b32 v14, 32, v60\n\t"
    "v_lshl_or_b32 v14, v35, 3, v14\n\t"
    "v_lshrrev_b32 v34, 2, v60\n\t"
    "v_lshl_or_b32 v14, v34, 6, v14\n\t"
    "v_or_b32 v14, %[pb], v14\n\t"
    "v_and_b32 v61, 3, v60\n\t"
    "v_lshlrev_b32 v61, 6, v61\n\t"
    "v_lshl_or_b32 v4, v32, 4, v61\n\t"
    "v_and_b32 v61, 4, v60\n\t"
    "v_lshl_or_b32 v4, v61, 10, v4\n\t"
    "v_and_b32 v61, 8, v60\n\t"
    "v_lshl_or_b32 v4, v61, 14, v4\n\t"
    "v_or_b32 v4, 256, v4\n\t"
    "v_and_b32 v61, 2, v60\n\t"
    "v_mul_u32_u24 v61, 0x5555, v61\n\t"
    "v_or_b32 v6, %[c44], v61\n\t"
    "v_lshlrev_b32 v61, 2, v32\n\t"
    "v_lshl_or_b32 v15, v33, 4, v61\n\t"
    "3:\n\t"
    "v_mov_b32 v16, 0\n\t"
    "v_mov_b32 v17, 0\n\t"
    "v_mov_b32 v18, 0\n\t"
    "v_mov_b32 v19, 0\n\t"
    "v_mov_b32 v20, 0\n\t"
    "v_mov_b32 v21, 0\n\t"
    "v_mov_b32 v22, 0\n\t"
    "v_mov_b32 v23, 0\n\t"
    "s_waitcnt vmcnt(0) lgkmcnt(0)\n\t"
    "33:\n\t"
    "ds_write_b128 v5, v[24:27] offset:2048\n\t"
    "ds_write_b128 v5, v[28:31] offset:3072\n\t"
    "v_smfmac_f32_16x16x64_bf16 v[16:19], v[24:27], v[96:103], v6\n\t"
    "v_max3_u32 v60, v24, v25, v26\n\t"
    "v_max3_u32 v61, v27, v28, v29\n\t"
    "v_smfmac_f32_16x16x64_bf16 v[20:23], v[24:27], v[104:111], v6\n\t"
    "v_max3_u32 v60, v60, v30, v31\n\t"
    "v_max_u32 v60, v60, v61\n\t"
    "v_cmp_eq_u32 vcc, -1, v60\n\t"
    "s_cbranch_vccnz 1f\n\t"
    "2:\n\t"
    "s_waitcnt lgkmcnt(0)\n\t"
    "s_barrier\n\t"
    "ds_read_b128 v[32:35], v5 offset:0\n\t"
    "ds_read_b128 v[36:39], v5 offset:1024\n\t"
    "v_smfmac_f32_16x16x64_bf16 v[16:19], v[28:31], v[112:119], v6\n\t"
    "ds_read_b128 v[40:43], v5 offset:4096\n\t"
    "ds_read_b128 v[44:47], v5 offset:5120\n\t"
    "v_smfmac_f32_16x16x64_bf16 v[20:23], v[28:31], v[120:127], v6\n\t"
    "s_waitcnt lgkmcnt(3)\n\t"
    "v_smfmac_f32_16x16x64_bf16 v[16:19], v[32:35], v[64:71], v6\n\t"
    "ds_read_b128 v[48:51], v5 offset:6144\n\t"
    "v_smfmac_f32_16x16x64_bf16 v[20:23], v[32:35], v[72:79], v6\n\t"
    "ds_read_b128 v[52:55], v5 offset:7168\n\t"
    "s_waitcnt lgkmcnt(4)\n\t"
    "v_smfmac_f32_16x16x64_bf16 v[16:19], v[36:39], v[80:87], v6\n\t"
    "ds_read_b128 v[24:27], v5 offset:8192\n\t"
    "v_smfmac_f32_16x16x64_bf16 v[20:23], v[36:39], v[88:95], v6\n\t"
    "ds_read_b128 v[28:31], v5 offset:9216\n\t"
    "s_waitcnt lgkmcnt(5)\n\t"
    "v_smfmac_f32_16x16x64_bf16 v[16:19], v[40:43], v[128:135], v6\n\t"
    "ds_read_b128 v[56:59], v5 offset:10240\n\t"
    "v_smfmac_f32_16x16x64_bf16 v[20:23], v[40:43], v[136:143], v6\n\t"
    "ds_read_b128 v[60:63], v5 offset:11264\n\t"
    "ds_read_b128 v[36:39], v10 offset:35840\n\t"
    "s_waitcnt lgkmcnt(7)\n\t"
    "v_smfmac_f32_16x16x64_bf16 v[16:19], v[44:47], v[144:151], v6\n\t"
    "ds_read_b128 v[40:43], v10 offset:36864\n\t"
    "v_smfmac_f32_16x16x64_bf16 v[20:23], v[44:47], v[152:159], v6\n\t"
    "ds_read_b128 v[44:47], v10 offset:37888\n\t"
    "s_waitcnt lgkmcnt(8)\n\t"
    "v_smfmac_f32_16x16x64_bf16 v[16:19], v[48:51], v[160:167], v6\n\t"
    "v_smfmac_f32_16x16x64_bf16 v[20:23], v[48:51], v[168:175], v6\n\t"
    "ds_read_b128 v[48:51], v10 offset:38912\n\t"
    "s_waitcnt lgkmcnt(8)\n\t"
    "v_smfmac_f32_16x16x64_bf16 v[16:19], v[52:55], v[176:183], v6\n\t"
    "v_smfmac_f32_16x16x64_bf16 v[20:23], v[52:55], v[184:191], v6\n\t"
    "ds_read_b128 v[52:55], v10 offset:39936\n\t"
    "s_waitcnt lgkmcnt(8)\n\t"
    "v_smfmac_f32_16x16x64_bf16 v[16:19], v[24:27], v[192:199], v6\n\t"
    "v_smfmac_f32_16x16x64_bf16 v[20:23], v[24:27], v[200:207], v6\n\t"
    "ds_read_b128 v[24:27], v10 offset:32768\n\t"
    "s_waitcnt lgkmcnt(8)\n\t"
    "v_smfmac_f32_16x16x64_bf16 v[16:19], v[28:31], v[208:215], v6\n\t"
    "ds_read_b32 v8, v15 offset:28928\n\t"
    "ds_read_b128 v[32:35], v5 offset:25600\n\t"
    "s_setprio 3\n\t"
    "s_waitcnt lgkmcnt(9)\n\t"
    "v_smfmac_f32_16x16x64_bf16 v[16:19], v[56:59], v[224:231], v6\n\t"
    "s_waitcnt lgkmcnt(8)\n\t"
    "v_smfmac_f32_16x16x64_bf16 v[16:19], v[60:63], v[240:247], v6\n\t"
    "v_smfmac_f32_16x16x64_bf16 v[20:23], v[28:31], v[216:223], v6\n\t"
    "s_waitcnt lgkmcnt(0)\n\t"
    "21:\n\t"
    "v_cmp_ne_u32 vcc, %[sn], v8\n\t"
    "s_cbranch_vccnz 22f\n\t"
    "v_mul_f32 v32, %[lsc], v32\n\t"
    "v_mul_f32 v33, %[lsc], v33\n\t"
    "v_smfmac_f32_16x16x64_bf16 v[20:23], v[56:59], v[232:239], v6\n\t"
    "v_mul_f32 v34, %[lsc], v34\n\t"
    "v_mul_f32 v35, %[lsc], v35\n\t"
    "v_add_f32 v16, v16, v18\n\t"
    "v_add_f32 v17, v17, v19\n\t"
    "v_smfmac_f32_16x16x64_bf16 v[20:23], v[60:63], v[248:255], v6\n\t"
    "v_fma_f32 v16, v16, %[lsc], v32\n\t"
    "v_fma_f32 v17, v17, %[lsc], v33\n\t"
    "v_cvt_pk_bf16_f32 v16, v16, v17\n\t"
    "ds_write_b32 v15, v16 offset:16896\n\t"
    "s_mov_b32 %[bg], %[big]\n\t"
    "s_nop 2\n\t"
    "v_add_f32 v20, v20, v22\n\t"
    "v_add_f32 v21, v21, v23\n\t"
    "v_fma_f32 v20, v20, %[lsc], v34\n\t"
    "v_fma_f32 v21, v21, %[lsc], v35\n\t"
    "v_cvt_pk_bf16_f32 v20, v20, v21\n\t"
    "ds_write_b32 v15, v20 offset:17152\n\t"
    "ds_read_b128 v[28:31], v10 offset:33792\n\t"
    "ds_read_b128 v[32:35], v10 offset:34816\n\t"
    "s_waitcnt lgkmcnt(2)\n\t"
    "s_barrier\n\t"
    "ds_read_b64_tr_b16 v[16:17], v7 offset:16384\n\t"
    "ds_read_b64_tr_b16 v[18:19], v7 offset:16448\n\t"
    "ds_read_b64_tr_b16 v[20:21], v7 offset:16896\n\t"
    "ds_read_b64_tr_b16 v[22:23], v7 offset:16960\n\t"
    "ds_read_b64_tr_b16 v[56:57], v7 offset:17408\n\t"
    "ds_read_b64_tr_b16 v[58:59], v7 offset:17472\n\t"
    "ds_read_b64_tr_b16 v[60:61], v7 offset:17920\n\t"
    "ds_read_b64_tr_b16 v[62:63], v7 offset:17984\n\t"
    "global_load_dwordx4 v[6:9], v5, %[vs] sc1\n\t"
    "global_load_dwordx4 v[10:13], v5, %[vs] offset:1024 sc1\n\t"
    "s_waitcnt lgkmcnt(6)\n\t"
    "v_mfma_f32_16x16x32_bf16 v[24:27], v[24:27], v[16:19], 0\n\t"
    "v_mfma_f32_16x16x32_bf16 v[28:31], v[28:31], v[16:19], 0\n\t"
    "s_waitcnt lgkmcnt(4)\n\t"
    "v_mfma_f32_16x16x32_bf16 v[24:27], v[32:35], v[20:23], v[24:27]\n\t"
    "v_mfma_f32_16x16x32_bf16 v[28:31], v[36:39], v[20:23], v[28:31]\n\t"
    "v_xor_b32 v39, 32, v14\n\t"
    "ds_read_b64_tr_b16 v[32:33], v14 offset:43008\n\t"
    "ds_read_b64_tr_b16 v[34:35], v14 offset:44032\n\t"
    "ds_read_b64_tr_b16 v[36:37], v39 offset:43008\n\t"
    "ds_read_b64_tr_b16 v[38:39], v39 offset:44032\n\t"
    "s_waitcnt lgkmcnt(6)\n\t"
    "v_mfma_f32_16x16x32_bf16 v[24:27], v[40:43], v[56:59], v[24:27]\n\t"
    "v_mfma_f32_16x16x32_bf16 v[28:31], v[44:47], v[56:59], v[28:31]\n\t"
    "s_waitcnt lgkmcnt(4)\n\t"
    "v_mfma_f32_16x16x32_bf16 v[24:27], v[48:51], v[60:63], v[24:27]\n\t"
    "v_mfma_f32_16x16x32_bf16 v[28:31], v[52:55], v[60:63], v[28:31]\n\t"
    "v_mov_b32 v52, %[one]\n\t"
    "v_mov_b32 v53, %[one]\n\t"
    "v_mov_b32 v54, %[one]\n\t"
    "v_mov_b32 v55, %[one]\n\t"
    "v_mov_b32 v45, 0\n\t"
    "v_mov_b32 v47, 0\n\t"
    "v_mov_b32 v48, %[one]\n\t"
    "v_max3_f32 v14, |v24|, |v25|, |v26|\n\t"
    "v_max3_f32 v40, |v27|, |v28|, |v29|\n\t"
    "v_max3_f32 v14, v14, |v30|, |v31|\n\t"
    "v_max_f32 v14, v14, v40\n\t"
    "v_cmp_nge_f32 vcc, %[big], v14\n\t"
    "v_mov_b32 v14, 0\n\t"
    "s_cbranch_vccnz 12f\n\t"
    "13:\n\t"
    "v_exp_f32 v24, v24\n\t"
    "v_exp_f32 v25, v25\n\t"
    "v_exp_f32 v26, v26\n\t"
    "v_exp_f32 v27, v27\n\t"
    "v_exp_f32 v28, v28\n\t"
    "v_exp_f32 v29, v29\n\t"
    "v_exp_f32 v30, v30\n\t"
    "v_exp_f32 v31, v31\n\t"
    "v_cvt_pk_bf16_f32 v40, v24, v25\n\t"
    "v_cvt_pk_bf16_f32 v41, v26, v27\n\t"
    "v_cvt_pk_bf16_f32 v42, v28, v29\n\t"
    "v_cvt_pk_bf16_f32 v43, v30, v31\n\t"
    "v_mov_b32 v49, 0\n\t"
    "v_mov_b32 v51, 0\n\t"
    "s_waitcnt lgkmcnt(0)\n\t"
    "v_mfma_f32_16x16x32_bf16 v[52:55], v[52:55], v[40:43], 0\n\t"
    "v_mfma_f32_16x16x32_bf16 v[24:27], v[32:35], v[40:43], 0\n\t"
    "v_mfma_f32_16x16x32_bf16 v[28:31], v[36:39], v[40:43], 0\n\t"
    "s_waitcnt vmcnt(1)\n\t"
    "5:\n\t"
    "v_mfma_f32_16x16x32_bf16 v[32:35], v[6:9], v[16:19], 0\n\t"
    "v_mfma_f32_16x16x32_bf16 v[36:39], v[6:9], v[20:23], 0\n\t"
    "s_waitcnt vmcnt(0)\n\t"
    "v_mfma_f32_16x16x32_bf16 v[32:35], v[10:13], v[56:59], v[32:35]\n\t"
    "v_mfma_f32_16x16x32_bf16 v[36:39], v[10:13], v[60:63], v[36:39]\n\t"
    "ds_read_b32 v44, v15 offset:18944\n\t"
    "ds_read_b32 v46, v15 offset:19200\n\t"
    "v_max3_u32 v40, v6, v7, v8\n\t"
    "v_max3_u32 v41, v9, v10, v11\n\t"
    "v_max3_u32 v40, v40, v12, v13\n\t"
    "v_max_u32 v40, v40, v41\n\t"
    "v_cmp_eq_u32 vcc, -1, v40\n\t"
    "s_cbranch_vccnz 4f\n\t"
    "6:\n\t"
    "s_nop 0\n\t"
    "v_add_f32 v32, v32, v38\n\t"
    "v_add_f32 v33, v33, v39\n\t"
    "v_exp_f32 v34, v32\n\t"
    "v_max_f32 v40, v32, v33\n\t"
    "v_exp_f32 v35, v33\n\t"
    "v_cmp_nge_f32 vcc, %[bg], v40\n\t"
    "s_cbranch_vccnz 10f\n\t"
    "11:\n\t"
    "v_cvt_pk_bf16_f32 v50, v34, v35\n\t"
    "s_nop 1\n\t"
    "v_mfma_f32_16x16x16_bf16 v[52:55], v[48:49], v[50:51], v[52:55]\n\t"
    "17:\n\t"
    "s_waitcnt vmcnt(0) lgkmcnt(0)\n\t"
    "v_mfma_f32_16x16x16_bf16 v[32:35], v[44:45], v[50:51], v[24:27]\n\t"
    "v_mfma_f32_16x16x16_bf16 v[36:39], v[46:47], v[50:51], v[28:31]\n\t"
    "v_max_u32 v40, v44, v46\n\t"
    "v_cmp_eq_u32 vcc, -1, v40\n\t"
    "s_nop 2\n\t"
    "v_rcp_f32 v42, v52\n\t"
    "s_cbranch_vccnz 18f\n\t"
    "19:\n\t"
    "v_pk_mul_f32 v[32:33], v[32:33], v[42:43] op_sel_hi:[1,0]\n\t"
    "v_pk_mul_f32 v[34:35], v[34:35], v[42:43] op_sel_hi:[1,0]\n\t"
    "v_pk_mul_f32 v[36:37], v[36:37], v[42:43] op_sel_hi:[1,0]\n\t"
    "v_pk_mul_f32 v[38:39], v[38:39], v[42:43] op_sel_hi:[1,0]\n\t"
    "v_cvt_pk_bf16_f32 v24, v32, v33\n\t"
    "v_cvt_pk_bf16_f32 v25, v34, v35\n\t"
    "v_cvt_pk_bf16_f32 v26, v36, v37\n\t"
    "v_cvt_pk_bf16_f32 v27, v38, v39\n\t"
    "v_min_u32 v24, -2, v24\n\t"
    "v_min_u32 v25, -2, v25\n\t"
    "v_min_u32 v26, -2, v26\n\t"
    "v_min_u32 v27, -2, v27\n\t"
    "global_store_dwordx4 v4, v[24:27], %[od] sc1\n\t"
    "ds_write_b128 v5, v[10:13] offset:21504\n\t"
    "s_setprio 1\n\t"
    "s_waitcnt lgkmcnt(0)\n\t"
    "s_barrier\n\t"
    "s_waitcnt lgkmcnt(0)\n\t"
    "s_branch 9f\n\t"
    "1:\n\t"
    "s_add_u32 %[sp], %[sp], 1\n\t"
    "s_cmp_ge_u32 %[sp], %[cap]\n\t"
    "s_cbranch_scc1 7f\n\t"
    "global_load_dwordx4 v[24:27], v5, %[ag] sc1\n\t"
    "global_load_dwordx4 v[28:31], v5, %[ag] offset:1024 sc1\n\t"
    "s_waitcnt lgkmcnt(0)\n\t"
    "s_memtime %[tc]\n\t"
    "s_nop 7\n\t"
    "s_branch 3b\n\t"
    "7:\n\t"
    "s_mov_b32 %[cap], 0\n\t"
    "s_branch 2b\n\t"
    "4:\n\t"
    "s_add_u32 %[sp], %[sp], 0x10000\n\t"
    "s_lshr_b32 %[st], %[sp], 16\n\t"
    "s_cmp_ge_u32 %[st], %[cap]\n\t"
    "s_cbranch_scc1 8f\n\t"
    "global_load_dwordx4 v[6:9], v5, %[vs] sc1\n\t"
    "global_load_dwordx4 v[10:13], v5, %[vs] offset:1024 sc1\n\t"
    "s_waitcnt vmcnt(0) lgkmcnt(0)\n\t"
    "s_branch 5b\n\t"
    "8:\n\t"
    "s_mov_b32 %[cap], 0\n\t"
    "s_branch 6b\n\t"
    "18:\n\t"
    "s_add_u32 %[sp], %[sp], 0x10000\n\t"
    "s_lshr_b32 %[st], %[sp], 16\n\t"
    "s_cmp_ge_u32 %[st], %[cap]\n\t"
    "s_cbranch_scc1 20f\n\t"
    "ds_read_b32 v44, v15 offset:18944\n\t"
    "ds_read_b32 v46, v15 offset:19200\n\t"
    "s_waitcnt vmcnt(0) lgkmcnt(0)\n\t"
    "s_branch 17b\n\t"
    "20:\n\t"
    "s_mov_b32 %[cap], 0\n\t"
    "s_branch 19b\n\t"
    "22:\n\t"
    "ds_read_b32 v8, v15 offset:28928\n\t"
    "ds_read_b128 v[32:35], v5 offset:25600\n\t"
    "s_waitcnt lgkmcnt(0)\n\t"
    "s_branch 21b\n\t"
    "10:\n\t"
    "v_max_f32 v40, v40, v14\n\t"
    "v_mov_b32 v42, v40\n\t"
    "s_nop 1\n\t"
    "v_permlane16_swap_b32 v40, v42\n\t"
    "v_max_f32 v40, v40, v42\n\t"
    "v_mov_b32 v42, v40\n\t"
    "s_nop 1\n\t"
    "v_permlane32_swap_b32 v40, v42\n\t"
    "v_max_f32 v40, v40, v42\n\t"
    "v_sub_f32 v34, v32, v40\n\t"
    "v_sub_f32 v35, v33, v40\n\t"
    "v_sub_f32 v40, v14, v40\n\t"
    "v_exp_f32 v40, v40\n\t"
    "v_exp_f32 v34, v34\n\t"
    "v_exp_f32 v35, v35\n\t"
    "v_mul_f32 v24, v24, v40\n\t"
    "v_mul_f32 v25, v25, v40\n\t"
    "v_mul_f32 v26, v26, v40\n\t"
    "v_mul_f32 v27, v27, v40\n\t"
    "v_mul_f32 v28, v28, v40\n\t"
    "v_mul_f32 v29, v29, v40\n\t"
    "v_mul_f32 v30, v30, v40\n\t"
    "v_mul_f32 v31, v31, v40\n\t"
    "v_mul_f32 v52, v52, v40\n\t"
    "v_mul_f32 v53, v53, v40\n\t"
    "v_mul_f32 v54, v54, v40\n\t"
    "v_mul_f32 v55, v55, v40\n\t"
    "s_branch 11b\n\t"
    "12:\n\t"
    "v_max3_f32 v14, v24, v25, v26\n\t"
    "v_max3_f32 v40, v27, v28, v29\n\t"
    "v_max3_f32 v14, v14, v30, v31\n\t"
    "v_max3_f32 v14, v14, v40, %[neg]\n\t"
    "v_mov_b32 v40, v14\n\t"
    "s_nop 1\n\t"
    "v_permlane16_swap_b32 v14, v40\n\t"
    "v_max_f32 v14, v14, v40\n\t"
    "v_mov_b32 v40, v14\n\t"
    "s_nop 1\n\t"
    "v_permlane32_swap_b32 v14, v40\n\t"
    "v_max_f32 v14, v14, v40\n\t"
    "v_sub_f32 v24, v24, v14\n\t"
    "v_sub_f32 v25, v25, v14\n\t"
    "v_sub_f32 v26, v26, v14\n\t"
    "v_sub_f32 v27, v27, v14\n\t"
    "v_sub_f32 v28, v28, v14\n\t"
    "v_sub_f32 v29, v29, v14\n\t"
    "v_sub_f32 v30, v30, v14\n\t"
    "v_sub_f32 v31, v31, v14\n\t"
    "s_mov_b32 %[bg], 0x7fc00000\n\t"
    "s_branch 13b\n\t"
    "9:"
    : [tc] "=&s"(tc), [sp] "=&s"(sp), [cap] "+s"(cap), [st] "=&s"(st), [bg] "=&s"(bg)
    : [ag] "s"(ag), [c44] "s"(0x4444u), [lsc] "s"(LSC), [sn] "s"(sn), [pb] "s"(pb), [vs] "s"(vs), [od] "s"(od), [neg] "s"(-1.0e38f), [one] "s"(0x3F803F80u), [big] "s"(24.0f), "{v[64:95]}"(W.w0), "{v[96:127]}"(W.w1), "{v[128:159]}"(W.w2), "{v[160:191]}"(W.w3), "{v[192:223]}"(W.w4), "{v[224:255]}"(W.w5)
    : "v4", "v5", "v6", "v7", "v8", "v9", "v10", "v11", "v12", "v13", "v14", "v15", "v16", "v17", "v18", "v19", "v20", "v21", "v22", "v23", "v24", "v25", "v26", "v27", "v28", "v29", "v30", "v31", "v32", "v33", "v34", "v35", "v36", "v37", "v38", "v39", "v40", "v41", "v42", "v43", "v44", "v45", "v46", "v47", "v48", "v49", "v50", "v51", "v52", "v53", "v54", "v55", "v56", "v57", "v58", "v59", "v60", "v61", "v62", "v63", "vcc", "scc", "m0", "memory");
}
template <> __device__ __forceinline__ void qk_step<2>(const QKW& W, const char* ag, const char* vs, const char* v2, const char* db, const char* od, unsigned pb, unsigned sn, unsigned& cap, unsigned& sp,
    unsigned long long& tc, unsigned long long* ts, unsigned& d0, unsigned& d1) {
  unsigned st, bg;
  asm volatile("v_mbcnt_lo_u32_b32 v60, -1, 0\n\t"
    "v_mbcnt_hi_u32_b32 v60, -1, v60\n\t"
    "v_lshlrev_b32 v5, 4, v60\n\t"
    "s_mov_b32 %[sp], 0\n\t"
    "s_setprio 2\n\t"
    "global_load_dwordx4 v[24:27], v5, %[ag] sc1\n\t"
    "global_load_dwordx4 v[28:31], v5, %[ag] offset:1024 sc1\n\t"
    "s_memtime %[tc]\n\t"
    "v_lshrrev_b32 v32, 4, v60\n\t"
    "v_and_b32 v33, 15, v60\n\t"
    "v_lshrrev_b32 v34, 2, v33\n\t"
    "v_and_b32 v35, 3, v60\n\t"
    "v_and_b32 v61, 1, v60\n\t"
    "v_lshlrev_b32 v61, 3, v61\n\t"
    "v_lshl_or_b32 v7, v34, 4, v61\n\t"
    "v_lshl_or_b32 v7, v32, 7, v7\n\t"
    "v_and_b32 v10, 2, v34\n\t"
    "v_xor_b32 v10, v10, v32\n\t"
    "v_lshl_or_b32 v10, v10, 4, %[pb]\n\t"
    "v_lshl_or_b32 v10, v33, 6, v10\n\t"
    "v_and_b32 v14, 32, v60\n\t"
    "v_lshl_or_b32 v14, v35, 3, v14\n\t"
    "v_lshrrev_b32 v34, 2, v60\n\t"
    "v_lshl_or_b32 v14, v34, 6, v14\n\t"
    "v_or_b32 v14, %[pb], v14\n\t"
    "v_and_b32 v61, 3, v60\n\t"
    "v_lshlrev_b32 v61, 6, v61\n\t"
    "v_lshl_or_b32 v4, v32, 4, v61\n\t"
    "v_and_b32 v61, 4, v60\n\t"
    "v_lshl_or_b32 v4, v61, 10, v4\n\t"
    "v_and_b32 v61, 8, v60\n\t"
    "v_lshl_or_b32 v4, v61, 14, v4\n\t"
    "v_or_b32 v4, 512, v4\n\t"
    "v_and_b32 v61, 2, v60\n\t"
    "v_mul_u32_u24 v61, 0x5555, v61\n\t"
    "v_or_b32 v6, %[c44], v61\n\t"
    "v_lshlrev_b32 v61, 2, v32\n\t"
    "v_lshl_or_b32 v15, v33, 4, v61\n\t"
    "3:\n\t"
    "v_mov_b32 v16, 0\n\t"
    "v_mov_b32 v17, 0\n\t"
    "v_mov_b32 v18, 0\n\t"
    "v_mov_b32 v19, 0\n\t"
    "v_mov_b32 v20, 0\n\t"
    "v_mov_b32 v21, 0\n\t"
    "v_mov_b32 v22, 0\n\t"
    "v_mov_b32 v23, 0\n\t"
    "s_waitcnt vmcnt(0) lgkmcnt(0)\n\t"
    "33:\n\t"
    "ds_write_b128 v5, v[24:27] offset:4096\n\t"
    "ds_write_b128 v5, v[28:31] offset:5120\n\t"
    "v_smfmac_f32_16x16x64_bf16 v[16:19], v[24:27], v[128:135], v6\n\t"
    "v_max3_u32 v60, v24, v25, v26\n\t"
    "v_max3_u32 v61, v27, v28, v29\n\t"
    "v_smfmac_f32_16x16x64_bf16 v[20:23], v[24:27], v[136:143], v6\n\t"
    "v_max3_u32 v60, v60, v30, v31\n\t"
    "v_max_u32 v60, v60, v61\n\t"
    "v_cmp_eq_u32 vcc, -1, v60\n\t"
    "s_cbranch_vccnz 1f\n\t"
    "2:\n\t"
    "s_waitcnt lgkmcnt(0)\n\t"
    "s_barrier\n\t"
    "ds_read_b128 v[32:35], v5 offset:0\n\t"
    "ds_read_b128 v[36:39], v5 offset:1024\n\t"
    "v_smfmac_f32_16x16x64_bf16 v[16:19], v[28:31], v[144:151], v6\n\t"
    "ds_read_b128 v[40:43], v5 offset:2048\n\t"
    "ds_read_b128 v[44:47], v5 offset:3072\n\t"
    "v_smfmac_f32_16x16x64_bf16 v[20:23], v[28:31], v[152:159], v6\n\t"
    "s_waitcnt lgkmcnt(3)\n\t"
    "v_smfmac_f32_16x16x64_bf16 v[16:19], v[32:35], v[64:71], v6\n\t"
    "ds_read_b128 v[48:51], v5 offset:6144\n\t"
    "v_smfmac_f32_16x16x64_bf16 v[20:23], v[32:35], v[72:79], v6\n\t"
    "ds_read_b128 v[52:55], v5 offset:7168\n\t"
    "s_waitcnt lgkmcnt(4)\n\t"
    "v_smfmac_f32_16x16x64_bf16 v[16:19], v[36:39], v[80:87], v6\n\t"
    "ds_read_b128 v[24:27], v5 offset:8192\n\t"
    "v_smfmac_f32_16x16x64_bf16 v[20:23], v[36:39], v[88:95], v6\n\t"
    "ds_read_b128 v[28:31], v5 offset:9216\n\t"
    "s_waitcnt lgkmcnt(5)\n\t"
    "v_smfmac_f32_16x16x64_bf16 v[16:19], v[40:43], v[96:103], v6\n\t"
    "ds_read_b128 v[56:59], v5 offset:10240\n\t"
    "v_smfmac_f32_16x16x64_bf16 v[20:23], v[40:43], v[104:111], v6\n\t"
    "ds_read_b128 v[60:63], v5 offset:11264\n\t"
    "ds_read_b128 v[36:39], v10 offset:35840\n\t"
    "s_waitcnt lgkmcnt(7)\n\t"
    "v_smfmac_f32_16x16x64_bf16 v[16:19], v[44:47], v[112:119], v6\n\t"
    "ds_read_b128 v[40:43], v10 offset:36864\n\t"
    "v_smfmac_f32_16x16x64_bf16 v[20:23], v[44:47], v[120:127], v6\n\t"
    "ds_read_b128 v[44:47], v10 offset:37888\n\t"
    "s_waitcnt lgkmcnt(8)\n\t"
    "v_smfmac_f32_16x16x64_bf16 v[16:19], v[48:51], v[160:167], v6\n\t"
    "v_smfmac_f32_16x16x64_bf16 v[20:23], v[48:51], v[168:175], v6\n\t"
    "ds_read_b128 v[48:51], v10 offset:38912\n\t"
    "s_waitcnt lgkmcnt(8)\n\t"
    "v_smfmac_f32_16x16x64_bf16 v[16:19], v[52:55], v[176:183], v6\n\t"
    "v_smfmac_f32_16x16x64_bf16 v[20:23], v[52:55], v[184:191], v6\n\t"
    "ds_read_b128 v[52:55], v10 offset:39936\n\t"
    "s_waitcnt lgkmcnt(8)\n\t"
    "v_smfmac_f32_16x16x64_bf16 v[16:19], v[24:27], v[192:199], v6\n\t"
    "v_smfmac_f32_16x16x64_bf16 v[20:23], v[24:27], v[200:207], v6\n\t"
    "ds_read_b128 v[24:27], v10 offset:32768\n\t"
    "s_waitcnt lgkmcnt(8)\n\t"
    "v_smfmac_f32_16x16x64_bf16 v[16:19], v[28:31], v[208:215], v6\n\t"
    "ds_read_b32 v8, v15 offset:29184\n\t"
    "ds_read_b128 v[32:35], v5 offset:26624\n\t"
    "s_setprio 3\n\t"
    "s_waitcnt lgkmcnt(9)\n\t"
    "v_smfmac_f32_16x16x64_bf16 v[16:19], v[56:59], v[224:231], v6\n\t"
    "s_waitcnt lgkmcnt(8)\n\t"
    "v_smfmac_f32_16x16x64_bf16 v[16:19], v[60:63], v[240:247], v6\n\t"
    "v_smfmac_f32_16x16x64_bf16 v[20:23], v[28:31], v[216:223], v6\n\t"
    "s_waitcnt lgkmcnt(0)\n\t"
    "21:\n\t"
    "v_cmp_ne_u32 vcc, %[sn], v8\n\t"
    "s_cbranch_vccnz 22f\n\t"
    "v_mul_f32 v32, %[lsc], v32\n\t"
    "v_mul_f32 v33, %[lsc], v33\n\t"
    "v_smfmac_f32_16x16x64_bf16 v[20:23], v[56:59], v[232:239], v6\n\t"
    "v_mul_f32 v34, %[lsc], v34\n\t"
    "v_mul_f32 v35, %[lsc], v35\n\t"
    "v_add_f32 v16, v16, v18\n\t"
    "v_add_f32 v17, v17, v19\n\t"
    "v_smfmac_f32_16x16x64_bf16 v[20:23], v[60:63], v[248:255], v6\n\t"
    "v_fma_f32 v16, v16, %[lsc], v32\n\t"
    "v_fma_f32 v17, v17, %[lsc], v33\n\t"
    "v_cvt_pk_bf16_f32 v16, v16, v17\n\t"
    "ds_write_b32 v15, v16 offset:17408\n\t"
    "s_mov_b32 %[bg], %[big]\n\t"
    "s_nop 2\n\t"
    "v_add_f32 v20, v20, v22\n\t"
    "v_add_f32 v21, v21, v23\n\t"
    "v_fma_f32 v20, v20, %[lsc], v34\n\t"
    "v_fma_f32 v21, v21, %[lsc], v35\n\t"
    "v_cvt_pk_bf16_f32 v20, v20, v21\n\t"
    "ds_write_b32 v15, v20 offset:17664\n\t"
    "ds_read_b128 v[28:31], v10 offset:33792\n\t"
    "ds_read_b128 v[32:35], v10 offset:34816\n\t"
    "s_waitcnt lgkmcnt(2)\n\t"
    "s_barrier\n\t"
    "ds_read_b64_tr_b16 v[16:17], v7 offset:16384\n\t"
    "ds_read_b64_tr_b16 v[18:19], v7 offset:16448\n\t"
    "ds_read_b64_tr_b16 v[20:21], v7 offset:16896\n\t"
    "ds_read_b64_tr_b16 v[22:23], v7 offset:16960\n\t"
    "ds_read_b64_tr_b16 v[56:57], v7 offset:17408\n\t"
    "ds_read_b64_tr_b16 v[58:59], v7 offset:17472\n\t"
    "ds_read_b64_tr_b16 v[60:61], v7 offset:17920\n\t"
    "ds_read_b64_tr_b16 v[62:63], v7 offset:17984\n\t"
    "global_load_dwordx4 v[6:9], v5, %[vs] sc1\n\t"
    "global_load_dwordx4 v[10:13], v5, %[vs] offset:1024 sc1\n\t"
    "s_waitcnt lgkmcnt(6)\n\t"
    "v_mfma_f32_16x16x32_bf16 v[24:27], v[24:27], v[16:19], 0\n\t"
    "v_mfma_f32_16x16x32_bf16 v[28:31], v[28:31], v[16:19], 0\n\t"
    "s_waitcnt lgkmcnt(4)\n\t"
    "v_mfma_f32_16x16x32_bf16 v[24:27], v[32:35], v[20:23], v[24:27]\n\t"
    "v_mfma_f32_16x16x32_bf16 v[28:31], v[36:39], v[20:23], v[28:31]\n\t"
    "v_xor_b32 v39, 32, v14\n\t"
    "ds_read_b64_tr_b16 v[32:33], v14 offset:45056\n\t"
    "ds_read_b64_tr_b16 v[34:35], v14 offset:46080\n\t"
    "ds_read_b64_tr_b16 v[36:37], v39 offset:45056\n\t"
    "ds_read_b64_tr_b16 v[38:39], v39 offset:46080\n\t"
    "s_waitcnt lgkmcnt(6)\n\t"
    "v_mfma_f32_16x16x32_bf16 v[24:27], v[40:43], v[56:59], v[24:27]\n\t"
    "v_mfma_f32_16x16x32_bf16 v[28:31], v[44:47], v[56:59], v[28:31]\n\t"
    "s_waitcnt lgkmcnt(4)\n\t"
    "v_mfma_f32_16x16x32_bf16 v[24:27], v[48:51], v[60:63], v[24:27]\n\t"
    "v_mfma_f32_16x16x32_bf16 v[28:31], v[52:55], v[60:63], v[28:31]\n\t"
    "v_mov_b32 v52, %[one]\n\t"
    "v_mov_b32 v53, %[one]\n\t"
    "v_mov_b32 v54, %[one]\n\t"
    "v_mov_b32 v55, %[one]\n\t"
    "v_mov_b32 v45, 0\n\t"
    "v_mov_b32 v47, 0\n\t"
    "v_mov_b32 v48, %[one]\n\t"
    "v_max3_f32 v14, |v24|, |v25|, |v26|\n\t"
    "v_max3_f32 v40, |v27|, |v28|, |v29|\n\t"
    "v_max3_f32 v14, v14, |v30|, |v31|\n\t"
    "v_max_f32 v14, v14, v40\n\t"
    "v_cmp_nge_f32 vcc, %[big], v14\n\t"
    "v_mov_b32 v14, 0\n\t"
    "s_cbranch_vccnz 12f\n\t"
    "13:\n\t"
    "v_exp_f32 v24, v24\n\t"
    "v_exp_f32 v25, v25\n\t"
    "v_exp_f32 v26, v26\n\t"
    "v_exp_f32 v27, v27\n\t"
    "v_exp_f32 v28, v28\n\t"
    "v_exp_f32 v29, v29\n\t"
    "v_exp_f32 v30, v30\n\t"
    "v_exp_f32 v31, v31\n\t"
    "v_lshrrev_b32 v46, 2, v5\n\t"
    "global_load_dword v44, v46, %[v2] sc1\n\t"
    "global_load_dword v46, v46, %[v2] offset:256 sc1\n\t"
    "v_cvt_pk_bf16_f32 v40, v24, v25\n\t"
    "v_cvt_pk_bf16_f32 v41, v26, v27\n\t"
    "v_cvt_pk_bf16_f32 v42, v28, v29\n\t"
    "v_cvt_pk_bf16_f32 v43, v30, v31\n\t"
    "v_mov_b32 v49, 0\n\t"
    "v_mov_b32 v51, 0\n\t"
    "s_waitcnt lgkmcnt(0)\n\t"
    "v_mfma_f32_16x16x32_bf16 v[52:55], v[52:55], v[40:43], 0\n\t"
    "v_mfma_f32_16x16x32_bf16 v[24:27], v[32:35], v[40:43], 0\n\t"
    "v_mfma_f32_16x16x32_bf16 v[28:31], v[36:39], v[40:43], 0\n\t"
    "s_waitcnt vmcnt(3)\n\t"
    "5:\n\t"
    "v_mfma_f32_16x16x32_bf16 v[32:35], v[6:9], v[16:19], 0\n\t"
    "v_mfma_f32_16x16x32_bf16 v[36:39], v[6:9], v[20:23], 0\n\t"
    "s_waitcnt vmcnt(2)\n\t"
    "v_mfma_f32_16x16x32_bf16 v[32:35], v[10:13], v[56:59], v[32:35]\n\t"
    "v_mfma_f32_16x16x32_bf16 v[36:39], v[10:13], v[60:63], v[36:39]\n\t"
    "v_max3_u32 v40, v6, v7, v8\n\t"
    "v_max3_u32 v41, v9, v10, v11\n\t"
    "v_max3_u32 v40, v40, v12, v13\n\t"
    "v_max_u32 v40, v40, v41\n\t"
    "v_cmp_eq_u32 vcc, -1, v40\n\t"
    "s_cbranch_vccnz 4f\n\t"
    "6:\n\t"
    "s_nop 0\n\t"
    "s_nop 0\n\t"
    "v_add_f32 v32, v32, v38\n\t"
    "v_add_f32 v33, v33, v39\n\t"
    "v_exp_f32 v34, v32\n\t"
    "v_max_f32 v40, v32, v33\n\t"
    "v_exp_f32 v35, v33\n\t"
    "v_cmp_nge_f32 vcc, %[bg], v40\n\t"
    "s_cbranch_vccnz 10f\n\t"
    "11:\n\t"
    "v_cvt_pk_bf16_f32 v50, v34, v35\n\t"
    "s_nop 1\n\t"
    "v_mfma_f32_16x16x16_bf16 v[52:55], v[48:49], v[50:51], v[52:55]\n\t"
    "17:\n\t"
    "s_waitcnt vmcnt(0) lgkmcnt(0)\n\t"
    "v_mfma_f32_16x16x16_bf16 v[32:35], v[44:45], v[50:51], v[24:27]\n\t"
    "v_mfma_f32_16x16x16_bf16 v[36:39], v[46:47], v[50:51], v[28:31]\n\t"
    "v_max_u32 v40, v44, v46\n\t"
    "v_cmp_eq_u32 vcc, -1, v40\n\t"
    "s_memrealtime s[36:37]\n\t"
    "s_nop 1\n\t"
    "v_rcp_f32 v42, v52\n\t"
    "s_cbranch_vccnz 18f\n\t"
    "19:\n\t"
    "v_pk_mul_f32 v[32:33], v[32:33], v[42:43] op_sel_hi:[1,0]\n\t"
    "v_pk_mul_f32 v[34:35], v[34:35], v[42:43] op_sel_hi:[1,0]\n\t"
    "v_pk_mul_f32 v[36:37], v[36:37], v[42:43] op_sel_hi:[1,0]\n\t"
    "v_pk_mul_f32 v[38:39], v[38:39], v[42:43] op_sel_hi:[1,0]\n\t"
    "v_cvt_pk_bf16_f32 v24, v32, v33\n\t"
    "v_cvt_pk_bf16_f32 v25, v34, v35\n\t"
    "v_cvt_pk_bf16_f32 v26, v36, v37\n\t"
    "v_cvt_pk_bf16_f32 v27, v38, v39\n\t"
    "v_min_u32 v24, -2, v24\n\t"
    "v_min_u32 v25, -2, v25\n\t"
    "v_min_u32 v26, -2, v26\n\t"
    "v_min_u32 v27, -2, v27\n\t"
    "global_store_dwordx4 v4, v[24:27], %[od] sc1\n\t"
    "v_lshlrev_b32 v40, 10, v5\n\t"
    "s_waitcnt lgkmcnt(0)\n\t"
    "s_and_b32 s36, s36, 0xffffff\n\t"
    "s_or_b32 s36, s36, %[sn]\n\t"
    "v_mov_b32 v41, s36\n\t"
    "s_mov_b64 exec, 7\n\t"
    "global_store_dword v40, v41, %[db] offset:0 sc1\n\t"
    "s_mov_b64 exec, -1\n\t"
    "ds_write_b32 v15, v44 offset:19456\n\t"
    "ds_write_b32 v15, v46 offset:19712\n\t"
    "s_setprio 1\n\t"
    "s_waitcnt lgkmcnt(0)\n\t"
    "s_barrier\n\t"
    "s_waitcnt lgkmcnt(0)\n\t"
    "s_branch 9f\n\t"
    "1:\n\t"
    "s_add_u32 %[sp], %[sp], 1\n\t"
    "s_cmp_ge_u32 %[sp], %[cap]\n\t"
    "s_cbranch_scc1 7f\n\t"
    "global_load_dwordx4 v[24:27], v5, %[ag] sc1\n\t"
    "global_load_dwordx4 v[28:31], v5, %[ag] offset:1024 sc1\n\t"
    "s_waitcnt lgkmcnt(0)\n\t"
    "s_memtime %[tc]\n\t"
    "s_nop 7\n\t"
    "s_branch 3b\n\t"
    "7:\n\t"
    "s_mov_b32 %[cap], 0\n\t"
    "s_branch 2b\n\t"
    "4:\n\t"
    "s_add_u32 %[sp], %[sp], 0x10000\n\t"
    "s_lshr_b32 %[st], %[sp], 16\n\t"
    "s_cmp_ge_u32 %[st], %[cap]\n\t"
    "s_cbranch_scc1 8f\n\t"
    "global_load_dwordx4 v[6:9], v5, %[vs] sc1\n\t"
    "global_load_dwordx4 v[10:13], v5, %[vs] offset:1024 sc1\n\t"
    "s_waitcnt vmcnt(0) lgkmcnt(0)\n\t"
    "s_branch 5b\n\t"
    "8:\n\t"
    "s_mov_b32 %[cap], 0\n\t"
    "s_branch 6b\n\t"
    "18:\n\t"
    "s_add_u32 %[sp], %[sp], 0x10000\n\t"
    "s_lshr_b32 %[st], %[sp], 16\n\t"
    "s_cmp_ge_u32 %[st], %[cap]\n\t"
    "s_cbranch_scc1 20f\n\t"
    "v_lshrrev_b32 v46, 2, v5\n\t"
    "global_load_dword v44, v46, %[v2] sc1\n\t"
    "global_load_dword v46, v46, %[v2] offset:256 sc1\n\t"
    "s_waitcnt vmcnt(0) lgkmcnt(0)\n\t"
    "s_branch 17b\n\t"
    "20:\n\t"
    "s_mov_b32 %[cap], 0\n\t"
    "s_branch 19b\n\t"
    "22:\n\t"
    "ds_read_b32 v8, v15 offset:29184\n\t"
    "ds_read_b128 v[32:35], v5 offset:26624\n\t"
    "s_waitcnt lgkmcnt(0)\n\t"
    "s_branch 21b\n\t"
    "10:\n\t"
    "v_max_f32 v40, v40, v14\n\t"
    "v_mov_b32 v42, v40\n\t"
    "s_nop 1\n\t"
    "v_permlane16_swap_b32 v40, v42\n\t"
    "v_max_f32 v40, v40, v42\n\t"
    "v_mov_b32 v42, v40\n\t"
    "s_nop 1\n\t"
    "v_permlane32_swap_b32 v40, v42\n\t"
    "v_max_f32 v40, v40, v42\n\t"
    "v_sub_f32 v34, v32, v40\n\t"
    "v_sub_f32 v35, v33, v40\n\t"
    "v_sub_f32 v40, v14, v40\n\t"
    "v_exp_f32 v40, v40\n\t"
    "v_exp_f32 v34, v34\n\t"
    "v_exp_f32 v35, v35\n\t"
    "v_mul_f32 v24, v24, v40\n\t"
    "v_mul_f32 v25, v25, v40\n\t"
    "v_mul_f32 v26, v26, v40\n\t"
    "v_mul_f32 v27, v27, v40\n\t"
    "v_mul_f32 v28, v28, v40\n\t"
    "v_mul_f32 v29, v29, v40\n\t"
    "v_mul_f32 v30, v30, v40\n\t"
    "v_mul_f32 v31, v31, v40\n\t"
    "v_mul_f32 v52, v52, v40\n\t"
    "v_mul_f32 v53, v53, v40\n\t"
    "v_mul_f32 v54, v54, v40\n\t"
    "v_mul_f32 v55, v55, v40\n\t"
    "s_branch 11b\n\t"
    "12:\n\t"
    "v_max3_f32 v14, v24, v25, v26\n\t"
    "v_max3_f32 v40, v27, v28, v29\n\t"
    "v_max3_f32 v14, v14, v30, v31\n\t"
    "v_max3_f32 v14, v14, v40, %[neg]\n\t"
    "v_mov_b32 v40, v14\n\t"
    "s_nop 1\n\t"
    "v_permlane16_swap_b32 v14, v40\n\t"
    "v_max_f32 v14, v14, v40\n\t"
    "v_mov_b32 v40, v14\n\t"
    "s_nop 1\n\t"
    "v_permlane32_swap_b32 v14, v40\n\t"
    "v_max_f32 v14, v14, v40\n\t"
    "v_sub_f32 v24, v24, v14\n\t"
    "v_sub_f32 v25, v25, v14\n\t"
    "v_sub_f32 v26, v26, v14\n\t"
    "v_sub_f32 v27, v27, v14\n\t"
    "v_sub_f32 v28, v28, v14\n\t"
    "v_sub_f32 v29, v29, v14\n\t"
    "v_sub_f32 v30, v30, v14\n\t"
    "v_sub_f32 v31, v31, v14\n\t"
    "s_mov_b32 %[bg], 0x7fc00000\n\t"
    "s_branch 13b\n\t"
    "9:"
    : [tc] "=&s"(tc), [sp] "=&s"(sp), [cap] "+s"(cap), [st] "=&s"(st), [bg] "=&s"(bg)
    : [ag] "s"(ag), [c44] "s"(0x4444u), [lsc] "s"(LSC), [sn] "s"(sn), [pb] "s"(pb), [vs] "s"(vs), [od] "s"(od), [neg] "s"(-1.0e38f), [one] "s"(0x3F803F80u), [big] "s"(24.0f), [v2] "s"(v2), [db] "s"(db), "{v[64:95]}"(W.w0), "{v[96:127]}"(W.w1), "{v[128:159]}"(W.w2), "{v[160:191]}"(W.w3), "{v[192:223]}"(W.w4), "{v[224:255]}"(W.w5)
    : "v4", "v5", "v6", "v7", "v8", "v9", "v10", "v11", "v12", "v13", "v14", "v15", "v16", "v17", "v18", "v19", "v20", "v21", "v22", "v23", "v24", "v25", "v26", "v27", "v28", "v29", "v30", "v31", "v32", "v33", "v34", "v35", "v36", "v37", "v38", "v39", "v40", "v41", "v42", "v43", "v44", "v45", "v46", "v47", "v48", "v49", "v50", "v51", "v52", "v53", "v54", "v55", "v56", "v57", "v58", "v59", "v60", "v61", "v62", "v63", "vcc", "scc", "m0", "memory", "s36", "s37", "s38");
}
template <> __device__ __forceinline__ void qk_step<3>(const QKW& W, const char* ag, const char* vs, const char* v2, const char* db, const char* od, unsigned pb, unsigned sn, unsigned& cap, unsigned& sp,
    unsigned long long& tc, unsigned long long* ts, unsigned& d0, unsigned& d1) {
  unsigned st, bg;
  asm volatile("v_mbcnt_lo_u32_b32 v60, -1, 0\n\t"
    "v_mbcnt_hi_u32_b32 v60, -1, v60\n\t"
    "v_lshlrev_b32 v5, 4, v60\n\t"
    "s_mov_b32 %[sp], 0\n\t"
    "s_setprio 2\n\t"
    "global_load_dwordx4 v[24:27], v5, %[ag] sc1\n\t"
    "global_load_dwordx4 v[28:31], v5, %[ag] offset:1024 sc1\n\t"
    "s_memtime %[tc]\n\t"
    "v_lshrrev_b32 v32, 4, v60\n\t"
    "v_and_b32 v33, 15, v60\n\t"
    "v_lshrrev_b32 v34, 2, v33\n\t"
    "v_and_b32 v35, 3, v60\n\t"
    "v_and_b32 v61, 1, v60\n\t"
    "v_lshlrev_b32 v61, 3, v61\n\t"
    "v_lshl_or_b32 v7, v34, 4, v61\n\t"
    "v_lshl_or_b32 v7, v32, 7, v7\n\t"
    "v_and_b32 v10, 2, v34\n\t"
    "v_xor_b32 v10, v10, v32\n\t"
    "v_lshl_or_b32 v10, v10, 4, %[pb]\n\t"
    "v_lshl_or_b32 v10, v33, 6, v10\n\t"
    "v_and_b32 v14, 32, v60\n\t"
    "v_lshl_or_b32 v14, v35, 3, v14\n\t"
    "v_lshrrev_b32 v34, 2, v60\n\t"
    "v_lshl_or_b32 v14, v34, 6, v14\n\t"
    "v_or_b32 v14, %[pb], v14\n\t"
    "v_and_b32 v61, 3, v60\n\t"
    "v_lshlrev_b32 v61, 6, v61\n\t"
    "v_lshl_or_b32 v4, v32, 4, v61\n\t"
    "v_and_b32 v61, 4, v60\n\t"
    "v_lshl_or_b32 v4, v61, 10, v4\n\t"
    "v_and_b32 v61, 8, v60\n\t"
    "v_lshl_or_b32 v4, v61, 14, v4\n\t"
    "v_or_b32 v4, 768, v4\n\t"
    "v_and_b32 v61, 2, v60\n\t"
    "v_mul_u32_u24 v61, 0x5555, v61\n\t"
    "v_or_b32 v6, %[c44], v61\n\t"
    "v_lshlrev_b32 v61, 2, v32\n\t"
    "v_lshl_or_b32 v15, v33, 4, v61\n\t"
    "3:\n\t"
    "v_mov_b32 v16, 0\n\t"
    "v_mov_b32 v17, 0\n\t"
    "v_mov_b32 v18, 0\n\t"
    "v_mov_b32 v19, 0\n\t"
    "v_mov_b32 v20, 0\n\t"
    "v_mov_b32 v21, 0\n\t"
    "v_mov_b32 v22, 0\n\t"
    "v_mov_b32 v23, 0\n\t"
    "s_waitcnt vmcnt(0) lgkmcnt(0)\n\t"
    "33:\n\t"
    "ds_write_b128 v5, v[24:27] offset:6144\n\t"
    "ds_write_b128 v5, v[28:31] offset:7168\n\t"
    "v_smfmac_f32_16x16x64_bf16 v[16:19], v[24:27], v[160:167], v6\n\t"
    "v_max3_u32 v60, v24, v25, v26\n\t"
    "v_max3_u32 v61, v27, v28, v29\n\t"
    "v_smfmac_f32_16x16x64_bf16 v[20:23], v[24:27], v[168:175], v6\n\t"
    "v_max3_u32 v60, v60, v30, v31\n\t"
    "v_max_u32 v60, v60, v61\n\t"
    "v_cmp_eq_u32 vcc, -1, v60\n\t"
    "s_cbranch_vccnz 1f\n\t"
    "2:\n\t"
    "s_waitcnt lgkmcnt(0)\n\t"
    "s_barrier\n\t"
    "ds_read_b128 v[32:35], v5 offset:0\n\t"
    "ds_read_b128 v[36:39], v5 offset:1024\n\t"
    "v_smfmac_f32_16x16x64_bf16 v[16:19], v[28:31], v[176:183], v6\n\t"
    "ds_read_b128 v[40:43], v5 offset:2048\n\t"
    "ds_read_b128 v[44:47], v5 offset:3072\n\t"
    "v_smfmac_f32_16x16x64_bf16 v[20:23], v[28:31], v[184:191], v6\n\t"
    "s_waitcnt lgkmcnt(3)\n\t"
    "v_smfmac_f32_16x16x64_bf16 v[16:19], v[32:35], v[64:71], v6\n\t"
    "ds_read_b128 v[48:51], v5 offset:4096\n\t"
    "v_smfmac_f32_16x16x64_bf16 v[20:23], v[32:35], v[72:79], v6\n\t"
    "ds_read_b128 v[52:55], v5 offset:5120\n\t"
    "s_waitcnt lgkmcnt(4)\n\t"
    "v_smfmac_f32_16x16x64_bf16 v[16:19], v[36:39], v[80:87], v6\n\t"
    "ds_read_b128 v[24:27], v5 offset:8192\n\t"
    "v_smfmac_f32_16x16x64_bf16 v[20:23], v[36:39], v[88:95], v6\n\t"
    "ds_read_b128 v[28:31], v5 offset:9216\n\t"
    "s_waitcnt lgkmcnt(5)\n\t"
    "v_smfmac_f32_16x16x64_bf16 v[16:19], v[40:43], v[96:103], v6\n\t"
    "ds_read_b128 v[56:59], v5 offset:10240\n\t"
    "v_smfmac_f32_16x16x64_bf16 v[20:23], v[40:43], v[104:111], v6\n\t"
    "ds_read_b128 v[60:63], v5 offset:11264\n\t"
    "ds_read_b128 v[36:39], v10 offset:35840\n\t"
    "s_waitcnt lgkmcnt(7)\n\t"
    "v_smfmac_f32_16x16x64_bf16 v[16:19], v[44:47], v[112:119], v6\n\t"
    "ds_read_b128 v[40:43], v10 offset:36864\n\t"
    "v_smfmac_f32_16x16x64_bf16 v[20:23], v[44:47], v[120:127], v6\n\t"
    "ds_read_b128 v[44:47], v10 offset:37888\n\t"
    "s_waitcnt lgkmcnt(8)\n\t"
    "v_smfmac_f32_16x16x64_bf16 v[16:19], v[48:51], v[128:135], v6\n\t"
    "v_smfmac_f32_16x16x64_bf16 v[20:23], v[48:51], v[136:143], v6\n\t"
    "ds_read_b128 v[48:51], v10 offset:38912\n\t"
    "s_waitcnt lgkmcnt(8)\n\t"
    "v_smfmac_f32_16x16x64_bf16 v[16:19], v[52:55], v[144:151], v6\n\t"
    "v_smfmac_f32_16x16x64_bf16 v[20:23], v[52:55], v[152:159], v6\n\t"
    "ds_read_b128 v[52:55], v10 offset:39936\n\t"
    "s_waitcnt lgkmcnt(8)\n\t"
    "v_smfmac_f32_16x16x64_bf16 v[16:19], v[24:27], v[192:199], v6\n\t"
    "v_smfmac_f32_16x16x64_bf16 v[20:23], v[24:27], v[200:207], v6\n\t"
    "ds_read_b128 v[24:27], v10 offset:32768\n\t"
    "s_waitcnt lgkmcnt(8)\n\t"
    "v_smfmac_f32_16x16x64_bf16 v[16:19], v[28:31], v[208:215], v6\n\t"
    "ds_read_b32 v8, v15 offset:29440\n\t"
    "ds_read_b128 v[32:35], v5 offset:27648\n\t"
    "s_setprio 3\n\t"
    "s_waitcnt lgkmcnt(9)\n\t"
    "v_smfmac_f32_16x16x64_bf16 v[16:19], v[56:59], v[224:231], v6\n\t"
    "s_waitcnt lgkmcnt(8)\n\t"
    "v_smfmac_f32_16x16x64_bf16 v[16:19], v[60:63], v[240:247], v6\n\t"
    "v_smfmac_f32_16x16x64_bf16 v[20:23], v[28:31], v[216:223], v6\n\t"
    "s_waitcnt lgkmcnt(0)\n\t"
    "21:\n\t"
    "v_cmp_ne_u32 vcc, %[sn], v8\n\t"
    "s_cbranch_vccnz 22f\n\t"
    "v_mul_f32 v32, %[lsc], v32\n\t"
    "v_mul_f32 v33, %[lsc], v33\n\t"
    "v_smfmac_f32_16x16x64_bf16 v[20:23], v[56:59], v[232:239], v6\n\t"
    "v_mul_f32 v34, %[lsc], v34\n\t"
    "v_mul_f32 v35, %[lsc], v35\n\t"
    "v_add_f32 v16, v16, v18\n\t"
    "v_add_f32 v17, v17, v19\n\t"
    "v_smfmac_f32_16x16x64_bf16 v[20:23], v[60:63], v[248:255], v6\n\t"
    "v_fma_f32 v16, v16, %[lsc], v32\n\t"
    "v_fma_f32 v17, v17, %[lsc], v33\n\t"
    "v_cvt_pk_bf16_f32 v16, v16, v17\n\t"
    "ds_write_b32 v15, v16 offset:17920\n\t"
    "s_mov_b32 %[bg], %[big]\n\t"
    "s_nop 2\n\t"
    "v_add_f32 v20, v20, v22\n\t"
    "v_add_f32 v21, v21, v23\n\t"
    "v_fma_f32 v20, v20, %[lsc], v34\n\t"
    "v_fma_f32 v21, v21, %[lsc], v35\n\t"
    "v_cvt_pk_bf16_f32 v20, v20, v21\n\t"
    "ds_write_b32 v15, v20 offset:18176\n\t"
    "ds_read_b128 v[28:31], v10 offset:33792\n\t"
    "ds_read_b128 v[32:35], v10 offset:34816\n\t"
    "s_waitcnt lgkmcnt(2)\n\t"
    "s_barrier\n\t"
    "ds_read_b64_tr_b16 v[16:17], v7 offset:16384\n\t"
    "ds_read_b64_tr_b16 v[18:19], v7 offset:16448\n\t"
    "ds_read_b64_tr_b16 v[20:21], v7 offset:16896\n\t"
    "ds_read_b64_tr_b16 v[22:23], v7 offset:16960\n\t"
    "ds_read_b64_tr_b16 v[56:57], v7 offset:17408\n\t"
    "ds_read_b64_tr_b16 v[58:59], v7 offset:17472\n\t"
    "ds_read_b64_tr_b16 v[60:61], v7 offset:17920\n\t"
    "ds_read_b64_tr_b16 v[62:63], v7 offset:17984\n\t"
    "global_load_dwordx4 v[6:9], v5, %[vs] sc1\n\t"
    "global_load_dwordx4 v[10:13], v5, %[vs] offset:1024 sc1\n\t"
    "s_waitcnt lgkmcnt(6)\n\t"
    "v_mfma_f32_16x16x32_bf16 v[24:27], v[24:27], v[16:19], 0\n\t"
    "v_mfma_f32_16x16x32_bf16 v[28:31], v[28:31], v[16:19], 0\n\t"
    "s_waitcnt lgkmcnt(4)\n\t"
    "v_mfma_f32_16x16x32_bf16 v[24:27], v[32:35], v[20:23], v[24:27]\n\t"
    "v_mfma_f32_16x16x32_bf16 v[28:31], v[36:39], v[20:23], v[28:31]\n\t"
    "v_xor_b32 v39, 32, v14\n\t"
    "ds_read_b64_tr_b16 v[32:33], v14 offset:47104\n\t"
    "ds_read_b64_tr_b16 v[34:35], v14 offset:48128\n\t"
    "ds_read_b64_tr_b16 v[36:37], v39 offset:47104\n\t"
    "ds_read_b64_tr_b16 v[38:39], v39 offset:48128\n\t"
    "s_waitcnt lgkmcnt(6)\n\t"
    "v_mfma_f32_16x16x32_bf16 v[24:27], v[40:43], v[56:59], v[24:27]\n\t"
    "v_mfma_f32_16x16x32_bf16 v[28:31], v[44:47], v[56:59], v[28:31]\n\t"
    "s_waitcnt lgkmcnt(4)\n\t"
    "v_mfma_f32_16x16x32_bf16 v[24:27], v[48:51], v[60:63], v[24:27]\n\t"
    "v_mfma_f32_16x16x32_bf16 v[28:31], v[52:55], v[60:63], v[28:31]\n\t"
    "v_mov_b32 v52, %[one]\n\t"
    "v_mov_b32 v53, %[one]\n\t"
    "v_mov_b32 v54, %[one]\n\t"
    "v_mov_b32 v55, %[one]\n\t"
    "v_mov_b32 v45, 0\n\t"
    "v_mov_b32 v47, 0\n\t"
    "v_mov_b32 v48, %[one]\n\t"
    "v_max3_f32 v14, |v24|, |v25|, |v26|\n\t"
    "v_max3_f32 v40, |v27|, |v28|, |v29|\n\t"
    "v_max3_f32 v14, v14, |v30|, |v31|\n\t"
    "v_max_f32 v14, v14, v40\n\t"
    "v_cmp_nge_f32 vcc, %[big], v14\n\t"
    "v_mov_b32 v14, 0\n\t"
    "s_cbranch_vccnz 12f\n\t"
    "13:\n\t"
    "v_exp_f32 v24, v24\n\t"
    "v_exp_f32 v25, v25\n\t"
    "v_exp_f32 v26, v26\n\t"
    "v_exp_f32 v27, v27\n\t"
    "v_exp_f32 v28, v28\n\t"
    "v_exp_f32 v29, v29\n\t"
    "v_exp_f32 v30, v30\n\t"
    "v_exp_f32 v31, v31\n\t"
    "v_lshrrev_b32 v46, 2, v5\n\t"
    "global_load_dword v44, v46, %[v2] sc1\n\t"
    "global_load_dword v46, v46, %[v2] offset:256 sc1\n\t"
    "v_cvt_pk_bf16_f32 v40, v24, v25\n\t"
    "v_cvt_pk_bf16_f32 v41, v26, v27\n\t"
    "v_cvt_pk_bf16_f32 v42, v28, v29\n\t"
    "v_cvt_pk_bf16_f32 v43, v30, v31\n\t"
    "v_mov_b32 v49, 0\n\t"
    "v_mov_b32 v51, 0\n\t"
    "s_waitcnt lgkmcnt(0)\n\t"
    "v_mfma_f32_16x16x32_bf16 v[52:55], v[52:55], v[40:43], 0\n\t"
    "v_mfma_f32_16x16x32_bf16 v[24:27], v[32:35], v[40:43], 0\n\t"
    "v_mfma_f32_16x16x32_bf16 v[28:31], v[36:39], v[40:43], 0\n\t"
    "s_waitcnt vmcnt(3)\n\t"
    "5:\n\t"
    "v_mfma_f32_16x16x32_bf16 v[32:35], v[6:9], v[16:19], 0\n\t"
    "v_mfma_f32_16x16x32_bf16 v[36:39], v[6:9], v[20:23], 0\n\t"
    "s_waitcnt vmcnt(2)\n\t"
    "v_mfma_f32_16x16x32_bf16 v[32:35], v[10:13], v[56:59], v[32:35]\n\t"
    "v_mfma_f32_16x16x32_bf16 v[36:39], v[10:13], v[60:63], v[36:39]\n\t"
    "v_max3_u32 v40, v6, v7, v8\n\t"
    "v_max3_u32 v41, v9, v10, v11\n\t"
    "v_max3_u32 v40, v40, v12, v13\n\t"
    "v_max_u32 v40, v40, v41\n\t"
    "v_cmp_eq_u32 vcc, -1, v40\n\t"
    "s_cbranch_vccnz 4f\n\t"
    "6:\n\t"
    "s_nop 0\n\t"
    "s_nop 0\n\t"
    "v_add_f32 v32, v32, v38\n\t"
    "v_add_f32 v33, v33, v39\n\t"
    "v_exp_f32 v34, v32\n\t"
    "v_max_f32 v40, v32, v33\n\t"
    "v_exp_f32 v35, v33\n\t"
    "v_cmp_nge_f32 vcc, %[bg], v40\n\t"
    "s_cbranch_vccnz 10f\n\t"
    "11:\n\t"
    "v_cvt_pk_bf16_f32 v50, v34, v35\n\t"
    "s_nop 1\n\t"
    "v_mfma_f32_16x16x16_bf16 v[52:55], v[48:49], v[50:51], v[52:55]\n\t"
    "17:\n\t"
    "s_waitcnt vmcnt(0) lgkmcnt(0)\n\t"
    "v_mfma_f32_16x16x16_bf16 v[32:35], v[44:45], v[50:51], v[24:27]\n\t"
    "v_mfma_f32_16x16x16_bf16 v[36:39], v[46:47], v[50:51], v[28:31]\n\t"
    "v_max_u32 v40, v44, v46\n\t"
    "v_cmp_eq_u32 vcc, -1, v40\n\t"
    "s_memrealtime s[36:37]\n\t"
    "s_nop 1\n\t"
    "v_rcp_f32 v42, v52\n\t"
    "s_cbranch_vccnz 18f\n\t"
    "19:\n\t"
    "v_pk_mul_f32 v[32:33], v[32:33], v[42:43] op_sel_hi:[1,0]\n\t"
    "v_pk_mul_f32 v[34:35], v[34:35], v[42:43] op_sel_hi:[1,0]\n\t"
    "v_pk_mul_f32 v[36:37], v[36:37], v[42:43] op_sel_hi:[1,0]\n\t"
    "v_pk_mul_f32 v[38:39], v[38:39], v[42:43] op_sel_hi:[1,0]\n\t"
    "v_cvt_pk_bf16_f32 v24, v32, v33\n\t"
    "v_cvt_pk_bf16_f32 v25, v34, v35\n\t"
    "v_cvt_pk_bf16_f32 v26, v36, v37\n\t"
    "v_cvt_pk_bf16_f32 v27, v38, v39\n\t"
    "v_min_u32 v24, -2, v24\n\t"
    "v_min_u32 v25, -2, v25\n\t"
    "v_min_u32 v26, -2, v26\n\t"
    "v_min_u32 v27, -2, v27\n\t"
    "global_store_dwordx4 v4, v[24:27], %[od] sc1\n\t"
    "v_lshlrev_b32 v40, 10, v5\n\t"
    "s_waitcnt lgkmcnt(0)\n\t"
    "s_and_b32 s36, s36, 0xffffff\n\t"
    "s_or_b32 s36, s36, %[sn]\n\t"
    "v_mov_b32 v41, s36\n\t"
    "s_mov_b64 exec, 7\n\t"
    "global_store_dword v40, v41, %[db] offset:8 sc1\n\t"
    "s_mov_b64 exec, -1\n\t"
    "ds_write_b32 v15, v44 offset:19968\n\t"
    "ds_write_b32 v15, v46 offset:20224\n\t"
    "s_setprio 1\n\t"
    "s_waitcnt lgkmcnt(0)\n\t"
    "s_barrier\n\t"
    "s_waitcnt lgkmcnt(0)\n\t"
    "s_branch 9f\n\t"
    "1:\n\t"
    "s_add_u32 %[sp], %[sp], 1\n\t"
    "s_cmp_ge_u32 %[sp], %[cap]\n\t"
    "s_cbranch_scc1 7f\n\t"
    "global_load_dwordx4 v[24:27], v5, %[ag] sc1\n\t"
    "global_load_dwordx4 v[28:31], v5, %[ag] offset:1024 sc1\n\t"
    "s_waitcnt lgkmcnt(0)\n\t"
    "s_memtime %[tc]\n\t"
    "s_nop 7\n\t"
    "s_branch 3b\n\t"
    "7:\n\t"
    "s_mov_b32 %[cap], 0\n\t"
    "s_branch 2b\n\t"
    "4:\n\t"
    "s_add_u32 %[sp], %[sp], 0x10000\n\t"
    "s_lshr_b32 %[st], %[sp], 16\n\t"
    "s_cmp_ge_u32 %[st], %[cap]\n\t"
    "s_cbranch_scc1 8f\n\t"
    "global_load_dwordx4 v[6:9], v5, %[vs] sc1\n\t"
    "global_load_dwordx4 v[10:13], v5, %[vs] offset:1024 sc1\n\t"
    "s_waitcnt vmcnt(0) lgkmcnt(0)\n\t"
    "s_branch 5b\n\t"
    "8:\n\t"
    "s_mov_b32 %[cap], 0\n\t"
    "s_branch 6b\n\t"
    "18:\n\t"
    "s_add_u32 %[sp], %[sp], 0x10000\n\t"
    "s_lshr_b32 %[st], %[sp], 16\n\t"
    "s_cmp_ge_u32 %[st], %[cap]\n\t"
    "s_cbranch_scc1 20f\n\t"
    "v_lshrrev_b32 v46, 2, v5\n\t"
    "global_load_dword v44, v46, %[v2] sc1\n\t"
    "global_load_dword v46, v46, %[v2] offset:256 sc1\n\t"
    "s_waitcnt vmcnt(0) lgkmcnt(0)\n\t"
    "s_branch 17b\n\t"
    "20:\n\t"
    "s_mov_b32 %[cap], 0\n\t"
    "s_branch 19b\n\t"
    "22:\n\t"
    "ds_read_b32 v8, v15 offset:29440\n\t"
    "ds_read_b128 v[32:35], v5 offset:27648\n\t"
    "s_waitcnt lgkmcnt(0)\n\t"
    "s_branch 21b\n\t"
    "10:\n\t"
    "v_max_f32 v40, v40, v14\n\t"
    "v_mov_b32 v42, v40\n\t"
    "s_nop 1\n\t"
    "v_permlane16_swap_b32 v40, v42\n\t"
    "v_max_f32 v40, v40, v42\n\t"
    "v_mov_b32 v42, v40\n\t"
    "s_nop 1\n\t"
    "v_permlane32_swap_b32 v40, v42\n\t"
    "v_max_f32 v40, v40, v42\n\t"
    "v_sub_f32 v34, v32, v40\n\t"
    "v_sub_f32 v35, v33, v40\n\t"
    "v_sub_f32 v40, v14, v40\n\t"
    "v_exp_f32 v40, v40\n\t"
    "v_exp_f32 v34, v34\n\t"
    "v_exp_f32 v35, v35\n\t"
    "v_mul_f32 v24, v24, v40\n\t"
    "v_mul_f32 v25, v25, v40\n\t"
    "v_mul_f32 v26, v26, v40\n\t"
    "v_mul_f32 v27, v27, v40\n\t"
    "v_mul_f32 v28, v28, v40\n\t"
    "v_mul_f32 v29, v29, v40\n\t"
    "v_mul_f32 v30, v30, v40\n\t"
    "v_mul_f32 v31, v31, v40\n\t"
    "v_mul_f32 v52, v52, v40\n\t"
    "v_mul_f32 v53, v53, v40\n\t"
    "v_mul_f32 v54, v54, v40\n\t"
    "v_mul_f32 v55, v55, v40\n\t"
    "s_branch 11b\n\t"
    "12:\n\t"
    "v_max3_f32 v14, v24, v25, v26\n\t"
    "v_max3_f32 v40, v27, v28, v29\n\t"
    "v_max3_f32 v14, v14, v30, v31\n\t"
    "v_max3_f32 v14, v14, v40, %[neg]\n\t"
    "v_mov_b32 v40, v14\n\t"
    "s_nop 1\n\t"
    "v_permlane16_swap_b32 v14, v40\n\t"
    "v_max_f32 v14, v14, v40\n\t"
    "v_mov_b32 v40, v14\n\t"
    "s_nop 1\n\t"
    "v_permlane32_swap_b32 v14, v40\n\t"
    "v_max_f32 v14, v14, v40\n\t"
    "v_sub_f32 v24, v24, v14\n\t"
    "v_sub_f32 v25, v25, v14\n\t"
    "v_sub_f32 v26, v26, v14\n\t"
    "v_sub_f32 v27, v27, v14\n\t"
    "v_sub_f32 v28, v28, v14\n\t"
    "v_sub_f32 v29, v29, v14\n\t"
    "v_sub_f32 v30, v30, v14\n\t"
    "v_sub_f32 v31, v31, v14\n\t"
    "s_mov_b32 %[bg], 0x7fc00000\n\t"
    "s_branch 13b\n\t"
    "9:"
    : [tc] "=&s"(tc), [sp] "=&s"(sp), [cap] "+s"(cap), [st] "=&s"(st), [bg] "=&s"(bg)
    : [ag] "s"(ag), [c44] "s"(0x4444u), [lsc] "s"(LSC), [sn] "s"(sn), [pb] "s"(pb), [vs] "s"(vs), [od] "s"(od), [neg] "s"(-1.0e38f), [one] "s"(0x3F803F80u), [big] "s"(24.0f), [v2] "s"(v2), [db] "s"(db), "{v[64:95]}"(W.w0), "{v[96:127]}"(W.w1), "{v[128:159]}"(W.w2), "{v[160:191]}"(W.w3), "{v[192:223]}"(W.w4), "{v[224:255]}"(W.w5)
    : "v4", "v5", "v6", "v7", "v8", "v9", "v10", "v11", "v12", "v13", "v14", "v15", "v16", "v17", "v18", "v19", "v20", "v21", "v22", "v23", "v24", "v25", "v26", "v27", "v28", "v29", "v30", "v31", "v32", "v33", "v34", "v35", "v36", "v37", "v38", "v39", "v40", "v41", "v42", "v43", "v44", "v45", "v46", "v47", "v48", "v49", "v50", "v51", "v52", "v53", "v54", "v55", "v56", "v57", "v58", "v59", "v60", "v61", "v62", "v63", "vcc", "scc", "m0", "memory", "s36", "s37", "s38");
}
template <> __device__ __forceinline__ void qk_step<4>(const QKW& W, const char* ag, const char* vs, const char* v2, const char* db, const char* od, unsigned pb, unsigned sn, unsigned& cap, unsigned& sp,
    unsigned long long& tc, unsigned long long* ts, unsigned& d0, unsigned& d1) {
  unsigned st, bg;
  asm volatile("v_mbcnt_lo_u32_b32 v60, -1, 0\n\t"
    "v_mbcnt_hi_u32_b32 v60, -1, v60\n\t"
    "v_lshlrev_b32 v6, 4, v60\n\t"
    "s_mov_b32 %[sp], 0\n\t"
    "s_setprio 3\n\t"
    "global_load_dwordx4 v[24:27], v6, %[ag] sc1\n\t"
    "global_load_dwordx4 v[28:31], v6, %[ag] offset:-4096 sc1\n\t"
    "s_memtime %[tc]\n\t"
    "v_lshrrev_b32 v56, 4, v60\n\t"
    "v_and_b32 v57, 15, v60\n\t"
    "v_mov_b32 v20, 0\n\t"
    "v_mov_b32 v21, 0\n\t"
    "v_mov_b32 v22, 0\n\t"
    "v_mov_b32 v23, 0\n\t"
    "v_mov_b32 v11, %[sn]\n\t"
    "v_mov_b32 v10, -1\n\t"
    "v_and_b32 v61, 2, v60\n\t"
    "v_mul_u32_u24 v61, 0x5555, v61\n\t"
    "v_or_b32 v8, %[c44], v61\n\t"
    "v_lshlrev_b32 v61, 2, v56\n\t"
    "v_lshl_or_b32 v9, v57, 4, v61\n\t"
    "3:\n\t"
    "v_mov_b32 v12, 0\n\t"
    "v_mov_b32 v13, 0\n\t"
    "v_mov_b32 v14, 0\n\t"
    "v_mov_b32 v15, 0\n\t"
    "v_mov_b32 v16, 0\n\t"
    "v_mov_b32 v17, 0\n\t"
    "v_mov_b32 v18, 0\n\t"
    "v_mov_b32 v19, 0\n\t"
    "s_waitcnt vmcnt(0) lgkmcnt(0)\n\t"
    "33:\n\t"
    "ds_write_b128 v6, v[24:27] offset:12288\n\t"
    "ds_write_b128 v6, v[28:31] offset:8192\n\t"
    "v_smfmac_f32_16x16x64_bf16 v[12:15], v[24:27], v[64:71], v8\n\t"
    "v_max3_u32 v60, v24, v25, v26\n\t"
    "v_max3_u32 v61, v27, v28, v29\n\t"
    "v_smfmac_f32_16x16x64_bf16 v[16:19], v[24:27], v[72:79], v8\n\t"
    "v_max3_u32 v60, v60, v30, v31\n\t"
    "v_max_u32 v60, v60, v61\n\t"
    "v_cmp_eq_u32 vcc, -1, v60\n\t"
    "s_cbranch_vccnz 1f\n\t"
    "2:\n\t"
    "s_waitcnt lgkmcnt(0)\n\t"
    "s_barrier\n\t"
    "ds_read_b128 v[32:35], v6 offset:13312\n\t"
    "ds_read_b128 v[36:39], v6 offset:14336\n\t"
    "ds_read_b128 v[40:43], v6 offset:15360\n\t"
    "ds_write_b32 v9, v10 offset:18432\n\t"
    "s_waitcnt lgkmcnt(3)\n\t"
    "v_smfmac_f32_16x16x64_bf16 v[12:15], v[32:35], v[80:87], v8\n\t"
    "v_smfmac_f32_16x16x64_bf16 v[16:19], v[32:35], v[88:95], v8\n\t"
    "s_waitcnt lgkmcnt(2)\n\t"
    "v_smfmac_f32_16x16x64_bf16 v[12:15], v[36:39], v[96:103], v8\n\t"
    "v_smfmac_f32_16x16x64_bf16 v[16:19], v[36:39], v[104:111], v8\n\t"
    "s_waitcnt lgkmcnt(1)\n\t"
    "v_smfmac_f32_16x16x64_bf16 v[12:15], v[40:43], v[112:119], v8\n\t"
    "v_smfmac_f32_16x16x64_bf16 v[16:19], v[40:43], v[120:127], v8\n\t"
    "s_setprio 0\n\t"
    "s_sleep 1\n\t"
    "s_nop 6\n\t"
    "v_add_f32 v12, v12, v14\n\t"
    "v_add_f32 v13, v13, v15\n\t"
    "v_add_f32 v14, v16, v18\n\t"
    "v_add_f32 v15, v17, v19\n\t"
    "ds_write_b128 v6, v[12:15] offset:24576\n\t"
    "ds_write_b32 v9, v11 offset:28672\n\t"
    "s_setprio 0\n\t"
    "s_memrealtime s[36:37]\n\t"
    "s_waitcnt lgkmcnt(0)\n\t"
    "s_mov_b32 s38, s36\n\t"
    "s_min_u32 s37, %[d0], %[d1]\n\t"
    "s_add_u32 s36, s36, s37\n\t"
    "s_and_b32 s36, s36, 0xffffff\n\t"
    "s_or_b32 s36, s36, %[sn]\n\t"
    "v_lshlrev_b32 v10, 10, v6\n\t"
    "v_mov_b32 v5, s36\n\t"
    "s_mov_b64 exec, 15\n\t"
    "global_store_dword v10, v5, %[vs] offset:-4096 sc1\n\t"
    "s_mov_b64 exec, -1\n\t"
    "ds_read_b128 v[44:47], v6 offset:0\n\t"
    "ds_read_b128 v[48:51], v6 offset:1024\n\t"
    "ds_read_b128 v[52:55], v6 offset:2048\n\t"
    "ds_read_b128 v[56:59], v6 offset:3072\n\t"
    "ds_read_b128 v[60:63], v6 offset:4096\n\t"
    "s_sleep 1\n\t"
    "v_smfmac_f32_16x16x64_bf16 v[20:23], v[28:31], v[192:199], v8\n\t"
    "ds_read_b128 v[28:31], v6 offset:5120\n\t"
    "v_smfmac_f32_16x16x64_bf16 v[20:23], v[24:27], v[224:231], v8\n\t"
    "ds_read_b128 v[24:27], v6 offset:6144\n\t"
    "v_smfmac_f32_16x16x64_bf16 v[20:23], v[32:35], v[240:247], v8\n\t"
    "ds_read_b128 v[32:35], v6 offset:7168\n\t"
    "v_smfmac_f32_16x16x64_bf16 v[20:23], v[36:39], v[232:239], v8\n\t"
    "ds_read_b128 v[36:39], v6 offset:9216\n\t"
    "v_smfmac_f32_16x16x64_bf16 v[20:23], v[40:43], v[248:255], v8\n\t"
    "ds_read_b128 v[40:43], v6 offset:10240\n\t"
    "s_waitcnt lgkmcnt(9)\n\t"
    "v_smfmac_f32_16x16x64_bf16 v[20:23], v[44:47], v[128:135], v8\n\t"
    "ds_read_b128 v[44:47], v6 offset:11264\n\t"
    "s_barrier\n\t"
    "s_setprio 0\n\t"
    "s_waitcnt lgkmcnt(9)\n\t"
    "v_smfmac_f32_16x16x64_bf16 v[20:23], v[48:51], v[144:151], v8\n\t"
    "s_waitcnt lgkmcnt(8)\n\t"
    "v_smfmac_f32_16x16x64_bf16 v[20:23], v[52:55], v[136:143], v8\n\t"
    "s_waitcnt lgkmcnt(7)\n\t"
    "v_smfmac_f32_16x16x64_bf16 v[20:23], v[56:59], v[152:159], v8\n\t"
    "s_waitcnt lgkmcnt(6)\n\t"
    "v_smfmac_f32_16x16x64_bf16 v[20:23], v[60:63], v[160:167], v8\n\t"
    "s_waitcnt lgkmcnt(5)\n\t"
    "v_smfmac_f32_16x16x64_bf16 v[20:23], v[28:31], v[176:183], v8\n\t"
    "s_waitcnt lgkmcnt(4)\n\t"
    "v_smfmac_f32_16x16x64_bf16 v[20:23], v[24:27], v[168:175], v8\n\t"
    "s_waitcnt lgkmcnt(3)\n\t"
    "v_smfmac_f32_16x16x64_bf16 v[20:23], v[32:35], v[184:191], v8\n\t"
    "s_waitcnt lgkmcnt(2)\n\t"
    "v_smfmac_f32_16x16x64_bf16 v[20:23], v[36:39], v[208:215], v8\n\t"
    "s_waitcnt lgkmcnt(1)\n\t"
    "v_smfmac_f32_16x16x64_bf16 v[20:23], v[40:43], v[200:207], v8\n\t"
    "s_waitcnt lgkmcnt(0)\n\t"
    "v_smfmac_f32_16x16x64_bf16 v[20:23], v[44:47], v[216:223], v8\n\t"
    "s_nop 7\n\t"
    "v_add_f32 v20, v20, v22\n\t"
    "v_add_f32 v21, v21, v23\n\t"
    "v_cvt_pk_bf16_f32 v20, v20, v21\n\t"
    "v_min_u32 v20, -2, v20\n\t"
    "ds_write_b32 v9, v20 offset:18432\n\t"
    "s_waitcnt lgkmcnt(0)\n\t"
    "s_setprio 3\n\t"
    "s_barrier\n\t"
    "s_memrealtime s[36:37]\n\t"
    "s_mov_b32 %[d1], %[d0]\n\t"
    "s_waitcnt lgkmcnt(0)\n\t"
    "s_sub_u32 %[d0], s36, s38\n\t"
    "s_min_u32 %[d0], %[d0], 200\n\t"
    "s_waitcnt lgkmcnt(0)\n\t"
    "s_branch 9f\n\t"
    "1:\n\t"
    "s_add_u32 %[sp], %[sp], 1\n\t"
    "s_cmp_ge_u32 %[sp], %[cap]\n\t"
    "s_cbranch_scc1 7f\n\t"
    "global_load_dwordx4 v[24:27], v6, %[ag] sc1\n\t"
    "global_load_dwordx4 v[28:31], v6, %[ag] offset:-4096 sc1\n\t"
    "s_waitcnt lgkmcnt(0)\n\t"
    "s_memtime %[tc]\n\t"
    "s_nop 7\n\t"
    "s_branch 3b\n\t"
    "7:\n\t"
    "s_mov_b32 %[cap], 0\n\t"
    "s_branch 2b\n\t"
    "9:"
    : [tc] "=&s"(tc), [sp] "=&s"(sp), [cap] "+s"(cap), [st] "=&s"(st), [d0] "+s"(d0), [d1] "+s"(d1)
    : [ag] "s"(ag), [c44] "s"(0x4444u), [lsc] "s"(LSC), [sn] "s"(sn), [vs] "s"(vs), "{v[64:95]}"(W.w0), "{v[96:127]}"(W.w1), "{v[128:159]}"(W.w2), "{v[160:191]}"(W.w3), "{v[192:223]}"(W.w4), "{v[224:255]}"(W.w5)
    : "v5", "v6", "v7", "v8", "v9", "v10", "v11", "v12", "v13", "v14", "v15", "v16", "v17", "v18", "v19", "v20", "v21", "v22", "v23", "v24", "v25", "v26", "v27", "v28", "v29", "v30", "v31", "v32", "v33", "v34", "v35", "v36", "v37", "v38", "v39", "v40", "v41", "v42", "v43", "v44", "v45", "v46", "v47", "v48", "v49", "v50", "v51", "v52", "v53", "v54", "v55", "v56", "v57", "v58", "v59", "v60", "v61", "v62", "v63", "vcc", "scc", "m0", "memory", "s36", "s37", "s38");
}
template <> __device__ __forceinline__ void qk_step<5>(const QKW& W, const char* ag, const char* vs, const char* v2, const char* db, const char* od, unsigned pb, unsigned sn, unsigned& cap, unsigned& sp,
    unsigned long long& tc, unsigned long long* ts, unsigned& d0, unsigned& d1) {
  unsigned st, bg;
  asm volatile("v_mbcnt_lo_u32_b32 v60, -1, 0\n\t"
    "v_mbcnt_hi_u32_b32 v60, -1, v60\n\t"
    "v_lshlrev_b32 v6, 4, v60\n\t"
    "s_mov_b32 %[sp], 0\n\t"
    "s_setprio 3\n\t"
    "global_load_dwordx4 v[24:27], v6, %[ag] sc1\n\t"
    "global_load_dwordx4 v[28:31], v6, %[ag] offset:-4096 sc1\n\t"
    "s_memtime %[tc]\n\t"
    "v_lshrrev_b32 v56, 4, v60\n\t"
    "v_and_b32 v57, 15, v60\n\t"
    "v_mov_b32 v20, 0\n\t"
    "v_mov_b32 v21, 0\n\t"
    "v_mov_b32 v22, 0\n\t"
    "v_mov_b32 v23, 0\n\t"
    "v_mov_b32 v11, %[sn]\n\t"
    "v_mov_b32 v10, -1\n\t"
    "v_and_b32 v61, 2, v60\n\t"
    "v_mul_u32_u24 v61, 0x5555, v61\n\t"
    "v_or_b32 v8, %[c44], v61\n\t"
    "v_lshlrev_b32 v61, 2, v56\n\t"
    "v_lshl_or_b32 v9, v57, 4, v61\n\t"
    "3:\n\t"
    "v_mov_b32 v12, 0\n\t"
    "v_mov_b32 v13, 0\n\t"
    "v_mov_b32 v14, 0\n\t"
    "v_mov_b32 v15, 0\n\t"
    "v_mov_b32 v16, 0\n\t"
    "v_mov_b32 v17, 0\n\t"
    "v_mov_b32 v18, 0\n\t"
    "v_mov_b32 v19, 0\n\t"
    "s_waitcnt vmcnt(0) lgkmcnt(0)\n\t"
    "33:\n\t"
    "ds_write_b128 v6, v[24:27] offset:13312\n\t"
    "ds_write_b128 v6, v[28:31] offset:9216\n\t"
    "v_smfmac_f32_16x16x64_bf16 v[12:15], v[24:27], v[80:87], v8\n\t"
    "v_max3_u32 v60, v24, v25, v26\n\t"
    "v_max3_u32 v61, v27, v28, v29\n\t"
    "v_smfmac_f32_16x16x64_bf16 v[16:19], v[24:27], v[88:95], v8\n\t"
    "v_max3_u32 v60, v60, v30, v31\n\t"
    "v_max_u32 v60, v60, v61\n\t"
    "v_cmp_eq_u32 vcc, -1, v60\n\t"
    "s_cbranch_vccnz 1f\n\t"
    "2:\n\t"
    "s_waitcnt lgkmcnt(0)\n\t"
    "s_barrier\n\t"
    "ds_read_b128 v[32:35], v6 offset:12288\n\t"
    "ds_read_b128 v[36:39], v6 offset:14336\n\t"
    "ds_read_b128 v[40:43], v6 offset:15360\n\t"
    "ds_write_b32 v9, v10 offset:18688\n\t"
    "s_waitcnt lgkmcnt(3)\n\t"
    "v_smfmac_f32_16x16x64_bf16 v[12:15], v[32:35], v[64:71], v8\n\t"
    "v_smfmac_f32_16x16x64_bf16 v[16:19], v[32:35], v[72:79], v8\n\t"
    "s_waitcnt lgkmcnt(2)\n\t"
    "v_smfmac_f32_16x16x64_bf16 v[12:15], v[36:39], v[96:103], v8\n\t"
    "v_smfmac_f32_16x16x64_bf16 v[16:19], v[36:39], v[104:111], v8\n\t"
    "s_waitcnt lgkmcnt(1)\n\t"
    "v_smfmac_f32_16x16x64_bf16 v[12:15], v[40:43], v[112:119], v8\n\t"
    "v_smfmac_f32_16x16x64_bf16 v[16:19], v[40:43], v[120:127], v8\n\t"
    "s_setprio 0\n\t"
    "s_sleep 1\n\t"
    "s_nop 6\n\t"
    "v_add_f32 v12, v12, v14\n\t"
    "v_add_f32 v13, v13, v15\n\t"
    "v_add_f32 v14, v16, v18\n\t"
    "v_add_f32 v15, v17, v19\n\t"
    "ds_write_b128 v6, v[12:15] offset:25600\n\t"
    "ds_write_b32 v9, v11 offset:28928\n\t"
    "s_setprio 0\n\t"
    "ds_read_b128 v[44:47], v6 offset:0\n\t"
    "ds_read_b128 v[48:51], v6 offset:1024\n\t"
    "ds_read_b128 v[52:55], v6 offset:2048\n\t"
    "ds_read_b128 v[56:59], v6 offset:3072\n\t"
    "ds_read_b128 v[60:63], v6 offset:4096\n\t"
    "s_sleep 1\n\t"
    "v_smfmac_f32_16x16x64_bf16 v[20:23], v[28:31], v[208:215], v8\n\t"
    "ds_read_b128 v[28:31], v6 offset:5120\n\t"
    "v_smfmac_f32_16x16x64_bf16 v[20:23], v[24:27], v[240:247], v8\n\t"
    "ds_read_b128 v[24:27], v6 offset:6144\n\t"
    "v_smfmac_f32_16x16x64_bf16 v[20:23], v[32:35], v[224:231], v8\n\t"
    "ds_read_b128 v[32:35], v6 offset:7168\n\t"
    "v_smfmac_f32_16x16x64_bf16 v[20:23], v[36:39], v[232:239], v8\n\t"
    "ds_read_b128 v[36:39], v6 offset:8192\n\t"
    "v_smfmac_f32_16x16x64_bf16 v[20:23], v[40:43], v[248:255], v8\n\t"
    "ds_read_b128 v[40:43], v6 offset:10240\n\t"
    "s_waitcnt lgkmcnt(9)\n\t"
    "v_smfmac_f32_16x16x64_bf16 v[20:23], v[44:47], v[128:135], v8\n\t"
    "ds_read_b128 v[44:47], v6 offset:11264\n\t"
    "s_barrier\n\t"
    "s_setprio 0\n\t"
    "s_waitcnt lgkmcnt(9)\n\t"
    "v_smfmac_f32_16x16x64_bf16 v[20:23], v[48:51], v[144:151], v8\n\t"
    "s_waitcnt lgkmcnt(8)\n\t"
    "v_smfmac_f32_16x16x64_bf16 v[20:23], v[52:55], v[136:143], v8\n\t"
    "s_waitcnt lgkmcnt(7)\n\t"
    "v_smfmac_f32_16x16x64_bf16 v[20:23], v[56:59], v[152:159], v8\n\t"
    "s_waitcnt lgkmcnt(6)\n\t"
    "v_smfmac_f32_16x16x64_bf16 v[20:23], v[60:63], v[160:167], v8\n\t"
    "s_waitcnt lgkmcnt(5)\n\t"
    "v_smfmac_f32_16x16x64_bf16 v[20:23], v[28:31], v[176:183], v8\n\t"
    "s_waitcnt lgkmcnt(4)\n\t"
    "v_smfmac_f32_16x16x64_bf16 v[20:23], v[24:27], v[168:175], v8\n\t"
    "s_waitcnt lgkmcnt(3)\n\t"
    "v_smfmac_f32_16x16x64_bf16 v[20:23], v[32:35], v[184:191], v8\n\t"
    "s_waitcnt lgkmcnt(2)\n\t"
    "v_smfmac_f32_16x16x64_bf16 v[20:23], v[36:39], v[192:199], v8\n\t"
    "s_waitcnt lgkmcnt(1)\n\t"
    "v_smfmac_f32_16x16x64_bf16 v[20:23], v[40:43], v[200:207], v8\n\t"
    "s_waitcnt lgkmcnt(0)\n\t"
    "v_smfmac_f32_16x16x64_bf16 v[20:23], v[44:47], v[216:223], v8\n\t"
    "s_nop 7\n\t"
    "v_add_f32 v20, v20, v22\n\t"
    "v_add_f32 v21, v21, v23\n\t"
    "v_cvt_pk_bf16_f32 v20, v20, v21\n\t"
    "v_min_u32 v20, -2, v20\n\t"
    "ds_write_b32 v9, v20 offset:18688\n\t"
    "s_waitcnt lgkmcnt(0)\n\t"
    "s_setprio 0\n\t"
    "s_setprio 0\n\t"
    "s_waitcnt lgkmcnt(0)\n\t"
    "s_branch 9f\n\t"
    "1:\n\t"
    "s_add_u32 %[sp], %[sp], 1\n\t"
    "s_cmp_ge_u32 %[sp], %[cap]\n\t"
    "s_cbranch_scc1 7f\n\t"
    "global_load_dwordx4 v[24:27], v6, %[ag] sc1\n\t"
    "global_load_dwordx4 v[28:31], v6, %[ag] offset:-4096 sc1\n\t"
    "s_waitcnt lgkmcnt(0)\n\t"
    "s_memtime %[tc]\n\t"
    "s_nop 7\n\t"
    "s_branch 3b\n\t"
    "7:\n\t"
    "s_mov_b32 %[cap], 0\n\t"
    "s_branch 2b\n\t"
    "9:"
    : [tc] "=&s"(tc), [sp] "=&s"(sp), [cap] "+s"(cap), [st] "=&s"(st)
    : [ag] "s"(ag), [c44] "s"(0x4444u), [lsc] "s"(LSC), [sn] "s"(sn), "{v[64:95]}"(W.w0), "{v[96:127]}"(W.w1), "{v[128:159]}"(W.w2), "{v[160:191]}"(W.w3), "{v[192:223]}"(W.w4), "{v[224:255]}"(W.w5)
    : "v6", "v7", "v8", "v9", "v10", "v11", "v12", "v13", "v14", "v15", "v16", "v17", "v18", "v19", "v20", "v21", "v22", "v23", "v24", "v25", "v26", "v27", "v28", "v29", "v30", "v31", "v32", "v33", "v34", "v35", "v36", "v37", "v38", "v39", "v40", "v41", "v42", "v43", "v44", "v45", "v46", "v47", "v48", "v49", "v50", "v51", "v52", "v53", "v54", "v55", "v56", "v57", "v58", "v59", "v60", "v61", "v62", "v63", "vcc", "scc", "m0", "memory");
}
template <> __device__ __forceinline__ void qk_step<6>(const QKW& W, const char* ag, const char* vs, const char* v2, const char* db, const char* od, unsigned pb, unsigned sn, unsigned& cap, unsigned& sp,
    unsigned long long& tc, unsigned long long* ts, unsigned& d0, unsigned& d1) {
  unsigned st, bg;
  asm volatile("v_mbcnt_lo_u32_b32 v60, -1, 0\n\t"
    "v_mbcnt_hi_u32_b32 v60, -1, v60\n\t"
    "v_lshlrev_b32 v6, 4, v60\n\t"
    "s_mov_b32 %[sp], 0\n\t"
    "s_setprio 3\n\t"
    "global_load_dwordx4 v[24:27], v6, %[ag] sc1\n\t"
    "global_load_dwordx4 v[28:31], v6, %[ag] offset:-4096 sc1\n\t"
    "s_memtime %[tc]\n\t"
    "v_lshrrev_b32 v56, 4, v60\n\t"
    "v_and_b32 v57, 15, v60\n\t"
    "v_mov_b32 v20, 0\n\t"
    "v_mov_b32 v21, 0\n\t"
    "v_mov_b32 v22, 0\n\t"
    "v_mov_b32 v23, 0\n\t"
    "v_mov_b32 v11, %[sn]\n\t"
    "v_mov_b32 v10, -1\n\t"
    "v_and_b32 v61, 2, v60\n\t"
    "v_mul_u32_u24 v61, 0x5555, v61\n\t"
    "v_or_b32 v8, %[c44], v61\n\t"
    "v_lshlrev_b32 v61, 2, v56\n\t"
    "v_lshl_or_b32 v9, v57, 4, v61\n\t"
    "s_setprio 0\n\t"
    "s_setprio 3\n\t"
    "3:\n\t"
    "v_mov_b32 v12, 0\n\t"
    "v_mov_b32 v13, 0\n\t"
    "v_mov_b32 v14, 0\n\t"
    "v_mov_b32 v15, 0\n\t"
    "v_mov_b32 v16, 0\n\t"
    "v_mov_b32 v17, 0\n\t"
    "v_mov_b32 v18, 0\n\t"
    "v_mov_b32 v19, 0\n\t"
    "s_waitcnt vmcnt(0) lgkmcnt(0)\n\t"
    "33:\n\t"
    "ds_write_b128 v6, v[24:27] offset:14336\n\t"
    "ds_write_b128 v6, v[28:31] offset:10240\n\t"
    "v_smfmac_f32_16x16x64_bf16 v[12:15], v[24:27], v[96:103], v8\n\t"
    "v_max3_u32 v60, v24, v25, v26\n\t"
    "v_max3_u32 v61, v27, v28, v29\n\t"
    "v_smfmac_f32_16x16x64_bf16 v[16:19], v[24:27], v[104:111], v8\n\t"
    "v_max3_u32 v60, v60, v30, v31\n\t"
    "v_max_u32 v60, v60, v61\n\t"
    "v_cmp_eq_u32 vcc, -1, v60\n\t"
    "s_cbranch_vccnz 1f\n\t"
    "2:\n\t"
    "s_waitcnt lgkmcnt(0)\n\t"
    "s_barrier\n\t"
    "ds_read_b128 v[32:35], v6 offset:12288\n\t"
    "ds_read_b128 v[36:39], v6 offset:13312\n\t"
    "ds_read_b128 v[40:43], v6 offset:15360\n\t"
    "ds_write_b32 v9, v10 offset:18944\n\t"
    "s_waitcnt lgkmcnt(3)\n\t"
    "v_smfmac_f32_16x16x64_bf16 v[12:15], v[32:35], v[64:71], v8\n\t"
    "v_smfmac_f32_16x16x64_bf16 v[16:19], v[32:35], v[72:79], v8\n\t"
    "s_waitcnt lgkmcnt(2)\n\t"
    "v_smfmac_f32_16x16x64_bf16 v[12:15], v[36:39], v[80:87], v8\n\t"
    "v_smfmac_f32_16x16x64_bf16 v[16:19], v[36:39], v[88:95], v8\n\t"
    "s_waitcnt lgkmcnt(1)\n\t"
    "v_smfmac_f32_16x16x64_bf16 v[12:15], v[40:43], v[112:119], v8\n\t"
    "v_smfmac_f32_16x16x64_bf16 v[16:19], v[40:43], v[120:127], v8\n\t"
    "s_setprio 0\n\t"
    "s_sleep 1\n\t"
    "s_nop 6\n\t"
    "v_add_f32 v12, v12, v14\n\t"
    "v_add_f32 v13, v13, v15\n\t"
    "v_add_f32 v14, v16, v18\n\t"
    "v_add_f32 v15, v17, v19\n\t"
    "ds_write_b128 v6, v[12:15] offset:26624\n\t"
    "ds_write_b32 v9, v11 offset:29184\n\t"
    "s_setprio 0\n\t"
    "ds_read_b128 v[44:47], v6 offset:0\n\t"
    "ds_read_b128 v[48:51], v6 offset:1024\n\t"
    "ds_read_b128 v[52:55], v6 offset:2048\n\t"
    "ds_read_b128 v[56:59], v6 offset:3072\n\t"
    "ds_read_b128 v[60:63], v6 offset:4096\n\t"
    "s_sleep 1\n\t"
    "v_smfmac_f32_16x16x64_bf16 v[20:23], v[28:31], v[200:207], v8\n\t"
    "ds_read_b128 v[28:31], v6 offset:5120\n\t"
    "v_smfmac_f32_16x16x64_bf16 v[20:23], v[24:27], v[232:239], v8\n\t"
    "ds_read_b128 v[24:27], v6 offset:6144\n\t"
    "v_smfmac_f32_16x16x64_bf16 v[20:23], v[32:35], v[224:231], v8\n\t"
    "ds_read_b128 v[32:35], v6 offset:7168\n\t"
    "v_smfmac_f32_16x16x64_bf16 v[20:23], v[36:39], v[240:247], v8\n\t"
    "ds_read_b128 v[36:39], v6 offset:8192\n\t"
    "v_smfmac_f32_16x16x64_bf16 v[20:23], v[40:43], v[248:255], v8\n\t"
    "ds_read_b128 v[40:43], v6 offset:9216\n\t"
    "s_waitcnt lgkmcnt(9)\n\t"
    "v_smfmac_f32_16x16x64_bf16 v[20:23], v[44:47], v[128:135], v8\n\t"
    "ds_read_b128 v[44:47], v6 offset:11264\n\t"
    "s_barrier\n\t"
    "s_setprio 0\n\t"
    "s_waitcnt lgkmcnt(9)\n\t"
    "v_smfmac_f32_16x16x64_bf16 v[20:23], v[48:51], v[144:151], v8\n\t"
    "s_waitcnt lgkmcnt(8)\n\t"
    "v_smfmac_f32_16x16x64_bf16 v[20:23], v[52:55], v[136:143], v8\n\t"
    "s_waitcnt lgkmcnt(7)\n\t"
    "v_smfmac_f32_16x16x64_bf16 v[20:23], v[56:59], v[152:159], v8\n\t"
    "s_waitcnt lgkmcnt(6)\n\t"
    "v_smfmac_f32_16x16x64_bf16 v[20:23], v[60:63], v[160:167], v8\n\t"
    "s_waitcnt lgkmcnt(5)\n\t"
    "v_smfmac_f32_16x16x64_bf16 v[20:23], v[28:31], v[176:183], v8\n\t"
    "s_waitcnt lgkmcnt(4)\n\t"
    "v_smfmac_f32_16x16x64_bf16 v[20:23], v[24:27], v[168:175], v8\n\t"
    "s_waitcnt lgkmcnt(3)\n\t"
    "v_smfmac_f32_16x16x64_bf16 v[20:23], v[32:35], v[184:191], v8\n\t"
    "s_waitcnt lgkmcnt(2)\n\t"
    "v_smfmac_f32_16x16x64_bf16 v[20:23], v[36:39], v[192:199], v8\n\t"
    "s_waitcnt lgkmcnt(1)\n\t"
    "v_smfmac_f32_16x16x64_bf16 v[20:23], v[40:43], v[208:215], v8\n\t"
    "s_waitcnt lgkmcnt(0)\n\t"
    "v_smfmac_f32_16x16x64_bf16 v[20:23], v[44:47], v[216:223], v8\n\t"
    "s_nop 7\n\t"
    "v_add_f32 v20, v20, v22\n\t"
    "v_add_f32 v21, v21, v23\n\t"
    "v_cvt_pk_bf16_f32 v20, v20, v21\n\t"
    "v_min_u32 v20, -2, v20\n\t"
    "ds_write_b32 v9, v20 offset:18944\n\t"
    "s_waitcnt lgkmcnt(0)\n\t"
    "s_setprio 0\n\t"
    "s_setprio 0\n\t"
    "s_waitcnt lgkmcnt(0)\n\t"
    "s_branch 9f\n\t"
    "1:\n\t"
    "s_add_u32 %[sp], %[sp], 1\n\t"
    "s_cmp_ge_u32 %[sp], %[cap]\n\t"
    "s_cbranch_scc1 7f\n\t"
    "global_load_dwordx4 v[24:27], v6, %[ag] sc1\n\t"
    "global_load_dwordx4 v[28:31], v6, %[ag] offset:-4096 sc1\n\t"
    "s_waitcnt lgkmcnt(0)\n\t"
    "s_memtime %[tc]\n\t"
    "s_nop 7\n\t"
    "s_branch 3b\n\t"
    "7:\n\t"
    "s_mov_b32 %[cap], 0\n\t"
    "s_branch 2b\n\t"
    "9:"
    : [tc] "=&s"(tc), [sp] "=&s"(sp), [cap] "+s"(cap), [st] "=&s"(st)
    : [ag] "s"(ag), [c44] "s"(0x4444u), [lsc] "s"(LSC), [sn] "s"(sn), "{v[64:95]}"(W.w0), "{v[96:127]}"(W.w1), "{v[128:159]}"(W.w2), "{v[160:191]}"(W.w3), "{v[192:223]}"(W.w4), "{v[224:255]}"(W.w5)
    : "v6", "v7", "v8", "v9", "v10", "v11", "v12", "v13", "v14", "v15", "v16", "v17", "v18", "v19", "v20", "v21", "v22", "v23", "v24", "v25", "v26", "v27", "v28", "v29", "v30", "v31", "v32", "v33", "v34", "v35", "v36", "v37", "v38", "v39", "v40", "v41", "v42", "v43", "v44", "v45", "v46", "v47", "v48", "v49", "v50", "v51", "v52", "v53", "v54", "v55", "v56", "v57", "v58", "v59", "v60", "v61", "v62", "v63", "vcc", "scc", "m0", "memory");
}
template <> __device__ __forceinline__ void qk_step<7>(const QKW& W, const char* ag, const char* vs, const char* v2, const char* db, const char* od, unsigned pb, unsigned sn, unsigned& cap, unsigned& sp,
    unsigned long long& tc, unsigned long long* ts, unsigned& d0, unsigned& d1) {
  unsigned st, bg;
  asm volatile("v_mbcnt_lo_u32_b32 v60, -1, 0\n\t"
    "v_mbcnt_hi_u32_b32 v60, -1, v60\n\t"
    "v_lshlrev_b32 v6, 4, v60\n\t"
    "s_mov_b32 %[sp], 0\n\t"
    "s_setprio 3\n\t"
    "global_load_dwordx4 v[24:27], v6, %[ag] sc1\n\t"
    "global_load_dwordx4 v[28:31], v6, %[ag] offset:-4096 sc1\n\t"
    "s_memtime %[tc]\n\t"
    "v_lshrrev_b32 v56, 4, v60\n\t"
    "v_and_b32 v57, 15, v60\n\t"
    "v_mov_b32 v20, 0\n\t"
    "v_mov_b32 v21, 0\n\t"
    "v_mov_b32 v22, 0\n\t"
    "v_mov_b32 v23, 0\n\t"
    "v_mov_b32 v11, %[sn]\n\t"
    "v_mov_b32 v10, -1\n\t"
    "v_and_b32 v61, 2, v60\n\t"
    "v_mul_u32_u24 v61, 0x5555, v61\n\t"
    "v_or_b32 v8, %[c44], v61\n\t"
    "v_lshlrev_b32 v61, 2, v56\n\t"
    "v_lshl_or_b32 v9, v57, 4, v61\n\t"
    "s_setprio 0\n\t"
    "s_setprio 3\n\t"
    "3:\n\t"
    "v_mov_b32 v12, 0\n\t"
    "v_mov_b32 v13, 0\n\t"
    "v_mov_b32 v14, 0\n\t"
    "v_mov_b32 v15, 0\n\t"
    "v_mov_b32 v16, 0\n\t"
    "v_mov_b32 v17, 0\n\t"
    "v_mov_b32 v18, 0\n\t"
    "v_mov_b32 v19, 0\n\t"
    "s_waitcnt vmcnt(0) lgkmcnt(0)\n\t"
    "33:\n\t"
    "ds_write_b128 v6, v[24:27] offset:15360\n\t"
    "ds_write_b128 v6, v[28:31] offset:11264\n\t"
    "v_smfmac_f32_16x16x64_bf16 v[12:15], v[24:27], v[112:119], v8\n\t"
    "v_max3_u32 v60, v24, v25, v26\n\t"
    "v_max3_u32 v61, v27, v28, v29\n\t"
    "v_smfmac_f32_16x16x64_bf16 v[16:19], v[24:27], v[120:127], v8\n\t"
    "v_max3_u32 v60, v60, v30, v31\n\t"
    "v_max_u32 v60, v60, v61\n\t"
    "v_cmp_eq_u32 vcc, -1, v60\n\t"
    "s_cbranch_vccnz 1f\n\t"
    "2:\n\t"
    "s_waitcnt lgkmcnt(0)\n\t"
    "s_barrier\n\t"
    "ds_read_b128 v[32:35], v6 offset:12288\n\t"
    "ds_read_b128 v[36:39], v6 offset:13312\n\t"
    "ds_read_b128 v[40:43], v6 offset:14336\n\t"
    "ds_write_b32 v9, v10 offset:19200\n\t"
    "s_waitcnt lgkmcnt(3)\n\t"
    "v_smfmac_f32_16x16x64_bf16 v[12:15], v[32:35], v[64:71], v8\n\t"
    "v_smfmac_f32_16x16x64_bf16 v[16:19], v[32:35], v[72:79], v8\n\t"
    "s_waitcnt lgkmcnt(2)\n\t"
    "v_smfmac_f32_16x16x64_bf16 v[12:15], v[36:39], v[80:87], v8\n\t"
    "v_smfmac_f32_16x16x64_bf16 v[16:19], v[36:39], v[88:95], v8\n\t"
    "s_waitcnt lgkmcnt(1)\n\t"
    "v_smfmac_f32_16x16x64_bf16 v[12:15], v[40:43], v[96:103], v8\n\t"
    "v_smfmac_f32_16x16x64_bf16 v[16:19], v[40:43], v[104:111], v8\n\t"
    "s_setprio 0\n\t"
    "s_sleep 1\n\t"
    "s_nop 6\n\t"
    "v_add_f32 v12, v12, v14\n\t"
    "v_add_f32 v13, v13, v15\n\t"
    "v_add_f32 v14, v16, v18\n\t"
    "v_add_f32 v15, v17, v19\n\t"
    "ds_write_b128 v6, v[12:15] offset:27648\n\t"
    "ds_write_b32 v9, v11 offset:29440\n\t"
    "s_setprio 0\n\t"
    "v_lshrrev_b32 v10, 24, v11\n\t"
    "v_add_u32 v10, -1, v10\n\t"
    "s_mov_b64 exec, 1\n\t"
    "global_store_dword v6, v10, %[vs] sc1\n\t"
    "s_mov_b64 exec, -1\n\t"
    "ds_read_b128 v[44:47], v6 offset:0\n\t"
    "ds_read_b128 v[48:51], v6 offset:1024\n\t"
    "ds_read_b128 v[52:55], v6 offset:2048\n\t"
    "ds_read_b128 v[56:59], v6 offset:3072\n\t"
    "ds_read_b128 v[60:63], v6 offset:4096\n\t"
    "s_sleep 1\n\t"
    "v_smfmac_f32_16x16x64_bf16 v[20:23], v[28:31], v[216:223], v8\n\t"
    "ds_read_b128 v[28:31], v6 offset:5120\n\t"
    "v_smfmac_f32_16x16x64_bf16 v[20:23], v[24:27], v[248:255], v8\n\t"
    "ds_read_b128 v[24:27], v6 offset:6144\n\t"
    "v_smfmac_f32_16x16x64_bf16 v[20:23], v[32:35], v[224:231], v8\n\t"
    "ds_read_b128 v[32:35], v6 offset:7168\n\t"
    "v_smfmac_f32_16x16x64_bf16 v[20:23], v[36:39], v[240:247], v8\n\t"
    "ds_read_b128 v[36:39], v6 offset:8192\n\t"
    "v_smfmac_f32_16x16x64_bf16 v[20:23], v[40:43], v[232:239], v8\n\t"
    "ds_read_b128 v[40:43], v6 offset:9216\n\t"
    "s_waitcnt lgkmcnt(9)\n\t"
    "v_smfmac_f32_16x16x64_bf16 v[20:23], v[44:47], v[128:135], v8\n\t"
    "ds_read_b128 v[44:47], v6 offset:10240\n\t"
    "s_barrier\n\t"
    "s_setprio 0\n\t"
    "s_waitcnt lgkmcnt(9)\n\t"
    "v_smfmac_f32_16x16x64_bf16 v[20:23], v[48:51], v[144:151], v8\n\t"
    "s_waitcnt lgkmcnt(8)\n\t"
    "v_smfmac_f32_16x16x64_bf16 v[20:23], v[52:55], v[136:143], v8\n\t"
    "s_waitcnt lgkmcnt(7)\n\t"
    "v_smfmac_f32_16x16x64_bf16 v[20:23], v[56:59], v[152:159], v8\n\t"
    "s_waitcnt lgkmcnt(6)\n\t"
    "v_smfmac_f32_16x16x64_bf16 v[20:23], v[60:63], v[160:167], v8\n\t"
    "s_waitcnt lgkmcnt(5)\n\t"
    "v_smfmac_f32_16x16x64_bf16 v[20:23], v[28:31], v[176:183], v8\n\t"
    "s_waitcnt lgkmcnt(4)\n\t"
    "v_smfmac_f32_16x16x64_bf16 v[20:23], v[24:27], v[168:175], v8\n\t"
    "s_waitcnt lgkmcnt(3)\n\t"
    "v_smfmac_f32_16x16x64_bf16 v[20:23], v[32:35], v[184:191], v8\n\t"
    "s_waitcnt lgkmcnt(2)\n\t"
    "v_smfmac_f32_16x16x64_bf16 v[20:23], v[36:39], v[192:199], v8\n\t"
    "s_waitcnt lgkmcnt(1)\n\t"
    "v_smfmac_f32_16x16x64_bf16 v[20:23], v[40:43], v[208:215], v8\n\t"
    "s_waitcnt lgkmcnt(0)\n\t"
    "v_smfmac_f32_16x16x64_bf16 v[20:23], v[44:47], v[200:207], v8\n\t"
    "s_nop 7\n\t"
    "v_add_f32 v20, v20, v22\n\t"
    "v_add_f32 v21, v21, v23\n\t"
    "v_cvt_pk_bf16_f32 v20, v20, v21\n\t"
    "v_min_u32 v20, -2, v20\n\t"
    "ds_write_b32 v9, v20 offset:19200\n\t"
    "s_waitcnt lgkmcnt(0)\n\t"
    "s_setprio 0\n\t"
    "s_waitcnt lgkmcnt(0)\n\t"
    "s_branch 9f\n\t"
    "1:\n\t"
    "s_add_u32 %[sp], %[sp], 1\n\t"
    "s_cmp_ge_u32 %[sp], %[cap]\n\t"
    "s_cbranch_scc1 7f\n\t"
    "global_load_dwordx4 v[24:27], v6, %[ag] sc1\n\t"
    "global_load_dwordx4 v[28:31], v6, %[ag] offset:-4096 sc1\n\t"
    "s_waitcnt lgkmcnt(0)\n\t"
    "s_memtime %[tc]\n\t"
    "s_nop 7\n\t"
    "s_branch 3b\n\t"
    "7:\n\t"
    "s_mov_b32 %[cap], 0\n\t"
    "s_branch 2b\n\t"
    "9:"
    : [tc] "=&s"(tc), [sp] "=&s"(sp), [cap] "+s"(cap), [st] "=&s"(st)
    : [ag] "s"(ag), [c44] "s"(0x4444u), [lsc] "s"(LSC), [sn] "s"(sn), [vs] "s"(vs), "{v[64:95]}"(W.w0), "{v[96:127]}"(W.w1), "{v[128:159]}"(W.w2), "{v[160:191]}"(W.w3), "{v[192:223]}"(W.w4), "{v[224:255]}"(W.w5)
    : "v6", "v7", "v8", "v9", "v10", "v11", "v12", "v13", "v14", "v15", "v16", "v17", "v18", "v19", "v20", "v21", "v22", "v23", "v24", "v25", "v26", "v27", "v28", "v29", "v30", "v31", "v32", "v33", "v34", "v35", "v36", "v37", "v38", "v39", "v40", "v41", "v42", "v43", "v44", "v45", "v46", "v47", "v48", "v49", "v50", "v51", "v52", "v53", "v54", "v55", "v56", "v57", "v58", "v59", "v60", "v61", "v62", "v63", "vcc", "scc", "m0", "memory");
}
template <int W_> __device__ __forceinline__ void seg_attn(const QKW& W, unsigned pb, const char* od, unsigned long long* ts) { }
template <> __device__ __forceinline__ void seg_attn<0>(const QKW& W, unsigned pb, const char* od, unsigned long long* ts) {
  asm volatile("v_mbcnt_lo_u32_b32 v32, -1, 0\n\t"
    "v_mbcnt_hi_u32_b32 v32, -1, v32\n\t"
    "v_lshlrev_b32 v35, 4, v32\n\t"
    "ds_read_b128 v[24:27], v35 offset:20480\n\t"
    "ds_read_b128 v[28:31], v35 offset:21504\n\t"
    "v_lshrrev_b32 v33, 4, v32\n\t"
    "v_and_b32 v34, 15, v32\n\t"
    "v_lshrrev_b32 v4, 2, v34\n\t"
    "v_and_b32 v4, 2, v4\n\t"
    "v_xor_b32 v4, v33, v4\n\t"
    "v_lshl_or_b32 v4, v4, 4, %[pb]\n\t"
    "v_lshl_or_b32 v4, v34, 6, v4\n\t"
    "ds_read_b128 v[8:11], v4 offset:49152\n\t"
    "ds_read_b128 v[12:15], v4 offset:51200\n\t"
    "ds_read_b128 v[16:19], v4 offset:53248\n\t"
    "ds_read_b128 v[20:23], v4 offset:55296\n\t"
    "ds_read_b128 v[40:43], v4 offset:32768\n\t"
    "ds_read_b128 v[44:47], v4 offset:34816\n\t"
    "ds_read_b128 v[48:51], v4 offset:36864\n\t"
    "ds_read_b128 v[52:55], v4 offset:38912\n\t"
    "v_lshlrev_b32 v35, 2, v33\n\t"
    "v_sub_u32 v35, v34, v35\n\t"
    "v_ashrrev_i32 v60, 31, v35\n\t"
    "v_and_b32 v60, %[neg], v60\n\t"
    "v_add_u32 v61, -1, v35\n\t"
    "v_ashrrev_i32 v61, 31, v61\n\t"
    "v_and_b32 v61, %[neg], v61\n\t"
    "v_add_u32 v62, -2, v35\n\t"
    "v_ashrrev_i32 v62, 31, v62\n\t"
    "v_and_b32 v62, %[neg], v62\n\t"
    "v_add_u32 v63, -3, v35\n\t"
    "v_ashrrev_i32 v63, 31, v63\n\t"
    "v_and_b32 v63, %[neg], v63\n\t"
    "s_waitcnt lgkmcnt(6)\n\t"
    "v_mfma_f32_16x16x32_bf16 v[32:35], v[24:27], v[8:11], 0\n\t"
    "v_mfma_f32_16x16x32_bf16 v[36:39], v[24:27], v[12:15], 0\n\t"
    "s_waitcnt lgkmcnt(4)\n\t"
    "v_mfma_f32_16x16x32_bf16 v[32:35], v[28:31], v[16:19], v[32:35]\n\t"
    "v_mfma_f32_16x16x32_bf16 v[36:39], v[28:31], v[20:23], v[36:39]\n\t"
    "s_waitcnt lgkmcnt(3)\n\t"
    "v_mfma_f32_16x16x32_bf16 v[60:63], v[40:43], v[8:11], v[60:63]\n\t"
    "s_waitcnt lgkmcnt(2)\n\t"
    "v_mfma_f32_16x16x32_bf16 v[60:63], v[44:47], v[12:15], v[60:63]\n\t"
    "v_mbcnt_lo_u32_b32 v7, -1, 0\n\t"
    "v_mbcnt_hi_u32_b32 v7, -1, v7\n\t"
    "s_waitcnt lgkmcnt(1)\n\t"
    "v_mfma_f32_16x16x32_bf16 v[60:63], v[48:51], v[16:19], v[60:63]\n\t"
    "v_lshrrev_b32 v5, 4, v7\n\t"
    "v_and_b32 v5, 2, v5\n\t"
    "s_waitcnt lgkmcnt(0)\n\t"
    "v_mfma_f32_16x16x32_bf16 v[60:63], v[52:55], v[20:23], v[60:63]\n\t"
    "v_xor_b32 v5, v5, v7\n\t"
    "v_lshl_or_b32 v5, v5, 4, %[pb]\n\t"
    "ds_read_b64_tr_b16 v[48:49], v5 offset:40960\n\t"
    "ds_read_b64_tr_b16 v[52:53], v5 offset:40968\n\t"
    "v_add_f32 v32, v32, v38\n\t"
    "v_add_f32 v33, v33, v39\n\t"
    "v_and_b32 v6, 12, v7\n\t"
    "v_and_b32 v4, 3, v7\n\t"
    "v_lshl_add_u32 v4, v6, 1, v4\n\t"
    "v_lshrrev_b32 v6, 4, v7\n\t"
    "v_lshl_add_u32 v4, v4, 2, v6\n\t"
    "v_lshlrev_b32 v4, 2, v4\n\t"
    "v_add_u32 v4, 18432, v4\n\t"
    "ds_read_b64_tr_b16 v[40:41], v5 offset:43008\n\t"
    "ds_read_b64_tr_b16 v[44:45], v5 offset:43016\n\t"
    "ds_read2_b32 v[36:37], v4 offset0:0 offset1:16\n\t"
    "ds_read2_b32 v[38:39], v4 offset0:128 offset1:144\n\t"
    "v_and_b32 v4, 7, v7\n\t"
    "v_xor_b32 v6, v6, v4\n\t"
    "v_max3_f32 v34, v32, v33, %[lo]\n\t"
    "v_max3_f32 v34, v34, v60, v61\n\t"
    "v_max3_f32 v34, v34, v62, v63\n\t"
    "v_mov_b32 v35, v34\n\t"
    "v_lshlrev_b32 v6, 4, v6\n\t"
    "v_and_b32 v4, 15, v7\n\t"
    "v_permlane16_swap_b32 v34, v35\n\t"
    "v_max_f32 v34, v34, v35\n\t"
    "v_mov_b32 v35, v34\n\t"
    "v_lshl_or_b32 v6, v4, 7, v6\n\t"
    "v_xor_b32 v4, 64, v6\n\t"
    "v_permlane32_swap_b32 v34, v35\n\t"
    "v_max_f32 v34, v34, v35\n\t"
    "v_mul_f32 v34, %[nl], v34\n\t"
    "v_fma_f32 v32, v32, %[lsc], v34\n\t"
    "v_fma_f32 v33, v33, %[lsc], v34\n\t"
    "v_fma_f32 v60, v60, %[lsc], v34\n\t"
    "v_fma_f32 v61, v61, %[lsc], v34\n\t"
    "v_fma_f32 v62, v62, %[lsc], v34\n\t"
    "v_fma_f32 v63, v63, %[lsc], v34\n\t"
    "v_exp_f32 v60, v60\n\t"
    "v_exp_f32 v61, v61\n\t"
    "v_exp_f32 v62, v62\n\t"
    "v_exp_f32 v63, v63\n\t"
    "v_exp_f32 v32, v32\n\t"
    "v_exp_f32 v33, v33\n\t"
    "v_cvt_pk_bf16_f32 v24, v60, v61\n\t"
    "v_cvt_pk_bf16_f32 v25, v62, v63\n\t"
    "v_cvt_pk_bf16_f32 v28, v32, v33\n\t"
    "s_waitcnt lgkmcnt(0)\n\t"
    "v_mfma_f32_16x16x16_bf16 v[8:11], v[48:49], v[24:25], 0\n\t"
    "v_mov_b32 v56, %[one]\n\t"
    "v_mov_b32 v57, %[one]\n\t"
    "v_mfma_f32_16x16x16_bf16 v[12:15], v[52:53], v[24:25], 0\n\t"
    "v_mov_b32 v58, %[one]\n\t"
    "v_mov_b32 v59, %[one]\n\t"
    "v_mfma_f32_16x16x16_bf16 v[16:19], v[40:41], v[24:25], 0\n\t"
    "v_mov_b32 v29, 0\n\t"
    "v_mov_b32 v30, 0\n\t"
    "v_mfma_f32_16x16x16_bf16 v[60:63], v[56:57], v[24:25], 0\n\t"
    "v_mov_b32 v31, v28\n\t"
    "v_mfma_f32_16x16x16_bf16 v[20:23], v[44:45], v[24:25], 0\n\t"
    "v_mfma_f32_16x16x16_bf16 v[8:11], v[36:37], v[28:29], v[8:11]\n\t"
    "v_mfma_f32_16x16x16_bf16 v[12:15], v[36:37], v[30:31], v[12:15]\n\t"
    "s_nop 1\n\t"
    "v_mfma_f32_16x16x16_bf16 v[16:19], v[38:39], v[28:29], v[16:19]\n\t"
    "v_mfma_f32_16x16x16_bf16 v[60:63], v[56:57], v[28:29], v[60:63]\n\t"
    "s_nop 1\n\t"
    "v_mfma_f32_16x16x16_bf16 v[20:23], v[38:39], v[30:31], v[20:23]\n\t"
    "s_nop 4\n\t"
    "v_rcp_f32 v60, v60\n\t"
    "s_nop 0\n\t"
    "v_pk_mul_f32 v[8:9], v[8:9], v[60:61] op_sel_hi:[1,0]\n\t"
    "v_pk_mul_f32 v[10:11], v[10:11], v[60:61] op_sel_hi:[1,0]\n\t"
    "v_pk_mul_f32 v[12:13], v[12:13], v[60:61] op_sel_hi:[1,0]\n\t"
    "v_pk_mul_f32 v[14:15], v[14:15], v[60:61] op_sel_hi:[1,0]\n\t"
    "v_cvt_pk_bf16_f32 v24, v8, v9\n\t"
    "v_cvt_pk_bf16_f32 v25, v10, v11\n\t"
    "v_cvt_pk_bf16_f32 v26, v12, v13\n\t"
    "v_cvt_pk_bf16_f32 v27, v14, v15\n\t"
    "global_store_dwordx4 v6, v[24:27], %[od] offset:0 sc1\n\t"
    "v_pk_mul_f32 v[16:17], v[16:17], v[60:61] op_sel_hi:[1,0]\n\t"
    "v_pk_mul_f32 v[18:19], v[18:19], v[60:61] op_sel_hi:[1,0]\n\t"
    "v_pk_mul_f32 v[20:21], v[20:21], v[60:61] op_sel_hi:[1,0]\n\t"
    "v_pk_mul_f32 v[22:23], v[22:23], v[60:61] op_sel_hi:[1,0]\n\t"
    "v_cvt_pk_bf16_f32 v28, v16, v17\n\t"
    "v_cvt_pk_bf16_f32 v29, v18, v19\n\t"
    "v_cvt_pk_bf16_f32 v30, v20, v21\n\t"
    "v_cvt_pk_bf16_f32 v31, v22, v23\n\t"
    "global_store_dwordx4 v4, v[28:31], %[od] offset:0 sc1\n\t"
    "s_waitcnt lgkmcnt(0)\n\t"
    "s_nop 0"
    :
    : [pb] "s"(pb), [od] "s"(od), [neg] "s"(-3.0e38f), [lo] "s"(-1.0e38f), [one] "s"(0x3F803F80u), [lsc] "s"(LSC), [nl] "s"(-LSC), "{v[64:95]}"(W.w0), "{v[96:127]}"(W.w1), "{v[128:159]}"(W.w2), "{v[160:191]}"(W.w3), "{v[192:223]}"(W.w4), "{v[224:255]}"(W.w5)
    : "v4", "v5", "v6", "v7", "v8", "v9", "v10", "v11", "v12", "v13", "v14", "v15", "v16", "v17", "v18", "v19", "v20", "v21", "v22", "v23", "v24", "v25", "v26", "v27", "v28", "v29", "v30", "v31", "v32", "v33", "v34", "v35", "v36", "v37", "v38", "v39", "v40", "v41", "v42", "v43", "v44", "v45", "v46", "v47", "v48", "v49", "v50", "v51", "v52", "v53", "v54", "v55", "v56", "v57", "v58", "v59", "v60", "v61", "v62", "v63", "memory");
}
template <> __device__ __forceinline__ void seg_attn<1>(const QKW& W, unsigned pb, const char* od, unsigned long long* ts) {
  asm volatile("v_mbcnt_lo_u32_b32 v32, -1, 0\n\t"
    "v_mbcnt_hi_u32_b32 v32, -1, v32\n\t"
    "v_lshlrev_b32 v35, 4, v32\n\t"
    "ds_read_b128 v[24:27], v35 offset:20480\n\t"
    "ds_read_b128 v[28:31], v35 offset:21504\n\t"
    "v_lshrrev_b32 v33, 4, v32\n\t"
    "v_and_b32 v34, 15, v32\n\t"
    "v_lshrrev_b32 v4, 2, v34\n\t"
    "v_and_b32 v4, 2, v4\n\t"
    "v_xor_b32 v4, v33, v4\n\t"
    "v_lshl_or_b32 v4, v4, 4, %[pb]\n\t"
    "v_lshl_or_b32 v4, v34, 6, v4\n\t"
    "ds_read_b128 v[8:11], v4 offset:50176\n\t"
    "ds_read_b128 v[12:15], v4 offset:52224\n\t"
    "ds_read_b128 v[16:19], v4 offset:54272\n\t"
    "ds_read_b128 v[20:23], v4 offset:56320\n\t"
    "ds_read_b128 v[40:43], v4 offset:32768\n\t"
    "ds_read_b128 v[44:47], v4 offset:34816\n\t"
    "ds_read_b128 v[48:51], v4 offset:36864\n\t"
    "ds_read_b128 v[52:55], v4 offset:38912\n\t"
    "v_lshlrev_b32 v35, 2, v33\n\t"
    "v_sub_u32 v35, v34, v35\n\t"
    "v_ashrrev_i32 v60, 31, v35\n\t"
    "v_and_b32 v60, %[neg], v60\n\t"
    "v_add_u32 v61, -1, v35\n\t"
    "v_ashrrev_i32 v61, 31, v61\n\t"
    "v_and_b32 v61, %[neg], v61\n\t"
    "v_add_u32 v62, -2, v35\n\t"
    "v_ashrrev_i32 v62, 31, v62\n\t"
    "v_and_b32 v62, %[neg], v62\n\t"
    "v_add_u32 v63, -3, v35\n\t"
    "v_ashrrev_i32 v63, 31, v63\n\t"
    "v_and_b32 v63, %[neg], v63\n\t"
    "s_waitcnt lgkmcnt(6)\n\t"
    "v_mfma_f32_16x16x32_bf16 v[32:35], v[24:27], v[8:11], 0\n\t"
    "v_mfma_f32_16x16x32_bf16 v[36:39], v[24:27], v[12:15], 0\n\t"
    "s_waitcnt lgkmcnt(4)\n\t"
    "v_mfma_f32_16x16x32_bf16 v[32:35], v[28:31], v[16:19], v[32:35]\n\t"
    "v_mfma_f32_16x16x32_bf16 v[36:39], v[28:31], v[20:23], v[36:39]\n\t"
    "ds_read_b128 v[24:27], v4 offset:33792\n\t"
    "ds_read_b128 v[28:31], v4 offset:35840\n\t"
    "s_waitcnt lgkmcnt(5)\n\t"
    "v_mfma_f32_16x16x32_bf16 v[56:59], v[40:43], v[8:11], 0\n\t"
    "s_waitcnt lgkmcnt(4)\n\t"
    "v_mfma_f32_16x16x32_bf16 v[56:59], v[44:47], v[12:15], v[56:59]\n\t"
    "ds_read_b128 v[40:43], v4 offset:37888\n\t"
    "ds_read_b128 v[44:47], v4 offset:39936\n\t"
    "v_mbcnt_lo_u32_b32 v7, -1, 0\n\t"
    "v_mbcnt_hi_u32_b32 v7, -1, v7\n\t"
    "s_waitcnt lgkmcnt(5)\n\t"
    "v_mfma_f32_16x16x32_bf16 v[56:59], v[48:51], v[16:19], v[56:59]\n\t"
    "v_lshrrev_b32 v5, 4, v7\n\t"
    "v_and_b32 v5, 2, v5\n\t"
    "s_waitcnt lgkmcnt(4)\n\t"
    "v_mfma_f32_16x16x32_bf16 v[56:59], v[52:55], v[20:23], v[56:59]\n\t"
    "v_xor_b32 v5, v5, v7\n\t"
    "v_lshl_or_b32 v5, v5, 4, %[pb]\n\t"
    "ds_read_b64_tr_b16 v[48:49], v5 offset:40960\n\t"
    "ds_read_b64_tr_b16 v[50:51], v5 offset:41984\n\t"
    "ds_read_b64_tr_b16 v[52:53], v5 offset:40968\n\t"
    "ds_read_b64_tr_b16 v[54:55], v5 offset:41992\n\t"
    "v_add_f32 v32, v32, v38\n\t"
    "v_add_f32 v33, v33, v39\n\t"
    "s_waitcnt lgkmcnt(7)\n\t"
    "v_mfma_f32_16x16x32_bf16 v[60:63], v[24:27], v[8:11], v[60:63]\n\t"
    "v_and_b32 v6, 12, v7\n\t"
    "v_and_b32 v4, 3, v7\n\t"
    "s_waitcnt lgkmcnt(6)\n\t"
    "v_mfma_f32_16x16x32_bf16 v[60:63], v[28:31], v[12:15], v[60:63]\n\t"
    "v_lshl_add_u32 v4, v6, 1, v4\n\t"
    "v_lshrrev_b32 v6, 4, v7\n\t"
    "s_waitcnt lgkmcnt(5)\n\t"
    "v_mfma_f32_16x16x32_bf16 v[60:63], v[40:43], v[16:19], v[60:63]\n\t"
    "v_lshl_add_u32 v4, v4, 2, v6\n\t"
    "v_lshlrev_b32 v4, 2, v4\n\t"
    "s_waitcnt lgkmcnt(4)\n\t"
    "v_mfma_f32_16x16x32_bf16 v[60:63], v[44:47], v[20:23], v[60:63]\n\t"
    "v_add_u32 v4, 18432, v4\n\t"
    "ds_read_b64_tr_b16 v[40:41], v5 offset:43008\n\t"
    "ds_read_b64_tr_b16 v[42:43], v5 offset:44032\n\t"
    "ds_read_b64_tr_b16 v[44:45], v5 offset:43016\n\t"
    "ds_read_b64_tr_b16 v[46:47], v5 offset:44040\n\t"
    "ds_read2_b32 v[36:37], v4 offset0:0 offset1:16\n\t"
    "ds_read2_b32 v[38:39], v4 offset0:128 offset1:144\n\t"
    "v_and_b32 v4, 7, v7\n\t"
    "v_xor_b32 v6, v6, v4\n\t"
    "v_max3_f32 v34, v32, v33, %[lo]\n\t"
    "v_max3_f32 v34, v34, v56, v57\n\t"
    "v_max3_f32 v34, v34, v58, v59\n\t"
    "v_max3_f32 v34, v34, v60, v61\n\t"
    "v_max3_f32 v34, v34, v62, v63\n\t"
    "v_mov_b32 v35, v34\n\t"
    "v_lshlrev_b32 v6, 4, v6\n\t"
    "v_and_b32 v4, 15, v7\n\t"
    "v_permlane16_swap_b32 v34, v35\n\t"
    "v_max_f32 v34, v34, v35\n\t"
    "v_mov_b32 v35, v34\n\t"
    "v_lshl_or_b32 v6, v4, 7, v6\n\t"
    "v_xor_b32 v4, 64, v6\n\t"
    "v_permlane32_swap_b32 v34, v35\n\t"
    "v_max_f32 v34, v34, v35\n\t"
    "v_mul_f32 v34, %[nl], v34\n\t"
    "v_fma_f32 v32, v32, %[lsc], v34\n\t"
    "v_fma_f32 v33, v33, %[lsc], v34\n\t"
    "v_fma_f32 v56, v56, %[lsc], v34\n\t"
    "v_fma_f32 v57, v57, %[lsc], v34\n\t"
    "v_fma_f32 v58, v58, %[lsc], v34\n\t"
    "v_fma_f32 v59, v59, %[lsc], v34\n\t"
    "v_fma_f32 v60, v60, %[lsc], v34\n\t"
    "v_fma_f32 v61, v61, %[lsc], v34\n\t"
    "v_fma_f32 v62, v62, %[lsc], v34\n\t"
    "v_fma_f32 v63, v63, %[lsc], v34\n\t"
    "v_exp_f32 v56, v56\n\t"
    "v_exp_f32 v57, v57\n\t"
    "v_exp_f32 v58, v58\n\t"
    "v_exp_f32 v59, v59\n\t"
    "v_exp_f32 v60, v60\n\t"
    "v_exp_f32 v61, v61\n\t"
    "v_exp_f32 v62, v62\n\t"
    "v_exp_f32 v63, v63\n\t"
    "v_exp_f32 v32, v32\n\t"
    "v_exp_f32 v33, v33\n\t"
    "v_cvt_pk_bf16_f32 v24, v56, v57\n\t"
    "v_cvt_pk_bf16_f32 v25, v58, v59\n\t"
    "v_cvt_pk_bf16_f32 v26, v60, v61\n\t"
    "v_cvt_pk_bf16_f32 v27, v62, v63\n\t"
    "v_cvt_pk_bf16_f32 v28, v32, v33\n\t"
    "s_waitcnt lgkmcnt(0)\n\t"
    "v_mfma_f32_16x16x32_bf16 v[8:11], v[48:51], v[24:27], 0\n\t"
    "v_mov_b32 v56, %[one]\n\t"
    "v_mov_b32 v57, %[one]\n\t"
    "v_mfma_f32_16x16x32_bf16 v[12:15], v[52:55], v[24:27], 0\n\t"
    "v_mov_b32 v58, %[one]\n\t"
    "v_mov_b32 v59, %[one]\n\t"
    "v_mfma_f32_16x16x32_bf16 v[16:19], v[40:43], v[24:27], 0\n\t"
    "v_mov_b32 v29, 0\n\t"
    "v_mov_b32 v30, 0\n\t"
    "v_mfma_f32_16x16x32_bf16 v[60:63], v[56:59], v[24:27], 0\n\t"
    "v_mov_b32 v31, v28\n\t"
    "v_mfma_f32_16x16x32_bf16 v[20:23], v[44:47], v[24:27], 0\n\t"
    "v_mfma_f32_16x16x16_bf16 v[8:11], v[36:37], v[28:29], v[8:11]\n\t"
    "v_mfma_f32_16x16x16_bf16 v[12:15], v[36:37], v[30:31], v[12:15]\n\t"
    "s_nop 1\n\t"
    "v_mfma_f32_16x16x16_bf16 v[16:19], v[38:39], v[28:29], v[16:19]\n\t"
    "v_mfma_f32_16x16x16_bf16 v[60:63], v[56:57], v[28:29], v[60:63]\n\t"
    "s_nop 1\n\t"
    "v_mfma_f32_16x16x16_bf16 v[20:23], v[38:39], v[30:31], v[20:23]\n\t"
    "s_nop 4\n\t"
    "v_rcp_f32 v60, v60\n\t"
    "s_nop 0\n\t"
    "v_pk_mul_f32 v[8:9], v[8:9], v[60:61] op_sel_hi:[1,0]\n\t"
    "v_pk_mul_f32 v[10:11], v[10:11], v[60:61] op_sel_hi:[1,0]\n\t"
    "v_pk_mul_f32 v[12:13], v[12:13], v[60:61] op_sel_hi:[1,0]\n\t"
    "v_pk_mul_f32 v[14:15], v[14:15], v[60:61] op_sel_hi:[1,0]\n\t"
    "v_cvt_pk_bf16_f32 v24, v8, v9\n\t"
    "v_cvt_pk_bf16_f32 v25, v10, v11\n\t"
    "v_cvt_pk_bf16_f32 v26, v12, v13\n\t"
    "v_cvt_pk_bf16_f32 v27, v14, v15\n\t"
    "global_store_dwordx4 v6, v[24:27], %[od] offset:2048 sc1\n\t"
    "v_pk_mul_f32 v[16:17], v[16:17], v[60:61] op_sel_hi:[1,0]\n\t"
    "v_pk_mul_f32 v[18:19], v[18:19], v[60:61] op_sel_hi:[1,0]\n\t"
    "v_pk_mul_f32 v[20:21], v[20:21], v[60:61] op_sel_hi:[1,0]\n\t"
    "v_pk_mul_f32 v[22:23], v[22:23], v[60:61] op_sel_hi:[1,0]\n\t"
    "v_cvt_pk_bf16_f32 v28, v16, v17\n\t"
    "v_cvt_pk_bf16_f32 v29, v18, v19\n\t"
    "v_cvt_pk_bf16_f32 v30, v20, v21\n\t"
    "v_cvt_pk_bf16_f32 v31, v22, v23\n\t"
    "global_store_dwordx4 v4, v[28:31], %[od] offset:2048 sc1\n\t"
    "s_waitcnt lgkmcnt(0)\n\t"
    "s_nop 0"
    :
    : [pb] "s"(pb), [od] "s"(od), [neg] "s"(-3.0e38f), [lo] "s"(-1.0e38f), [one] "s"(0x3F803F80u), [lsc] "s"(LSC), [nl] "s"(-LSC), "{v[64:95]}"(W.w0), "{v[96:127]}"(W.w1), "{v[128:159]}"(W.w2), "{v[160:191]}"(W.w3), "{v[192:223]}"(W.w4), "{v[224:255]}"(W.w5)
    : "v4", "v5", "v6", "v7", "v8", "v9", "v10", "v11", "v12", "v13", "v14", "v15", "v16", "v17", "v18", "v19", "v20", "v21", "v22", "v23", "v24", "v25", "v26", "v27", "v28", "v29", "v30", "v31", "v32", "v33", "v34", "v35", "v36", "v37", "v38", "v39", "v40", "v41", "v42", "v43", "v44", "v45", "v46", "v47", "v48", "v49", "v50", "v51", "v52", "v53", "v54", "v55", "v56", "v57", "v58", "v59", "v60", "v61", "v62", "v63", "memory");
}
template <> __device__ __forceinline__ void seg_attn<2>(const QKW& W, unsigned pb, const char* od, unsigned long long* ts) {
  asm volatile("v_mbcnt_lo_u32_b32 v32, -1, 0\n\t"
    "v_mbcnt_hi_u32_b32 v32, -1, v32\n\t"
    "v_lshlrev_b32 v35, 4, v32\n\t"
    "ds_read_b128 v[24:27], v35 offset:20480\n\t"
    "ds_read_b128 v[28:31], v35 offset:21504\n\t"
    "v_lshrrev_b32 v33, 4, v32\n\t"
    "v_and_b32 v34, 15, v32\n\t"
    "v_lshrrev_b32 v4, 2, v34\n\t"
    "v_and_b32 v4, 2, v4\n\t"
    "v_xor_b32 v4, v33, v4\n\t"
    "v_lshl_or_b32 v4, v4, 4, %[pb]\n\t"
    "v_lshl_or_b32 v4, v34, 6, v4\n\t"
    "ds_read_b128 v[8:11], v4 offset:49152\n\t"
    "ds_read_b128 v[12:15], v4 offset:51200\n\t"
    "ds_read_b128 v[16:19], v4 offset:53248\n\t"
    "ds_read_b128 v[20:23], v4 offset:55296\n\t"
    "ds_read_b128 v[40:43], v4 offset:32768\n\t"
    "ds_read_b128 v[44:47], v4 offset:34816\n\t"
    "ds_read_b128 v[48:51], v4 offset:36864\n\t"
    "ds_read_b128 v[52:55], v4 offset:38912\n\t"
    "v_lshlrev_b32 v35, 2, v33\n\t"
    "v_sub_u32 v35, v34, v35\n\t"
    "v_ashrrev_i32 v60, 31, v35\n\t"
    "v_and_b32 v60, %[neg], v60\n\t"
    "v_add_u32 v61, -1, v35\n\t"
    "v_ashrrev_i32 v61, 31, v61\n\t"
    "v_and_b32 v61, %[neg], v61\n\t"
    "v_add_u32 v62, -2, v35\n\t"
    "v_ashrrev_i32 v62, 31, v62\n\t"
    "v_and_b32 v62, %[neg], v62\n\t"
    "v_add_u32 v63, -3, v35\n\t"
    "v_ashrrev_i32 v63, 31, v63\n\t"
    "v_and_b32 v63, %[neg], v63\n\t"
    "s_waitcnt lgkmcnt(6)\n\t"
    "v_mfma_f32_16x16x32_bf16 v[32:35], v[24:27], v[8:11], 0\n\t"
    "v_mfma_f32_16x16x32_bf16 v[36:39], v[24:27], v[12:15], 0\n\t"
    "s_waitcnt lgkmcnt(4)\n\t"
    "v_mfma_f32_16x16x32_bf16 v[32:35], v[28:31], v[16:19], v[32:35]\n\t"
    "v_mfma_f32_16x16x32_bf16 v[36:39], v[28:31], v[20:23], v[36:39]\n\t"
    "s_waitcnt lgkmcnt(3)\n\t"
    "v_mfma_f32_16x16x32_bf16 v[60:63], v[40:43], v[8:11], v[60:63]\n\t"
    "s_waitcnt lgkmcnt(2)\n\t"
    "v_mfma_f32_16x16x32_bf16 v[60:63], v[44:47], v[12:15], v[60:63]\n\t"
    "v_mbcnt_lo_u32_b32 v7, -1, 0\n\t"
    "v_mbcnt_hi_u32_b32 v7, -1, v7\n\t"
    "s_waitcnt lgkmcnt(1)\n\t"
    "v_mfma_f32_16x16x32_bf16 v[60:63], v[48:51], v[16:19], v[60:63]\n\t"
    "v_lshrrev_b32 v5, 4, v7\n\t"
    "v_and_b32 v5, 2, v5\n\t"
    "s_waitcnt lgkmcnt(0)\n\t"
    "v_mfma_f32_16x16x32_bf16 v[60:63], v[52:55], v[20:23], v[60:63]\n\t"
    "v_xor_b32 v5, v5, v7\n\t"
    "v_lshl_or_b32 v5, v5, 4, %[pb]\n\t"
    "ds_read_b64_tr_b16 v[48:49], v5 offset:45056\n\t"
    "ds_read_b64_tr_b16 v[52:53], v5 offset:45064\n\t"
    "v_add_f32 v32, v32, v38\n\t"
    "v_add_f32 v33, v33, v39\n\t"
    "v_and_b32 v6, 12, v7\n\t"
    "v_and_b32 v4, 3, v7\n\t"
    "v_lshl_add_u32 v4, v6, 1, v4\n\t"
    "v_lshrrev_b32 v6, 4, v7\n\t"
    "v_lshl_add_u32 v4, v4, 2, v6\n\t"
    "v_lshlrev_b32 v4, 2, v4\n\t"
    "v_add_u32 v4, 19456, v4\n\t"
    "ds_read_b64_tr_b16 v[40:41], v5 offset:47104\n\t"
    "ds_read_b64_tr_b16 v[44:45], v5 offset:47112\n\t"
    "ds_read2_b32 v[36:37], v4 offset0:0 offset1:16\n\t"
    "ds_read2_b32 v[38:39], v4 offset0:128 offset1:144\n\t"
    "v_and_b32 v4, 7, v7\n\t"
    "v_xor_b32 v6, v6, v4\n\t"
    "v_max3_f32 v34, v32, v33, %[lo]\n\t"
    "v_max3_f32 v34, v34, v60, v61\n\t"
    "v_max3_f32 v34, v34, v62, v63\n\t"
    "v_mov_b32 v35, v34\n\t"
    "v_lshlrev_b32 v6, 4, v6\n\t"
    "v_and_b32 v4, 15, v7\n\t"
    "v_permlane16_swap_b32 v34, v35\n\t"
    "v_max_f32 v34, v34, v35\n\t"
    "v_mov_b32 v35, v34\n\t"
    "v_lshl_or_b32 v6, v4, 7, v6\n\t"
    "v_xor_b32 v4, 64, v6\n\t"
    "v_permlane32_swap_b32 v34, v35\n\t"
    "v_max_f32 v34, v34, v35\n\t"
    "v_mul_f32 v34, %[nl], v34\n\t"
    "v_fma_f32 v32, v32, %[lsc], v34\n\t"
    "v_fma_f32 v33, v33, %[lsc], v34\n\t"
    "v_fma_f32 v60, v60, %[lsc], v34\n\t"
    "v_fma_f32 v61, v61, %[lsc], v34\n\t"
    "v_fma_f32 v62, v62, %[lsc], v34\n\t"
    "v_fma_f32 v63, v63, %[lsc], v34\n\t"
    "v_exp_f32 v60, v60\n\t"
    "v_exp_f32 v61, v61\n\t"
    "v_exp_f32 v62, v62\n\t"
    "v_exp_f32 v63, v63\n\t"
    "v_exp_f32 v32, v32\n\t"
    "v_exp_f32 v33, v33\n\t"
    "v_cvt_pk_bf16_f32 v24, v60, v61\n\t"
    "v_cvt_pk_bf16_f32 v25, v62, v63\n\t"
    "v_cvt_pk_bf16_f32 v28, v32, v33\n\t"
    "s_waitcnt lgkmcnt(0)\n\t"
    "v_mfma_f32_16x16x16_bf16 v[8:11], v[48:49], v[24:25], 0\n\t"
    "v_mov_b32 v56, %[one]\n\t"
    "v_mov_b32 v57, %[one]\n\t"
    "v_mfma_f32_16x16x16_bf16 v[12:15], v[52:53], v[24:25], 0\n\t"
    "v_mov_b32 v58, %[one]\n\t"
    "v_mov_b32 v59, %[one]\n\t"
    "v_mfma_f32_16x16x16_bf16 v[16:19], v[40:41], v[24:25], 0\n\t"
    "v_mov_b32 v29, 0\n\t"
    "v_mov_b32 v30, 0\n\t"
    "v_mfma_f32_16x16x16_bf16 v[60:63], v[56:57], v[24:25], 0\n\t"
    "v_mov_b32 v31, v28\n\t"
    "v_mfma_f32_16x16x16_bf16 v[20:23], v[44:45], v[24:25], 0\n\t"
    "v_mfma_f32_16x16x16_bf16 v[8:11], v[36:37], v[28:29], v[8:11]\n\t"
    "v_mfma_f32_16x16x16_bf16 v[12:15], v[36:37], v[30:31], v[12:15]\n\t"
    "s_nop 1\n\t"
    "v_mfma_f32_16x16x16_bf16 v[16:19], v[38:39], v[28:29], v[16:19]\n\t"
    "v_mfma_f32_16x16x16_bf16 v[60:63], v[56:57], v[28:29], v[60:63]\n\t"
    "s_nop 1\n\t"
    "v_mfma_f32_16x16x16_bf16 v[20:23], v[38:39], v[30:31], v[20:23]\n\t"
    "s_nop 4\n\t"
    "v_rcp_f32 v60, v60\n\t"
    "s_nop 0\n\t"
    "v_pk_mul_f32 v[8:9], v[8:9], v[60:61] op_sel_hi:[1,0]\n\t"
    "v_pk_mul_f32 v[10:11], v[10:11], v[60:61] op_sel_hi:[1,0]\n\t"
    "v_pk_mul_f32 v[12:13], v[12:13], v[60:61] op_sel_hi:[1,0]\n\t"
    "v_pk_mul_f32 v[14:15], v[14:15], v[60:61] op_sel_hi:[1,0]\n\t"
    "v_cvt_pk_bf16_f32 v24, v8, v9\n\t"
    "v_cvt_pk_bf16_f32 v25, v10, v11\n\t"
    "v_cvt_pk_bf16_f32 v26, v12, v13\n\t"
    "v_cvt_pk_bf16_f32 v27, v14, v15\n\t"
    "global_store_dwordx4 v6, v[24:27], %[od] offset:0 sc1\n\t"
    "v_pk_mul_f32 v[16:17], v[16:17], v[60:61] op_sel_hi:[1,0]\n\t"
    "v_pk_mul_f32 v[18:19], v[18:19], v[60:61] op_sel_hi:[1,0]\n\t"
    "v_pk_mul_f32 v[20:21], v[20:21], v[60:61] op_sel_hi:[1,0]\n\t"
    "v_pk_mul_f32 v[22:23], v[22:23], v[60:61] op_sel_hi:[1,0]\n\t"
    "v_cvt_pk_bf16_f32 v28, v16, v17\n\t"
    "v_cvt_pk_bf16_f32 v29, v18, v19\n\t"
    "v_cvt_pk_bf16_f32 v30, v20, v21\n\t"
    "v_cvt_pk_bf16_f32 v31, v22, v23\n\t"
    "global_store_dwordx4 v4, v[28:31], %[od] offset:0 sc1\n\t"
    "s_waitcnt lgkmcnt(0)\n\t"
    "s_nop 0"
    :
    : [pb] "s"(pb), [od] "s"(od), [neg] "s"(-3.0e38f), [lo] "s"(-1.0e38f), [one] "s"(0x3F803F80u), [lsc] "s"(LSC), [nl] "s"(-LSC), "{v[64:95]}"(W.w0), "{v[96:127]}"(W.w1), "{v[128:159]}"(W.w2), "{v[160:191]}"(W.w3), "{v[192:223]}"(W.w4), "{v[224:255]}"(W.w5)
    : "v4", "v5", "v6", "v7", "v8", "v9", "v10", "v11", "v12", "v13", "v14", "v15", "v16", "v17", "v18", "v19", "v20", "v21", "v22", "v23", "v24", "v25", "v26", "v27", "v28", "v29", "v30", "v31", "v32", "v33", "v34", "v35", "v36", "v37", "v38", "v39", "v40", "v41", "v42", "v43", "v44", "v45", "v46", "v47", "v48", "v49", "v50", "v51", "v52", "v53", "v54", "v55", "v56", "v57", "v58", "v59", "v60", "v61", "v62", "v63", "memory");
}
template <> __device__ __forceinline__ void seg_attn<3>(const QKW& W, unsigned pb, const char* od, unsigned long long* ts) {
  asm volatile("v_mbcnt_lo_u32_b32 v32, -1, 0\n\t"
    "v_mbcnt_hi_u32_b32 v32, -1, v32\n\t"
    "v_lshlrev_b32 v35, 4, v32\n\t"
    "ds_read_b128 v[24:27], v35 offset:20480\n\t"
    "ds_read_b128 v[28:31], v35 offset:21504\n\t"
    "v_lshrrev_b32 v33, 4, v32\n\t"
    "v_and_b32 v34, 15, v32\n\t"
    "v_lshrrev_b32 v4, 2, v34\n\t"
    "v_and_b32 v4, 2, v4\n\t"
    "v_xor_b32 v4, v33, v4\n\t"
    "v_lshl_or_b32 v4, v4, 4, %[pb]\n\t"
    "v_lshl_or_b32 v4, v34, 6, v4\n\t"
    "ds_read_b128 v[8:11], v4 offset:50176\n\t"
    "ds_read_b128 v[12:15], v4 offset:52224\n\t"
    "ds_read_b128 v[16:19], v4 offset:54272\n\t"
    "ds_read_b128 v[20:23], v4 offset:56320\n\t"
    "ds_read_b128 v[40:43], v4 offset:32768\n\t"
    "ds_read_b128 v[44:47], v4 offset:34816\n\t"
    "ds_read_b128 v[48:51], v4 offset:36864\n\t"
    "ds_read_b128 v[52:55], v4 offset:38912\n\t"
    "v_lshlrev_b32 v35, 2, v33\n\t"
    "v_sub_u32 v35, v34, v35\n\t"
    "v_ashrrev_i32 v60, 31, v35\n\t"
    "v_and_b32 v60, %[neg], v60\n\t"
    "v_add_u32 v61, -1, v35\n\t"
    "v_ashrrev_i32 v61, 31, v61\n\t"
    "v_and_b32 v61, %[neg], v61\n\t"
    "v_add_u32 v62, -2, v35\n\t"
    "v_ashrrev_i32 v62, 31, v62\n\t"
    "v_and_b32 v62, %[neg], v62\n\t"
    "v_add_u32 v63, -3, v35\n\t"
    "v_ashrrev_i32 v63, 31, v63\n\t"
    "v_and_b32 v63, %[neg], v63\n\t"
    "s_waitcnt lgkmcnt(6)\n\t"
    "v_mfma_f32_16x16x32_bf16 v[32:35], v[24:27], v[8:11], 0\n\t"
    "v_mfma_f32_16x16x32_bf16 v[36:39], v[24:27], v[12:15], 0\n\t"
    "s_waitcnt lgkmcnt(4)\n\t"
    "v_mfma_f32_16x16x32_bf16 v[32:35], v[28:31], v[16:19], v[32:35]\n\t"
    "v_mfma_f32_16x16x32_bf16 v[36:39], v[28:31], v[20:23], v[36:39]\n\t"
    "ds_read_b128 v[24:27], v4 offset:33792\n\t"
    "ds_read_b128 v[28:31], v4 offset:35840\n\t"
    "s_waitcnt lgkmcnt(5)\n\t"
    "v_mfma_f32_16x16x32_bf16 v[56:59], v[40:43], v[8:11], 0\n\t"
    "s_waitcnt lgkmcnt(4)\n\t"
    "v_mfma_f32_16x16x32_bf16 v[56:59], v[44:47], v[12:15], v[56:59]\n\t"
    "ds_read_b128 v[40:43], v4 offset:37888\n\t"
    "ds_read_b128 v[44:47], v4 offset:39936\n\t"
    "v_mbcnt_lo_u32_b32 v7, -1, 0\n\t"
    "v_mbcnt_hi_u32_b32 v7, -1, v7\n\t"
    "s_waitcnt lgkmcnt(5)\n\t"
    "v_mfma_f32_16x16x32_bf16 v[56:59], v[48:51], v[16:19], v[56:59]\n\t"
    "v_lshrrev_b32 v5, 4, v7\n\t"
    "v_and_b32 v5, 2, v5\n\t"
    "s_waitcnt lgkmcnt(4)\n\t"
    "v_mfma_f32_16x16x32_bf16 v[56:59], v[52:55], v[20:23], v[56:59]\n\t"
    "v_xor_b32 v5, v5, v7\n\t"
    "v_lshl_or_b32 v5, v5, 4, %[pb]\n\t"
    "ds_read_b64_tr_b16 v[48:49], v5 offset:45056\n\t"
    "ds_read_b64_tr_b16 v[50:51], v5 offset:46080\n\t"
    "ds_read_b64_tr_b16 v[52:53], v5 offset:45064\n\t"
    "ds_read_b64_tr_b16 v[54:55], v5 offset:46088\n\t"
    "v_add_f32 v32, v32, v38\n\t"
    "v_add_f32 v33, v33, v39\n\t"
    "s_waitcnt lgkmcnt(7)\n\t"
    "v_mfma_f32_16x16x32_bf16 v[60:63], v[24:27], v[8:11], v[60:63]\n\t"
    "v_and_b32 v6, 12, v7\n\t"
    "v_and_b32 v4, 3, v7\n\t"
    "s_waitcnt lgkmcnt(6)\n\t"
    "v_mfma_f32_16x16x32_bf16 v[60:63], v[28:31], v[12:15], v[60:63]\n\t"
    "v_lshl_add_u32 v4, v6, 1, v4\n\t"
    "v_lshrrev_b32 v6, 4, v7\n\t"
    "s_waitcnt lgkmcnt(5)\n\t"
    "v_mfma_f32_16x16x32_bf16 v[60:63], v[40:43], v[16:19], v[60:63]\n\t"
    "v_lshl_add_u32 v4, v4, 2, v6\n\t"
    "v_lshlrev_b32 v4, 2, v4\n\t"
    "s_waitcnt lgkmcnt(4)\n\t"
    "v_mfma_f32_16x16x32_bf16 v[60:63], v[44:47], v[20:23], v[60:63]\n\t"
    "v_add_u32 v4, 19456, v4\n\t"
    "ds_read_b64_tr_b16 v[40:41], v5 offset:47104\n\t"
    "ds_read_b64_tr_b16 v[42:43], v5 offset:48128\n\t"
    "ds_read_b64_tr_b16 v[44:45], v5 offset:47112\n\t"
    "ds_read_b64_tr_b16 v[46:47], v5 offset:48136\n\t"
    "ds_read2_b32 v[36:37], v4 offset0:0 offset1:16\n\t"
    "ds_read2_b32 v[38:39], v4 offset0:128 offset1:144\n\t"
    "v_and_b32 v4, 7, v7\n\t"
    "v_xor_b32 v6, v6, v4\n\t"
    "v_max3_f32 v34, v32, v33, %[lo]\n\t"
    "v_max3_f32 v34, v34, v56, v57\n\t"
    "v_max3_f32 v34, v34, v58, v59\n\t"
    "v_max3_f32 v34, v34, v60, v61\n\t"
    "v_max3_f32 v34, v34, v62, v63\n\t"
    "v_mov_b32 v35, v34\n\t"
    "v_lshlrev_b32 v6, 4, v6\n\t"
    "v_and_b32 v4, 15, v7\n\t"
    "v_permlane16_swap_b32 v34, v35\n\t"
    "v_max_f32 v34, v34, v35\n\t"
    "v_mov_b32 v35, v34\n\t"
    "v_lshl_or_b32 v6, v4, 7, v6\n\t"
    "v_xor_b32 v4, 64, v6\n\t"
    "v_permlane32_swap_b32 v34, v35\n\t"
    "v_max_f32 v34, v34, v35\n\t"
    "v_mul_f32 v34, %[nl], v34\n\t"
    "v_fma_f32 v32, v32, %[lsc], v34\n\t"
    "v_fma_f32 v33, v33, %[lsc], v34\n\t"
    "v_fma_f32 v56, v56, %[lsc], v34\n\t"
    "v_fma_f32 v57, v57, %[lsc], v34\n\t"
    "v_fma_f32 v58, v58, %[lsc], v34\n\t"
    "v_fma_f32 v59, v59, %[lsc], v34\n\t"
    "v_fma_f32 v60, v60, %[lsc], v34\n\t"
    "v_fma_f32 v61, v61, %[lsc], v34\n\t"
    "v_fma_f32 v62, v62, %[lsc], v34\n\t"
    "v_fma_f32 v63, v63, %[lsc], v34\n\t"
    "v_exp_f32 v56, v56\n\t"
    "v_exp_f32 v57, v57\n\t"
    "v_exp_f32 v58, v58\n\t"
    "v_exp_f32 v59, v59\n\t"
    "v_exp_f32 v60, v60\n\t"
    "v_exp_f32 v61, v61\n\t"
    "v_exp_f32 v62, v62\n\t"
    "v_exp_f32 v63, v63\n\t"
    "v_exp_f32 v32, v32\n\t"
    "v_exp_f32 v33, v33\n\t"
    "v_cvt_pk_bf16_f32 v24, v56, v57\n\t"
    "v_cvt_pk_bf16_f32 v25, v58, v59\n\t"
    "v_cvt_pk_bf16_f32 v26, v60, v61\n\t"
    "v_cvt_pk_bf16_f32 v27, v62, v63\n\t"
    "v_cvt_pk_bf16_f32 v28, v32, v33\n\t"
    "s_waitcnt lgkmcnt(0)\n\t"
    "v_mfma_f32_16x16x32_bf16 v[8:11], v[48:51], v[24:27], 0\n\t"
    "v_mov_b32 v56, %[one]\n\t"
    "v_mov_b32 v57, %[one]\n\t"
    "v_mfma_f32_16x16x32_bf16 v[12:15], v[52:55], v[24:27], 0\n\t"
    "v_mov_b32 v58, %[one]\n\t"
    "v_mov_b32 v59, %[one]\n\t"
    "v_mfma_f32_16x16x32_bf16 v[16:19], v[40:43], v[24:27], 0\n\t"
    "v_mov_b32 v29, 0\n\t"
    "v_mov_b32 v30, 0\n\t"
    "v_mfma_f32_16x16x32_bf16 v[60:63], v[56:59], v[24:27], 0\n\t"
    "v_mov_b32 v31, v28\n\t"
    "v_mfma_f32_16x16x32_bf16 v[20:23], v[44:47], v[24:27], 0\n\t"
    "v_mfma_f32_16x16x16_bf16 v[8:11], v[36:37], v[28:29], v[8:11]\n\t"
    "v_mfma_f32_16x16x16_bf16 v[12:15], v[36:37], v[30:31], v[12:15]\n\t"
    "s_nop 1\n\t"
    "v_mfma_f32_16x16x16_bf16 v[16:19], v[38:39], v[28:29], v[16:19]\n\t"
    "v_mfma_f32_16x16x16_bf16 v[60:63], v[56:57], v[28:29], v[60:63]\n\t"
    "s_nop 1\n\t"
    "v_mfma_f32_16x16x16_bf16 v[20:23], v[38:39], v[30:31], v[20:23]\n\t"
    "s_nop 4\n\t"
    "v_rcp_f32 v60, v60\n\t"
    "s_nop 0\n\t"
    "v_pk_mul_f32 v[8:9], v[8:9], v[60:61] op_sel_hi:[1,0]\n\t"
    "v_pk_mul_f32 v[10:11], v[10:11], v[60:61] op_sel_hi:[1,0]\n\t"
    "v_pk_mul_f32 v[12:13], v[12:13], v[60:61] op_sel_hi:[1,0]\n\t"
    "v_pk_mul_f32 v[14:15], v[14:15], v[60:61] op_sel_hi:[1,0]\n\t"
    "v_cvt_pk_bf16_f32 v24, v8, v9\n\t"
    "v_cvt_pk_bf16_f32 v25, v10, v11\n\t"
    "v_cvt_pk_bf16_f32 v26, v12, v13\n\t"
    "v_cvt_pk_bf16_f32 v27, v14, v15\n\t"
    "global_store_dwordx4 v6, v[24:27], %[od] offset:2048 sc1\n\t"
    "v_pk_mul_f32 v[16:17], v[16:17], v[60:61] op_sel_hi:[1,0]\n\t"
    "v_pk_mul_f32 v[18:19], v[18:19], v[60:61] op_sel_hi:[1,0]\n\t"
    "v_pk_mul_f32 v[20:21], v[20:21], v[60:61] op_sel_hi:[1,0]\n\t"
    "v_pk_mul_f32 v[22:23], v[22:23], v[60:61] op_sel_hi:[1,0]\n\t"
    "v_cvt_pk_bf16_f32 v28, v16, v17\n\t"
    "v_cvt_pk_bf16_f32 v29, v18, v19\n\t"
    "v_cvt_pk_bf16_f32 v30, v20, v21\n\t"
    "v_cvt_pk_bf16_f32 v31, v22, v23\n\t"
    "global_store_dwordx4 v4, v[28:31], %[od] offset:2048 sc1\n\t"
    "s_waitcnt lgkmcnt(0)\n\t"
    "s_nop 0"
    :
    : [pb] "s"(pb), [od] "s"(od), [neg] "s"(-3.0e38f), [lo] "s"(-1.0e38f), [one] "s"(0x3F803F80u), [lsc] "s"(LSC), [nl] "s"(-LSC), "{v[64:95]}"(W.w0), "{v[96:127]}"(W.w1), "{v[128:159]}"(W.w2), "{v[160:191]}"(W.w3), "{v[192:223]}"(W.w4), "{v[224:255]}"(W.w5)
    : "v4", "v5", "v6", "v7", "v8", "v9", "v10", "v11", "v12", "v13", "v14", "v15", "v16", "v17", "v18", "v19", "v20", "v21", "v22", "v23", "v24", "v25", "v26", "v27", "v28", "v29", "v30", "v31", "v32", "v33", "v34", "v35", "v36", "v37", "v38", "v39", "v40", "v41", "v42", "v43", "v44", "v45", "v46", "v47", "v48", "v49", "v50", "v51", "v52", "v53", "v54", "v55", "v56", "v57", "v58", "v59", "v60", "v61", "v62", "v63", "memory");
}
template <int I> __device__ __forceinline__ void qk_wload(u32x32& w, unsigned a0, unsigned a1);
template <> __device__ __forceinline__ void qk_wload<0>(u32x32& w, unsigned a0, unsigned a1) {
  asm volatile("ds_read_b64_tr_b16 v[64:65], %1 offset:0\n\t"
    "ds_read_b64_tr_b16 v[66:67], %1 offset:512\n\t"
    "ds_read_b64_tr_b16 v[68:69], %1 offset:8192\n\t"
    "ds_read_b64_tr_b16 v[70:71], %1 offset:8704\n\t"
    "ds_read_b64_tr_b16 v[72:73], %2 offset:0\n\t"
    "ds_read_b64_tr_b16 v[74:75], %2 offset:512\n\t"
    "ds_read_b64_tr_b16 v[76:77], %2 offset:8192\n\t"
    "ds_read_b64_tr_b16 v[78:79], %2 offset:8704\n\t"
    "s_waitcnt lgkmcnt(0)\n\t"
    "ds_read_b64_tr_b16 v[80:81], %1 offset:16384\n\t"
    "ds_read_b64_tr_b16 v[82:83], %1 offset:16896\n\t"
    "ds_read_b64_tr_b16 v[84:85], %1 offset:24576\n\t"
    "ds_read_b64_tr_b16 v[86:87], %1 offset:25088\n\t"
    "ds_read_b64_tr_b16 v[88:89], %2 offset:16384\n\t"
    "ds_read_b64_tr_b16 v[90:91], %2 offset:16896\n\t"
    "ds_read_b64_tr_b16 v[92:93], %2 offset:24576\n\t"
    "ds_read_b64_tr_b16 v[94:95], %2 offset:25088\n\t"
    "s_waitcnt lgkmcnt(0)"
    : "=&{v[64:95]}"(w) : "v"(a0), "v"(a1) : "memory");
}
template <> __device__ __forceinline__ void qk_wload<1>(u32x32& w, unsigned a0, unsigned a1) {
  asm volatile("ds_read_b64_tr_b16 v[96:97], %1 offset:0\n\t"
    "ds_read_b64_tr_b16 v[98:99], %1 offset:512\n\t"
    "ds_read_b64_tr_b16 v[100:101], %1 offset:8192\n\t"
    "ds_read_b64_tr_b16 v[102:103], %1 offset:8704\n\t"
    "ds_read_b64_tr_b16 v[104:105], %2 offset:0\n\t"
    "ds_read_b64_tr_b16 v[106:107], %2 offset:512\n\t"
    "ds_read_b64_tr_b16 v[108:109], %2 offset:8192\n\t"
    "ds_read_b64_tr_b16 v[110:111], %2 offset:8704\n\t"
    "s_waitcnt lgkmcnt(0)\n\t"
    "ds_read_b64_tr_b16 v[112:113], %1 offset:16384\n\t"
    "ds_read_b64_tr_b16 v[114:115], %1 offset:16896\n\t"
    "ds_read_b64_tr_b16 v[116:117], %1 offset:24576\n\t"
    "ds_read_b64_tr_b16 v[118:119], %1 offset:25088\n\t"
    "ds_read_b64_tr_b16 v[120:121], %2 offset:16384\n\t"
    "ds_read_b64_tr_b16 v[122:123], %2 offset:16896\n\t"
    "ds_read_b64_tr_b16 v[124:125], %2 offset:24576\n\t"
    "ds_read_b64_tr_b16 v[126:127], %2 offset:25088\n\t"
    "s_waitcnt lgkmcnt(0)"
    : "=&{v[96:127]}"(w) : "v"(a0), "v"(a1) : "memory");
}
template <> __device__ __forceinline__ void qk_wload<2>(u32x32& w, unsigned a0, unsigned a1) {
  asm volatile("ds_read_b64_tr_b16 v[128:129], %1 offset:0\n\t"
    "ds_read_b64_tr_b16 v[130:131], %1 offset:512\n\t"
    "ds_read_b64_tr_b16 v[132:133], %1 offset:8192\n\t"
    "ds_read_b64_tr_b16 v[134:135], %1 offset:8704\n\t"
    "ds_read_b64_tr_b16 v[136:137], %2 offset:0\n\t"
    "ds_read_b64_tr_b16 v[138:139], %2 offset:512\n\t"
    "ds_read_b64_tr_b16 v[140:141], %2 offset:8192\n\t"
    "ds_read_b64_tr_b16 v[142:143], %2 offset:8704\n\t"
    "s_waitcnt lgkmcnt(0)\n\t"
    "ds_read_b64_tr_b16 v[144:145], %1 offset:16384\n\t"
    "ds_read_b64_tr_b16 v[146:147], %1 offset:16896\n\t"
    "ds_read_b64_tr_b16 v[148:149], %1 offset:24576\n\t"
    "ds_read_b64_tr_b16 v[150:151], %1 offset:25088\n\t"
    "ds_read_b64_tr_b16 v[152:153], %2 offset:16384\n\t"
    "ds_read_b64_tr_b16 v[154:155], %2 offset:16896\n\t"
    "ds_read_b64_tr_b16 v[156:157], %2 offset:24576\n\t"
    "ds_read_b64_tr_b16 v[158:159], %2 offset:25088\n\t"
    "s_waitcnt lgkmcnt(0)"
    : "=&{v[128:159]}"(w) : "v"(a0), "v"(a1) : "memory");
}
template <> __device__ __forceinline__ void qk_wload<3>(u32x32& w, unsigned a0, unsigned a1) {
  asm volatile("ds_read_b64_tr_b16 v[160:161], %1 offset:0\n\t"
    "ds_read_b64_tr_b16 v[162:163], %1 offset:512\n\t"
    "ds_read_b64_tr_b16 v[164:165], %1 offset:8192\n\t"
    "ds_read_b64_tr_b16 v[166:167], %1 offset:8704\n\t"
    "ds_read_b64_tr_b16 v[168:169], %2 offset:0\n\t"
    "ds_read_b64_tr_b16 v[170:171], %2 offset:512\n\t"
    "ds_read_b64_tr_b16 v[172:173], %2 offset:8192\n\t"
    "ds_read_b64_tr_b16 v[174:175], %2 offset:8704\n\t"
    "s_waitcnt lgkmcnt(0)\n\t"
    "ds_read_b64_tr_b16 v[176:177], %1 offset:16384\n\t"
    "ds_read_b64_tr_b16 v[178:179], %1 offset:16896\n\t"
    "ds_read_b64_tr_b16 v[180:181], %1 offset:24576\n\t"
    "ds_read_b64_tr_b16 v[182:183], %1 offset:25088\n\t"
    "ds_read_b64_tr_b16 v[184:185], %2 offset:16384\n\t"
    "ds_read_b64_tr_b16 v[186:187], %2 offset:16896\n\t"
    "ds_read_b64_tr_b16 v[188:189], %2 offset:24576\n\t"
    "ds_read_b64_tr_b16 v[190:191], %2 offset:25088\n\t"
    "s_waitcnt lgkmcnt(0)"
    : "=&{v[160:191]}"(w) : "v"(a0), "v"(a1) : "memory");
}
template <> __device__ __forceinline__ void qk_wload<4>(u32x32& w, unsigned a0, unsigned a1) {
  asm volatile("ds_read_b64_tr_b16 v[192:193], %1 offset:0\n\t"
    "ds_read_b64_tr_b16 v[194:195], %1 offset:512\n\t"
    "ds_read_b64_tr_b16 v[196:197], %1 offset:8192\n\t"
    "ds_read_b64_tr_b16 v[198:199], %1 offset:8704\n\t"
    "ds_read_b64_tr_b16 v[200:201], %2 offset:0\n\t"
    "ds_read_b64_tr_b16 v[202:203], %2 offset:512\n\t"
    "ds_read_b64_tr_b16 v[204:205], %2 offset:8192\n\t"
    "ds_read_b64_tr_b16 v[206:207], %2 offset:8704\n\t"
    "s_waitcnt lgkmcnt(0)\n\t"
    "ds_read_b64_tr_b16 v[208:209], %1 offset:16384\n\t"
    "ds_read_b64_tr_b16 v[210:211], %1 offset:16896\n\t"
    "ds_read_b64_tr_b16 v[212:213], %1 offset:24576\n\t"
    "ds_read_b64_tr_b16 v[214:215], %1 offset:25088\n\t"
    "ds_read_b64_tr_b16 v[216:217], %2 offset:16384\n\t"
    "ds_read_b64_tr_b16 v[218:219], %2 offset:16896\n\t"
    "ds_read_b64_tr_b16 v[220:221], %2 offset:24576\n\t"
    "ds_read_b64_tr_b16 v[222:223], %2 offset:25088\n\t"
    "s_waitcnt lgkmcnt(0)"
    : "=&{v[192:223]}"(w) : "v"(a0), "v"(a1) : "memory");
}
template <> __device__ __forceinline__ void qk_wload<5>(u32x32& w, unsigned a0, unsigned a1) {
  asm volatile("ds_read_b64_tr_b16 v[224:225], %1 offset:0\n\t"
    "ds_read_b64_tr_b16 v[226:227], %1 offset:512\n\t"
    "ds_read_b64_tr_b16 v[228:229], %1 offset:8192\n\t"
    "ds_read_b64_tr_b16 v[230:231], %1 offset:8704\n\t"
    "ds_read_b64_tr_b16 v[232:233], %2 offset:0\n\t"
    "ds_read_b64_tr_b16 v[234:235], %2 offset:512\n\t"
    "ds_read_b64_tr_b16 v[236:237], %2 offset:8192\n\t"
    "ds_read_b64_tr_b16 v[238:239], %2 offset:8704\n\t"
    "s_waitcnt lgkmcnt(0)\n\t"
    "ds_read_b64_tr_b16 v[240:241], %1 offset:16384\n\t"
    "ds_read_b64_tr_b16 v[242:243], %1 offset:16896\n\t"
    "ds_read_b64_tr_b16 v[244:245], %1 offset:24576\n\t"
    "ds_read_b64_tr_b16 v[246:247], %1 offset:25088\n\t"
    "ds_read_b64_tr_b16 v[248:249], %2 offset:16384\n\t"
    "ds_read_b64_tr_b16 v[250:251], %2 offset:16896\n\t"
    "ds_read_b64_tr_b16 v[252:253], %2 offset:24576\n\t"
    "ds_read_b64_tr_b16 v[254:255], %2 offset:25088\n\t"
    "s_waitcnt lgkmcnt(0)"
    : "=&{v[224:255]}"(w) : "v"(a0), "v"(a1) : "memory");
}
#define OREP 2
#define AREP 2
#define BELL1 0
#define RINGA 1
constexpr int V_P1 = 131072, V_PV = 149632, V_P2 = 0;
struct VRW { u32x32 k0, k1, o0, o1, o2, o3; };
__device__ __forceinline__ void vr_v(const VRW& W, const char* ag, const char* kd, const char* vd, unsigned wl, unsigned wa, unsigned wb, unsigned ra, unsigned rb, unsigned& cap, unsigned& sp, unsigned long long& tc, unsigned long long* ts) {
  asm volatile("s_mov_b32 %[sp], 0\n\t"
    "v_mbcnt_lo_u32_b32 v20, -1, 0\n\t"
    "v_mbcnt_hi_u32_b32 v20, -1, v20\n\t"
    "v_lshlrev_b32 v21, 4, v20\n\t"
    "s_nop 0\n\t"
    "global_load_dwordx4 v[24:27], v21, %[ag] sc1\n\t"
    "global_load_dwordx4 v[28:31], v21, %[ag] offset:1024 sc1\n\t"
    "s_memtime %[tc]\n\t"
    "v_lshl_add_u32 v6, v20, 4, %[wl]\n\t"
    "ds_read_b128 v[32:35], v6 offset:0\n\t"
    "ds_read_b128 v[36:39], v6 offset:1024\n\t"
    "ds_read_b128 v[40:43], v6 offset:2048\n\t"
    "ds_read_b128 v[44:47], v6 offset:3072\n\t"
    "ds_read_b128 v[48:51], v6 offset:4096\n\t"
    "ds_read_b128 v[52:55], v6 offset:5120\n\t"
    "ds_read_b128 v[56:59], v6 offset:6144\n\t"
    "ds_read_b128 v[60:63], v6 offset:7168\n\t"
    "s_setprio 3\n\t"
    "v_and_b32 v6, 2, v20\n\t"
    "v_mul_u32_u24 v6, 0x5555, v6\n\t"
    "v_or_b32 v4, %[c44], v6\n\t"
    "v_and_b32 v6, 15, v20\n\t"
    "v_lshl_add_u32 v5, v6, 2, %[wa]\n\t"
    "v_and_b32 v6, 48, v20\n\t"
    "v_mad_u32_u24 v5, v6, 36, v5\n\t"
    "v_and_b32 v6, 32, v20\n\t"
    "v_mul_u32_u24 v7, 18, v6\n\t"
    "v_and_b32 v6, 4, v20\n\t"
    "v_mul_u32_u24 v6, 72, v6\n\t"
    "v_add_u32 v7, v7, v6\n\t"
    "v_and_b32 v6, 16, v20\n\t"
    "v_lshl_add_u32 v7, v6, 3, v7\n\t"
    "v_and_b32 v6, 3, v20\n\t"
    "v_lshl_add_u32 v7, v6, 3, v7\n\t"
    "v_and_b32 v6, 8, v20\n\t"
    "v_mul_u32_u24 v6, 1160, v6\n\t"
    "v_add3_u32 v7, v7, v6, %[ra]\n\t"
    "3:\n\t"
    "v_mov_b32 v8, 0\n\t"
    "v_mov_b32 v9, 0\n\t"
    "v_mov_b32 v10, 0\n\t"
    "v_mov_b32 v11, 0\n\t"
    "v_mov_b32 v12, 0\n\t"
    "v_mov_b32 v13, 0\n\t"
    "v_mov_b32 v14, 0\n\t"
    "v_mov_b32 v15, 0\n\t"
    "v_mov_b32 v16, 0\n\t"
    "v_mov_b32 v17, 0\n\t"
    "v_mov_b32 v18, 0\n\t"
    "v_mov_b32 v19, 0\n\t"
    "v_mov_b32 v20, 0\n\t"
    "v_mov_b32 v21, 0\n\t"
    "v_mov_b32 v22, 0\n\t"
    "v_mov_b32 v23, 0\n\t"
    "s_waitcnt vmcnt(0) lgkmcnt(0)\n\t"
    "v_smfmac_f32_16x16x64_bf16 v[8:11], v[24:27], v[64:71], v4\n\t"
    "v_smfmac_f32_16x16x64_bf16 v[8:11], v[28:31], v[72:79], v4\n\t"
    "v_max3_u32 v6, v24, v25, v26\n\t"
    "v_max3_u32 v6, v6, v27, v28\n\t"
    "v_smfmac_f32_16x16x64_bf16 v[12:15], v[24:27], v[80:87], v4\n\t"
    "v_max3_u32 v6, v6, v29, v30\n\t"
    "v_max_u32 v6, v6, v31\n\t"
    "v_smfmac_f32_16x16x64_bf16 v[12:15], v[28:31], v[88:95], v4\n\t"
    "v_cmp_eq_u32 vcc, -1, v6\n\t"
    "s_cbranch_vccnz 1f\n\t"
    "2:\n\t"
    "v_smfmac_f32_16x16x64_bf16 v[16:19], v[24:27], v[96:103], v4\n\t"
    "v_smfmac_f32_16x16x64_bf16 v[16:19], v[28:31], v[104:111], v4\n\t"
    "v_add_f32 v8, v8, v10\n\t"
    "v_add_f32 v9, v9, v11\n\t"
    "ds_write2_b32 v5, v8, v9 offset0:0 offset1:72\n\t"
    "v_smfmac_f32_16x16x64_bf16 v[20:23], v[24:27], v[112:119], v4\n\t"
    "v_smfmac_f32_16x16x64_bf16 v[20:23], v[28:31], v[120:127], v4\n\t"
    "v_add_f32 v12, v12, v14\n\t"
    "v_add_f32 v13, v13, v15\n\t"
    "ds_write2_b32 v5, v12, v13 offset0:16 offset1:88\n\t"
    "v_add_f32 v16, v16, v18\n\t"
    "v_add_f32 v17, v17, v19\n\t"
    "ds_write2_b32 v5, v16, v17 offset0:32 offset1:104\n\t"
    "s_nop 1\n\t"
    "v_add_f32 v20, v20, v22\n\t"
    "v_add_f32 v21, v21, v23\n\t"
    "ds_write2_b32 v5, v20, v21 offset0:48 offset1:120\n\t"
    "v_mov_b32 v8, 0\n\t"
    "v_mov_b32 v9, 0\n\t"
    "v_mov_b32 v10, 0\n\t"
    "v_mov_b32 v11, 0\n\t"
    "v_mov_b32 v12, 0\n\t"
    "v_mov_b32 v13, 0\n\t"
    "v_mov_b32 v14, 0\n\t"
    "v_mov_b32 v15, 0\n\t"
    "v_mbcnt_lo_u32_b32 v5, -1, 0\n\t"
    "v_mbcnt_hi_u32_b32 v5, -1, v5\n\t"
    "s_waitcnt lgkmcnt(0)\n\t"
    "s_barrier\n\t"
    "ds_read_b64 v[16:17], v7 offset:0\n\t"
    "ds_read_b64 v[18:19], v7 offset:2304\n\t"
    "ds_read_b64 v[20:21], v7 offset:4608\n\t"
    "ds_read_b64 v[22:23], v7 offset:6912\n\t"
    "v_smfmac_f32_16x16x64_bf16 v[8:11], v[24:27], v[32:39], v4\n\t"
    "v_smfmac_f32_16x16x64_bf16 v[8:11], v[28:31], v[40:47], v4\n\t"
    "v_smfmac_f32_16x16x64_bf16 v[12:15], v[24:27], v[48:55], v4\n\t"
    "v_smfmac_f32_16x16x64_bf16 v[12:15], v[28:31], v[56:63], v4\n\t"
    "v_and_b32 v6, 48, v5\n\t"
    "v_and_b32 v7, 7, v5\n\t"
    "v_lshlrev_b32 v7, 2, v7\n\t"
    "v_lshl_or_b32 v7, v6, 1, v7\n\t"
    "s_waitcnt lgkmcnt(2)\n\t"
    "v_pk_add_f32 v[16:17], v[16:17], v[18:19]\n\t"
    "s_waitcnt lgkmcnt(0)\n\t"
    "v_pk_add_f32 v[20:21], v[20:21], v[22:23]\n\t"
    "v_pk_add_f32 v[16:17], v[16:17], v[20:21]\n\t"
    "s_nop 1\n\t"
    "v_add_f32_dpp v16, v16, v16 row_ror:8 row_mask:0xf bank_mask:0xf\n\t"
    "v_add_f32_dpp v17, v17, v17 row_ror:8 row_mask:0xf bank_mask:0xf\n\t"
    "v_cvt_pk_bf16_f32 v6, v16, v17\n\t"
    "v_min_u32 v6, -2, v6\n\t"
    "s_mov_b64 exec, %[em]\n\t"
    "global_store_dword v7, v6, %[kd] sc1\n\t"
    "s_mov_b64 exec, -1\n\t"
    "v_lshl_add_u32 v16, v5, 3, %[wb]\n\t"
    "v_add_f32 v8, v8, v10\n\t"
    "v_add_f32 v9, v9, v11\n\t"
    "v_add_f32 v12, v12, v14\n\t"
    "v_add_f32 v13, v13, v15\n\t"
    "ds_write2st64_b64 v16, v[8:9], v[12:13] offset1:1\n\t"
    "v_and_b32 v6, 8, v5\n\t"
    "v_mul_u32_u24 v6, 528, v6\n\t"
    "v_lshl_add_u32 v5, v7, 1, v6\n\t"
    "v_add_u32 v5, %[rb], v5\n\t"
    "s_cmp_ge_u32 %[wb], 153728\n\t"
    "s_waitcnt lgkmcnt(0)\n\t"
    "s_barrier\n\t"
    "s_cbranch_scc1 8f\n\t"
    "ds_read_b64 v[16:17], v5 offset:0\n\t"
    "ds_read_b64 v[18:19], v5 offset:1024\n\t"
    "ds_read_b64 v[20:21], v5 offset:2048\n\t"
    "ds_read_b64 v[22:23], v5 offset:3072\n\t"
    "s_waitcnt lgkmcnt(2)\n\t"
    "v_pk_add_f32 v[16:17], v[16:17], v[18:19]\n\t"
    "s_waitcnt lgkmcnt(0)\n\t"
    "v_pk_add_f32 v[20:21], v[20:21], v[22:23]\n\t"
    "v_pk_add_f32 v[16:17], v[16:17], v[20:21]\n\t"
    "s_nop 1\n\t"
    "v_add_f32_dpp v16, v16, v16 row_ror:8 row_mask:0xf bank_mask:0xf\n\t"
    "v_add_f32_dpp v17, v17, v17 row_ror:8 row_mask:0xf bank_mask:0xf\n\t"
    "v_cvt_pk_bf16_f32 v6, v16, v17\n\t"
    "v_min_u32 v6, -2, v6\n\t"
    "s_mov_b64 exec, %[em]\n\t"
    "global_store_dword v7, v6, %[vd] sc1\n\t"
    "s_mov_b64 exec, -1\n\t"
    "8:\n\t"
    "s_setprio 2\n\t"
    "s_waitcnt lgkmcnt(0)\n\t"
    "s_branch 9f\n\t"
    "1:\n\t"
    "s_add_u32 %[sp], %[sp], 1\n\t"
    "s_cmp_ge_u32 %[sp], %[cap]\n\t"
    "s_cbranch_scc1 7f\n\t"
    "v_mbcnt_lo_u32_b32 v20, -1, 0\n\t"
    "v_mbcnt_hi_u32_b32 v20, -1, v20\n\t"
    "v_lshlrev_b32 v21, 4, v20\n\t"
    "s_nop 0\n\t"
    "global_load_dwordx4 v[24:27], v21, %[ag] sc1\n\t"
    "global_load_dwordx4 v[28:31], v21, %[ag] offset:1024 sc1\n\t"
    "s_waitcnt lgkmcnt(0)\n\t"
    "s_memtime %[tc]\n\t"
    "s_nop 7\n\t"
    "s_branch 3b\n\t"
    "7:\n\t"
    "s_mov_b32 %[cap], 0\n\t"
    "s_branch 2b\n\t"
    "9:"
    : [tc] "=&s"(tc), [sp] "=&s"(sp), [cap] "+s"(cap)
    : [ag] "s"(ag), [kd] "s"(kd), [vd] "s"(vd), [wl] "s"(wl), [wa] "s"(wa), [wb] "s"(wb), [ra] "s"(ra), [rb] "s"(rb), [c44] "s"(0x4444u), [em] "s"(0x00FF00FF00FF00FFull), "{v[64:95]}"(W.k0), "{v[96:127]}"(W.k1), "{v[128:159]}"(W.o0), "{v[160:191]}"(W.o1), "{v[192:223]}"(W.o2), "{v[224:255]}"(W.o3)
    : "v4", "v5", "v6", "v7", "v8", "v9", "v10", "v11", "v12", "v13", "v14", "v15", "v16", "v17", "v18", "v19", "v20", "v21", "v22", "v23", "v24", "v25", "v26", "v27", "v28", "v29", "v30", "v31", "v32", "v33", "v34", "v35", "v36", "v37", "v38", "v39", "v40", "v41", "v42", "v43", "v44", "v45", "v46", "v47", "v48", "v49", "v50", "v51", "v52", "v53", "v54", "v55", "v56", "v57", "v58", "v59", "v60", "v61", "v62", "v63", "vcc", "scc", "memory");
}
__device__ __forceinline__ void vr_w0(const VRW& W, const char* ag, const char* vd, unsigned wl, unsigned wa, unsigned ra, unsigned& cap, unsigned& sp, unsigned long long& tc, unsigned long long* ts) {
  unsigned st;
  asm volatile("s_mov_b32 %[sp], 0\n\t"
    "v_mbcnt_lo_u32_b32 v20, -1, 0\n\t"
    "v_mbcnt_hi_u32_b32 v20, -1, v20\n\t"
    "v_lshlrev_b32 v21, 4, v20\n\t"
    "s_nop 0\n\t"
    "global_load_dwordx4 v[24:27], v21, %[ag] sc1\n\t"
    "global_load_dwordx4 v[28:31], v21, %[ag] offset:1024 sc1\n\t"
    "s_memtime %[tc]\n\t"
    "v_lshl_add_u32 v6, v20, 4, %[wl]\n\t"
    "ds_read_b128 v[32:35], v6 offset:0\n\t"
    "ds_read_b128 v[36:39], v6 offset:1024\n\t"
    "ds_read_b128 v[40:43], v6 offset:2048\n\t"
    "ds_read_b128 v[44:47], v6 offset:3072\n\t"
    "ds_read_b128 v[48:51], v6 offset:4096\n\t"
    "ds_read_b128 v[52:55], v6 offset:5120\n\t"
    "ds_read_b128 v[56:59], v6 offset:6144\n\t"
    "ds_read_b128 v[60:63], v6 offset:7168\n\t"
    "s_setprio 3\n\t"
    "v_and_b32 v6, 2, v20\n\t"
    "v_mul_u32_u24 v6, 0x5555, v6\n\t"
    "v_or_b32 v4, %[c44], v6\n\t"
    "v_and_b32 v6, 15, v20\n\t"
    "v_lshl_add_u32 v5, v6, 2, %[wa]\n\t"
    "v_and_b32 v6, 48, v20\n\t"
    "v_mad_u32_u24 v5, v6, 52, v5\n\t"
    "v_mul_u32_u24 v7, 52, v6\n\t"
    "v_and_b32 v6, 4, v20\n\t"
    "v_mul_u32_u24 v6, 104, v6\n\t"
    "v_add_u32 v7, v7, v6\n\t"
    "v_and_b32 v6, 8, v20\n\t"
    "v_lshl_add_u32 v7, v6, 4, v7\n\t"
    "v_and_b32 v6, 3, v20\n\t"
    "v_lshl_add_u32 v7, v6, 3, v7\n\t"
    "v_add_u32 v7, %[ra], v7\n\t"
    "3:\n\t"
    "v_mov_b32 v8, 0\n\t"
    "v_mov_b32 v9, 0\n\t"
    "v_mov_b32 v10, 0\n\t"
    "v_mov_b32 v11, 0\n\t"
    "v_mov_b32 v12, 0\n\t"
    "v_mov_b32 v13, 0\n\t"
    "v_mov_b32 v14, 0\n\t"
    "v_mov_b32 v15, 0\n\t"
    "v_mov_b32 v16, 0\n\t"
    "v_mov_b32 v17, 0\n\t"
    "v_mov_b32 v18, 0\n\t"
    "v_mov_b32 v19, 0\n\t"
    "v_mov_b32 v20, 0\n\t"
    "v_mov_b32 v21, 0\n\t"
    "v_mov_b32 v22, 0\n\t"
    "v_mov_b32 v23, 0\n\t"
    "s_waitcnt vmcnt(0) lgkmcnt(0)\n\t"
    "v_smfmac_f32_16x16x64_bf16 v[8:11], v[24:27], v[32:39], v4\n\t"
    "v_smfmac_f32_16x16x64_bf16 v[8:11], v[28:31], v[40:47], v4\n\t"
    "v_mov_b32 v32, 0\n\t"
    "v_mov_b32 v33, 0\n\t"
    "v_smfmac_f32_16x16x64_bf16 v[12:15], v[24:27], v[48:55], v4\n\t"
    "v_mov_b32 v34, 0\n\t"
    "v_mov_b32 v35, 0\n\t"
    "v_smfmac_f32_16x16x64_bf16 v[12:15], v[28:31], v[56:63], v4\n\t"
    "v_mov_b32 v36, 0\n\t"
    "v_mov_b32 v37, 0\n\t"
    "v_smfmac_f32_16x16x64_bf16 v[16:19], v[24:27], v[64:71], v4\n\t"
    "v_mov_b32 v38, 0\n\t"
    "v_mov_b32 v39, 0\n\t"
    "v_smfmac_f32_16x16x64_bf16 v[16:19], v[28:31], v[72:79], v4\n\t"
    "v_add_f32 v8, v8, v10\n\t"
    "v_add_f32 v9, v9, v11\n\t"
    "ds_write2_b32 v5, v8, v9 offset0:64 offset1:168\n\t"
    "v_smfmac_f32_16x16x64_bf16 v[20:23], v[24:27], v[80:87], v4\n\t"
    "v_smfmac_f32_16x16x64_bf16 v[20:23], v[28:31], v[88:95], v4\n\t"
    "v_add_f32 v12, v12, v14\n\t"
    "v_add_f32 v13, v13, v15\n\t"
    "ds_write2_b32 v5, v12, v13 offset0:80 offset1:184\n\t"
    "v_smfmac_f32_16x16x64_bf16 v[32:35], v[24:27], v[96:103], v4\n\t"
    "v_smfmac_f32_16x16x64_bf16 v[32:35], v[28:31], v[104:111], v4\n\t"
    "v_add_f32 v16, v16, v18\n\t"
    "v_add_f32 v17, v17, v19\n\t"
    "ds_write2_b32 v5, v16, v17 offset0:0 offset1:104\n\t"
    "v_smfmac_f32_16x16x64_bf16 v[36:39], v[24:27], v[112:119], v4\n\t"
    "v_smfmac_f32_16x16x64_bf16 v[36:39], v[28:31], v[120:127], v4\n\t"
    "v_max3_u32 v6, v24, v25, v26\n\t"
    "v_add_f32 v20, v20, v22\n\t"
    "v_add_f32 v21, v21, v23\n\t"
    "ds_write2_b32 v5, v20, v21 offset0:16 offset1:120\n\t"
    "v_add_f32 v32, v32, v34\n\t"
    "v_add_f32 v33, v33, v35\n\t"
    "ds_write2_b32 v5, v32, v33 offset0:32 offset1:136\n\t"
    "v_max3_u32 v6, v6, v27, v28\n\t"
    "v_add_f32 v36, v36, v38\n\t"
    "v_add_f32 v37, v37, v39\n\t"
    "ds_write2_b32 v5, v36, v37 offset0:48 offset1:152\n\t"
    "v_max3_u32 v6, v6, v29, v30\n\t"
    "v_max_u32 v6, v6, v31\n\t"
    "v_cmp_eq_u32 vcc, -1, v6\n\t"
    "v_mbcnt_lo_u32_b32 v48, -1, 0\n\t"
    "v_mbcnt_hi_u32_b32 v48, -1, v48\n\t"
    "v_lshlrev_b32 v48, 2, v48\n\t"
    "s_cbranch_vccnz 1f\n\t"
    "2:\n\t"
    "s_waitcnt lgkmcnt(0)\n\t"
    "s_barrier\n\t"
    "ds_read_b64 v[8:9], v7 offset:0\n\t"
    "ds_read_b64 v[10:11], v7 offset:3328\n\t"
    "ds_read_b64 v[12:13], v7 offset:6656\n\t"
    "ds_read_b64 v[14:15], v7 offset:9984\n\t"
    "ds_read_b64 v[16:17], v7 offset:13376\n\t"
    "ds_read_b64 v[18:19], v7 offset:16704\n\t"
    "ds_read_b64 v[20:21], v7 offset:20032\n\t"
    "ds_read_b64 v[22:23], v7 offset:23360\n\t"
    "s_waitcnt lgkmcnt(6)\n\t"
    "v_pk_add_f32 v[8:9], v[8:9], v[10:11]\n\t"
    "s_waitcnt lgkmcnt(4)\n\t"
    "v_pk_add_f32 v[12:13], v[12:13], v[14:15]\n\t"
    "v_pk_add_f32 v[8:9], v[8:9], v[12:13]\n\t"
    "s_waitcnt lgkmcnt(2)\n\t"
    "v_pk_add_f32 v[16:17], v[16:17], v[18:19]\n\t"
    "s_waitcnt lgkmcnt(0)\n\t"
    "v_pk_add_f32 v[20:21], v[20:21], v[22:23]\n\t"
    "v_pk_add_f32 v[16:17], v[16:17], v[20:21]\n\t"
    "v_pk_add_f32 v[8:9], v[8:9], v[16:17]\n\t"
    "v_cvt_pk_bf16_f32 v6, v8, v9\n\t"
    "v_min_u32 v6, -2, v6\n\t"
    "global_store_dword v48, v6, %[vd] sc1\n\t"
    "s_setprio 2\n\t"
    "s_waitcnt lgkmcnt(0)\n\t"
    "s_branch 9f\n\t"
    "1:\n\t"
    "s_add_u32 %[sp], %[sp], 1\n\t"
    "s_cmp_ge_u32 %[sp], %[cap]\n\t"
    "s_cbranch_scc1 7f\n\t"
    "s_waitcnt lgkmcnt(0)\n\t"
    "v_mbcnt_lo_u32_b32 v20, -1, 0\n\t"
    "v_mbcnt_hi_u32_b32 v20, -1, v20\n\t"
    "v_lshlrev_b32 v21, 4, v20\n\t"
    "s_nop 0\n\t"
    "global_load_dwordx4 v[24:27], v21, %[ag] sc1\n\t"
    "global_load_dwordx4 v[28:31], v21, %[ag] offset:1024 sc1\n\t"
    "s_memtime %[tc]\n\t"
    "v_lshl_add_u32 v6, v20, 4, %[wl]\n\t"
    "ds_read_b128 v[32:35], v6 offset:0\n\t"
    "ds_read_b128 v[36:39], v6 offset:1024\n\t"
    "ds_read_b128 v[40:43], v6 offset:2048\n\t"
    "ds_read_b128 v[44:47], v6 offset:3072\n\t"
    "ds_read_b128 v[48:51], v6 offset:4096\n\t"
    "ds_read_b128 v[52:55], v6 offset:5120\n\t"
    "ds_read_b128 v[56:59], v6 offset:6144\n\t"
    "ds_read_b128 v[60:63], v6 offset:7168\n\t"
    "s_branch 3b\n\t"
    "7:\n\t"
    "s_mov_b32 %[cap], 0\n\t"
    "s_branch 2b\n\t"
    "9:"
    : [tc] "=&s"(tc), [sp] "=&s"(sp), [cap] "+s"(cap), [st] "=&s"(st)
    : [ag] "s"(ag), [vd] "s"(vd), [wl] "s"(wl), [wa] "s"(wa), [ra] "s"(ra), [c44] "s"(0x4444u), [em] "s"(0x00FF00FF00FF00FFull), "{v[64:95]}"(W.k0), "{v[96:127]}"(W.k1), "{v[128:159]}"(W.o0), "{v[160:191]}"(W.o1), "{v[192:223]}"(W.o2), "{v[224:255]}"(W.o3)
    : "v4", "v5", "v6", "v7", "v8", "v9", "v10", "v11", "v12", "v13", "v14", "v15", "v16", "v17", "v18", "v19", "v20", "v21", "v22", "v23", "v24", "v25", "v26", "v27", "v28", "v29", "v30", "v31", "v32", "v33", "v34", "v35", "v36", "v37", "v38", "v39", "v40", "v41", "v42", "v43", "v44", "v45", "v46", "v47", "v48", "v49", "v50", "v51", "v52", "v53", "v54", "v55", "v56", "v57", "v58", "v59", "v60", "v61", "v62", "v63", "vcc", "scc", "memory");
}
__device__ __forceinline__ void vr_w1(const VRW& W, const char* ag, const char* vd, unsigned wl, unsigned wa, unsigned ra, unsigned& cap, unsigned& sp, unsigned long long& tc, unsigned long long* ts) {
  unsigned st;
  asm volatile("s_mov_b32 %[sp], 0\n\t"
    "v_mbcnt_lo_u32_b32 v20, -1, 0\n\t"
    "v_mbcnt_hi_u32_b32 v20, -1, v20\n\t"
    "v_lshlrev_b32 v21, 4, v20\n\t"
    "s_nop 0\n\t"
    "global_load_dwordx4 v[24:27], v21, %[ag] sc1\n\t"
    "global_load_dwordx4 v[28:31], v21, %[ag] offset:1024 sc1\n\t"
    "s_memtime %[tc]\n\t"
    "v_lshl_add_u32 v6, v20, 4, %[wl]\n\t"
    "ds_read_b128 v[32:35], v6 offset:0\n\t"
    "ds_read_b128 v[36:39], v6 offset:1024\n\t"
    "ds_read_b128 v[40:43], v6 offset:2048\n\t"
    "ds_read_b128 v[44:47], v6 offset:3072\n\t"
    "ds_read_b128 v[48:51], v6 offset:4096\n\t"
    "ds_read_b128 v[52:55], v6 offset:5120\n\t"
    "ds_read_b128 v[56:59], v6 offset:6144\n\t"
    "ds_read_b128 v[60:63], v6 offset:7168\n\t"
    "s_setprio 3\n\t"
    "v_and_b32 v6, 2, v20\n\t"
    "v_mul_u32_u24 v6, 0x5555, v6\n\t"
    "v_or_b32 v4, %[c44], v6\n\t"
    "v_and_b32 v6, 15, v20\n\t"
    "v_lshl_add_u32 v5, v6, 2, %[wa]\n\t"
    "v_and_b32 v6, 48, v20\n\t"
    "v_mad_u32_u24 v5, v6, 52, v5\n\t"
    "v_and_b32 v6, 32, v20\n\t"
    "v_mul_u32_u24 v7, 26, v6\n\t"
    "v_and_b32 v6, 16, v20\n\t"
    "v_lshl_add_u32 v7, v6, 1, v7\n\t"
    "v_and_b32 v6, 7, v20\n\t"
    "v_lshl_add_u32 v7, v6, 2, v7\n\t"
    "v_and_b32 v6, 8, v20\n\t"
    "v_mul_u32_u24 v6, 1672, v6\n\t"
    "v_add_u32 v7, v7, v6\n\t"
    "v_add_u32 v7, %[ra], v7\n\t"
    "s_setprio 0\n\t"
    "s_setprio 3\n\t"
    "3:\n\t"
    "v_mov_b32 v8, 0\n\t"
    "v_mov_b32 v9, 0\n\t"
    "v_mov_b32 v10, 0\n\t"
    "v_mov_b32 v11, 0\n\t"
    "v_mov_b32 v12, 0\n\t"
    "v_mov_b32 v13, 0\n\t"
    "v_mov_b32 v14, 0\n\t"
    "v_mov_b32 v15, 0\n\t"
    "v_mov_b32 v16, 0\n\t"
    "v_mov_b32 v17, 0\n\t"
    "v_mov_b32 v18, 0\n\t"
    "v_mov_b32 v19, 0\n\t"
    "v_mov_b32 v20, 0\n\t"
    "v_mov_b32 v21, 0\n\t"
    "v_mov_b32 v22, 0\n\t"
    "v_mov_b32 v23, 0\n\t"
    "s_waitcnt vmcnt(0) lgkmcnt(0)\n\t"
    "v_smfmac_f32_16x16x64_bf16 v[8:11], v[24:27], v[32:39], v4\n\t"
    "v_smfmac_f32_16x16x64_bf16 v[8:11], v[28:31], v[40:47], v4\n\t"
    "v_mov_b32 v32, 0\n\t"
    "v_mov_b32 v33, 0\n\t"
    "v_smfmac_f32_16x16x64_bf16 v[12:15], v[24:27], v[48:55], v4\n\t"
    "v_mov_b32 v34, 0\n\t"
    "v_mov_b32 v35, 0\n\t"
    "v_smfmac_f32_16x16x64_bf16 v[12:15], v[28:31], v[56:63], v4\n\t"
    "v_mov_b32 v36, 0\n\t"
    "v_mov_b32 v37, 0\n\t"
    "v_smfmac_f32_16x16x64_bf16 v[16:19], v[24:27], v[64:71], v4\n\t"
    "v_mov_b32 v38, 0\n\t"
    "v_mov_b32 v39, 0\n\t"
    "v_smfmac_f32_16x16x64_bf16 v[16:19], v[28:31], v[72:79], v4\n\t"
    "v_add_f32 v8, v8, v10\n\t"
    "v_add_f32 v9, v9, v11\n\t"
    "ds_write2_b32 v5, v8, v9 offset0:64 offset1:168\n\t"
    "v_smfmac_f32_16x16x64_bf16 v[20:23], v[24:27], v[80:87], v4\n\t"
    "v_smfmac_f32_16x16x64_bf16 v[20:23], v[28:31], v[88:95], v4\n\t"
    "v_add_f32 v12, v12, v14\n\t"
    "v_add_f32 v13, v13, v15\n\t"
    "ds_write2_b32 v5, v12, v13 offset0:80 offset1:184\n\t"
    "v_smfmac_f32_16x16x64_bf16 v[32:35], v[24:27], v[96:103], v4\n\t"
    "v_smfmac_f32_16x16x64_bf16 v[32:35], v[28:31], v[104:111], v4\n\t"
    "v_add_f32 v16, v16, v18\n\t"
    "v_add_f32 v17, v17, v19\n\t"
    "ds_write2_b32 v5, v16, v17 offset0:0 offset1:104\n\t"
    "v_smfmac_f32_16x16x64_bf16 v[36:39], v[24:27], v[112:119], v4\n\t"
    "v_smfmac_f32_16x16x64_bf16 v[36:39], v[28:31], v[120:127], v4\n\t"
    "v_max3_u32 v6, v24, v25, v26\n\t"
    "v_add_f32 v20, v20, v22\n\t"
    "v_add_f32 v21, v21, v23\n\t"
    "ds_write2_b32 v5, v20, v21 offset0:16 offset1:120\n\t"
    "v_add_f32 v32, v32, v34\n\t"
    "v_add_f32 v33, v33, v35\n\t"
    "ds_write2_b32 v5, v32, v33 offset0:32 offset1:136\n\t"
    "v_max3_u32 v6, v6, v27, v28\n\t"
    "v_add_f32 v36, v36, v38\n\t"
    "v_add_f32 v37, v37, v39\n\t"
    "ds_write2_b32 v5, v36, v37 offset0:48 offset1:152\n\t"
    "v_max3_u32 v6, v6, v29, v30\n\t"
    "v_max_u32 v6, v6, v31\n\t"
    "v_cmp_eq_u32 vcc, -1, v6\n\t"
    "v_mbcnt_lo_u32_b32 v48, -1, 0\n\t"
    "v_mbcnt_hi_u32_b32 v48, -1, v48\n\t"
    "v_and_b32 v6, 48, v48\n\t"
    "v_and_b32 v48, 7, v48\n\t"
    "v_lshlrev_b32 v48, 2, v48\n\t"
    "v_lshl_or_b32 v48, v6, 1, v48\n\t"
    "s_cbranch_vccnz 1f\n\t"
    "2:\n\t"
    "s_waitcnt lgkmcnt(0)\n\t"
    "s_barrier\n\t"
    "ds_read2_b32 v[8:9], v7 offset1:104\n\t"
    "v_add_u32 v7, 3328, v7\n\t"
    "ds_read2_b32 v[10:11], v7 offset1:104\n\t"
    "v_add_u32 v7, 3328, v7\n\t"
    "ds_read2_b32 v[12:13], v7 offset1:104\n\t"
    "v_add_u32 v7, 3328, v7\n\t"
    "ds_read2_b32 v[14:15], v7 offset1:104\n\t"
    "s_waitcnt lgkmcnt(2)\n\t"
    "v_pk_add_f32 v[8:9], v[8:9], v[10:11]\n\t"
    "s_waitcnt lgkmcnt(0)\n\t"
    "v_pk_add_f32 v[12:13], v[12:13], v[14:15]\n\t"
    "v_pk_add_f32 v[8:9], v[8:9], v[12:13]\n\t"
    "s_nop 1\n\t"
    "v_add_f32_dpp v8, v8, v8 row_ror:8 row_mask:0xf bank_mask:0xf\n\t"
    "v_add_f32_dpp v9, v9, v9 row_ror:8 row_mask:0xf bank_mask:0xf\n\t"
    "v_cvt_pk_bf16_f32 v6, v8, v9\n\t"
    "v_min_u32 v6, -2, v6\n\t"
    "s_mov_b64 exec, %[em]\n\t"
    "global_store_dword v48, v6, %[vd] sc1\n\t"
    "s_mov_b64 exec, -1\n\t"
    "s_setprio 2\n\t"
    "s_waitcnt lgkmcnt(0)\n\t"
    "s_branch 9f\n\t"
    "1:\n\t"
    "s_add_u32 %[sp], %[sp], 1\n\t"
    "s_cmp_ge_u32 %[sp], %[cap]\n\t"
    "s_cbranch_scc1 7f\n\t"
    "s_waitcnt lgkmcnt(0)\n\t"
    "v_mbcnt_lo_u32_b32 v20, -1, 0\n\t"
    "v_mbcnt_hi_u32_b32 v20, -1, v20\n\t"
    "v_lshlrev_b32 v21, 4, v20\n\t"
    "s_nop 0\n\t"
    "global_load_dwordx4 v[24:27], v21, %[ag] sc1\n\t"
    "global_load_dwordx4 v[28:31], v21, %[ag] offset:1024 sc1\n\t"
    "s_memtime %[tc]\n\t"
    "v_lshl_add_u32 v6, v20, 4, %[wl]\n\t"
    "ds_read_b128 v[32:35], v6 offset:0\n\t"
    "ds_read_b128 v[36:39], v6 offset:1024\n\t"
    "ds_read_b128 v[40:43], v6 offset:2048\n\t"
    "ds_read_b128 v[44:47], v6 offset:3072\n\t"
    "ds_read_b128 v[48:51], v6 offset:4096\n\t"
    "ds_read_b128 v[52:55], v6 offset:5120\n\t"
    "ds_read_b128 v[56:59], v6 offset:6144\n\t"
    "ds_read_b128 v[60:63], v6 offset:7168\n\t"
    "s_branch 3b\n\t"
    "7:\n\t"
    "s_mov_b32 %[cap], 0\n\t"
    "s_branch 2b\n\t"
    "9:"
    : [tc] "=&s"(tc), [sp] "=&s"(sp), [cap] "+s"(cap), [st] "=&s"(st)
    : [ag] "s"(ag), [vd] "s"(vd), [wl] "s"(wl), [wa] "s"(wa), [ra] "s"(ra), [c44] "s"(0x4444u), [em] "s"(0x00FF00FF00FF00FFull), "{v[64:95]}"(W.k0), "{v[96:127]}"(W.k1), "{v[128:159]}"(W.o0), "{v[160:191]}"(W.o1), "{v[192:223]}"(W.o2), "{v[224:255]}"(W.o3)
    : "v4", "v5", "v6", "v7", "v8", "v9", "v10", "v11", "v12", "v13", "v14", "v15", "v16", "v17", "v18", "v19", "v20", "v21", "v22", "v23", "v24", "v25", "v26", "v27", "v28", "v29", "v30", "v31", "v32", "v33", "v34", "v35", "v36", "v37", "v38", "v39", "v40", "v41", "v42", "v43", "v44", "v45", "v46", "v47", "v48", "v49", "v50", "v51", "v52", "v53", "v54", "v55", "v56", "v57", "v58", "v59", "v60", "v61", "v62", "v63", "vcc", "scc", "memory");
}
__device__ __forceinline__ void vr_r(const VRW& W, const char* os, const char* ad, unsigned wa, unsigned ra, unsigned& cap, unsigned& sp, unsigned long long* ts) {
  asm volatile("v_mbcnt_lo_u32_b32 v4, -1, 0\n\t"
    "v_mbcnt_hi_u32_b32 v4, -1, v4\n\t"
    "v_lshlrev_b32 v5, 4, v4\n\t"
    "global_load_dwordx4 v[12:15], v5, %[os] offset:0 sc1\n\t"
    "global_load_dwordx4 v[16:19], v5, %[os] offset:1024 sc1\n\t"
    "global_load_dwordx4 v[20:23], v5, %[os] offset:2048 sc1\n\t"
    "global_load_dwordx4 v[24:27], v5, %[os] offset:3072 sc1\n\t"
    "s_mov_b32 %[sp], 0\n\t"
    "v_and_b32 v10, 1, v4\n\t"
    "v_mul_u32_u24 v10, 0xaaaa, v10\n\t"
    "v_or_b32 v6, %[c44], v10\n\t"
    "v_and_b32 v10, 15, v4\n\t"
    "v_lshl_add_u32 v7, v10, 2, %[wa]\n\t"
    "v_and_b32 v10, 48, v4\n\t"
    "v_mad_u32_u24 v7, v10, 12, v7\n\t"
    "v_and_b32 v10, 32, v4\n\t"
    "v_mul_u32_u24 v8, 12, v10\n\t"
    "v_and_b32 v10, 4, v4\n\t"
    "v_mad_u32_u24 v8, v10, 48, v8\n\t"
    "v_and_b32 v10, 16, v4\n\t"
    "v_lshl_add_u32 v8, v10, 1, v8\n\t"
    "v_and_b32 v10, 3, v4\n\t"
    "v_lshl_add_u32 v8, v10, 3, v8\n\t"
    "v_and_b32 v10, 8, v4\n\t"
    "v_lshlrev_b32 v9, 12, v10\n\t"
    "v_mul_u32_u24 v10, 400, v10\n\t"
    "v_add3_u32 v8, v8, v10, %[ra]\n\t"
    "v_and_b32 v10, 48, v4\n\t"
    "v_lshl_or_b32 v9, v10, 1, v9\n\t"
    "v_and_b32 v10, 7, v4\n\t"
    "v_lshl_or_b32 v9, v10, 2, v9\n\t"
    "3:\n\t"
    "v_mov_b32 v28, 0\n\t"
    "v_mov_b32 v29, 0\n\t"
    "v_mov_b32 v30, 0\n\t"
    "v_mov_b32 v31, 0\n\t"
    "v_mov_b32 v32, 0\n\t"
    "v_mov_b32 v33, 0\n\t"
    "v_mov_b32 v34, 0\n\t"
    "v_mov_b32 v35, 0\n\t"
    "v_mov_b32 v36, 0\n\t"
    "v_mov_b32 v37, 0\n\t"
    "v_mov_b32 v38, 0\n\t"
    "v_mov_b32 v39, 0\n\t"
    "v_mov_b32 v40, 0\n\t"
    "v_mov_b32 v41, 0\n\t"
    "v_mov_b32 v42, 0\n\t"
    "v_mov_b32 v43, 0\n\t"
    "v_mov_b32 v10, 0\n\t"
    "s_waitcnt vmcnt(3)\n\t"
    "v_smfmac_f32_16x16x64_bf16 v[28:31], v[12:15], v[128:135], v6\n\t"
    "v_max3_u32 v10, v10, v12, v13\n\t"
    "v_smfmac_f32_16x16x64_bf16 v[32:35], v[12:15], v[136:143], v6\n\t"
    "v_max3_u32 v10, v10, v14, v15\n\t"
    "v_smfmac_f32_16x16x64_bf16 v[36:39], v[12:15], v[144:151], v6\n\t"
    "v_smfmac_f32_16x16x64_bf16 v[40:43], v[12:15], v[152:159], v6\n\t"
    "s_waitcnt vmcnt(2)\n\t"
    "v_smfmac_f32_16x16x64_bf16 v[28:31], v[16:19], v[160:167], v6\n\t"
    "v_max3_u32 v10, v10, v16, v17\n\t"
    "v_smfmac_f32_16x16x64_bf16 v[32:35], v[16:19], v[168:175], v6\n\t"
    "v_max3_u32 v10, v10, v18, v19\n\t"
    "v_smfmac_f32_16x16x64_bf16 v[36:39], v[16:19], v[176:183], v6\n\t"
    "v_smfmac_f32_16x16x64_bf16 v[40:43], v[16:19], v[184:191], v6\n\t"
    "s_waitcnt vmcnt(1)\n\t"
    "v_smfmac_f32_16x16x64_bf16 v[28:31], v[20:23], v[192:199], v6\n\t"
    "v_max3_u32 v10, v10, v20, v21\n\t"
    "v_smfmac_f32_16x16x64_bf16 v[32:35], v[20:23], v[200:207], v6\n\t"
    "v_max3_u32 v10, v10, v22, v23\n\t"
    "v_smfmac_f32_16x16x64_bf16 v[36:39], v[20:23], v[208:215], v6\n\t"
    "v_smfmac_f32_16x16x64_bf16 v[40:43], v[20:23], v[216:223], v6\n\t"
    "s_waitcnt vmcnt(0)\n\t"
    "v_smfmac_f32_16x16x64_bf16 v[28:31], v[24:27], v[224:231], v6\n\t"
    "v_max3_u32 v10, v10, v24, v25\n\t"
    "v_smfmac_f32_16x16x64_bf16 v[32:35], v[24:27], v[232:239], v6\n\t"
    "v_max3_u32 v10, v10, v26, v27\n\t"
    "v_smfmac_f32_16x16x64_bf16 v[36:39], v[24:27], v[240:247], v6\n\t"
    "v_cmp_eq_u32 vcc, -1, v10\n\t"
    "v_smfmac_f32_16x16x64_bf16 v[40:43], v[24:27], v[248:255], v6\n\t"
    "s_cbranch_vccnz 4f\n\t"
    "5:\n\t"
    "s_cmp_ge_u32 %[wa], 1536\n\t"
    "s_nop 1\n\t"
    "v_add_f32 v28, v28, v29\n\t"
    "v_add_f32 v32, v32, v33\n\t"
    "v_add_f32 v36, v38, v39\n\t"
    "v_add_f32 v28, v28, v36\n\t"
    "v_add_f32 v40, v42, v43\n\t"
    "v_add_f32 v32, v32, v40\n\t"
    "ds_write2_b32 v7, v28, v32 offset1:16\n\t"
    "s_waitcnt lgkmcnt(0)\n\t"
    "s_barrier\n\t"
    "s_cbranch_scc1 9f\n\t"
    "ds_read_b64 v[44:45], v8 offset:0\n\t"
    "ds_read_b64 v[46:47], v8 offset:768\n\t"
    "ds_read_b64 v[48:49], v8 offset:1536\n\t"
    "ds_read_b64 v[50:51], v8 offset:2304\n\t"
    "s_waitcnt lgkmcnt(2)\n\t"
    "v_pk_add_f32 v[44:45], v[44:45], v[46:47]\n\t"
    "s_waitcnt lgkmcnt(0)\n\t"
    "v_pk_add_f32 v[48:49], v[48:49], v[50:51]\n\t"
    "v_pk_add_f32 v[44:45], v[44:45], v[48:49]\n\t"
    "s_nop 1\n\t"
    "v_add_f32_dpp v44, v44, v44 row_ror:8 row_mask:0xf bank_mask:0xf\n\t"
    "v_add_f32_dpp v45, v45, v45 row_ror:8 row_mask:0xf bank_mask:0xf\n\t"
    "v_cvt_pk_bf16_f32 v10, v44, v45\n\t"
    "v_min_u32 v10, -2, v10\n\t"
    "global_store_dword v9, v10, %[ad] sc1\n\t"
    "s_waitcnt lgkmcnt(0)\n\t"
    "s_branch 9f\n\t"
    "4:\n\t"
    "s_add_u32 %[sp], %[sp], 1\n\t"
    "s_cmp_ge_u32 %[sp], %[cap]\n\t"
    "s_cbranch_scc1 7f\n\t"
    "global_load_dwordx4 v[12:15], v5, %[os] offset:0 sc1\n\t"
    "global_load_dwordx4 v[16:19], v5, %[os] offset:1024 sc1\n\t"
    "global_load_dwordx4 v[20:23], v5, %[os] offset:2048 sc1\n\t"
    "global_load_dwordx4 v[24:27], v5, %[os] offset:3072 sc1\n\t"
    "s_nop 3\n\t"
    "s_branch 3b\n\t"
    "7:\n\t"
    "s_mov_b32 %[cap], 0\n\t"
    "s_branch 5b\n\t"
    "9:"
    : [sp] "=&s"(sp), [cap] "+s"(cap)
    : [os] "s"(os), [ad] "s"(ad), [wa] "s"(wa), [ra] "s"(ra), [c44] "s"(0x4444u), "{v[64:95]}"(W.k0), "{v[96:127]}"(W.k1), "{v[128:159]}"(W.o0), "{v[160:191]}"(W.o1), "{v[192:223]}"(W.o2), "{v[224:255]}"(W.o3)
    : "v4", "v5", "v6", "v7", "v8", "v9", "v10", "v11", "v12", "v13", "v14", "v15", "v16", "v17", "v18", "v19", "v20", "v21", "v22", "v23", "v24", "v25", "v26", "v27", "v28", "v29", "v30", "v31", "v32", "v33", "v34", "v35", "v36", "v37", "v38", "v39", "v40", "v41", "v42", "v43", "v44", "v45", "v46", "v47", "v48", "v49", "v50", "v51", "v52", "v53", "v54", "v55", "v56", "v57", "v58", "v59", "vcc", "scc", "memory");
}
__device__ __forceinline__ void kr_load_k0(VRW& W, unsigned a) {
  asm volatile("ds_read_b64_tr_b16 v[64:65], %[a] offset:0\n\t"
    "ds_read_b64_tr_b16 v[66:67], %[a] offset:256\n\t"
    "ds_read_b64_tr_b16 v[68:69], %[a] offset:4096\n\t"
    "ds_read_b64_tr_b16 v[70:71], %[a] offset:4352\n\t"
    "ds_read_b64_tr_b16 v[80:81], %[a] offset:32\n\t"
    "ds_read_b64_tr_b16 v[82:83], %[a] offset:288\n\t"
    "ds_read_b64_tr_b16 v[84:85], %[a] offset:4128\n\t"
    "ds_read_b64_tr_b16 v[86:87], %[a] offset:4384\n\t"
    "s_waitcnt lgkmcnt(0)\n\t"
    "ds_read_b64_tr_b16 v[96:97], %[a] offset:64\n\t"
    "ds_read_b64_tr_b16 v[98:99], %[a] offset:320\n\t"
    "ds_read_b64_tr_b16 v[100:101], %[a] offset:4160\n\t"
    "ds_read_b64_tr_b16 v[102:103], %[a] offset:4416\n\t"
    "ds_read_b64_tr_b16 v[112:113], %[a] offset:96\n\t"
    "ds_read_b64_tr_b16 v[114:115], %[a] offset:352\n\t"
    "ds_read_b64_tr_b16 v[116:117], %[a] offset:4192\n\t"
    "ds_read_b64_tr_b16 v[118:119], %[a] offset:4448\n\t"
    "s_waitcnt lgkmcnt(0)"
    : "=&{v[64:95]}"(W.k0), "=&{v[96:127]}"(W.k1) : [a] "v"(a) : "memory");
}
__device__ __forceinline__ void kr_load_k1(VRW& W, unsigned a) {
  asm volatile("ds_read_b64_tr_b16 v[72:73], %[a] offset:0\n\t"
    "ds_read_b64_tr_b16 v[74:75], %[a] offset:256\n\t"
    "ds_read_b64_tr_b16 v[76:77], %[a] offset:4096\n\t"
    "ds_read_b64_tr_b16 v[78:79], %[a] offset:4352\n\t"
    "ds_read_b64_tr_b16 v[88:89], %[a] offset:32\n\t"
    "ds_read_b64_tr_b16 v[90:91], %[a] offset:288\n\t"
    "ds_read_b64_tr_b16 v[92:93], %[a] offset:4128\n\t"
    "ds_read_b64_tr_b16 v[94:95], %[a] offset:4384\n\t"
    "s_waitcnt lgkmcnt(0)\n\t"
    "ds_read_b64_tr_b16 v[104:105], %[a] offset:64\n\t"
    "ds_read_b64_tr_b16 v[106:107], %[a] offset:320\n\t"
    "ds_read_b64_tr_b16 v[108:109], %[a] offset:4160\n\t"
    "ds_read_b64_tr_b16 v[110:111], %[a] offset:4416\n\t"
    "ds_read_b64_tr_b16 v[120:121], %[a] offset:96\n\t"
    "ds_read_b64_tr_b16 v[122:123], %[a] offset:352\n\t"
    "ds_read_b64_tr_b16 v[124:125], %[a] offset:4192\n\t"
    "ds_read_b64_tr_b16 v[126:127], %[a] offset:4448\n\t"
    "s_waitcnt lgkmcnt(0)"
    : "+&{v[64:95]}"(W.k0), "+&{v[96:127]}"(W.k1) : [a] "v"(a) : "memory");
}
__device__ __forceinline__ void kr_load_o0(VRW& W, unsigned a) {
  asm volatile("ds_read_b64_tr_b16 v[128:129], %[a] offset:0\n\t"
    "ds_read_b64_tr_b16 v[130:131], %[a] offset:128\n\t"
    "ds_read_b64_tr_b16 v[132:133], %[a] offset:1024\n\t"
    "ds_read_b64_tr_b16 v[134:135], %[a] offset:1152\n\t"
    "ds_read_b64_tr_b16 v[136:137], %[a] offset:32\n\t"
    "ds_read_b64_tr_b16 v[138:139], %[a] offset:160\n\t"
    "ds_read_b64_tr_b16 v[140:141], %[a] offset:1056\n\t"
    "ds_read_b64_tr_b16 v[142:143], %[a] offset:1184\n\t"
    "s_waitcnt lgkmcnt(0)\n\t"
    "ds_read_b64_tr_b16 v[144:145], %[a] offset:4096\n\t"
    "ds_read_b64_tr_b16 v[146:147], %[a] offset:4224\n\t"
    "ds_read_b64_tr_b16 v[148:149], %[a] offset:5120\n\t"
    "ds_read_b64_tr_b16 v[150:151], %[a] offset:5248\n\t"
    "ds_read_b64_tr_b16 v[152:153], %[a] offset:4128\n\t"
    "ds_read_b64_tr_b16 v[154:155], %[a] offset:4256\n\t"
    "ds_read_b64_tr_b16 v[156:157], %[a] offset:5152\n\t"
    "ds_read_b64_tr_b16 v[158:159], %[a] offset:5280\n\t"
    "s_waitcnt lgkmcnt(0)"
    : "=&{v[128:159]}"(W.o0) : [a] "v"(a) : "memory");
}
__device__ __forceinline__ void kr_load_o1(VRW& W, unsigned a) {
  asm volatile("ds_read_b64_tr_b16 v[160:161], %[a] offset:0\n\t"
    "ds_read_b64_tr_b16 v[162:163], %[a] offset:128\n\t"
    "ds_read_b64_tr_b16 v[164:165], %[a] offset:1024\n\t"
    "ds_read_b64_tr_b16 v[166:167], %[a] offset:1152\n\t"
    "ds_read_b64_tr_b16 v[168:169], %[a] offset:32\n\t"
    "ds_read_b64_tr_b16 v[170:171], %[a] offset:160\n\t"
    "ds_read_b64_tr_b16 v[172:173], %[a] offset:1056\n\t"
    "ds_read_b64_tr_b16 v[174:175], %[a] offset:1184\n\t"
    "s_waitcnt lgkmcnt(0)\n\t"
    "ds_read_b64_tr_b16 v[176:177], %[a] offset:4096\n\t"
    "ds_read_b64_tr_b16 v[178:179], %[a] offset:4224\n\t"
    "ds_read_b64_tr_b16 v[180:181], %[a] offset:5120\n\t"
    "ds_read_b64_tr_b16 v[182:183], %[a] offset:5248\n\t"
    "ds_read_b64_tr_b16 v[184:185], %[a] offset:4128\n\t"
    "ds_read_b64_tr_b16 v[186:187], %[a] offset:4256\n\t"
    "ds_read_b64_tr_b16 v[188:189], %[a] offset:5152\n\t"
    "ds_read_b64_tr_b16 v[190:191], %[a] offset:5280\n\t"
    "s_waitcnt lgkmcnt(0)"
    : "=&{v[160:191]}"(W.o1) : [a] "v"(a) : "memory");
}
__device__ __forceinline__ void kr_load_o2(VRW& W, unsigned a) {
  asm volatile("ds_read_b64_tr_b16 v[192:193], %[a] offset:0\n\t"
    "ds_read_b64_tr_b16 v[194:195], %[a] offset:128\n\t"
    "ds_read_b64_tr_b16 v[196:197], %[a] offset:1024\n\t"
    "ds_read_b64_tr_b16 v[198:199], %[a] offset:1152\n\t"
    "ds_read_b64_tr_b16 v[200:201], %[a] offset:32\n\t"
    "ds_read_b64_tr_b16 v[202:203], %[a] offset:160\n\t"
    "ds_read_b64_tr_b16 v[204:205], %[a] offset:1056\n\t"
    "ds_read_b64_tr_b16 v[206:207], %[a] offset:1184\n\t"
    "s_waitcnt lgkmcnt(0)\n\t"
    "ds_read_b64_tr_b16 v[208:209], %[a] offset:4096\n\t"
    "ds_read_b64_tr_b16 v[210:211], %[a] offset:4224\n\t"
    "ds_read_b64_tr_b16 v[212:213], %[a] offset:5120\n\t"
    "ds_read_b64_tr_b16 v[214:215], %[a] offset:5248\n\t"
    "ds_read_b64_tr_b16 v[216:217], %[a] offset:4128\n\t"
    "ds_read_b64_tr_b16 v[218:219], %[a] offset:4256\n\t"
    "ds_read_b64_tr_b16 v[220:221], %[a] offset:5152\n\t"
    "ds_read_b64_tr_b16 v[222:223], %[a] offset:5280\n\t"
    "s_waitcnt lgkmcnt(0)"
    : "=&{v[192:223]}"(W.o2) : [a] "v"(a) : "memory");
}
__device__ __forceinline__ void kr_load_o3(VRW& W, unsigned a) {
  asm volatile("ds_read_b64_tr_b16 v[224:225], %[a] offset:0\n\t"
    "ds_read_b64_tr_b16 v[226:227], %[a] offset:128\n\t"
    "ds_read_b64_tr_b16 v[228:229], %[a] offset:1024\n\t"
    "ds_read_b64_tr_b16 v[230:231], %[a] offset:1152\n\t"
    "ds_read_b64_tr_b16 v[232:233], %[a] offset:32\n\t"
    "ds_read_b64_tr_b16 v[234:235], %[a] offset:160\n\t"
    "ds_read_b64_tr_b16 v[236:237], %[a] offset:1056\n\t"
    "ds_read_b64_tr_b16 v[238:239], %[a] offset:1184\n\t"
    "s_waitcnt lgkmcnt(0)\n\t"
    "ds_read_b64_tr_b16 v[240:241], %[a] offset:4096\n\t"
    "ds_read_b64_tr_b16 v[242:243], %[a] offset:4224\n\t"
    "ds_read_b64_tr_b16 v[244:245], %[a] offset:5120\n\t"
    "ds_read_b64_tr_b16 v[246:247], %[a] offset:5248\n\t"
    "ds_read_b64_tr_b16 v[248:249], %[a] offset:4128\n\t"
    "ds_read_b64_tr_b16 v[250:251], %[a] offset:4256\n\t"
    "ds_read_b64_tr_b16 v[252:253], %[a] offset:5152\n\t"
    "ds_read_b64_tr_b16 v[254:255], %[a] offset:5280\n\t"
    "s_waitcnt lgkmcnt(0)"
    : "=&{v[224:255]}"(W.o3) : [a] "v"(a) : "memory");
}
#define XPTRS(l) const unsigned par_ = ini_resolve(ws, l, tag); char* xch_ = xch_base(ws); \
  const char* xb = uni(xch_ + ((size_t)3 << 21) + (((par_ >> 1) & 1) << 13)); const char* ob = uni(xch_ + ((par_ & 1) << 13)); \
  unsigned* cnt = (unsigned*)(xch_ + ((size_t)4 << 21) + ((par_ >> 2) << 13))
__device__ __forceinline__ void qk_body(char* smem, int wave_id_, int xg, int h, int wg, const u16* __restrict__ Wq, const u16* __restrict__ Wv,
                                        char* ws, unsigned* tmo, unsigned long long tag) {
  const int wave = WAVE_ID;
  const int ty = wave >> 2, w4 = wave & 3;
  const char* wq = uni((const char*)(Wq + h * DH)); const char* wv = uni((const char*)(Wv + h * DH));
  auto stage2 = [&](int i) {
    const int tid = wave * 64 + lane_id();
    if (i < 6) {
      stage_proj<false>(wq + (size_t)i * 128 * HD * 2, Q_RING + ((2 * i) & 3) * 32768, tid, wave);
      if (i < 2) stage_proj<false>(wq + (size_t)(i + 6) * 128 * HD * 2, Q_RING + ((2 * i + 1) & 3) * 32768, tid, wave);
      else stage_proj<true>(wv + (size_t)(i - 2) * 256 * HD * 2, Q_RING + ((2 * i + 1) & 3) * 32768, tid, wave);
    }
  };
  QKW W;
  delay64<QDL>();
  stage2(0); stage2(1);
#define WLD(i, w) do { if ((i) < 5) WAIT_V(8); else WAIT_V(0); \
    WBAR(); { const int l_ = lane_id(); const unsigned o_ = Q_RING + ty * 32768 + ((i) & 1) * 65536; const bool v_ = ty && (i) >= 2; \
      qk_wload<i>(w, proj_base(v_ ? w4 : 2 * w4, l_) + o_, proj_base(v_ ? w4 + 4 : 2 * w4 + 1, l_) + o_); } \
    WBAR(); stage2((i) + 2); if (wave == 0) STS(ws, wg, 1 + (i)); if ((i) == 4 && wave == 7) ini_request(ws, 30720); } while (0)
  WLD(0, W.w0); WLD(1, W.w1); WLD(2, W.w2); WLD(3, W.w3); WLD(4, W.w4); WLD(5, W.w5);
#undef WLD
  XPTRS(smem + 30720);
  if (opaque_tid() == 64) st4_sc1(cnt + (PQKV + h) * 32 + 3, 1u);
  const char* oh = ob + (size_t)(h >> 2) * 16384 + (h & 3) * 1024;
  auto rearm = [&](int ps) {
    unsigned z = SENT; asm volatile("" : "+v"(z));
    const unsigned l16 = lane_id() * 16;
#pragma unroll
    for (int r = 0; r < 2 * OREP; ++r) st16s(oh + (size_t)(ps * 4 + (r >> 1)) * 131072, (r & 1) * 4096u + l16, u32x4{z, z, z, z});
  };
  const char* dbw = xb + (size_t)48 * 16384 + 4096 + h * 128;
  if (wave == 4) *(volatile LAS u32x4*)(smem + Q_TM) = u32x4{0u, 0u, 0u, (unsigned)__builtin_amdgcn_s_memrealtime()};
  if (wave == 6) *(u32x4*)(smem + Q_PQ + 4096 + lane_id() * 16) = u32x4{0u, 0u, 0u, 0u};
  BAR();
  if (wave == 0) STS(ws, wg, 0);
  unsigned avail = 0;
  auto kvq = [&](int s) { return ws + (wave == 5 ? OFF_K : (wave == 6 ? OFF_V : OFF_Q)) + ((size_t)s * NH + h) * 8192; };
  auto stage_kv = [&](int s, int buf) {
    if (avail <= (unsigned)s) {
      for (int spins = 0;; ++spins) {
        avail = qkv_avail<0>(cnt + (PQKV + h) * 32);
        if (avail > (unsigned)s) break;
        __builtin_amdgcn_s_sleep(8);
        if (give_up(spins, tmo, 5)) break;
      }
    }
    const unsigned lane = lane_id(), v0 = (lane ^ ((lane >> 4) & 2)) * 16;
    const char* src = uni(kvq(s));
#pragma unroll
    for (int i = 0; i < 4; ++i) glds16x2s<0>(src + i * 2048, v0, Q_K + buf * 32768 + (wave - 5) * 8192 + i * 2048);
  };
  auto loop = [&](auto w_c) __attribute__((always_inline)) {
    constexpr int WV = decltype(w_c)::value;
    if (WV >= 5 && !DRY) stage_kv(0, 0);
    unsigned t0 = 0, cap = 60000, d0 = D_0, d1 = D_0;
    int dq = 0, e0 = D_Q, e1 = D_Q;
    unsigned tp = 0;
    int sl = DRY ? 2 : 0;
    unsigned fc = 0;
    for (int s = -DRY; s < NSEG; ++s) {
      TSDECL;
      const bool dry = DRY && s < 0;
      const char* dump = ws + OFF_DUMP + 262144;
      const char* ag = xb + (size_t)(((dry ? 0 : sl * 4) + (AREP == 4 ? xg : xg >> 1)) * 2 + (WV >> 2)) * 16384 + (WV < 4 ? WV * 2048 : WV * 1024);
      const int nl = sl == 2 ? 0 : sl + 1;
      const unsigned pb = sl * 32768;
      const char* vs = dry ? (WV < 4 ? ag : dump) : WV == 4 ? dbw : WV == 7 ? (const char*)(cnt + (PROG + h) * 32) : xb + (size_t)(24 + sl * 8 + (h >> 2)) * 16384 + (h & 3) * 2048;
      const char* v2 = dry ? ag : xb + (size_t)(52 + sl * 4 + (h >> 3)) * 16384 + (h & 7) * 1024 + (WV & 1) * 512;
      const char* od = dry ? dump : oh + (size_t)sl * 524288;
      const unsigned sn = (unsigned)(dry ? 255 : s + 1) << 24;
      if (s > 0) wait_rt(t0, dq);
      TS(0);
      unsigned long long tc; unsigned sp;
      qk_step<WV>(W, ag, vs, v2, dry ? dump : dbw, od, pb, sn, cap, sp, tc, nullptr, d0, d1);
      if (dry) d0 = D_0;
      dq = (sp & 0xFFFFu) ? min(dq + DQU, DQM) : max(dq - DQD, 0);
      if (WV >= 5) {
        if (RAE && WV == RAW && s > 0 && s < NSEG - 1) rearm(sl == 0 ? 2 : sl - 1);
        WBAR();
        delay64<E_S>();
        if (s + 1 < NSEG) stage_kv(s + 1, nl);
      }
      if (!RAE && WV == RAW && s > 0 && s < NSEG - 1) rearm(sl == 0 ? 2 : sl - 1);
      unsigned long long* tsp = nullptr;
      seg_attn<WV>(W, pb, dry ? dump : ws + OFF_O + ((size_t)s * 64 + 2 * h + (WV >> 1)) * 4096, tsp);
      if (WV == 1) TSFLUSH(0x8000);
      if (WV < 4 && s >= BEARLY && lane_id() == 0 && (__hip_atomic_fetch_add((LAS unsigned*)(smem + Q_TM + 8), 1u, __ATOMIC_RELAXED, __HIP_MEMORY_SCOPE_WORKGROUP) & 3u) == 3u)
        st4_sc1(cnt + (PROG + h) * 32 + 1, (unsigned)(s + 1));
      if (WV == 6 && s == ARMW) wait_arm(cnt, tmo);
      if (s < NSEG - 1 && !dry) {
        if (WV == 4) {
          delay64<E_B>();
          u32x2 v;
          for (unsigned spins = 0;; ++spins) {
            const int lane = lane_id();
            asm volatile("s_nop 4\n\tglobal_load_dwordx2 %0, %1, %2 sc1\n\ts_waitcnt vmcnt(0)" : "=&v"(v) : "v"((lane & 31) * 128 + (lane >> 5) * 8), "s"(xb + (size_t)(48 + xg) * 16384 + 4096) : "memory");
            if (__all((v.x >> 24) > (unsigned)s) || spins >= cap) break;
          }
          const unsigned now = (unsigned)__builtin_amdgcn_s_memrealtime();
          const unsigned age = rowmin16((now - v.x) & 0xFFFFFFu);
          const unsigned tb = __builtin_amdgcn_readfirstlane(now - min(min(min(rdl(age, 0), rdl(age, 16)), min(rdl(age, 32), rdl(age, 48))), 500u));
          t0 = tb + min(e0, e1);
          *(volatile LAS u32x2*)(smem + Q_TM) = u32x2{(unsigned)(s + 1), t0};
          if (ADQ && s > 1 && __all((v.y >> 24) == (unsigned)s)) {
            const unsigned ag2 = rowmin16((now - v.y) & 0xFFFFFFu);
            e1 = e0;
            e0 = __builtin_amdgcn_readfirstlane(min(max((int)(now - min(min(rdl(ag2, 0), rdl(ag2, 16)), min(rdl(ag2, 32), rdl(ag2, 48))) - tp) + D_V, D_Q - DQM), D_Q + DQM));
          }
          tp = tb;
          asm volatile("" : "+s"(t0), "+s"(e0), "+s"(e1), "+s"(tp));
          TS(14);
          TSFLUSH(0x4000);
        } else {
          for (;;) {
            const u32x2 v = *(volatile LAS u32x2*)(smem + Q_TM);
            t0 = __builtin_amdgcn_readfirstlane(v.y);
            if (__builtin_amdgcn_readfirstlane(v.x) == (unsigned)(s + 1)) break;
            __builtin_amdgcn_s_sleep(1);
          }
        }
      }
      sl = sl == 2 ? 0 : sl + 1;
    }
    WAIT_V(0);
    WBAR();
    if (WV == 7 && lane_id() == 0) st4_sc1(cnt + (PROG + h) * 32, (unsigned)NSEG);
    if (cap == 0 && lane_id() == 0) st4_sc1(tmo, 1u);
    FCS();
  };
  switch (wave) {
    case 0: loop(std::integral_constant<int, 0>{}); break;
    case 1: loop(std::integral_constant<int, 1>{}); break;
    case 2: loop(std::integral_constant<int, 2>{}); break;
    case 3: loop(std::integral_constant<int, 3>{}); break;
    case 4: loop(std::integral_constant<int, 4>{}); break;
    case 5: loop(std::integral_constant<int, 5>{}); break;
    case 6: loop(std::integral_constant<int, 6>{}); break;
    default: loop(std::integral_constant<int, 7>{}); break;
  }
  __builtin_amdgcn_s_setprio(0);
}

__device__ __forceinline__ void vr_body(char* smem, int wave_id_, int xg, int h, int c, int wg, const u16* __restrict__ Wk, const u16* __restrict__ Wv,
                                        const u16* __restrict__ Wo, char* ws, unsigned* tmo, unsigned long long tag) {
  const int wave = WAVE_ID;
  const unsigned po = (unsigned)(h >> 4) * 16384u + ((h >> 1) & 7) * 1024 + (h & 1) * 512 + c * 128;
  const unsigned ko_ = (unsigned)(24 + (h >> 2)) * 16384u + (h & 3) * 2048 + c * 1024;
  const unsigned vo_ = (unsigned)(52 + (h >> 3)) * 16384u + (h & 7) * 1024 + c * 512;
  const int hg = (wave + ROTW * ((2 * h + c) & 3)) & 7;
  VRW W;
  {
    const int lane = lane_id();
    const int g = lane >> 4, q4 = (lane & 15) >> 2, p4 = lane & 3, fr = 4 * g + (q4 & 1) + 32 * (q4 >> 1);
    const unsigned lw = wave * 16384;
    const char* sk = uni((const char*)(Wk + (size_t)(128 * wave) * HD + h * DH + 64 * c));
    const char* sv_ = uni((const char*)(Wv + (size_t)(128 * wave) * HD + h * DH + 64 + 32 * c));
    const char* so = uni((const char*)(Wo + (size_t)(512 * hg) * DM + 32 * h));
    const int kp = lane >> 2, sr = 32 * (kp >> 3) + 16 * ((kp >> 2) & 1) + (kp & 3);
    auto issue = [&](int p) {
#pragma unroll
      for (int i = 0; i < 8; ++i) {
        const unsigned d = lw + (p & 1) * 8192 + i * 1024;
        if (p < 2) gldsw(sk + (size_t)(8 * p + i) * 8 * HD * 2, (unsigned)((lane >> 3) * HD * 2 + (lane & 7) * 16), d);
        else if (p < 6 && (NTM & 8)) gldsw(so + (size_t)(128 * (p - 2) + 64 * (i & 1) + 4 * (i >> 1)) * DM * 2, (unsigned)(sr * DM * 2 + p4 * 16), d);
        else if (p < 6) glds16s<0>(so + (size_t)(128 * (p - 2) + 64 * (i & 1) + 4 * (i >> 1)) * DM * 2, (unsigned)(sr * DM * 2 + p4 * 16), d);
        else if (NTM & 16) gldsw(sv_ + (size_t)i * 16 * HD * 2, (unsigned)(kp * HD * 2 + p4 * 16), d);
        else glds16s<0>(sv_ + (size_t)i * 16 * HD * 2, (unsigned)(kp * HD * 2 + p4 * 16), d);
      }
    };
#define VTS(p) do { } while (0)
    const int fm = 16 * (g >> 1) + 4 * (g & 1) + (q4 & 1) + 8 * (q4 >> 1);
    const unsigned ak = lw + fm * 128 + p4 * 8, ao = lw + fr * 64 + p4 * 8;
#define KB() do { if (KFB) WBAR(); } while (0)
    issue(2); KB(); issue(3);
    WAIT_V(8); VTS(1); kr_load_o0(W, ao); KB(); issue(4);
    WAIT_V(8); VTS(2); kr_load_o1(W, ao + 8192); KB(); issue(5);
    WAIT_V(8); VTS(3); kr_load_o2(W, ao); KB(); issue(0);
    WAIT_V(8); VTS(4); kr_load_o3(W, ao + 8192); KB(); issue(1);
    WAIT_V(8); VTS(5); kr_load_k0(W, ak); KB(); issue(6);
    WAIT_V(8); VTS(6); kr_load_k1(W, ak + 8192);
    if (wave == 0) ini_request(ws, 163328);
    if (wave == 1) *(volatile LAS u32x2*)(smem + 163320) = u32x2{0u, (unsigned)__builtin_amdgcn_s_memrealtime()};
    WAIT_V(0);
    u32x8 x[4];
#pragma unroll
    for (int j = 0; j < 4; ++j) {
      const unsigned a = lw + fm * 64 + p4 * 8 + (j & 1) * 4096 + (j >> 1) * 32;
      const u32x2 t0 = trread(smem, a), t1 = trread(smem, a + 128), t2 = trread(smem, a + 2048), t3 = trread(smem, a + 2176);
      x[j] = u32x8{t0.x, t0.y, t1.x, t1.y, t2.x, t2.y, t3.x, t3.y};
    }
    char* d = smem + lw + 8192 + lane * 16;
#pragma unroll
    for (int j = 0; j < 4; ++j) { *(u32x4*)(d + j * 2048) = u32x4{x[j][0], x[j][1], x[j][2], x[j][3]}; *(u32x4*)(d + j * 2048 + 1024) = u32x4{x[j][4], x[j][5], x[j][6], x[j][7]}; }
    VTS(7);
  }
  BAR();
  XPTRS(smem + 163328);
  (void)cnt;
  auto rearm = [&](int ps) {
    const int lane = lane_id();
    unsigned z = SENT; asm volatile("" : "+v"(z));
    const u32x4 sv = u32x4{z, z, z, z};
    static_assert(AREP == 2, "");
    st16s(xb, lane < 32 ? (unsigned)(ps * 8 + (lane >> 4) * 2) * 16384u + po + ((lane >> 3) & 1) * 256 + (lane & 7) * 16 : (unsigned)ps * 65536u + vo_ + (lane & 31) * 16, sv);
    st16s(xb, (unsigned)ps * 131072u + ko_ + lane * 16, sv);
  };
  if (wave == 0) STS(ws, wg, 0);
  __builtin_amdgcn_s_setprio(2);
  auto loop = [&](auto cl_c) __attribute__((always_inline)) {
    constexpr int CL = decltype(cl_c)::value;
    unsigned cap = 60000, tr = 0, fc = 0, tm = 0;
    int da = D_A, dd = D_O;
    int sl = 0;
    unsigned ur = 0, up = 0; int x0 = K_0, x1 = K_0, xf = 0;
    const unsigned tz = __builtin_amdgcn_readfirstlane(*(volatile LAS unsigned*)(smem + 163324));
    for (int s = 0; s < NSEG; ++s) {
      TSDECL;
      const int sn = sl == 2 ? 0 : sl + 1;
      const char* ag = xb + (size_t)((sl * 4 + (AREP == 4 ? xg : xg >> 1)) * 2 + (wave >> 2)) * 16384 + (wave & 3) * 2048;
      const char* kd = xb + (size_t)sl * 131072 + ko_ + wave * 128;
      const char* vd = xb + (size_t)sl * 65536 + vo_ + (wave & 3) * 128;
      const char* os = ob + (size_t)((sl * 4 + ORSEL) * 8 + hg) * 16384 + c * 4096;
      const char* ad = xb + (size_t)(sn * 8) * 16384 + po + (wave & 1) * 256;
      if (s > 0) { if (wave < AREP) delay64<E_AG - E_W>(); else delay64<E_AG>(); }
      TS(0);
      unsigned long long tc; unsigned sp;
      unsigned long long* tsp = nullptr;
      if (KV3) (CL ? vr_w1 : vr_w0)(W, ag, CL ? vd : kd + wave * 128, wave * 16384 + 8192, V_P1 + wave * 3328 + (wave >> 2) * 64,
                                    CL ? V_P1 + 256 + ((wave >> 1) & 1) * 64 + (wave & 1) * 1664 : V_P1 + wave * 32, cap, sp, tc, tsp);
      else vr_v(W, ag, kd, vd, wave * 16384 + 8192, V_P1 + wave * 2304 + (wave >> 2) * 64, V_PV + wave * 1024 + (wave >> 2) * 128, V_P1 + (wave >> 1) * 32 + (wave & 1) * 1152,
           V_PV + (wave & 3) * 256, cap, sp, tc, tsp);
      TN(12, sp + 1);
      if (s > 3 && sp) fc += 1u;
      da = sp ? min(da + DQU, D_A + DQM) : max(da - DQD, D_A);
      xf = sp ? min(xf + 4, 12) : max(xf - 1, 0);
      if (s == NSEG - 1) break;
      if (ADQ && wave == 0 && s > 0) { const int l_ = lane_id(); if ((l_ & 3) < 3 && l_ < (ADK ? 8 : 4)) st4s(xb, (unsigned)(48 + (l_ & 3)) * 16384u + (l_ < 4 ? 4096 : 0) + h * 128 + c * 8 + 4, (tm & 0xFFFFFFu) | (unsigned)s << 24); }
      if (wave == 7 && s > 0) rearm(sl == 0 ? 2 : sl - 1);
      wait_clk((unsigned)tc, E_O);
      TS(3);
      const unsigned tb = bell_time(xb + (size_t)(48 + xg) * 16384 + hg * 512, (lane_id() & 3) * 128, (unsigned)(s + 1), cap);
      tr = tb;
      wait_rt(tb, dd);
      TS(4);
      vr_r(W, os, ad, V_P2 + wave * 768 + (wave >> 2) * 128, V_P2 + (wave & 1) * 64, cap, sp, tsp);
      if (ADQ && wave == 0) tm = (unsigned)__builtin_amdgcn_s_memrealtime();
      if (s > 3 && sp) fc += 0x10000u;
      dd = sp ? min(dd + DOU, D_O + DOM) : max(dd - DQD, D_O);
      TN(14, (sp & 0xFFFF) + 1);
      if (wave == 1) TSFLUSH(0x6078);
      sl = sn;
    }
    if (cap == 0 && lane_id() == 0) st4_sc1(tmo, 2u);
    FCS();
  };
  if (KV3 && wave >= 4) loop(std::integral_constant<int, 1>{}); else loop(std::integral_constant<int, 0>{});
  __builtin_amdgcn_s_setprio(0);
}
#define NCS 6
__device__ __forceinline__ int calibrate(char* ws, int i) {
  const int t = lane_id(), ci = (i & 15) % 3, w = (i >> 4) * 4 + (i & 15) / 3, chunk = ci == 0 ? 0 : ci + 2;
  char* base = xch_base(ws) + ((size_t)chunk << 21) + 127 * 16384 + w * 512;
  const char* sb[2] = {uni(base + (w & 1) * 8192), uni(base + (~w & 1) * 8192)};
  if ((t & 31) < 24) st16_sc1(base + (t >> 5) * 8192 + (t & 31) * 16, u32x4{SENT, SENT, SENT, SENT});
  unsigned v, best[2] = {1u << 30, 1u << 30};
  asm volatile("s_load_dword %0, %1, 384 glc\n\ts_waitcnt vmcnt(0) lgkmcnt(0)" : "=&s"(v) : "s"(sb[0]) : "memory");
#pragma unroll
  for (int r = 0; r < NCS; ++r) {
    unsigned long long t0, t1;
    asm volatile("s_memtime %0\n\ts_waitcnt lgkmcnt(0)\n\ts_load_dword %2, %3, %4 glc\n\ts_waitcnt lgkmcnt(0)\n\ts_memtime %1\n\ts_waitcnt lgkmcnt(0)"
                 : "=&s"(t0), "=&s"(t1), "=&s"(v) : "s"(sb[r & 1]), "i"((r >> 1) * 128) : "memory");
    best[r & 1] = min(best[r & 1], (unsigned)(t1 - t0));
  }
  return (w & 1) ? (int)best[0] - (int)best[1] : (int)best[1] - (int)best[0];
}
__device__ __forceinline__ void init_sums(char* smem, int t, int i, const u16* __restrict__ cvec, const u16* __restrict__ wmod) {
  const int cg = t & 15, r0 = t >> 4, k0 = (i >> 4) * 256 + r0 * 16;
  u32x4 w[16];
#pragma unroll
  for (int j = 0; j < 16; ++j) w[j] = ldnt<1>(wmod + (size_t)(k0 + j) * (6 * DM) + (i & 15) * 128 + cg * 8);
  const u32x4 c0 = *(const u32x4*)(cvec + k0), c1 = *(const u32x4*)(cvec + k0 + 8);
  f32x2 acc[4] = {f32x2{0, 0}, f32x2{0, 0}, f32x2{0, 0}, f32x2{0, 0}};
#pragma unroll
  for (int j = 0; j < 16; ++j) {
    const unsigned cw = j < 8 ? c0[(j >> 1) & 3] : c1[(j >> 1) & 3];
    const float c = (j & 1) ? bfhi(cw) : bflo(cw), sv = bfr(c / (1.0f + __expf(-c)));
#pragma unroll
    for (int e = 0; e < 4; ++e) acc[e] = pkfma(f32x2{sv, sv}, f32x2{bflo(w[j][e]), bfhi(w[j][e])}, acc[e]);
  }
  float* red = (float*)smem;
  *(f32x4*)(red + r0 * 128 + cg * 8) = f32x4{acc[0][0], acc[0][1], acc[1][0], acc[1][1]};
  *(f32x4*)(red + r0 * 128 + cg * 8 + 4) = f32x4{acc[2][0], acc[2][1], acc[3][0], acc[3][1]};
}
__device__ __forceinline__ void init_role(char* smem, int wave_id_, int i, const u16* __restrict__ mem0, char* ws, unsigned long long tag, unsigned vote) {
  const int t = opaque_tid();
  char* xch = xch_base(ws);
  if (t < 128) {
    const float* red = (const float*)smem;
    float s = 0;
#pragma unroll
    for (int r = 0; r < 16; ++r) s += red[r * 128 + t];
    st4_sc1(ws + OFF_MOD + ((i >> 4) * (2 * DM) + (i & 15) * 128 + t) * 4, __builtin_bit_cast(unsigned, s));
  }
  static_assert(OREP == 2 && AREP == 2, "");
  for (int b = i; b < 105; b += 64) {
    if (b < 101) {
      char* p = b < 48 ? xch + (size_t)((b >> 4) * 32 + (b & 15)) * 16384
              : (b < 56 ? xch + ((size_t)3 << 21) + (size_t)(8 + ((b - 48) >> 2) * 8 + (b & 3)) * 16384
              : (b < 96 ? xch + ((size_t)3 << 21) + (size_t)(b - 56 + 24) * 16384 : xch + ((size_t)4 << 21) + (size_t)(b - 96) * 16384));
      const unsigned v = b < 80 || (b >= 84 && b < 96) ? SENT : 0u;
#pragma unroll
      for (int j = 0; j < 2; ++j) st16_sc1(p + j * 8192 + t * 16, u32x4{v, v, v, v});
    } else {
      const int rep = (b - 101) & 1, par = (b - 101) >> 1;
      if (b == 101 && t == 0) st4_sc1(ws + OFF_TMO, 0u);
#pragma unroll
      for (int j = 0; j < 2; ++j) {
        const int u = t + 512 * j, lane = u & 63, g = lane >> 4, hf = (lane >> 1) & 1, row = ((lane >> 1) & 6) + (lane & 1);
        u32x4 v = *(const u32x4*)(mem0 + row * DM + 64 * (u >> 6) + 16 * g + 8 * hf);
        v.x = nosent(v.x); v.y = nosent(v.y); v.z = nosent(v.z); v.w = nosent(v.w);
        st16_sc1(xch + ((size_t)3 << 21) + (size_t)(rep * 2 + j) * 16384 + par * 8192 + t * 16, v);
      }
    }
  }
  if (wave_id_ == 7) IS(4);
  WAIT_V(0);
  BAR();
  if (wave_id_ == 7) IS(5);
  if (t == 0) st8_sc1(ws + OFF_INI + i * 128, u32x2{(unsigned)tag, ((unsigned)(tag >> 48) << 16) | vote});
}
#define GEMMX gemm7
#define CPF 136
#define PFD 2
#define PFW 0
#define NIMM 1
enum { T_NONE, T_QV, T_KR, T_WO, T_F1, T_F2, T_F2C, T_FIN, T_LN, T_SA };
__device__ __attribute__((aligned(16))) const unsigned ROLES[256][6] = {
  {0x1, 0x0, 0x0, 0x0, 0x0, 0x0},
  {0x11, 0x0, 0x0, 0x0, 0x0, 0x0},
  {0x21, 0x0, 0x0, 0x0, 0x0, 0x0},
  {0x31, 0x0, 0x0, 0x0, 0x0, 0x0},
  {0x41, 0x0, 0x0, 0x0, 0x0, 0x0},
  {0x51, 0x0, 0x0, 0x0, 0x0, 0x0},
  {0x61, 0x0, 0x0, 0x0, 0x0, 0x0},
  {0x71, 0x0, 0x0, 0x0, 0x0, 0x0},
  {0x81, 0x0, 0x0, 0x0, 0x0, 0x0},
  {0x91, 0x0, 0x0, 0x0, 0x0, 0x0},
  {0xa1, 0x0, 0x0, 0x0, 0x0, 0x0},
  {0xb1, 0x0, 0x0, 0x0, 0x0, 0x0},
  {0x2, 0x0, 0x0, 0x0, 0x0, 0x0},
  {0x12, 0x0, 0x0, 0x0, 0x0, 0x0},
  {0x22, 0x0, 0x0, 0x0, 0x0, 0x0},
  {0x32, 0x0, 0x0, 0x0, 0x0, 0x0},
  {0x42, 0x0, 0x0, 0x0, 0x0, 0x0},
  {0x52, 0x0, 0x0, 0x0, 0x0, 0x0},
  {0x62, 0x0, 0x0, 0x0, 0x0, 0x0},
  {0x72, 0x0, 0x0, 0x0, 0x0, 0x0},
  {0x82, 0x0, 0x0, 0x0, 0x0, 0x0},
  {0x92, 0x0, 0x0, 0x0, 0x0, 0x0},
  {0xa2, 0x0, 0x0, 0x0, 0x0, 0x0},
  {0xb2, 0x0, 0x0, 0x0, 0x0, 0x0},
  {0xc2, 0x0, 0x0, 0x0, 0x0, 0x0},
  {0xd2, 0x0, 0x0, 0x0, 0x0, 0x0},
  {0xe2, 0x0, 0x0, 0x0, 0x0, 0x0},
  {0xf2, 0x0, 0x0, 0x0, 0x0, 0x0},
  {0x102, 0x0, 0x0, 0x0, 0x0, 0x0},
  {0x112, 0x0, 0x0, 0x0, 0x0, 0x0},
  {0x122, 0x0, 0x0, 0x0, 0x0, 0x0},
  {0x132, 0x0, 0x0, 0x0, 0x0, 0x0},
  {0xc1, 0x0, 0x0, 0x0, 0x0, 0x0},
  {0xd1, 0x0, 0x0, 0x0, 0x0, 0x0},
  {0xe1, 0x0, 0x0, 0x0, 0x0, 0x0},
  {0xf1, 0x0, 0x0, 0x0, 0x0, 0x0},
  {0x101, 0x0, 0x0, 0x0, 0x0, 0x0},
  {0x111, 0x0, 0x0, 0x0, 0x0, 0x0},
  {0x121, 0x0, 0x0, 0x0, 0x0, 0x0},
  {0x131, 0x0, 0x0, 0x0, 0x0, 0x0},
  {0x142, 0x0, 0x0, 0x0, 0x0, 0x0},
  {0x152, 0x0, 0x0, 0x0, 0x0, 0x0},
  {0x162, 0x0, 0x0, 0x0, 0x0, 0x0},
  {0x172, 0x0, 0x0, 0x0, 0x0, 0x0},
  {0x182, 0x0, 0x0, 0x0, 0x0, 0x0},
  {0x192, 0x0, 0x0, 0x0, 0x0, 0x0},
  {0x1a2, 0x0, 0x0, 0x0, 0x0, 0x0},
  {0x1b2, 0x0, 0x0, 0x0, 0x0, 0x0},
  {0x1c2, 0x0, 0x0, 0x0, 0x0, 0x0},
  {0x1d2, 0x0, 0x0, 0x0, 0x0, 0x0},
  {0x1e2, 0x0, 0x0, 0x0, 0x0, 0x0},
  {0x1f2, 0x0, 0x0, 0x0, 0x0, 0x0},
  {0x202, 0x0, 0x0, 0x0, 0x0, 0x0},
  {0x212, 0x0, 0x0, 0x0, 0x0, 0x0},
  {0x222, 0x0, 0x0, 0x0, 0x0, 0x0},
  {0x232, 0x0, 0x0, 0x0, 0x0, 0x0},
  {0x242, 0x0, 0x0, 0x0, 0x0, 0x0},
  {0x252, 0x0, 0x0, 0x0, 0x0, 0x0},
  {0x262, 0x0, 0x0, 0x0, 0x0, 0x0},
  {0x272, 0x0, 0x0, 0x0, 0x0, 0x0},
  {0x282, 0x0, 0x0, 0x0, 0x0, 0x0},
  {0x292, 0x0, 0x0, 0x0, 0x0, 0x0},
  {0x2a2, 0x0, 0x0, 0x0, 0x0, 0x0},
  {0x2b2, 0x0, 0x0, 0x0, 0x0, 0x0},
  {0x141, 0x0, 0x0, 0x0, 0x0, 0x0},
  {0x151, 0x0, 0x0, 0x0, 0x0, 0x0},
  {0x161, 0x0, 0x0, 0x0, 0x0, 0x0},
  {0x171, 0x0, 0x0, 0x0, 0x0, 0x0},
  {0x181, 0x0, 0x0, 0x0, 0x0, 0x0},
  {0x191, 0x0, 0x0, 0x0, 0x0, 0x0},
  {0x1a1, 0x0, 0x0, 0x0, 0x0, 0x0},
  {0x1b1, 0x0, 0x0, 0x0, 0x0, 0x0},
  {0x1c1, 0x0, 0x0, 0x0, 0x0, 0x0},
  {0x1d1, 0x0, 0x0, 0x0, 0x0, 0x0},
  {0x1e1, 0x0, 0x0, 0x0, 0x0, 0x0},
  {0x1f1, 0x0, 0x0, 0x0, 0x0, 0x0},
  {0x2c2, 0x0, 0x0, 0x0, 0x0, 0x0},
  {0x2d2, 0x0, 0x0, 0x0, 0x0, 0x0},
  {0x2e2, 0x0, 0x0, 0x0, 0x0, 0x0},
  {0x2f2, 0x0, 0x0, 0x0, 0x0, 0x0},
  {0x302, 0x0, 0x0, 0x0, 0x0, 0x0},
  {0x312, 0x0, 0x0, 0x0, 0x0, 0x0},
  {0x322, 0x0, 0x0, 0x0, 0x0, 0x0},
  {0x332, 0x0, 0x0, 0x0, 0x0, 0x0},
  {0x342, 0x0, 0x0, 0x0, 0x0, 0x0},
  {0x352, 0x0, 0x0, 0x0, 0x0, 0x0},
  {0x362, 0x0, 0x0, 0x0, 0x0, 0x0},
  {0x372, 0x0, 0x0, 0x0, 0x0, 0x0},
  {0x382, 0x0, 0x0, 0x0, 0x0, 0x0},
  {0x392, 0x0, 0x0, 0x0, 0x0, 0x0},
  {0x3a2, 0x0, 0x0, 0x0, 0x0, 0x0},
  {0x3b2, 0x0, 0x0, 0x0, 0x0, 0x0},
  {0x3c2, 0x0, 0x0, 0x0, 0x0, 0x0},
  {0x3d2, 0x0, 0x0, 0x0, 0x0, 0x0},
  {0x3e2, 0x0, 0x0, 0x0, 0x0, 0x0},
  {0x3f2, 0x0, 0x0, 0x0, 0x0, 0x0},
  {0x100005, 0x2f0000, 0x0, 0x0, 0x0, 0x0},
  {0x100015, 0x2f0004, 0x0, 0x0, 0x0, 0x0},
  {0x100025, 0x2f0008, 0x0, 0x0, 0x0, 0x0},
  {0x100035, 0x2f000c, 0x0, 0x0, 0x0, 0x0},
  {0x100045, 0x2f0010, 0x0, 0x0, 0x0, 0x0},
  {0x100055, 0x2f0014, 0x0, 0x0, 0x0, 0x0},
  {0x100065, 0x2f0018, 0x0, 0x0, 0x0, 0x0},
  {0x200037, 0x2f001c, 0x140271e, 0x0, 0x0, 0x0},
  {0x100075, 0x2f0020, 0x0, 0x0, 0x0, 0x0},
  {0x100085, 0x2f0024, 0x0, 0x0, 0x0, 0x0},
  {0x100095, 0x2f0028, 0x0, 0x0, 0x0, 0x0},
  {0x1000a5, 0x2f002c, 0x0, 0x0, 0x0, 0x0},
  {0x1000b5, 0x2f0030, 0x0, 0x0, 0x0, 0x0},
  {0x1000c5, 0x2f0034, 0x0, 0x0, 0x0, 0x0},
  {0x1000d5, 0x2f0038, 0x0, 0x0, 0x0, 0x0},
  {0x200077, 0x2f003c, 0x140273e, 0x0, 0x0, 0x0},
  {0x1000e5, 0x2f0040, 0x0, 0x0, 0x0, 0x0},
  {0x1000f5, 0x2f0044, 0x0, 0x0, 0x0, 0x0},
  {0x100105, 0x2f0048, 0x0, 0x0, 0x0, 0x0},
  {0x100115, 0x2f004c, 0x0, 0x0, 0x0, 0x0},
  {0x100125, 0x2f0050, 0x0, 0x0, 0x0, 0x0},
  {0x100135, 0x2f0054, 0x0, 0x0, 0x0, 0x0},
  {0x100145, 0x2f0058, 0x0, 0x0, 0x0, 0x0},
  {0x2000b7, 0x2f005c, 0x140275e, 0x0, 0x0, 0x0},
  {0x100155, 0x2f0060, 0x0, 0x0, 0x0, 0x0},
  {0x100165, 0x2f0064, 0x0, 0x0, 0x0, 0x0},
  {0x100175, 0x2f0068, 0x0, 0x0, 0x0, 0x0},
  {0x100006, 0x2f006c, 0x0, 0x0, 0x0, 0x0},
  {0x100016, 0x2f0070, 0x0, 0x0, 0x0, 0x0},
  {0x100026, 0x2f0074, 0x0, 0x0, 0x0, 0x0},
  {0x100036, 0x2f0078, 0x0, 0x0, 0x0, 0x0},
  {0x2000f7, 0x2f007c, 0x140277e, 0x0, 0x0, 0x0},
  {0x100004, 0x5402402, 0x0, 0x0, 0x0, 0x0},
  {0x100014, 0x5402706, 0x0, 0x0, 0x0, 0x0},
  {0x100024, 0x540240a, 0x0, 0x0, 0x0, 0x0},
  {0x100044, 0x540270e, 0x0, 0x0, 0x0, 0x0},
  {0x100003, 0x5402f00, 0x0, 0x0, 0x0, 0x0},
  {0x100013, 0x5402f04, 0x0, 0x0, 0x0, 0x0},
  {0x100023, 0x5402f08, 0x0, 0x0, 0x0, 0x0},
  {0x100033, 0x5402f0c, 0x0, 0x0, 0x0, 0x0},
  {0x200000, 0x400001, 0x2402e12, 0x0, 0x0, 0x0},
  {0x200000, 0x400005, 0x2402e16, 0x0, 0x0, 0x0},
  {0x200000, 0x400009, 0x2402e19, 0x0, 0x0, 0x0},
  {0x200007, 0x40000d, 0x2402e1a, 0x0, 0x0, 0x0},
  {0x200017, 0x400011, 0x2402e1d, 0x0, 0x0, 0x0},
  {0x200027, 0x400015, 0x1402f18, 0x0, 0x0, 0x0},
  {0x100054, 0x270019, 0x0, 0x0, 0x0, 0x0},
  {0x100064, 0x27001d, 0x0, 0x0, 0x0, 0x0},
  {0x100034, 0x240002, 0x0, 0x0, 0x0, 0x0},
  {0x100074, 0x270006, 0x0, 0x0, 0x0, 0x0},
  {0x1000b4, 0x24000a, 0x0, 0x0, 0x0, 0x0},
  {0x100084, 0x27000e, 0x0, 0x0, 0x0, 0x0},
  {0x100094, 0x270012, 0x0, 0x0, 0x0, 0x0},
  {0x1000a4, 0x270016, 0x0, 0x0, 0x0, 0x0},
  {0x1000c4, 0x27001a, 0x0, 0x0, 0x0, 0x0},
  {0x1000d4, 0x27001e, 0x0, 0x0, 0x0, 0x0},
  {0x101043, 0x12e2712, 0x0, 0x0, 0x0, 0x0},
  {0x102053, 0x12e271a, 0x0, 0x0, 0x0, 0x0},
  {0x103063, 0x12e2716, 0x0, 0x0, 0x0, 0x0},
  {0x104073, 0x12e2719, 0x0, 0x0, 0x0, 0x0},
  {0x105008, 0x1402f10, 0x0, 0x0, 0x0, 0x0},
  {0x106088, 0x12e271d, 0x0, 0x0, 0x0, 0x0},
  {0x107018, 0x1402f14, 0x0, 0x0, 0x0, 0x0},
  {0x108098, 0x1402f1c, 0x0, 0x0, 0x0, 0x0},
  {0x1000e4, 0x5402422, 0x0, 0x0, 0x0, 0x0},
  {0x1000f4, 0x5402726, 0x0, 0x0, 0x0, 0x0},
  {0x100104, 0x540242a, 0x0, 0x0, 0x0, 0x0},
  {0x100114, 0x540272e, 0x0, 0x0, 0x0, 0x0},
  {0x100083, 0x5402f20, 0x0, 0x0, 0x0, 0x0},
  {0x100093, 0x5402f24, 0x0, 0x0, 0x0, 0x0},
  {0x1000a3, 0x5402f28, 0x0, 0x0, 0x0, 0x0},
  {0x1000b3, 0x5402f2c, 0x0, 0x0, 0x0, 0x0},
  {0x200000, 0x400021, 0x2402e32, 0x0, 0x0, 0x0},
  {0x200000, 0x400025, 0x2402e36, 0x0, 0x0, 0x0},
  {0x200000, 0x400029, 0x2402e39, 0x0, 0x0, 0x0},
  {0x200047, 0x40002d, 0x2402e3a, 0x0, 0x0, 0x0},
  {0x200057, 0x400031, 0x2402e3d, 0x0, 0x0, 0x0},
  {0x200067, 0x400035, 0x1402f38, 0x0, 0x0, 0x0},
  {0x100124, 0x270039, 0x0, 0x0, 0x0, 0x0},
  {0x100144, 0x27003d, 0x0, 0x0, 0x0, 0x0},
  {0x100134, 0x240022, 0x0, 0x0, 0x0, 0x0},
  {0x100154, 0x270026, 0x0, 0x0, 0x0, 0x0},
  {0x1001b4, 0x24002a, 0x0, 0x0, 0x0, 0x0},
  {0x100164, 0x27002e, 0x0, 0x0, 0x0, 0x0},
  {0x100174, 0x270032, 0x0, 0x0, 0x0, 0x0},
  {0x100184, 0x270036, 0x0, 0x0, 0x0, 0x0},
  {0x100194, 0x27003a, 0x0, 0x0, 0x0, 0x0},
  {0x1001a4, 0x27003e, 0x0, 0x0, 0x0, 0x0},
  {0x1090c3, 0x12e2732, 0x0, 0x0, 0x0, 0x0},
  {0x10a0d3, 0x12e273a, 0x0, 0x0, 0x0, 0x0},
  {0x10b0e3, 0x12e2736, 0x0, 0x0, 0x0, 0x0},
  {0x10c0f3, 0x12e2739, 0x0, 0x0, 0x0, 0x0},
  {0x10d028, 0x1402f30, 0x0, 0x0, 0x0, 0x0},
  {0x10e0a8, 0x12e273d, 0x0, 0x0, 0x0, 0x0},
  {0x10f038, 0x1402f34, 0x0, 0x0, 0x0, 0x0},
  {0x1100b8, 0x1402f3c, 0x0, 0x0, 0x0, 0x0},
  {0x1001d4, 0x5402442, 0x0, 0x0, 0x0, 0x0},
  {0x1001e4, 0x5402746, 0x0, 0x0, 0x0, 0x0},
  {0x1001f4, 0x540244a, 0x0, 0x0, 0x0, 0x0},
  {0x100204, 0x540274e, 0x0, 0x0, 0x0, 0x0},
  {0x100103, 0x5402f40, 0x0, 0x0, 0x0, 0x0},
  {0x100113, 0x5402f44, 0x0, 0x0, 0x0, 0x0},
  {0x100123, 0x5402f48, 0x0, 0x0, 0x0, 0x0},
  {0x100133, 0x5402f4c, 0x0, 0x0, 0x0, 0x0},
  {0x200000, 0x400041, 0x2402e52, 0x0, 0x0, 0x0},
  {0x200000, 0x400045, 0x2402e56, 0x0, 0x0, 0x0},
  {0x200000, 0x400049, 0x2402e59, 0x0, 0x0, 0x0},
  {0x200087, 0x40004d, 0x2402e5a, 0x0, 0x0, 0x0},
  {0x200097, 0x400051, 0x2402e5d, 0x0, 0x0, 0x0},
  {0x2000a7, 0x400055, 0x1402f58, 0x0, 0x0, 0x0},
  {0x100214, 0x270059, 0x0, 0x0, 0x0, 0x0},
  {0x100224, 0x27005d, 0x0, 0x0, 0x0, 0x0},
  {0x100234, 0x240042, 0x0, 0x0, 0x0, 0x0},
  {0x100244, 0x270046, 0x0, 0x0, 0x0, 0x0},
  {0x1001c4, 0x24004a, 0x0, 0x0, 0x0, 0x0},
  {0x100254, 0x27004e, 0x0, 0x0, 0x0, 0x0},
  {0x100264, 0x270052, 0x0, 0x0, 0x0, 0x0},
  {0x100274, 0x270056, 0x0, 0x0, 0x0, 0x0},
  {0x100284, 0x27005a, 0x0, 0x0, 0x0, 0x0},
  {0x100294, 0x27005e, 0x0, 0x0, 0x0, 0x0},
  {0x111143, 0x12e2752, 0x0, 0x0, 0x0, 0x0},
  {0x112153, 0x12e275a, 0x0, 0x0, 0x0, 0x0},
  {0x113163, 0x12e2756, 0x0, 0x0, 0x0, 0x0},
  {0x114173, 0x12e2759, 0x0, 0x0, 0x0, 0x0},
  {0x115048, 0x1402f50, 0x0, 0x0, 0x0, 0x0},
  {0x1160c8, 0x12e275d, 0x0, 0x0, 0x0, 0x0},
  {0x117058, 0x1402f54, 0x0, 0x0, 0x0, 0x0},
  {0x1180d8, 0x1402f5c, 0x0, 0x0, 0x0, 0x0},
  {0x1002a4, 0x5402462, 0x0, 0x0, 0x0, 0x0},
  {0x1002c4, 0x5402766, 0x0, 0x0, 0x0, 0x0},
  {0x1002d4, 0x540246a, 0x0, 0x0, 0x0, 0x0},
  {0x1002e4, 0x540276e, 0x0, 0x0, 0x0, 0x0},
  {0x100183, 0x5402f60, 0x0, 0x0, 0x0, 0x0},
  {0x100193, 0x5402f64, 0x0, 0x0, 0x0, 0x0},
  {0x1001a3, 0x5402f68, 0x0, 0x0, 0x0, 0x0},
  {0x1001b3, 0x5402f6c, 0x0, 0x0, 0x0, 0x0},
  {0x200000, 0x400061, 0x2402e72, 0x0, 0x0, 0x0},
  {0x200000, 0x400065, 0x2402e76, 0x0, 0x0, 0x0},
  {0x200000, 0x400069, 0x2402e79, 0x0, 0x0, 0x0},
  {0x2000c7, 0x40006d, 0x2402e7a, 0x0, 0x0, 0x0},
  {0x2000d7, 0x400071, 0x2402e7d, 0x0, 0x0, 0x0},
  {0x2000e7, 0x400075, 0x1402f78, 0x0, 0x0, 0x0},
  {0x1002f4, 0x270079, 0x0, 0x0, 0x0, 0x0},
  {0x100304, 0x27007d, 0x0, 0x0, 0x0, 0x0},
  {0x1002b4, 0x240062, 0x0, 0x0, 0x0, 0x0},
  {0x100314, 0x270066, 0x0, 0x0, 0x0, 0x0},
  {0x100334, 0x24006a, 0x0, 0x0, 0x0, 0x0},
  {0x100324, 0x27006e, 0x0, 0x0, 0x0, 0x0},
  {0x100344, 0x270072, 0x0, 0x0, 0x0, 0x0},
  {0x100354, 0x270076, 0x0, 0x0, 0x0, 0x0},
  {0x100364, 0x27007a, 0x0, 0x0, 0x0, 0x0},
  {0x100374, 0x27007e, 0x0, 0x0, 0x0, 0x0},
  {0x1191c3, 0x12e2772, 0x0, 0x0, 0x0, 0x0},
  {0x11a1d3, 0x12e277a, 0x0, 0x0, 0x0, 0x0},
  {0x11b1e3, 0x12e2776, 0x0, 0x0, 0x0, 0x0},
  {0x11c1f3, 0x12e2779, 0x0, 0x0, 0x0, 0x0},
  {0x11d068, 0x1402f70, 0x0, 0x0, 0x0, 0x0},
  {0x11e0e8, 0x12e277d, 0x0, 0x0, 0x0, 0x0},
  {0x11f078, 0x1402f74, 0x0, 0x0, 0x0, 0x0},
  {0x1200f8, 0x1402f7c, 0x0, 0x0, 0x0, 0x0},
};
constexpr unsigned NQV = 0xc080c;
__device__ __attribute__((aligned(16))) const unsigned PFT[8][128] = {
  {0x40000f, 0x40000e, 0x0, 0x0, 0x41000f, 0x41000e, 0x0, 0x0, 0x42000f, 0x42000e, 0x0, 0x0, 0x43000f, 0x43000e, 0x0, 0x0, 0x44000f, 0x40002d, 0x0, 0x0, 0x45000f, 0x41002d, 0x0, 0x0, 0x46000f, 0x42002d, 0x0, 0x0, 0x47000f, 0x43002d, 0x0, 0x0, 0x48000f, 0x40002e, 0x0, 0x0, 0x49000f, 0x41002e, 0x0, 0x0, 0x4a000f, 0x42002e, 0x0, 0x0, 0x4b000f, 0x43002e, 0x0, 0x0, 0x4c000f, 0x40004d, 0x0, 0x0, 0x4d000f, 0x41004d, 0x0, 0x0, 0x4e000f, 0x42004d, 0x0, 0x0, 0x4f000f, 0x43004d, 0x0, 0x0, 0x40000c, 0x40004e, 0x0, 0x0, 0x41000c, 0x41004e, 0x0, 0x0, 0x42000c, 0x42004e, 0x0, 0x0, 0x43000c, 0x43004e, 0x0, 0x0, 0x40002c, 0x400003, 0x0, 0x0, 0x41002c, 0x400007, 0x0, 0x0, 0x42002c, 0x40000b, 0x0, 0x0, 0x43002c, 0x400013, 0x0, 0x0, 0x40004c, 0x420003, 0x0, 0x0, 0x41004c, 0x420007, 0x0, 0x0, 0x42004c, 0x42000b, 0x0, 0x0, 0x43004c, 0x420013, 0x0, 0x0, 0x40000d, 0x440003, 0x0, 0x0, 0x41000d, 0x440007, 0x0, 0x0, 0x42000d, 0x44000b, 0x0, 0x0, 0x43000d, 0x440013, 0x0, 0x0},
  {0x40006c, 0x400017, 0x0, 0x0, 0x41006c, 0x40001b, 0x0, 0x0, 0x42006c, 0x40001f, 0x0, 0x0, 0x43006c, 0x400023, 0x0, 0x0, 0x40008c, 0x400027, 0x0, 0x0, 0x41008c, 0x40002b, 0x0, 0x0, 0x42008c, 0x420017, 0x0, 0x0, 0x43008c, 0x42001b, 0x0, 0x0, 0x40006d, 0x42001f, 0x0, 0x0, 0x41006d, 0x420023, 0x0, 0x0, 0x42006d, 0x420027, 0x0, 0x0, 0x43006d, 0x42002b, 0x0, 0x0, 0x40006e, 0x440017, 0x0, 0x0, 0x41006e, 0x44001b, 0x0, 0x0, 0x42006e, 0x44001f, 0x0, 0x0, 0x43006e, 0x440023, 0x0, 0x0, 0x40008d, 0x440027, 0x0, 0x0, 0x41008d, 0x44002b, 0x0, 0x0, 0x42008d, 0x460017, 0x0, 0x0, 0x43008d, 0x46001b, 0x0, 0x0, 0x40008e, 0x46001f, 0x0, 0x0, 0x41008e, 0x460023, 0x0, 0x0, 0x42008e, 0x460027, 0x0, 0x0, 0x43008e, 0x46002b, 0x0, 0x0, 0x4000ad, 0x480017, 0x0, 0x0, 0x4100ad, 0x48001b, 0x0, 0x0, 0x4200ad, 0x48001f, 0x0, 0x0, 0x4300ad, 0x480023, 0x0, 0x0, 0x4000ae, 0x480027, 0x0, 0x0, 0x4100ae, 0x48002b, 0x0, 0x0, 0x4200ae, 0x4a0017, 0x0, 0x0, 0x4300ae, 0x4a001b, 0x0, 0x0},
  {0x40002f, 0x4000ce, 0x0, 0x0, 0x41002f, 0x4100ce, 0x0, 0x0, 0x42002f, 0x4200ce, 0x0, 0x0, 0x43002f, 0x4300ce, 0x0, 0x0, 0x44002f, 0x4000ed, 0x0, 0x0, 0x45002f, 0x4100ed, 0x0, 0x0, 0x46002f, 0x4200ed, 0x0, 0x0, 0x47002f, 0x4300ed, 0x0, 0x0, 0x48002f, 0x4000ee, 0x0, 0x0, 0x49002f, 0x4100ee, 0x0, 0x0, 0x4a002f, 0x4200ee, 0x0, 0x0, 0x4b002f, 0x4300ee, 0x0, 0x0, 0x4c002f, 0x4000fe, 0x0, 0x0, 0x4d002f, 0x4100fe, 0x0, 0x0, 0x4e002f, 0x4200fe, 0x0, 0x0, 0x4f002f, 0x4300fe, 0x0, 0x0, 0x4000ac, 0x4000fd, 0x0, 0x0, 0x4100ac, 0x4100fd, 0x0, 0x0, 0x4200ac, 0x4200fd, 0x0, 0x0, 0x4300ac, 0x4300fd, 0x0, 0x0, 0x4000cc, 0x40003f, 0x0, 0x0, 0x4100cc, 0x41003f, 0x0, 0x0, 0x4200cc, 0x42003f, 0x0, 0x0, 0x4300cc, 0x43003f, 0x0, 0x0, 0x4000ec, 0x44003f, 0x0, 0x0, 0x4100ec, 0x45003f, 0x0, 0x0, 0x4200ec, 0x46003f, 0x0, 0x0, 0x4300ec, 0x47003f, 0x0, 0x0, 0x4000cd, 0x48003f, 0x0, 0x0, 0x4100cd, 0x49003f, 0x0, 0x0, 0x4200cd, 0x4a003f, 0x0, 0x0, 0x4300cd, 0x4b003f, 0x0, 0x0},
  {0x40000c, 0x0, 0x0, 0x0, 0x41000c, 0x0, 0x0, 0x0, 0x42000c, 0x0, 0x0, 0x0, 0x43000c, 0x0, 0x0, 0x0, 0x40002c, 0x0, 0x0, 0x0, 0x41002c, 0x0, 0x0, 0x0, 0x42002c, 0x0, 0x0, 0x0, 0x43002c, 0x0, 0x0, 0x0, 0x40004c, 0x0, 0x0, 0x0, 0x41004c, 0x0, 0x0, 0x0, 0x42004c, 0x0, 0x0, 0x0, 0x43004c, 0x0, 0x0, 0x0, 0x40006c, 0x0, 0x0, 0x0, 0x41006c, 0x0, 0x0, 0x0, 0x42006c, 0x0, 0x0, 0x0, 0x43006c, 0x0, 0x0, 0x0, 0x40008c, 0x0, 0x0, 0x0, 0x41008c, 0x0, 0x0, 0x0, 0x42008c, 0x0, 0x0, 0x0, 0x43008c, 0x0, 0x0, 0x0, 0x4000ac, 0x0, 0x0, 0x0, 0x4100ac, 0x0, 0x0, 0x0, 0x4200ac, 0x0, 0x0, 0x0, 0x4300ac, 0x0, 0x0, 0x0, 0x4000cc, 0x0, 0x0, 0x0, 0x4100cc, 0x0, 0x0, 0x0, 0x4200cc, 0x0, 0x0, 0x0, 0x4300cc, 0x0, 0x0, 0x0, 0x4000ec, 0x0, 0x0, 0x0, 0x4100ec, 0x0, 0x0, 0x0, 0x4200ec, 0x0, 0x0, 0x0, 0x4300ec, 0x0, 0x0, 0x0},
  {0x0, 0x0, 0x0, 0x0, 0x0, 0x0, 0x0, 0x0, 0x0, 0x0, 0x0, 0x0, 0x0, 0x0, 0x0, 0x0, 0x0, 0x0, 0x0, 0x0, 0x0, 0x0, 0x0, 0x0, 0x0, 0x0, 0x0, 0x0, 0x0, 0x0, 0x0, 0x0, 0x40000d, 0x0, 0x0, 0x0, 0x41000d, 0x0, 0x0, 0x0, 0x42000d, 0x0, 0x0, 0x0, 0x43000d, 0x0, 0x0, 0x0, 0x40002d, 0x0, 0x0, 0x0, 0x41002d, 0x0, 0x0, 0x0, 0x42002d, 0x0, 0x0, 0x0, 0x43002d, 0x0, 0x0, 0x0, 0x40000e, 0x0, 0x0, 0x0, 0x41000e, 0x0, 0x0, 0x0, 0x42000e, 0x0, 0x0, 0x0, 0x43000e, 0x0, 0x0, 0x0, 0x40002e, 0x0, 0x0, 0x0, 0x41002e, 0x0, 0x0, 0x0, 0x42002e, 0x0, 0x0, 0x0, 0x43002e, 0x0, 0x0, 0x0, 0x0, 0x0, 0x0, 0x0, 0x0, 0x0, 0x0, 0x0, 0x0, 0x0, 0x0, 0x0, 0x0, 0x0, 0x0, 0x0, 0x0, 0x0, 0x0, 0x0, 0x0, 0x0, 0x0, 0x0, 0x0, 0x0, 0x0, 0x0, 0x0, 0x0, 0x0, 0x0},
  {0x0, 0x0, 0x0, 0x0, 0x0, 0x0, 0x0, 0x0, 0x0, 0x0, 0x0, 0x0, 0x0, 0x0, 0x0, 0x0, 0x0, 0x0, 0x0, 0x0, 0x0, 0x0, 0x0, 0x0, 0x0, 0x0, 0x0, 0x0, 0x0, 0x0, 0x0, 0x0, 0x40004d, 0x0, 0x0, 0x0, 0x41004d, 0x0, 0x0, 0x0, 0x42004d, 0x0, 0x0, 0x0, 0x43004d, 0x0, 0x0, 0x0, 0x40006d, 0x0, 0x0, 0x0, 0x41006d, 0x0, 0x0, 0x0, 0x42006d, 0x0, 0x0, 0x0, 0x43006d, 0x0, 0x0, 0x0, 0x40004e, 0x0, 0x0, 0x0, 0x41004e, 0x0, 0x0, 0x0, 0x42004e, 0x0, 0x0, 0x0, 0x43004e, 0x0, 0x0, 0x0, 0x40006e, 0x0, 0x0, 0x0, 0x41006e, 0x0, 0x0, 0x0, 0x42006e, 0x0, 0x0, 0x0, 0x43006e, 0x0, 0x0, 0x0, 0x0, 0x0, 0x0, 0x0, 0x0, 0x0, 0x0, 0x0, 0x0, 0x0, 0x0, 0x0, 0x0, 0x0, 0x0, 0x0, 0x0, 0x0, 0x0, 0x0, 0x0, 0x0, 0x0, 0x0, 0x0, 0x0, 0x0, 0x0, 0x0, 0x0, 0x0, 0x0},
  {0x0, 0x0, 0x0, 0x0, 0x0, 0x0, 0x0, 0x0, 0x0, 0x0, 0x0, 0x0, 0x0, 0x0, 0x0, 0x0, 0x0, 0x0, 0x0, 0x0, 0x0, 0x0, 0x0, 0x0, 0x0, 0x0, 0x0, 0x0, 0x0, 0x0, 0x0, 0x0, 0x40008d, 0x0, 0x0, 0x0, 0x41008d, 0x0, 0x0, 0x0, 0x42008d, 0x0, 0x0, 0x0, 0x43008d, 0x0, 0x0, 0x0, 0x4000ad, 0x0, 0x0, 0x0, 0x4100ad, 0x0, 0x0, 0x0, 0x4200ad, 0x0, 0x0, 0x0, 0x4300ad, 0x0, 0x0, 0x0, 0x40008e, 0x0, 0x0, 0x0, 0x41008e, 0x0, 0x0, 0x0, 0x42008e, 0x0, 0x0, 0x0, 0x43008e, 0x0, 0x0, 0x0, 0x4000ae, 0x0, 0x0, 0x0, 0x4100ae, 0x0, 0x0, 0x0, 0x4200ae, 0x0, 0x0, 0x0, 0x4300ae, 0x0, 0x0, 0x0, 0x0, 0x0, 0x0, 0x0, 0x0, 0x0, 0x0, 0x0, 0x0, 0x0, 0x0, 0x0, 0x0, 0x0, 0x0, 0x0, 0x0, 0x0, 0x0, 0x0, 0x0, 0x0, 0x0, 0x0, 0x0, 0x0, 0x0, 0x0, 0x0, 0x0, 0x0, 0x0},
  {0x0, 0x0, 0x0, 0x0, 0x0, 0x0, 0x0, 0x0, 0x0, 0x0, 0x0, 0x0, 0x0, 0x0, 0x0, 0x0, 0x0, 0x0, 0x0, 0x0, 0x0, 0x0, 0x0, 0x0, 0x0, 0x0, 0x0, 0x0, 0x0, 0x0, 0x0, 0x0, 0x4000cd, 0x0, 0x0, 0x0, 0x4100cd, 0x0, 0x0, 0x0, 0x4200cd, 0x0, 0x0, 0x0, 0x4300cd, 0x0, 0x0, 0x0, 0x4000ed, 0x0, 0x0, 0x0, 0x4100ed, 0x0, 0x0, 0x0, 0x4200ed, 0x0, 0x0, 0x0, 0x4300ed, 0x0, 0x0, 0x0, 0x4000ce, 0x0, 0x0, 0x0, 0x4100ce, 0x0, 0x0, 0x0, 0x4200ce, 0x0, 0x0, 0x0, 0x4300ce, 0x0, 0x0, 0x0, 0x4000ee, 0x0, 0x0, 0x0, 0x4100ee, 0x0, 0x0, 0x0, 0x4200ee, 0x0, 0x0, 0x0, 0x4300ee, 0x0, 0x0, 0x0, 0x0, 0x0, 0x0, 0x0, 0x0, 0x0, 0x0, 0x0, 0x0, 0x0, 0x0, 0x0, 0x0, 0x0, 0x0, 0x0, 0x0, 0x0, 0x0, 0x0, 0x0, 0x0, 0x0, 0x0, 0x0, 0x0, 0x0, 0x0, 0x0, 0x0, 0x0, 0x0},
};
__global__ void __launch_bounds__(512, 2) k_mega(const u16* __restrict__ Wq, const u16* __restrict__ Wk, const u16* __restrict__ Wv,
                                                 const u16* __restrict__ Wo, const u16* __restrict__ w1, const u16* __restrict__ w3,
                                                 const u16* __restrict__ w2, const u16* __restrict__ x, const u16* __restrict__ cvec,
                                                 const u16* __restrict__ wmod, const u16* __restrict__ mem0, char* ws, u16* out) {
  extern __shared__ __attribute__((aligned(16))) char smem[];
  int wave_id_ = __builtin_amdgcn_readfirstlane((int)threadIdx.x >> 6);
  asm volatile("" : "+s"(wave_id_));
  unsigned* tmo = (unsigned*)(ws + OFF_TMO);
  unsigned long long* mts = nullptr;
  const float* mod = (const float*)(ws + OFF_MOD);
  volatile LAS unsigned* tslot = (volatile LAS unsigned*)(smem + L_SLOT + 768);
  unsigned xcc; asm volatile("s_getreg_b32 %0, hwreg(HW_REG_XCC_ID)" : "=s"(xcc)); xcc &= 7;
  auto ini_of = [](int sl) { return sl >= 128 && ((sl + 8) & 31) < 16 ? ((sl >> 5) - 4) * 16 + (sl & 15) : -1; };
  const int guess = xcc * 32 + ((blockIdx.x >> 3) & 31);
  if (wave_id_ == 3) {
    const char* pc; asm volatile("s_getpc_b64 %0" : "=s"(pc));
    const unsigned o = (unsigned)(lane_id() * 32 + ((blockIdx.x >> 3) & 31)) * 128;
    if (o < CPF * 1024) { const unsigned v = *(const unsigned*)(pc + o); asm volatile("" :: "v"(v)); }
  }
  if (wave_id_ >= 4 && ini_of(guess) >= 0) { init_sums(smem, opaque_tid() - 256, ini_of(guess), cvec, wmod); if (wave_id_ == 7) IS(3); }
  u32x2 rt_; u32x4 pfe;
  unsigned long long tag;
  asm volatile("s_load_dwordx2 %0, %3, %6\n\ts_load_dwordx2 %1, %4, 0\n\ts_load_dwordx4 %2, %5, 0\n\ts_waitcnt lgkmcnt(0)" : "=&s"(tag), "=&s"(rt_), "=&s"(pfe)
               : "s"(ws), "s"(&ROLES[guess][0]), "s"(&PFT[xcc][((blockIdx.x >> 3) & 31) * 4]), "i"((int)OFF_EP) : "memory");
  tag = (tag ^ (tag >> 31) ^ 0x9E3779B97F4A7C15ull) * 0xBF58476D1CE4E5B9ull; tag = (tag ^ (tag >> 29)) * 0x94D049BB133111EBull; tag ^= tag >> 32;
  if (wave_id_ == 0) {
    if (lane_id() == 0) {
      const unsigned long long g = tag; IS(0);
      unsigned sl = 0xFFFFu;
      for (int i = 0; i < 256; ++i)
        if (__hip_atomic_exchange((unsigned long long*)(ws + OFF_CLM) + ((guess + i) & 255), g, __ATOMIC_RELAXED, __HIP_MEMORY_SCOPE_AGENT) != g) { sl = (guess + i) & 255; break; }
      IS(1);
      tslot[0] = sl;
    }
  } else if (wave_id_ == 1) {
    const int ig = ini_of(guess);
    tslot[1] = ig >= 0 && (ig & 15) < 12 ? (unsigned)calibrate(ws, ig) : 0u;
    IS(2);
  } else if (ini_of(guess) >= 0) {
    if (wave_id_ < 4) { const unsigned v = *(const unsigned*)(mem0 + (opaque_tid() - 128) * 64); asm volatile("" :: "v"(v)); }
  } else if (wave_id_ >= 4) {
    delay64<PFD>();
#pragma unroll
    for (int i = 0; i < 4; ++i) {
      const unsigned e = pfe[i];
      const int t = opaque_tid() - 256, m = e & 3, wide = (e >> 21) & 1, row = ((e >> 8) & 8191) + (wide ? t >> 4 : t), cl = ((e >> 2) & 63) + (wide ? t & 15 : 0);
      const u16* p = (m == 0 ? Wq : (m == 1 ? Wk : (m == 2 ? Wv : Wo))) + (size_t)row * (m == 3 ? DM : HD) + cl * 64;
      if (e) asm volatile("s_mov_b32 m0, %1\n\ts_nop 0\n\tglobal_load_lds_dword %0, off" :: "v"(p), "s"((rt_.x & 15) == T_QV ? 31232 : L_GB + 8192) : "memory", "m0");
    }
    if (PFW) WAIT_V(0);
  }
  BAR();
  const int slot = (int)__builtin_amdgcn_readfirstlane(tslot[0]);
  const int cd = (int)__builtin_amdgcn_readfirstlane(tslot[1]);
  if (slot != guess) { rt_ = *(const u32x2*)ROLES[slot & 255]; WAIT_V(0); }
  BAR();
  asm volatile("" : "+s"(wave_id_));
  if (slot >= 256) return;
  const int xg = slot >> 5;
  const unsigned rw = rt_[0];
  const int role = rw & 15, idx = (rw >> 4) & 255;
  if (role == T_QV || role == T_KR) {
    const int wg = role == T_QV ? (idx >> 3) * 32 + (idx & 7) : (idx >> 4) * 32 + 8 + (idx & 15);
    if (role == T_QV) qk_body(smem, wave_id_, xg, idx, wg, Wq, Wv, ws, tmo, tag);
    else vr_body(smem, wave_id_, xg, idx >> 1, idx & 1, wg, Wk, Wv, Wo, ws, tmo, tag);
    return;
  }
  unsigned* cnt = (unsigned*)(xch_base(ws) + ((size_t)4 << 21));
  const int ini = ini_of(slot);
  if (ini >= 0) {
    if (slot != guess) { if (wave_id_ < 4) init_sums(smem, opaque_tid(), ini, cvec, wmod); BAR(); }
    init_role(smem, wave_id_, ini, mem0, ws, tag, slot != guess || cd == 0 ? 0u : ((cd > 0) == !(xcc & 4) ? 1u : 2u));
    if ((rw >> 12) & 255) l1_role(smem, wave_id_, ((rw >> 12) & 255) - 1, x, mod, cvec, wmod, ws + OFF_H1, cnt, tmo, mts, tag);
    else cnt += (ini_resolve1(smem, wave_id_, ws, tag) >> 2) << 11;
  }
  const int ntask = (rw >> 20) & 15;
  for (int it = 0; it < ntask; ++it) {
    const unsigned tk = it == 0 ? rt_[1] : ROLES[slot][1 + it];
    const int m = tk & 3, h = (tk >> 2) & 31, bo = (tk >> 8) & 255, ns = ((tk >> 16) & 255) - bo, part = (tk >> 24) & 3;
    const int lane = lane_id();
    const Dep d{uni((const char*)cnt), (unsigned)(PL1 + (lane & 31)) * 128, 8u, (unsigned)-bo};
    unsigned par = 0;
    if (wave_id_ == 0) {
      if ((tk >> 26) & 1) wait_k0(cnt, h, tmo);
    }
    BAR();
    gemm7<R_QKV>(smem, wave_id_, (m == 0 ? Wq : (m == 1 ? Wk : Wv)) + h * DH, nullptr, HD, ws + OFF_H1 + (size_t)bo * 65536, 65536, d,
                    ws + (m == 0 ? OFF_Q : (m == 1 ? OFF_K : OFF_V)) + h * 8192 + (size_t)bo * 32 * 8192, 32 * 8192,
                    cnt + (PQKV + h) * 32 + 4 * part + m, tmo, part ? 319 : (h >> 3) * 32 + m * 8 + (h & 7), mts, false,
                    bo ? 0 : NIMM, ns, bo ? ns : 0, bo * 257, tag, &par);
    cnt += (par >> 2) << 11;
  }
  __syncthreads();
  const char* cb = uni((const char*)cnt);
  if (role == T_WO || role == T_F2) {
    const bool wo = role == T_WO;
    const int ks = idx >> 3, cn = idx & 7;
    const int lane = lane_id();
    const Dep d{cb, (unsigned)(wo ? PROG + 8 * ks + (lane & 7) : PF1 + 16 * ks + (lane & 15)) * 128, 1u, 0u};
    gemm7<R_PART>(smem, wave_id_, (wo ? Wo : w2) + (size_t)ks * 1024 * DM + cn * 128, nullptr, DM, ws + (wo ? OFF_O : OFF_G) + ks * 16 * 4096, (wo ? 64 : 56) * 4096, d,
                  ws + (wo ? OFF_P : OFF_P2) + (ks * DM + cn * 128) * (TLP ? 64 : 2), (size_t)32 * PROW, cnt + ((wo ? PWO : PF2) + idx) * 32, tmo,
                  wo ? 128 + ks * 32 + cn : 128 + (idx / 7) * 32 + 22 + idx % 7, mts, !wo);
  } else if (role == T_F2C) {
    const int lane = lane_id();
    const Dep d{cb, (unsigned)(PF1 + 48 + (lane & 7)) * 128, 1u, 0u};
    gemm7<R_PART2>(smem, wave_id_, w2 + (size_t)3072 * DM + idx * 256, nullptr, DM, ws + OFF_G + 48 * 4096, 56 * 4096, d,
                   ws + OFF_P2 + (3 * DM + idx * 256) * (TLP ? 64 : 2), (size_t)32 * PROW, cnt + (PF2 + 24 + idx) * 32, tmo, 128 + 96 + 25 + idx, mts, true);
  } else if (role == T_F1) {
    const int lane = lane_id();
    const Dep d{cb, (unsigned)(PLN + (lane & 15)) * 128, 2u, (unsigned)((lane >> 3) & 1)};
    gemm7<R_F1>(smem, wave_id_, w1 + 64 * idx, w3 + 64 * idx, FF, ws + OFF_H2, 65536, d, ws + OFF_G + idx * 4096, 56 * 4096, cnt + (PF1 + idx) * 32, tmo,
                128 + (idx / 14) * 32 + 8 + idx % 14, mts, false);
  } else if (role == T_FIN) {
    fin_role(smem, wave_id_, idx, mod, ws + OFF_P2, (const u16*)(ws + OFF_X1), out, cnt, tmo, mts);
    if (idx == 15 && opaque_tid() == 0) st8_sc1(ws + OFF_EP, u32x2{(unsigned)tag, (unsigned)(tag >> 32)});
  } else if (role == T_LN) {
    row_role(smem, wave_id_, idx & 7, idx >> 3, mod, ws + OFF_P, x, (u16*)(ws + OFF_X1), ws + OFF_H2, cnt, tmo, 256 + ((idx >> 1) & 3), mts);
  }
}

extern "C" void kernel_launch(void* const* d_in, const int* in_sizes, int n_in, void* d_out, int out_size, void* d_ws, size_t ws_size,
                              hipStream_t stream) {
  const u16* x    = (const u16*)d_in[0];
  const u16* mem0 = (const u16*)d_in[1];
  const u16* Wq   = (const u16*)d_in[2];
  const u16* Wk   = (const u16*)d_in[3];
  const u16* Wv   = (const u16*)d_in[4];
  const u16* Wo   = (const u16*)d_in[5];
  const u16* w1   = (const u16*)d_in[8];
  const u16* w3   = (const u16*)d_in[9];
  const u16* w2   = (const u16*)d_in[10];
  const u16* c    = (const u16*)d_in[11];
  const u16* wmod = (const u16*)d_in[12];
#define WSHIFT 0
  char* ws = (char*)d_ws + ((size_t)WSHIFT << 20);
  hipFuncSetAttribute((const void*)k_mega, hipFuncAttributeMaxDynamicSharedMemorySize, MEGA_LDS);
  k_mega<<<256, 512, MEGA_LDS, stream>>>(Wq, Wk, Wv, Wo, w1, w3, w2, x, c, wmod, mem0, ws, (u16*)d_out);
}
```
